```python
import torch

def _softmax(x, axis=-1):
    return torch.softmax(x, dim=axis)
def _gelu(x):
    return torch.nn.functional.gelu(x, approximate="tanh")
def _silu(x):
    return torch.nn.functional.silu(x)
def _sigmoid(x):
    return torch.sigmoid(x)
def _layernorm(x, axis=-1, eps=1e-5):
    m = x.mean(dim=axis, keepdim=True)
    v = x.var(dim=axis, keepdim=True, unbiased=False)
    return (x - m) / torch.sqrt(v + eps)
def _scatter_add(tgt, idx, src, axis=0):
    return tgt.clone().index_add_(axis, idx, src)
def _segment_sum(src, seg, n):
    out = torch.zeros((n,) + src.shape[1:], dtype=src.dtype)
    return out.index_add_(0, seg, src)
def _pad(x, pw):
    flat = tuple(v for pair in reversed(pw) for v in pair)
    return torch.nn.functional.pad(x, flat)

kernel_name = 'standalone__gqa_softmax__near_ridge'

def setup_inputs():

    _w = (lambda a: (lambda t: t.to(torch.bfloat16) if t.is_floating_point() else t)(torch.as_tensor(a)))
    B, Hq, Hkv, Sq, Sk, D = 16, 32, 32, 848, 512, 192
    return {
        "q": _w((lambda *s: torch.randn(*s, dtype=torch.bfloat16) * 0.8)(B, Hq, Sq, D)),
        "k": _w((lambda *s: torch.randn(*s, dtype=torch.bfloat16) * 0.8)(B, Hkv, Sk, D)),
        "v": _w((lambda *s: torch.randn(*s, dtype=torch.bfloat16) * 0.05)(B, Hkv, Sk, D)),
    }

def reference(q, k, v):
    B, Hq, Sq, D = q.shape
    Hkv = k.shape[1]
    rep = Hq // Hkv
    k = (lambda x, n, axis: torch.repeat_interleave(x, n, dim=axis))(k, rep, axis=1)
    v = (lambda x, n, axis: torch.repeat_interleave(x, n, dim=axis))(v, rep, axis=1)
    scale = D ** -0.5
    scores = torch.einsum("bhqd,bhkd->bhqk", q, k) * scale
    p = _softmax(scores, axis=-1)
    return torch.einsum("bhqk,bhkd->bhqd", p, v)
```

```cpp
#include <hip/hip_runtime.h>
#include <hip/hip_bf16.h>

typedef __bf16   bf16x8 __attribute__((ext_vector_type(8)));
typedef __bf16   bf16x2 __attribute__((ext_vector_type(2)));
typedef float    f32x4  __attribute__((ext_vector_type(4)));
typedef float    f32x2  __attribute__((ext_vector_type(2)));
typedef unsigned u32x4  __attribute__((ext_vector_type(4)));
typedef unsigned u32x2  __attribute__((ext_vector_type(2)));
typedef unsigned short u16x8 __attribute__((ext_vector_type(8)));

constexpr int SQ = 848, SK = 512, DH = 192, ROWB = 384;
constexpr int LDS_BYTES = 163840;
constexpr float CSC = 0.07216878364870322f * 1.4426950408889634f;

#define DI __device__ __forceinline__
DI unsigned cvtpk(float lo, float hi) { f32x2 v = {lo, hi}; bf16x2 b = __builtin_convertvector(v, bf16x2); return __builtin_bit_cast(unsigned, b); }
DI float xmax(float x) {
  auto r = __builtin_amdgcn_permlane32_swap(__float_as_uint(x), __float_as_uint(x), false, false);
  x = fmaxf(__uint_as_float(r[0]), __uint_as_float(r[1]));
  auto q = __builtin_amdgcn_permlane16_swap(__float_as_uint(x), __float_as_uint(x), false, false);
  return fmaxf(__uint_as_float(q[0]), __uint_as_float(q[1]));
}
DI float xsum(float x) {
  auto r = __builtin_amdgcn_permlane32_swap(__float_as_uint(x), __float_as_uint(x), false, false);
  x = __uint_as_float(r[0]) + __uint_as_float(r[1]);
  auto q = __builtin_amdgcn_permlane16_swap(__float_as_uint(x), __float_as_uint(x), false, false);
  return __uint_as_float(q[0]) + __uint_as_float(q[1]);
}
DI f32x4 score_tile(const unsigned short* krow, const bf16x8 (&qf)[6]) {
  f32x4 acc = {0.f, 0.f, 0.f, 0.f};
#pragma unroll
  for (int s = 0; s < 6; ++s) acc = __builtin_amdgcn_mfma_f32_16x16x32_bf16(*(const bf16x8*)(krow + 32 * s), qf[s], acc, 0, 0, 0);
  return acc;
}
DI void exact_tile(const unsigned short* Q, const unsigned short* K, const unsigned short* V, unsigned short* O, int head, int row0, int lo, int hi) {
  const unsigned lane = __builtin_amdgcn_mbcnt_hi(~0u, __builtin_amdgcn_mbcnt_lo(~0u, 0u));
  const int c = lane & 15, g4 = lane >> 4;
  const int qrow = min(row0 + c, SQ - 1);
  bf16x8 qf[6];
#pragma unroll
  for (int s = 0; s < 6; ++s) qf[s] = *(const bf16x8*)(Q + ((size_t)head * SQ + qrow) * DH + 32 * s + 8 * g4);
  const unsigned short* Kh = K + ((size_t)head * SK + c) * DH + 8 * g4;
  float m = -__builtin_huge_valf();
#pragma unroll 1
  for (int kb = 0; kb < SK / 16; ++kb) {
    const f32x4 a = score_tile(Kh + (size_t)kb * 16 * DH, qf);
    m = fmaxf(fmaxf(m, fmaxf(a[0], a[1])), fmaxf(a[2], a[3]));
  }
  const float mc = -xmax(m) * CSC;
  float l = 0.f;
  f32x4 o[12];
#pragma unroll
  for (int dt = 0; dt < 12; ++dt) o[dt] = f32x4{0.f, 0.f, 0.f, 0.f};
#pragma unroll 1
  for (int j = 0; j < SK / 32; ++j) {
    u32x4 p;
#pragma unroll
    for (int kt = 0; kt < 2; ++kt) {
      f32x4 a = score_tile(Kh + (size_t)(32 * j + 16 * kt) * DH, qf);
#pragma unroll
      for (int r = 0; r < 4; ++r) { a[r] = __builtin_amdgcn_exp2f(fmaf(a[r], CSC, mc)); l += a[r]; }
      p[2 * kt] = cvtpk(a[0], a[1]); p[2 * kt + 1] = cvtpk(a[2], a[3]);
    }
    const unsigned short* Vj = V + ((size_t)head * SK + 32 * j + 4 * g4) * DH + c;
#pragma unroll
    for (int dt = 0; dt < 12; ++dt) {
      u16x8 vf;
#pragma unroll
      for (int i = 0; i < 8; ++i) vf[i] = Vj[(16 * (i >> 2) + (i & 3)) * DH + 16 * dt];
      o[dt] = __builtin_amdgcn_mfma_f32_16x16x32_bf16(__builtin_bit_cast(bf16x8, vf), __builtin_bit_cast(bf16x8, p), o[dt], 0, 0, 0);
    }
  }
  const float inv = 1.0f / xsum(l);
  if (row0 + c < SQ && c >= lo && c < hi) {
#pragma unroll
    for (int dt = 0; dt < 12; ++dt)
      *(u32x2*)(O + ((size_t)head * SQ + qrow) * DH + 16 * dt + 4 * g4) = u32x2{cvtpk(o[dt][0] * inv, o[dt][1] * inv), cvtpk(o[dt][2] * inv, o[dt][3] * inv)};
  }
}

__global__ __launch_bounds__(256, 1)
void attn_fwd_g(const unsigned short* __restrict__ Qg, const unsigned short* __restrict__ Kg,
                const unsigned short* __restrict__ Vg, unsigned short* __restrict__ Og) {
  extern __shared__ __attribute__((aligned(512))) char lds[];
  const int tid0 = __builtin_amdgcn_readfirstlane(threadIdx.x), wg = blockIdx.x, wid = tid0 >> 6;
  unsigned bad;
  asm volatile(
  ".p2align 4\n"
  "s_load_dwordx8 s[84:91], %1, 0\n"
  "L_PC_%=:\n"
  "s_getpc_b64 s[52:53]\n"
  "s_lshr_b32 s79, %2, 3\n"
  "s_lshl_b32 s79, s79, 15\n"
  "s_add_u32 s52, s52, s79\n"
  "s_addc_u32 s53, s53, 0\n"
  "s_sub_i32 s54, L_END_%=-L_PC_%=, s79\n"
  "s_max_i32 s54, s54, 0\n"
  "s_mov_b32 s55, 0x20000\n"
  "s_add_u32 m0, %4, 2048\n"
  "v_mbcnt_lo_u32_b32 v0, -1, 0\n"
  "v_mbcnt_hi_u32_b32 v0, -1, v0\n"
  "v_add_lshl_u32 v1, v0, %3, 7\n"
  "buffer_load_dword v1, s[52:55], 0 offen lds\n"
  "s_bitcmp1_b32 %2, 0\n"
  "s_cbranch_scc1 2\n"
  "s_waitcnt vmcnt(0) lgkmcnt(0)\n"
  "s_sleep 110\n"
  "s_lshr_b32 s38, %3, 6\n"
  "s_lshr_b32 s79, %2, 3\n"
  "s_mul_i32 s100, s79, 43\n"
  "s_lshr_b32 s100, s100, 7\n"
  "s_mul_i32 s71, s100, 3\n"
  "s_sub_u32 s95, s79, s71\n"
  "s_and_b32 s101, %2, 7\n"
  "s_lshl_b32 s101, s101, 0\n"
  "s_lshr_b32 s79, s100, 0\n"
  "s_mul_i32 s79, s79, 7\n"
  "s_add_u32 s79, s79, s100\n"
  "s_add_u32 s79, s79, s101\n"
  "s_mul_i32 s37, s79, 196608\n"
  "s_mul_i32 s92, s79, 325632\n"
  "s_mul_i32 s79, s95, 110592\n"
  "s_add_u32 s92, s92, s79\n"
  "s_mul_i32 s79, s38, 3072\n"
  "s_add_u32 s96, s79, s37\n"
  "s_lshr_b32 s71, s38, 1\n"
  "s_lshl_b32 s71, s71, 6\n"
  "s_add_u32 s79, s79, s71\n"
  "s_add_u32 s79, s79, %4\n"
  "s_add_u32 s66, s79, 41152\n"
  "s_add_u32 s67, s79, 53504\n"
  "s_add_u32 s68, s79, 65856\n"
  "s_mul_i32 s79, s38, 3088\n"
  "s_add_u32 s79, s79, %4\n"
  "s_add_u32 s58, s79, 4096\n"
  "s_add_u32 s59, s79, 16448\n"
  "s_add_u32 s60, s79, 28800\n"
  "s_mul_i32 s72, s38, 21312\n"
  "s_add_u32 s72, s72, %4\n"
  "s_add_u32 s72, s72, 78208\n"
  "s_movk_i32 s42, 0xc00\n"
  "s_movk_i32 s31, 12288\n"
  "s_mov_b32 s43, 0x20000\n"
  "s_mov_b32 s51, 0x20000\n"
  "s_bitcmp1_b32 s95, 1\n"
  "s_movk_i32 s93, 288\n"
  "s_cmovk_i32 s93, 272\n"
  "s_mul_i32 s93, s93, 384\n"
  "s_mov_b32 s50, s93\n"
  "s_mul_i32 s80, s38, 24576\n"
  "s_add_u32 s81, s80, 6144\n"
  "v_lshrrev_b32 v1, 3, v0\n"
  "v_and_b32 v2, 2, v1\n"
  "v_and_b32 v3, 7, v0\n"
  "v_xor_b32 v2, v2, v3\n"
  "v_mul_u32_u24 v221, 0x180, v1\n"
  "v_lshl_add_u32 v221, v2, 4, v221\n"
  "s_waitcnt lgkmcnt(0)\n"
  "s_add_u32 s40, s86, s96\n"
  "s_addc_u32 s41, s87, 0\n"
  "s_add_u32 s48, s84, s92\n"
  "s_addc_u32 s49, s85, 0\n"
  "s_mov_b32 m0, s68\n"
  "s_nop 0\n"
  "buffer_load_dwordx4 v221, s[40:43], 0 offen offset:0 lds\n"
  "s_addk_i32 m0, 896\n"
  "s_nop 0\n"
  "buffer_load_dwordx4 v221, s[40:43], 0 offen offset:128 lds\n"
  "s_addk_i32 m0, 896\n"
  "s_nop 0\n"
  "buffer_load_dwordx4 v221, s[40:43], 0 offen offset:256 lds\n"
  "s_add_u32 s40, s40, s31\n"
  "s_addc_u32 s41, s41, 0\n"
  "s_add_u32 s44, s88, s96\n"
  "s_addc_u32 s45, s89, 0\n"
  "s_mov_b64 s[62:63], s[90:91]\n"
  "s_mov_b32 s90, 100663296\n"
  "s_mov_b32 s91, 0x20000\n"
  "s_add_u32 s29, %4, 163456\n"
  "v_mbcnt_lo_u32_b32 v4, -1, 0\n"
  "v_mbcnt_hi_u32_b32 v4, -1, v4\n"
  "v_mul_u32_u24 v4, 0x18c, v4\n"
  "s_mov_b32 m0, s29\n"
  "s_nop 0\n"
  "buffer_load_dword v4, s[88:91], s37 offen lds\n"
  "s_mul_i32 s56, s38, 3072\n"
  "s_add_u32 s56, s56, 98304\n"
  "s_add_u32 m0, s72, 17920\n"
  "s_nop 0\n"
  "buffer_load_dwordx4 v221, s[48:51], s56 offen offset:0  lds\n"
  "s_add_u32 s82, s80, 12288\n"
  "s_add_u32 s83, s80, 18432\n"
  "s_mov_b64 s[46:47], s[42:43]\n"
  "s_mov_b64 s[34:35], s[42:43]\n"
  "s_bitcmp1_b32 s38, 0\n"
  "s_movk_i32 s70, 512\n"
  "s_cmovk_i32 s70, -512\n"
  "s_movk_i32 s97, 0\n"
  "s_cmovk_i32 s97, -21312\n"
  "s_movk_i32 s61, 0\n"
  "s_cmovk_i32 s61, -21308\n"
  "s_movk_i32 s69, 20196\n"
  "s_cmovk_i32 s69, 20188\n"
  "s_add_u32 m0, s72, 18912\n"
  "s_nop 0\n"
  "buffer_load_dwordx4 v221, s[48:51], s56 offen offset:128  lds\n"
  "s_mov_b32 s98, 0xff00ff00\n"
  "s_mov_b32 s99, s98\n"
  "s_mov_b32 s73, 0x3dd53b94\n"
  "s_mov_b32 s74, 0xbdd53b94\n"
  "s_mov_b32 s75, 0x53800000\n"
  "s_mov_b32 s76, -1\n"
  "s_add_u32 m0, s72, 19904\n"
  "s_nop 0\n"
  "buffer_load_dwordx4 v221, s[48:51], s56 offen offset:256  lds\n"
  "s_mov_b32 s77, 1\n"
  "s_mov_b32 s78, 0\n"
  "s_mov_b32 s57, s92\n"
  "s_mul_i32 s39, s38, 3072\n"
  "s_mov_b32 s54, 0\n"
  "s_add_u32 m0, s58, 1792\n"
  "s_nop 0\n"
  "buffer_load_dwordx4 v221, s[48:51], s80 offen offset:256  lds\n"
  "s_mov_b32 s55, 0x20000\n"
  "L_PC2_%=:\n"
  "s_getpc_b64 s[20:21]\n"
  "s_lshl_b32 s24, s38, 13\n"
  "s_add_u32 s24, s24, L_BIG_%=-L_PC2_%=-4\n"
  "s_cmp_lt_u32 s38, 2\n"
  "s_cselect_b32 s24, s24, L_TAIL_%=-L_PC2_%=-4\n"
  "s_sub_u32 s25, L_END_%=-L_PC2_%=-4, s24\n"
  "s_add_u32 s20, s20, s24\n"
  "s_addc_u32 s21, s21, 0\n"
  "s_mov_b32 s23, 0x20000\n"
  "s_lshr_b32 s24, s38, 1\n"
  "s_mul_i32 s24, s24, s38\n"
  "s_mul_i32 s24, s24, 5\n"
  "s_lshr_b32 s24, s24, 1\n"
  "v_lshrrev_b32 v1, 4, v0\n"
  "v_and_b32 v2, 2, v0\n"
  "v_xor_b32_e64 v1, v1, v2\n"
  "s_add_u32 m0, s60, -1280\n"
  "s_nop 0\n"
  "buffer_load_dwordx4 v221, s[48:51], s80 offen offset:3328  lds\n"
  "v_lshlrev_b32 v1, 4, v1\n"
  "v_and_b32 v2, 7, v0\n"
  "v_lshl_add_u32 v72, v2, 7, v1\n"
  "v_bfe_u32 v3, v0, 3, 1\n"
  "v_mul_u32_u24 v2, 0x440, v3\n"
  "s_add_u32 m0, s66, -128\n"
  "s_nop 0\n"
  "buffer_load_dwordx4 v221, s[48:51], s80 offen offset:128  lds\n"
  "v_add3_u32 v222, v72, v2, s72\n"
  "v_mul_u32_u24 v2, 0x6080, v3\n"
  "v_add3_u32 v74, v72, v2, s58\n"
  "v_lshl_add_u32 v72, v3, 10, v72\n"
  "v_add_u32 v73, s66, v72\n"
  "s_add_u32 m0, s66, -2176\n"
  "s_nop 0\n"
  "buffer_load_dwordx4 v221, s[48:51], s80 offen offset:3200  lds\n"
  "v_add_u32 v72, s58, v72\n"
  "v_lshl_add_u32 v1, v3, 6, v1\n"
  "v_and_b32 v2, 3, v0\n"
  "v_lshl_add_u32 v1, v2, 7, v1\n"
  "v_bfe_u32 v2, v0, 2, 2\n"
  "s_add_u32 m0, s58, 0\n"
  "s_nop 0\n"
  "buffer_load_dwordx4 v221, s[48:51], s80 offen offset:0  lds\n"
  "v_mul_u32_u24 v2, 0xc00, v2\n"
  "s_and_b32 s79, s38, 1\n"
  "s_lshl_b32 s79, s79, 9\n"
  "s_add_u32 s79, s79, %4\n"
  "s_add_u32 s71, s79, 41152\n"
  "s_add_u32 m0, s58, -2048\n"
  "s_nop 0\n"
  "buffer_load_dwordx4 v221, s[48:51], s80 offen offset:3072  lds\n"
  "v_add3_u32 v216, v1, v2, s71\n"
  "v_add_u32 v235, s70, v216\n"
  "v_add_u32 v233, s97, v222\n"
  "s_mov_b64 exec, s[98:99]\n"
  "v_add_u32 v233, 0x4f00, v233\n"
  "s_mov_b64 exec, -1\n"
  "v_lshrrev_b32_e64 v1, 4, v0\n"
  "v_mul_u32_u24 v1, 0xc10, v1\n"
  "s_mov_b32 m0, s67\n"
  "s_nop 0\n"
  "buffer_load_dwordx4 v221, s[40:43], 0 offen offset:0 lds\n"
  "v_bfe_u32 v2, v0, 2, 2\n"
  "v_lshl_add_u32 v1, v2, 7, v1\n"
  "v_bfe_u32 v2, v0, 1, 1\n"
  "v_lshl_add_u32 v1, v2, 6, v1\n"
  "v_and_b32_e64 v2, 1, v0\n"
  "s_addk_i32 m0, 896\n"
  "s_nop 0\n"
  "buffer_load_dwordx4 v221, s[40:43], 0 offen offset:128 lds\n"
  "v_lshl_add_u32 v1, v2, 3, v1\n"
  "s_add_u32 s71, s79, 4096\n"
  "v_add_u32 v1, s71, v1\n"
  "v_lshrrev_b32 v2, 3, v0\n"
  "v_xor_b32_e64 v2, s38, v2\n"
  "s_addk_i32 m0, 896\n"
  "s_nop 0\n"
  "buffer_load_dwordx4 v221, s[40:43], 0 offen offset:256 lds\n"
  "s_add_u32 s40, s40, s31\n"
  "s_addc_u32 s41, s41, 0\n"
  "v_and_b32_e64 v2, 1, v2\n"
  "v_lshl_add_u32 v217, v2, 5, v1\n"
  "v_xor_b32_e64 v2, 1, v2\n"
  "v_lshl_add_u32 v219, v2, 5, v1\n"
  "v_add_u32_e64 v218, s70, v217\n"
  "s_add_u32 m0, s59, 896\n"
  "s_nop 0\n"
  "buffer_load_dwordx4 v221, s[44:47], 0 offen offset:128 lds\n"
  "v_add_u32_e64 v220, s70, v219\n"
  "s_lshl_b32 s71, s38, 9\n"
  "s_add_u32 s71, s71, %4\n"
  "s_add_u32 s71, s71, 0\n"
  "v_lshl_add_u32 v231, v0, 3, s71\n"
  "s_addk_i32 m0, 896\n"
  "s_nop 0\n"
  "buffer_load_dwordx4 v221, s[44:47], 0 offen offset:256 lds\n"
  "s_mov_b64 s[32:33], s[44:45]\n"
  "s_add_u32 s44, s44, s31\n"
  "s_addc_u32 s45, s45, 0\n"
  "v_add_u32 v232, s70, v231\n"
  "v_mov_b32 v248, 0x43400000\n"
  "v_mov_b32 v249, 0x43400000\n"
  "v_mov_b32 v250, 0x43400000\n"
  "v_mov_b32 v251, 0x43400000\n"
  "s_add_u32 m0, s72, 0\n"
  "s_nop 0\n"
  "buffer_load_dwordx4 v221, s[48:51], s81 offen offset:0  lds\n"
  "v_mov_b32 v1, 0x3f803f80\n"
  "v_accvgpr_write_b32 a152, v1\n"
  "v_accvgpr_write_b32 a153, v1\n"
  "v_accvgpr_write_b32 a154, v1\n"
  "v_accvgpr_write_b32 a155, v1\n"
  "s_add_u32 m0, s72, -1984\n"
  "s_nop 0\n"
  "buffer_load_dwordx4 v221, s[48:51], s81 offen offset:3072  lds\n"
  "v_bfe_u32 v1, v0, 3, 1\n"
  "v_lshrrev_b32 v230, 4, v0\n"
  "v_lshl_add_u32 v1, v1, 2, v230\n"
  "v_mul_u32_u24 v228, 0x180, v1\n"
  "s_and_b32 s71, s38, 1\n"
  "s_add_u32 m0, s72, 2112\n"
  "s_nop 0\n"
  "buffer_load_dwordx4 v221, s[48:51], s81 offen offset:128  lds\n"
  "s_lshl_b32 s71, s71, 1\n"
  "v_and_b32 v1, 7, v0\n"
  "v_xor_b32_e64 v1, s71, v1\n"
  "v_lshl_add_u32 v228, v1, 4, v228\n"
  "v_mul_u32_u24 v230, 0x120, v230\n"
  "s_add_u32 m0, s72, 128\n"
  "s_nop 0\n"
  "buffer_load_dwordx4 v221, s[48:51], s81 offen offset:3200  lds\n"
  "v_and_b32 v1, 3, v0\n"
  "v_lshl_add_u32 v230, v1, 6, v230\n"
  "v_bfe_u32 v1, v0, 2, 2\n"
  "v_lshl_add_u32 v230, v1, 3, v230\n"
  "v_add_u32_e64 v230, s72, v230\n"
  "s_add_u32 m0, s60, -256\n"
  "s_nop 0\n"
  "buffer_load_dwordx4 v221, s[48:51], s81 offen offset:256  lds\n"
  "v_and_b32 v1, 31, v0\n"
  "v_lshlrev_b32_e64 v229, 3, v1\n"
  "v_lshrrev_b32_e64 v1, 5, v0\n"
  "v_mul_u32_u24 v1, 0x460, v1\n"
  "v_add3_u32 v229, v1, v229, s72\n"
  "s_waitcnt vmcnt(16)\n"
  "s_barrier\n"
  "ds_read_b128 a[164:167], v216 offset:24704\n"
  "ds_read_b128 a[168:171], v216 offset:24768\n"
  "ds_read_b128 a[172:175], v216 offset:25728\n"
  "ds_read_b128 a[176:179], v216 offset:25792\n"
  "ds_read_b128 a[180:183], v216 offset:26752\n"
  "ds_read_b128 a[184:187], v216 offset:26816\n"
  "ds_read_b128 a[96:99], v233 offset:17920\n"
  "ds_read_b128 a[100:103], v233 offset:17984\n"
  "ds_read_b128 a[104:107], v233 offset:19040\n"
  "ds_read_b128 a[108:111], v233 offset:19104\n"
  "ds_read_b128 a[112:115], v233 offset:20160\n"
  "ds_read_b128 a[116:119], v233 offset:20224\n"
  "v_bfe_u32 v5, v231, 1, 8\n"
  "v_add_u32_e64 v5, s29, v5\n"
  "v_bfe_u32 v234, v231, 1, 6\n"
  "ds_read_b128 a[188:191], v235 offset:24704\n"
  "ds_read_b128 a[192:195], v235 offset:24768\n"
  "ds_read_b128 a[196:199], v235 offset:25728\n"
  "ds_read_b32 v4, v5\n"
  "ds_read_b128 a[200:203], v235 offset:25792\n"
  "ds_read_b128 a[204:207], v235 offset:26752\n"
  "ds_read_b128 a[208:211], v235 offset:26816\n"
  "s_waitcnt lgkmcnt(12)\n"
  "v_mfma_f32_16x16x32_bf16 v[64:67], a[164:167], a[96:99], v[248:251]\n"
  "s_waitcnt lgkmcnt(11)\n"
  "v_mfma_f32_16x16x32_bf16 v[64:67], a[168:171], a[100:103], v[64:67]\n"
  "s_add_u32 m0, s60, -2304\n"
  "s_nop 0\n"
  "buffer_load_dwordx4 v221, s[48:51], s81 offen offset:3328  lds\n"
  "s_waitcnt lgkmcnt(10)\n"
  "v_mfma_f32_16x16x32_bf16 v[64:67], a[172:175], a[104:107], v[64:67]\n"
  "s_waitcnt lgkmcnt(3)\n"
  "v_and_b32 v4, 0x7fff7fff, v4\n"
  "v_lshlrev_b32 v5, 16, v4\n"
  "v_max_u32_e64 v4, v4, v5\n"
  "v_mfma_f32_16x16x32_bf16 v[64:67], a[176:179], a[108:111], v[64:67]\n"
  "s_nop 0\n"
  "v_max_u32_dpp v4, v4, v4 quad_perm:[1,0,3,2] row_mask:0xf bank_mask:0xf\n"
  "v_mfma_f32_16x16x32_bf16 v[64:67], a[180:183], a[112:115], v[64:67]\n"
  "s_nop 0\n"
  "v_max_u32_dpp v4, v4, v4 quad_perm:[2,3,0,1] row_mask:0xf bank_mask:0xf\n"
  "v_add_u32_e64 v4, -1, v4\n"
  "v_mfma_f32_16x16x32_bf16 v[64:67], a[184:187], a[116:119], v[64:67]\n"
  "s_nop 0\n"
  "v_min_u32_dpp v4, v4, v4 row_shr:4 row_mask:0xf bank_mask:0xf\n"
  "v_mfma_f32_16x16x32_bf16 v[240:243], a[188:191], a[96:99], v[248:251]\n"
  "s_add_u32 m0, s72, 8704\n"
  "s_nop 0\n"
  "buffer_load_dwordx4 v221, s[48:51], s82 offen offset:256  lds\n"
  "v_min_u32_dpp v4, v4, v4 row_shr:8 row_mask:0xf bank_mask:0xf\n"
  "v_mfma_f32_16x16x32_bf16 v[240:243], a[192:195], a[100:103], v[240:243]\n"
  "s_nop 0\n"
  "v_min_u32_dpp v4, v4, v4 row_bcast:15 row_mask:0xa bank_mask:0xf\n"
  "v_mfma_f32_16x16x32_bf16 v[240:243], a[196:199], a[104:107], v[240:243]\n"
  "s_waitcnt lgkmcnt(2)\n"
  "v_min_u32_dpp v4, v4, v4 row_bcast:31 row_mask:0xc bank_mask:0xf\n"
  "v_max3_f32 v227, v64, v65, v66\n"
  "v_mfma_f32_16x16x32_bf16 v[240:243], a[200:203], a[108:111], v[240:243]\n"
  "s_waitcnt lgkmcnt(1)\n"
  "v_readlane_b32 s65, v4, 63\n"
  "v_mfma_f32_16x16x32_bf16 v[240:243], a[204:207], a[112:115], v[240:243]\n"
  "s_waitcnt lgkmcnt(0)\n"
  "v_mfma_f32_16x16x32_bf16 v[240:243], a[208:211], a[116:119], v[240:243]\n"
  "s_add_u32 m0, s72, 6720\n"
  "s_nop 0\n"
  "buffer_load_dwordx4 v221, s[48:51], s82 offen offset:3328  lds\n"
  "s_waitcnt vmcnt(13)\n"
  "ds_read_b128 a[20:23], v74 offset:2112\n"
  "ds_read_b128 a[16:19], v74 offset:2048\n"
  "ds_read_b128 a[12:15], v73 offset:64\n"
  "s_add_u32 s65, s65, 1\n"
  "ds_read_b128 a[8:11], v73 offset:0\n"
  "ds_read_b128 a[4:7], v72 offset:64\n"
  "ds_read_b128 a[0:3], v72 offset:0\n"
  "s_lshr_b32 s65, s65, 23\n"
  "v_max3_f32 v227, v227, v67, v240\n"
  "v_max3_f32 v227, v227, v241, v242\n"
  "v_max_f32 v227, v227, v243\n"
  "v_mul_f32 v227, s74, v227\n"
  "s_sub_i32 s65, 0x77, s65\n"
  "s_waitcnt lgkmcnt(5)\n"
  "v_mfma_f32_16x16x32_bf16 v[0:3], a[184:187], a[20:23], v[248:251]\n"
  "s_add_u32 m0, s72, 6592\n"
  "s_nop 0\n"
  "buffer_load_dwordx4 v221, s[48:51], s82 offen offset:128  lds\n"
  "s_waitcnt lgkmcnt(4)\n"
  "v_mfma_f32_16x16x32_bf16 v[0:3], a[180:183], a[16:19], v[0:3]\n"
  "s_and_b32 s65, s65, -4\n"
  "s_waitcnt lgkmcnt(3)\n"
  "v_mfma_f32_16x16x32_bf16 v[0:3], a[176:179], a[12:15], v[0:3]\n"
  "s_max_i32 s65, s65, 0xffffffc4\n"
  "s_waitcnt lgkmcnt(2)\n"
  "v_cvt_f32_i32_e64 v4, s65\n"
  "v_mfma_f32_16x16x32_bf16 v[0:3], a[172:175], a[8:11], v[0:3]\n"
  "s_add_u32 s65, s65, 0xab\n"
  "s_waitcnt lgkmcnt(1)\n"
  "v_readfirstlane_b32 s64, v4\n"
  "s_nop 1\n"
  "v_add_f32 v227, s64, v227\n"
  "v_mfma_f32_16x16x32_bf16 v[0:3], a[168:171], a[4:7], v[0:3]\n"
  "s_cmpk_gt_u32 s65, 0xd3\n"
  "s_cselect_b32 s65, 0, s65\n"
  "ds_bpermute_b32 v227, v234, v227\n"
  "s_waitcnt lgkmcnt(1)\n"
  "v_mfma_f32_16x16x32_bf16 v[0:3], a[164:167], a[0:3], v[0:3]\n"
  "s_add_u32 m0, s72, 4608\n"
  "s_nop 0\n"
  "buffer_load_dwordx4 v221, s[48:51], s82 offen offset:3200  lds\n"
  "v_mfma_f32_16x16x32_bf16 v[4:7], a[208:211], a[20:23], v[248:251]\n"
  "s_lshl_b32 s65, s65, 23\n"
  "v_mfma_f32_16x16x32_bf16 v[4:7], a[204:207], a[16:19], v[4:7]\n"
  "s_mov_b32 s75, s65\n"
  "v_mfma_f32_16x16x32_bf16 v[4:7], a[200:203], a[12:15], v[4:7]\n"
  "v_max3_f32 v223, v0, v1, v2\n"
  "v_max_f32 v223, v223, v3\n"
  "v_mul_f32 v223, s74, v223\n"
  "v_add_f32 v223, s64, v223\n"
  "v_mfma_f32_16x16x32_bf16 v[4:7], a[196:199], a[8:11], v[4:7]\n"
  "ds_bpermute_b32 v223, v234, v223\n"
  "v_mfma_f32_16x16x32_bf16 v[4:7], a[192:195], a[4:7], v[4:7]\n"
  "s_add_u32 m0, s72, 4480\n"
  "s_nop 0\n"
  "buffer_load_dwordx4 v221, s[48:51], s82 offen offset:0  lds\n"
  "v_mfma_f32_16x16x32_bf16 v[4:7], a[188:191], a[0:3], v[4:7]\n"
  "s_waitcnt lgkmcnt(1)\n"
  "v_fma_f32 v64, v64, s73, v227\n"
  "v_exp_f32 v64, v64\n"
  "v_fma_f32 v65, v65, s73, v227\n"
  "v_exp_f32_e64 v65, v65\n"
  "v_fma_f32 v66, v66, s73, v227\n"
  "v_exp_f32 v66, v66\n"
  "v_fma_f32 v67, v67, s73, v227\n"
  "v_exp_f32 v67, v67\n"
  "v_cvt_pk_bf16_f32 v64, v64, v65\n"
  "v_cvt_pk_bf16_f32 v65, v66, v67\n"
  "ds_write_b64 v231, v[64:65] offset:0\n"
  "s_waitcnt lgkmcnt(1)\n"
  "v_fma_f32 v0, v0, s73, v223\n"
  "v_exp_f32 v0, v0\n"
  "v_fma_f32 v1, v1, s73, v223\n"
  "s_waitcnt vmcnt(11) lgkmcnt(0)\n"
  "s_barrier\n"
  "ds_read_b128 a[212:215], v216 offset:12352\n"
  "ds_read_b128 a[216:219], v216 offset:12416\n"
  "ds_read_b128 a[220:223], v216 offset:13376\n"
  "ds_read_b128 a[224:227], v216 offset:13440\n"
  "v_exp_f32_e64 v1, v1\n"
  "ds_read_b128 a[228:231], v216 offset:14400\n"
  "ds_read_b128 a[232:235], v216 offset:14464\n"
  "v_fma_f32 v2, v2, s73, v223\n"
  "v_fma_f32 v3, v3, s73, v223\n"
  "ds_read_b64 a[156:157], v231 offset:0\n"
  "ds_read_b64 a[158:159], v232 offset:0\n"
  "s_waitcnt lgkmcnt(7)\n"
  "v_exp_f32 v2, v2\n"
  "v_mfma_f32_16x16x32_bf16 v[64:67], a[212:215], a[96:99], v[248:251]\n"
  "s_add_u32 m0, s72, 2496\n"
  "s_nop 0\n"
  "buffer_load_dwordx4 v221, s[48:51], s82 offen offset:3072  lds\n"
  "ds_read_b64_tr_b16 a[144:145], v217 offset:14400\n"
  "ds_read_b64_tr_b16 a[146:147], v218 offset:14400\n"
  "s_waitcnt lgkmcnt(8)\n"
  "v_mfma_f32_16x16x32_bf16 v[64:67], a[216:219], a[100:103], v[64:67]\n"
  "ds_read_b64_tr_b16 a[148:149], v217 offset:14416\n"
  "ds_read_b64_tr_b16 a[150:151], v218 offset:14416\n"
  "s_waitcnt lgkmcnt(9)\n"
  "v_exp_f32 v3, v3\n"
  "v_mfma_f32_16x16x32_bf16 v[64:67], a[220:223], a[104:107], v[64:67]\n"
  "s_nop 0\n"
  "ds_read_b64_tr_b16 v[76:77], v217 offset:13376\n"
  "ds_read_b64_tr_b16 v[78:79], v218 offset:13376\n"
  "s_waitcnt lgkmcnt(10)\n"
  "v_cvt_pk_bf16_f32 v8, v0, v1\n"
  "v_cvt_pk_bf16_f32 v9, v2, v3\n"
  "v_mfma_f32_16x16x32_bf16 v[64:67], a[224:227], a[108:111], v[64:67]\n"
  "ds_read_b64_tr_b16 v[80:81], v217 offset:13392\n"
  "ds_read_b64_tr_b16 v[82:83], v218 offset:13392\n"
  "s_waitcnt lgkmcnt(11)\n"
  "v_fma_f32 v4, v4, s73, v223\n"
  "v_fma_f32 v5, v5, s73, v223\n"
  "v_mfma_f32_16x16x32_bf16 v[64:67], a[228:231], a[112:115], v[64:67]\n"
  "ds_read_b128 a[120:123], v235 offset:12352\n"
  "ds_read_b128 a[124:127], v235 offset:12416\n"
  "s_waitcnt lgkmcnt(12)\n"
  "v_exp_f32_e64 v4, v4\n"
  "v_mfma_f32_16x16x32_bf16 v[64:67], a[232:235], a[116:119], v[64:67]\n"
  "s_add_u32 m0, s72, 11200\n"
  "s_nop 0\n"
  "buffer_load_dwordx4 v221, s[48:51], s83 offen offset:0  lds\n"
  "ds_read_b128 a[128:131], v235 offset:13376\n"
  "ds_read_b128 a[132:135], v235 offset:13440\n"
  "ds_read_b128 a[136:139], v235 offset:14400\n"
  "ds_read_b128 a[140:143], v235 offset:14464\n"
  "s_waitcnt lgkmcnt(12)\n"
  "v_exp_f32 v5, v5\n"
  "v_mfma_f32_16x16x32_bf16 a[244:247], a[156:159], a[144:147], 1.0\n"
  "ds_read_b64_tr_b16 v[84:85], v219 offset:14400\n"
  "ds_read_b64_tr_b16 v[86:87], v220 offset:14400\n"
  "s_waitcnt lgkmcnt(12)\n"
  "v_fma_f32 v6, v6, s73, v223\n"
  "v_fma_f32 v7, v7, s73, v223\n"
  "v_mfma_f32_16x16x32_bf16 a[248:251], a[156:159], a[148:151], 1.0\n"
  "ds_read_b64_tr_b16 v[244:245], v219 offset:14416\n"
  "ds_read_b64_tr_b16 v[246:247], v220 offset:14416\n"
  "s_waitcnt lgkmcnt(12)\n"
  "v_exp_f32 v6, v6\n"
  "v_mfma_f32_16x16x32_bf16 a[236:239], a[156:159], v[76:79], 1.0\n"
  "ds_read_b64_tr_b16 a[252:253], v219 offset:13376\n"
  "ds_read_b64_tr_b16 a[254:255], v220 offset:13376\n"
  "s_waitcnt lgkmcnt(12)\n"
  "v_exp_f32_e64 v7, v7\n"
  "v_mfma_f32_16x16x32_bf16 a[240:243], a[156:159], v[80:83], 1.0\n"
  "s_add_u32 m0, s72, 9216\n"
  "s_nop 0\n"
  "buffer_load_dwordx4 v221, s[48:51], s83 offen offset:3072  lds\n"
  "ds_read_b64_tr_b16 v[236:237], v219 offset:13392\n"
  "ds_read_b64_tr_b16 v[238:239], v220 offset:13392\n"
  "v_mfma_f32_16x16x32_bf16 v[0:3], a[232:235], a[20:23], v[248:251]\n"
  "v_cvt_pk_bf16_f32 v10, v4, v5\n"
  "v_cvt_pk_bf16_f32 v11, v6, v7\n"
  "v_fma_f32 v64, v64, s73, v227\n"
  "v_fma_f32 v65, v65, s73, v227\n"
  "v_mfma_f32_16x16x32_bf16 v[0:3], a[228:231], a[16:19], v[0:3]\n"
  "v_exp_f32 v64, v64\n"
  "v_exp_f32 v65, v65\n"
  "v_mfma_f32_16x16x32_bf16 v[0:3], a[224:227], a[12:15], v[0:3]\n"
  "v_fma_f32 v66, v66, s73, v227\n"
  "v_exp_f32 v66, v66\n"
  "v_fma_f32 v67, v67, s73, v227\n"
  "v_mfma_f32_16x16x32_bf16 v[0:3], a[220:223], a[8:11], v[0:3]\n"
  "v_exp_f32 v67, v67\n"
  "v_cvt_pk_bf16_f32 v64, v64, v65\n"
  "v_mfma_f32_16x16x32_bf16 v[0:3], a[216:219], a[4:7], v[0:3]\n"
  "s_add_u32 m0, s72, 13312\n"
  "s_nop 0\n"
  "buffer_load_dwordx4 v221, s[48:51], s83 offen offset:128  lds\n"
  "v_cvt_pk_bf16_f32 v65, v66, v67\n"
  "v_mfma_f32_16x16x32_bf16 v[0:3], a[212:215], a[0:3], v[0:3]\n"
  "ds_write_b64 v231, v[64:65] offset:2048\n"
  "s_waitcnt lgkmcnt(14)\n"
  "v_mfma_f32_16x16x32_bf16 v[4:7], a[120:123], a[0:3], v[248:251]\n"
  "s_waitcnt lgkmcnt(13)\n"
  "v_mfma_f32_16x16x32_bf16 v[4:7], a[124:127], a[4:7], v[4:7]\n"
  "s_waitcnt lgkmcnt(12)\n"
  "v_mfma_f32_16x16x32_bf16 v[4:7], a[128:131], a[8:11], v[4:7]\n"
  "s_waitcnt lgkmcnt(11)\n"
  "v_fma_f32 v0, v0, s73, v223\n"
  "v_exp_f32 v0, v0\n"
  "v_fma_f32 v1, v1, s73, v223\n"
  "v_mfma_f32_16x16x32_bf16 v[4:7], a[132:135], a[12:15], v[4:7]\n"
  "s_add_u32 m0, s72, 11328\n"
  "s_nop 0\n"
  "buffer_load_dwordx4 v221, s[48:51], s83 offen offset:3200  lds\n"
  "s_waitcnt lgkmcnt(10)\n"
  "v_exp_f32_e64 v1, v1\n"
  "v_mfma_f32_16x16x32_bf16 v[4:7], a[136:139], a[16:19], v[4:7]\n"
  "s_waitcnt lgkmcnt(9)\n"
  "v_fma_f32 v2, v2, s73, v223\n"
  "v_exp_f32 v2, v2\n"
  "v_fma_f32 v3, v3, s73, v223\n"
  "v_mfma_f32_16x16x32_bf16 v[4:7], a[140:143], a[20:23], v[4:7]\n"
  "v_exp_f32 v3, v3\n"
  "v_cvt_pk_bf16_f32 v12, v0, v1\n"
  "s_waitcnt vmcnt(10)\n"
  "ds_read_b128 a[24:27], v222 offset:0\n"
  "ds_read_b128 a[28:31], v222 offset:64\n"
  "ds_read_b128 a[32:35], v222 offset:2240\n"
  "v_cvt_pk_bf16_f32 v13, v2, v3\n"
  "ds_read_b128 a[36:39], v222 offset:2304\n"
  "ds_read_b128 a[40:43], v72 offset:24704\n"
  "ds_read_b128 a[44:47], v72 offset:24768\n"
  "v_fma_f32 v4, v4, s73, v223\n"
  "v_exp_f32_e64 v4, v4\n"
  "v_fma_f32 v5, v5, s73, v223\n"
  "s_waitcnt lgkmcnt(5)\n"
  "v_exp_f32 v5, v5\n"
  "v_fma_f32 v6, v6, s73, v223\n"
  "v_fma_f32 v7, v7, s73, v223\n"
  "v_mfma_f32_16x16x32_bf16 v[16:19], a[164:167], a[24:27], v[248:251]\n"
  "s_add_u32 m0, s72, 15424\n"
  "s_nop 0\n"
  "buffer_load_dwordx4 v221, s[48:51], s83 offen offset:256  lds\n"
  "s_waitcnt lgkmcnt(4)\n"
  "v_exp_f32 v6, v6\n"
  "v_mfma_f32_16x16x32_bf16 v[16:19], a[168:171], a[28:31], v[16:19]\n"
  "s_waitcnt lgkmcnt(3)\n"
  "v_exp_f32_e64 v7, v7\n"
  "v_cvt_pk_bf16_f32 v14, v4, v5\n"
  "v_mfma_f32_16x16x32_bf16 v[16:19], a[172:175], a[32:35], v[16:19]\n"
  "s_waitcnt lgkmcnt(2)\n"
  "v_cvt_pk_bf16_f32 v15, v6, v7\n"
  "v_mfma_f32_16x16x32_bf16 v[16:19], a[176:179], a[36:39], v[16:19]\n"
  "s_waitcnt lgkmcnt(1)\n"
  "v_mfma_f32_16x16x32_bf16 v[16:19], a[180:183], a[40:43], v[16:19]\n"
  "s_waitcnt lgkmcnt(0)\n"
  "v_mfma_f32_16x16x32_bf16 v[16:19], a[184:187], a[44:47], v[16:19]\n"
  "s_add_u32 m0, s72, 13440\n"
  "s_nop 0\n"
  "buffer_load_dwordx4 v221, s[48:51], s83 offen offset:3328  lds\n"
  "v_mfma_f32_16x16x32_bf16 v[20:23], a[188:191], a[24:27], v[248:251]\n"
  "v_mfma_f32_16x16x32_bf16 v[20:23], a[192:195], a[28:31], v[20:23]\n"
  "v_mfma_f32_16x16x32_bf16 v[20:23], a[196:199], a[32:35], v[20:23]\n"
  "v_max3_f32 v224, v16, v17, v18\n"
  "v_max_f32 v224, v224, v19\n"
  "v_mul_f32 v224, s74, v224\n"
  "v_add_f32 v224, s64, v224\n"
  "v_mfma_f32_16x16x32_bf16 v[20:23], a[200:203], a[36:39], v[20:23]\n"
  "ds_bpermute_b32 v224, v234, v224\n"
  "v_mfma_f32_16x16x32_bf16 v[20:23], a[204:207], a[40:43], v[20:23]\n"
  "s_mov_b32 m0, s66\n"
  "s_nop 0\n"
  "buffer_load_dwordx4 v221, s[40:43], 0 offen offset:0 lds\n"
  "v_mfma_f32_16x16x32_bf16 v[20:23], a[208:211], a[44:47], v[20:23]\n"
  "v_mfma_f32_16x16x32_bf16 v[104:107], v[8:11], a[144:147], 1.0\n"
  "v_mfma_f32_16x16x32_bf16 v[108:111], v[8:11], a[148:151], 1.0\n"
  "v_mfma_f32_16x16x32_bf16 v[112:115], v[8:11], v[84:87], 1.0\n"
  "v_mfma_f32_16x16x32_bf16 v[116:119], v[8:11], v[244:247], 1.0\n"
  "s_addk_i32 m0, 896\n"
  "s_nop 0\n"
  "buffer_load_dwordx4 v221, s[40:43], 0 offen offset:128 lds\n"
  "v_mfma_f32_16x16x32_bf16 v[88:91], v[8:11], v[76:79], 1.0\n"
  "s_waitcnt lgkmcnt(0)\n"
  "v_fma_f32 v16, v16, s73, v224\n"
  "v_exp_f32 v16, v16\n"
  "v_fma_f32 v17, v17, s73, v224\n"
  "v_mfma_f32_16x16x32_bf16 v[92:95], v[8:11], v[80:83], 1.0\n"
  "v_exp_f32 v17, v17\n"
  "v_fma_f32 v18, v18, s73, v224\n"
  "v_fma_f32 v19, v19, s73, v224\n"
  "v_mfma_f32_16x16x32_bf16 v[96:99], v[8:11], a[252:255], 1.0\n"
  "v_exp_f32 v18, v18\n"
  "v_exp_f32 v19, v19\n"
  "v_mfma_f32_16x16x32_bf16 v[100:103], v[8:11], v[236:239], 1.0\n"
  "v_cvt_pk_bf16_f32 v24, v16, v17\n"
  "v_cvt_pk_bf16_f32 v25, v18, v19\n"
  "v_fma_f32 v20, v20, s73, v224\n"
  "v_fma_f32 v21, v21, s73, v224\n"
  "v_exp_f32 v20, v20\n"
  "v_exp_f32 v21, v21\n"
  "v_fma_f32 v22, v22, s73, v224\n"
  "v_exp_f32_e64 v22, v22\n"
  "v_fma_f32 v23, v23, s73, v224\n"
  "v_mfma_f32_16x16x32_bf16 v[16:19], a[212:215], a[24:27], v[248:251]\n"
  "s_addk_i32 m0, 896\n"
  "s_nop 0\n"
  "buffer_load_dwordx4 v221, s[40:43], 0 offen offset:256 lds\n"
  "s_add_u32 s40, s40, s31\n"
  "s_addc_u32 s41, s41, 0\n"
  "v_exp_f32 v23, v23\n"
  "v_mfma_f32_16x16x32_bf16 v[16:19], a[216:219], a[28:31], v[16:19]\n"
  "v_cvt_pk_bf16_f32 v26, v20, v21\n"
  "v_cvt_pk_bf16_f32 v27, v22, v23\n"
  "v_mfma_f32_16x16x32_bf16 v[16:19], a[220:223], a[32:35], v[16:19]\n"
  "v_mfma_f32_16x16x32_bf16 v[16:19], a[224:227], a[36:39], v[16:19]\n"
  "v_mfma_f32_16x16x32_bf16 v[16:19], a[228:231], a[40:43], v[16:19]\n"
  "v_mfma_f32_16x16x32_bf16 v[16:19], a[232:235], a[44:47], v[16:19]\n"
  "s_mov_b32 m0, s58\n"
  "s_nop 0\n"
  "buffer_load_dwordx4 v221, s[32:35], 0 offen offset:0 lds\n"
  "v_mfma_f32_16x16x32_bf16 v[20:23], a[140:143], a[44:47], v[248:251]\n"
  "v_mfma_f32_16x16x32_bf16 v[20:23], a[136:139], a[40:43], v[20:23]\n"
  "v_mfma_f32_16x16x32_bf16 v[20:23], a[132:135], a[36:39], v[20:23]\n"
  "v_fma_f32 v16, v16, s73, v224\n"
  "v_exp_f32 v16, v16\n"
  "v_fma_f32 v17, v17, s73, v224\n"
  "v_mfma_f32_16x16x32_bf16 v[20:23], a[128:131], a[32:35], v[20:23]\n"
  "v_exp_f32_e64 v17, v17\n"
  "v_fma_f32 v18, v18, s73, v224\n"
  "v_fma_f32 v19, v19, s73, v224\n"
  "v_mfma_f32_16x16x32_bf16 v[20:23], a[124:127], a[28:31], v[20:23]\n"
  "s_addk_i32 m0, 1024\n"
  "s_nop 0\n"
  "buffer_load_dwordx4 v221, s[44:47], 0 offen offset:0 lds\n"
  "v_exp_f32_e64 v18, v18\n"
  "v_mfma_f32_16x16x32_bf16 v[20:23], a[120:123], a[24:27], v[20:23]\n"
  "v_exp_f32 v19, v19\n"
  "v_cvt_pk_bf16_f32 v28, v16, v17\n"
  "s_waitcnt vmcnt(11)\n"
  "ds_read_b128 a[68:71], v222 offset:9024\n"
  "ds_read_b128 a[64:67], v222 offset:8960\n"
  "ds_read_b128 a[60:63], v222 offset:6784\n"
  "v_cvt_pk_bf16_f32 v29, v18, v19\n"
  "ds_read_b128 a[56:59], v222 offset:6720\n"
  "ds_read_b128 a[52:55], v222 offset:4544\n"
  "ds_read_b128 a[48:51], v222 offset:4480\n"
  "v_fma_f32 v20, v20, s73, v224\n"
  "v_exp_f32_e64 v20, v20\n"
  "v_fma_f32 v21, v21, s73, v224\n"
  "s_waitcnt lgkmcnt(5)\n"
  "v_exp_f32 v21, v21\n"
  "v_fma_f32 v22, v22, s73, v224\n"
  "v_fma_f32 v23, v23, s73, v224\n"
  "v_mfma_f32_16x16x32_bf16 v[32:35], a[184:187], a[68:71], v[248:251]\n"
  "s_addk_i32 m0, 768\n"
  "s_nop 0\n"
  "buffer_load_dwordx4 v221, s[44:47], 0 offen offset:256 lds\n"
  "s_mov_b64 s[32:33], s[44:45]\n"
  "s_add_u32 s44, s44, s31\n"
  "s_addc_u32 s45, s45, 0\n"
  "s_waitcnt lgkmcnt(4)\n"
  "v_exp_f32 v22, v22\n"
  "v_mfma_f32_16x16x32_bf16 v[32:35], a[180:183], a[64:67], v[32:35]\n"
  "s_waitcnt lgkmcnt(3)\n"
  "v_exp_f32_e64 v23, v23\n"
  "v_cvt_pk_bf16_f32 v30, v20, v21\n"
  "v_mfma_f32_16x16x32_bf16 v[32:35], a[176:179], a[60:63], v[32:35]\n"
  "s_waitcnt lgkmcnt(2)\n"
  "v_cvt_pk_bf16_f32 v31, v22, v23\n"
  "v_mfma_f32_16x16x32_bf16 v[32:35], a[172:175], a[56:59], v[32:35]\n"
  "s_waitcnt lgkmcnt(1)\n"
  "v_mfma_f32_16x16x32_bf16 v[32:35], a[168:171], a[52:55], v[32:35]\n"
  "s_waitcnt lgkmcnt(0)\n"
  "v_mfma_f32_16x16x32_bf16 v[32:35], a[164:167], a[48:51], v[32:35]\n"
  "v_mfma_f32_16x16x32_bf16 v[36:39], a[208:211], a[68:71], v[248:251]\n"
  "v_mfma_f32_16x16x32_bf16 v[36:39], a[204:207], a[64:67], v[36:39]\n"
  "v_mfma_f32_16x16x32_bf16 v[36:39], a[200:203], a[60:63], v[36:39]\n"
  "v_max3_f32 v225, v32, v33, v34\n"
  "v_max_f32 v225, v225, v35\n"
  "v_mul_f32 v225, s74, v225\n"
  "v_add_f32 v225, s64, v225\n"
  "v_mfma_f32_16x16x32_bf16 v[36:39], a[196:199], a[56:59], v[36:39]\n"
  "ds_bpermute_b32 v225, v234, v225\n"
  "v_mfma_f32_16x16x32_bf16 v[36:39], a[192:195], a[52:55], v[36:39]\n"
  "v_mfma_f32_16x16x32_bf16 v[36:39], a[188:191], a[48:51], v[36:39]\n"
  "v_mfma_f32_16x16x32_bf16 v[136:139], v[24:27], a[144:147], 1.0\n"
  "v_mfma_f32_16x16x32_bf16 v[140:143], v[24:27], a[148:151], 1.0\n"
  "v_mfma_f32_16x16x32_bf16 v[144:147], v[24:27], v[84:87], 1.0\n"
  "v_mfma_f32_16x16x32_bf16 v[148:151], v[24:27], v[244:247], 1.0\n"
  "s_waitcnt lgkmcnt(0)\n"
  "v_fma_f32 v32, v32, s73, v225\n"
  "v_exp_f32 v32, v32\n"
  "v_fma_f32 v33, v33, s73, v225\n"
  "v_mfma_f32_16x16x32_bf16 v[120:123], v[24:27], v[76:79], 1.0\n"
  "v_exp_f32_e64 v33, v33\n"
  "v_fma_f32 v34, v34, s73, v225\n"
  "v_fma_f32 v35, v35, s73, v225\n"
  "v_mfma_f32_16x16x32_bf16 v[124:127], v[24:27], v[80:83], 1.0\n"
  "v_exp_f32 v34, v34\n"
  "v_exp_f32 v35, v35\n"
  "v_mfma_f32_16x16x32_bf16 v[128:131], v[24:27], a[252:255], 1.0\n"
  "v_cvt_pk_bf16_f32 v40, v32, v33\n"
  "v_cvt_pk_bf16_f32 v41, v34, v35\n"
  "v_fma_f32 v36, v36, s73, v225\n"
  "v_fma_f32 v37, v37, s73, v225\n"
  "v_mfma_f32_16x16x32_bf16 v[132:135], v[24:27], v[236:239], 1.0\n"
  "v_exp_f32 v36, v36\n"
  "v_exp_f32 v37, v37\n"
  "v_fma_f32 v38, v38, s73, v225\n"
  "v_exp_f32 v38, v38\n"
  "v_fma_f32 v39, v39, s73, v225\n"
  "v_exp_f32 v39, v39\n"
  "v_cvt_pk_bf16_f32 v42, v36, v37\n"
  "v_mfma_f32_16x16x32_bf16 v[32:35], a[232:235], a[68:71], v[248:251]\n"
  "v_cvt_pk_bf16_f32 v43, v38, v39\n"
  "v_mfma_f32_16x16x32_bf16 v[32:35], a[228:231], a[64:67], v[32:35]\n"
  "v_mfma_f32_16x16x32_bf16 v[32:35], a[224:227], a[60:63], v[32:35]\n"
  "v_mfma_f32_16x16x32_bf16 v[32:35], a[220:223], a[56:59], v[32:35]\n"
  "v_mfma_f32_16x16x32_bf16 v[32:35], a[216:219], a[52:55], v[32:35]\n"
  "v_mfma_f32_16x16x32_bf16 v[32:35], a[212:215], a[48:51], v[32:35]\n"
  "v_mfma_f32_16x16x32_bf16 v[36:39], a[120:123], a[48:51], v[248:251]\n"
  "v_mfma_f32_16x16x32_bf16 v[36:39], a[124:127], a[52:55], v[36:39]\n"
  "v_mfma_f32_16x16x32_bf16 v[36:39], a[128:131], a[56:59], v[36:39]\n"
  "v_fma_f32 v32, v32, s73, v225\n"
  "v_exp_f32_e64 v32, v32\n"
  "v_fma_f32 v33, v33, s73, v225\n"
  "v_mfma_f32_16x16x32_bf16 v[36:39], a[132:135], a[60:63], v[36:39]\n"
  "v_exp_f32_e64 v33, v33\n"
  "v_fma_f32 v34, v34, s73, v225\n"
  "v_fma_f32 v35, v35, s73, v225\n"
  "v_mfma_f32_16x16x32_bf16 v[36:39], a[136:139], a[64:67], v[36:39]\n"
  "v_exp_f32_e64 v34, v34\n"
  "v_exp_f32 v35, v35\n"
  "v_mfma_f32_16x16x32_bf16 v[36:39], a[140:143], a[68:71], v[36:39]\n"
  "v_cvt_pk_bf16_f32 v44, v32, v33\n"
  "v_cvt_pk_bf16_f32 v45, v34, v35\n"
  "s_waitcnt vmcnt(6)\n"
  "ds_read_b128 a[72:75], v222 offset:11200\n"
  "ds_read_b128 a[76:79], v222 offset:11264\n"
  "ds_read_b128 a[80:83], v222 offset:13440\n"
  "ds_read_b128 a[84:87], v222 offset:13504\n"
  "ds_read_b128 a[88:91], v222 offset:15680\n"
  "ds_read_b128 a[92:95], v222 offset:15744\n"
  "v_fma_f32 v36, v36, s73, v225\n"
  "v_exp_f32_e64 v36, v36\n"
  "v_fma_f32 v37, v37, s73, v225\n"
  "s_waitcnt lgkmcnt(5)\n"
  "v_exp_f32 v37, v37\n"
  "v_fma_f32 v38, v38, s73, v225\n"
  "v_fma_f32 v39, v39, s73, v225\n"
  "v_mfma_f32_16x16x32_bf16 v[48:51], a[164:167], a[72:75], v[248:251]\n"
  "s_waitcnt lgkmcnt(4)\n"
  "v_exp_f32 v38, v38\n"
  "v_exp_f32 v39, v39\n"
  "v_mfma_f32_16x16x32_bf16 v[48:51], a[168:171], a[76:79], v[48:51]\n"
  "s_waitcnt lgkmcnt(3)\n"
  "v_cvt_pk_bf16_f32 v46, v36, v37\n"
  "v_cvt_pk_bf16_f32 v47, v38, v39\n"
  "v_mfma_f32_16x16x32_bf16 v[48:51], a[172:175], a[80:83], v[48:51]\n"
  "s_waitcnt lgkmcnt(2)\n"
  "v_mfma_f32_16x16x32_bf16 v[48:51], a[176:179], a[84:87], v[48:51]\n"
  "s_waitcnt lgkmcnt(1)\n"
  "v_mfma_f32_16x16x32_bf16 v[48:51], a[180:183], a[88:91], v[48:51]\n"
  "s_waitcnt lgkmcnt(0)\n"
  "v_mfma_f32_16x16x32_bf16 v[48:51], a[184:187], a[92:95], v[48:51]\n"
  "v_mfma_f32_16x16x32_bf16 v[52:55], a[188:191], a[72:75], v[248:251]\n"
  "v_mfma_f32_16x16x32_bf16 v[52:55], a[192:195], a[76:79], v[52:55]\n"
  "v_mfma_f32_16x16x32_bf16 v[52:55], a[196:199], a[80:83], v[52:55]\n"
  "v_max3_f32 v226, v48, v49, v50\n"
  "v_max_f32 v226, v226, v51\n"
  "v_mul_f32 v226, s74, v226\n"
  "v_add_f32 v226, s64, v226\n"
  "v_mfma_f32_16x16x32_bf16 v[52:55], a[200:203], a[84:87], v[52:55]\n"
  "ds_bpermute_b32 v226, v234, v226\n"
  "v_mfma_f32_16x16x32_bf16 v[52:55], a[204:207], a[88:91], v[52:55]\n"
  "v_mfma_f32_16x16x32_bf16 v[52:55], a[208:211], a[92:95], v[52:55]\n"
  "v_mfma_f32_16x16x32_bf16 v[168:171], v[40:43], a[144:147], 1.0\n"
  "v_mfma_f32_16x16x32_bf16 v[172:175], v[40:43], a[148:151], 1.0\n"
  "v_mfma_f32_16x16x32_bf16 v[176:179], v[40:43], v[84:87], 1.0\n"
  "v_mfma_f32_16x16x32_bf16 v[180:183], v[40:43], v[244:247], 1.0\n"
  "s_waitcnt lgkmcnt(0)\n"
  "v_fma_f32 v48, v48, s73, v226\n"
  "v_exp_f32 v48, v48\n"
  "v_fma_f32 v49, v49, s73, v226\n"
  "v_mfma_f32_16x16x32_bf16 v[152:155], v[40:43], v[76:79], 1.0\n"
  "v_exp_f32_e64 v49, v49\n"
  "v_fma_f32 v50, v50, s73, v226\n"
  "v_fma_f32 v51, v51, s73, v226\n"
  "v_mfma_f32_16x16x32_bf16 v[156:159], v[40:43], v[80:83], 1.0\n"
  "v_exp_f32 v50, v50\n"
  "v_exp_f32 v51, v51\n"
  "v_mfma_f32_16x16x32_bf16 v[160:163], v[40:43], a[252:255], 1.0\n"
  "v_cvt_pk_bf16_f32 v56, v48, v49\n"
  "v_cvt_pk_bf16_f32 v57, v50, v51\n"
  "v_fma_f32 v52, v52, s73, v226\n"
  "v_fma_f32 v53, v53, s73, v226\n"
  "v_mfma_f32_16x16x32_bf16 v[164:167], v[40:43], v[236:239], 1.0\n"
  "v_exp_f32 v52, v52\n"
  "v_exp_f32 v53, v53\n"
  "v_fma_f32 v54, v54, s73, v226\n"
  "v_exp_f32 v54, v54\n"
  "v_fma_f32 v55, v55, s73, v226\n"
  "v_exp_f32 v55, v55\n"
  "v_cvt_pk_bf16_f32 v58, v52, v53\n"
  "v_mfma_f32_16x16x32_bf16 v[48:51], a[212:215], a[72:75], v[248:251]\n"
  "v_cvt_pk_bf16_f32 v59, v54, v55\n"
  "v_mfma_f32_16x16x32_bf16 v[48:51], a[216:219], a[76:79], v[48:51]\n"
  "v_mfma_f32_16x16x32_bf16 v[48:51], a[220:223], a[80:83], v[48:51]\n"
  "v_mfma_f32_16x16x32_bf16 v[48:51], a[224:227], a[84:87], v[48:51]\n"
  "v_mfma_f32_16x16x32_bf16 v[48:51], a[228:231], a[88:91], v[48:51]\n"
  "v_mfma_f32_16x16x32_bf16 v[48:51], a[232:235], a[92:95], v[48:51]\n"
  "v_mfma_f32_16x16x32_bf16 v[52:55], a[140:143], a[92:95], v[248:251]\n"
  "v_mfma_f32_16x16x32_bf16 v[52:55], a[136:139], a[88:91], v[52:55]\n"
  "v_mfma_f32_16x16x32_bf16 v[52:55], a[132:135], a[84:87], v[52:55]\n"
  "v_fma_f32 v48, v48, s73, v226\n"
  "v_exp_f32 v48, v48\n"
  "v_fma_f32 v49, v49, s73, v226\n"
  "v_mfma_f32_16x16x32_bf16 v[52:55], a[128:131], a[80:83], v[52:55]\n"
  "v_exp_f32 v49, v49\n"
  "v_fma_f32 v50, v50, s73, v226\n"
  "v_fma_f32 v51, v51, s73, v226\n"
  "v_mfma_f32_16x16x32_bf16 v[52:55], a[124:127], a[76:79], v[52:55]\n"
  "v_exp_f32_e64 v50, v50\n"
  "v_exp_f32_e64 v51, v51\n"
  "v_mfma_f32_16x16x32_bf16 v[52:55], a[120:123], a[72:75], v[52:55]\n"
  "v_cvt_pk_bf16_f32 v60, v48, v49\n"
  "v_cvt_pk_bf16_f32 v61, v50, v51\n"
  "v_mfma_f32_16x16x32_bf16 v[200:203], v[56:59], a[144:147], 1.0\n"
  "s_nop 4\n"
  "v_fma_f32 v52, v52, s73, v226\n"
  "s_nop 3\n"
  "v_exp_f32 v52, v52\n"
  "s_nop 2\n"
  "v_fma_f32 v53, v53, s73, v226\n"
  "v_mfma_f32_16x16x32_bf16 v[204:207], v[56:59], a[148:151], 1.0\n"
  "s_nop 0\n"
  "v_exp_f32 v53, v53\n"
  "v_fma_f32 v54, v54, s73, v226\n"
  "v_fma_f32 v55, v55, s73, v226\n"
  "v_mfma_f32_16x16x32_bf16 v[208:211], v[56:59], v[84:87], 1.0\n"
  "v_exp_f32 v54, v54\n"
  "v_exp_f32 v55, v55\n"
  "v_mfma_f32_16x16x32_bf16 v[212:215], v[56:59], v[244:247], 1.0\n"
  "v_cvt_pk_bf16_f32 v62, v52, v53\n"
  "v_cvt_pk_bf16_f32 v63, v54, v55\n"
  "v_mfma_f32_16x16x32_bf16 v[184:187], v[56:59], v[76:79], 1.0\n"
  "v_mfma_f32_16x16x32_bf16 v[188:191], v[56:59], v[80:83], 1.0\n"
  "v_mfma_f32_16x16x32_bf16 v[192:195], v[56:59], a[252:255], 1.0\n"
  "v_mfma_f32_16x16x32_bf16 v[196:199], v[56:59], v[236:239], 1.0\n"
  "s_add_i32 s76, s76, 1\n"
  "s_mov_b32 m0, s67\n"
  "s_waitcnt vmcnt(0)\n"
  "s_barrier\n"
  "ds_read_b128 a[124:127], v216 offset:0\n"
  "ds_read_b64 a[160:161], v231 offset:2048\n"
  "ds_read_b64 a[162:163], v232 offset:2048\n"
  "buffer_load_dwordx4 v221, s[40:43], 0 offen offset:0 lds\n"
  "ds_read_b128 a[120:123], v216 offset:64\n"
  "s_addk_i32 m0, 896\n"
  "ds_read_b128 a[140:143], v216 offset:1024\n"
  "ds_read_b128 a[136:139], v216 offset:1088\n"
  "buffer_load_dwordx4 v221, s[40:43], 0 offen offset:128 lds\n"
  "s_addk_i32 m0, 896\n"
  "ds_read_b128 a[144:147], v216 offset:2048\n"
  "ds_read_b128 a[148:151], v216 offset:2112\n"
  "buffer_load_dwordx4 v221, s[40:43], 0 offen offset:256 lds\n"
  "s_waitcnt lgkmcnt(4)\n"
  "v_mfma_f32_16x16x32_bf16 v[64:67], a[124:127], a[96:99], v[248:251]\n"
  "s_add_u32 m0, s59, -128\n"
  "v_mfma_f32_16x16x32_bf16 v[64:67], a[120:123], a[100:103], v[64:67]\n"
  "s_waitcnt lgkmcnt(2)\n"
  "v_mfma_f32_16x16x32_bf16 v[64:67], a[140:143], a[104:107], v[64:67]\n"
  "buffer_load_dwordx4 v221, s[32:35], 0 offen offset:128 lds\n"
  "v_mfma_f32_16x16x32_bf16 v[64:67], a[136:139], a[108:111], v[64:67]\n"
  "s_waitcnt lgkmcnt(0)\n"
  "v_mfma_f32_16x16x32_bf16 v[64:67], a[144:147], a[112:115], v[64:67]\n"
  "s_addk_i32 m0, 1024\n"
  "v_mfma_f32_16x16x32_bf16 v[64:67], a[148:151], a[116:119], v[64:67]\n"
  "s_mov_b64 s[32:33], s[44:45]\n"
  "v_mfma_f32_16x16x32_bf16 v[0:3], a[148:151], a[20:23], v[248:251]\n"
  "buffer_load_dwordx4 v221, s[44:47], 0 offen offset:128 lds\n"
  "v_mfma_f32_16x16x32_bf16 v[0:3], a[144:147], a[16:19], v[0:3]\n"
  "s_addk_i32 m0, 896\n"
  "v_mfma_f32_16x16x32_bf16 v[0:3], a[136:139], a[12:15], v[0:3]\n"
  "s_add_u32 s40, s40, s31\n"
  "v_mfma_f32_16x16x32_bf16 v[0:3], a[140:143], a[8:11], v[0:3]\n"
  "s_addc_u32 s41, s41, 0\n"
  "v_mfma_f32_16x16x32_bf16 v[0:3], a[120:123], a[4:7], v[0:3]\n"
  "buffer_load_dwordx4 v221, s[44:47], 0 offen offset:256 lds\n"
  "v_mfma_f32_16x16x32_bf16 v[0:3], a[124:127], a[0:3], v[0:3]\n"
  "s_add_u32 s44, s44, s31\n"
  "v_mfma_f32_16x16x32_bf16 v[16:19], a[124:127], a[24:27], v[248:251]\n"
  "s_addc_u32 s45, s45, 0\n"
  "v_mfma_f32_16x16x32_bf16 v[16:19], a[120:123], a[28:31], v[16:19]\n"
  "v_mfma_f32_16x16x32_bf16 v[16:19], a[140:143], a[32:35], v[16:19]\n"
  "ds_read_b128 a[132:135], v235 offset:0\n"
  "v_mfma_f32_16x16x32_bf16 v[16:19], a[136:139], a[36:39], v[16:19]\n"
  "ds_read_b128 a[128:131], v235 offset:64\n"
  "v_mfma_f32_16x16x32_bf16 v[16:19], a[144:147], a[40:43], v[16:19]\n"
  "v_mfma_f32_16x16x32_bf16 v[16:19], a[148:151], a[44:47], v[16:19]\n"
  "v_mfma_f32_16x16x32_bf16 v[32:35], a[148:151], a[68:71], v[248:251]\n"
  "v_mfma_f32_16x16x32_bf16 v[32:35], a[144:147], a[64:67], v[32:35]\n"
  "v_mfma_f32_16x16x32_bf16 v[32:35], a[136:139], a[60:63], v[32:35]\n"
  "v_mfma_f32_16x16x32_bf16 v[32:35], a[140:143], a[56:59], v[32:35]\n"
  "v_mfma_f32_16x16x32_bf16 v[32:35], a[120:123], a[52:55], v[32:35]\n"
  "v_mfma_f32_16x16x32_bf16 v[32:35], a[124:127], a[48:51], v[32:35]\n"
  "v_mfma_f32_16x16x32_bf16 v[48:51], a[124:127], a[72:75], v[248:251]\n"
  "ds_read_b128 a[124:127], v235 offset:1024\n"
  "v_mfma_f32_16x16x32_bf16 v[48:51], a[120:123], a[76:79], v[48:51]\n"
  "ds_read_b128 a[120:123], v235 offset:1088\n"
  "v_mfma_f32_16x16x32_bf16 v[48:51], a[140:143], a[80:83], v[48:51]\n"
  "ds_read_b128 a[140:143], v235 offset:2048\n"
  "v_mfma_f32_16x16x32_bf16 v[48:51], a[136:139], a[84:87], v[48:51]\n"
  "ds_read_b128 a[136:139], v235 offset:2112\n"
  "v_mfma_f32_16x16x32_bf16 v[48:51], a[144:147], a[88:91], v[48:51]\n"
  "v_mfma_f32_16x16x32_bf16 v[48:51], a[148:151], a[92:95], v[48:51]\n"
  "s_waitcnt lgkmcnt(4)\n"
  "v_mfma_f32_16x16x32_bf16 v[4:7], a[132:135], a[0:3], v[248:251]\n"
  "v_mfma_f32_16x16x32_bf16 v[4:7], a[128:131], a[4:7], v[4:7]\n"
  "s_waitcnt lgkmcnt(2)\n"
  "v_mfma_f32_16x16x32_bf16 v[4:7], a[124:127], a[8:11], v[4:7]\n"
  "v_mfma_f32_16x16x32_bf16 v[4:7], a[120:123], a[12:15], v[4:7]\n"
  "s_waitcnt lgkmcnt(0)\n"
  "v_mfma_f32_16x16x32_bf16 v[4:7], a[140:143], a[16:19], v[4:7]\n"
  "v_mfma_f32_16x16x32_bf16 v[4:7], a[136:139], a[20:23], v[4:7]\n"
  "v_mfma_f32_16x16x32_bf16 v[20:23], a[136:139], a[44:47], v[248:251]\n"
  "v_mfma_f32_16x16x32_bf16 v[20:23], a[140:143], a[40:43], v[20:23]\n"
  "v_mfma_f32_16x16x32_bf16 v[20:23], a[120:123], a[36:39], v[20:23]\n"
  "ds_read_b64_tr_b16 a[144:145], v217 offset:2048\n"
  "v_mfma_f32_16x16x32_bf16 v[20:23], a[124:127], a[32:35], v[20:23]\n"
  "ds_read_b64_tr_b16 a[146:147], v218 offset:2048\n"
  "v_mfma_f32_16x16x32_bf16 v[20:23], a[128:131], a[28:31], v[20:23]\n"
  "ds_read_b64_tr_b16 a[148:149], v217 offset:2064\n"
  "v_mfma_f32_16x16x32_bf16 v[20:23], a[132:135], a[24:27], v[20:23]\n"
  "ds_read_b64_tr_b16 a[150:151], v218 offset:2064\n"
  "v_mfma_f32_16x16x32_bf16 v[36:39], a[132:135], a[48:51], v[248:251]\n"
  "v_mfma_f32_16x16x32_bf16 v[36:39], a[128:131], a[52:55], v[36:39]\n"
  "v_mfma_f32_16x16x32_bf16 v[36:39], a[124:127], a[56:59], v[36:39]\n"
  "v_mfma_f32_16x16x32_bf16 v[36:39], a[120:123], a[60:63], v[36:39]\n"
  "v_mfma_f32_16x16x32_bf16 v[36:39], a[140:143], a[64:67], v[36:39]\n"
  "v_mfma_f32_16x16x32_bf16 v[36:39], a[136:139], a[68:71], v[36:39]\n"
  "v_mfma_f32_16x16x32_bf16 v[52:55], a[136:139], a[92:95], v[248:251]\n"
  "ds_read_b64_tr_b16 a[136:137], v219 offset:2048\n"
  "v_mfma_f32_16x16x32_bf16 v[52:55], a[140:143], a[88:91], v[52:55]\n"
  "ds_read_b64_tr_b16 a[138:139], v220 offset:2048\n"
  "v_mfma_f32_16x16x32_bf16 v[52:55], a[120:123], a[84:87], v[52:55]\n"
  "ds_read_b64_tr_b16 a[140:141], v219 offset:2064\n"
  "v_mfma_f32_16x16x32_bf16 v[52:55], a[124:127], a[80:83], v[52:55]\n"
  "ds_read_b64_tr_b16 a[142:143], v220 offset:2064\n"
  "v_fma_f32 v64, v64, s73, v227\n"
  "v_mfma_f32_16x16x32_bf16 v[52:55], a[128:131], a[76:79], v[52:55]\n"
  "v_exp_f32 v64, v64\n"
  "v_mfma_f32_16x16x32_bf16 v[52:55], a[132:135], a[72:75], v[52:55]\n"
  "s_waitcnt lgkmcnt(4)\n"
  "v_fma_f32 v65, v65, s73, v227\n"
  "v_fma_f32 v66, v66, s73, v227\n"
  "v_mfma_f32_16x16x32_bf16 v[104:107], v[12:15], a[144:147], v[104:107]\n"
  "v_exp_f32 v65, v65\n"
  "v_mfma_f32_16x16x32_bf16 v[108:111], v[12:15], a[148:151], v[108:111]\n"
  "v_mfma_f32_16x16x32_bf16 v[136:139], v[28:31], a[144:147], v[136:139]\n"
  "ds_read_b64_tr_b16 a[120:121], v217 offset:0\n"
  "v_mfma_f32_16x16x32_bf16 v[140:143], v[28:31], a[148:151], v[140:143]\n"
  "ds_read_b64_tr_b16 a[122:123], v218 offset:0\n"
  "v_mfma_f32_16x16x32_bf16 v[168:171], v[44:47], a[144:147], v[168:171]\n"
  "ds_read_b64_tr_b16 a[124:125], v217 offset:1024\n"
  "v_mfma_f32_16x16x32_bf16 v[172:175], v[44:47], a[148:151], v[172:175]\n"
  "ds_read_b64_tr_b16 a[126:127], v218 offset:1024\n"
  "v_mfma_f32_16x16x32_bf16 v[200:203], v[60:63], a[144:147], v[200:203]\n"
  "v_mfma_f32_16x16x32_bf16 v[204:207], v[60:63], a[148:151], v[204:207]\n"
  "v_exp_f32 v66, v66\n"
  "v_mfma_f32_16x16x32_bf16 a[244:247], a[160:163], a[144:147], a[244:247]\n"
  "v_mfma_f32_16x16x32_bf16 a[248:251], a[160:163], a[148:151], a[248:251]\n"
  "s_waitcnt lgkmcnt(4)\n"
  "v_mfma_f32_16x16x32_bf16 v[208:211], v[60:63], a[136:139], v[208:211]\n"
  "v_mfma_f32_16x16x32_bf16 v[212:215], v[60:63], a[140:143], v[212:215]\n"
  "v_mfma_f32_16x16x32_bf16 v[176:179], v[44:47], a[136:139], v[176:179]\n"
  "ds_read_b64_tr_b16 a[128:129], v217 offset:16\n"
  "v_mfma_f32_16x16x32_bf16 v[180:183], v[44:47], a[140:143], v[180:183]\n"
  "ds_read_b64_tr_b16 a[130:131], v218 offset:16\n"
  "v_mfma_f32_16x16x32_bf16 v[144:147], v[28:31], a[136:139], v[144:147]\n"
  "ds_read_b64_tr_b16 a[132:133], v217 offset:1040\n"
  "v_mfma_f32_16x16x32_bf16 v[148:151], v[28:31], a[140:143], v[148:151]\n"
  "ds_read_b64_tr_b16 a[134:135], v218 offset:1040\n"
  "v_mfma_f32_16x16x32_bf16 v[112:115], v[12:15], a[136:139], v[112:115]\n"
  "v_mfma_f32_16x16x32_bf16 v[116:119], v[12:15], a[140:143], v[116:119]\n"
  "s_waitcnt lgkmcnt(4)\n"
  "v_mfma_f32_16x16x32_bf16 a[164:167], v[8:11], a[120:123], 1.0\n"
  "v_fma_f32 v67, v67, s73, v227\n"
  "v_mfma_f32_16x16x32_bf16 a[164:167], v[12:15], a[124:127], a[164:167]\n"
  "v_exp_f32_e64 v67, v67\n"
  "v_mfma_f32_16x16x32_bf16 a[180:183], v[24:27], a[120:123], 1.0\n"
  "v_mfma_f32_16x16x32_bf16 a[180:183], v[28:31], a[124:127], a[180:183]\n"
  "v_cvt_pk_bf16_f32 v64, v64, v65\n"
  "v_cvt_pk_bf16_f32 v65, v66, v67\n"
  "v_mfma_f32_16x16x32_bf16 a[196:199], v[40:43], a[120:123], 1.0\n"
  "s_mov_b32 m0, s68\n"
  "ds_read_b64_tr_b16 a[136:137], v219 offset:0\n"
  "v_fma_f32 v0, v0, s73, v223\n"
  "v_mfma_f32_16x16x32_bf16 a[196:199], v[44:47], a[124:127], a[196:199]\n"
  "ds_read_b64_tr_b16 a[138:139], v220 offset:0\n"
  "v_fma_f32 v1, v1, s73, v223\n"
  "v_mfma_f32_16x16x32_bf16 a[212:215], v[56:59], a[120:123], 1.0\n"
  "ds_read_b64_tr_b16 a[140:141], v219 offset:1024\n"
  "v_fma_f32 v2, v2, s73, v223\n"
  "v_mfma_f32_16x16x32_bf16 a[212:215], v[60:63], a[124:127], a[212:215]\n"
  "ds_read_b64_tr_b16 a[142:143], v220 offset:1024\n"
  "v_fma_f32 v3, v3, s73, v223\n"
  "v_mfma_f32_16x16x32_bf16 a[228:231], a[156:159], a[120:123], 1.0\n"
  "buffer_load_dwordx4 v221, s[40:43], 0 offen offset:0 lds\n"
  "v_mfma_f32_16x16x32_bf16 a[228:231], a[160:163], a[124:127], a[228:231]\n"
  "s_waitcnt lgkmcnt(4)\n"
  "v_mfma_f32_16x16x32_bf16 a[232:235], a[156:159], a[128:131], 1.0\n"
  "s_addk_i32 m0, 896\n"
  "v_mfma_f32_16x16x32_bf16 a[232:235], a[160:163], a[132:135], a[232:235]\n"
  "ds_write_b64 v231, v[64:65] offset:0\n"
  "v_mfma_f32_16x16x32_bf16 a[216:219], v[56:59], a[128:131], 1.0\n"
  "buffer_load_dwordx4 v221, s[40:43], 0 offen offset:128 lds\n"
  "v_fma_f32 v16, v16, s73, v224\n"
  "v_mfma_f32_16x16x32_bf16 a[216:219], v[60:63], a[132:135], a[216:219]\n"
  "s_addk_i32 m0, 896\n"
  "ds_read_b64_tr_b16 a[120:121], v219 offset:16\n"
  "v_fma_f32 v17, v17, s73, v224\n"
  "v_mfma_f32_16x16x32_bf16 a[200:203], v[40:43], a[128:131], 1.0\n"
  "ds_read_b64_tr_b16 a[122:123], v220 offset:16\n"
  "v_fma_f32 v18, v18, s73, v224\n"
  "v_mfma_f32_16x16x32_bf16 a[200:203], v[44:47], a[132:135], a[200:203]\n"
  "s_nop 0\n"
  "ds_read_b64_tr_b16 a[124:125], v219 offset:1040\n"
  "v_fma_f32 v19, v19, s73, v224\n"
  "v_mfma_f32_16x16x32_bf16 a[184:187], v[24:27], a[128:131], 1.0\n"
  "buffer_load_dwordx4 v221, s[40:43], 0 offen offset:256 lds\n"
  "v_fma_f32 v32, v32, s73, v225\n"
  "v_mfma_f32_16x16x32_bf16 a[184:187], v[28:31], a[132:135], a[184:187]\n"
  "s_mov_b32 m0, s60\n"
  "ds_read_b64_tr_b16 a[126:127], v220 offset:1040\n"
  "v_fma_f32 v33, v33, s73, v225\n"
  "v_mfma_f32_16x16x32_bf16 a[168:171], v[8:11], a[128:131], 1.0\n"
  "s_add_u32 s28, s96, 184448\n"
  "v_exp_f32 v0, v0\n"
  "v_mfma_f32_16x16x32_bf16 a[168:171], v[12:15], a[132:135], a[168:171]\n"
  "s_waitcnt lgkmcnt(5)\n"
  "v_exp_f32 v1, v1\n"
  "v_mfma_f32_16x16x32_bf16 a[172:175], v[8:11], a[136:139], 1.0\n"
  "buffer_load_dwordx4 v221, s[32:35], 0 offen offset:0 lds\n"
  "v_add_f32_e64 v252, v0, v1\n"
  "v_mfma_f32_16x16x32_bf16 a[172:175], v[12:15], a[140:143], a[172:175]\n"
  "s_addk_i32 m0, 1024\n"
  "v_exp_f32 v2, v2\n"
  "v_mfma_f32_16x16x32_bf16 a[188:191], v[24:27], a[136:139], 1.0\n"
  "s_add_u32 s40, s40, s31\n"
  "v_exp_f32 v3, v3\n"
  "v_mfma_f32_16x16x32_bf16 a[188:191], v[28:31], a[140:143], a[188:191]\n"
  "s_addc_u32 s41, s41, 0\n"
  "v_exp_f32 v16, v16\n"
  "v_mfma_f32_16x16x32_bf16 a[204:207], v[40:43], a[136:139], 1.0\n"
  "buffer_load_dwordx4 v221, s[44:47], 0 offen offset:0 lds\n"
  "v_add_f32 v252, v252, v2\n"
  "v_mfma_f32_16x16x32_bf16 a[204:207], v[44:47], a[140:143], a[204:207]\n"
  "s_addk_i32 m0, 768\n"
  "v_exp_f32 v17, v17\n"
  "v_mfma_f32_16x16x32_bf16 a[220:223], v[56:59], a[136:139], 1.0\n"
  "s_mov_b64 s[32:33], s[44:45]\n"
  "v_exp_f32 v18, v18\n"
  "v_mfma_f32_16x16x32_bf16 a[220:223], v[60:63], a[140:143], a[220:223]\n"
  "s_waitcnt lgkmcnt(0)\n"
  "v_exp_f32_e64 v19, v19\n"
  "v_mfma_f32_16x16x32_bf16 a[224:227], v[56:59], a[120:123], 1.0\n"
  "buffer_load_dwordx4 v221, s[44:47], 0 offen offset:256 lds\n"
  "v_add_f32 v252, v252, v3\n"
  "v_mfma_f32_16x16x32_bf16 a[224:227], v[60:63], a[124:127], a[224:227]\n"
  "s_add_u32 s44, s44, s31\n"
  "v_exp_f32 v32, v32\n"
  "v_mfma_f32_16x16x32_bf16 a[208:211], v[40:43], a[120:123], 1.0\n"
  "s_addc_u32 s45, s45, 0\n"
  "v_exp_f32 v33, v33\n"
  "v_mfma_f32_16x16x32_bf16 a[208:211], v[44:47], a[124:127], a[208:211]\n"
  "s_cmp_eq_u32 s95, 0\n"
  "s_cselect_b32 s95, 3, s95\n"
  "s_subb_u32 s100, s100, -11\n"
  "v_add_f32 v253, v16, v17\n"
  "v_add_f32 v253, v253, v18\n"
  "v_mfma_f32_16x16x32_bf16 a[192:195], v[24:27], a[120:123], 1.0\n"
  "s_sub_u32 s95, s95, 1\n"
  "v_add_f32 v253, v253, v19\n"
  "v_fma_f32 v34, v34, s73, v225\n"
  "v_mfma_f32_16x16x32_bf16 a[192:195], v[28:31], a[124:127], a[192:195]\n"
  "s_waitcnt vmcnt(6)\n"
  "s_barrier\n"
  "ds_read_b128 a[128:131], v216 offset:12352\n"
  "v_mfma_f32_16x16x32_bf16 a[176:179], v[8:11], a[120:123], 1.0\n"
  "s_lshr_b32 s79, s100, 0\n"
  "ds_read_b128 a[132:135], v216 offset:12416\n"
  "v_fma_f32 v35, v35, s73, v225\n"
  "v_mfma_f32_16x16x32_bf16 a[176:179], v[12:15], a[124:127], a[176:179]\n"
  "s_mul_i32 s79, s79, 7\n"
  "ds_read_b128 a[120:123], v216 offset:13376\n"
  "v_add_f32 v254, v32, v33\n"
  "v_mfma_f32_16x16x32_bf16 v[68:71], v[8:11], a[152:155], 0\n"
  "s_add_u32 s79, s79, s100\n"
  "ds_read_b128 a[124:127], v216 offset:13440\n"
  "v_cvt_pk_bf16_f32 v8, v0, v1\n"
  "v_mfma_f32_16x16x32_bf16 v[68:71], v[12:15], a[152:155], v[68:71]\n"
  "s_add_u32 s79, s79, s101\n"
  "ds_read_b128 a[136:139], v216 offset:14400\n"
  "v_cvt_pk_bf16_f32 v9, v2, v3\n"
  "v_mfma_f32_16x16x32_bf16 v[72:75], v[24:27], a[152:155], 0\n"
  "ds_read_b128 a[140:143], v216 offset:14464\n"
  "v_cvt_pk_bf16_f32 v24, v16, v17\n"
  "v_mfma_f32_16x16x32_bf16 v[72:75], v[28:31], a[152:155], v[72:75]\n"
  "s_mov_b32 m0, s66\n"
  "v_exp_f32 v34, v34\n"
  "v_mfma_f32_16x16x32_bf16 v[76:79], v[40:43], a[152:155], 0\n"
  "s_min_u32 s79, s79, 0x1ff\n"
  "v_exp_f32 v35, v35\n"
  "v_mfma_f32_16x16x32_bf16 v[76:79], v[44:47], a[152:155], v[76:79]\n"
  "s_mul_i32 s96, s79, 196608\n"
  "v_cvt_pk_bf16_f32 v25, v18, v19\n"
  "v_add_f32 v254, v254, v34\n"
  "v_mfma_f32_16x16x32_bf16 v[80:83], v[56:59], a[152:155], 0\n"
  "s_mul_i32 s92, s79, 325632\n"
  "v_add_f32 v254, v254, v35\n"
  "v_cvt_pk_bf16_f32 v40, v32, v33\n"
  "v_mfma_f32_16x16x32_bf16 v[80:83], v[60:63], a[152:155], v[80:83]\n"
  "buffer_load_dwordx4 v221, s[40:43], 0 offen offset:0 lds\n"
  "v_mfma_f32_16x16x32_bf16 v[84:87], a[156:159], a[152:155], 0\n"
  "s_add_u32 s96, s96, s39\n"
  "ds_read_b64 a[156:157], v231 offset:0\n"
  "v_cvt_pk_bf16_f32 v41, v34, v35\n"
  "v_mfma_f32_16x16x32_bf16 v[84:87], a[160:163], a[152:155], v[84:87]\n"
  "s_branch L_CUT_%=\n"
  ".p2align 5\n"
  "L_BIG_%=:\n"
  "v_mfma_f32_16x16x32_bf16 v[16:19], a[120:123], a[24:27], v[248:251]\n"
  "s_mov_b32 m0, s60\n"
  "v_fma_f32 v64, v64, s73, v227\n"
  "v_fma_f32 v65, v65, s73, v227\n"
  "v_mfma_f32_16x16x32_bf16 v[16:19], a[124:127], a[28:31], v[16:19]\n"
  "buffer_load_dwordx4 v221, s[32:35], 0 offen offset:0 lds\n"
  "v_fma_f32 v66, v66, s73, v227\n"
  "v_mfma_f32_16x16x32_bf16 v[16:19], a[128:131], a[32:35], v[16:19]\n"
  "s_addk_i32 m0, 1024\n"
  "ds_read_b64_tr_b16 a[144:145], v217 offset:0\n"
  "v_fma_f32 v67, v67, s73, v227\n"
  "v_mfma_f32_16x16x32_bf16 v[16:19], a[132:135], a[36:39], v[16:19]\n"
  "s_mov_b64 s[36:37], s[44:45]\n"
  "ds_read_b64_tr_b16 a[146:147], v218 offset:0\n"
  "v_fma_f32 v0, v0, s73, v223\n"
  "v_mfma_f32_16x16x32_bf16 v[16:19], a[136:139], a[40:43], v[16:19]\n"
  "ds_read_b64_tr_b16 a[148:149], v217 offset:1024\n"
  "v_fma_f32 v1, v1, s73, v223\n"
  "v_mfma_f32_16x16x32_bf16 v[16:19], a[140:143], a[44:47], v[16:19]\n"
  "buffer_load_dwordx4 v221, s[44:47], 0 offen offset:0 lds\n"
  "v_fma_f32 v2, v2, s73, v223\n"
  "v_mfma_f32_16x16x32_bf16 v[32:35], a[140:143], a[68:71], v[248:251]\n"
  "s_addk_i32 m0, 768\n"
  "ds_read_b64_tr_b16 a[150:151], v218 offset:1024\n"
  "v_fma_f32 v3, v3, s73, v223\n"
  "v_mfma_f32_16x16x32_bf16 v[32:35], a[136:139], a[64:67], v[32:35]\n"
  "s_add_u32 s32, s88, s28\n"
  "v_exp_f32 v64, v64\n"
  "v_mfma_f32_16x16x32_bf16 v[32:35], a[132:135], a[60:63], v[32:35]\n"
  "s_addc_u32 s33, s89, 0\n"
  "v_exp_f32 v65, v65\n"
  "v_mfma_f32_16x16x32_bf16 v[32:35], a[128:131], a[56:59], v[32:35]\n"
  "buffer_load_dwordx4 v221, s[44:47], 0 offen offset:256 lds\n"
  "v_cvt_pk_bf16_f32 v64, v64, v65\n"
  "v_mfma_f32_16x16x32_bf16 v[32:35], a[124:127], a[52:55], v[32:35]\n"
  "s_add_u32 s44, s32, s31\n"
  "v_exp_f32 v66, v66\n"
  "v_mfma_f32_16x16x32_bf16 v[32:35], a[120:123], a[48:51], v[32:35]\n"
  "s_addc_u32 s45, s33, 0\n"
  "v_exp_f32 v67, v67\n"
  "v_mfma_f32_16x16x32_bf16 v[48:51], a[120:123], a[72:75], v[248:251]\n"
  "s_mov_b32 s46, s30\n"
  "v_exp_f32 v0, v0\n"
  "v_mfma_f32_16x16x32_bf16 v[48:51], a[124:127], a[76:79], v[48:51]\n"
  "v_exp_f32 v1, v1\n"
  "v_mfma_f32_16x16x32_bf16 v[48:51], a[128:131], a[80:83], v[48:51]\n"
  "ds_read_b64_tr_b16 a[120:121], v217 offset:16\n"
  "v_cvt_pk_bf16_f32 v65, v66, v67\n"
  "v_mfma_f32_16x16x32_bf16 v[48:51], a[132:135], a[84:87], v[48:51]\n"
  "ds_read_b64_tr_b16 a[122:123], v218 offset:16\n"
  "v_add_f32 v252, v252, v0\n"
  "v_mfma_f32_16x16x32_bf16 v[48:51], a[136:139], a[88:91], v[48:51]\n"
  "ds_read_b64_tr_b16 a[124:125], v217 offset:1040\n"
  "v_add_f32 v252, v252, v1\n"
  "v_mfma_f32_16x16x32_bf16 v[48:51], a[140:143], a[92:95], v[48:51]\n"
  "s_waitcnt lgkmcnt(3)\n"
  "ds_read_b64_tr_b16 a[126:127], v218 offset:1040\n"
  "v_fma_f32 v16, v16, s73, v224\n"
  "v_mfma_f32_16x16x32_bf16 a[228:231], a[156:159], a[144:147], a[228:231]\n"
  "v_exp_f32 v2, v2\n"
  "v_mfma_f32_16x16x32_bf16 a[228:231], a[160:163], a[148:151], a[228:231]\n"
  "v_exp_f32 v3, v3\n"
  "v_mfma_f32_16x16x32_bf16 a[212:215], v[56:59], a[144:147], a[212:215]\n"
  "v_exp_f32 v16, v16\n"
  "v_mfma_f32_16x16x32_bf16 a[212:215], v[60:63], a[148:151], a[212:215]\n"
  "v_add_f32 v252, v252, v2\n"
  "v_add_f32 v252, v252, v3\n"
  "v_mfma_f32_16x16x32_bf16 a[196:199], v[40:43], a[144:147], a[196:199]\n"
  "v_fma_f32 v17, v17, s73, v224\n"
  "v_fma_f32 v18, v18, s73, v224\n"
  "v_mfma_f32_16x16x32_bf16 a[196:199], v[44:47], a[148:151], a[196:199]\n"
  "v_exp_f32 v17, v17\n"
  "v_mfma_f32_16x16x32_bf16 a[180:183], v[24:27], a[144:147], a[180:183]\n"
  "ds_read_b64_tr_b16 a[128:129], v219 offset:0\n"
  "v_fma_f32 v19, v19, s73, v224\n"
  "v_mfma_f32_16x16x32_bf16 a[180:183], v[28:31], a[148:151], a[180:183]\n"
  "s_waitcnt vmcnt(14)\n"
  "ds_read_b64_tr_b16 a[130:131], v220 offset:0\n"
  "v_add_f32 v253, v253, v16\n"
  "v_mfma_f32_16x16x32_bf16 a[164:167], v[8:11], a[144:147], a[164:167]\n"
  "s_waitcnt vmcnt(12)\n"
  "ds_read_b64_tr_b16 a[132:133], v219 offset:1024\n"
  "v_add_f32 v253, v253, v17\n"
  "v_mfma_f32_16x16x32_bf16 a[164:167], v[12:15], a[148:151], a[164:167]\n"
  "ds_read_b64_tr_b16 a[134:135], v220 offset:1024\n"
  "v_fma_f32 v32, v32, s73, v225\n"
  "v_mfma_f32_16x16x32_bf16 v[84:87], a[156:159], a[152:155], v[84:87]\n"
  "v_exp_f32 v18, v18\n"
  "v_mfma_f32_16x16x32_bf16 v[84:87], a[160:163], a[152:155], v[84:87]\n"
  "s_waitcnt lgkmcnt(4)\n"
  "v_exp_f32 v19, v19\n"
  "v_mfma_f32_16x16x32_bf16 a[168:171], v[8:11], a[120:123], a[168:171]\n"
  "v_exp_f32 v32, v32\n"
  "v_mfma_f32_16x16x32_bf16 a[168:171], v[12:15], a[124:127], a[168:171]\n"
  "v_add_f32 v253, v253, v18\n"
  "v_add_f32 v253, v253, v19\n"
  "v_mfma_f32_16x16x32_bf16 a[184:187], v[24:27], a[120:123], a[184:187]\n"
  "ds_read_b128 a[136:139], v235 offset:0\n"
  "v_fma_f32 v33, v33, s73, v225\n"
  "v_mfma_f32_16x16x32_bf16 a[184:187], v[28:31], a[124:127], a[184:187]\n"
  "ds_read_b128 a[140:143], v235 offset:64\n"
  "v_fma_f32 v34, v34, s73, v225\n"
  "v_mfma_f32_16x16x32_bf16 a[200:203], v[40:43], a[120:123], a[200:203]\n"
  "ds_read_b128 a[144:147], v235 offset:1024\n"
  "v_fma_f32 v35, v35, s73, v225\n"
  "v_mfma_f32_16x16x32_bf16 a[200:203], v[44:47], a[124:127], a[200:203]\n"
  "ds_read_b128 a[148:151], v235 offset:1088\n"
  "v_add_f32 v254, v254, v32\n"
  "v_mfma_f32_16x16x32_bf16 a[216:219], v[56:59], a[120:123], a[216:219]\n"
  "v_exp_f32 v33, v33\n"
  "v_mfma_f32_16x16x32_bf16 a[216:219], v[60:63], a[124:127], a[216:219]\n"
  "v_exp_f32 v34, v34\n"
  "v_mfma_f32_16x16x32_bf16 a[232:235], a[156:159], a[120:123], a[232:235]\n"
  "ds_read_b128 a[120:123], v235 offset:2048\n"
  "v_add_f32 v254, v254, v33\n"
  "v_mfma_f32_16x16x32_bf16 a[232:235], a[160:163], a[124:127], a[232:235]\n"
  "s_waitcnt lgkmcnt(5)\n"
  "ds_read_b128 a[124:127], v235 offset:2112\n"
  "v_add_f32 v254, v254, v34\n"
  "v_mfma_f32_16x16x32_bf16 a[220:223], v[56:59], a[128:131], a[220:223]\n"
  "v_exp_f32 v35, v35\n"
  "v_mfma_f32_16x16x32_bf16 a[220:223], v[60:63], a[132:135], a[220:223]\n"
  "ds_write_b64 v231, v[64:65] offset:0\n"
  "v_mfma_f32_16x16x32_bf16 a[204:207], v[40:43], a[128:131], a[204:207]\n"
  "v_add_f32 v254, v254, v35\n"
  "v_fma_f32 v48, v48, s73, v226\n"
  "v_mfma_f32_16x16x32_bf16 a[204:207], v[44:47], a[132:135], a[204:207]\n"
  "v_exp_f32 v48, v48\n"
  "v_mfma_f32_16x16x32_bf16 a[188:191], v[24:27], a[128:131], a[188:191]\n"
  "v_fma_f32 v49, v49, s73, v226\n"
  "v_fma_f32 v50, v50, s73, v226\n"
  "v_mfma_f32_16x16x32_bf16 a[188:191], v[28:31], a[132:135], a[188:191]\n"
  "v_exp_f32 v49, v49\n"
  "v_mfma_f32_16x16x32_bf16 a[172:175], v[8:11], a[128:131], a[172:175]\n"
  "v_exp_f32 v50, v50\n"
  "v_mfma_f32_16x16x32_bf16 a[172:175], v[12:15], a[132:135], a[172:175]\n"
  "s_waitcnt lgkmcnt(5)\n"
  "v_fma_f32 v51, v51, s73, v226\n"
  "v_add_f32 v255, v255, v48\n"
  "v_mfma_f32_16x16x32_bf16 v[4:7], a[136:139], a[0:3], v[248:251]\n"
  "v_exp_f32 v51, v51\n"
  "v_mfma_f32_16x16x32_bf16 v[4:7], a[140:143], a[4:7], v[4:7]\n"
  "s_waitcnt lgkmcnt(3)\n"
  "v_add_f32 v255, v255, v49\n"
  "v_add_f32 v255, v255, v50\n"
  "v_mfma_f32_16x16x32_bf16 v[4:7], a[144:147], a[8:11], v[4:7]\n"
  "v_add_f32 v255, v255, v51\n"
  "v_mov_b32 v237, 0\n"
  "v_mfma_f32_16x16x32_bf16 v[4:7], a[148:151], a[12:15], v[4:7]\n"
  "s_waitcnt lgkmcnt(1)\n"
  "v_mov_b32 v238, 0\n"
  "v_mov_b32 v239, 0\n"
  "v_mfma_f32_16x16x32_bf16 v[4:7], a[120:123], a[16:19], v[4:7]\n"
  "v_mov_b32 v241, 0\n"
  "v_mov_b32 v242, 0\n"
  "v_mfma_f32_16x16x32_bf16 v[4:7], a[124:127], a[20:23], v[4:7]\n"
  "v_mov_b32 v243, 0\n"
  "v_bfe_u32 v234, v231, 1, 6\n"
  "v_mfma_f32_16x16x32_bf16 v[20:23], a[124:127], a[44:47], v[248:251]\n"
  "v_mfma_f32_16x16x32_bf16 v[20:23], a[120:123], a[40:43], v[20:23]\n"
  "v_mfma_f32_16x16x32_bf16 v[20:23], a[148:151], a[36:39], v[20:23]\n"
  "ds_read_b64_tr_b16 a[128:129], v219 offset:16\n"
  "v_fma_f32 v4, v4, s73, v223\n"
  "v_mfma_f32_16x16x32_bf16 v[20:23], a[144:147], a[32:35], v[20:23]\n"
  "ds_read_b64_tr_b16 a[130:131], v220 offset:16\n"
  "v_fma_f32 v5, v5, s73, v223\n"
  "v_mfma_f32_16x16x32_bf16 v[20:23], a[140:143], a[28:31], v[20:23]\n"
  "ds_read_b64_tr_b16 a[132:133], v219 offset:1040\n"
  "v_fma_f32 v6, v6, s73, v223\n"
  "v_mfma_f32_16x16x32_bf16 v[20:23], a[136:139], a[24:27], v[20:23]\n"
  "ds_read_b64_tr_b16 a[134:135], v220 offset:1040\n"
  "v_fma_f32 v7, v7, s73, v223\n"
  "v_mfma_f32_16x16x32_bf16 v[36:39], a[136:139], a[48:51], v[248:251]\n"
  "v_exp_f32 v4, v4\n"
  "v_mfma_f32_16x16x32_bf16 v[36:39], a[140:143], a[52:55], v[36:39]\n"
  "v_exp_f32 v5, v5\n"
  "v_mfma_f32_16x16x32_bf16 v[36:39], a[144:147], a[56:59], v[36:39]\n"
  "v_exp_f32 v6, v6\n"
  "v_mfma_f32_16x16x32_bf16 v[36:39], a[148:151], a[60:63], v[36:39]\n"
  "v_exp_f32 v7, v7\n"
  "v_mfma_f32_16x16x32_bf16 v[36:39], a[120:123], a[64:67], v[36:39]\n"
  "v_add_f32 v252, v252, v4\n"
  "v_add_f32 v252, v252, v5\n"
  "v_mfma_f32_16x16x32_bf16 v[36:39], a[124:127], a[68:71], v[36:39]\n"
  "v_add_f32 v252, v252, v6\n"
  "v_add_f32 v252, v252, v7\n"
  "v_mfma_f32_16x16x32_bf16 v[52:55], a[124:127], a[92:95], v[248:251]\n"
  "ds_read_b64_tr_b16 a[124:125], v217 offset:2048\n"
  "v_fma_f32 v20, v20, s73, v224\n"
  "v_mfma_f32_16x16x32_bf16 v[52:55], a[120:123], a[88:91], v[52:55]\n"
  "ds_read_b64_tr_b16 a[126:127], v218 offset:2048\n"
  "v_fma_f32 v21, v21, s73, v224\n"
  "v_mfma_f32_16x16x32_bf16 v[52:55], a[148:151], a[84:87], v[52:55]\n"
  "ds_read_b64_tr_b16 a[120:121], v217 offset:2064\n"
  "v_fma_f32 v22, v22, s73, v224\n"
  "v_mfma_f32_16x16x32_bf16 v[52:55], a[144:147], a[80:83], v[52:55]\n"
  "ds_read_b64_tr_b16 a[122:123], v218 offset:2064\n"
  "v_fma_f32 v23, v23, s73, v224\n"
  "v_mfma_f32_16x16x32_bf16 v[52:55], a[140:143], a[76:79], v[52:55]\n"
  "v_exp_f32 v20, v20\n"
  "v_mfma_f32_16x16x32_bf16 v[52:55], a[136:139], a[72:75], v[52:55]\n"
  "s_waitcnt lgkmcnt(4)\n"
  "v_exp_f32 v21, v21\n"
  "v_mfma_f32_16x16x32_bf16 a[176:179], v[8:11], a[128:131], a[176:179]\n"
  "v_cvt_pk_bf16_f32 v8, v0, v1\n"
  "v_cvt_pk_bf16_f32 v9, v2, v3\n"
  "v_mfma_f32_16x16x32_bf16 a[176:179], v[12:15], a[132:135], a[176:179]\n"
  "v_exp_f32 v22, v22\n"
  "v_mfma_f32_16x16x32_bf16 a[192:195], v[24:27], a[128:131], a[192:195]\n"
  "ds_read_b64_tr_b16 a[140:141], v219 offset:2048\n"
  "v_cvt_pk_bf16_f32 v24, v16, v17\n"
  "v_mfma_f32_16x16x32_bf16 a[192:195], v[28:31], a[132:135], a[192:195]\n"
  "ds_read_b64_tr_b16 a[142:143], v220 offset:2048\n"
  "v_cvt_pk_bf16_f32 v25, v18, v19\n"
  "v_mfma_f32_16x16x32_bf16 a[208:211], v[40:43], a[128:131], a[208:211]\n"
  "ds_read_b64_tr_b16 a[136:137], v219 offset:2064\n"
  "v_cvt_pk_bf16_f32 v40, v32, v33\n"
  "v_mfma_f32_16x16x32_bf16 a[208:211], v[44:47], a[132:135], a[208:211]\n"
  "ds_read_b64_tr_b16 a[138:139], v220 offset:2064\n"
  "v_cvt_pk_bf16_f32 v41, v34, v35\n"
  "v_mfma_f32_16x16x32_bf16 a[224:227], v[56:59], a[128:131], a[224:227]\n"
  "v_exp_f32 v23, v23\n"
  "v_mfma_f32_16x16x32_bf16 a[224:227], v[60:63], a[132:135], a[224:227]\n"
  "s_waitcnt lgkmcnt(4)\n"
  "v_cvt_pk_bf16_f32 v56, v48, v49\n"
  "v_cvt_pk_bf16_f32 v57, v50, v51\n"
  "v_mfma_f32_16x16x32_bf16 a[244:247], a[160:163], a[124:127], a[244:247]\n"
  "v_cvt_pk_bf16_f32 v10, v4, v5\n"
  "v_cvt_pk_bf16_f32 v11, v6, v7\n"
  "v_mfma_f32_16x16x32_bf16 a[248:251], a[160:163], a[120:123], a[248:251]\n"
  "v_add_f32 v253, v253, v20\n"
  "v_add_f32 v253, v253, v21\n"
  "v_mfma_f32_16x16x32_bf16 v[200:203], v[60:63], a[124:127], v[200:203]\n"
  "v_add_f32 v253, v253, v22\n"
  "v_add_f32 v253, v253, v23\n"
  "v_mfma_f32_16x16x32_bf16 v[204:207], v[60:63], a[120:123], v[204:207]\n"
  "v_cvt_pk_bf16_f32 v26, v20, v21\n"
  "v_cvt_pk_bf16_f32 v27, v22, v23\n"
  "v_mfma_f32_16x16x32_bf16 v[168:171], v[44:47], a[124:127], v[168:171]\n"
  "v_fma_f32 v36, v36, s73, v225\n"
  "v_fma_f32 v37, v37, s73, v225\n"
  "v_mfma_f32_16x16x32_bf16 v[172:175], v[44:47], a[120:123], v[172:175]\n"
  "s_waitcnt vmcnt(6) lgkmcnt(0)\n"
  "s_barrier\n"
  "ds_read_b128 a[128:131], v216 offset:12352\n"
  "v_mfma_f32_16x16x32_bf16 v[136:139], v[28:31], a[124:127], v[136:139]\n"
  "ds_read_b64 a[156:157], v231 offset:0\n"
  "v_fma_f32 v38, v38, s73, v225\n"
  "v_mfma_f32_16x16x32_bf16 v[140:143], v[28:31], a[120:123], v[140:143]\n"
  "ds_read_b64 a[158:159], v232 offset:0\n"
  "v_fma_f32 v39, v39, s73, v225\n"
  "v_mfma_f32_16x16x32_bf16 v[104:107], v[12:15], a[124:127], v[104:107]\n"
  "ds_read_b128 a[132:135], v216 offset:12416\n"
  "v_fma_f32 v52, v52, s73, v226\n"
  "v_mfma_f32_16x16x32_bf16 v[108:111], v[12:15], a[120:123], v[108:111]\n"
  "ds_read_b128 a[124:127], v216 offset:13376\n"
  "v_fma_f32 v53, v53, s73, v226\n"
  "v_mfma_f32_16x16x32_bf16 v[112:115], v[12:15], a[140:143], v[112:115]\n"
  "ds_read_b128 a[120:123], v216 offset:13440\n"
  "v_fma_f32 v54, v54, s73, v226\n"
  "v_mfma_f32_16x16x32_bf16 v[116:119], v[12:15], a[136:139], v[116:119]\n"
  "s_mov_b32 m0, s66\n"
  "ds_read_b128 a[148:151], v216 offset:14400\n"
  "v_fma_f32 v55, v55, s73, v226\n"
  "v_mfma_f32_16x16x32_bf16 v[144:147], v[28:31], a[140:143], v[144:147]\n"
  "ds_read_b128 a[144:147], v216 offset:14464\n"
  "v_and_b32 v236, 0xffff0000, v252\n"
  "v_mfma_f32_16x16x32_bf16 v[148:151], v[28:31], a[136:139], v[148:151]\n"
  "v_exp_f32 v36, v36\n"
  "v_mfma_f32_16x16x32_bf16 v[176:179], v[44:47], a[140:143], v[176:179]\n"
  "v_exp_f32 v37, v37\n"
  "v_mfma_f32_16x16x32_bf16 v[180:183], v[44:47], a[136:139], v[180:183]\n"
  "buffer_load_dwordx4 v221, s[40:43], 0 offen offset:0 lds\n"
  "v_mfma_f32_16x16x32_bf16 v[208:211], v[60:63], a[140:143], v[208:211]\n"
  "s_addk_i32 m0, 896\n"
  "v_exp_f32 v38, v38\n"
  "v_mfma_f32_16x16x32_bf16 v[212:215], v[60:63], a[136:139], v[212:215]\n"
  "s_waitcnt lgkmcnt(4)\n"
  "v_exp_f32 v39, v39\n"
  "v_mfma_f32_16x16x32_bf16 v[64:67], a[128:131], a[96:99], v[248:251]\n"
  "v_exp_f32 v52, v52\n"
  "v_mfma_f32_16x16x32_bf16 v[64:67], a[132:135], a[100:103], v[64:67]\n"
  "s_waitcnt lgkmcnt(2)\n"
  "buffer_load_dwordx4 v221, s[40:43], 0 offen offset:128 lds\n"
  "v_add_f32 v254, v254, v36\n"
  "v_mfma_f32_16x16x32_bf16 v[64:67], a[124:127], a[104:107], v[64:67]\n"
  "s_addk_i32 m0, 896\n"
  "v_exp_f32 v53, v53\n"
  "v_mfma_f32_16x16x32_bf16 v[64:67], a[120:123], a[108:111], v[64:67]\n"
  "s_waitcnt lgkmcnt(0)\n"
  "v_exp_f32 v54, v54\n"
  "v_mfma_f32_16x16x32_bf16 v[64:67], a[148:151], a[112:115], v[64:67]\n"
  "v_exp_f32 v55, v55\n"
  "v_mfma_f32_16x16x32_bf16 v[64:67], a[144:147], a[116:119], v[64:67]\n"
  "buffer_load_dwordx4 v221, s[40:43], 0 offen offset:256 lds\n"
  "v_add_f32 v254, v254, v37\n"
  "v_mfma_f32_16x16x32_bf16 v[0:3], a[144:147], a[20:23], v[248:251]\n"
  "s_add_u32 s40, s40, -12288\n"
  "v_add_f32 v254, v254, v38\n"
  "v_add_f32 v254, v254, v39\n"
  "v_mfma_f32_16x16x32_bf16 v[0:3], a[148:151], a[16:19], v[0:3]\n"
  "s_addc_u32 s41, s41, -1\n"
  "v_cvt_pk_bf16_f32 v42, v36, v37\n"
  "v_cvt_pk_bf16_f32 v43, v38, v39\n"
  "v_mfma_f32_16x16x32_bf16 v[0:3], a[120:123], a[12:15], v[0:3]\n"
  "v_add_f32 v255, v255, v52\n"
  "v_add_f32 v255, v255, v53\n"
  "v_mfma_f32_16x16x32_bf16 v[0:3], a[124:127], a[8:11], v[0:3]\n"
  "s_mov_b32 m0, s58\n"
  "v_add_f32 v255, v255, v54\n"
  "v_add_f32 v255, v255, v55\n"
  "v_mfma_f32_16x16x32_bf16 v[0:3], a[132:135], a[4:7], v[0:3]\n"
  "v_cvt_pk_bf16_f32 v58, v52, v53\n"
  "v_cvt_pk_bf16_f32 v59, v54, v55\n"
  "v_mfma_f32_16x16x32_bf16 v[0:3], a[128:131], a[0:3], v[0:3]\n"
  "v_sub_f32 v252, v252, v236\n"
  "v_or_b32_sdwa v236, v236, v252 dst_sel:DWORD dst_unused:UNUSED_PAD src0_sel:DWORD src1_sel:WORD_1\n"
  "v_mfma_f32_16x16x32_bf16 v[16:19], a[128:131], a[24:27], v[248:251]\n"
  "s_add_u32 s71, s92, s83\n"
  "v_and_b32 v240, 0xffff0000, v253\n"
  "v_sub_f32 v253, v253, v240\n"
  "v_mfma_f32_16x16x32_bf16 v[16:19], a[132:135], a[28:31], v[16:19]\n"
  "buffer_load_dwordx4 v221, s[32:35], 0 offen offset:0 lds\n"
  "v_or_b32_sdwa v240, v240, v253 dst_sel:DWORD dst_unused:UNUSED_PAD src0_sel:DWORD src1_sel:WORD_1\n"
  "v_mfma_f32_16x16x32_bf16 v[16:19], a[124:127], a[32:35], v[16:19]\n"
  "s_addk_i32 m0, 1024\n"
  "ds_read_b128 a[140:143], v235 offset:12352\n"
  "v_fma_f32 v64, v64, s73, v227\n"
  "v_mfma_f32_16x16x32_bf16 v[16:19], a[120:123], a[36:39], v[16:19]\n"
  "s_mov_b64 s[32:33], s[36:37]\n"
  "ds_read_b128 a[136:139], v235 offset:12416\n"
  "v_fma_f32 v65, v65, s73, v227\n"
  "v_mfma_f32_16x16x32_bf16 v[16:19], a[148:151], a[40:43], v[16:19]\n"
  "v_exp_f32 v64, v64\n"
  "v_mfma_f32_16x16x32_bf16 v[16:19], a[144:147], a[44:47], v[16:19]\n"
  "buffer_load_dwordx4 v221, s[44:47], 0 offen offset:0 lds\n"
  "v_fma_f32 v66, v66, s73, v227\n"
  "v_mfma_f32_16x16x32_bf16 v[32:35], a[144:147], a[68:71], v[248:251]\n"
  "s_addk_i32 m0, 768\n"
  "v_exp_f32 v65, v65\n"
  "v_mfma_f32_16x16x32_bf16 v[32:35], a[148:151], a[64:67], v[32:35]\n"
  "v_exp_f32 v66, v66\n"
  "v_mfma_f32_16x16x32_bf16 v[32:35], a[120:123], a[60:63], v[32:35]\n"
  "v_fma_f32 v67, v67, s73, v227\n"
  "v_cvt_pk_bf16_f32 v64, v64, v65\n"
  "v_mfma_f32_16x16x32_bf16 v[32:35], a[124:127], a[56:59], v[32:35]\n"
  "buffer_load_dwordx4 v221, s[44:47], 0 offen offset:256 lds\n"
  "v_fma_f32 v0, v0, s73, v223\n"
  "v_mfma_f32_16x16x32_bf16 v[32:35], a[132:135], a[52:55], v[32:35]\n"
  "s_add_u32 s44, s44, -12288\n"
  "v_exp_f32 v67, v67\n"
  "v_mfma_f32_16x16x32_bf16 v[32:35], a[128:131], a[48:51], v[32:35]\n"
  "s_addc_u32 s45, s45, -1\n"
  "v_exp_f32 v0, v0\n"
  "v_mfma_f32_16x16x32_bf16 v[48:51], a[128:131], a[72:75], v[248:251]\n"
  "ds_read_b128 a[128:131], v235 offset:13376\n"
  "v_cvt_pk_bf16_f32 v65, v66, v67\n"
  "v_mfma_f32_16x16x32_bf16 v[48:51], a[132:135], a[76:79], v[48:51]\n"
  "ds_read_b128 a[132:135], v235 offset:13440\n"
  "v_fma_f32 v1, v1, s73, v223\n"
  "v_mfma_f32_16x16x32_bf16 v[48:51], a[124:127], a[80:83], v[48:51]\n"
  "ds_read_b128 a[124:127], v235 offset:14400\n"
  "v_fma_f32 v2, v2, s73, v223\n"
  "v_mfma_f32_16x16x32_bf16 v[48:51], a[120:123], a[84:87], v[48:51]\n"
  "ds_read_b128 a[120:123], v235 offset:14464\n"
  "v_fma_f32 v3, v3, s73, v223\n"
  "v_mfma_f32_16x16x32_bf16 v[48:51], a[148:151], a[88:91], v[48:51]\n"
  "v_exp_f32 v1, v1\n"
  "v_mfma_f32_16x16x32_bf16 v[48:51], a[144:147], a[92:95], v[48:51]\n"
  "s_waitcnt lgkmcnt(4)\n"
  "v_exp_f32 v2, v2\n"
  "v_mfma_f32_16x16x32_bf16 v[4:7], a[140:143], a[0:3], v[248:251]\n"
  "ds_read_b128 a[0:3], v222 offset:0\n"
  "v_fma_f32 v16, v16, s73, v224\n"
  "v_mfma_f32_16x16x32_bf16 v[4:7], a[136:139], a[4:7], v[4:7]\n"
  "s_waitcnt lgkmcnt(3)\n"
  "ds_read_b128 a[4:7], v222 offset:64\n"
  "v_fma_f32 v17, v17, s73, v224\n"
  "v_mfma_f32_16x16x32_bf16 v[4:7], a[128:131], a[8:11], v[4:7]\n"
  "ds_read_b128 a[8:11], v222 offset:2240\n"
  "v_fma_f32 v18, v18, s73, v224\n"
  "v_mfma_f32_16x16x32_bf16 v[4:7], a[132:135], a[12:15], v[4:7]\n"
  "s_waitcnt lgkmcnt(3)\n"
  "ds_read_b128 a[12:15], v222 offset:2304\n"
  "v_fma_f32 v19, v19, s73, v224\n"
  "v_mfma_f32_16x16x32_bf16 v[4:7], a[124:127], a[16:19], v[4:7]\n"
  "ds_read_b128 a[16:19], v222 offset:4480\n"
  "v_fma_f32 v32, v32, s73, v225\n"
  "v_mfma_f32_16x16x32_bf16 v[4:7], a[120:123], a[20:23], v[4:7]\n"
  "ds_read_b128 a[20:23], v222 offset:4544\n"
  "v_fma_f32 v33, v33, s73, v225\n"
  "v_mfma_f32_16x16x32_bf16 v[20:23], a[120:123], a[44:47], v[248:251]\n"
  "ds_read_b128 a[44:47], v222 offset:6784\n"
  "v_fma_f32 v34, v34, s73, v225\n"
  "v_mfma_f32_16x16x32_bf16 v[20:23], a[124:127], a[40:43], v[20:23]\n"
  "ds_read_b128 a[40:43], v222 offset:6720\n"
  "v_fma_f32 v35, v35, s73, v225\n"
  "v_mfma_f32_16x16x32_bf16 v[20:23], a[132:135], a[36:39], v[20:23]\n"
  "ds_read_b64_tr_b16 a[148:149], v217 offset:12352\n"
  "v_fma_f32 v4, v4, s73, v223\n"
  "v_mfma_f32_16x16x32_bf16 v[20:23], a[128:131], a[32:35], v[20:23]\n"
  "ds_read_b64_tr_b16 a[150:151], v218 offset:12352\n"
  "v_fma_f32 v5, v5, s73, v223\n"
  "v_mfma_f32_16x16x32_bf16 v[20:23], a[136:139], a[28:31], v[20:23]\n"
  "s_add_u32 m0, s72, -256\n"
  "ds_read_b64_tr_b16 a[144:145], v217 offset:13376\n"
  "v_mfma_f32_16x16x32_bf16 v[20:23], a[140:143], a[24:27], v[20:23]\n"
  "s_add_u32 s52, s62, s57\n"
  "ds_read_b64_tr_b16 a[146:147], v218 offset:13376\n"
  "v_fma_f32 v6, v6, s73, v223\n"
  "v_mfma_f32_16x16x32_bf16 v[36:39], a[140:143], a[48:51], v[248:251]\n"
  "s_addc_u32 s53, s63, 0\n"
  "v_exp_f32 v3, v3\n"
  "v_mfma_f32_16x16x32_bf16 v[36:39], a[136:139], a[52:55], v[36:39]\n"
  "s_mov_b32 s54, s93\n"
  "v_exp_f32 v4, v4\n"
  "v_mfma_f32_16x16x32_bf16 v[36:39], a[128:131], a[56:59], v[36:39]\n"
  "s_waitcnt lgkmcnt(10)\n"
  "buffer_load_dwordx4 v221, s[48:51], 0 offen offset:256  lds\n"
  "v_mfma_f32_16x16x32_bf16 v[36:39], a[132:135], a[60:63], v[36:39]\n"
  "s_add_u32 m0, s72, -2240\n"
  "v_exp_f32 v5, v5\n"
  "v_mfma_f32_16x16x32_bf16 v[36:39], a[124:127], a[64:67], v[36:39]\n"
  "s_mov_b32 s57, s92\n"
  "v_exp_f32 v6, v6\n"
  "v_mfma_f32_16x16x32_bf16 v[36:39], a[120:123], a[68:71], v[36:39]\n"
  "s_mov_b32 s93, s94\n"
  "v_exp_f32 v16, v16\n"
  "v_mfma_f32_16x16x32_bf16 v[52:55], a[120:123], a[92:95], v[248:251]\n"
  "buffer_load_dwordx4 v221, s[48:51], 0 offen offset:3328  lds\n"
  "v_fma_f32 v7, v7, s73, v223\n"
  "v_mfma_f32_16x16x32_bf16 v[52:55], a[124:127], a[88:91], v[52:55]\n"
  "s_add_u32 s48, s84, s71\n"
  "ds_read_b64_tr_b16 a[120:121], v217 offset:12368\n"
  "v_fma_f32 v20, v20, s73, v224\n"
  "v_mfma_f32_16x16x32_bf16 v[52:55], a[132:135], a[84:87], v[52:55]\n"
  "s_addc_u32 s49, s85, 0\n"
  "ds_read_b64_tr_b16 a[122:123], v218 offset:12368\n"
  "v_fma_f32 v21, v21, s73, v224\n"
  "v_mfma_f32_16x16x32_bf16 v[52:55], a[128:131], a[80:83], v[52:55]\n"
  "s_add_u32 m0, s72, 2240\n"
  "ds_read_b64_tr_b16 a[124:125], v217 offset:13392\n"
  "v_mfma_f32_16x16x32_bf16 v[52:55], a[136:139], a[76:79], v[52:55]\n"
  "s_sub_i32 s71, s38, s83\n"
  "s_max_i32 s50, s71, 0\n"
  "s_waitcnt lgkmcnt(11)\n"
  "buffer_load_dwordx4 v221, s[48:51], 0 offen offset:0 nt lds\n"
  "v_fma_f32 v22, v22, s73, v224\n"
  "v_mfma_f32_16x16x32_bf16 v[52:55], a[140:143], a[72:75], v[52:55]\n"
  "s_waitcnt lgkmcnt(3)\n"
  "ds_read_b64_tr_b16 a[126:127], v218 offset:13392\n"
  "v_fma_f32 v23, v23, s73, v224\n"
  "v_mfma_f32_16x16x32_bf16 a[236:239], a[160:163], a[148:151], a[236:239]\n"
  "v_exp_f32 v7, v7\n"
  "v_mfma_f32_16x16x32_bf16 a[236:239], a[156:159], a[144:147], a[236:239]\n"
  "s_add_u32 m0, s72, 256\n"
  "ds_write_b64 v231, v[64:65] offset:2048\n"
  "v_mfma_f32_16x16x32_bf16 v[184:187], v[60:63], a[148:151], v[184:187]\n"
  "buffer_load_dwordx4 v221, s[48:51], 0 offen offset:3072 nt lds\n"
  "v_fma_f32 v36, v36, s73, v225\n"
  "v_mfma_f32_16x16x32_bf16 v[184:187], v[56:59], a[144:147], v[184:187]\n"
  "s_add_u32 m0, s72, 4352\n"
  "v_exp_f32 v17, v17\n"
  "v_mfma_f32_16x16x32_bf16 v[152:155], v[44:47], a[148:151], v[152:155]\n"
  "v_exp_f32 v18, v18\n"
  "v_mfma_f32_16x16x32_bf16 v[152:155], v[40:43], a[144:147], v[152:155]\n"
  "ds_read_b64_tr_b16 a[132:133], v219 offset:12352\n"
  "v_fma_f32 v37, v37, s73, v225\n"
  "v_mfma_f32_16x16x32_bf16 v[120:123], v[28:31], a[148:151], v[120:123]\n"
  "buffer_load_dwordx4 v221, s[48:51], 0 offen offset:128 nt lds\n"
  "v_fma_f32 v38, v38, s73, v225\n"
  "v_mfma_f32_16x16x32_bf16 v[120:123], v[24:27], a[144:147], v[120:123]\n"
  "s_add_u32 m0, s72, 2368\n"
  "ds_read_b64_tr_b16 a[134:135], v220 offset:12352\n"
  "v_mfma_f32_16x16x32_bf16 v[88:91], v[12:15], a[148:151], v[88:91]\n"
  "ds_read_b64_tr_b16 a[128:129], v219 offset:13376\n"
  "v_fma_f32 v39, v39, s73, v225\n"
  "v_mfma_f32_16x16x32_bf16 v[88:91], v[8:11], a[144:147], v[88:91]\n"
  "s_waitcnt lgkmcnt(4)\n"
  "ds_read_b64_tr_b16 a[130:131], v220 offset:13376\n"
  "v_fma_f32 v48, v48, s73, v226\n"
  "v_mfma_f32_16x16x32_bf16 v[92:95], v[12:15], a[120:123], v[92:95]\n"
  "buffer_load_dwordx4 v221, s[48:51], 0 offen offset:3200 nt lds\n"
  "v_fma_f32 v49, v49, s73, v226\n"
  "v_mfma_f32_16x16x32_bf16 v[92:95], v[8:11], a[124:127], v[92:95]\n"
  "s_add_u32 m0, s72, 6464\n"
  "v_exp_f32 v19, v19\n"
  "v_mfma_f32_16x16x32_bf16 v[124:127], v[28:31], a[120:123], v[124:127]\n"
  "v_exp_f32 v20, v20\n"
  "v_mfma_f32_16x16x32_bf16 v[124:127], v[24:27], a[124:127], v[124:127]\n"
  "v_exp_f32 v21, v21\n"
  "v_mfma_f32_16x16x32_bf16 v[156:159], v[44:47], a[120:123], v[156:159]\n"
  "buffer_load_dwordx4 v221, s[48:51], 0 offen offset:256 nt lds\n"
  "v_fma_f32 v50, v50, s73, v226\n"
  "v_mfma_f32_16x16x32_bf16 v[156:159], v[40:43], a[124:127], v[156:159]\n"
  "s_add_u32 m0, s72, 4480\n"
  "ds_read_b64_tr_b16 a[136:137], v219 offset:12368\n"
  "v_mfma_f32_16x16x32_bf16 v[188:191], v[60:63], a[120:123], v[188:191]\n"
  "ds_read_b64_tr_b16 a[138:139], v220 offset:12368\n"
  "v_fma_f32 v51, v51, s73, v226\n"
  "v_mfma_f32_16x16x32_bf16 v[188:191], v[56:59], a[124:127], v[188:191]\n"
  "ds_read_b64_tr_b16 a[140:141], v219 offset:13392\n"
  "v_fma_f32 v52, v52, s73, v226\n"
  "v_mfma_f32_16x16x32_bf16 a[240:243], a[160:163], a[120:123], a[240:243]\n"
  "buffer_load_dwordx4 v221, s[48:51], 0 offen offset:3328 nt lds\n"
  "v_fma_f32 v53, v53, s73, v226\n"
  "v_mfma_f32_16x16x32_bf16 a[240:243], a[156:159], a[124:127], a[240:243]\n"
  "ds_read_b64_tr_b16 a[142:143], v220 offset:13392\n"
  "v_fma_f32 v54, v54, s73, v226\n"
  "v_mfma_f32_16x16x32_bf16 v[68:71], v[236:239], a[152:155], v[68:71]\n"
  "s_waitcnt lgkmcnt(4)\n"
  "v_exp_f32 v22, v22\n"
  "v_mfma_f32_16x16x32_bf16 v[192:195], v[60:63], a[132:135], v[192:195]\n"
  "v_exp_f32 v23, v23\n"
  "v_mfma_f32_16x16x32_bf16 v[192:195], v[56:59], a[128:131], v[192:195]\n"
  "v_exp_f32 v32, v32\n"
  "v_mfma_f32_16x16x32_bf16 v[160:163], v[44:47], a[132:135], v[160:163]\n"
  "ds_read_b64_tr_b16 a[120:121], v217 offset:14400\n"
  "v_and_b32 v236, 0xffff0000, v254\n"
  "v_mfma_f32_16x16x32_bf16 v[160:163], v[40:43], a[128:131], v[160:163]\n"
  "ds_read_b64_tr_b16 a[122:123], v218 offset:14400\n"
  "v_sub_f32 v254, v254, v236\n"
  "v_mfma_f32_16x16x32_bf16 v[128:131], v[28:31], a[132:135], v[128:131]\n"
  "ds_read_b64_tr_b16 a[124:125], v217 offset:14416\n"
  "v_or_b32_sdwa v236, v236, v254 dst_sel:DWORD dst_unused:UNUSED_PAD src0_sel:DWORD src1_sel:WORD_1\n"
  "v_mfma_f32_16x16x32_bf16 v[128:131], v[24:27], a[128:131], v[128:131]\n"
  "ds_read_b64_tr_b16 a[126:127], v218 offset:14416\n"
  "v_fma_f32 v55, v55, s73, v226\n"
  "v_mfma_f32_16x16x32_bf16 v[96:99], v[12:15], a[132:135], v[96:99]\n"
  "v_exp_f32 v33, v33\n"
  "v_mfma_f32_16x16x32_bf16 v[96:99], v[8:11], a[128:131], v[96:99]\n"
  "v_exp_f32 v34, v34\n"
  "v_mfma_f32_16x16x32_bf16 v[72:75], v[240:243], a[152:155], v[72:75]\n"
  "s_waitcnt lgkmcnt(4)\n"
  "v_exp_f32 v35, v35\n"
  "v_mfma_f32_16x16x32_bf16 v[100:103], v[12:15], a[136:139], v[100:103]\n"
  "v_exp_f32 v36, v36\n"
  "v_mfma_f32_16x16x32_bf16 v[100:103], v[8:11], a[140:143], v[100:103]\n"
  "v_exp_f32 v37, v37\n"
  "v_mfma_f32_16x16x32_bf16 v[132:135], v[28:31], a[136:139], v[132:135]\n"
  "v_exp_f32 v38, v38\n"
  "v_mfma_f32_16x16x32_bf16 v[132:135], v[24:27], a[140:143], v[132:135]\n"
  "v_exp_f32 v39, v39\n"
  "v_mfma_f32_16x16x32_bf16 v[164:167], v[44:47], a[136:139], v[164:167]\n"
  "ds_read_b64_tr_b16 a[132:133], v219 offset:14400\n"
  "v_and_b32 v240, 0xffff0000, v255\n"
  "v_mfma_f32_16x16x32_bf16 v[164:167], v[40:43], a[140:143], v[164:167]\n"
  "ds_read_b64_tr_b16 a[134:135], v220 offset:14400\n"
  "v_sub_f32 v255, v255, v240\n"
  "v_mfma_f32_16x16x32_bf16 v[196:199], v[60:63], a[136:139], v[196:199]\n"
  "ds_read_b64_tr_b16 a[128:129], v219 offset:14416\n"
  "v_or_b32_sdwa v240, v240, v255 dst_sel:DWORD dst_unused:UNUSED_PAD src0_sel:DWORD src1_sel:WORD_1\n"
  "v_mfma_f32_16x16x32_bf16 v[196:199], v[56:59], a[140:143], v[196:199]\n"
  "ds_read_b64_tr_b16 a[130:131], v220 offset:14416\n"
  "v_cvt_pk_bf16_f32 v12, v0, v1\n"
  "v_mfma_f32_16x16x32_bf16 v[76:79], v[236:239], a[152:155], v[76:79]\n"
  "s_waitcnt lgkmcnt(4)\n"
  "v_exp_f32 v48, v48\n"
  "v_mfma_f32_16x16x32_bf16 a[244:247], a[156:159], a[120:123], a[244:247]\n"
  "v_exp_f32 v49, v49\n"
  "v_mfma_f32_16x16x32_bf16 a[248:251], a[156:159], a[124:127], a[248:251]\n"
  "v_exp_f32_e64 v50, v50\n"
  "v_mfma_f32_16x16x32_bf16 v[200:203], v[56:59], a[120:123], v[200:203]\n"
  "v_add_u32 v233, s97, v222\n"
  "s_mov_b64 exec, s[98:99]\n"
  "v_add_u32 v233, 0x4f00, v233\n"
  "s_mov_b64 exec, -1\n"
  "v_mfma_f32_16x16x32_bf16 v[204:207], v[56:59], a[124:127], v[204:207]\n"
  "ds_read_b128 a[96:99], v233 offset:17920\n"
  "ds_read_b128 a[100:103], v233 offset:17984\n"
  "ds_read_b128 a[104:107], v233 offset:19040\n"
  "ds_read_b128 a[108:111], v233 offset:19104\n"
  "ds_read_b128 a[112:115], v233 offset:20160\n"
  "ds_read_b128 a[116:119], v233 offset:20224\n"
  "v_mfma_f32_16x16x32_bf16 v[168:171], v[40:43], a[120:123], v[168:171]\n"
  "v_exp_f32 v51, v51\n"
  "v_mfma_f32_16x16x32_bf16 v[172:175], v[40:43], a[124:127], v[172:175]\n"
  "s_mov_b32 m0, s67\n"
  "v_exp_f32 v52, v52\n"
  "v_mfma_f32_16x16x32_bf16 v[136:139], v[24:27], a[120:123], v[136:139]\n"
  "s_waitcnt vmcnt(14) lgkmcnt(0)\n"
  "s_barrier\n"
  "v_mfma_f32_16x16x32_bf16 v[140:143], v[24:27], a[124:127], v[140:143]\n"
  "ds_read_b64 a[160:161], v231 offset:2048\n"
  "v_cvt_pk_bf16_f32 v13, v2, v3\n"
  "v_mfma_f32_16x16x32_bf16 v[104:107], v[8:11], a[120:123], v[104:107]\n"
  "ds_read_b64 a[162:163], v232 offset:2048\n"
  "v_cvt_pk_bf16_f32 v14, v4, v5\n"
  "v_mfma_f32_16x16x32_bf16 v[108:111], v[8:11], a[124:127], v[108:111]\n"
  "buffer_load_dwordx4 v221, s[40:43], 0 offen offset:0 lds\n"
  "v_mfma_f32_16x16x32_bf16 v[80:83], v[240:243], a[152:155], v[80:83]\n"
  "ds_read_b64_tr_b16 a[120:121], v217 offset:26752\n"
  "v_cvt_pk_bf16_f32 v15, v6, v7\n"
  "v_mfma_f32_16x16x32_bf16 v[112:115], v[8:11], a[132:135], v[112:115]\n"
  "s_addk_i32 m0, 896\n"
  "ds_read_b64_tr_b16 a[122:123], v218 offset:26752\n"
  "v_cvt_pk_bf16_f32 v28, v16, v17\n"
  "v_mfma_f32_16x16x32_bf16 v[116:119], v[8:11], a[128:131], v[116:119]\n"
  "ds_read_b64_tr_b16 a[124:125], v217 offset:26768\n"
  "v_cvt_pk_bf16_f32 v29, v18, v19\n"
  "v_mfma_f32_16x16x32_bf16 v[144:147], v[24:27], a[132:135], v[144:147]\n"
  "buffer_load_dwordx4 v221, s[40:43], 0 offen offset:128 lds\n"
  "v_cvt_pk_bf16_f32 v30, v20, v21\n"
  "v_mfma_f32_16x16x32_bf16 v[148:151], v[24:27], a[128:131], v[148:151]\n"
  "s_addk_i32 m0, 896\n"
  "ds_read_b64_tr_b16 a[126:127], v218 offset:26768\n"
  "v_cvt_pk_bf16_f32 v31, v22, v23\n"
  "v_mfma_f32_16x16x32_bf16 v[176:179], v[40:43], a[132:135], v[176:179]\n"
  "v_exp_f32 v53, v53\n"
  "v_mfma_f32_16x16x32_bf16 v[180:183], v[40:43], a[128:131], v[180:183]\n"
  "v_exp_f32 v54, v54\n"
  "v_mfma_f32_16x16x32_bf16 v[208:211], v[56:59], a[132:135], v[208:211]\n"
  "buffer_load_dwordx4 v221, s[40:43], 0 offen offset:256 lds\n"
  "v_cvt_pk_bf16_f32 v44, v32, v33\n"
  "v_mfma_f32_16x16x32_bf16 v[212:215], v[56:59], a[128:131], v[212:215]\n"
  "s_cmp_eq_i32 s76, 5\n"
  "s_cbranch_scc1 L_TAIL_%=\n"
  "v_exp_f32 v55, v55\n"
  "v_mfma_f32_16x16x32_bf16 v[68:71], v[12:15], a[152:155], v[68:71]\n"
  "ds_read_b64_tr_b16 a[132:133], v219 offset:26752\n"
  "v_cvt_pk_bf16_f32 v45, v34, v35\n"
  "v_cvt_pk_bf16_f32 v46, v36, v37\n"
  "v_cvt_pk_bf16_f32 v47, v38, v39\n"
  "v_mfma_f32_16x16x32_bf16 v[72:75], v[28:31], a[152:155], v[72:75]\n"
  "s_add_u32 m0, s59, -128\n"
  "ds_read_b64_tr_b16 a[134:135], v220 offset:26752\n"
  "v_cvt_pk_bf16_f32 v60, v48, v49\n"
  "v_cvt_pk_bf16_f32 v61, v50, v51\n"
  "v_cvt_pk_bf16_f32 v62, v52, v53\n"
  "v_cvt_pk_bf16_f32 v63, v54, v55\n"
  "v_mfma_f32_16x16x32_bf16 v[76:79], v[44:47], a[152:155], v[76:79]\n"
  "buffer_load_dwordx4 v221, s[32:35], 0 offen offset:128 lds\n"
  "ds_read_b128 a[24:27], v222 offset:11200\n"
  "v_mfma_f32_16x16x32_bf16 v[80:83], v[60:63], a[152:155], v[80:83]\n"
  "s_addk_i32 m0, 1024\n"
  "ds_read_b64_tr_b16 a[128:129], v219 offset:26768\n"
  "ds_read_b128 a[28:31], v222 offset:11264\n"
  "v_mfma_f32_16x16x32_bf16 v[84:87], a[156:159], a[152:155], v[84:87]\n"
  "s_waitcnt lgkmcnt(9)\n"
  "ds_read_b64_tr_b16 a[130:131], v220 offset:26768\n"
  "ds_read_b128 a[32:35], v222 offset:8960\n"
  "v_mfma_f32_16x16x32_bf16 v[84:87], a[160:163], a[152:155], v[84:87]\n"
  "s_waitcnt lgkmcnt(7)\n"
  "ds_read_b128 a[36:39], v222 offset:9024\n"
  "ds_read_b128 a[48:51], v222 offset:13440\n"
  "v_mfma_f32_16x16x32_bf16 a[244:247], a[160:163], a[120:123], a[244:247]\n"
  "buffer_load_dwordx4 v221, s[44:47], 0 offen offset:128 lds\n"
  "ds_read_b128 a[52:55], v222 offset:13504\n"
  "v_mfma_f32_16x16x32_bf16 a[248:251], a[160:163], a[124:127], a[248:251]\n"
  "s_addk_i32 m0, 896\n"
  "ds_read_b128 a[56:59], v222 offset:15680\n"
  "ds_read_b128 a[60:63], v222 offset:15744\n"
  "v_mfma_f32_16x16x32_bf16 v[200:203], v[60:63], a[120:123], v[200:203]\n"
  "ds_read_b64_tr_b16 a[136:137], v217 offset:24704\n"
  "v_maximum3_f32 v236, v68, v69, v70\n"
  "v_mfma_f32_16x16x32_bf16 v[204:207], v[60:63], a[124:127], v[204:207]\n"
  "ds_read_b64_tr_b16 a[138:139], v218 offset:24704\n"
  "v_maximum3_f32 v236, v236, v71, v72\n"
  "v_mfma_f32_16x16x32_bf16 v[168:171], v[44:47], a[120:123], v[168:171]\n"
  "buffer_load_dwordx4 v221, s[44:47], 0 offen offset:256 lds\n"
  "v_maximum3_f32 v236, v236, v73, v74\n"
  "v_mfma_f32_16x16x32_bf16 v[172:175], v[44:47], a[124:127], v[172:175]\n"
  "ds_read_b64_tr_b16 a[140:141], v217 offset:25728\n"
  "v_maximum3_f32 v236, v236, v75, v76\n"
  "v_mfma_f32_16x16x32_bf16 v[136:139], v[28:31], a[120:123], v[136:139]\n"
  "ds_read_b64_tr_b16 a[142:143], v218 offset:25728\n"
  "v_maximum3_f32 v236, v236, v77, v78\n"
  "v_mfma_f32_16x16x32_bf16 v[140:143], v[28:31], a[124:127], v[140:143]\n"
  "v_maximum3_f32 v236, v236, v79, v80\n"
  "v_maximum3_f32 v236, v236, v81, v82\n"
  "v_mfma_f32_16x16x32_bf16 v[104:107], v[12:15], a[120:123], v[104:107]\n"
  "v_maximum3_f32 v236, v236, v83, v84\n"
  "v_maximum3_f32 v236, v236, v85, v86\n"
  "v_mfma_f32_16x16x32_bf16 v[108:111], v[12:15], a[124:127], v[108:111]\n"
  "s_waitcnt lgkmcnt(10)\n"
  "v_rcp_f32 v84, v84\n"
  "v_mfma_f32_16x16x32_bf16 v[112:115], v[12:15], a[132:135], v[112:115]\n"
  "s_add_u32 s40, s40, 24576\n"
  "v_rcp_f32 v85, v85\n"
  "v_mfma_f32_16x16x32_bf16 v[116:119], v[12:15], a[128:131], v[116:119]\n"
  "s_addc_u32 s41, s41, 0\n"
  "v_accvgpr_read_b32 v240, a244\n"
  "v_accvgpr_read_b32 v241, a245\n"
  "v_mfma_f32_16x16x32_bf16 v[144:147], v[28:31], a[132:135], v[144:147]\n"
  "ds_read_b64_tr_b16 a[120:121], v217 offset:24720\n"
  "v_accvgpr_read_b32 v242, a248\n"
  "v_mfma_f32_16x16x32_bf16 v[148:151], v[28:31], a[128:131], v[148:151]\n"
  "ds_read_b64_tr_b16 a[122:123], v218 offset:24720\n"
  "v_accvgpr_read_b32 v243, a249\n"
  "v_mfma_f32_16x16x32_bf16 v[176:179], v[44:47], a[132:135], v[176:179]\n"
  "ds_read_b64_tr_b16 a[124:125], v217 offset:25744\n"
  "v_maximum3_f32 v236, v236, v87, v87\n"
  "v_mfma_f32_16x16x32_bf16 v[180:183], v[44:47], a[128:131], v[180:183]\n"
  "ds_read_b64_tr_b16 a[126:127], v218 offset:25744\n"
  "v_cmp_ngt_f32 vcc, s75, v236\n"
  "v_mfma_f32_16x16x32_bf16 v[208:211], v[60:63], a[132:135], v[208:211]\n"
  "s_mov_b32 s75, s65\n"
  "v_rcp_f32 v68, v68\n"
  "v_mfma_f32_16x16x32_bf16 v[212:215], v[60:63], a[128:131], v[212:215]\n"
  "s_waitcnt lgkmcnt(4)\n"
  "v_rcp_f32 v69, v69\n"
  "v_mfma_f32_16x16x32_bf16 a[228:231], a[156:159], a[136:139], a[228:231]\n"
  "s_add_u32 s32, s44, s31\n"
  "v_rcp_f32 v70, v70\n"
  "v_mfma_f32_16x16x32_bf16 a[228:231], a[160:163], a[140:143], a[228:231]\n"
  "s_addc_u32 s33, s45, 0\n"
  "v_rcp_f32 v71, v71\n"
  "v_mfma_f32_16x16x32_bf16 a[212:215], v[56:59], a[136:139], a[212:215]\n"
  "s_add_u32 s44, s44, 24576\n"
  "v_rcp_f32 v72, v72\n"
  "v_mfma_f32_16x16x32_bf16 a[212:215], v[60:63], a[140:143], a[212:215]\n"
  "s_addc_u32 s45, s45, 0\n"
  "v_rcp_f32 v73, v73\n"
  "v_mfma_f32_16x16x32_bf16 a[196:199], v[40:43], a[136:139], a[196:199]\n"
  "ds_read_b128 a[132:135], v216 offset:24704\n"
  "v_accvgpr_read_b32 v236, a246\n"
  "v_mfma_f32_16x16x32_bf16 a[196:199], v[44:47], a[140:143], a[196:199]\n"
  "ds_read_b128 a[128:131], v216 offset:24768\n"
  "v_accvgpr_read_b32 v237, a247\n"
  "v_mfma_f32_16x16x32_bf16 a[180:183], v[24:27], a[136:139], a[180:183]\n"
  "ds_read_b128 a[148:151], v216 offset:25728\n"
  "v_accvgpr_read_b32 v238, a250\n"
  "v_mfma_f32_16x16x32_bf16 a[180:183], v[28:31], a[140:143], a[180:183]\n"
  "ds_read_b128 a[144:147], v216 offset:25792\n"
  "v_add_u32 v233, s61, v229\n"
  "s_mov_b32 exec_lo, 0\n"
  "v_add_u32 v233, s69, v233\n"
  "s_mov_b32 exec_lo, -1\n"
  "v_rcp_f32_e64 v74, v74\n"
  "v_pk_fma_f32 v[240:241], v[240:241], v[84:85], v[84:85] neg_lo:[0,0,1] neg_hi:[0,0,1]\n"
  "v_pk_fma_f32 v[242:243], v[242:243], v[84:85], v[84:85] neg_lo:[0,0,1] neg_hi:[0,0,1]\n"
  "v_pk_fma_f32 v[104:105], v[104:105], v[68:69], v[68:69] neg_lo:[0,0,1] neg_hi:[0,0,1]\n"
  "v_pk_fma_f32 v[108:109], v[108:109], v[68:69], v[68:69] neg_lo:[0,0,1] neg_hi:[0,0,1]\n"
  "v_mfma_f32_16x16x32_bf16 a[164:167], v[8:11], a[136:139], a[164:167]\n"
  "s_nop 0\n"
  "ds_read_b128 a[136:139], v216 offset:26752\n"
  "v_cvt_pk_bf16_f32 v240, v240, v242\n"
  "v_mfma_f32_16x16x32_bf16 a[164:167], v[12:15], a[140:143], a[164:167]\n"
  "s_waitcnt lgkmcnt(5)\n"
  "ds_read_b128 a[140:143], v216 offset:26816\n"
  "v_cvt_pk_bf16_f32 v241, v241, v243\n"
  "v_cvt_pk_bf16_f32 v104, v104, v108\n"
  "v_rcp_f32_e64 v75, v75\n"
  "v_pk_fma_f32 v[112:113], v[112:113], v[68:69], v[68:69] neg_lo:[0,0,1] neg_hi:[0,0,1]\n"
  "v_pk_fma_f32 v[116:117], v[116:117], v[68:69], v[68:69] neg_lo:[0,0,1] neg_hi:[0,0,1]\n"
  "v_pk_fma_f32 v[106:107], v[106:107], v[70:71], v[70:71] neg_lo:[0,0,1] neg_hi:[0,0,1]\n"
  "v_pk_fma_f32 v[110:111], v[110:111], v[70:71], v[70:71] neg_lo:[0,0,1] neg_hi:[0,0,1]\n"
  "v_mfma_f32_16x16x32_bf16 a[168:171], v[8:11], a[120:123], a[168:171]\n"
  "s_cmp_lg_u64 vcc, 0\n"
  "s_cselect_b32 s79, s77, 0\n"
  "s_or_b32 s78, s78, s79\n"
  "s_lshl_b32 s77, s77, 1\n"
  "v_rcp_f32 v76, v76\n"
  "v_mfma_f32_16x16x32_bf16 a[168:171], v[12:15], a[124:127], a[168:171]\n"
  "v_cvt_pk_bf16_f32 v108, v105, v109\n"
  "v_rcp_f32 v77, v77\n"
  "v_rcp_f32_e64 v78, v78\n"
  "v_pk_fma_f32 v[114:115], v[114:115], v[70:71], v[70:71] neg_lo:[0,0,1] neg_hi:[0,0,1]\n"
  "v_pk_fma_f32 v[118:119], v[118:119], v[70:71], v[70:71] neg_lo:[0,0,1] neg_hi:[0,0,1]\n"
  "v_pk_fma_f32 v[136:137], v[136:137], v[72:73], v[72:73] neg_lo:[0,0,1] neg_hi:[0,0,1]\n"
  "v_pk_fma_f32 v[140:141], v[140:141], v[72:73], v[72:73] neg_lo:[0,0,1] neg_hi:[0,0,1]\n"
  "v_mfma_f32_16x16x32_bf16 a[184:187], v[24:27], a[120:123], a[184:187]\n"
  "v_rcp_f32_e64 v79, v79\n"
  "v_mfma_f32_16x16x32_bf16 a[184:187], v[28:31], a[124:127], a[184:187]\n"
  "v_cvt_pk_bf16_f32 v105, v112, v116\n"
  "v_rcp_f32_e64 v80, v80\n"
  "v_rcp_f32_e64 v81, v81\n"
  "v_pk_fma_f32 v[144:145], v[144:145], v[72:73], v[72:73] neg_lo:[0,0,1] neg_hi:[0,0,1]\n"
  "v_pk_fma_f32 v[148:149], v[148:149], v[72:73], v[72:73] neg_lo:[0,0,1] neg_hi:[0,0,1]\n"
  "v_pk_fma_f32 v[138:139], v[138:139], v[74:75], v[74:75] neg_lo:[0,0,1] neg_hi:[0,0,1]\n"
  "v_pk_fma_f32 v[142:143], v[142:143], v[74:75], v[74:75] neg_lo:[0,0,1] neg_hi:[0,0,1]\n"
  "v_mfma_f32_16x16x32_bf16 a[200:203], v[40:43], a[120:123], a[200:203]\n"
  "v_rcp_f32_e64 v82, v82\n"
  "v_mfma_f32_16x16x32_bf16 a[200:203], v[44:47], a[124:127], a[200:203]\n"
  "ds_write_b32 v233, v240 offset:20160\n"
  "v_cvt_pk_bf16_f32 v109, v113, v117\n"
  "v_rcp_f32_e64 v83, v83\n"
  "v_pk_fma_f32 v[146:147], v[146:147], v[74:75], v[74:75] neg_lo:[0,0,1] neg_hi:[0,0,1]\n"
  "v_pk_fma_f32 v[150:151], v[150:151], v[74:75], v[74:75] neg_lo:[0,0,1] neg_hi:[0,0,1]\n"
  "v_pk_fma_f32 v[168:169], v[168:169], v[76:77], v[76:77] neg_lo:[0,0,1] neg_hi:[0,0,1]\n"
  "v_pk_fma_f32 v[172:173], v[172:173], v[76:77], v[76:77] neg_lo:[0,0,1] neg_hi:[0,0,1]\n"
  "v_mfma_f32_16x16x32_bf16 a[216:219], v[56:59], a[120:123], a[216:219]\n"
  "ds_write_b32 v233, v241 offset:20448\n"
  "v_mfma_f32_16x16x32_bf16 a[216:219], v[60:63], a[124:127], a[216:219]\n"
  "ds_write_b64 v229, v[104:105] offset:8960\n"
  "v_cvt_pk_bf16_f32 v106, v106, v110\n"
  "v_cvt_pk_bf16_f32 v110, v107, v111\n"
  "v_pk_fma_f32 v[176:177], v[176:177], v[76:77], v[76:77] neg_lo:[0,0,1] neg_hi:[0,0,1]\n"
  "v_pk_fma_f32 v[180:181], v[180:181], v[76:77], v[76:77] neg_lo:[0,0,1] neg_hi:[0,0,1]\n"
  "v_pk_fma_f32 v[170:171], v[170:171], v[78:79], v[78:79] neg_lo:[0,0,1] neg_hi:[0,0,1]\n"
  "v_pk_fma_f32 v[174:175], v[174:175], v[78:79], v[78:79] neg_lo:[0,0,1] neg_hi:[0,0,1]\n"
  "v_mfma_f32_16x16x32_bf16 a[232:235], a[156:159], a[120:123], a[232:235]\n"
  "s_nop 0\n"
  "v_cvt_pk_bf16_f32 v107, v114, v118\n"
  "v_cvt_pk_bf16_f32 v111, v115, v119\n"
  "v_mfma_f32_16x16x32_bf16 a[232:235], a[160:163], a[124:127], a[232:235]\n"
  "s_waitcnt lgkmcnt(7)\n"
  "ds_write_b64 v229, v[108:109] offset:9248\n"
  "v_cvt_pk_bf16_f32 v136, v136, v140\n"
  "v_cvt_pk_bf16_f32 v140, v137, v141\n"
  "v_pk_fma_f32 v[178:179], v[178:179], v[78:79], v[78:79] neg_lo:[0,0,1] neg_hi:[0,0,1]\n"
  "v_pk_fma_f32 v[182:183], v[182:183], v[78:79], v[78:79] neg_lo:[0,0,1] neg_hi:[0,0,1]\n"
  "v_pk_fma_f32 v[200:201], v[200:201], v[80:81], v[80:81] neg_lo:[0,0,1] neg_hi:[0,0,1]\n"
  "v_pk_fma_f32 v[204:205], v[204:205], v[80:81], v[80:81] neg_lo:[0,0,1] neg_hi:[0,0,1]\n"
  "v_mfma_f32_16x16x32_bf16 v[64:67], a[132:135], a[96:99], v[248:251]\n"
  "v_cvt_pk_bf16_f32 v137, v144, v148\n"
  "v_cvt_pk_bf16_f32 v141, v145, v149\n"
  "v_mfma_f32_16x16x32_bf16 v[64:67], a[128:131], a[100:103], v[64:67]\n"
  "s_waitcnt lgkmcnt(6)\n"
  "v_cvt_pk_bf16_f32 v138, v138, v142\n"
  "v_cvt_pk_bf16_f32 v142, v139, v143\n"
  "v_mfma_f32_16x16x32_bf16 v[64:67], a[148:151], a[104:107], v[64:67]\n"
  "s_waitcnt vmcnt(12)\n"
  "ds_read_b128 a[64:67], v222 offset:0\n"
  "ds_read_b128 a[68:71], v222 offset:64\n"
  "v_mfma_f32_16x16x32_bf16 v[64:67], a[144:147], a[108:111], v[64:67]\n"
  "s_waitcnt lgkmcnt(6)\n"
  "v_cvt_pk_bf16_f32 v139, v146, v150\n"
  "v_cvt_pk_bf16_f32 v143, v147, v151\n"
  "v_mfma_f32_16x16x32_bf16 v[64:67], a[136:139], a[112:115], v[64:67]\n"
  "s_nop 0\n"
  "v_cvt_pk_bf16_f32 v168, v168, v172\n"
  "v_cvt_pk_bf16_f32 v172, v169, v173\n"
  "v_mfma_f32_16x16x32_bf16 v[64:67], a[140:143], a[116:119], v[64:67]\n"
  "ds_write_b64 v229, v[106:107] offset:9536\n"
  "v_cvt_pk_bf16_f32 v169, v176, v180\n"
  "v_cvt_pk_bf16_f32 v173, v177, v181\n"
  "v_pk_fma_f32 v[208:209], v[208:209], v[80:81], v[80:81] neg_lo:[0,0,1] neg_hi:[0,0,1]\n"
  "v_pk_fma_f32 v[212:213], v[212:213], v[80:81], v[80:81] neg_lo:[0,0,1] neg_hi:[0,0,1]\n"
  "v_pk_fma_f32 v[202:203], v[202:203], v[82:83], v[82:83] neg_lo:[0,0,1] neg_hi:[0,0,1]\n"
  "v_pk_fma_f32 v[206:207], v[206:207], v[82:83], v[82:83] neg_lo:[0,0,1] neg_hi:[0,0,1]\n"
  "v_mfma_f32_16x16x32_bf16 v[0:3], a[140:143], a[20:23], v[248:251]\n"
  "v_cvt_pk_bf16_f32 v170, v170, v174\n"
  "v_cvt_pk_bf16_f32 v174, v171, v175\n"
  "v_mfma_f32_16x16x32_bf16 v[0:3], a[136:139], a[16:19], v[0:3]\n"
  "v_cvt_pk_bf16_f32 v171, v178, v182\n"
  "v_cvt_pk_bf16_f32 v175, v179, v183\n"
  "v_mfma_f32_16x16x32_bf16 v[0:3], a[144:147], a[12:15], v[0:3]\n"
  "s_waitcnt vmcnt(10)\n"
  "ds_read_b128 a[72:75], v222 offset:2240\n"
  "ds_read_b128 a[76:79], v222 offset:2304\n"
  "v_mfma_f32_16x16x32_bf16 v[0:3], a[148:151], a[8:11], v[0:3]\n"
  "s_waitcnt vmcnt(8)\n"
  "ds_read_b128 a[80:83], v222 offset:4480\n"
  "ds_read_b128 a[84:87], v222 offset:4544\n"
  "v_mfma_f32_16x16x32_bf16 v[0:3], a[128:131], a[4:7], v[0:3]\n"
  "s_waitcnt vmcnt(6)\n"
  "ds_read_b128 a[88:91], v222 offset:6720\n"
  "ds_read_b128 a[92:95], v222 offset:6784\n"
  "v_mfma_f32_16x16x32_bf16 v[0:3], a[132:135], a[0:3], v[0:3]\n"
  "s_nop 0\n"
  "ds_write_b64 v229, v[110:111] offset:9824\n"
  "v_max3_f32 v227, v64, v65, v66\n"
  "v_cvt_pk_bf16_f32 v200, v200, v204\n"
  "v_pk_fma_f32 v[210:211], v[210:211], v[82:83], v[82:83] neg_lo:[0,0,1] neg_hi:[0,0,1]\n"
  "v_pk_fma_f32 v[214:215], v[214:215], v[82:83], v[82:83] neg_lo:[0,0,1] neg_hi:[0,0,1]\n"
  "v_mfma_f32_16x16x32_bf16 v[16:19], a[132:135], a[24:27], v[248:251]\n"
  "v_rcp_f32 v86, v86\n"
  "v_mfma_f32_16x16x32_bf16 v[16:19], a[128:131], a[28:31], v[16:19]\n"
  "ds_read_b64_tr_b16 a[252:253], v230 offset:8960\n"
  "ds_read_b64_tr_b16 a[254:255], v230 offset:8992\n"
  "v_mfma_f32_16x16x32_bf16 v[16:19], a[148:151], a[32:35], v[16:19]\n"
  "ds_read_b128 a[120:123], v235 offset:24704\n"
  "v_max3_f32 v223, v0, v1, v2\n"
  "v_mfma_f32_16x16x32_bf16 v[16:19], a[144:147], a[36:39], v[16:19]\n"
  "ds_read_b128 a[124:127], v235 offset:24768\n"
  "v_max_f32 v223, v223, v3\n"
  "v_mfma_f32_16x16x32_bf16 v[16:19], a[136:139], a[40:43], v[16:19]\n"
  "v_mul_f32 v223, s74, v223\n"
  "v_add_f32 v223, s64, v223\n"
  "v_mfma_f32_16x16x32_bf16 v[16:19], a[140:143], a[44:47], v[16:19]\n"
  "s_waitcnt lgkmcnt(12)\n"
  "ds_bpermute_b32 v223, v234, v223\n"
  "ds_write_b64 v229, v[136:137] offset:11200\n"
  "v_mfma_f32_16x16x32_bf16 v[32:35], a[140:143], a[68:71], v[248:251]\n"
  "ds_read_b64_tr_b16 v[244:245], v230 offset:10080\n"
  "ds_read_b64_tr_b16 v[246:247], v230 offset:10112\n"
  "v_mfma_f32_16x16x32_bf16 v[32:35], a[136:139], a[64:67], v[32:35]\n"
  "v_rcp_f32 v87, v87\n"
  "v_mfma_f32_16x16x32_bf16 v[32:35], a[144:147], a[60:63], v[32:35]\n"
  "v_max3_f32 v224, v16, v17, v18\n"
  "v_max_f32 v224, v224, v19\n"
  "v_mfma_f32_16x16x32_bf16 v[32:35], a[148:151], a[56:59], v[32:35]\n"
  "v_mul_f32 v224, s74, v224\n"
  "v_add_f32_e64 v224, s64, v224\n"
  "v_mfma_f32_16x16x32_bf16 v[32:35], a[128:131], a[52:55], v[32:35]\n"
  "ds_bpermute_b32 v224, v234, v224\n"
  "v_mfma_f32_16x16x32_bf16 v[32:35], a[132:135], a[48:51], v[32:35]\n"
  "s_waitcnt lgkmcnt(10)\n"
  "s_waitcnt lgkmcnt(7)\n"
  "buffer_store_dwordx4 a[252:255], v228, s[52:55], s80 offen offset:256 nt\n"
  "ds_write_b64 v229, v[140:141] offset:11488\n"
  "v_cvt_pk_bf16_f32 v204, v201, v205\n"
  "v_cvt_pk_bf16_f32 v201, v208, v212\n"
  "v_cvt_pk_bf16_f32 v205, v209, v213\n"
  "v_pk_fma_f32 v[236:237], v[236:237], v[86:87], v[86:87] neg_lo:[0,0,1] neg_hi:[0,0,1]\n"
  "v_mfma_f32_16x16x32_bf16 v[48:51], a[132:135], a[72:75], v[248:251]\n"
  "s_waitcnt lgkmcnt(5)\n"
  "ds_read_b128 a[132:135], v235 offset:25728\n"
  "v_fma_f32 v0, v0, s73, v223\n"
  "v_mfma_f32_16x16x32_bf16 v[48:51], a[128:131], a[76:79], v[48:51]\n"
  "ds_read_b128 a[128:131], v235 offset:25792\n"
  "v_fma_f32 v1, v1, s73, v223\n"
  "v_mfma_f32_16x16x32_bf16 v[48:51], a[148:151], a[80:83], v[48:51]\n"
  "ds_read_b128 a[148:151], v235 offset:26752\n"
  "v_fma_f32 v2, v2, s73, v223\n"
  "v_mfma_f32_16x16x32_bf16 v[48:51], a[144:147], a[84:87], v[48:51]\n"
  "ds_read_b128 a[144:147], v235 offset:26816\n"
  "v_fma_f32 v3, v3, s73, v223\n"
  "v_mfma_f32_16x16x32_bf16 v[48:51], a[136:139], a[88:91], v[48:51]\n"
  "s_waitcnt lgkmcnt(5)\n"
  "v_exp_f32 v0, v0\n"
  "v_mfma_f32_16x16x32_bf16 v[48:51], a[140:143], a[92:95], v[48:51]\n"
  "buffer_store_dwordx4 v[244:247], v228, s[52:55], s80 offen offset:3328 nt\n"
  "ds_write_b64 v229, v[138:139] offset:11776\n"
  "v_exp_f32 v1, v1\n"
  "v_max3_f32 v225, v32, v33, v34\n"
  "v_mfma_f32_16x16x32_bf16 v[240:243], a[120:123], a[96:99], v[248:251]\n"
  "v_exp_f32 v2, v2\n"
  "v_mfma_f32_16x16x32_bf16 v[240:243], a[124:127], a[100:103], v[240:243]\n"
  "s_waitcnt lgkmcnt(3)\n"
  "v_exp_f32 v3, v3\n"
  "v_mfma_f32_16x16x32_bf16 v[240:243], a[132:135], a[104:107], v[240:243]\n"
  "v_max_f32 v225, v225, v35\n"
  "v_mul_f32 v225, s74, v225\n"
  "v_mfma_f32_16x16x32_bf16 v[240:243], a[128:131], a[108:111], v[240:243]\n"
  "s_waitcnt lgkmcnt(1)\n"
  "v_add_f32 v225, s64, v225\n"
  "v_fma_f32 v16, v16, s73, v224\n"
  "v_mfma_f32_16x16x32_bf16 v[240:243], a[148:151], a[112:115], v[240:243]\n"
  "v_exp_f32 v16, v16\n"
  "v_mfma_f32_16x16x32_bf16 v[240:243], a[144:147], a[116:119], v[240:243]\n"
  "ds_bpermute_b32 v225, v234, v225\n"
  "ds_write_b64 v229, v[142:143] offset:12064\n"
  "v_mfma_f32_16x16x32_bf16 v[4:7], a[144:147], a[20:23], v[248:251]\n"
  "v_fma_f32 v17, v17, s73, v224\n"
  "v_fma_f32 v18, v18, s73, v224\n"
  "v_mfma_f32_16x16x32_bf16 v[4:7], a[148:151], a[16:19], v[4:7]\n"
  "v_exp_f32 v17, v17\n"
  "v_mfma_f32_16x16x32_bf16 v[4:7], a[128:131], a[12:15], v[4:7]\n"
  "v_max3_f32 v227, v227, v67, v240\n"
  "v_max3_f32 v227, v227, v241, v242\n"
  "v_mfma_f32_16x16x32_bf16 v[4:7], a[132:135], a[8:11], v[4:7]\n"
  "v_max_f32 v227, v227, v243\n"
  "v_mul_f32 v227, s74, v227\n"
  "v_mfma_f32_16x16x32_bf16 v[4:7], a[124:127], a[4:7], v[4:7]\n"
  "v_add_f32_e64 v227, s64, v227\n"
  "v_fma_f32 v19, v19, s73, v224\n"
  "v_mfma_f32_16x16x32_bf16 v[4:7], a[120:123], a[0:3], v[4:7]\n"
  "s_waitcnt lgkmcnt(1)\n"
  "ds_bpermute_b32 v227, v234, v227\n"
  "ds_write_b64 v229, v[168:169] offset:13440\n"
  "v_mfma_f32_16x16x32_bf16 v[20:23], a[120:123], a[24:27], v[248:251]\n"
  "v_exp_f32 v18, v18\n"
  "v_mfma_f32_16x16x32_bf16 v[20:23], a[124:127], a[28:31], v[20:23]\n"
  "v_exp_f32 v19, v19\n"
  "v_mfma_f32_16x16x32_bf16 v[20:23], a[132:135], a[32:35], v[20:23]\n"
  "ds_read_b64_tr_b16 a[136:137], v219 offset:24704\n"
  "v_max3_f32 v226, v48, v49, v50\n"
  "v_mfma_f32_16x16x32_bf16 v[20:23], a[128:131], a[36:39], v[20:23]\n"
  "ds_read_b64_tr_b16 a[138:139], v220 offset:24704\n"
  "v_max_f32 v226, v226, v51\n"
  "v_mfma_f32_16x16x32_bf16 v[20:23], a[148:151], a[40:43], v[20:23]\n"
  "ds_read_b64_tr_b16 a[140:141], v219 offset:25728\n"
  "v_mul_f32_e64 v226, s74, v226\n"
  "v_mfma_f32_16x16x32_bf16 v[20:23], a[144:147], a[44:47], v[20:23]\n"
  "s_waitcnt lgkmcnt(4)\n"
  "ds_read_b64_tr_b16 a[142:143], v220 offset:25728\n"
  "ds_write_b64 v229, v[172:173] offset:13728\n"
  "v_fma_f32 v64, v64, s73, v227\n"
  "v_mfma_f32_16x16x32_bf16 v[36:39], a[144:147], a[68:71], v[248:251]\n"
  "v_exp_f32 v64, v64\n"
  "v_mfma_f32_16x16x32_bf16 v[36:39], a[148:151], a[64:67], v[36:39]\n"
  "v_fma_f32 v65, v65, s73, v227\n"
  "v_fma_f32 v66, v66, s73, v227\n"
  "v_mfma_f32_16x16x32_bf16 v[36:39], a[128:131], a[60:63], v[36:39]\n"
  "v_exp_f32 v65, v65\n"
  "v_mfma_f32_16x16x32_bf16 v[36:39], a[132:135], a[56:59], v[36:39]\n"
  "v_exp_f32_e64 v66, v66\n"
  "v_mfma_f32_16x16x32_bf16 v[36:39], a[124:127], a[52:55], v[36:39]\n"
  "v_fma_f32 v67, v67, s73, v227\n"
  "v_cvt_pk_bf16_f32 v64, v64, v65\n"
  "v_mfma_f32_16x16x32_bf16 v[36:39], a[120:123], a[48:51], v[36:39]\n"
  "ds_write_b64 v229, v[170:171] offset:14016\n"
  "v_exp_f32_e64 v67, v67\n"
  "v_mfma_f32_16x16x32_bf16 v[52:55], a[120:123], a[72:75], v[248:251]\n"
  "ds_read_b64_tr_b16 a[120:121], v219 offset:24720\n"
  "v_cvt_pk_bf16_f32 v65, v66, v67\n"
  "v_mfma_f32_16x16x32_bf16 v[52:55], a[124:127], a[76:79], v[52:55]\n"
  "ds_read_b64_tr_b16 a[122:123], v220 offset:24720\n"
  "v_add_f32 v226, s64, v226\n"
  "v_mfma_f32_16x16x32_bf16 v[52:55], a[132:135], a[80:83], v[52:55]\n"
  "ds_read_b64_tr_b16 a[124:125], v219 offset:25744\n"
  "v_fma_f32 v32, v32, s73, v225\n"
  "v_mfma_f32_16x16x32_bf16 v[52:55], a[128:131], a[84:87], v[52:55]\n"
  "ds_read_b64_tr_b16 a[126:127], v220 offset:25744\n"
  "v_fma_f32 v33, v33, s73, v225\n"
  "v_mfma_f32_16x16x32_bf16 v[52:55], a[148:151], a[88:91], v[52:55]\n"
  "v_exp_f32_e64 v32, v32\n"
  "v_mfma_f32_16x16x32_bf16 v[52:55], a[144:147], a[92:95], v[52:55]\n"
  "s_waitcnt lgkmcnt(6)\n"
  "ds_read_b64_tr_b16 a[252:253], v230 offset:11200\n"
  "ds_read_b64_tr_b16 a[254:255], v230 offset:11232\n"
  "ds_write_b64 v231, v[64:65] offset:0\n"
  "v_mfma_f32_16x16x32_bf16 a[220:223], v[56:59], a[136:139], a[220:223]\n"
  "v_exp_f32_e64 v33, v33\n"
  "v_mfma_f32_16x16x32_bf16 a[220:223], v[60:63], a[140:143], a[220:223]\n"
  "ds_bpermute_b32 v226, v234, v226\n"
  "ds_write_b64 v229, v[174:175] offset:14304\n"
  "v_mfma_f32_16x16x32_bf16 a[204:207], v[40:43], a[136:139], a[204:207]\n"
  "v_fma_f32 v34, v34, s73, v225\n"
  "v_fma_f32 v35, v35, s73, v225\n"
  "v_mfma_f32_16x16x32_bf16 a[204:207], v[44:47], a[140:143], a[204:207]\n"
  "s_waitcnt vmcnt(2) lgkmcnt(2)\n"
  "s_barrier\n"
  "ds_read_b64_tr_b16 a[132:133], v217 offset:12352\n"
  "ds_read_b64_tr_b16 v[244:245], v230 offset:12320\n"
  "ds_read_b64_tr_b16 v[246:247], v230 offset:12352\n"
  "v_mfma_f32_16x16x32_bf16 a[188:191], v[24:27], a[136:139], a[188:191]\n"
  "ds_read_b64 a[156:157], v231 offset:0\n"
  "v_fma_f32 v4, v4, s73, v223\n"
  "v_mfma_f32_16x16x32_bf16 a[188:191], v[28:31], a[140:143], a[188:191]\n"
  "buffer_store_dwordx4 a[252:255], v228, s[52:55], s81 offen offset:256 nt\n"
  "ds_read_b64 a[158:159], v232 offset:0\n"
  "ds_write_b64 v229, v[200:201] offset:15680\n"
  "v_exp_f32_e64 v34, v34\n"
  "v_mfma_f32_16x16x32_bf16 a[172:175], v[8:11], a[136:139], a[172:175]\n"
  "ds_read_b64_tr_b16 a[134:135], v218 offset:12352\n"
  "v_fma_f32 v5, v5, s73, v223\n"
  "v_mfma_f32_16x16x32_bf16 a[172:175], v[12:15], a[140:143], a[172:175]\n"
  "ds_read_b64_tr_b16 a[128:129], v217 offset:12368\n"
  "ds_write_b64 v229, v[204:205] offset:15968\n"
  "v_fma_f32 v6, v6, s73, v223\n"
  "v_mfma_f32_16x16x32_bf16 a[176:179], v[8:11], a[120:123], a[176:179]\n"
  "s_waitcnt lgkmcnt(10)\n"
  "ds_read_b64_tr_b16 a[130:131], v218 offset:12368\n"
  "v_cvt_pk_bf16_f32 v8, v0, v1\n"
  "v_mfma_f32_16x16x32_bf16 a[176:179], v[12:15], a[124:127], a[176:179]\n"
  "s_nop 0\n"
  "ds_read_b64_tr_b16 a[252:253], v230 offset:13440\n"
  "ds_read_b64_tr_b16 a[254:255], v230 offset:13472\n"
  "v_cvt_pk_bf16_f32 v9, v2, v3\n"
  "v_fma_f32 v48, v48, s73, v226\n"
  "v_cvt_pk_bf16_f32 v202, v202, v206\n"
  "v_mfma_f32_16x16x32_bf16 a[192:195], v[24:27], a[120:123], a[192:195]\n"
  "ds_read_b128 a[136:139], v216 offset:12352\n"
  "v_cvt_pk_bf16_f32 v24, v16, v17\n"
  "v_mfma_f32_16x16x32_bf16 a[192:195], v[28:31], a[124:127], a[192:195]\n"
  "s_waitcnt lgkmcnt(10)\n"
  "buffer_store_dwordx4 v[244:247], v228, s[52:55], s81 offen offset:3328 nt\n"
  "ds_read_b128 a[140:143], v216 offset:12416\n"
  "v_cvt_pk_bf16_f32 v25, v18, v19\n"
  "v_exp_f32 v35, v35\n"
  "v_cvt_pk_bf16_f32 v206, v203, v207\n"
  "v_cvt_pk_bf16_f32 v203, v210, v214\n"
  "v_mfma_f32_16x16x32_bf16 a[208:211], v[40:43], a[120:123], a[208:211]\n"
  "ds_read_b128 a[148:151], v216 offset:13376\n"
  "v_cvt_pk_bf16_f32 v40, v32, v33\n"
  "v_mfma_f32_16x16x32_bf16 a[208:211], v[44:47], a[124:127], a[208:211]\n"
  "ds_read_b128 a[144:147], v216 offset:13440\n"
  "ds_write_b64 v229, v[202:203] offset:16256\n"
  "v_cvt_pk_bf16_f32 v41, v34, v35\n"
  "v_mfma_f32_16x16x32_bf16 a[224:227], v[56:59], a[120:123], a[224:227]\n"
  "s_nop 0\n"
  "ds_read_b128 a[120:123], v216 offset:14400\n"
  "v_fma_f32 v49, v49, s73, v226\n"
  "v_mfma_f32_16x16x32_bf16 a[224:227], v[60:63], a[124:127], a[224:227]\n"
  "L_JOIN_%=:\n"
  "s_waitcnt lgkmcnt(8)\n"
  "ds_read_b128 a[124:127], v216 offset:14464\n"
  "ds_read_b64_tr_b16 v[244:245], v230 offset:14560\n"
  "ds_read_b64_tr_b16 v[246:247], v230 offset:14592\n"
  "v_fma_f32 v50, v50, s73, v226\n"
  "v_cvt_pk_bf16_f32 v207, v211, v215\n"
  "v_mfma_f32_16x16x32_bf16 a[236:239], a[160:163], a[132:135], a[236:239]\n"
  "s_add_i32 s76, s76, 1\n"
  "v_exp_f32_e64 v48, v48\n"
  "v_mfma_f32_16x16x32_bf16 a[240:243], a[160:163], a[128:131], a[240:243]\n"
  "s_waitcnt lgkmcnt(9)\n"
  "buffer_store_dwordx4 a[252:255], v228, s[52:55], s82 offen offset:256 nt\n"
  "ds_write_b64 v229, v[206:207] offset:16544\n"
  "v_exp_f32 v49, v49\n"
  "v_fma_f32 v51, v51, s73, v226\n"
  "v_mfma_f32_16x16x32_bf16 v[184:187], v[60:63], a[132:135], v[184:187]\n"
  "v_exp_f32 v50, v50\n"
  "v_accvgpr_read_b32 v239, a251\n"
  "v_accvgpr_read_b32 v136, a180\n"
  "v_accvgpr_read_b32 v137, a181\n"
  "v_pk_fma_f32 v[136:137], v[136:137], v[72:73], v[72:73] neg_lo:[0,0,1] neg_hi:[0,0,1]\n"
  "v_pk_fma_f32 v[238:239], v[238:239], v[86:87], v[86:87] neg_lo:[0,0,1] neg_hi:[0,0,1]\n"
  "v_mfma_f32_16x16x32_bf16 v[188:191], v[60:63], a[128:131], v[188:191]\n"
  "v_exp_f32_e64 v51, v51\n"
  "v_mfma_f32_16x16x32_bf16 v[152:155], v[44:47], a[132:135], v[152:155]\n"
  "ds_read_b64_tr_b16 a[252:253], v230 offset:15680\n"
  "ds_read_b64_tr_b16 a[254:255], v230 offset:15712\n"
  "v_cvt_pk_bf16_f32 v56, v48, v49\n"
  "v_cvt_pk_bf16_f32 v57, v50, v51\n"
  "v_accvgpr_read_b32 v140, a184\n"
  "v_mfma_f32_16x16x32_bf16 v[156:159], v[44:47], a[128:131], v[156:159]\n"
  "v_exp_f32_e64 v4, v4\n"
  "v_accvgpr_read_b32 v141, a185\n"
  "v_accvgpr_read_b32 v144, a188\n"
  "v_accvgpr_read_b32 v145, a189\n"
  "v_pk_fma_f32 v[140:141], v[140:141], v[72:73], v[72:73] neg_lo:[0,0,1] neg_hi:[0,0,1]\n"
  "v_pk_fma_f32 v[144:145], v[144:145], v[72:73], v[72:73] neg_lo:[0,0,1] neg_hi:[0,0,1]\n"
  "v_pk_fma_f32 v[184:185], v[184:185], v[80:81], v[80:81] neg_lo:[0,0,1] neg_hi:[0,0,1]\n"
  "v_pk_fma_f32 v[186:187], v[186:187], v[82:83], v[82:83] neg_lo:[0,0,1] neg_hi:[0,0,1]\n"
  "v_mfma_f32_16x16x32_bf16 v[120:123], v[28:31], a[132:135], v[120:123]\n"
  "v_exp_f32_e64 v5, v5\n"
  "v_mfma_f32_16x16x32_bf16 v[124:127], v[28:31], a[128:131], v[124:127]\n"
  "s_waitcnt lgkmcnt(3)\n"
  "buffer_store_dwordx4 v[244:247], v228, s[52:55], s82 offen offset:3328 nt\n"
  "v_exp_f32 v6, v6\n"
  "v_fma_f32 v7, v7, s73, v223\n"
  "v_cvt_pk_bf16_f32 v10, v4, v5\n"
  "v_accvgpr_read_b32 v148, a192\n"
  "v_accvgpr_read_b32 v149, a193\n"
  "v_pk_fma_f32 v[148:149], v[148:149], v[72:73], v[72:73] neg_lo:[0,0,1] neg_hi:[0,0,1]\n"
  "v_pk_fma_f32 v[188:189], v[188:189], v[80:81], v[80:81] neg_lo:[0,0,1] neg_hi:[0,0,1]\n"
  "v_pk_fma_f32 v[190:191], v[190:191], v[82:83], v[82:83] neg_lo:[0,0,1] neg_hi:[0,0,1]\n"
  "v_pk_fma_f32 v[152:153], v[152:153], v[76:77], v[76:77] neg_lo:[0,0,1] neg_hi:[0,0,1]\n"
  "v_mfma_f32_16x16x32_bf16 v[88:91], v[12:15], a[132:135], v[88:91]\n"
  "ds_read_b64_tr_b16 v[244:245], v230 offset:16800\n"
  "ds_read_b64_tr_b16 v[246:247], v230 offset:16832\n"
  "v_exp_f32_e64 v7, v7\n"
  "v_accvgpr_read_b32 v138, a182\n"
  "v_pk_fma_f32 v[156:157], v[156:157], v[76:77], v[76:77] neg_lo:[0,0,1] neg_hi:[0,0,1]\n"
  "v_pk_fma_f32 v[154:155], v[154:155], v[78:79], v[78:79] neg_lo:[0,0,1] neg_hi:[0,0,1]\n"
  "v_pk_fma_f32 v[158:159], v[158:159], v[78:79], v[78:79] neg_lo:[0,0,1] neg_hi:[0,0,1]\n"
  "v_mfma_f32_16x16x32_bf16 v[92:95], v[12:15], a[128:131], v[92:95]\n"
  "v_cvt_pk_bf16_f32 v11, v6, v7\n"
  "v_fma_f32 v20, v20, s73, v224\n"
  "v_accvgpr_read_b32 v139, a183\n"
  "v_accvgpr_read_b32 v142, a186\n"
  "v_accvgpr_read_b32 v143, a187\n"
  "v_pk_fma_f32 v[138:139], v[138:139], v[74:75], v[74:75] neg_lo:[0,0,1] neg_hi:[0,0,1]\n"
  "v_pk_fma_f32 v[142:143], v[142:143], v[74:75], v[74:75] neg_lo:[0,0,1] neg_hi:[0,0,1]\n"
  "v_pk_fma_f32 v[120:121], v[120:121], v[72:73], v[72:73] neg_lo:[0,0,1] neg_hi:[0,0,1]\n"
  "v_pk_fma_f32 v[122:123], v[122:123], v[74:75], v[74:75] neg_lo:[0,0,1] neg_hi:[0,0,1]\n"
  "v_mfma_f32_16x16x32_bf16 v[64:67], a[136:139], a[96:99], v[248:251]\n"
  "v_exp_f32 v20, v20\n"
  "v_mfma_f32_16x16x32_bf16 v[64:67], a[140:143], a[100:103], v[64:67]\n"
  "v_fma_f32 v21, v21, s73, v224\n"
  "v_fma_f32 v22, v22, s73, v224\n"
  "v_mfma_f32_16x16x32_bf16 v[64:67], a[148:151], a[104:107], v[64:67]\n"
  "v_exp_f32 v21, v21\n"
  "v_mfma_f32_16x16x32_bf16 v[64:67], a[144:147], a[108:111], v[64:67]\n"
  "v_exp_f32_e64 v22, v22\n"
  "v_mfma_f32_16x16x32_bf16 v[64:67], a[120:123], a[112:115], v[64:67]\n"
  "v_fma_f32 v23, v23, s73, v224\n"
  "v_cvt_pk_bf16_f32 v26, v20, v21\n"
  "v_mfma_f32_16x16x32_bf16 v[64:67], a[124:127], a[116:119], v[64:67]\n"
  "s_waitcnt lgkmcnt(2)\n"
  "buffer_store_dwordx4 a[252:255], v228, s[52:55], s83 offen offset:256 nt\n"
  "v_exp_f32 v23, v23\n"
  "v_fma_f32 v36, v36, s73, v225\n"
  "v_mfma_f32_16x16x32_bf16 v[0:3], a[124:127], a[20:23], v[248:251]\n"
  "v_exp_f32 v36, v36\n"
  "v_mfma_f32_16x16x32_bf16 v[0:3], a[120:123], a[16:19], v[0:3]\n"
  "v_cvt_pk_bf16_f32 v27, v22, v23\n"
  "v_fma_f32 v37, v37, s73, v225\n"
  "v_mfma_f32_16x16x32_bf16 v[0:3], a[144:147], a[12:15], v[0:3]\n"
  "v_exp_f32 v37, v37\n"
  "v_mfma_f32_16x16x32_bf16 v[0:3], a[148:151], a[8:11], v[0:3]\n"
  "v_fma_f32 v38, v38, s73, v225\n"
  "v_fma_f32 v39, v39, s73, v225\n"
  "v_mfma_f32_16x16x32_bf16 v[0:3], a[140:143], a[4:7], v[0:3]\n"
  "v_exp_f32 v38, v38\n"
  "v_mfma_f32_16x16x32_bf16 v[0:3], a[136:139], a[0:3], v[0:3]\n"
  "s_waitcnt lgkmcnt(0)\n"
  "buffer_store_dwordx4 v[244:247], v228, s[52:55], s83 offen offset:3328 nt\n"
  "v_exp_f32_e64 v39, v39\n"
  "v_cvt_pk_bf16_f32 v42, v36, v37\n"
  "v_fma_f32 v52, v52, s73, v226\n"
  "v_accvgpr_read_b32 v146, a190\n"
  "v_accvgpr_read_b32 v147, a191\n"
  "v_pk_fma_f32 v[146:147], v[146:147], v[74:75], v[74:75] neg_lo:[0,0,1] neg_hi:[0,0,1]\n"
  "v_pk_fma_f32 v[124:125], v[124:125], v[72:73], v[72:73] neg_lo:[0,0,1] neg_hi:[0,0,1]\n"
  "v_pk_fma_f32 v[126:127], v[126:127], v[74:75], v[74:75] neg_lo:[0,0,1] neg_hi:[0,0,1]\n"
  "v_pk_fma_f32 v[88:89], v[88:89], v[68:69], v[68:69] neg_lo:[0,0,1] neg_hi:[0,0,1]\n"
  "v_mfma_f32_16x16x32_bf16 v[16:19], a[136:139], a[24:27], v[248:251]\n"
  "v_exp_f32_e64 v52, v52\n"
  "v_mfma_f32_16x16x32_bf16 v[16:19], a[140:143], a[28:31], v[16:19]\n"
  "v_cvt_pk_bf16_f32 v43, v38, v39\n"
  "v_fma_f32 v53, v53, s73, v226\n"
  "v_mfma_f32_16x16x32_bf16 v[16:19], a[148:151], a[32:35], v[16:19]\n"
  "ds_read_b64_tr_b16 a[132:133], v219 offset:12352\n"
  "v_fma_f32 v54, v54, s73, v226\n"
  "v_mfma_f32_16x16x32_bf16 v[16:19], a[144:147], a[36:39], v[16:19]\n"
  "ds_read_b64_tr_b16 a[134:135], v220 offset:12352\n"
  "v_fma_f32 v55, v55, s73, v226\n"
  "v_mfma_f32_16x16x32_bf16 v[16:19], a[120:123], a[40:43], v[16:19]\n"
  "ds_read_b64_tr_b16 a[128:129], v219 offset:12368\n"
  "v_accvgpr_read_b32 v150, a194\n"
  "v_mfma_f32_16x16x32_bf16 v[16:19], a[124:127], a[44:47], v[16:19]\n"
  "ds_read_b64_tr_b16 a[130:131], v220 offset:12368\n"
  "v_accvgpr_read_b32 v151, a195\n"
  "v_cvt_pk_bf16_f32 v136, v136, v140\n"
  "v_cvt_pk_bf16_f32 v140, v137, v141\n"
  "v_cvt_pk_bf16_f32 v137, v144, v148\n"
  "v_pk_fma_f32 v[150:151], v[150:151], v[74:75], v[74:75] neg_lo:[0,0,1] neg_hi:[0,0,1]\n"
  "v_pk_fma_f32 v[92:93], v[92:93], v[68:69], v[68:69] neg_lo:[0,0,1] neg_hi:[0,0,1]\n"
  "v_pk_fma_f32 v[90:91], v[90:91], v[70:71], v[70:71] neg_lo:[0,0,1] neg_hi:[0,0,1]\n"
  "v_pk_fma_f32 v[94:95], v[94:95], v[70:71], v[70:71] neg_lo:[0,0,1] neg_hi:[0,0,1]\n"
  "v_mfma_f32_16x16x32_bf16 v[32:35], a[124:127], a[68:71], v[248:251]\n"
  "v_exp_f32 v53, v53\n"
  "v_mfma_f32_16x16x32_bf16 v[32:35], a[120:123], a[64:67], v[32:35]\n"
  "v_exp_f32 v54, v54\n"
  "v_mfma_f32_16x16x32_bf16 v[32:35], a[144:147], a[60:63], v[32:35]\n"
  "v_exp_f32 v55, v55\n"
  "v_mfma_f32_16x16x32_bf16 v[32:35], a[148:151], a[56:59], v[32:35]\n"
  "v_cvt_pk_bf16_f32 v58, v52, v53\n"
  "v_cvt_pk_bf16_f32 v59, v54, v55\n"
  "v_mfma_f32_16x16x32_bf16 v[32:35], a[140:143], a[52:55], v[32:35]\n"
  "v_cvt_pk_bf16_f32 v138, v138, v142\n"
  "v_cvt_pk_bf16_f32 v142, v139, v143\n"
  "v_mfma_f32_16x16x32_bf16 v[32:35], a[136:139], a[48:51], v[32:35]\n"
  "v_cvt_pk_bf16_f32 v139, v146, v150\n"
  "v_accvgpr_read_b32 v104, a164\n"
  "v_mfma_f32_16x16x32_bf16 v[48:51], a[136:139], a[72:75], v[248:251]\n"
  "ds_read_b128 a[136:139], v235 offset:12352\n"
  "v_accvgpr_read_b32 v105, a165\n"
  "v_mfma_f32_16x16x32_bf16 v[48:51], a[140:143], a[76:79], v[48:51]\n"
  "ds_read_b128 a[140:143], v235 offset:12416\n"
  "v_accvgpr_read_b32 v108, a168\n"
  "v_mfma_f32_16x16x32_bf16 v[48:51], a[148:151], a[80:83], v[48:51]\n"
  "ds_read_b128 a[148:151], v235 offset:13376\n"
  "v_accvgpr_read_b32 v109, a169\n"
  "v_mfma_f32_16x16x32_bf16 v[48:51], a[144:147], a[84:87], v[48:51]\n"
  "ds_read_b128 a[144:147], v235 offset:13440\n"
  "v_accvgpr_read_b32 v112, a172\n"
  "v_mfma_f32_16x16x32_bf16 v[48:51], a[120:123], a[88:91], v[48:51]\n"
  "ds_read_b128 a[120:123], v235 offset:14400\n"
  "v_accvgpr_read_b32 v113, a173\n"
  "v_mfma_f32_16x16x32_bf16 v[48:51], a[124:127], a[92:95], v[48:51]\n"
  "s_waitcnt lgkmcnt(5)\n"
  "ds_read_b128 a[124:127], v235 offset:14464\n"
  "ds_write_b64 v229, v[136:137] offset:2240\n"
  "v_accvgpr_read_b32 v116, a176\n"
  "v_pk_fma_f32 v[104:105], v[104:105], v[68:69], v[68:69] neg_lo:[0,0,1] neg_hi:[0,0,1]\n"
  "v_pk_fma_f32 v[108:109], v[108:109], v[68:69], v[68:69] neg_lo:[0,0,1] neg_hi:[0,0,1]\n"
  "v_pk_fma_f32 v[112:113], v[112:113], v[68:69], v[68:69] neg_lo:[0,0,1] neg_hi:[0,0,1]\n"
  "v_mfma_f32_16x16x32_bf16 v[96:99], v[12:15], a[132:135], v[96:99]\n"
  "ds_write_b64 v229, v[138:139] offset:2816\n"
  "v_accvgpr_read_b32 v117, a177\n"
  "v_accvgpr_read_b32 v106, a166\n"
  "v_pk_fma_f32 v[116:117], v[116:117], v[68:69], v[68:69] neg_lo:[0,0,1] neg_hi:[0,0,1]\n"
  "v_mfma_f32_16x16x32_bf16 v[100:103], v[12:15], a[128:131], v[100:103]\n"
  "v_accvgpr_read_b32 v107, a167\n"
  "v_accvgpr_read_b32 v110, a170\n"
  "v_mfma_f32_16x16x32_bf16 v[128:131], v[28:31], a[132:135], v[128:131]\n"
  "v_accvgpr_read_b32 v111, a171\n"
  "v_accvgpr_read_b32 v114, a174\n"
  "v_accvgpr_read_b32 v115, a175\n"
  "v_accvgpr_read_b32 v118, a178\n"
  "v_accvgpr_read_b32 v119, a179\n"
  "v_pk_fma_f32 v[106:107], v[106:107], v[70:71], v[70:71] neg_lo:[0,0,1] neg_hi:[0,0,1]\n"
  "v_pk_fma_f32 v[110:111], v[110:111], v[70:71], v[70:71] neg_lo:[0,0,1] neg_hi:[0,0,1]\n"
  "v_pk_fma_f32 v[114:115], v[114:115], v[70:71], v[70:71] neg_lo:[0,0,1] neg_hi:[0,0,1]\n"
  "v_pk_fma_f32 v[118:119], v[118:119], v[70:71], v[70:71] neg_lo:[0,0,1] neg_hi:[0,0,1]\n"
  "v_mfma_f32_16x16x32_bf16 v[132:135], v[28:31], a[128:131], v[132:135]\n"
  "v_cvt_pk_bf16_f32 v141, v145, v149\n"
  "v_cvt_pk_bf16_f32 v143, v147, v151\n"
  "v_cvt_pk_bf16_f32 v104, v104, v108\n"
  "v_cvt_pk_bf16_f32 v108, v105, v109\n"
  "v_cvt_pk_bf16_f32 v105, v112, v116\n"
  "v_pk_fma_f32 v[96:97], v[96:97], v[68:69], v[68:69] neg_lo:[0,0,1] neg_hi:[0,0,1]\n"
  "v_pk_fma_f32 v[98:99], v[98:99], v[70:71], v[70:71] neg_lo:[0,0,1] neg_hi:[0,0,1]\n"
  "v_mfma_f32_16x16x32_bf16 v[160:163], v[44:47], a[132:135], v[160:163]\n"
  "ds_write_b64 v229, v[140:141] offset:2528\n"
  "v_cvt_pk_bf16_f32 v109, v113, v117\n"
  "v_cvt_pk_bf16_f32 v106, v106, v110\n"
  "v_pk_fma_f32 v[100:101], v[100:101], v[68:69], v[68:69] neg_lo:[0,0,1] neg_hi:[0,0,1]\n"
  "v_pk_fma_f32 v[102:103], v[102:103], v[70:71], v[70:71] neg_lo:[0,0,1] neg_hi:[0,0,1]\n"
  "v_mfma_f32_16x16x32_bf16 v[164:167], v[44:47], a[128:131], v[164:167]\n"
  "v_cvt_pk_bf16_f32 v110, v107, v111\n"
  "v_cvt_pk_bf16_f32 v107, v114, v118\n"
  "v_mfma_f32_16x16x32_bf16 v[192:195], v[60:63], a[132:135], v[192:195]\n"
  "ds_write_b64 v229, v[142:143] offset:3104\n"
  "v_cvt_pk_bf16_f32 v111, v115, v119\n"
  "v_cvt_pk_bf16_f32 v236, v236, v238\n"
  "v_pk_fma_f32 v[128:129], v[128:129], v[72:73], v[72:73] neg_lo:[0,0,1] neg_hi:[0,0,1]\n"
  "v_pk_fma_f32 v[132:133], v[132:133], v[72:73], v[72:73] neg_lo:[0,0,1] neg_hi:[0,0,1]\n"
  "v_pk_fma_f32 v[130:131], v[130:131], v[74:75], v[74:75] neg_lo:[0,0,1] neg_hi:[0,0,1]\n"
  "v_pk_fma_f32 v[134:135], v[134:135], v[74:75], v[74:75] neg_lo:[0,0,1] neg_hi:[0,0,1]\n"
  "v_mfma_f32_16x16x32_bf16 v[196:199], v[60:63], a[128:131], v[196:199]\n"
  "s_nop 0\n"
  "s_waitcnt lgkmcnt(8)\n"
  "ds_write_b64 v229, v[104:105] offset:0\n"
  "v_cvt_pk_bf16_f32 v237, v237, v239\n"
  "v_cvt_pk_bf16_f32 v184, v184, v188\n"
  "v_pk_fma_f32 v[160:161], v[160:161], v[76:77], v[76:77] neg_lo:[0,0,1] neg_hi:[0,0,1]\n"
  "v_pk_fma_f32 v[162:163], v[162:163], v[78:79], v[78:79] neg_lo:[0,0,1] neg_hi:[0,0,1]\n"
  "v_mfma_f32_16x16x32_bf16 v[4:7], a[136:139], a[0:3], v[248:251]\n"
  "ds_write_b32 v233, v236 offset:20736\n"
  "v_mfma_f32_16x16x32_bf16 v[4:7], a[140:143], a[4:7], v[4:7]\n"
  "s_waitcnt lgkmcnt(8)\n"
  "ds_write_b32 v233, v237 offset:21024\n"
  "v_mfma_f32_16x16x32_bf16 v[4:7], a[148:151], a[8:11], v[4:7]\n"
  "v_cvt_pk_bf16_f32 v188, v185, v189\n"
  "v_cvt_pk_bf16_f32 v186, v186, v190\n"
  "v_mfma_f32_16x16x32_bf16 v[4:7], a[144:147], a[12:15], v[4:7]\n"
  "s_waitcnt lgkmcnt(7)\n"
  "v_cvt_pk_bf16_f32 v190, v187, v191\n"
  "v_accvgpr_read_b32 v240, a236\n"
  "v_mfma_f32_16x16x32_bf16 v[4:7], a[120:123], a[16:19], v[4:7]\n"
  "v_accvgpr_read_b32 v241, a237\n"
  "v_accvgpr_read_b32 v236, a238\n"
  "v_mfma_f32_16x16x32_bf16 v[4:7], a[124:127], a[20:23], v[4:7]\n"
  "ds_write_b64 v229, v[108:109] offset:288\n"
  "v_mfma_f32_16x16x32_bf16 v[20:23], a[124:127], a[44:47], v[248:251]\n"
  "v_accvgpr_read_b32 v237, a239\n"
  "v_accvgpr_read_b32 v242, a240\n"
  "v_mfma_f32_16x16x32_bf16 v[20:23], a[120:123], a[40:43], v[20:23]\n"
  "v_accvgpr_read_b32 v243, a241\n"
  "v_accvgpr_read_b32 v238, a242\n"
  "v_mfma_f32_16x16x32_bf16 v[20:23], a[144:147], a[36:39], v[20:23]\n"
  "ds_read_b64_tr_b16 a[132:133], v217 offset:14400\n"
  "v_accvgpr_read_b32 v239, a243\n"
  "v_mfma_f32_16x16x32_bf16 v[20:23], a[148:151], a[32:35], v[20:23]\n"
  "ds_read_b64_tr_b16 a[134:135], v218 offset:14400\n"
  "v_fma_f32 v64, v64, s73, v227\n"
  "v_mfma_f32_16x16x32_bf16 v[20:23], a[140:143], a[28:31], v[20:23]\n"
  "ds_read_b64_tr_b16 a[128:129], v217 offset:14416\n"
  "v_fma_f32 v65, v65, s73, v227\n"
  "v_mfma_f32_16x16x32_bf16 v[20:23], a[136:139], a[24:27], v[20:23]\n"
  "ds_read_b64_tr_b16 a[130:131], v218 offset:14416\n"
  "ds_write_b64 v229, v[106:107] offset:576\n"
  "v_fma_f32 v66, v66, s73, v227\n"
  "v_pk_fma_f32 v[192:193], v[192:193], v[80:81], v[80:81] neg_lo:[0,0,1] neg_hi:[0,0,1]\n"
  "v_pk_fma_f32 v[196:197], v[196:197], v[80:81], v[80:81] neg_lo:[0,0,1] neg_hi:[0,0,1]\n"
  "v_pk_fma_f32 v[194:195], v[194:195], v[82:83], v[82:83] neg_lo:[0,0,1] neg_hi:[0,0,1]\n"
  "v_pk_fma_f32 v[198:199], v[198:199], v[82:83], v[82:83] neg_lo:[0,0,1] neg_hi:[0,0,1]\n"
  "v_mfma_f32_16x16x32_bf16 v[36:39], a[136:139], a[48:51], v[248:251]\n"
  "v_exp_f32 v64, v64\n"
  "v_mfma_f32_16x16x32_bf16 v[36:39], a[140:143], a[52:55], v[36:39]\n"
  "v_exp_f32 v65, v65\n"
  "v_mfma_f32_16x16x32_bf16 v[36:39], a[148:151], a[56:59], v[36:39]\n"
  "v_exp_f32_e64 v66, v66\n"
  "v_mfma_f32_16x16x32_bf16 v[36:39], a[144:147], a[60:63], v[36:39]\n"
  "v_cvt_pk_bf16_f32 v185, v192, v196\n"
  "v_cvt_pk_bf16_f32 v187, v194, v198\n"
  "v_mfma_f32_16x16x32_bf16 v[36:39], a[120:123], a[64:67], v[36:39]\n"
  "v_cvt_pk_bf16_f32 v189, v193, v197\n"
  "v_cvt_pk_bf16_f32 v191, v195, v199\n"
  "v_mfma_f32_16x16x32_bf16 v[36:39], a[124:127], a[68:71], v[36:39]\n"
  "ds_write_b64 v229, v[110:111] offset:864\n"
  "v_fma_f32 v67, v67, s73, v227\n"
  "v_cvt_pk_bf16_f32 v152, v152, v156\n"
  "v_pk_fma_f32 v[164:165], v[164:165], v[76:77], v[76:77] neg_lo:[0,0,1] neg_hi:[0,0,1]\n"
  "v_pk_fma_f32 v[166:167], v[166:167], v[78:79], v[78:79] neg_lo:[0,0,1] neg_hi:[0,0,1]\n"
  "v_pk_fma_f32 v[240:241], v[240:241], v[84:85], v[84:85] neg_lo:[0,0,1] neg_hi:[0,0,1]\n"
  "v_pk_fma_f32 v[242:243], v[242:243], v[84:85], v[84:85] neg_lo:[0,0,1] neg_hi:[0,0,1]\n"
  "v_mfma_f32_16x16x32_bf16 v[52:55], a[124:127], a[92:95], v[248:251]\n"
  "ds_read_b64_tr_b16 a[124:125], v219 offset:14400\n"
  "v_cvt_pk_bf16_f32 v156, v153, v157\n"
  "v_mfma_f32_16x16x32_bf16 v[52:55], a[120:123], a[88:91], v[52:55]\n"
  "ds_read_b64_tr_b16 a[126:127], v220 offset:14400\n"
  "ds_read_b64_tr_b16 a[252:253], v230 offset:0\n"
  "v_mfma_f32_16x16x32_bf16 v[52:55], a[144:147], a[84:87], v[52:55]\n"
  "ds_read_b64_tr_b16 a[120:121], v219 offset:14416\n"
  "ds_read_b64_tr_b16 a[254:255], v230 offset:32\n"
  "v_mfma_f32_16x16x32_bf16 v[52:55], a[148:151], a[80:83], v[52:55]\n"
  "ds_read_b64_tr_b16 a[122:123], v220 offset:14416\n"
  "ds_read_b64_tr_b16 v[244:245], v230 offset:1120\n"
  "v_mfma_f32_16x16x32_bf16 v[52:55], a[140:143], a[76:79], v[52:55]\n"
  "v_exp_f32 v67, v67\n"
  "v_mfma_f32_16x16x32_bf16 v[52:55], a[136:139], a[72:75], v[52:55]\n"
  "s_waitcnt lgkmcnt(9)\n"
  "ds_read_b64_tr_b16 v[246:247], v230 offset:1152\n"
  "v_cvt_pk_bf16_f32 v153, v160, v164\n"
  "v_mfma_f32_16x16x32_bf16 a[244:247], a[156:159], a[132:135], 1.0\n"
  "ds_write_b64 v229, v[184:185] offset:15680\n"
  "v_cvt_pk_bf16_f32 v154, v154, v158\n"
  "v_cvt_pk_bf16_f32 v158, v155, v159\n"
  "v_pk_fma_f32 v[236:237], v[236:237], v[86:87], v[86:87] neg_lo:[0,0,1] neg_hi:[0,0,1]\n"
  "v_pk_fma_f32 v[238:239], v[238:239], v[86:87], v[86:87] neg_lo:[0,0,1] neg_hi:[0,0,1]\n"
  "v_mfma_f32_16x16x32_bf16 a[248:251], a[156:159], a[128:131], 1.0\n"
  "ds_write_b64 v229, v[186:187] offset:16256\n"
  "v_cvt_pk_bf16_f32 v155, v162, v166\n"
  "v_cvt_pk_bf16_f32 v64, v64, v65\n"
  "v_mfma_f32_16x16x32_bf16 v[200:203], v[56:59], a[132:135], 1.0\n"
  "s_nop 0\n"
  "s_waitcnt lgkmcnt(5)\n"
  "buffer_store_dwordx4 a[252:255], v228, s[52:55], s80 offen offset:0 nt\n"
  "ds_read_b64_tr_b16 a[140:141], v217 offset:13376\n"
  "ds_write_b64 v229, v[188:189] offset:15968\n"
  "v_cvt_pk_bf16_f32 v65, v66, v67\n"
  "v_cvt_pk_bf16_f32 v157, v161, v165\n"
  "v_mfma_f32_16x16x32_bf16 v[204:207], v[56:59], a[128:131], 1.0\n"
  "ds_read_b64_tr_b16 a[142:143], v218 offset:13376\n"
  "ds_read_b64_tr_b16 a[252:253], v230 offset:2240\n"
  "v_mfma_f32_16x16x32_bf16 v[168:171], v[40:43], a[132:135], 1.0\n"
  "ds_read_b64_tr_b16 a[136:137], v217 offset:13392\n"
  "ds_read_b64_tr_b16 a[254:255], v230 offset:2272\n"
  "ds_write_b64 v229, v[190:191] offset:16544\n"
  "v_mfma_f32_16x16x32_bf16 v[172:175], v[40:43], a[128:131], 1.0\n"
  "ds_read_b64_tr_b16 a[138:139], v218 offset:13392\n"
  "ds_write_b64 v229, v[152:153] offset:13440\n"
  "v_cvt_pk_bf16_f32 v159, v163, v167\n"
  "v_mfma_f32_16x16x32_bf16 v[136:139], v[24:27], a[132:135], 1.0\n"
  "ds_write_b64 v229, v[154:155] offset:14016\n"
  "v_mfma_f32_16x16x32_bf16 v[140:143], v[24:27], a[128:131], 1.0\n"
  "ds_write_b64 v231, v[64:65] offset:2048\n"
  "v_cvt_pk_bf16_f32 v120, v120, v124\n"
  "v_cvt_pk_bf16_f32 v124, v121, v125\n"
  "v_mfma_f32_16x16x32_bf16 v[104:107], v[8:11], a[132:135], 1.0\n"
  "s_nop 0\n"
  "s_waitcnt lgkmcnt(13)\n"
  "buffer_store_dwordx4 v[244:247], v228, s[52:55], s80 offen offset:3072 nt\n"
  "ds_write_b64 v229, v[156:157] offset:13728\n"
  "v_cvt_pk_bf16_f32 v121, v128, v132\n"
  "v_cvt_pk_bf16_f32 v122, v122, v126\n"
  "v_cvt_pk_bf16_f32 v126, v123, v127\n"
  "v_mfma_f32_16x16x32_bf16 v[108:111], v[8:11], a[128:131], 1.0\n"
  "ds_read_b64_tr_b16 v[244:245], v230 offset:3360\n"
  "ds_read_b64_tr_b16 v[246:247], v230 offset:3392\n"
  "v_mfma_f32_16x16x32_bf16 v[112:115], v[8:11], a[124:127], 1.0\n"
  "ds_write_b64 v229, v[158:159] offset:14304\n"
  "v_cvt_pk_bf16_f32 v123, v130, v134\n"
  "v_cvt_pk_bf16_f32 v125, v129, v133\n"
  "v_mfma_f32_16x16x32_bf16 v[116:119], v[8:11], a[120:123], 1.0\n"
  "ds_write_b64 v229, v[120:121] offset:11200\n"
  "v_cvt_pk_bf16_f32 v127, v131, v135\n"
  "v_cvt_pk_bf16_f32 v88, v88, v92\n"
  "v_mfma_f32_16x16x32_bf16 v[144:147], v[24:27], a[124:127], 1.0\n"
  "ds_read_b64_tr_b16 a[132:133], v219 offset:13376\n"
  "ds_write_b64 v229, v[122:123] offset:11776\n"
  "v_cvt_pk_bf16_f32 v92, v89, v93\n"
  "v_mfma_f32_16x16x32_bf16 v[148:151], v[24:27], a[120:123], 1.0\n"
  "s_nop 0\n"
  "ds_read_b64_tr_b16 a[134:135], v220 offset:13376\n"
  "v_cvt_pk_bf16_f32 v89, v96, v100\n"
  "v_mfma_f32_16x16x32_bf16 v[176:179], v[40:43], a[124:127], 1.0\n"
  "s_waitcnt lgkmcnt(13)\n"
  "buffer_store_dwordx4 a[252:255], v228, s[52:55], s81 offen offset:0 nt\n"
  "ds_read_b64_tr_b16 a[128:129], v219 offset:13392\n"
  "ds_write_b64 v229, v[124:125] offset:11488\n"
  "v_cvt_pk_bf16_f32 v90, v90, v94\n"
  "v_cvt_pk_bf16_f32 v94, v91, v95\n"
  "v_mfma_f32_16x16x32_bf16 v[180:183], v[40:43], a[120:123], 1.0\n"
  "ds_read_b64_tr_b16 a[130:131], v220 offset:13392\n"
  "ds_write_b64 v229, v[126:127] offset:12064\n"
  "v_cvt_pk_bf16_f32 v91, v98, v102\n"
  "v_mfma_f32_16x16x32_bf16 v[208:211], v[56:59], a[124:127], 1.0\n"
  "s_nop 0\n"
  "v_cvt_pk_bf16_f32 v93, v97, v101\n"
  "v_cvt_pk_bf16_f32 v95, v99, v103\n"
  "v_mfma_f32_16x16x32_bf16 v[212:215], v[56:59], a[120:123], 1.0\n"
  "s_waitcnt lgkmcnt(15)\n"
  "ds_write_b64 v229, v[88:89] offset:8960\n"
  "v_fma_f32 v0, v0, s73, v223\n"
  "v_fma_f32 v1, v1, s73, v223\n"
  "v_mfma_f32_16x16x32_bf16 a[236:239], a[156:159], a[140:143], 1.0\n"
  "ds_write_b64 v229, v[90:91] offset:9536\n"
  "v_exp_f32_e64 v0, v0\n"
  "v_mfma_f32_16x16x32_bf16 a[240:243], a[156:159], a[136:139], 1.0\n"
  "s_nop 0\n"
  "ds_write_b64 v229, v[92:93] offset:9248\n"
  "v_mfma_f32_16x16x32_bf16 v[184:187], v[56:59], a[140:143], 1.0\n"
  "s_waitcnt lgkmcnt(12)\n"
  "buffer_store_dwordx4 v[244:247], v228, s[52:55], s81 offen offset:3072 nt\n"
  "ds_write_b64 v229, v[94:95] offset:9824\n"
  "v_exp_f32_e64 v1, v1\n"
  "v_fma_f32 v2, v2, s73, v223\n"
  "v_mfma_f32_16x16x32_bf16 v[188:191], v[56:59], a[136:139], 1.0\n"
  "v_exp_f32 v2, v2\n"
  "v_cvt_pk_bf16_f32 v240, v240, v242\n"
  "v_cvt_pk_bf16_f32 v241, v241, v243\n"
  "v_cvt_pk_bf16_f32 v236, v236, v238\n"
  "v_mfma_f32_16x16x32_bf16 v[152:155], v[40:43], a[140:143], 1.0\n"
  "s_mov_b32 m0, s67\n"
  "ds_write_b32 v233, v240 offset:19040\n"
  "v_fma_f32 v3, v3, s73, v223\n"
  "v_cvt_pk_bf16_f32 v12, v0, v1\n"
  "v_cvt_pk_bf16_f32 v237, v237, v239\n"
  "v_mfma_f32_16x16x32_bf16 v[156:159], v[40:43], a[136:139], 1.0\n"
  "s_waitcnt lgkmcnt(6)\n"
  "s_barrier\n"
  "ds_read_b128 a[124:127], v216 offset:0\n"
  "v_mfma_f32_16x16x32_bf16 v[120:123], v[24:27], a[140:143], 1.0\n"
  "ds_read_b64 a[160:161], v231 offset:2048\n"
  "ds_write_b32 v233, v241 offset:19328\n"
  "v_fma_f32 v16, v16, s73, v224\n"
  "v_accvgpr_read_b32 v240, a196\n"
  "v_mfma_f32_16x16x32_bf16 v[124:127], v[24:27], a[136:139], 1.0\n"
  "ds_read_b64 a[162:163], v232 offset:2048\n"
  "ds_write_b32 v233, v236 offset:19616\n"
  "v_fma_f32 v17, v17, s73, v224\n"
  "v_accvgpr_read_b32 v241, a197\n"
  "v_pk_fma_f32 v[240:241], v[240:241], v[76:77], v[76:77] neg_lo:[0,0,1] neg_hi:[0,0,1]\n"
  "v_mfma_f32_16x16x32_bf16 v[88:91], v[8:11], a[140:143], 1.0\n"
  "buffer_load_dwordx4 v221, s[40:43], 0 offen offset:0 lds\n"
  "v_mfma_f32_16x16x32_bf16 v[92:95], v[8:11], a[136:139], 1.0\n"
  "s_nop 0\n"
  "ds_read_b128 a[120:123], v216 offset:64\n"
  "v_fma_f32 v18, v18, s73, v224\n"
  "v_mfma_f32_16x16x32_bf16 v[96:99], v[8:11], a[132:135], 1.0\n"
  "s_addk_i32 m0, 896\n"
  "ds_read_b128 a[140:143], v216 offset:1024\n"
  "ds_write_b32 v233, v237 offset:19904\n"
  "v_fma_f32 v19, v19, s73, v224\n"
  "v_accvgpr_read_b32 v242, a200\n"
  "v_mfma_f32_16x16x32_bf16 v[100:103], v[8:11], a[128:131], 1.0\n"
  "ds_read_b128 a[136:139], v216 offset:1088\n"
  "v_fma_f32 v32, v32, s73, v225\n"
  "v_accvgpr_read_b32 v243, a201\n"
  "v_accvgpr_read_b32 v236, a204\n"
  "v_accvgpr_read_b32 v237, a205\n"
  "v_pk_fma_f32 v[242:243], v[242:243], v[76:77], v[76:77] neg_lo:[0,0,1] neg_hi:[0,0,1]\n"
  "v_pk_fma_f32 v[236:237], v[236:237], v[76:77], v[76:77] neg_lo:[0,0,1] neg_hi:[0,0,1]\n"
  "v_mfma_f32_16x16x32_bf16 v[128:131], v[24:27], a[132:135], 1.0\n"
  "buffer_load_dwordx4 v221, s[40:43], 0 offen offset:128 lds\n"
  "v_fma_f32 v33, v33, s73, v225\n"
  "v_mfma_f32_16x16x32_bf16 v[132:135], v[24:27], a[128:131], 1.0\n"
  "s_addk_i32 m0, 896\n"
  "ds_read_b128 a[144:147], v216 offset:2048\n"
  "v_fma_f32 v34, v34, s73, v225\n"
  "v_mfma_f32_16x16x32_bf16 v[160:163], v[40:43], a[132:135], 1.0\n"
  "s_nop 0\n"
  "ds_read_b128 a[148:151], v216 offset:2112\n"
  "v_fma_f32 v35, v35, s73, v225\n"
  "v_accvgpr_read_b32 v238, a208\n"
  "v_accvgpr_read_b32 v239, a209\n"
  "v_cvt_pk_bf16_f32 v240, v240, v242\n"
  "v_pk_fma_f32 v[238:239], v[238:239], v[76:77], v[76:77] neg_lo:[0,0,1] neg_hi:[0,0,1]\n"
  "v_mfma_f32_16x16x32_bf16 v[164:167], v[40:43], a[128:131], 1.0\n"
  "v_exp_f32_e64 v3, v3\n"
  "v_cvt_pk_bf16_f32 v242, v241, v243\n"
  "v_cvt_pk_bf16_f32 v241, v236, v238\n"
  "v_cvt_pk_bf16_f32 v243, v237, v239\n"
  "v_mfma_f32_16x16x32_bf16 v[192:195], v[56:59], a[132:135], 1.0\n"
  "buffer_load_dwordx4 v221, s[40:43], 0 offen offset:256 lds\n"
  "v_cvt_pk_bf16_f32 v13, v2, v3\n"
  "v_mfma_f32_16x16x32_bf16 v[196:199], v[56:59], a[128:131], 1.0\n"
  "s_waitcnt lgkmcnt(5)\n"
  "v_exp_f32 v16, v16\n"
  "v_accvgpr_read_b32 v236, a198\n"
  "v_accvgpr_read_b32 v237, a199\n"
  "v_accvgpr_read_b32 v238, a202\n"
  "v_pk_fma_f32 v[236:237], v[236:237], v[78:79], v[78:79] neg_lo:[0,0,1] neg_hi:[0,0,1]\n"
  "v_mfma_f32_16x16x32_bf16 v[64:67], a[124:127], a[96:99], v[248:251]\n"
  "s_add_u32 m0, s59, -128\n"
  "v_exp_f32 v17, v17\n"
  "v_mfma_f32_16x16x32_bf16 v[64:67], a[120:123], a[100:103], v[64:67]\n"
  "s_waitcnt lgkmcnt(2)\n"
  "v_exp_f32 v18, v18\n"
  "v_mfma_f32_16x16x32_bf16 v[64:67], a[140:143], a[104:107], v[64:67]\n"
  "buffer_load_dwordx4 v221, s[32:35], 0 offen offset:128 lds\n"
  "v_cvt_pk_bf16_f32 v28, v16, v17\n"
  "v_mfma_f32_16x16x32_bf16 v[64:67], a[136:139], a[108:111], v[64:67]\n"
  "s_waitcnt lgkmcnt(0)\n"
  "v_exp_f32 v19, v19\n"
  "v_mfma_f32_16x16x32_bf16 v[64:67], a[144:147], a[112:115], v[64:67]\n"
  "s_addk_i32 m0, 1024\n"
  "v_exp_f32 v32, v32\n"
  "v_mfma_f32_16x16x32_bf16 v[64:67], a[148:151], a[116:119], v[64:67]\n"
  "s_mov_b64 s[32:33], s[44:45]\n"
  "v_exp_f32 v33, v33\n"
  "v_mfma_f32_16x16x32_bf16 v[0:3], a[148:151], a[20:23], v[248:251]\n"
  "buffer_load_dwordx4 v221, s[44:47], 0 offen offset:128 lds\n"
  "v_cvt_pk_bf16_f32 v29, v18, v19\n"
  "v_mfma_f32_16x16x32_bf16 v[0:3], a[144:147], a[16:19], v[0:3]\n"
  "s_addk_i32 m0, 896\n"
  "v_exp_f32 v34, v34\n"
  "v_mfma_f32_16x16x32_bf16 v[0:3], a[136:139], a[12:15], v[0:3]\n"
  "s_add_u32 s40, s40, s31\n"
  "v_exp_f32 v35, v35\n"
  "v_mfma_f32_16x16x32_bf16 v[0:3], a[140:143], a[8:11], v[0:3]\n"
  "s_addc_u32 s41, s41, 0\n"
  "v_cvt_pk_bf16_f32 v44, v32, v33\n"
  "v_cvt_pk_bf16_f32 v45, v34, v35\n"
  "v_mfma_f32_16x16x32_bf16 v[0:3], a[120:123], a[4:7], v[0:3]\n"
  "buffer_load_dwordx4 v221, s[44:47], 0 offen offset:256 lds\n"
  "v_fma_f32 v48, v48, s73, v226\n"
  "v_mfma_f32_16x16x32_bf16 v[0:3], a[124:127], a[0:3], v[0:3]\n"
  "s_add_u32 s44, s44, s31\n"
  "v_exp_f32 v48, v48\n"
  "v_accvgpr_read_b32 v239, a203\n"
  "v_mfma_f32_16x16x32_bf16 v[16:19], a[124:127], a[24:27], v[248:251]\n"
  "s_addc_u32 s45, s45, 0\n"
  "v_fma_f32 v49, v49, s73, v226\n"
  "v_fma_f32 v50, v50, s73, v226\n"
  "v_mfma_f32_16x16x32_bf16 v[16:19], a[120:123], a[28:31], v[16:19]\n"
  "v_exp_f32 v49, v49\n"
  "v_mfma_f32_16x16x32_bf16 v[16:19], a[140:143], a[32:35], v[16:19]\n"
  "ds_read_b128 a[132:135], v235 offset:0\n"
  "v_fma_f32 v51, v51, s73, v226\n"
  "v_mfma_f32_16x16x32_bf16 v[16:19], a[136:139], a[36:39], v[16:19]\n"
  "ds_read_b128 a[128:131], v235 offset:64\n"
  "v_cvt_pk_bf16_f32 v60, v48, v49\n"
  "v_mfma_f32_16x16x32_bf16 v[16:19], a[144:147], a[40:43], v[16:19]\n"
  "v_exp_f32 v50, v50\n"
  "v_mfma_f32_16x16x32_bf16 v[16:19], a[148:151], a[44:47], v[16:19]\n"
  "v_exp_f32 v51, v51\n"
  "v_mfma_f32_16x16x32_bf16 v[32:35], a[148:151], a[68:71], v[248:251]\n"
  "v_cvt_pk_bf16_f32 v61, v50, v51\n"
  "v_fma_f32 v4, v4, s73, v223\n"
  "v_mfma_f32_16x16x32_bf16 v[32:35], a[144:147], a[64:67], v[32:35]\n"
  "v_exp_f32 v4, v4\n"
  "v_mfma_f32_16x16x32_bf16 v[32:35], a[136:139], a[60:63], v[32:35]\n"
  "v_fma_f32 v5, v5, s73, v223\n"
  "v_fma_f32 v6, v6, s73, v223\n"
  "v_mfma_f32_16x16x32_bf16 v[32:35], a[140:143], a[56:59], v[32:35]\n"
  "v_exp_f32 v5, v5\n"
  "v_mfma_f32_16x16x32_bf16 v[32:35], a[120:123], a[52:55], v[32:35]\n"
  "v_exp_f32 v6, v6\n"
  "v_mfma_f32_16x16x32_bf16 v[32:35], a[124:127], a[48:51], v[32:35]\n"
  "v_fma_f32 v7, v7, s73, v223\n"
  "v_cvt_pk_bf16_f32 v14, v4, v5\n"
  "v_mfma_f32_16x16x32_bf16 v[48:51], a[124:127], a[72:75], v[248:251]\n"
  "ds_read_b128 a[124:127], v235 offset:1024\n"
  "v_fma_f32 v20, v20, s73, v224\n"
  "v_mfma_f32_16x16x32_bf16 v[48:51], a[120:123], a[76:79], v[48:51]\n"
  "ds_read_b128 a[120:123], v235 offset:1088\n"
  "v_fma_f32 v21, v21, s73, v224\n"
  "v_mfma_f32_16x16x32_bf16 v[48:51], a[140:143], a[80:83], v[48:51]\n"
  "ds_read_b128 a[140:143], v235 offset:2048\n"
  "v_fma_f32 v22, v22, s73, v224\n"
  "v_mfma_f32_16x16x32_bf16 v[48:51], a[136:139], a[84:87], v[48:51]\n"
  "ds_read_b128 a[136:139], v235 offset:2112\n"
  "v_fma_f32 v23, v23, s73, v224\n"
  "v_mfma_f32_16x16x32_bf16 v[48:51], a[144:147], a[88:91], v[48:51]\n"
  "v_exp_f32 v7, v7\n"
  "v_mfma_f32_16x16x32_bf16 v[48:51], a[148:151], a[92:95], v[48:51]\n"
  "s_waitcnt lgkmcnt(4)\n"
  "v_cvt_pk_bf16_f32 v15, v6, v7\n"
  "v_fma_f32 v36, v36, s73, v225\n"
  "v_mfma_f32_16x16x32_bf16 v[4:7], a[132:135], a[0:3], v[248:251]\n"
  "v_exp_f32 v20, v20\n"
  "v_mfma_f32_16x16x32_bf16 v[4:7], a[128:131], a[4:7], v[4:7]\n"
  "s_waitcnt lgkmcnt(2)\n"
  "v_exp_f32 v21, v21\n"
  "v_mfma_f32_16x16x32_bf16 v[4:7], a[124:127], a[8:11], v[4:7]\n"
  "v_exp_f32 v22, v22\n"
  "v_mfma_f32_16x16x32_bf16 v[4:7], a[120:123], a[12:15], v[4:7]\n"
  "s_waitcnt lgkmcnt(0)\n"
  "v_exp_f32_e64 v23, v23\n"
  "v_mfma_f32_16x16x32_bf16 v[4:7], a[140:143], a[16:19], v[4:7]\n"
  "v_cvt_pk_bf16_f32 v30, v20, v21\n"
  "v_cvt_pk_bf16_f32 v31, v22, v23\n"
  "v_mfma_f32_16x16x32_bf16 v[4:7], a[136:139], a[20:23], v[4:7]\n"
  "v_exp_f32 v36, v36\n"
  "v_fma_f32 v37, v37, s73, v225\n"
  "v_exp_f32 v37, v37\n"
  "v_mfma_f32_16x16x32_bf16 v[20:23], a[136:139], a[44:47], v[248:251]\n"
  "v_fma_f32 v38, v38, s73, v225\n"
  "v_fma_f32 v39, v39, s73, v225\n"
  "v_mfma_f32_16x16x32_bf16 v[20:23], a[140:143], a[40:43], v[20:23]\n"
  "v_exp_f32 v38, v38\n"
  "v_mfma_f32_16x16x32_bf16 v[20:23], a[120:123], a[36:39], v[20:23]\n"
  "ds_read_b64_tr_b16 a[144:145], v217 offset:2048\n"
  "v_cvt_pk_bf16_f32 v46, v36, v37\n"
  "v_mfma_f32_16x16x32_bf16 v[20:23], a[124:127], a[32:35], v[20:23]\n"
  "ds_read_b64_tr_b16 a[146:147], v218 offset:2048\n"
  "v_fma_f32 v52, v52, s73, v226\n"
  "v_mfma_f32_16x16x32_bf16 v[20:23], a[128:131], a[28:31], v[20:23]\n"
  "ds_read_b64_tr_b16 a[148:149], v217 offset:2064\n"
  "v_exp_f32 v39, v39\n"
  "v_mfma_f32_16x16x32_bf16 v[20:23], a[132:135], a[24:27], v[20:23]\n"
  "ds_read_b64_tr_b16 a[150:151], v218 offset:2064\n"
  "ds_write_b64 v229, v[240:241] offset:4480\n"
  "v_cvt_pk_bf16_f32 v47, v38, v39\n"
  "v_exp_f32 v52, v52\n"
  "v_pk_fma_f32 v[238:239], v[238:239], v[78:79], v[78:79] neg_lo:[0,0,1] neg_hi:[0,0,1]\n"
  "v_mfma_f32_16x16x32_bf16 v[36:39], a[132:135], a[48:51], v[248:251]\n"
  "v_fma_f32 v53, v53, s73, v226\n"
  "v_fma_f32 v54, v54, s73, v226\n"
  "v_mfma_f32_16x16x32_bf16 v[36:39], a[128:131], a[52:55], v[36:39]\n"
  "v_exp_f32 v53, v53\n"
  "v_mfma_f32_16x16x32_bf16 v[36:39], a[124:127], a[56:59], v[36:39]\n"
  "v_exp_f32 v54, v54\n"
  "v_mfma_f32_16x16x32_bf16 v[36:39], a[120:123], a[60:63], v[36:39]\n"
  "v_fma_f32 v55, v55, s73, v226\n"
  "v_cvt_pk_bf16_f32 v62, v52, v53\n"
  "v_mfma_f32_16x16x32_bf16 v[36:39], a[140:143], a[64:67], v[36:39]\n"
  "v_exp_f32 v55, v55\n"
  "v_mfma_f32_16x16x32_bf16 v[36:39], a[136:139], a[68:71], v[36:39]\n"
  "v_cvt_pk_bf16_f32 v63, v54, v55\n"
  "v_accvgpr_read_b32 v240, a206\n"
  "v_mfma_f32_16x16x32_bf16 v[52:55], a[136:139], a[92:95], v[248:251]\n"
  "ds_read_b64_tr_b16 a[136:137], v219 offset:2048\n"
  "v_accvgpr_read_b32 v241, a207\n"
  "v_mfma_f32_16x16x32_bf16 v[52:55], a[140:143], a[88:91], v[52:55]\n"
  "ds_read_b64_tr_b16 a[138:139], v220 offset:2048\n"
  "v_cvt_pk_bf16_f32 v236, v236, v238\n"
  "v_mfma_f32_16x16x32_bf16 v[52:55], a[120:123], a[84:87], v[52:55]\n"
  "ds_read_b64_tr_b16 a[140:141], v219 offset:2064\n"
  "v_cvt_pk_bf16_f32 v238, v237, v239\n"
  "v_mfma_f32_16x16x32_bf16 v[52:55], a[124:127], a[80:83], v[52:55]\n"
  "ds_read_b64_tr_b16 a[142:143], v220 offset:2064\n"
  "v_fma_f32 v64, v64, s73, v227\n"
  "v_mfma_f32_16x16x32_bf16 v[52:55], a[128:131], a[76:79], v[52:55]\n"
  "v_exp_f32 v64, v64\n"
  "v_mfma_f32_16x16x32_bf16 v[52:55], a[132:135], a[72:75], v[52:55]\n"
  "s_waitcnt lgkmcnt(5)\n"
  "ds_write_b64 v229, v[242:243] offset:4768\n"
  "v_fma_f32 v65, v65, s73, v227\n"
  "v_fma_f32 v66, v66, s73, v227\n"
  "v_pk_fma_f32 v[240:241], v[240:241], v[78:79], v[78:79] neg_lo:[0,0,1] neg_hi:[0,0,1]\n"
  "v_mfma_f32_16x16x32_bf16 v[104:107], v[12:15], a[144:147], v[104:107]\n"
  "v_accvgpr_read_b32 v242, a210\n"
  "v_accvgpr_read_b32 v243, a211\n"
  "v_exp_f32_e64 v65, v65\n"
  "v_pk_fma_f32 v[242:243], v[242:243], v[78:79], v[78:79] neg_lo:[0,0,1] neg_hi:[0,0,1]\n"
  "v_mfma_f32_16x16x32_bf16 v[108:111], v[12:15], a[148:151], v[108:111]\n"
  "v_cvt_pk_bf16_f32 v237, v240, v242\n"
  "v_cvt_pk_bf16_f32 v239, v241, v243\n"
  "v_mfma_f32_16x16x32_bf16 v[136:139], v[28:31], a[144:147], v[136:139]\n"
  "ds_read_b64_tr_b16 a[120:121], v217 offset:0\n"
  "ds_write_b64 v229, v[236:237] offset:5056\n"
  "v_accvgpr_read_b32 v240, a212\n"
  "v_mfma_f32_16x16x32_bf16 v[140:143], v[28:31], a[148:151], v[140:143]\n"
  "ds_read_b64_tr_b16 a[122:123], v218 offset:0\n"
  "v_accvgpr_read_b32 v241, a213\n"
  "v_mfma_f32_16x16x32_bf16 v[168:171], v[44:47], a[144:147], v[168:171]\n"
  "ds_read_b64_tr_b16 a[124:125], v217 offset:1024\n"
  "ds_write_b64 v229, v[238:239] offset:5344\n"
  "v_accvgpr_read_b32 v242, a216\n"
  "v_pk_fma_f32 v[240:241], v[240:241], v[80:81], v[80:81] neg_lo:[0,0,1] neg_hi:[0,0,1]\n"
  "v_mfma_f32_16x16x32_bf16 v[172:175], v[44:47], a[148:151], v[172:175]\n"
  "ds_read_b64_tr_b16 a[126:127], v218 offset:1024\n"
  "v_accvgpr_read_b32 v243, a217\n"
  "v_accvgpr_read_b32 v236, a220\n"
  "v_accvgpr_read_b32 v237, a221\n"
  "v_accvgpr_read_b32 v238, a224\n"
  "v_pk_fma_f32 v[242:243], v[242:243], v[80:81], v[80:81] neg_lo:[0,0,1] neg_hi:[0,0,1]\n"
  "v_pk_fma_f32 v[236:237], v[236:237], v[80:81], v[80:81] neg_lo:[0,0,1] neg_hi:[0,0,1]\n"
  "v_mfma_f32_16x16x32_bf16 v[200:203], v[60:63], a[144:147], v[200:203]\n"
  "v_accvgpr_read_b32 v239, a225\n"
  "v_cvt_pk_bf16_f32 v240, v240, v242\n"
  "v_mfma_f32_16x16x32_bf16 v[204:207], v[60:63], a[148:151], v[204:207]\n"
  "ds_read_b64_tr_b16 a[252:253], v230 offset:4480\n"
  "ds_read_b64_tr_b16 a[254:255], v230 offset:4512\n"
  "v_cvt_pk_bf16_f32 v242, v241, v243\n"
  "v_exp_f32 v66, v66\n"
  "v_pk_fma_f32 v[238:239], v[238:239], v[80:81], v[80:81] neg_lo:[0,0,1] neg_hi:[0,0,1]\n"
  "v_mfma_f32_16x16x32_bf16 a[244:247], a[160:163], a[144:147], a[244:247]\n"
  "v_cvt_pk_bf16_f32 v241, v236, v238\n"
  "v_cvt_pk_bf16_f32 v243, v237, v239\n"
  "v_mfma_f32_16x16x32_bf16 a[248:251], a[160:163], a[148:151], a[248:251]\n"
  "s_waitcnt lgkmcnt(9)\n"
  "ds_write_b64 v229, v[240:241] offset:6720\n"
  "v_accvgpr_read_b32 v236, a214\n"
  "v_accvgpr_read_b32 v237, a215\n"
  "v_pk_fma_f32 v[236:237], v[236:237], v[82:83], v[82:83] neg_lo:[0,0,1] neg_hi:[0,0,1]\n"
  "v_mfma_f32_16x16x32_bf16 v[208:211], v[60:63], a[136:139], v[208:211]\n"
  "ds_write_b64 v229, v[242:243] offset:7008\n"
  "v_accvgpr_read_b32 v238, a218\n"
  "v_accvgpr_read_b32 v239, a219\n"
  "v_pk_fma_f32 v[238:239], v[238:239], v[82:83], v[82:83] neg_lo:[0,0,1] neg_hi:[0,0,1]\n"
  "v_mfma_f32_16x16x32_bf16 v[212:215], v[60:63], a[140:143], v[212:215]\n"
  "v_accvgpr_read_b32 v240, a222\n"
  "v_accvgpr_read_b32 v241, a223\n"
  "v_mfma_f32_16x16x32_bf16 v[176:179], v[44:47], a[136:139], v[176:179]\n"
  "ds_read_b64_tr_b16 a[128:129], v217 offset:16\n"
  "v_accvgpr_read_b32 v242, a226\n"
  "v_accvgpr_read_b32 v243, a227\n"
  "v_cvt_pk_bf16_f32 v236, v236, v238\n"
  "v_cvt_pk_bf16_f32 v238, v237, v239\n"
  "v_pk_fma_f32 v[240:241], v[240:241], v[82:83], v[82:83] neg_lo:[0,0,1] neg_hi:[0,0,1]\n"
  "v_pk_fma_f32 v[242:243], v[242:243], v[82:83], v[82:83] neg_lo:[0,0,1] neg_hi:[0,0,1]\n"
  "v_mfma_f32_16x16x32_bf16 v[180:183], v[44:47], a[140:143], v[180:183]\n"
  "ds_read_b64_tr_b16 a[130:131], v218 offset:16\n"
  "v_cvt_pk_bf16_f32 v237, v240, v242\n"
  "v_mfma_f32_16x16x32_bf16 v[144:147], v[28:31], a[136:139], v[144:147]\n"
  "ds_read_b64_tr_b16 a[132:133], v217 offset:1040\n"
  "ds_write_b64 v229, v[236:237] offset:7296\n"
  "v_cvt_pk_bf16_f32 v239, v241, v243\n"
  "v_mfma_f32_16x16x32_bf16 v[148:151], v[28:31], a[140:143], v[148:151]\n"
  "ds_read_b64_tr_b16 a[134:135], v218 offset:1040\n"
  "ds_write_b64 v229, v[238:239] offset:7584\n"
  "v_accvgpr_read_b32 v240, a228\n"
  "v_mfma_f32_16x16x32_bf16 v[112:115], v[12:15], a[136:139], v[112:115]\n"
  "s_nop 0\n"
  "v_accvgpr_read_b32 v241, a229\n"
  "v_accvgpr_read_b32 v236, a230\n"
  "v_mfma_f32_16x16x32_bf16 v[116:119], v[12:15], a[140:143], v[116:119]\n"
  "s_waitcnt lgkmcnt(10)\n"
  "v_accvgpr_read_b32 v237, a231\n"
  "v_accvgpr_read_b32 v242, a232\n"
  "v_accvgpr_read_b32 v243, a233\n"
  "v_accvgpr_read_b32 v238, a234\n"
  "v_accvgpr_read_b32 v239, a235\n"
  "v_pk_fma_f32 v[240:241], v[240:241], v[84:85], v[84:85] neg_lo:[0,0,1] neg_hi:[0,0,1]\n"
  "v_pk_fma_f32 v[242:243], v[242:243], v[84:85], v[84:85] neg_lo:[0,0,1] neg_hi:[0,0,1]\n"
  "v_pk_fma_f32 v[236:237], v[236:237], v[86:87], v[86:87] neg_lo:[0,0,1] neg_hi:[0,0,1]\n"
  "v_pk_fma_f32 v[238:239], v[238:239], v[86:87], v[86:87] neg_lo:[0,0,1] neg_hi:[0,0,1]\n"
  "v_mfma_f32_16x16x32_bf16 a[164:167], v[8:11], a[120:123], 1.0\n"
  "v_fma_f32 v67, v67, s73, v227\n"
  "v_cvt_pk_bf16_f32 v240, v240, v242\n"
  "v_mfma_f32_16x16x32_bf16 a[164:167], v[12:15], a[124:127], a[164:167]\n"
  "v_exp_f32_e64 v67, v67\n"
  "v_mfma_f32_16x16x32_bf16 a[180:183], v[24:27], a[120:123], 1.0\n"
  "ds_write_b32 v233, v240 offset:17920\n"
  "v_mfma_f32_16x16x32_bf16 a[180:183], v[28:31], a[124:127], a[180:183]\n"
  "v_cvt_pk_bf16_f32 v241, v241, v243\n"
  "v_cvt_pk_bf16_f32 v236, v236, v238\n"
  "v_cvt_pk_bf16_f32 v237, v237, v239\n"
  "v_cvt_pk_bf16_f32 v64, v64, v65\n"
  "v_cvt_pk_bf16_f32 v65, v66, v67\n"
  "v_mfma_f32_16x16x32_bf16 a[196:199], v[40:43], a[120:123], 1.0\n"
  "s_mov_b32 m0, s68\n"
  "ds_read_b64_tr_b16 a[136:137], v219 offset:0\n"
  "v_fma_f32 v0, v0, s73, v223\n"
  "v_mfma_f32_16x16x32_bf16 a[196:199], v[44:47], a[124:127], a[196:199]\n"
  "s_nop 0\n"
  "ds_read_b64_tr_b16 a[138:139], v220 offset:0\n"
  "ds_read_b64_tr_b16 v[244:245], v230 offset:5600\n"
  "ds_write_b32 v233, v241 offset:18208\n"
  "v_fma_f32 v1, v1, s73, v223\n"
  "v_mfma_f32_16x16x32_bf16 a[212:215], v[56:59], a[120:123], 1.0\n"
  "ds_read_b64_tr_b16 a[140:141], v219 offset:1024\n"
  "v_fma_f32 v2, v2, s73, v223\n"
  "v_mfma_f32_16x16x32_bf16 a[212:215], v[60:63], a[124:127], a[212:215]\n"
  "ds_read_b64_tr_b16 a[142:143], v220 offset:1024\n"
  "v_fma_f32 v3, v3, s73, v223\n"
  "v_mfma_f32_16x16x32_bf16 a[228:231], a[156:159], a[120:123], 1.0\n"
  "buffer_load_dwordx4 v221, s[40:43], 0 offen offset:0 lds\n"
  "v_mfma_f32_16x16x32_bf16 a[228:231], a[160:163], a[124:127], a[228:231]\n"
  "s_waitcnt lgkmcnt(8)\n"
  "ds_write_b32 v233, v236 offset:18496\n"
  "v_mfma_f32_16x16x32_bf16 a[232:235], a[156:159], a[128:131], 1.0\n"
  "s_addk_i32 m0, 896\n"
  "ds_write_b32 v233, v237 offset:18784\n"
  "v_mfma_f32_16x16x32_bf16 a[232:235], a[160:163], a[132:135], a[232:235]\n"
  "ds_write_b64 v231, v[64:65] offset:0\n"
  "v_mfma_f32_16x16x32_bf16 a[216:219], v[56:59], a[128:131], 1.0\n"
  "buffer_load_dwordx4 v221, s[40:43], 0 offen offset:128 lds\n"
  "v_fma_f32 v16, v16, s73, v224\n"
  "v_mfma_f32_16x16x32_bf16 a[216:219], v[60:63], a[132:135], a[216:219]\n"
  "s_addk_i32 m0, 896\n"
  "ds_read_b64_tr_b16 a[120:121], v219 offset:16\n"
  "v_fma_f32 v17, v17, s73, v224\n"
  "v_mfma_f32_16x16x32_bf16 a[200:203], v[40:43], a[128:131], 1.0\n"
  "ds_read_b64_tr_b16 a[122:123], v220 offset:16\n"
  "v_fma_f32 v18, v18, s73, v224\n"
  "v_mfma_f32_16x16x32_bf16 a[200:203], v[44:47], a[132:135], a[200:203]\n"
  "ds_read_b64_tr_b16 a[124:125], v219 offset:1040\n"
  "v_fma_f32 v19, v19, s73, v224\n"
  "v_mfma_f32_16x16x32_bf16 a[184:187], v[24:27], a[128:131], 1.0\n"
  "buffer_load_dwordx4 v221, s[40:43], 0 offen offset:256 lds\n"
  "v_fma_f32 v32, v32, s73, v225\n"
  "v_mfma_f32_16x16x32_bf16 a[184:187], v[28:31], a[132:135], a[184:187]\n"
  "s_mov_b32 m0, s60\n"
  "ds_read_b64_tr_b16 a[126:127], v220 offset:1040\n"
  "v_fma_f32 v33, v33, s73, v225\n"
  "v_mfma_f32_16x16x32_bf16 a[168:171], v[8:11], a[128:131], 1.0\n"
  "s_add_u32 s28, s96, 184448\n"
  "v_exp_f32 v0, v0\n"
  "v_mfma_f32_16x16x32_bf16 a[168:171], v[12:15], a[132:135], a[168:171]\n"
  "s_waitcnt lgkmcnt(7)\n"
  "v_exp_f32 v1, v1\n"
  "v_mfma_f32_16x16x32_bf16 a[172:175], v[8:11], a[136:139], 1.0\n"
  "buffer_load_dwordx4 v221, s[32:35], 0 offen offset:0 lds\n"
  "v_add_f32 v252, v0, v1\n"
  "v_mfma_f32_16x16x32_bf16 a[172:175], v[12:15], a[140:143], a[172:175]\n"
  "s_addk_i32 m0, 1024\n"
  "v_exp_f32 v2, v2\n"
  "v_mfma_f32_16x16x32_bf16 a[188:191], v[24:27], a[136:139], 1.0\n"
  "s_add_u32 s40, s40, s31\n"
  "v_exp_f32 v3, v3\n"
  "v_mfma_f32_16x16x32_bf16 a[188:191], v[28:31], a[140:143], a[188:191]\n"
  "s_addc_u32 s41, s41, 0\n"
  "v_exp_f32 v16, v16\n"
  "v_mfma_f32_16x16x32_bf16 a[204:207], v[40:43], a[136:139], 1.0\n"
  "buffer_load_dwordx4 v221, s[44:47], 0 offen offset:0 lds\n"
  "v_add_f32 v252, v252, v2\n"
  "v_mfma_f32_16x16x32_bf16 a[204:207], v[44:47], a[140:143], a[204:207]\n"
  "s_addk_i32 m0, 768\n"
  "v_exp_f32 v17, v17\n"
  "v_mfma_f32_16x16x32_bf16 a[220:223], v[56:59], a[136:139], 1.0\n"
  "s_mov_b64 s[32:33], s[44:45]\n"
  "v_exp_f32 v18, v18\n"
  "v_mfma_f32_16x16x32_bf16 a[220:223], v[60:63], a[140:143], a[220:223]\n"
  "s_waitcnt lgkmcnt(0)\n"
  "v_exp_f32 v19, v19\n"
  "v_mfma_f32_16x16x32_bf16 a[224:227], v[56:59], a[120:123], 1.0\n"
  "buffer_load_dwordx4 v221, s[44:47], 0 offen offset:256 lds\n"
  "v_add_f32 v252, v252, v3\n"
  "v_mfma_f32_16x16x32_bf16 a[224:227], v[60:63], a[124:127], a[224:227]\n"
  "s_add_u32 s44, s44, s31\n"
  "v_exp_f32 v32, v32\n"
  "v_mfma_f32_16x16x32_bf16 a[208:211], v[40:43], a[120:123], 1.0\n"
  "s_addc_u32 s45, s45, 0\n"
  "v_exp_f32 v33, v33\n"
  "v_mfma_f32_16x16x32_bf16 a[208:211], v[44:47], a[124:127], a[208:211]\n"
  "s_cmp_eq_u32 s95, 0\n"
  "s_cselect_b32 s95, 3, s95\n"
  "s_subb_u32 s100, s100, -11\n"
  "v_add_f32 v253, v16, v17\n"
  "v_add_f32 v253, v253, v18\n"
  "v_mfma_f32_16x16x32_bf16 a[192:195], v[24:27], a[120:123], 1.0\n"
  "s_sub_u32 s95, s95, 1\n"
  "v_add_f32 v253, v253, v19\n"
  "v_fma_f32 v34, v34, s73, v225\n"
  "v_mfma_f32_16x16x32_bf16 a[192:195], v[28:31], a[124:127], a[192:195]\n"
  "s_waitcnt vmcnt(6)\n"
  "s_barrier\n"
  "ds_read_b128 a[128:131], v216 offset:12352\n"
  "v_mfma_f32_16x16x32_bf16 a[176:179], v[8:11], a[120:123], 1.0\n"
  "s_lshr_b32 s79, s100, 0\n"
  "ds_read_b128 a[132:135], v216 offset:12416\n"
  "v_fma_f32 v35, v35, s73, v225\n"
  "v_mfma_f32_16x16x32_bf16 a[176:179], v[12:15], a[124:127], a[176:179]\n"
  "s_mul_i32 s79, s79, 7\n"
  "ds_read_b128 a[120:123], v216 offset:13376\n"
  "v_add_f32 v254, v32, v33\n"
  "v_mfma_f32_16x16x32_bf16 v[68:71], v[8:11], a[152:155], 0\n"
  "s_add_u32 s79, s79, s100\n"
  "ds_read_b128 a[124:127], v216 offset:13440\n"
  "v_cvt_pk_bf16_f32 v8, v0, v1\n"
  "v_mfma_f32_16x16x32_bf16 v[68:71], v[12:15], a[152:155], v[68:71]\n"
  "s_add_u32 s79, s79, s101\n"
  "ds_read_b128 a[136:139], v216 offset:14400\n"
  "v_cvt_pk_bf16_f32 v9, v2, v3\n"
  "v_mfma_f32_16x16x32_bf16 v[72:75], v[24:27], a[152:155], 0\n"
  "ds_read_b128 a[140:143], v216 offset:14464\n"
  "v_cvt_pk_bf16_f32 v24, v16, v17\n"
  "v_mfma_f32_16x16x32_bf16 v[72:75], v[28:31], a[152:155], v[72:75]\n"
  "s_mov_b32 m0, s66\n"
  "v_exp_f32 v34, v34\n"
  "v_mfma_f32_16x16x32_bf16 v[76:79], v[40:43], a[152:155], 0\n"
  "s_min_u32 s79, s79, 0x1ff\n"
  "v_exp_f32 v35, v35\n"
  "v_mfma_f32_16x16x32_bf16 v[76:79], v[44:47], a[152:155], v[76:79]\n"
  "s_mul_i32 s96, s79, 196608\n"
  "v_cvt_pk_bf16_f32 v25, v18, v19\n"
  "v_add_f32 v254, v254, v34\n"
  "v_mfma_f32_16x16x32_bf16 v[80:83], v[56:59], a[152:155], 0\n"
  "s_mul_i32 s92, s79, 325632\n"
  "v_add_f32 v254, v254, v35\n"
  "v_cvt_pk_bf16_f32 v40, v32, v33\n"
  "v_mfma_f32_16x16x32_bf16 v[80:83], v[60:63], a[152:155], v[80:83]\n"
  "buffer_load_dwordx4 v221, s[40:43], 0 offen offset:0 lds\n"
  "v_mfma_f32_16x16x32_bf16 v[84:87], a[156:159], a[152:155], 0\n"
  "s_add_u32 s96, s96, s39\n"
  "ds_read_b64 a[156:157], v231 offset:0\n"
  "v_cvt_pk_bf16_f32 v41, v34, v35\n"
  "v_mfma_f32_16x16x32_bf16 v[84:87], a[160:163], a[152:155], v[84:87]\n"
  "L_CUT_%=:\n"
  "s_waitcnt lgkmcnt(5)\n"
  "ds_read_b64 a[158:159], v232 offset:0\n"
  "v_fma_f32 v48, v48, s73, v226\n"
  "v_mfma_f32_16x16x32_bf16 v[64:67], a[128:131], a[96:99], v[248:251]\n"
  "s_addk_i32 m0, 896\n"
  "v_exp_f32 v48, v48\n"
  "v_mfma_f32_16x16x32_bf16 v[64:67], a[132:135], a[100:103], v[64:67]\n"
  "s_waitcnt lgkmcnt(4)\n"
  "buffer_load_dwordx4 v221, s[40:43], 0 offen offset:128 lds\n"
  "v_fma_f32 v49, v49, s73, v226\n"
  "v_mfma_f32_16x16x32_bf16 v[64:67], a[120:123], a[104:107], v[64:67]\n"
  "s_addk_i32 m0, 896\n"
  "v_exp_f32 v49, v49\n"
  "v_mfma_f32_16x16x32_bf16 v[64:67], a[124:127], a[108:111], v[64:67]\n"
  "s_waitcnt lgkmcnt(2)\n"
  "v_fma_f32 v50, v50, s73, v226\n"
  "v_fma_f32 v51, v51, s73, v226\n"
  "v_mfma_f32_16x16x32_bf16 v[64:67], a[136:139], a[112:115], v[64:67]\n"
  "s_mul_i32 s79, s95, 110592\n"
  "v_exp_f32 v50, v50\n"
  "v_mfma_f32_16x16x32_bf16 v[64:67], a[140:143], a[116:119], v[64:67]\n"
  "buffer_load_dwordx4 v221, s[40:43], 0 offen offset:256 lds\n"
  "v_add_f32 v255, v48, v49\n"
  "v_mfma_f32_16x16x32_bf16 v[0:3], a[140:143], a[20:23], v[248:251]\n"
  "s_add_u32 s92, s92, s79\n"
  "v_exp_f32 v51, v51\n"
  "v_mfma_f32_16x16x32_bf16 v[0:3], a[136:139], a[16:19], v[0:3]\n"
  "s_add_u32 s71, s92, s80\n"
  "v_add_f32 v255, v255, v50\n"
  "v_add_f32 v255, v255, v51\n"
  "v_mfma_f32_16x16x32_bf16 v[0:3], a[124:127], a[12:15], v[0:3]\n"
  "v_cvt_pk_bf16_f32 v56, v48, v49\n"
  "v_cvt_pk_bf16_f32 v57, v50, v51\n"
  "v_mfma_f32_16x16x32_bf16 v[0:3], a[120:123], a[8:11], v[0:3]\n"
  "s_add_u32 m0, s58, -128\n"
  "v_fma_f32 v52, v52, s73, v226\n"
  "v_mfma_f32_16x16x32_bf16 v[0:3], a[132:135], a[4:7], v[0:3]\n"
  "s_add_u32 s48, s84, s71\n"
  "v_exp_f32 v52, v52\n"
  "v_mfma_f32_16x16x32_bf16 v[0:3], a[128:131], a[0:3], v[0:3]\n"
  "s_addc_u32 s49, s85, 0\n"
  "v_fma_f32 v53, v53, s73, v226\n"
  "v_fma_f32 v54, v54, s73, v226\n"
  "v_mfma_f32_16x16x32_bf16 v[16:19], a[128:131], a[24:27], v[248:251]\n"
  "s_cmp_eq_u32 s95, 2\n"
  "s_cselect_b32 s94, 17, 18\n"
  "v_exp_f32 v53, v53\n"
  "v_mfma_f32_16x16x32_bf16 v[16:19], a[132:135], a[28:31], v[16:19]\n"
  "buffer_load_dwordx4 v221, s[32:35], 0 offen offset:128 lds\n"
  "v_fma_f32 v55, v55, s73, v226\n"
  "v_mfma_f32_16x16x32_bf16 v[16:19], a[120:123], a[32:35], v[16:19]\n"
  "s_addk_i32 m0, 1024\n"
  "ds_read_b64_tr_b16 a[144:145], v217 offset:12352\n"
  "v_cvt_pk_bf16_f32 v58, v52, v53\n"
  "v_mfma_f32_16x16x32_bf16 v[16:19], a[124:127], a[36:39], v[16:19]\n"
  "s_mul_i32 s94, s94, 6144\n"
  "ds_read_b64_tr_b16 a[146:147], v218 offset:12352\n"
  "v_add_f32 v255, v255, v52\n"
  "v_mfma_f32_16x16x32_bf16 v[16:19], a[136:139], a[40:43], v[16:19]\n"
  "s_cmp_lt_i32 s76, 5\n"
  "s_cselect_b32 s38, s94, 0\n"
  "s_cselect_b32 s28, s96, s28\n"
  "ds_read_b64_tr_b16 a[148:149], v217 offset:13376\n"
  "v_mfma_f32_16x16x32_bf16 v[16:19], a[140:143], a[44:47], v[16:19]\n"
  "buffer_load_dwordx4 v221, s[44:47], 0 offen offset:128 lds\n"
  "v_fma_f32 v36, v36, s73, v225\n"
  "v_mfma_f32_16x16x32_bf16 v[32:35], a[140:143], a[68:71], v[248:251]\n"
  "s_addk_i32 m0, 896\n"
  "ds_read_b64_tr_b16 a[150:151], v218 offset:13376\n"
  "v_fma_f32 v37, v37, s73, v225\n"
  "v_mfma_f32_16x16x32_bf16 v[32:35], a[136:139], a[64:67], v[32:35]\n"
  "s_sub_i32 s71, s38, s80\n"
  "v_exp_f32 v54, v54\n"
  "v_mfma_f32_16x16x32_bf16 v[32:35], a[124:127], a[60:63], v[32:35]\n"
  "s_max_i32 s50, s71, 0\n"
  "v_exp_f32 v55, v55\n"
  "v_mfma_f32_16x16x32_bf16 v[32:35], a[120:123], a[56:59], v[32:35]\n"
  "buffer_load_dwordx4 v221, s[44:47], 0 offen offset:256 lds\n"
  "v_cvt_pk_bf16_f32 v59, v54, v55\n"
  "v_mfma_f32_16x16x32_bf16 v[32:35], a[132:135], a[52:55], v[32:35]\n"
  "s_add_u32 s40, s40, s31\n"
  "v_exp_f32 v36, v36\n"
  "v_mfma_f32_16x16x32_bf16 v[32:35], a[128:131], a[48:51], v[32:35]\n"
  "s_addc_u32 s41, s41, 0\n"
  "v_exp_f32 v37, v37\n"
  "v_mfma_f32_16x16x32_bf16 v[48:51], a[128:131], a[72:75], v[248:251]\n"
  "s_mov_b64 s[32:33], s[44:45]\n"
  "ds_read_b64_tr_b16 a[128:129], v217 offset:12368\n"
  "v_fma_f32 v38, v38, s73, v225\n"
  "v_mfma_f32_16x16x32_bf16 v[48:51], a[132:135], a[76:79], v[48:51]\n"
  "ds_read_b64_tr_b16 a[130:131], v218 offset:12368\n"
  "v_fma_f32 v39, v39, s73, v225\n"
  "v_mfma_f32_16x16x32_bf16 v[48:51], a[120:123], a[80:83], v[48:51]\n"
  "ds_read_b64_tr_b16 a[132:133], v217 offset:13392\n"
  "v_cvt_pk_bf16_f32 v42, v36, v37\n"
  "v_mfma_f32_16x16x32_bf16 v[48:51], a[124:127], a[84:87], v[48:51]\n"
  "s_add_u32 m0, s72, 0\n"
  "ds_read_b64_tr_b16 a[134:135], v218 offset:13392\n"
  "v_fma_f32 v20, v20, s73, v224\n"
  "v_mfma_f32_16x16x32_bf16 v[48:51], a[136:139], a[88:91], v[48:51]\n"
  "s_min_u32 s30, s38, 0xc00\n"
  "v_exp_f32 v38, v38\n"
  "v_mfma_f32_16x16x32_bf16 v[48:51], a[140:143], a[92:95], v[48:51]\n"
  "s_waitcnt lgkmcnt(4)\n"
  "v_exp_f32 v39, v39\n"
  "v_mfma_f32_16x16x32_bf16 a[236:239], a[160:163], a[144:147], a[236:239]\n"
  "v_exp_f32 v20, v20\n"
  "v_mfma_f32_16x16x32_bf16 a[236:239], a[156:159], a[148:151], a[236:239]\n"
  "buffer_load_dwordx4 v221, s[48:51], 0 offen offset:0 nt lds\n"
  "v_cvt_pk_bf16_f32 v43, v38, v39\n"
  "v_mfma_f32_16x16x32_bf16 v[184:187], v[60:63], a[144:147], v[184:187]\n"
  "v_fma_f32 v21, v21, s73, v224\n"
  "v_fma_f32 v22, v22, s73, v224\n"
  "v_mfma_f32_16x16x32_bf16 v[184:187], v[56:59], a[148:151], v[184:187]\n"
  "s_add_u32 s44, s44, s31\n"
  "v_exp_f32 v21, v21\n"
  "v_exp_f32_e64 v22, v22\n"
  "v_fma_f32 v23, v23, s73, v224\n"
  "v_exp_f32 v23, v23\n"
  "v_mfma_f32_16x16x32_bf16 v[152:155], v[44:47], a[144:147], v[152:155]\n"
  "s_addc_u32 s45, s45, 0\n"
  "ds_read_b64_tr_b16 a[120:121], v219 offset:12352\n"
  "v_cvt_pk_bf16_f32 v26, v20, v21\n"
  "v_mfma_f32_16x16x32_bf16 v[152:155], v[40:43], a[148:151], v[152:155]\n"
  "ds_read_b64_tr_b16 a[122:123], v220 offset:12352\n"
  "v_cvt_pk_bf16_f32 v27, v22, v23\n"
  "v_fma_f32 v4, v4, s73, v223\n"
  "v_exp_f32 v4, v4\n"
  "v_fma_f32 v5, v5, s73, v223\n"
  "v_exp_f32 v5, v5\n"
  "v_fma_f32 v6, v6, s73, v223\n"
  "v_exp_f32 v6, v6\n"
  "v_fma_f32 v7, v7, s73, v223\n"
  "v_exp_f32 v7, v7\n"
  "v_mfma_f32_16x16x32_bf16 v[120:123], v[28:31], a[144:147], v[120:123]\n"
  "ds_read_b64_tr_b16 a[124:125], v219 offset:13376\n"
  "v_cvt_pk_bf16_f32 v10, v4, v5\n"
  "v_mfma_f32_16x16x32_bf16 v[120:123], v[24:27], a[148:151], v[120:123]\n"
  "ds_read_b64_tr_b16 a[126:127], v220 offset:13376\n"
  "v_cvt_pk_bf16_f32 v11, v6, v7\n"
  "v_mfma_f32_16x16x32_bf16 v[88:91], v[12:15], a[144:147], v[88:91]\n"
  "s_add_u32 m0, s72, -1984\n"
  "v_add_f32 v252, v252, v4\n"
  "v_add_f32 v252, v252, v5\n"
  "v_mfma_f32_16x16x32_bf16 v[88:91], v[8:11], a[148:151], v[88:91]\n"
  "s_waitcnt lgkmcnt(4)\n"
  "buffer_load_dwordx4 v221, s[48:51], 0 offen offset:3072 nt lds\n"
  "v_add_f32 v252, v252, v6\n"
  "v_mfma_f32_16x16x32_bf16 v[92:95], v[12:15], a[128:131], v[92:95]\n"
  "v_add_f32 v252, v252, v7\n"
  "v_add_f32 v253, v253, v20\n"
  "v_mfma_f32_16x16x32_bf16 v[92:95], v[8:11], a[132:135], v[92:95]\n"
  "v_add_f32 v253, v253, v21\n"
  "v_add_f32 v253, v253, v22\n"
  "v_mfma_f32_16x16x32_bf16 v[124:127], v[28:31], a[128:131], v[124:127]\n"
  "ds_read_b128 a[136:139], v235 offset:12352\n"
  "v_add_f32 v253, v253, v23\n"
  "v_mfma_f32_16x16x32_bf16 v[124:127], v[24:27], a[132:135], v[124:127]\n"
  "s_add_u32 m0, s72, 2112\n"
  "ds_read_b128 a[140:143], v235 offset:12416\n"
  "v_add_f32 v254, v254, v36\n"
  "v_mfma_f32_16x16x32_bf16 v[156:159], v[44:47], a[128:131], v[156:159]\n"
  "ds_read_b128 a[144:147], v235 offset:13376\n"
  "v_add_f32 v254, v254, v37\n"
  "v_mfma_f32_16x16x32_bf16 v[156:159], v[40:43], a[132:135], v[156:159]\n"
  "ds_read_b128 a[148:151], v235 offset:13440\n"
  "v_add_f32 v254, v254, v38\n"
  "v_mfma_f32_16x16x32_bf16 v[188:191], v[60:63], a[128:131], v[188:191]\n"
  "v_add_f32 v254, v254, v39\n"
  "v_add_f32 v255, v255, v53\n"
  "v_mfma_f32_16x16x32_bf16 v[188:191], v[56:59], a[132:135], v[188:191]\n"
  "buffer_load_dwordx4 v221, s[48:51], 0 offen offset:128 nt lds\n"
  "v_add_f32 v255, v255, v54\n"
  "v_mfma_f32_16x16x32_bf16 a[240:243], a[160:163], a[128:131], a[240:243]\n"
  "ds_read_b128 a[128:131], v235 offset:14400\n"
  "v_add_f32 v255, v255, v55\n"
  "v_mfma_f32_16x16x32_bf16 a[240:243], a[156:159], a[132:135], a[240:243]\n"
  "s_waitcnt lgkmcnt(5)\n"
  "ds_read_b128 a[132:135], v235 offset:14464\n"
  "v_fma_f32 v64, v64, s73, v227\n"
  "v_mfma_f32_16x16x32_bf16 v[192:195], v[60:63], a[120:123], v[192:195]\n"
  "v_exp_f32 v64, v64\n"
  "v_mfma_f32_16x16x32_bf16 v[192:195], v[56:59], a[124:127], v[192:195]\n"
  "s_add_u32 m0, s72, 128\n"
  "v_fma_f32 v65, v65, s73, v227\n"
  "v_mfma_f32_16x16x32_bf16 v[160:163], v[44:47], a[120:123], v[160:163]\n"
  "v_exp_f32 v65, v65\n"
  "v_mfma_f32_16x16x32_bf16 v[160:163], v[40:43], a[124:127], v[160:163]\n"
  "v_fma_f32 v66, v66, s73, v227\n"
  "v_fma_f32 v67, v67, s73, v227\n"
  "v_mfma_f32_16x16x32_bf16 v[128:131], v[28:31], a[120:123], v[128:131]\n"
  "v_exp_f32 v66, v66\n"
  "v_mfma_f32_16x16x32_bf16 v[128:131], v[24:27], a[124:127], v[128:131]\n"
  "buffer_load_dwordx4 v221, s[48:51], 0 offen offset:3200 nt lds\n"
  "v_cvt_pk_bf16_f32 v64, v64, v65\n"
  "v_mfma_f32_16x16x32_bf16 v[96:99], v[12:15], a[120:123], v[96:99]\n"
  "v_exp_f32 v67, v67\n"
  "v_mfma_f32_16x16x32_bf16 v[96:99], v[8:11], a[124:127], v[96:99]\n"
  "s_waitcnt lgkmcnt(4)\n"
  "v_cvt_pk_bf16_f32 v65, v66, v67\n"
  "v_fma_f32 v0, v0, s73, v223\n"
  "v_mfma_f32_16x16x32_bf16 v[4:7], a[136:139], a[0:3], v[248:251]\n"
  "v_exp_f32 v0, v0\n"
  "v_mfma_f32_16x16x32_bf16 v[4:7], a[140:143], a[4:7], v[4:7]\n"
  "s_waitcnt lgkmcnt(2)\n"
  "v_fma_f32 v1, v1, s73, v223\n"
  "v_fma_f32 v2, v2, s73, v223\n"
  "v_mfma_f32_16x16x32_bf16 v[4:7], a[144:147], a[8:11], v[4:7]\n"
  "v_exp_f32 v1, v1\n"
  "v_mfma_f32_16x16x32_bf16 v[4:7], a[148:151], a[12:15], v[4:7]\n"
  "s_waitcnt lgkmcnt(0)\n"
  "v_exp_f32 v2, v2\n"
  "v_mfma_f32_16x16x32_bf16 v[4:7], a[128:131], a[16:19], v[4:7]\n"
  "v_fma_f32 v3, v3, s73, v223\n"
  "v_add_f32 v252, v252, v0\n"
  "v_mfma_f32_16x16x32_bf16 v[4:7], a[132:135], a[20:23], v[4:7]\n"
  "ds_write_b64 v231, v[64:65] offset:2048\n"
  "v_mfma_f32_16x16x32_bf16 v[20:23], a[132:135], a[44:47], v[248:251]\n"
  "v_exp_f32 v3, v3\n"
  "v_mfma_f32_16x16x32_bf16 v[20:23], a[128:131], a[40:43], v[20:23]\n"
  "v_add_f32 v252, v252, v1\n"
  "v_add_f32 v252, v252, v2\n"
  "v_mfma_f32_16x16x32_bf16 v[20:23], a[148:151], a[36:39], v[20:23]\n"
  "ds_read_b64_tr_b16 a[120:121], v219 offset:12368\n"
  "v_add_f32 v252, v252, v3\n"
  "v_mfma_f32_16x16x32_bf16 v[20:23], a[144:147], a[32:35], v[20:23]\n"
  "ds_read_b64_tr_b16 a[122:123], v220 offset:12368\n"
  "v_fma_f32 v16, v16, s73, v224\n"
  "v_mfma_f32_16x16x32_bf16 v[20:23], a[140:143], a[28:31], v[20:23]\n"
  "ds_read_b64_tr_b16 a[124:125], v219 offset:13392\n"
  "v_fma_f32 v17, v17, s73, v224\n"
  "v_mfma_f32_16x16x32_bf16 v[20:23], a[136:139], a[24:27], v[20:23]\n"
  "ds_read_b64_tr_b16 a[126:127], v220 offset:13392\n"
  "v_fma_f32 v18, v18, s73, v224\n"
  "v_mfma_f32_16x16x32_bf16 v[36:39], a[136:139], a[48:51], v[248:251]\n"
  "v_exp_f32 v16, v16\n"
  "v_mfma_f32_16x16x32_bf16 v[36:39], a[140:143], a[52:55], v[36:39]\n"
  "v_exp_f32 v17, v17\n"
  "v_mfma_f32_16x16x32_bf16 v[36:39], a[144:147], a[56:59], v[36:39]\n"
  "v_exp_f32 v18, v18\n"
  "v_mfma_f32_16x16x32_bf16 v[36:39], a[148:151], a[60:63], v[36:39]\n"
  "v_fma_f32 v19, v19, s73, v224\n"
  "v_add_f32 v253, v253, v16\n"
  "v_mfma_f32_16x16x32_bf16 v[36:39], a[128:131], a[64:67], v[36:39]\n"
  "v_exp_f32 v19, v19\n"
  "v_mfma_f32_16x16x32_bf16 v[36:39], a[132:135], a[68:71], v[36:39]\n"
  "v_add_f32 v253, v253, v17\n"
  "v_add_f32 v253, v253, v18\n"
  "v_mfma_f32_16x16x32_bf16 v[52:55], a[132:135], a[92:95], v[248:251]\n"
  "ds_read_b64_tr_b16 a[132:133], v217 offset:14400\n"
  "v_add_f32 v253, v253, v19\n"
  "v_mfma_f32_16x16x32_bf16 v[52:55], a[128:131], a[88:91], v[52:55]\n"
  "ds_read_b64_tr_b16 a[134:135], v218 offset:14400\n"
  "v_fma_f32 v32, v32, s73, v225\n"
  "v_mfma_f32_16x16x32_bf16 v[52:55], a[148:151], a[84:87], v[52:55]\n"
  "ds_read_b64_tr_b16 a[128:129], v217 offset:14416\n"
  "v_fma_f32 v33, v33, s73, v225\n"
  "v_mfma_f32_16x16x32_bf16 v[52:55], a[144:147], a[80:83], v[52:55]\n"
  "ds_read_b64_tr_b16 a[130:131], v218 offset:14416\n"
  "v_fma_f32 v34, v34, s73, v225\n"
  "v_mfma_f32_16x16x32_bf16 v[52:55], a[140:143], a[76:79], v[52:55]\n"
  "v_exp_f32 v32, v32\n"
  "v_mfma_f32_16x16x32_bf16 v[52:55], a[136:139], a[72:75], v[52:55]\n"
  "s_waitcnt lgkmcnt(4)\n"
  "v_exp_f32 v33, v33\n"
  "v_mfma_f32_16x16x32_bf16 v[100:103], v[12:15], a[120:123], v[100:103]\n"
  "v_exp_f32 v34, v34\n"
  "v_mfma_f32_16x16x32_bf16 v[100:103], v[8:11], a[124:127], v[100:103]\n"
  "v_cvt_pk_bf16_f32 v12, v0, v1\n"
  "v_cvt_pk_bf16_f32 v13, v2, v3\n"
  "v_mfma_f32_16x16x32_bf16 v[132:135], v[28:31], a[120:123], v[132:135]\n"
  "ds_read_b64_tr_b16 a[140:141], v219 offset:14400\n"
  "v_cvt_pk_bf16_f32 v28, v16, v17\n"
  "v_mfma_f32_16x16x32_bf16 v[132:135], v[24:27], a[124:127], v[132:135]\n"
  "ds_read_b64_tr_b16 a[142:143], v220 offset:14400\n"
  "v_cvt_pk_bf16_f32 v29, v18, v19\n"
  "v_mfma_f32_16x16x32_bf16 v[164:167], v[44:47], a[120:123], v[164:167]\n"
  "ds_read_b64_tr_b16 a[136:137], v219 offset:14416\n"
  "v_fma_f32 v35, v35, s73, v225\n"
  "v_mfma_f32_16x16x32_bf16 v[164:167], v[40:43], a[124:127], v[164:167]\n"
  "ds_read_b64_tr_b16 a[138:139], v220 offset:14416\n"
  "v_add_f32 v254, v254, v32\n"
  "v_mfma_f32_16x16x32_bf16 v[196:199], v[60:63], a[120:123], v[196:199]\n"
  "v_exp_f32 v35, v35\n"
  "v_mfma_f32_16x16x32_bf16 v[196:199], v[56:59], a[124:127], v[196:199]\n"
  "s_waitcnt lgkmcnt(4)\n"
  "v_add_f32 v254, v254, v33\n"
  "v_add_f32 v254, v254, v34\n"
  "v_mfma_f32_16x16x32_bf16 a[244:247], a[156:159], a[132:135], a[244:247]\n"
  "v_add_f32 v254, v254, v35\n"
  "v_cvt_pk_bf16_f32 v44, v32, v33\n"
  "v_mfma_f32_16x16x32_bf16 a[248:251], a[156:159], a[128:131], a[248:251]\n"
  "v_cvt_pk_bf16_f32 v45, v34, v35\n"
  "v_fma_f32 v48, v48, s73, v226\n"
  "v_mfma_f32_16x16x32_bf16 v[200:203], v[56:59], a[132:135], v[200:203]\n"
  "v_exp_f32 v48, v48\n"
  "v_mfma_f32_16x16x32_bf16 v[204:207], v[56:59], a[128:131], v[204:207]\n"
  "v_fma_f32 v49, v49, s73, v226\n"
  "v_fma_f32 v50, v50, s73, v226\n"
  "v_mfma_f32_16x16x32_bf16 v[168:171], v[40:43], a[132:135], v[168:171]\n"
  "v_exp_f32 v49, v49\n"
  "v_mfma_f32_16x16x32_bf16 v[172:175], v[40:43], a[128:131], v[172:175]\n"
  "s_waitcnt vmcnt(10) lgkmcnt(0)\n"
  "s_barrier\n"
  "ds_read_b128 a[120:123], v216 offset:24704\n"
  "v_mfma_f32_16x16x32_bf16 v[136:139], v[24:27], a[132:135], v[136:139]\n"
  "ds_read_b64 a[160:161], v231 offset:2048\n"
  "v_fma_f32 v51, v51, s73, v226\n"
  "v_mfma_f32_16x16x32_bf16 v[140:143], v[24:27], a[128:131], v[140:143]\n"
  "ds_read_b64 a[162:163], v232 offset:2048\n"
  "v_add_f32 v255, v255, v48\n"
  "v_mfma_f32_16x16x32_bf16 v[104:107], v[8:11], a[132:135], v[104:107]\n"
  "ds_read_b128 a[124:127], v216 offset:24768\n"
  "v_add_f32 v255, v255, v49\n"
  "v_mfma_f32_16x16x32_bf16 v[108:111], v[8:11], a[128:131], v[108:111]\n"
  "ds_read_b128 a[132:135], v216 offset:25728\n"
  "v_cvt_pk_bf16_f32 v60, v48, v49\n"
  "v_mfma_f32_16x16x32_bf16 v[112:115], v[8:11], a[140:143], v[112:115]\n"
  "ds_read_b128 a[128:131], v216 offset:25792\n"
  "v_fma_f32 v52, v52, s73, v226\n"
  "v_mfma_f32_16x16x32_bf16 v[116:119], v[8:11], a[136:139], v[116:119]\n"
  "s_mov_b32 m0, s67\n"
  "ds_read_b128 a[148:151], v216 offset:26752\n"
  "v_fma_f32 v53, v53, s73, v226\n"
  "v_mfma_f32_16x16x32_bf16 v[144:147], v[24:27], a[140:143], v[144:147]\n"
  "ds_read_b128 a[144:147], v216 offset:26816\n"
  "v_fma_f32 v54, v54, s73, v226\n"
  "v_mfma_f32_16x16x32_bf16 v[148:151], v[24:27], a[136:139], v[148:151]\n"
  "v_exp_f32 v50, v50\n"
  "v_mfma_f32_16x16x32_bf16 v[176:179], v[40:43], a[140:143], v[176:179]\n"
  "v_exp_f32 v51, v51\n"
  "v_mfma_f32_16x16x32_bf16 v[180:183], v[40:43], a[136:139], v[180:183]\n"
  "buffer_load_dwordx4 v221, s[40:43], 0 offen offset:0 lds\n"
  "v_mfma_f32_16x16x32_bf16 v[208:211], v[56:59], a[140:143], v[208:211]\n"
  "s_addk_i32 m0, 896\n"
  "v_exp_f32 v52, v52\n"
  "v_mfma_f32_16x16x32_bf16 v[212:215], v[56:59], a[136:139], v[212:215]\n"
  "s_waitcnt lgkmcnt(4)\n"
  "v_exp_f32 v53, v53\n"
  "v_mfma_f32_16x16x32_bf16 v[64:67], a[120:123], a[96:99], v[248:251]\n"
  "v_exp_f32 v54, v54\n"
  "v_mfma_f32_16x16x32_bf16 v[64:67], a[124:127], a[100:103], v[64:67]\n"
  "s_waitcnt lgkmcnt(2)\n"
  "buffer_load_dwordx4 v221, s[40:43], 0 offen offset:128 lds\n"
  "v_add_f32 v255, v255, v50\n"
  "v_mfma_f32_16x16x32_bf16 v[64:67], a[132:135], a[104:107], v[64:67]\n"
  "s_addk_i32 m0, 896\n"
  "v_add_f32 v255, v255, v51\n"
  "v_cvt_pk_bf16_f32 v61, v50, v51\n"
  "v_mfma_f32_16x16x32_bf16 v[64:67], a[128:131], a[108:111], v[64:67]\n"
  "s_waitcnt lgkmcnt(0)\n"
  "v_fma_f32 v55, v55, s73, v226\n"
  "v_cvt_pk_bf16_f32 v62, v52, v53\n"
  "v_mfma_f32_16x16x32_bf16 v[64:67], a[148:151], a[112:115], v[64:67]\n"
  "v_exp_f32 v55, v55\n"
  "v_mfma_f32_16x16x32_bf16 v[64:67], a[144:147], a[116:119], v[64:67]\n"
  "buffer_load_dwordx4 v221, s[40:43], 0 offen offset:256 lds\n"
  "v_cvt_pk_bf16_f32 v63, v54, v55\n"
  "v_mfma_f32_16x16x32_bf16 v[0:3], a[144:147], a[20:23], v[248:251]\n"
  "v_fma_f32 v36, v36, s73, v225\n"
  "v_fma_f32 v37, v37, s73, v225\n"
  "v_mfma_f32_16x16x32_bf16 v[0:3], a[148:151], a[16:19], v[0:3]\n"
  "s_add_u32 s40, s40, s31\n"
  "v_exp_f32 v36, v36\n"
  "v_mfma_f32_16x16x32_bf16 v[0:3], a[128:131], a[12:15], v[0:3]\n"
  "s_addc_u32 s41, s41, 0\n"
  "v_exp_f32 v37, v37\n"
  "v_mfma_f32_16x16x32_bf16 v[0:3], a[132:135], a[8:11], v[0:3]\n"
  "s_mov_b32 m0, s59\n"
  "v_fma_f32 v38, v38, s73, v225\n"
  "v_fma_f32 v39, v39, s73, v225\n"
  "v_mfma_f32_16x16x32_bf16 v[0:3], a[124:127], a[4:7], v[0:3]\n"
  "v_exp_f32 v38, v38\n"
  "v_mfma_f32_16x16x32_bf16 v[0:3], a[120:123], a[0:3], v[0:3]\n"
  "v_exp_f32 v39, v39\n"
  "v_mfma_f32_16x16x32_bf16 v[16:19], a[120:123], a[24:27], v[248:251]\n"
  "v_cvt_pk_bf16_f32 v46, v36, v37\n"
  "v_cvt_pk_bf16_f32 v47, v38, v39\n"
  "v_mfma_f32_16x16x32_bf16 v[16:19], a[124:127], a[28:31], v[16:19]\n"
  "buffer_load_dwordx4 v221, s[32:35], 0 offen offset:0 lds\n"
  "v_fma_f32 v20, v20, s73, v224\n"
  "v_mfma_f32_16x16x32_bf16 v[16:19], a[132:135], a[32:35], v[16:19]\n"
  "s_addk_i32 m0, 1024\n"
  "ds_read_b64_tr_b16 a[140:141], v217 offset:24704\n"
  "v_fma_f32 v21, v21, s73, v224\n"
  "v_mfma_f32_16x16x32_bf16 v[16:19], a[128:131], a[36:39], v[16:19]\n"
  "s_mov_b64 s[32:33], s[44:45]\n"
  "ds_read_b64_tr_b16 a[142:143], v218 offset:24704\n"
  "v_fma_f32 v22, v22, s73, v224\n"
  "v_mfma_f32_16x16x32_bf16 v[16:19], a[148:151], a[40:43], v[16:19]\n"
  "ds_read_b64_tr_b16 a[136:137], v217 offset:25728\n"
  "v_fma_f32 v23, v23, s73, v224\n"
  "v_mfma_f32_16x16x32_bf16 v[16:19], a[144:147], a[44:47], v[16:19]\n"
  "buffer_load_dwordx4 v221, s[44:47], 0 offen offset:0 lds\n"
  "v_fma_f32 v4, v4, s73, v223\n"
  "v_mfma_f32_16x16x32_bf16 v[32:35], a[144:147], a[68:71], v[248:251]\n"
  "s_addk_i32 m0, 768\n"
  "ds_read_b64_tr_b16 a[138:139], v218 offset:25728\n"
  "v_fma_f32 v5, v5, s73, v223\n"
  "v_mfma_f32_16x16x32_bf16 v[32:35], a[148:151], a[64:67], v[32:35]\n"
  "v_exp_f32 v20, v20\n"
  "v_mfma_f32_16x16x32_bf16 v[32:35], a[128:131], a[60:63], v[32:35]\n"
  "v_exp_f32 v21, v21\n"
  "v_mfma_f32_16x16x32_bf16 v[32:35], a[132:135], a[56:59], v[32:35]\n"
  "buffer_load_dwordx4 v221, s[44:47], 0 offen offset:256 lds\n"
  "v_cvt_pk_bf16_f32 v30, v20, v21\n"
  "v_mfma_f32_16x16x32_bf16 v[32:35], a[124:127], a[52:55], v[32:35]\n"
  "s_add_u32 s44, s44, s31\n"
  "v_exp_f32 v22, v22\n"
  "v_mfma_f32_16x16x32_bf16 v[32:35], a[120:123], a[48:51], v[32:35]\n"
  "s_addc_u32 s45, s45, 0\n"
  "v_exp_f32 v23, v23\n"
  "v_mfma_f32_16x16x32_bf16 v[48:51], a[120:123], a[72:75], v[248:251]\n"
  "v_exp_f32 v4, v4\n"
  "v_mfma_f32_16x16x32_bf16 v[48:51], a[124:127], a[76:79], v[48:51]\n"
  "v_exp_f32 v5, v5\n"
  "v_mfma_f32_16x16x32_bf16 v[48:51], a[132:135], a[80:83], v[48:51]\n"
  "ds_read_b64_tr_b16 a[120:121], v217 offset:24720\n"
  "v_cvt_pk_bf16_f32 v31, v22, v23\n"
  "v_mfma_f32_16x16x32_bf16 v[48:51], a[128:131], a[84:87], v[48:51]\n"
  "ds_read_b64_tr_b16 a[122:123], v218 offset:24720\n"
  "v_fma_f32 v6, v6, s73, v223\n"
  "v_mfma_f32_16x16x32_bf16 v[48:51], a[148:151], a[88:91], v[48:51]\n"
  "ds_read_b64_tr_b16 a[124:125], v217 offset:25744\n"
  "v_fma_f32 v7, v7, s73, v223\n"
  "v_mfma_f32_16x16x32_bf16 v[48:51], a[144:147], a[92:95], v[48:51]\n"
  "s_waitcnt lgkmcnt(3)\n"
  "ds_read_b64_tr_b16 a[126:127], v218 offset:25744\n"
  "v_cvt_pk_bf16_f32 v14, v4, v5\n"
  "v_mfma_f32_16x16x32_bf16 a[228:231], a[156:159], a[140:143], a[228:231]\n"
  "v_exp_f32 v6, v6\n"
  "v_mfma_f32_16x16x32_bf16 a[228:231], a[160:163], a[136:139], a[228:231]\n"
  "v_exp_f32 v7, v7\n"
  "v_mfma_f32_16x16x32_bf16 a[212:215], v[56:59], a[140:143], a[212:215]\n"
  "v_cvt_pk_bf16_f32 v15, v6, v7\n"
  "v_add_f32 v252, v252, v4\n"
  "v_mfma_f32_16x16x32_bf16 a[212:215], v[60:63], a[136:139], a[212:215]\n"
  "v_add_f32 v252, v252, v5\n"
  "v_add_f32 v252, v252, v6\n"
  "v_mfma_f32_16x16x32_bf16 a[196:199], v[40:43], a[140:143], a[196:199]\n"
  "v_add_f32 v252, v252, v7\n"
  "v_add_f32 v253, v253, v20\n"
  "v_mfma_f32_16x16x32_bf16 a[196:199], v[44:47], a[136:139], a[196:199]\n"
  "v_add_f32 v253, v253, v21\n"
  "v_add_f32 v253, v253, v22\n"
  "v_mfma_f32_16x16x32_bf16 a[180:183], v[24:27], a[140:143], a[180:183]\n"
  "ds_read_b64_tr_b16 a[132:133], v219 offset:24704\n"
  "v_add_f32 v253, v253, v23\n"
  "v_mfma_f32_16x16x32_bf16 a[180:183], v[28:31], a[136:139], a[180:183]\n"
  "ds_read_b64_tr_b16 a[134:135], v220 offset:24704\n"
  "v_add_f32 v254, v254, v36\n"
  "v_mfma_f32_16x16x32_bf16 a[164:167], v[8:11], a[140:143], a[164:167]\n"
  "ds_read_b64_tr_b16 a[128:129], v219 offset:25728\n"
  "v_add_f32 v254, v254, v37\n"
  "v_mfma_f32_16x16x32_bf16 a[164:167], v[12:15], a[136:139], a[164:167]\n"
  "ds_read_b64_tr_b16 a[130:131], v220 offset:25728\n"
  "v_add_f32 v254, v254, v38\n"
  "v_mfma_f32_16x16x32_bf16 v[84:87], a[156:159], a[152:155], v[84:87]\n"
  "v_add_f32 v254, v254, v39\n"
  "v_add_f32 v255, v255, v52\n"
  "v_mfma_f32_16x16x32_bf16 v[84:87], a[160:163], a[152:155], v[84:87]\n"
  "s_waitcnt lgkmcnt(4)\n"
  "v_add_f32 v255, v255, v53\n"
  "v_add_f32 v255, v255, v54\n"
  "v_mfma_f32_16x16x32_bf16 a[168:171], v[8:11], a[120:123], a[168:171]\n"
  "ds_read_b64_tr_b16 v[246:247], v230 offset:5632\n"
  "v_add_f32 v255, v255, v55\n"
  "v_mfma_f32_16x16x32_bf16 a[168:171], v[12:15], a[124:127], a[168:171]\n"
  "v_fma_f32 v64, v64, s73, v227\n"
  "v_fma_f32 v65, v65, s73, v227\n"
  "v_mfma_f32_16x16x32_bf16 a[184:187], v[24:27], a[120:123], a[184:187]\n"
  "ds_read_b128 a[140:143], v235 offset:24704\n"
  "v_fma_f32 v66, v66, s73, v227\n"
  "v_mfma_f32_16x16x32_bf16 a[184:187], v[28:31], a[124:127], a[184:187]\n"
  "ds_read_b128 a[136:139], v235 offset:24768\n"
  "v_fma_f32 v67, v67, s73, v227\n"
  "v_mfma_f32_16x16x32_bf16 a[200:203], v[40:43], a[120:123], a[200:203]\n"
  "ds_read_b128 a[148:151], v235 offset:25728\n"
  "v_fma_f32 v0, v0, s73, v223\n"
  "v_mfma_f32_16x16x32_bf16 a[200:203], v[44:47], a[124:127], a[200:203]\n"
  "ds_read_b128 a[144:147], v235 offset:25792\n"
  "v_fma_f32 v1, v1, s73, v223\n"
  "v_mfma_f32_16x16x32_bf16 a[216:219], v[56:59], a[120:123], a[216:219]\n"
  "v_exp_f32 v64, v64\n"
  "v_mfma_f32_16x16x32_bf16 a[216:219], v[60:63], a[124:127], a[216:219]\n"
  "v_exp_f32 v65, v65\n"
  "v_mfma_f32_16x16x32_bf16 a[232:235], a[156:159], a[120:123], a[232:235]\n"
  "ds_read_b128 a[120:123], v235 offset:26752\n"
  "v_cvt_pk_bf16_f32 v64, v64, v65\n"
  "v_mfma_f32_16x16x32_bf16 a[232:235], a[160:163], a[124:127], a[232:235]\n"
  "s_waitcnt lgkmcnt(6)\n"
  "ds_read_b128 a[124:127], v235 offset:26816\n"
  "v_fma_f32 v2, v2, s73, v223\n"
  "v_mfma_f32_16x16x32_bf16 a[220:223], v[56:59], a[132:135], a[220:223]\n"
  "v_exp_f32 v66, v66\n"
  "v_mfma_f32_16x16x32_bf16 a[220:223], v[60:63], a[128:131], a[220:223]\n"
  "v_exp_f32 v67, v67\n"
  "v_mfma_f32_16x16x32_bf16 a[204:207], v[40:43], a[132:135], a[204:207]\n"
  "v_exp_f32 v0, v0\n"
  "v_mfma_f32_16x16x32_bf16 a[204:207], v[44:47], a[128:131], a[204:207]\n"
  "buffer_store_dwordx4 a[252:255], v228, s[52:55], s82 offen offset:0 nt\n"
  "v_cvt_pk_bf16_f32 v65, v66, v67\n"
  "v_exp_f32 v1, v1\n"
  "v_mfma_f32_16x16x32_bf16 a[188:191], v[24:27], a[132:135], a[188:191]\n"
  "ds_read_b64_tr_b16 a[252:253], v230 offset:6720\n"
  "ds_read_b64_tr_b16 a[254:255], v230 offset:6752\n"
  "v_mfma_f32_16x16x32_bf16 a[188:191], v[28:31], a[128:131], a[188:191]\n"
  "ds_write_b64 v231, v[64:65] offset:0\n"
  "v_mfma_f32_16x16x32_bf16 a[172:175], v[8:11], a[132:135], a[172:175]\n"
  "v_exp_f32 v2, v2\n"
  "v_mfma_f32_16x16x32_bf16 a[172:175], v[12:15], a[128:131], a[172:175]\n"
  "s_waitcnt lgkmcnt(7)\n"
  "v_fma_f32 v3, v3, s73, v223\n"
  "v_add_f32 v252, v252, v0\n"
  "v_mfma_f32_16x16x32_bf16 v[4:7], a[140:143], a[0:3], v[248:251]\n"
  "v_exp_f32 v3, v3\n"
  "v_mfma_f32_16x16x32_bf16 v[4:7], a[136:139], a[4:7], v[4:7]\n"
  "s_waitcnt lgkmcnt(5)\n"
  "v_add_f32 v252, v252, v1\n"
  "v_add_f32 v252, v252, v2\n"
  "v_mfma_f32_16x16x32_bf16 v[4:7], a[148:151], a[8:11], v[4:7]\n"
  "v_add_f32 v252, v252, v3\n"
  "v_fma_f32 v16, v16, s73, v224\n"
  "v_mfma_f32_16x16x32_bf16 v[4:7], a[144:147], a[12:15], v[4:7]\n"
  "s_waitcnt lgkmcnt(3)\n"
  "v_exp_f32_e64 v16, v16\n"
  "v_mfma_f32_16x16x32_bf16 v[4:7], a[120:123], a[16:19], v[4:7]\n"
  "v_fma_f32 v17, v17, s73, v224\n"
  "v_fma_f32 v18, v18, s73, v224\n"
  "v_mfma_f32_16x16x32_bf16 v[4:7], a[124:127], a[20:23], v[4:7]\n"
  "buffer_store_dwordx4 v[244:247], v228, s[52:55], s82 offen offset:3072 nt\n"
  "v_exp_f32 v17, v17\n"
  "v_fma_f32 v19, v19, s73, v224\n"
  "v_mfma_f32_16x16x32_bf16 v[20:23], a[124:127], a[44:47], v[248:251]\n"
  "v_exp_f32 v18, v18\n"
  "v_mfma_f32_16x16x32_bf16 v[20:23], a[120:123], a[40:43], v[20:23]\n"
  "v_exp_f32 v19, v19\n"
  "v_mfma_f32_16x16x32_bf16 v[20:23], a[144:147], a[36:39], v[20:23]\n"
  "ds_read_b64_tr_b16 a[132:133], v219 offset:24720\n"
  "v_add_f32 v253, v253, v16\n"
  "v_mfma_f32_16x16x32_bf16 v[20:23], a[148:151], a[32:35], v[20:23]\n"
  "ds_read_b64_tr_b16 a[134:135], v220 offset:24720\n"
  "v_add_f32 v253, v253, v17\n"
  "v_mfma_f32_16x16x32_bf16 v[20:23], a[136:139], a[28:31], v[20:23]\n"
  "ds_read_b64_tr_b16 a[128:129], v219 offset:25744\n"
  "v_add_f32_e64 v253, v253, v18\n"
  "v_mfma_f32_16x16x32_bf16 v[20:23], a[140:143], a[24:27], v[20:23]\n"
  "s_waitcnt lgkmcnt(4)\n"
  "buffer_store_dwordx4 a[252:255], v228, s[52:55], s83 offen offset:0 nt\n"
  "ds_read_b64_tr_b16 a[130:131], v220 offset:25744\n"
  "v_add_f32 v253, v253, v19\n"
  "v_fma_f32 v32, v32, s73, v225\n"
  "v_mfma_f32_16x16x32_bf16 v[36:39], a[140:143], a[48:51], v[248:251]\n"
  "v_exp_f32 v32, v32\n"
  "v_mfma_f32_16x16x32_bf16 v[36:39], a[136:139], a[52:55], v[36:39]\n"
  "v_fma_f32 v33, v33, s73, v225\n"
  "v_fma_f32 v34, v34, s73, v225\n"
  "v_mfma_f32_16x16x32_bf16 v[36:39], a[148:151], a[56:59], v[36:39]\n"
  "v_exp_f32 v33, v33\n"
  "v_mfma_f32_16x16x32_bf16 v[36:39], a[144:147], a[60:63], v[36:39]\n"
  "v_exp_f32 v34, v34\n"
  "v_mfma_f32_16x16x32_bf16 v[36:39], a[120:123], a[64:67], v[36:39]\n"
  "v_fma_f32 v35, v35, s73, v225\n"
  "v_add_f32 v254, v254, v32\n"
  "v_mfma_f32_16x16x32_bf16 v[36:39], a[124:127], a[68:71], v[36:39]\n"
  "v_exp_f32 v35, v35\n"
  "v_mfma_f32_16x16x32_bf16 v[52:55], a[124:127], a[92:95], v[248:251]\n"
  "ds_read_b64_tr_b16 a[124:125], v217 offset:26752\n"
  "v_add_f32 v254, v254, v33\n"
  "v_mfma_f32_16x16x32_bf16 v[52:55], a[120:123], a[88:91], v[52:55]\n"
  "ds_read_b64_tr_b16 a[126:127], v218 offset:26752\n"
  "v_add_f32 v254, v254, v34\n"
  "v_mfma_f32_16x16x32_bf16 v[52:55], a[144:147], a[84:87], v[52:55]\n"
  "ds_read_b64_tr_b16 a[120:121], v217 offset:26768\n"
  "v_add_f32 v254, v254, v35\n"
  "v_mfma_f32_16x16x32_bf16 v[52:55], a[148:151], a[80:83], v[52:55]\n"
  "ds_read_b64_tr_b16 a[122:123], v218 offset:26768\n"
  "v_fma_f32 v48, v48, s73, v226\n"
  "v_mfma_f32_16x16x32_bf16 v[52:55], a[136:139], a[76:79], v[52:55]\n"
  "v_exp_f32 v48, v48\n"
  "v_mfma_f32_16x16x32_bf16 v[52:55], a[140:143], a[72:75], v[52:55]\n"
  "s_waitcnt lgkmcnt(4)\n"
  "v_fma_f32 v49, v49, s73, v226\n"
  "v_fma_f32 v50, v50, s73, v226\n"
  "v_mfma_f32_16x16x32_bf16 a[176:179], v[8:11], a[132:135], a[176:179]\n"
  "v_exp_f32 v49, v49\n"
  "v_mfma_f32_16x16x32_bf16 a[176:179], v[12:15], a[128:131], a[176:179]\n"
  "v_exp_f32 v50, v50\n"
  "v_mfma_f32_16x16x32_bf16 a[192:195], v[24:27], a[132:135], a[192:195]\n"
  "ds_read_b64_tr_b16 a[136:137], v219 offset:26752\n"
  "v_cvt_pk_bf16_f32 v8, v0, v1\n"
  "v_mfma_f32_16x16x32_bf16 a[192:195], v[28:31], a[128:131], a[192:195]\n"
  "ds_read_b64_tr_b16 a[138:139], v220 offset:26752\n"
  "v_cvt_pk_bf16_f32 v9, v2, v3\n"
  "v_mfma_f32_16x16x32_bf16 a[208:211], v[40:43], a[132:135], a[208:211]\n"
  "ds_read_b64_tr_b16 a[140:141], v219 offset:26768\n"
  "v_cvt_pk_bf16_f32 v24, v16, v17\n"
  "v_mfma_f32_16x16x32_bf16 a[208:211], v[44:47], a[128:131], a[208:211]\n"
  "ds_read_b64_tr_b16 a[142:143], v220 offset:26768\n"
  "v_cvt_pk_bf16_f32 v25, v18, v19\n"
  "v_mfma_f32_16x16x32_bf16 a[224:227], v[56:59], a[132:135], a[224:227]\n"
  "v_cvt_pk_bf16_f32 v40, v32, v33\n"
  "v_cvt_pk_bf16_f32 v41, v34, v35\n"
  "v_mfma_f32_16x16x32_bf16 a[224:227], v[60:63], a[128:131], a[224:227]\n"
  "s_waitcnt lgkmcnt(4)\n"
  "v_fma_f32 v51, v51, s73, v226\n"
  "v_add_f32 v255, v255, v48\n"
  "v_mfma_f32_16x16x32_bf16 a[244:247], a[160:163], a[124:127], a[244:247]\n"
  "v_exp_f32 v51, v51\n"
  "v_mfma_f32_16x16x32_bf16 a[248:251], a[160:163], a[120:123], a[248:251]\n"
  "v_add_f32 v255, v255, v49\n"
  "v_add_f32 v255, v255, v50\n"
  "v_mfma_f32_16x16x32_bf16 v[200:203], v[60:63], a[124:127], v[200:203]\n"
  "v_add_f32 v255, v255, v51\n"
  "v_cvt_pk_bf16_f32 v56, v48, v49\n"
  "v_mfma_f32_16x16x32_bf16 v[204:207], v[60:63], a[120:123], v[204:207]\n"
  "v_cvt_pk_bf16_f32 v57, v50, v51\n"
  "v_fma_f32 v52, v52, s73, v226\n"
  "v_mfma_f32_16x16x32_bf16 v[168:171], v[44:47], a[124:127], v[168:171]\n"
  "v_exp_f32 v52, v52\n"
  "v_mfma_f32_16x16x32_bf16 v[172:175], v[44:47], a[120:123], v[172:175]\n"
  "s_waitcnt vmcnt(13) lgkmcnt(0)\n"
  "s_barrier\n"
  "ds_read_b128 a[132:135], v216 offset:0\n"
  "v_mfma_f32_16x16x32_bf16 v[136:139], v[28:31], a[124:127], v[136:139]\n"
  "ds_read_b64 a[156:157], v231 offset:0\n"
  "v_fma_f32 v53, v53, s73, v226\n"
  "v_mfma_f32_16x16x32_bf16 v[140:143], v[28:31], a[120:123], v[140:143]\n"
  "ds_read_b64 a[158:159], v232 offset:0\n"
  "v_fma_f32 v54, v54, s73, v226\n"
  "v_mfma_f32_16x16x32_bf16 v[104:107], v[12:15], a[124:127], v[104:107]\n"
  "ds_read_b128 a[128:131], v216 offset:64\n"
  "v_fma_f32 v55, v55, s73, v226\n"
  "v_mfma_f32_16x16x32_bf16 v[108:111], v[12:15], a[120:123], v[108:111]\n"
  "ds_read_b128 a[124:127], v216 offset:1024\n"
  "v_fma_f32 v36, v36, s73, v225\n"
  "v_mfma_f32_16x16x32_bf16 v[112:115], v[12:15], a[136:139], v[112:115]\n"
  "ds_read_b128 a[120:123], v216 offset:1088\n"
  "v_fma_f32 v37, v37, s73, v225\n"
  "v_mfma_f32_16x16x32_bf16 v[116:119], v[12:15], a[140:143], v[116:119]\n"
  "s_mov_b32 m0, s68\n"
  "ds_read_b128 a[144:147], v216 offset:2048\n"
  "v_fma_f32 v38, v38, s73, v225\n"
  "v_mfma_f32_16x16x32_bf16 v[144:147], v[28:31], a[136:139], v[144:147]\n"
  "ds_read_b128 a[148:151], v216 offset:2112\n"
  "v_fma_f32 v39, v39, s73, v225\n"
  "v_mfma_f32_16x16x32_bf16 v[148:151], v[28:31], a[140:143], v[148:151]\n"
  "v_exp_f32 v53, v53\n"
  "v_mfma_f32_16x16x32_bf16 v[176:179], v[44:47], a[136:139], v[176:179]\n"
  "v_exp_f32 v54, v54\n"
  "v_mfma_f32_16x16x32_bf16 v[180:183], v[44:47], a[140:143], v[180:183]\n"
  "buffer_load_dwordx4 v221, s[40:43], 0 offen offset:0 lds\n"
  "v_mfma_f32_16x16x32_bf16 v[208:211], v[60:63], a[136:139], v[208:211]\n"
  "s_addk_i32 m0, 896\n"
  "v_exp_f32 v55, v55\n"
  "v_mfma_f32_16x16x32_bf16 v[212:215], v[60:63], a[140:143], v[212:215]\n"
  "s_waitcnt lgkmcnt(4)\n"
  "v_exp_f32 v36, v36\n"
  "v_mfma_f32_16x16x32_bf16 v[64:67], a[132:135], a[96:99], v[248:251]\n"
  "v_exp_f32 v37, v37\n"
  "v_mfma_f32_16x16x32_bf16 v[64:67], a[128:131], a[100:103], v[64:67]\n"
  "s_waitcnt lgkmcnt(2)\n"
  "buffer_load_dwordx4 v221, s[40:43], 0 offen offset:128 lds\n"
  "v_cvt_pk_bf16_f32 v58, v52, v53\n"
  "v_mfma_f32_16x16x32_bf16 v[64:67], a[124:127], a[104:107], v[64:67]\n"
  "s_addk_i32 m0, 896\n"
  "v_exp_f32 v38, v38\n"
  "v_mfma_f32_16x16x32_bf16 v[64:67], a[120:123], a[108:111], v[64:67]\n"
  "s_waitcnt lgkmcnt(0)\n"
  "v_exp_f32 v39, v39\n"
  "v_mfma_f32_16x16x32_bf16 v[64:67], a[144:147], a[112:115], v[64:67]\n"
  "v_cvt_pk_bf16_f32 v59, v54, v55\n"
  "v_cvt_pk_bf16_f32 v42, v36, v37\n"
  "v_mfma_f32_16x16x32_bf16 v[64:67], a[148:151], a[116:119], v[64:67]\n"
  "buffer_load_dwordx4 v221, s[40:43], 0 offen offset:256 lds\n"
  "v_cvt_pk_bf16_f32 v43, v38, v39\n"
  "v_mfma_f32_16x16x32_bf16 v[0:3], a[148:151], a[20:23], v[248:251]\n"
  "v_fma_f32 v20, v20, s73, v224\n"
  "v_fma_f32 v21, v21, s73, v224\n"
  "v_mfma_f32_16x16x32_bf16 v[0:3], a[144:147], a[16:19], v[0:3]\n"
  "s_add_u32 s40, s40, s31\n"
  "v_exp_f32 v20, v20\n"
  "v_mfma_f32_16x16x32_bf16 v[0:3], a[120:123], a[12:15], v[0:3]\n"
  "s_addc_u32 s41, s41, 0\n"
  "v_exp_f32 v21, v21\n"
  "v_mfma_f32_16x16x32_bf16 v[0:3], a[124:127], a[8:11], v[0:3]\n"
  "s_add_u32 m0, s60, -128\n"
  "v_fma_f32 v22, v22, s73, v224\n"
  "v_add_f32 v253, v253, v20\n"
  "v_mfma_f32_16x16x32_bf16 v[0:3], a[128:131], a[4:7], v[0:3]\n"
  "v_exp_f32 v22, v22\n"
  "v_mfma_f32_16x16x32_bf16 v[0:3], a[132:135], a[0:3], v[0:3]\n"
  "v_fma_f32 v23, v23, s73, v224\n"
  "v_cvt_pk_bf16_f32 v26, v20, v21\n"
  "v_mfma_f32_16x16x32_bf16 v[16:19], a[132:135], a[24:27], v[248:251]\n"
  "v_exp_f32 v23, v23\n"
  "v_mfma_f32_16x16x32_bf16 v[16:19], a[128:131], a[28:31], v[16:19]\n"
  "buffer_load_dwordx4 v221, s[32:35], 0 offen offset:128 lds\n"
  "v_cvt_pk_bf16_f32 v27, v22, v23\n"
  "v_mfma_f32_16x16x32_bf16 v[16:19], a[124:127], a[32:35], v[16:19]\n"
  "s_addk_i32 m0, 1024\n"
  "ds_read_b64_tr_b16 a[136:137], v217 offset:0\n"
  "v_fma_f32 v4, v4, s73, v223\n"
  "v_mfma_f32_16x16x32_bf16 v[16:19], a[120:123], a[36:39], v[16:19]\n"
  "s_mov_b64 s[32:33], s[44:45]\n"
  "ds_read_b64_tr_b16 a[138:139], v218 offset:0\n"
  "v_fma_f32 v5, v5, s73, v223\n"
  "v_mfma_f32_16x16x32_bf16 v[16:19], a[144:147], a[40:43], v[16:19]\n"
  "ds_read_b64_tr_b16 a[140:141], v217 offset:1024\n"
  "v_fma_f32 v6, v6, s73, v223\n"
  "v_mfma_f32_16x16x32_bf16 v[16:19], a[148:151], a[44:47], v[16:19]\n"
  "buffer_load_dwordx4 v221, s[44:47], 0 offen offset:128 lds\n"
  "v_fma_f32 v7, v7, s73, v223\n"
  "v_mfma_f32_16x16x32_bf16 v[32:35], a[148:151], a[68:71], v[248:251]\n"
  "s_addk_i32 m0, 896\n"
  "ds_read_b64_tr_b16 a[142:143], v218 offset:1024\n"
  "v_add_f32 v253, v253, v21\n"
  "v_mfma_f32_16x16x32_bf16 v[32:35], a[144:147], a[64:67], v[32:35]\n"
  "v_exp_f32 v4, v4\n"
  "v_mfma_f32_16x16x32_bf16 v[32:35], a[120:123], a[60:63], v[32:35]\n"
  "v_exp_f32 v5, v5\n"
  "v_mfma_f32_16x16x32_bf16 v[32:35], a[124:127], a[56:59], v[32:35]\n"
  "buffer_load_dwordx4 v221, s[44:47], 0 offen offset:256 lds\n"
  "v_cvt_pk_bf16_f32 v10, v4, v5\n"
  "v_mfma_f32_16x16x32_bf16 v[32:35], a[128:131], a[52:55], v[32:35]\n"
  "s_add_u32 s44, s44, s31\n"
  "v_exp_f32 v6, v6\n"
  "v_mfma_f32_16x16x32_bf16 v[32:35], a[132:135], a[48:51], v[32:35]\n"
  "s_addc_u32 s45, s45, 0\n"
  "v_exp_f32 v7, v7\n"
  "v_mfma_f32_16x16x32_bf16 v[48:51], a[132:135], a[72:75], v[248:251]\n"
  "ds_read_b64_tr_b16 a[132:133], v217 offset:16\n"
  "v_cvt_pk_bf16_f32 v11, v6, v7\n"
  "v_mfma_f32_16x16x32_bf16 v[48:51], a[128:131], a[76:79], v[48:51]\n"
  "ds_read_b64_tr_b16 a[134:135], v218 offset:16\n"
  "v_add_f32 v252, v252, v4\n"
  "v_mfma_f32_16x16x32_bf16 v[48:51], a[124:127], a[80:83], v[48:51]\n"
  "ds_read_b64_tr_b16 a[128:129], v217 offset:1040\n"
  "v_add_f32 v252, v252, v5\n"
  "v_mfma_f32_16x16x32_bf16 v[48:51], a[120:123], a[84:87], v[48:51]\n"
  "s_add_u32 m0, s72, 4224\n"
  "ds_read_b64_tr_b16 a[130:131], v218 offset:1040\n"
  "v_add_f32 v252, v252, v6\n"
  "v_mfma_f32_16x16x32_bf16 v[48:51], a[144:147], a[88:91], v[48:51]\n"
  "v_add_f32 v252, v252, v7\n"
  "v_add_f32 v253, v253, v22\n"
  "v_mfma_f32_16x16x32_bf16 v[48:51], a[148:151], a[92:95], v[48:51]\n"
  "s_waitcnt lgkmcnt(4)\n"
  "v_add_f32 v253, v253, v23\n"
  "v_add_f32 v254, v254, v36\n"
  "v_mfma_f32_16x16x32_bf16 a[236:239], a[160:163], a[136:139], a[236:239]\n"
  "v_add_f32 v254, v254, v37\n"
  "v_add_f32 v254, v254, v38\n"
  "v_mfma_f32_16x16x32_bf16 a[236:239], a[156:159], a[140:143], a[236:239]\n"
  "buffer_load_dwordx4 v221, s[48:51], 0 offen offset:256 nt lds\n"
  "v_add_f32 v254, v254, v39\n"
  "v_mfma_f32_16x16x32_bf16 v[184:187], v[60:63], a[136:139], v[184:187]\n"
  "v_add_f32 v255, v255, v52\n"
  "v_add_f32 v255, v255, v53\n"
  "v_mfma_f32_16x16x32_bf16 v[184:187], v[56:59], a[140:143], v[184:187]\n"
  "v_add_f32 v255, v255, v54\n"
  "v_add_f32 v255, v255, v55\n"
  "v_mfma_f32_16x16x32_bf16 v[152:155], v[44:47], a[136:139], v[152:155]\n"
  "ds_read_b64_tr_b16 a[124:125], v219 offset:0\n"
  "v_fma_f32 v64, v64, s73, v227\n"
  "v_mfma_f32_16x16x32_bf16 v[152:155], v[40:43], a[140:143], v[152:155]\n"
  "s_add_u32 m0, s72, 2240\n"
  "ds_read_b64_tr_b16 a[126:127], v220 offset:0\n"
  "v_mfma_f32_16x16x32_bf16 v[120:123], v[28:31], a[136:139], v[120:123]\n"
  "ds_read_b64_tr_b16 a[120:121], v219 offset:1024\n"
  "v_fma_f32 v65, v65, s73, v227\n"
  "v_mfma_f32_16x16x32_bf16 v[120:123], v[24:27], a[140:143], v[120:123]\n"
  "ds_read_b64_tr_b16 a[122:123], v220 offset:1024\n"
  "v_fma_f32 v66, v66, s73, v227\n"
  "v_mfma_f32_16x16x32_bf16 v[88:91], v[12:15], a[136:139], v[88:91]\n"
  "v_exp_f32 v64, v64\n"
  "v_mfma_f32_16x16x32_bf16 v[88:91], v[8:11], a[140:143], v[88:91]\n"
  "s_waitcnt lgkmcnt(4)\n"
  "buffer_load_dwordx4 v221, s[48:51], 0 offen offset:3328 nt lds\n"
  "v_fma_f32 v67, v67, s73, v227\n"
  "v_mfma_f32_16x16x32_bf16 v[92:95], v[12:15], a[132:135], v[92:95]\n"
  "v_exp_f32 v65, v65\n"
  "v_mfma_f32_16x16x32_bf16 v[92:95], v[8:11], a[128:131], v[92:95]\n"
  "v_exp_f32 v66, v66\n"
  "v_mfma_f32_16x16x32_bf16 v[124:127], v[28:31], a[132:135], v[124:127]\n"
  "ds_read_b128 a[136:139], v235 offset:0\n"
  "v_cvt_pk_bf16_f32 v64, v64, v65\n"
  "v_mfma_f32_16x16x32_bf16 v[124:127], v[24:27], a[128:131], v[124:127]\n"
  "ds_read_b128 a[140:143], v235 offset:64\n"
  "v_fma_f32 v0, v0, s73, v223\n"
  "v_mfma_f32_16x16x32_bf16 v[156:159], v[44:47], a[132:135], v[156:159]\n"
  "ds_read_b128 a[144:147], v235 offset:1024\n"
  "v_fma_f32 v1, v1, s73, v223\n"
  "v_mfma_f32_16x16x32_bf16 v[156:159], v[40:43], a[128:131], v[156:159]\n"
  "ds_read_b128 a[148:151], v235 offset:1088\n"
  "v_fma_f32 v2, v2, s73, v223\n"
  "v_mfma_f32_16x16x32_bf16 v[188:191], v[60:63], a[132:135], v[188:191]\n"
  "v_exp_f32 v67, v67\n"
  "v_mfma_f32_16x16x32_bf16 v[188:191], v[56:59], a[128:131], v[188:191]\n"
  "v_exp_f32 v0, v0\n"
  "v_mfma_f32_16x16x32_bf16 a[240:243], a[160:163], a[132:135], a[240:243]\n"
  "ds_read_b128 a[132:135], v235 offset:2048\n"
  "v_cvt_pk_bf16_f32 v65, v66, v67\n"
  "v_mfma_f32_16x16x32_bf16 a[240:243], a[156:159], a[128:131], a[240:243]\n"
  "s_waitcnt lgkmcnt(5)\n"
  "ds_read_b128 a[128:131], v235 offset:2112\n"
  "v_fma_f32 v3, v3, s73, v223\n"
  "v_mfma_f32_16x16x32_bf16 v[192:195], v[60:63], a[124:127], v[192:195]\n"
  "v_exp_f32_e64 v1, v1\n"
  "v_mfma_f32_16x16x32_bf16 v[192:195], v[56:59], a[120:123], v[192:195]\n"
  "ds_write_b64 v231, v[64:65] offset:2048\n"
  "v_mfma_f32_16x16x32_bf16 v[160:163], v[44:47], a[124:127], v[160:163]\n"
  "v_exp_f32 v2, v2\n"
  "v_mfma_f32_16x16x32_bf16 v[160:163], v[40:43], a[120:123], v[160:163]\n"
  "v_exp_f32 v3, v3\n"
  "v_mfma_f32_16x16x32_bf16 v[128:131], v[28:31], a[124:127], v[128:131]\n"
  "v_add_f32 v252, v252, v0\n"
  "v_add_f32 v252, v252, v1\n"
  "v_mfma_f32_16x16x32_bf16 v[128:131], v[24:27], a[120:123], v[128:131]\n"
  "v_add_f32 v252, v252, v2\n"
  "v_add_f32 v252, v252, v3\n"
  "v_mfma_f32_16x16x32_bf16 v[96:99], v[12:15], a[124:127], v[96:99]\n"
  "v_fma_f32 v16, v16, s73, v224\n"
  "v_fma_f32 v17, v17, s73, v224\n"
  "v_mfma_f32_16x16x32_bf16 v[96:99], v[8:11], a[120:123], v[96:99]\n"
  "s_waitcnt lgkmcnt(5)\n"
  "v_exp_f32 v16, v16\n"
  "v_mfma_f32_16x16x32_bf16 v[4:7], a[136:139], a[0:3], v[248:251]\n"
  "v_exp_f32 v17, v17\n"
  "v_mfma_f32_16x16x32_bf16 v[4:7], a[140:143], a[4:7], v[4:7]\n"
  "s_waitcnt lgkmcnt(3)\n"
  "v_fma_f32 v18, v18, s73, v224\n"
  "v_fma_f32 v19, v19, s73, v224\n"
  "v_mfma_f32_16x16x32_bf16 v[4:7], a[144:147], a[8:11], v[4:7]\n"
  "v_exp_f32 v18, v18\n"
  "v_mfma_f32_16x16x32_bf16 v[4:7], a[148:151], a[12:15], v[4:7]\n"
  "s_waitcnt lgkmcnt(1)\n"
  "v_exp_f32 v19, v19\n"
  "v_mfma_f32_16x16x32_bf16 v[4:7], a[132:135], a[16:19], v[4:7]\n"
  "v_add_f32 v253, v253, v16\n"
  "v_add_f32 v253, v253, v17\n"
  "v_mfma_f32_16x16x32_bf16 v[4:7], a[128:131], a[20:23], v[4:7]\n"
  "v_add_f32 v253, v253, v18\n"
  "v_add_f32 v253, v253, v19\n"
  "v_mfma_f32_16x16x32_bf16 v[20:23], a[128:131], a[44:47], v[248:251]\n"
  "v_fma_f32 v32, v32, s73, v225\n"
  "v_fma_f32 v33, v33, s73, v225\n"
  "v_mfma_f32_16x16x32_bf16 v[20:23], a[132:135], a[40:43], v[20:23]\n"
  "v_exp_f32 v32, v32\n"
  "v_mfma_f32_16x16x32_bf16 v[20:23], a[148:151], a[36:39], v[20:23]\n"
  "ds_read_b64_tr_b16 a[124:125], v219 offset:16\n"
  "v_fma_f32 v34, v34, s73, v225\n"
  "v_mfma_f32_16x16x32_bf16 v[20:23], a[144:147], a[32:35], v[20:23]\n"
  "ds_read_b64_tr_b16 a[126:127], v220 offset:16\n"
  "v_fma_f32 v35, v35, s73, v225\n"
  "v_mfma_f32_16x16x32_bf16 v[20:23], a[140:143], a[28:31], v[20:23]\n"
  "ds_read_b64_tr_b16 a[120:121], v219 offset:1040\n"
  "v_add_f32 v254, v254, v32\n"
  "v_mfma_f32_16x16x32_bf16 v[20:23], a[136:139], a[24:27], v[20:23]\n"
  "ds_read_b64_tr_b16 a[122:123], v220 offset:1040\n"
  "v_fma_f32 v48, v48, s73, v226\n"
  "v_mfma_f32_16x16x32_bf16 v[36:39], a[136:139], a[48:51], v[248:251]\n"
  "v_exp_f32 v33, v33\n"
  "v_mfma_f32_16x16x32_bf16 v[36:39], a[140:143], a[52:55], v[36:39]\n"
  "v_exp_f32 v34, v34\n"
  "v_mfma_f32_16x16x32_bf16 v[36:39], a[144:147], a[56:59], v[36:39]\n"
  "v_exp_f32 v35, v35\n"
  "v_mfma_f32_16x16x32_bf16 v[36:39], a[148:151], a[60:63], v[36:39]\n"
  "v_exp_f32 v48, v48\n"
  "v_mfma_f32_16x16x32_bf16 v[36:39], a[132:135], a[64:67], v[36:39]\n"
  "v_add_f32 v254, v254, v33\n"
  "v_add_f32 v254, v254, v34\n"
  "v_mfma_f32_16x16x32_bf16 v[36:39], a[128:131], a[68:71], v[36:39]\n"
  "v_add_f32 v254, v254, v35\n"
  "v_fma_f32 v49, v49, s73, v226\n"
  "v_mfma_f32_16x16x32_bf16 v[52:55], a[128:131], a[92:95], v[248:251]\n"
  "ds_read_b64_tr_b16 a[128:129], v217 offset:2048\n"
  "v_fma_f32 v50, v50, s73, v226\n"
  "v_mfma_f32_16x16x32_bf16 v[52:55], a[132:135], a[88:91], v[52:55]\n"
  "ds_read_b64_tr_b16 a[130:131], v218 offset:2048\n"
  "v_fma_f32 v51, v51, s73, v226\n"
  "v_mfma_f32_16x16x32_bf16 v[52:55], a[148:151], a[84:87], v[52:55]\n"
  "ds_read_b64_tr_b16 a[132:133], v217 offset:2064\n"
  "v_add_f32 v255, v255, v48\n"
  "v_mfma_f32_16x16x32_bf16 v[52:55], a[144:147], a[80:83], v[52:55]\n"
  "ds_read_b64_tr_b16 a[134:135], v218 offset:2064\n"
  "v_fma_f32 v36, v36, s73, v225\n"
  "v_mfma_f32_16x16x32_bf16 v[52:55], a[140:143], a[76:79], v[52:55]\n"
  "v_exp_f32 v49, v49\n"
  "v_mfma_f32_16x16x32_bf16 v[52:55], a[136:139], a[72:75], v[52:55]\n"
  "s_waitcnt lgkmcnt(4)\n"
  "v_exp_f32 v50, v50\n"
  "v_mfma_f32_16x16x32_bf16 v[100:103], v[12:15], a[124:127], v[100:103]\n"
  "v_exp_f32 v51, v51\n"
  "v_mfma_f32_16x16x32_bf16 v[100:103], v[8:11], a[120:123], v[100:103]\n"
  "v_cvt_pk_bf16_f32 v12, v0, v1\n"
  "v_cvt_pk_bf16_f32 v13, v2, v3\n"
  "v_mfma_f32_16x16x32_bf16 v[132:135], v[28:31], a[124:127], v[132:135]\n"
  "ds_read_b64_tr_b16 a[140:141], v219 offset:2048\n"
  "v_cvt_pk_bf16_f32 v28, v16, v17\n"
  "v_mfma_f32_16x16x32_bf16 v[132:135], v[24:27], a[120:123], v[132:135]\n"
  "ds_read_b64_tr_b16 a[142:143], v220 offset:2048\n"
  "v_cvt_pk_bf16_f32 v29, v18, v19\n"
  "v_mfma_f32_16x16x32_bf16 v[164:167], v[44:47], a[124:127], v[164:167]\n"
  "ds_read_b64_tr_b16 a[136:137], v219 offset:2064\n"
  "v_cvt_pk_bf16_f32 v44, v32, v33\n"
  "v_mfma_f32_16x16x32_bf16 v[164:167], v[40:43], a[120:123], v[164:167]\n"
  "ds_read_b64_tr_b16 a[138:139], v220 offset:2064\n"
  "v_cvt_pk_bf16_f32 v45, v34, v35\n"
  "v_mfma_f32_16x16x32_bf16 v[196:199], v[60:63], a[124:127], v[196:199]\n"
  "v_exp_f32 v36, v36\n"
  "v_mfma_f32_16x16x32_bf16 v[196:199], v[56:59], a[120:123], v[196:199]\n"
  "s_waitcnt lgkmcnt(4)\n"
  "v_add_f32 v255, v255, v49\n"
  "v_add_f32 v255, v255, v50\n"
  "v_mfma_f32_16x16x32_bf16 a[244:247], a[156:159], a[128:131], a[244:247]\n"
  "v_add_f32 v255, v255, v51\n"
  "v_cvt_pk_bf16_f32 v60, v48, v49\n"
  "v_mfma_f32_16x16x32_bf16 a[248:251], a[156:159], a[132:135], a[248:251]\n"
  "v_cvt_pk_bf16_f32 v61, v50, v51\n"
  "v_fma_f32 v52, v52, s73, v226\n"
  "v_mfma_f32_16x16x32_bf16 v[200:203], v[56:59], a[128:131], v[200:203]\n"
  "v_exp_f32 v52, v52\n"
  "v_mfma_f32_16x16x32_bf16 v[204:207], v[56:59], a[132:135], v[204:207]\n"
  "v_fma_f32 v53, v53, s73, v226\n"
  "v_fma_f32 v54, v54, s73, v226\n"
  "v_mfma_f32_16x16x32_bf16 v[168:171], v[40:43], a[128:131], v[168:171]\n"
  "v_exp_f32 v53, v53\n"
  "v_mfma_f32_16x16x32_bf16 v[172:175], v[40:43], a[132:135], v[172:175]\n"
  "s_waitcnt vmcnt(11) lgkmcnt(0)\n"
  "s_barrier\n"
  "ds_read_b128 a[124:127], v216 offset:12352\n"
  "v_mfma_f32_16x16x32_bf16 v[136:139], v[24:27], a[128:131], v[136:139]\n"
  "ds_read_b64 a[160:161], v231 offset:2048\n"
  "v_fma_f32 v55, v55, s73, v226\n"
  "v_mfma_f32_16x16x32_bf16 v[140:143], v[24:27], a[132:135], v[140:143]\n"
  "ds_read_b64 a[162:163], v232 offset:2048\n"
  "v_cvt_pk_bf16_f32 v62, v52, v53\n"
  "v_mfma_f32_16x16x32_bf16 v[104:107], v[8:11], a[128:131], v[104:107]\n"
  "ds_read_b128 a[120:123], v216 offset:12416\n"
  "v_fma_f32 v37, v37, s73, v225\n"
  "v_mfma_f32_16x16x32_bf16 v[108:111], v[8:11], a[132:135], v[108:111]\n"
  "ds_read_b128 a[128:131], v216 offset:13376\n"
  "v_fma_f32 v38, v38, s73, v225\n"
  "v_mfma_f32_16x16x32_bf16 v[112:115], v[8:11], a[140:143], v[112:115]\n"
  "ds_read_b128 a[132:135], v216 offset:13440\n"
  "v_fma_f32 v39, v39, s73, v225\n"
  "v_mfma_f32_16x16x32_bf16 v[116:119], v[8:11], a[136:139], v[116:119]\n"
  "s_mov_b32 m0, s66\n"
  "ds_read_b128 a[148:151], v216 offset:14400\n"
  "v_fma_f32 v20, v20, s73, v224\n"
  "v_mfma_f32_16x16x32_bf16 v[144:147], v[24:27], a[140:143], v[144:147]\n"
  "ds_read_b128 a[144:147], v216 offset:14464\n"
  "v_fma_f32 v21, v21, s73, v224\n"
  "v_mfma_f32_16x16x32_bf16 v[148:151], v[24:27], a[136:139], v[148:151]\n"
  "v_exp_f32 v54, v54\n"
  "v_mfma_f32_16x16x32_bf16 v[176:179], v[40:43], a[140:143], v[176:179]\n"
  "v_exp_f32 v55, v55\n"
  "v_mfma_f32_16x16x32_bf16 v[180:183], v[40:43], a[136:139], v[180:183]\n"
  "buffer_load_dwordx4 v221, s[40:43], 0 offen offset:0 lds\n"
  "v_mfma_f32_16x16x32_bf16 v[208:211], v[56:59], a[140:143], v[208:211]\n"
  "s_addk_i32 m0, 896\n"
  "v_exp_f32 v37, v37\n"
  "v_mfma_f32_16x16x32_bf16 v[212:215], v[56:59], a[136:139], v[212:215]\n"
  "s_waitcnt lgkmcnt(4)\n"
  "v_exp_f32 v38, v38\n"
  "v_mfma_f32_16x16x32_bf16 v[64:67], a[124:127], a[96:99], v[248:251]\n"
  "v_exp_f32 v39, v39\n"
  "v_mfma_f32_16x16x32_bf16 v[64:67], a[120:123], a[100:103], v[64:67]\n"
  "s_waitcnt lgkmcnt(2)\n"
  "buffer_load_dwordx4 v221, s[40:43], 0 offen offset:128 lds\n"
  "v_cvt_pk_bf16_f32 v63, v54, v55\n"
  "v_mfma_f32_16x16x32_bf16 v[64:67], a[128:131], a[104:107], v[64:67]\n"
  "s_addk_i32 m0, 896\n"
  "v_exp_f32 v20, v20\n"
  "v_mfma_f32_16x16x32_bf16 v[64:67], a[132:135], a[108:111], v[64:67]\n"
  "s_waitcnt lgkmcnt(0)\n"
  "v_exp_f32 v21, v21\n"
  "v_mfma_f32_16x16x32_bf16 v[64:67], a[148:151], a[112:115], v[64:67]\n"
  "v_cvt_pk_bf16_f32 v46, v36, v37\n"
  "v_cvt_pk_bf16_f32 v47, v38, v39\n"
  "v_mfma_f32_16x16x32_bf16 v[64:67], a[144:147], a[116:119], v[64:67]\n"
  "buffer_load_dwordx4 v221, s[40:43], 0 offen offset:256 lds\n"
  "v_fma_f32 v22, v22, s73, v224\n"
  "v_mfma_f32_16x16x32_bf16 v[0:3], a[144:147], a[20:23], v[248:251]\n"
  "s_add_u32 s40, s40, s31\n"
  "v_exp_f32 v22, v22\n"
  "v_mfma_f32_16x16x32_bf16 v[0:3], a[148:151], a[16:19], v[0:3]\n"
  "s_addc_u32 s41, s41, 0\n"
  "v_mbcnt_lo_u32_b32 v236, -1, 0\n"
  "v_mbcnt_hi_u32_b32 v236, -1, v236\n"
  "v_mfma_f32_16x16x32_bf16 v[0:3], a[132:135], a[12:15], v[0:3]\n"
  "s_sub_u32 s79, s96, s39\n"
  "v_mul_u32_u24 v236, 0x18c, v236\n"
  "v_fma_f32 v23, v23, s73, v224\n"
  "v_mfma_f32_16x16x32_bf16 v[0:3], a[128:131], a[8:11], v[0:3]\n"
  "s_mov_b32 m0, s58\n"
  "v_exp_f32 v23, v23\n"
  "v_mfma_f32_16x16x32_bf16 v[0:3], a[120:123], a[4:7], v[0:3]\n"
  "v_cvt_pk_bf16_f32 v30, v20, v21\n"
  "v_cvt_pk_bf16_f32 v31, v22, v23\n"
  "v_mfma_f32_16x16x32_bf16 v[0:3], a[124:127], a[0:3], v[0:3]\n"
  "v_fma_f32 v4, v4, s73, v223\n"
  "v_fma_f32 v5, v5, s73, v223\n"
  "v_mfma_f32_16x16x32_bf16 v[16:19], a[124:127], a[24:27], v[248:251]\n"
  "v_exp_f32 v4, v4\n"
  "v_mfma_f32_16x16x32_bf16 v[16:19], a[120:123], a[28:31], v[16:19]\n"
  "buffer_load_dwordx4 v221, s[32:35], 0 offen offset:0 lds\n"
  "v_fma_f32 v6, v6, s73, v223\n"
  "v_mfma_f32_16x16x32_bf16 v[16:19], a[128:131], a[32:35], v[16:19]\n"
  "s_addk_i32 m0, 1024\n"
  "ds_read_b64_tr_b16 a[140:141], v217 offset:12352\n"
  "v_fma_f32 v7, v7, s73, v223\n"
  "v_mfma_f32_16x16x32_bf16 v[16:19], a[132:135], a[36:39], v[16:19]\n"
  "s_mov_b64 s[32:33], s[44:45]\n"
  "ds_read_b64_tr_b16 a[142:143], v218 offset:12352\n"
  "v_add_f32 v252, v252, v4\n"
  "v_mfma_f32_16x16x32_bf16 v[16:19], a[148:151], a[40:43], v[16:19]\n"
  "ds_read_b64_tr_b16 a[136:137], v217 offset:13376\n"
  "v_add_f32 v253, v253, v20\n"
  "v_mfma_f32_16x16x32_bf16 v[16:19], a[144:147], a[44:47], v[16:19]\n"
  "buffer_load_dwordx4 v221, s[44:47], 0 offen offset:0 lds\n"
  "v_add_f32 v253, v253, v21\n"
  "v_mfma_f32_16x16x32_bf16 v[32:35], a[144:147], a[68:71], v[248:251]\n"
  "s_addk_i32 m0, 768\n"
  "ds_read_b64_tr_b16 a[138:139], v218 offset:13376\n"
  "v_add_f32 v253, v253, v22\n"
  "v_mfma_f32_16x16x32_bf16 v[32:35], a[148:151], a[64:67], v[32:35]\n"
  "v_exp_f32 v5, v5\n"
  "v_mfma_f32_16x16x32_bf16 v[32:35], a[132:135], a[60:63], v[32:35]\n"
  "v_exp_f32 v6, v6\n"
  "v_mfma_f32_16x16x32_bf16 v[32:35], a[128:131], a[56:59], v[32:35]\n"
  "buffer_load_dwordx4 v221, s[44:47], 0 offen offset:256 lds\n"
  "v_cvt_pk_bf16_f32 v14, v4, v5\n"
  "v_mfma_f32_16x16x32_bf16 v[32:35], a[120:123], a[52:55], v[32:35]\n"
  "s_add_u32 s44, s44, s31\n"
  "v_exp_f32 v7, v7\n"
  "v_mfma_f32_16x16x32_bf16 v[32:35], a[124:127], a[48:51], v[32:35]\n"
  "s_addc_u32 s45, s45, 0\n"
  "v_cvt_pk_bf16_f32 v15, v6, v7\n"
  "v_add_f32 v252, v252, v5\n"
  "v_mfma_f32_16x16x32_bf16 v[48:51], a[124:127], a[72:75], v[248:251]\n"
  "s_mov_b32 m0, s29\n"
  "v_add_f32 v252, v252, v6\n"
  "v_add_f32 v252, v252, v7\n"
  "v_mfma_f32_16x16x32_bf16 v[48:51], a[120:123], a[76:79], v[48:51]\n"
  "v_add_f32 v253, v253, v23\n"
  "v_add_f32 v254, v254, v36\n"
  "v_mfma_f32_16x16x32_bf16 v[48:51], a[128:131], a[80:83], v[48:51]\n"
  "ds_read_b64_tr_b16 a[124:125], v217 offset:12368\n"
  "v_add_f32 v254, v254, v37\n"
  "v_mfma_f32_16x16x32_bf16 v[48:51], a[132:135], a[84:87], v[48:51]\n"
  "buffer_load_dword v236, s[88:91], s79 offen lds\n"
  "v_add_f32 v254, v254, v38\n"
  "v_mfma_f32_16x16x32_bf16 v[48:51], a[148:151], a[88:91], v[48:51]\n"
  "ds_read_b64_tr_b16 a[126:127], v218 offset:12368\n"
  "v_add_f32 v254, v254, v39\n"
  "v_mfma_f32_16x16x32_bf16 v[48:51], a[144:147], a[92:95], v[48:51]\n"
  "s_waitcnt lgkmcnt(2)\n"
  "ds_read_b64_tr_b16 a[120:121], v217 offset:13392\n"
  "v_add_f32 v255, v255, v52\n"
  "v_mfma_f32_16x16x32_bf16 a[228:231], a[156:159], a[140:143], a[228:231]\n"
  "ds_read_b64_tr_b16 a[122:123], v218 offset:13392\n"
  "v_add_f32 v255, v255, v53\n"
  "v_mfma_f32_16x16x32_bf16 a[228:231], a[160:163], a[136:139], a[228:231]\n"
  "v_add_f32 v255, v255, v54\n"
  "v_add_f32 v255, v255, v55\n"
  "v_mfma_f32_16x16x32_bf16 a[212:215], v[56:59], a[140:143], a[212:215]\n"
  "ds_read_b64_tr_b16 v[244:245], v230 offset:7840\n"
  "ds_read_b64_tr_b16 v[246:247], v230 offset:7872\n"
  "v_mfma_f32_16x16x32_bf16 a[212:215], v[60:63], a[136:139], a[212:215]\n"
  "ds_read_b64_tr_b16 a[252:253], v230 offset:17920\n"
  "ds_read_b64_tr_b16 a[254:255], v230 offset:17952\n"
  "v_mfma_f32_16x16x32_bf16 a[196:199], v[40:43], a[140:143], a[196:199]\n"
  "v_fma_f32 v64, v64, s73, v227\n"
  "v_fma_f32 v65, v65, s73, v227\n"
  "v_mfma_f32_16x16x32_bf16 a[196:199], v[44:47], a[136:139], a[196:199]\n"
  "v_exp_f32 v64, v64\n"
  "v_mfma_f32_16x16x32_bf16 a[180:183], v[24:27], a[140:143], a[180:183]\n"
  "ds_read_b64_tr_b16 a[128:129], v219 offset:12352\n"
  "v_fma_f32 v66, v66, s73, v227\n"
  "v_mfma_f32_16x16x32_bf16 a[180:183], v[28:31], a[136:139], a[180:183]\n"
  "ds_read_b64_tr_b16 a[130:131], v220 offset:12352\n"
  "v_fma_f32 v67, v67, s73, v227\n"
  "v_mfma_f32_16x16x32_bf16 a[164:167], v[8:11], a[140:143], a[164:167]\n"
  "ds_read_b64_tr_b16 a[132:133], v219 offset:13376\n"
  "v_fma_f32 v0, v0, s73, v223\n"
  "v_mfma_f32_16x16x32_bf16 a[164:167], v[12:15], a[136:139], a[164:167]\n"
  "ds_read_b64_tr_b16 a[134:135], v220 offset:13376\n"
  "v_fma_f32 v1, v1, s73, v223\n"
  "v_mfma_f32_16x16x32_bf16 v[84:87], a[156:159], a[152:155], v[84:87]\n"
  "v_exp_f32 v65, v65\n"
  "v_mfma_f32_16x16x32_bf16 v[84:87], a[160:163], a[152:155], v[84:87]\n"
  "s_waitcnt lgkmcnt(8)\n"
  "v_exp_f32 v66, v66\n"
  "v_mfma_f32_16x16x32_bf16 a[168:171], v[8:11], a[124:127], a[168:171]\n"
  "v_exp_f32 v67, v67\n"
  "v_mfma_f32_16x16x32_bf16 a[168:171], v[12:15], a[120:123], a[168:171]\n"
  "v_exp_f32 v0, v0\n"
  "v_mfma_f32_16x16x32_bf16 a[184:187], v[24:27], a[124:127], a[184:187]\n"
  "ds_read_b128 a[140:143], v235 offset:12352\n"
  "v_cvt_pk_bf16_f32 v64, v64, v65\n"
  "v_mfma_f32_16x16x32_bf16 a[184:187], v[28:31], a[120:123], a[184:187]\n"
  "ds_read_b128 a[136:139], v235 offset:12416\n"
  "v_cvt_pk_bf16_f32 v65, v66, v67\n"
  "v_mfma_f32_16x16x32_bf16 a[200:203], v[40:43], a[124:127], a[200:203]\n"
  "ds_read_b128 a[148:151], v235 offset:13376\n"
  "v_fma_f32 v2, v2, s73, v223\n"
  "v_mfma_f32_16x16x32_bf16 a[200:203], v[44:47], a[120:123], a[200:203]\n"
  "ds_read_b128 a[144:147], v235 offset:13440\n"
  "v_fma_f32 v3, v3, s73, v223\n"
  "v_mfma_f32_16x16x32_bf16 a[216:219], v[56:59], a[124:127], a[216:219]\n"
  "v_exp_f32 v1, v1\n"
  "v_mfma_f32_16x16x32_bf16 a[216:219], v[60:63], a[120:123], a[216:219]\n"
  "v_exp_f32 v2, v2\n"
  "v_mfma_f32_16x16x32_bf16 a[232:235], a[156:159], a[124:127], a[232:235]\n"
  "ds_read_b128 a[124:127], v235 offset:14400\n"
  "v_add_f32 v252, v252, v0\n"
  "v_mfma_f32_16x16x32_bf16 a[232:235], a[160:163], a[120:123], a[232:235]\n"
  "s_waitcnt lgkmcnt(5)\n"
  "ds_read_b128 a[120:123], v235 offset:14464\n"
  "v_add_f32 v252, v252, v1\n"
  "v_mfma_f32_16x16x32_bf16 a[220:223], v[56:59], a[128:131], a[220:223]\n"
  "v_exp_f32 v3, v3\n"
  "v_mfma_f32_16x16x32_bf16 a[220:223], v[60:63], a[132:135], a[220:223]\n"
  "ds_write_b64 v231, v[64:65] offset:0\n"
  "v_mfma_f32_16x16x32_bf16 a[204:207], v[40:43], a[128:131], a[204:207]\n"
  "v_add_f32 v252, v252, v2\n"
  "v_add_f32 v252, v252, v3\n"
  "v_mfma_f32_16x16x32_bf16 a[204:207], v[44:47], a[132:135], a[204:207]\n"
  "buffer_store_dwordx4 v[244:247], v228, s[52:55], s83 offen offset:3072 nt\n"
  "v_fma_f32 v16, v16, s73, v224\n"
  "v_exp_f32 v16, v16\n"
  "v_mfma_f32_16x16x32_bf16 a[188:191], v[24:27], a[128:131], a[188:191]\n"
  "ds_read_b64_tr_b16 v[244:245], v230 offset:19040\n"
  "ds_read_b64_tr_b16 v[246:247], v230 offset:19072\n"
  "v_mfma_f32_16x16x32_bf16 a[188:191], v[28:31], a[132:135], a[188:191]\n"
  "v_fma_f32 v17, v17, s73, v224\n"
  "v_fma_f32 v18, v18, s73, v224\n"
  "v_mfma_f32_16x16x32_bf16 a[172:175], v[8:11], a[128:131], a[172:175]\n"
  "v_exp_f32 v17, v17\n"
  "v_mfma_f32_16x16x32_bf16 a[172:175], v[12:15], a[132:135], a[172:175]\n"
  "s_waitcnt lgkmcnt(7)\n"
  "v_exp_f32 v18, v18\n"
  "v_mfma_f32_16x16x32_bf16 v[4:7], a[140:143], a[0:3], v[248:251]\n"
  "v_fma_f32 v19, v19, s73, v224\n"
  "v_add_f32 v253, v253, v16\n"
  "v_mfma_f32_16x16x32_bf16 v[4:7], a[136:139], a[4:7], v[4:7]\n"
  "s_waitcnt lgkmcnt(5)\n"
  "v_exp_f32 v19, v19\n"
  "v_mfma_f32_16x16x32_bf16 v[4:7], a[148:151], a[8:11], v[4:7]\n"
  "v_add_f32 v253, v253, v17\n"
  "v_add_f32 v253, v253, v18\n"
  "v_mfma_f32_16x16x32_bf16 v[4:7], a[144:147], a[12:15], v[4:7]\n"
  "s_waitcnt lgkmcnt(3)\n"
  "v_add_f32 v253, v253, v19\n"
  "v_fma_f32 v32, v32, s73, v225\n"
  "v_mfma_f32_16x16x32_bf16 v[4:7], a[124:127], a[16:19], v[4:7]\n"
  "v_exp_f32 v32, v32\n"
  "v_mfma_f32_16x16x32_bf16 v[4:7], a[120:123], a[20:23], v[4:7]\n"
  "buffer_store_dwordx4 a[252:255], v228, s[52:55], s56 offen offset:0 nt\n"
  "v_fma_f32 v33, v33, s73, v225\n"
  "v_exp_f32 v33, v33\n"
  "v_mfma_f32_16x16x32_bf16 v[20:23], a[120:123], a[44:47], v[248:251]\n"
  "v_fma_f32 v34, v34, s73, v225\n"
  "v_fma_f32 v35, v35, s73, v225\n"
  "v_mfma_f32_16x16x32_bf16 v[20:23], a[124:127], a[40:43], v[20:23]\n"
  "v_exp_f32 v34, v34\n"
  "v_mfma_f32_16x16x32_bf16 v[20:23], a[144:147], a[36:39], v[20:23]\n"
  "ds_read_b64_tr_b16 a[128:129], v219 offset:12368\n"
  "v_add_f32 v254, v254, v32\n"
  "v_mfma_f32_16x16x32_bf16 v[20:23], a[148:151], a[32:35], v[20:23]\n"
  "ds_read_b64_tr_b16 a[130:131], v220 offset:12368\n"
  "v_add_f32 v254, v254, v33\n"
  "v_mfma_f32_16x16x32_bf16 v[20:23], a[136:139], a[28:31], v[20:23]\n"
  "ds_read_b64_tr_b16 a[132:133], v219 offset:13392\n"
  "v_add_f32 v254, v254, v34\n"
  "v_mfma_f32_16x16x32_bf16 v[20:23], a[140:143], a[24:27], v[20:23]\n"
  "s_waitcnt lgkmcnt(3)\n"
  "buffer_store_dwordx4 v[244:247], v228, s[52:55], s56 offen offset:128 nt\n"
  "ds_read_b64_tr_b16 a[134:135], v220 offset:13392\n"
  "v_exp_f32 v35, v35\n"
  "v_mfma_f32_16x16x32_bf16 v[36:39], a[140:143], a[48:51], v[248:251]\n"
  "v_add_f32 v254, v254, v35\n"
  "v_fma_f32 v48, v48, s73, v226\n"
  "v_mfma_f32_16x16x32_bf16 v[36:39], a[136:139], a[52:55], v[36:39]\n"
  "v_exp_f32 v48, v48\n"
  "v_mfma_f32_16x16x32_bf16 v[36:39], a[148:151], a[56:59], v[36:39]\n"
  "v_fma_f32 v49, v49, s73, v226\n"
  "v_fma_f32 v50, v50, s73, v226\n"
  "v_mfma_f32_16x16x32_bf16 v[36:39], a[144:147], a[60:63], v[36:39]\n"
  "v_exp_f32 v49, v49\n"
  "v_mfma_f32_16x16x32_bf16 v[36:39], a[124:127], a[64:67], v[36:39]\n"
  "v_exp_f32 v50, v50\n"
  "v_mfma_f32_16x16x32_bf16 v[36:39], a[120:123], a[68:71], v[36:39]\n"
  "v_fma_f32 v51, v51, s73, v226\n"
  "v_add_f32 v255, v255, v48\n"
  "v_mfma_f32_16x16x32_bf16 v[52:55], a[120:123], a[92:95], v[248:251]\n"
  "ds_read_b64_tr_b16 a[120:121], v217 offset:14400\n"
  "v_add_f32 v255, v255, v49\n"
  "v_mfma_f32_16x16x32_bf16 v[52:55], a[124:127], a[88:91], v[52:55]\n"
  "ds_read_b64_tr_b16 a[122:123], v218 offset:14400\n"
  "v_add_f32 v255, v255, v50\n"
  "v_mfma_f32_16x16x32_bf16 v[52:55], a[144:147], a[84:87], v[52:55]\n"
  "ds_read_b64_tr_b16 a[124:125], v217 offset:14416\n"
  "v_fma_f32 v36, v36, s73, v225\n"
  "v_mfma_f32_16x16x32_bf16 v[52:55], a[148:151], a[80:83], v[52:55]\n"
  "ds_read_b64_tr_b16 a[126:127], v218 offset:14416\n"
  "v_fma_f32 v37, v37, s73, v225\n"
  "v_mfma_f32_16x16x32_bf16 v[52:55], a[136:139], a[76:79], v[52:55]\n"
  "v_exp_f32 v51, v51\n"
  "v_mfma_f32_16x16x32_bf16 v[52:55], a[140:143], a[72:75], v[52:55]\n"
  "s_waitcnt lgkmcnt(4)\n"
  "v_exp_f32 v36, v36\n"
  "v_mfma_f32_16x16x32_bf16 a[176:179], v[8:11], a[128:131], a[176:179]\n"
  "v_cvt_pk_bf16_f32 v8, v0, v1\n"
  "v_cvt_pk_bf16_f32 v9, v2, v3\n"
  "v_mfma_f32_16x16x32_bf16 a[176:179], v[12:15], a[132:135], a[176:179]\n"
  "v_exp_f32 v37, v37\n"
  "v_mfma_f32_16x16x32_bf16 a[192:195], v[24:27], a[128:131], a[192:195]\n"
  "ds_read_b64_tr_b16 a[136:137], v219 offset:14400\n"
  "v_cvt_pk_bf16_f32 v24, v16, v17\n"
  "v_mfma_f32_16x16x32_bf16 a[192:195], v[28:31], a[132:135], a[192:195]\n"
  "ds_read_b64_tr_b16 a[138:139], v220 offset:14400\n"
  "v_cvt_pk_bf16_f32 v25, v18, v19\n"
  "v_mfma_f32_16x16x32_bf16 a[208:211], v[40:43], a[128:131], a[208:211]\n"
  "ds_read_b64_tr_b16 a[140:141], v219 offset:14416\n"
  "v_cvt_pk_bf16_f32 v40, v32, v33\n"
  "v_mfma_f32_16x16x32_bf16 a[208:211], v[44:47], a[132:135], a[208:211]\n"
  "ds_read_b64_tr_b16 a[142:143], v220 offset:14416\n"
  "v_cvt_pk_bf16_f32 v41, v34, v35\n"
  "v_mfma_f32_16x16x32_bf16 a[224:227], v[56:59], a[128:131], a[224:227]\n"
  "v_add_f32 v255, v255, v51\n"
  "v_cvt_pk_bf16_f32 v56, v48, v49\n"
  "v_mfma_f32_16x16x32_bf16 a[224:227], v[60:63], a[132:135], a[224:227]\n"
  "s_waitcnt lgkmcnt(4)\n"
  "v_cvt_pk_bf16_f32 v57, v50, v51\n"
  "v_fma_f32 v52, v52, s73, v226\n"
  "v_mfma_f32_16x16x32_bf16 a[244:247], a[160:163], a[120:123], a[244:247]\n"
  "v_exp_f32 v52, v52\n"
  "v_mfma_f32_16x16x32_bf16 a[248:251], a[160:163], a[124:127], a[248:251]\n"
  "v_fma_f32 v53, v53, s73, v226\n"
  "v_fma_f32 v54, v54, s73, v226\n"
  "v_mfma_f32_16x16x32_bf16 v[200:203], v[60:63], a[120:123], v[200:203]\n"
  "v_exp_f32 v53, v53\n"
  "v_mfma_f32_16x16x32_bf16 v[204:207], v[60:63], a[124:127], v[204:207]\n"
  "v_exp_f32_e64 v54, v54\n"
  "v_mfma_f32_16x16x32_bf16 v[168:171], v[44:47], a[120:123], v[168:171]\n"
  "v_fma_f32 v55, v55, s73, v226\n"
  "v_cvt_pk_bf16_f32 v58, v52, v53\n"
  "v_mfma_f32_16x16x32_bf16 v[172:175], v[44:47], a[124:127], v[172:175]\n"
  "s_waitcnt vmcnt(12) lgkmcnt(0)\n"
  "s_barrier\n"
  "ds_read_b128 a[128:131], v216 offset:24704\n"
  "v_mfma_f32_16x16x32_bf16 v[136:139], v[28:31], a[120:123], v[136:139]\n"
  "ds_read_b64 a[156:157], v231 offset:0\n"
  "v_fma_f32 v38, v38, s73, v225\n"
  "v_mfma_f32_16x16x32_bf16 v[140:143], v[28:31], a[124:127], v[140:143]\n"
  "ds_read_b64 a[158:159], v232 offset:0\n"
  "v_fma_f32 v39, v39, s73, v225\n"
  "v_mfma_f32_16x16x32_bf16 v[104:107], v[12:15], a[120:123], v[104:107]\n"
  "ds_read_b128 a[132:135], v216 offset:24768\n"
  "v_cvt_pk_bf16_f32 v42, v36, v37\n"
  "v_mfma_f32_16x16x32_bf16 v[108:111], v[12:15], a[124:127], v[108:111]\n"
  "ds_read_b128 a[120:123], v216 offset:25728\n"
  "v_fma_f32 v20, v20, s73, v224\n"
  "v_mfma_f32_16x16x32_bf16 v[112:115], v[12:15], a[136:139], v[112:115]\n"
  "s_add_u32 s71, s92, s81\n"
  "ds_read_b128 a[124:127], v216 offset:25792\n"
  "v_fma_f32 v21, v21, s73, v224\n"
  "v_mfma_f32_16x16x32_bf16 v[116:119], v[12:15], a[140:143], v[116:119]\n"
  "s_mov_b32 m0, s67\n"
  "ds_read_b128 a[144:147], v216 offset:26752\n"
  "v_fma_f32 v22, v22, s73, v224\n"
  "v_mfma_f32_16x16x32_bf16 v[144:147], v[28:31], a[136:139], v[144:147]\n"
  "s_add_u32 s48, s84, s71\n"
  "ds_read_b128 a[148:151], v216 offset:26816\n"
  "v_fma_f32 v23, v23, s73, v224\n"
  "v_mfma_f32_16x16x32_bf16 v[148:151], v[28:31], a[140:143], v[148:151]\n"
  "s_addc_u32 s49, s85, 0\n"
  "v_exp_f32 v55, v55\n"
  "v_mfma_f32_16x16x32_bf16 v[176:179], v[44:47], a[136:139], v[176:179]\n"
  "s_sub_i32 s71, s38, s81\n"
  "v_exp_f32 v38, v38\n"
  "v_mfma_f32_16x16x32_bf16 v[180:183], v[44:47], a[140:143], v[180:183]\n"
  "buffer_load_dwordx4 v221, s[40:43], 0 offen offset:0 lds\n"
  "v_mfma_f32_16x16x32_bf16 v[208:211], v[60:63], a[136:139], v[208:211]\n"
  "s_addk_i32 m0, 896\n"
  "v_exp_f32 v39, v39\n"
  "v_mfma_f32_16x16x32_bf16 v[212:215], v[60:63], a[140:143], v[212:215]\n"
  "s_waitcnt lgkmcnt(4)\n"
  "v_exp_f32 v20, v20\n"
  "v_mfma_f32_16x16x32_bf16 v[64:67], a[128:131], a[96:99], v[248:251]\n"
  "s_max_i32 s50, s71, 0\n"
  "v_exp_f32 v21, v21\n"
  "v_mfma_f32_16x16x32_bf16 v[64:67], a[132:135], a[100:103], v[64:67]\n"
  "s_waitcnt lgkmcnt(2)\n"
  "buffer_load_dwordx4 v221, s[40:43], 0 offen offset:128 lds\n"
  "v_cvt_pk_bf16_f32 v59, v54, v55\n"
  "v_mfma_f32_16x16x32_bf16 v[64:67], a[120:123], a[104:107], v[64:67]\n"
  "s_addk_i32 m0, 896\n"
  "v_exp_f32 v22, v22\n"
  "v_mfma_f32_16x16x32_bf16 v[64:67], a[124:127], a[108:111], v[64:67]\n"
  "s_waitcnt lgkmcnt(0)\n"
  "v_exp_f32 v23, v23\n"
  "v_mfma_f32_16x16x32_bf16 v[64:67], a[144:147], a[112:115], v[64:67]\n"
  "v_cvt_pk_bf16_f32 v43, v38, v39\n"
  "v_cvt_pk_bf16_f32 v26, v20, v21\n"
  "v_mfma_f32_16x16x32_bf16 v[64:67], a[148:151], a[116:119], v[64:67]\n"
  "buffer_load_dwordx4 v221, s[40:43], 0 offen offset:256 lds\n"
  "v_cvt_pk_bf16_f32 v27, v22, v23\n"
  "v_mfma_f32_16x16x32_bf16 v[0:3], a[148:151], a[20:23], v[248:251]\n"
  "v_fma_f32 v4, v4, s73, v223\n"
  "v_fma_f32 v5, v5, s73, v223\n"
  "v_mfma_f32_16x16x32_bf16 v[0:3], a[144:147], a[16:19], v[0:3]\n"
  "s_add_u32 s71, s92, s56\n"
  "v_exp_f32 v4, v4\n"
  "v_mfma_f32_16x16x32_bf16 v[0:3], a[124:127], a[12:15], v[0:3]\n"
  "s_add_u32 s40, s40, s31\n"
  "v_exp_f32 v5, v5\n"
  "v_mfma_f32_16x16x32_bf16 v[0:3], a[120:123], a[8:11], v[0:3]\n"
  "s_addc_u32 s41, s41, 0\n"
  "v_fma_f32 v6, v6, s73, v223\n"
  "v_fma_f32 v7, v7, s73, v223\n"
  "v_mfma_f32_16x16x32_bf16 v[0:3], a[132:135], a[4:7], v[0:3]\n"
  "s_add_u32 m0, s59, -128\n"
  "v_exp_f32 v6, v6\n"
  "v_mfma_f32_16x16x32_bf16 v[0:3], a[128:131], a[0:3], v[0:3]\n"
  "v_exp_f32 v7, v7\n"
  "v_mfma_f32_16x16x32_bf16 v[16:19], a[128:131], a[24:27], v[248:251]\n"
  "v_cvt_pk_bf16_f32 v10, v4, v5\n"
  "v_cvt_pk_bf16_f32 v11, v6, v7\n"
  "v_mfma_f32_16x16x32_bf16 v[16:19], a[132:135], a[28:31], v[16:19]\n"
  "buffer_load_dwordx4 v221, s[32:35], 0 offen offset:128 lds\n"
  "v_add_f32 v252, v252, v4\n"
  "v_mfma_f32_16x16x32_bf16 v[16:19], a[120:123], a[32:35], v[16:19]\n"
  "s_addk_i32 m0, 1024\n"
  "ds_read_b64_tr_b16 a[136:137], v217 offset:24704\n"
  "v_add_f32 v252, v252, v5\n"
  "v_mfma_f32_16x16x32_bf16 v[16:19], a[124:127], a[36:39], v[16:19]\n"
  "s_mov_b64 s[32:33], s[44:45]\n"
  "ds_read_b64_tr_b16 a[138:139], v218 offset:24704\n"
  "v_add_f32 v252, v252, v6\n"
  "v_mfma_f32_16x16x32_bf16 v[16:19], a[144:147], a[40:43], v[16:19]\n"
  "ds_read_b64_tr_b16 a[140:141], v217 offset:25728\n"
  "v_add_f32 v252, v252, v7\n"
  "v_mfma_f32_16x16x32_bf16 v[16:19], a[148:151], a[44:47], v[16:19]\n"
  "buffer_load_dwordx4 v221, s[44:47], 0 offen offset:128 lds\n"
  "v_add_f32 v253, v253, v20\n"
  "v_mfma_f32_16x16x32_bf16 v[32:35], a[148:151], a[68:71], v[248:251]\n"
  "s_addk_i32 m0, 896\n"
  "ds_read_b64_tr_b16 a[142:143], v218 offset:25728\n"
  "v_add_f32 v253, v253, v21\n"
  "v_mfma_f32_16x16x32_bf16 v[32:35], a[144:147], a[64:67], v[32:35]\n"
  "v_add_f32 v253, v253, v22\n"
  "v_add_f32 v253, v253, v23\n"
  "v_mfma_f32_16x16x32_bf16 v[32:35], a[124:127], a[60:63], v[32:35]\n"
  "v_add_f32 v254, v254, v36\n"
  "v_add_f32 v254, v254, v37\n"
  "v_mfma_f32_16x16x32_bf16 v[32:35], a[120:123], a[56:59], v[32:35]\n"
  "buffer_load_dwordx4 v221, s[44:47], 0 offen offset:256 lds\n"
  "v_add_f32 v254, v254, v38\n"
  "v_mfma_f32_16x16x32_bf16 v[32:35], a[132:135], a[52:55], v[32:35]\n"
  "s_add_u32 s44, s44, s31\n"
  "v_add_f32 v254, v254, v39\n"
  "v_add_f32 v255, v255, v52\n"
  "v_mfma_f32_16x16x32_bf16 v[32:35], a[128:131], a[48:51], v[32:35]\n"
  "s_addc_u32 s45, s45, 0\n"
  "v_add_f32 v255, v255, v53\n"
  "v_add_f32 v255, v255, v54\n"
  "v_mfma_f32_16x16x32_bf16 v[48:51], a[128:131], a[72:75], v[248:251]\n"
  "ds_read_b64_tr_b16 a[128:129], v217 offset:24720\n"
  "v_add_f32 v255, v255, v55\n"
  "v_mfma_f32_16x16x32_bf16 v[48:51], a[132:135], a[76:79], v[48:51]\n"
  "ds_read_b64_tr_b16 a[130:131], v218 offset:24720\n"
  "v_fma_f32 v64, v64, s73, v227\n"
  "v_mfma_f32_16x16x32_bf16 v[48:51], a[120:123], a[80:83], v[48:51]\n"
  "ds_read_b64_tr_b16 a[132:133], v217 offset:25744\n"
  "v_fma_f32 v65, v65, s73, v227\n"
  "v_mfma_f32_16x16x32_bf16 v[48:51], a[124:127], a[84:87], v[48:51]\n"
  "s_add_u32 m0, s72, 6464\n"
  "ds_read_b64_tr_b16 a[134:135], v218 offset:25744\n"
  "v_mfma_f32_16x16x32_bf16 v[48:51], a[144:147], a[88:91], v[48:51]\n"
  "v_exp_f32 v64, v64\n"
  "v_mfma_f32_16x16x32_bf16 v[48:51], a[148:151], a[92:95], v[48:51]\n"
  "s_waitcnt lgkmcnt(4)\n"
  "v_exp_f32 v65, v65\n"
  "v_mfma_f32_16x16x32_bf16 a[236:239], a[160:163], a[136:139], a[236:239]\n"
  "v_fma_f32 v66, v66, s73, v227\n"
  "v_fma_f32 v67, v67, s73, v227\n"
  "v_mfma_f32_16x16x32_bf16 a[236:239], a[156:159], a[140:143], a[236:239]\n"
  "buffer_load_dwordx4 v221, s[48:51], 0 offen offset:256 nt lds\n"
  "v_cvt_pk_bf16_f32 v64, v64, v65\n"
  "v_mfma_f32_16x16x32_bf16 v[184:187], v[60:63], a[136:139], v[184:187]\n"
  "v_exp_f32 v66, v66\n"
  "v_mfma_f32_16x16x32_bf16 v[184:187], v[56:59], a[140:143], v[184:187]\n"
  "v_exp_f32 v67, v67\n"
  "v_mfma_f32_16x16x32_bf16 v[152:155], v[44:47], a[136:139], v[152:155]\n"
  "ds_read_b64_tr_b16 a[120:121], v219 offset:24704\n"
  "v_cvt_pk_bf16_f32 v65, v66, v67\n"
  "v_mfma_f32_16x16x32_bf16 v[152:155], v[40:43], a[140:143], v[152:155]\n"
  "s_add_u32 m0, s72, 4480\n"
  "ds_read_b64_tr_b16 a[122:123], v220 offset:24704\n"
  "v_mfma_f32_16x16x32_bf16 v[120:123], v[28:31], a[136:139], v[120:123]\n"
  "ds_read_b64_tr_b16 a[124:125], v219 offset:25728\n"
  "v_fma_f32 v0, v0, s73, v223\n"
  "v_mfma_f32_16x16x32_bf16 v[120:123], v[24:27], a[140:143], v[120:123]\n"
  "ds_read_b64_tr_b16 a[126:127], v220 offset:25728\n"
  "v_fma_f32 v1, v1, s73, v223\n"
  "v_mfma_f32_16x16x32_bf16 v[88:91], v[12:15], a[136:139], v[88:91]\n"
  "v_exp_f32 v0, v0\n"
  "v_mfma_f32_16x16x32_bf16 v[88:91], v[8:11], a[140:143], v[88:91]\n"
  "s_waitcnt lgkmcnt(4)\n"
  "buffer_load_dwordx4 v221, s[48:51], 0 offen offset:3328 nt lds\n"
  "v_fma_f32 v2, v2, s73, v223\n"
  "v_mfma_f32_16x16x32_bf16 v[92:95], v[12:15], a[128:131], v[92:95]\n"
  "s_add_u32 s48, s84, s71\n"
  "v_exp_f32 v1, v1\n"
  "v_mfma_f32_16x16x32_bf16 v[92:95], v[8:11], a[132:135], v[92:95]\n"
  "s_addc_u32 s49, s85, 0\n"
  "v_exp_f32 v2, v2\n"
  "v_mfma_f32_16x16x32_bf16 v[124:127], v[28:31], a[128:131], v[124:127]\n"
  "s_sub_i32 s71, s38, s56\n"
  "ds_read_b128 a[136:139], v235 offset:24704\n"
  "v_fma_f32 v3, v3, s73, v223\n"
  "v_mfma_f32_16x16x32_bf16 v[124:127], v[24:27], a[132:135], v[124:127]\n"
  "s_add_u32 m0, s72, 17920\n"
  "ds_read_b128 a[140:143], v235 offset:24768\n"
  "v_add_f32 v252, v252, v0\n"
  "v_mfma_f32_16x16x32_bf16 v[156:159], v[44:47], a[128:131], v[156:159]\n"
  "s_max_i32 s50, s71, 0\n"
  "ds_read_b128 a[144:147], v235 offset:25728\n"
  "v_add_f32 v252, v252, v1\n"
  "v_mfma_f32_16x16x32_bf16 v[156:159], v[40:43], a[132:135], v[156:159]\n"
  "ds_read_b128 a[148:151], v235 offset:25792\n"
  "v_add_f32 v252, v252, v2\n"
  "v_mfma_f32_16x16x32_bf16 v[188:191], v[60:63], a[128:131], v[188:191]\n"
  "v_exp_f32 v3, v3\n"
  "v_mfma_f32_16x16x32_bf16 v[188:191], v[56:59], a[132:135], v[188:191]\n"
  "buffer_load_dwordx4 v221, s[48:51], 0 offen offset:0 nt lds\n"
  "v_add_f32 v252, v252, v3\n"
  "v_mfma_f32_16x16x32_bf16 a[240:243], a[160:163], a[128:131], a[240:243]\n"
  "ds_read_b128 a[128:131], v235 offset:26752\n"
  "v_fma_f32 v16, v16, s73, v224\n"
  "v_mfma_f32_16x16x32_bf16 a[240:243], a[156:159], a[132:135], a[240:243]\n"
  "s_waitcnt lgkmcnt(5)\n"
  "ds_read_b128 a[132:135], v235 offset:26816\n"
  "v_fma_f32 v17, v17, s73, v224\n"
  "v_mfma_f32_16x16x32_bf16 v[192:195], v[60:63], a[120:123], v[192:195]\n"
  "v_exp_f32 v16, v16\n"
  "v_mfma_f32_16x16x32_bf16 v[192:195], v[56:59], a[124:127], v[192:195]\n"
  "s_add_u32 m0, s72, 18912\n"
  "ds_write_b64 v231, v[64:65] offset:2048\n"
  "v_mfma_f32_16x16x32_bf16 v[160:163], v[44:47], a[120:123], v[160:163]\n"
  "v_exp_f32 v17, v17\n"
  "v_mfma_f32_16x16x32_bf16 v[160:163], v[40:43], a[124:127], v[160:163]\n"
  "v_fma_f32 v18, v18, s73, v224\n"
  "v_fma_f32 v19, v19, s73, v224\n"
  "v_mfma_f32_16x16x32_bf16 v[128:131], v[28:31], a[120:123], v[128:131]\n"
  "v_exp_f32 v18, v18\n"
  "v_mfma_f32_16x16x32_bf16 v[128:131], v[24:27], a[124:127], v[128:131]\n"
  "buffer_load_dwordx4 v221, s[48:51], 0 offen offset:128 nt lds\n"
  "v_add_f32 v253, v253, v16\n"
  "v_mfma_f32_16x16x32_bf16 v[96:99], v[12:15], a[120:123], v[96:99]\n"
  "v_exp_f32 v19, v19\n"
  "v_mfma_f32_16x16x32_bf16 v[96:99], v[8:11], a[124:127], v[96:99]\n"
  "s_waitcnt lgkmcnt(5)\n"
  "v_add_f32 v253, v253, v17\n"
  "v_add_f32 v253, v253, v18\n"
  "v_mfma_f32_16x16x32_bf16 v[4:7], a[136:139], a[0:3], v[248:251]\n"
  "v_add_f32 v253, v253, v19\n"
  "v_fma_f32 v32, v32, s73, v225\n"
  "v_mfma_f32_16x16x32_bf16 v[4:7], a[140:143], a[4:7], v[4:7]\n"
  "s_waitcnt lgkmcnt(3)\n"
  "v_exp_f32 v32, v32\n"
  "v_mfma_f32_16x16x32_bf16 v[4:7], a[144:147], a[8:11], v[4:7]\n"
  "v_fma_f32 v33, v33, s73, v225\n"
  "v_fma_f32 v34, v34, s73, v225\n"
  "v_mfma_f32_16x16x32_bf16 v[4:7], a[148:151], a[12:15], v[4:7]\n"
  "s_waitcnt lgkmcnt(1)\n"
  "v_exp_f32 v33, v33\n"
  "v_mfma_f32_16x16x32_bf16 v[4:7], a[128:131], a[16:19], v[4:7]\n"
  "v_exp_f32 v34, v34\n"
  "v_mfma_f32_16x16x32_bf16 v[4:7], a[132:135], a[20:23], v[4:7]\n"
  "v_fma_f32 v35, v35, s73, v225\n"
  "v_add_f32 v254, v254, v32\n"
  "v_mfma_f32_16x16x32_bf16 v[20:23], a[132:135], a[44:47], v[248:251]\n"
  "v_exp_f32 v35, v35\n"
  "v_mfma_f32_16x16x32_bf16 v[20:23], a[128:131], a[40:43], v[20:23]\n"
  "v_add_f32 v254, v254, v33\n"
  "v_add_f32 v254, v254, v34\n"
  "v_mfma_f32_16x16x32_bf16 v[20:23], a[148:151], a[36:39], v[20:23]\n"
  "ds_read_b64_tr_b16 a[120:121], v219 offset:24720\n"
  "v_add_f32 v254, v254, v35\n"
  "v_mfma_f32_16x16x32_bf16 v[20:23], a[144:147], a[32:35], v[20:23]\n"
  "ds_read_b64_tr_b16 a[122:123], v220 offset:24720\n"
  "v_fma_f32 v48, v48, s73, v226\n"
  "v_mfma_f32_16x16x32_bf16 v[20:23], a[140:143], a[28:31], v[20:23]\n"
  "ds_read_b64_tr_b16 a[124:125], v219 offset:25744\n"
  "v_fma_f32 v49, v49, s73, v226\n"
  "v_mfma_f32_16x16x32_bf16 v[20:23], a[136:139], a[24:27], v[20:23]\n"
  "ds_read_b64_tr_b16 a[126:127], v220 offset:25744\n"
  "v_fma_f32 v50, v50, s73, v226\n"
  "v_mfma_f32_16x16x32_bf16 v[36:39], a[136:139], a[48:51], v[248:251]\n"
  "v_exp_f32 v48, v48\n"
  "v_mfma_f32_16x16x32_bf16 v[36:39], a[140:143], a[52:55], v[36:39]\n"
  "v_exp_f32 v49, v49\n"
  "v_mfma_f32_16x16x32_bf16 v[36:39], a[144:147], a[56:59], v[36:39]\n"
  "v_exp_f32 v50, v50\n"
  "v_mfma_f32_16x16x32_bf16 v[36:39], a[148:151], a[60:63], v[36:39]\n"
  "v_fma_f32 v51, v51, s73, v226\n"
  "v_add_f32 v255, v255, v48\n"
  "v_mfma_f32_16x16x32_bf16 v[36:39], a[128:131], a[64:67], v[36:39]\n"
  "v_exp_f32 v51, v51\n"
  "v_mfma_f32_16x16x32_bf16 v[36:39], a[132:135], a[68:71], v[36:39]\n"
  "v_add_f32 v255, v255, v49\n"
  "v_add_f32 v255, v255, v50\n"
  "v_mfma_f32_16x16x32_bf16 v[52:55], a[132:135], a[92:95], v[248:251]\n"
  "ds_read_b64_tr_b16 a[132:133], v217 offset:26752\n"
  "v_add_f32 v255, v255, v51\n"
  "v_mfma_f32_16x16x32_bf16 v[52:55], a[128:131], a[88:91], v[52:55]\n"
  "ds_read_b64_tr_b16 a[134:135], v218 offset:26752\n"
  "v_fma_f32 v20, v20, s73, v224\n"
  "v_mfma_f32_16x16x32_bf16 v[52:55], a[148:151], a[84:87], v[52:55]\n"
  "ds_read_b64_tr_b16 a[128:129], v217 offset:26768\n"
  "v_fma_f32 v36, v36, s73, v225\n"
  "v_mfma_f32_16x16x32_bf16 v[52:55], a[144:147], a[80:83], v[52:55]\n"
  "ds_read_b64_tr_b16 a[130:131], v218 offset:26768\n"
  "v_fma_f32 v37, v37, s73, v225\n"
  "v_mfma_f32_16x16x32_bf16 v[52:55], a[140:143], a[76:79], v[52:55]\n"
  "v_exp_f32 v36, v36\n"
  "v_mfma_f32_16x16x32_bf16 v[52:55], a[136:139], a[72:75], v[52:55]\n"
  "s_waitcnt lgkmcnt(4)\n"
  "v_exp_f32 v37, v37\n"
  "v_mfma_f32_16x16x32_bf16 v[100:103], v[12:15], a[120:123], v[100:103]\n"
  "v_cvt_pk_bf16_f32 v12, v0, v1\n"
  "v_cvt_pk_bf16_f32 v13, v2, v3\n"
  "v_mfma_f32_16x16x32_bf16 v[100:103], v[8:11], a[124:127], v[100:103]\n"
  "v_exp_f32 v20, v20\n"
  "v_mfma_f32_16x16x32_bf16 v[132:135], v[28:31], a[120:123], v[132:135]\n"
  "ds_read_b64_tr_b16 a[140:141], v219 offset:26752\n"
  "v_cvt_pk_bf16_f32 v28, v16, v17\n"
  "v_mfma_f32_16x16x32_bf16 v[132:135], v[24:27], a[124:127], v[132:135]\n"
  "ds_read_b64_tr_b16 a[142:143], v220 offset:26752\n"
  "v_cvt_pk_bf16_f32 v29, v18, v19\n"
  "v_mfma_f32_16x16x32_bf16 v[164:167], v[44:47], a[120:123], v[164:167]\n"
  "ds_read_b64_tr_b16 a[136:137], v219 offset:26768\n"
  "v_cvt_pk_bf16_f32 v44, v32, v33\n"
  "v_mfma_f32_16x16x32_bf16 v[164:167], v[40:43], a[124:127], v[164:167]\n"
  "ds_read_b64_tr_b16 a[138:139], v220 offset:26768\n"
  "v_cvt_pk_bf16_f32 v45, v34, v35\n"
  "v_mfma_f32_16x16x32_bf16 v[196:199], v[60:63], a[120:123], v[196:199]\n"
  "v_cvt_pk_bf16_f32 v60, v48, v49\n"
  "v_cvt_pk_bf16_f32 v61, v50, v51\n"
  "v_mfma_f32_16x16x32_bf16 v[196:199], v[56:59], a[124:127], v[196:199]\n"
  "s_waitcnt lgkmcnt(4)\n"
  "v_fma_f32 v52, v52, s73, v226\n"
  "v_fma_f32 v53, v53, s73, v226\n"
  "v_mfma_f32_16x16x32_bf16 a[244:247], a[156:159], a[132:135], a[244:247]\n"
  "v_exp_f32 v52, v52\n"
  "v_mfma_f32_16x16x32_bf16 a[248:251], a[156:159], a[128:131], a[248:251]\n"
  "v_exp_f32 v53, v53\n"
  "v_mfma_f32_16x16x32_bf16 v[200:203], v[56:59], a[132:135], v[200:203]\n"
  "v_fma_f32 v54, v54, s73, v226\n"
  "v_fma_f32 v55, v55, s73, v226\n"
  "v_mfma_f32_16x16x32_bf16 v[204:207], v[56:59], a[128:131], v[204:207]\n"
  "v_exp_f32 v54, v54\n"
  "v_mfma_f32_16x16x32_bf16 v[168:171], v[40:43], a[132:135], v[168:171]\n"
  "v_exp_f32_e64 v55, v55\n"
  "v_mfma_f32_16x16x32_bf16 v[172:175], v[40:43], a[128:131], v[172:175]\n"
  "s_waitcnt vmcnt(14) lgkmcnt(0)\n"
  "s_barrier\n"
  "ds_read_b128 a[120:123], v216 offset:0\n"
  "v_mfma_f32_16x16x32_bf16 v[136:139], v[24:27], a[132:135], v[136:139]\n"
  "ds_read_b64 a[160:161], v231 offset:2048\n"
  "v_cvt_pk_bf16_f32 v62, v52, v53\n"
  "v_mfma_f32_16x16x32_bf16 v[140:143], v[24:27], a[128:131], v[140:143]\n"
  "ds_read_b64 a[162:163], v232 offset:2048\n"
  "v_cvt_pk_bf16_f32 v63, v54, v55\n"
  "v_mfma_f32_16x16x32_bf16 v[104:107], v[8:11], a[132:135], v[104:107]\n"
  "ds_read_b128 a[124:127], v216 offset:64\n"
  "v_fma_f32 v38, v38, s73, v225\n"
  "v_mfma_f32_16x16x32_bf16 v[108:111], v[8:11], a[128:131], v[108:111]\n"
  "ds_read_b128 a[132:135], v216 offset:1024\n"
  "v_fma_f32 v39, v39, s73, v225\n"
  "v_mfma_f32_16x16x32_bf16 v[112:115], v[8:11], a[140:143], v[112:115]\n"
  "ds_read_b128 a[128:131], v216 offset:1088\n"
  "v_cvt_pk_bf16_f32 v46, v36, v37\n"
  "v_mfma_f32_16x16x32_bf16 v[116:119], v[8:11], a[136:139], v[116:119]\n"
  "s_mov_b32 m0, s68\n"
  "ds_read_b128 a[148:151], v216 offset:2048\n"
  "v_fma_f32 v21, v21, s73, v224\n"
  "v_mfma_f32_16x16x32_bf16 v[144:147], v[24:27], a[140:143], v[144:147]\n"
  "ds_read_b128 a[144:147], v216 offset:2112\n"
  "v_fma_f32 v22, v22, s73, v224\n"
  "v_mfma_f32_16x16x32_bf16 v[148:151], v[24:27], a[136:139], v[148:151]\n"
  "v_exp_f32 v38, v38\n"
  "v_mfma_f32_16x16x32_bf16 v[176:179], v[40:43], a[140:143], v[176:179]\n"
  "v_exp_f32 v39, v39\n"
  "v_mfma_f32_16x16x32_bf16 v[180:183], v[40:43], a[136:139], v[180:183]\n"
  "buffer_load_dwordx4 v221, s[40:43], 0 offen offset:0 lds\n"
  "v_mfma_f32_16x16x32_bf16 v[208:211], v[56:59], a[140:143], v[208:211]\n"
  "s_addk_i32 m0, 896\n"
  "v_exp_f32 v21, v21\n"
  "v_mfma_f32_16x16x32_bf16 v[212:215], v[56:59], a[136:139], v[212:215]\n"
  "s_waitcnt lgkmcnt(4)\n"
  "v_exp_f32 v22, v22\n"
  "v_mfma_f32_16x16x32_bf16 v[64:67], a[120:123], a[96:99], v[248:251]\n"
  "v_cvt_pk_bf16_f32 v47, v38, v39\n"
  "v_fma_f32 v23, v23, s73, v224\n"
  "v_mfma_f32_16x16x32_bf16 v[64:67], a[124:127], a[100:103], v[64:67]\n"
  "s_waitcnt lgkmcnt(2)\n"
  "buffer_load_dwordx4 v221, s[40:43], 0 offen offset:128 lds\n"
  "v_cvt_pk_bf16_f32 v30, v20, v21\n"
  "v_mfma_f32_16x16x32_bf16 v[64:67], a[132:135], a[104:107], v[64:67]\n"
  "s_addk_i32 m0, 896\n"
  "v_exp_f32 v23, v23\n"
  "v_mfma_f32_16x16x32_bf16 v[64:67], a[128:131], a[108:111], v[64:67]\n"
  "s_waitcnt lgkmcnt(0)\n"
  "v_cvt_pk_bf16_f32 v31, v22, v23\n"
  "v_fma_f32 v4, v4, s73, v223\n"
  "v_mfma_f32_16x16x32_bf16 v[64:67], a[148:151], a[112:115], v[64:67]\n"
  "v_exp_f32 v4, v4\n"
  "v_mfma_f32_16x16x32_bf16 v[64:67], a[144:147], a[116:119], v[64:67]\n"
  "buffer_load_dwordx4 v221, s[40:43], 0 offen offset:256 lds\n"
  "v_fma_f32 v5, v5, s73, v223\n"
  "v_mfma_f32_16x16x32_bf16 v[0:3], a[144:147], a[20:23], v[248:251]\n"
  "s_add_u32 s40, s40, s31\n"
  "v_exp_f32 v5, v5\n"
  "v_mfma_f32_16x16x32_bf16 v[0:3], a[148:151], a[16:19], v[0:3]\n"
  "s_addc_u32 s41, s41, 0\n"
  "v_fma_f32 v6, v6, s73, v223\n"
  "v_fma_f32 v7, v7, s73, v223\n"
  "v_mfma_f32_16x16x32_bf16 v[0:3], a[128:131], a[12:15], v[0:3]\n"
  "v_exp_f32 v6, v6\n"
  "v_mfma_f32_16x16x32_bf16 v[0:3], a[132:135], a[8:11], v[0:3]\n"
  "s_mov_b32 m0, s60\n"
  "v_exp_f32 v7, v7\n"
  "v_mfma_f32_16x16x32_bf16 v[0:3], a[124:127], a[4:7], v[0:3]\n"
  "v_cvt_pk_bf16_f32 v14, v4, v5\n"
  "v_cvt_pk_bf16_f32 v15, v6, v7\n"
  "v_mfma_f32_16x16x32_bf16 v[0:3], a[120:123], a[0:3], v[0:3]\n"
  "v_add_f32 v252, v252, v4\n"
  "v_add_f32 v252, v252, v5\n"
  "v_mfma_f32_16x16x32_bf16 v[16:19], a[120:123], a[24:27], v[248:251]\n"
  "v_add_f32 v252, v252, v6\n"
  "v_add_f32 v252, v252, v7\n"
  "v_mfma_f32_16x16x32_bf16 v[16:19], a[124:127], a[28:31], v[16:19]\n"
  "buffer_load_dwordx4 v221, s[32:35], 0 offen offset:0 lds\n"
  "v_add_f32 v253, v253, v20\n"
  "v_mfma_f32_16x16x32_bf16 v[16:19], a[132:135], a[32:35], v[16:19]\n"
  "s_addk_i32 m0, 1024\n"
  "ds_read_b64_tr_b16 a[140:141], v217 offset:0\n"
  "v_add_f32 v253, v253, v21\n"
  "v_mfma_f32_16x16x32_bf16 v[16:19], a[128:131], a[36:39], v[16:19]\n"
  "s_mov_b64 s[32:33], s[44:45]\n"
  "ds_read_b64_tr_b16 a[142:143], v218 offset:0\n"
  "v_add_f32 v253, v253, v22\n"
  "v_mfma_f32_16x16x32_bf16 v[16:19], a[148:151], a[40:43], v[16:19]\n"
  "ds_read_b64_tr_b16 a[136:137], v217 offset:1024\n"
  "v_add_f32 v253, v253, v23\n"
  "v_mfma_f32_16x16x32_bf16 v[16:19], a[144:147], a[44:47], v[16:19]\n"
  "buffer_load_dwordx4 v221, s[44:47], 0 offen offset:0 lds\n"
  "v_add_f32 v254, v254, v36\n"
  "v_mfma_f32_16x16x32_bf16 v[32:35], a[144:147], a[68:71], v[248:251]\n"
  "s_addk_i32 m0, 768\n"
  "ds_read_b64_tr_b16 a[138:139], v218 offset:1024\n"
  "v_add_f32 v254, v254, v37\n"
  "v_mfma_f32_16x16x32_bf16 v[32:35], a[148:151], a[64:67], v[32:35]\n"
  "v_add_f32 v254, v254, v38\n"
  "v_add_f32 v254, v254, v39\n"
  "v_mfma_f32_16x16x32_bf16 v[32:35], a[128:131], a[60:63], v[32:35]\n"
  "v_add_f32 v255, v255, v52\n"
  "v_add_f32 v255, v255, v53\n"
  "v_mfma_f32_16x16x32_bf16 v[32:35], a[132:135], a[56:59], v[32:35]\n"
  "buffer_load_dwordx4 v221, s[44:47], 0 offen offset:256 lds\n"
  "v_add_f32 v255, v255, v54\n"
  "v_mfma_f32_16x16x32_bf16 v[32:35], a[124:127], a[52:55], v[32:35]\n"
  "s_add_u32 s44, s44, s31\n"
  "ds_read_b64_tr_b16 a[252:253], v230 offset:20160\n"
  "v_add_f32 v255, v255, v55\n"
  "v_mfma_f32_16x16x32_bf16 v[32:35], a[120:123], a[48:51], v[32:35]\n"
  "s_addc_u32 s45, s45, 0\n"
  "ds_read_b64_tr_b16 a[254:255], v230 offset:20192\n"
  "ds_read_b64_tr_b16 v[244:245], v230 offset:8960\n"
  "v_mfma_f32_16x16x32_bf16 v[48:51], a[120:123], a[72:75], v[248:251]\n"
  "ds_read_b64_tr_b16 v[246:247], v230 offset:8992\n"
  "v_fma_f32 v64, v64, s73, v227\n"
  "v_mfma_f32_16x16x32_bf16 v[48:51], a[124:127], a[76:79], v[48:51]\n"
  "v_exp_f32 v64, v64\n"
  "v_mfma_f32_16x16x32_bf16 v[48:51], a[132:135], a[80:83], v[48:51]\n"
  "ds_read_b64_tr_b16 a[120:121], v217 offset:16\n"
  "v_fma_f32 v65, v65, s73, v227\n"
  "v_mfma_f32_16x16x32_bf16 v[48:51], a[128:131], a[84:87], v[48:51]\n"
  "ds_read_b64_tr_b16 a[122:123], v218 offset:16\n"
  "v_fma_f32 v66, v66, s73, v227\n"
  "v_mfma_f32_16x16x32_bf16 v[48:51], a[148:151], a[88:91], v[48:51]\n"
  "ds_read_b64_tr_b16 a[124:125], v217 offset:1040\n"
  "v_fma_f32 v67, v67, s73, v227\n"
  "v_mfma_f32_16x16x32_bf16 v[48:51], a[144:147], a[92:95], v[48:51]\n"
  "s_waitcnt lgkmcnt(7)\n"
  "ds_read_b64_tr_b16 a[126:127], v218 offset:1040\n"
  "v_fma_f32 v0, v0, s73, v223\n"
  "v_mfma_f32_16x16x32_bf16 a[228:231], a[156:159], a[140:143], a[228:231]\n"
  "v_exp_f32 v65, v65\n"
  "v_mfma_f32_16x16x32_bf16 a[228:231], a[160:163], a[136:139], a[228:231]\n"
  "v_exp_f32 v66, v66\n"
  "v_mfma_f32_16x16x32_bf16 a[212:215], v[56:59], a[140:143], a[212:215]\n"
  "v_exp_f32 v67, v67\n"
  "v_mfma_f32_16x16x32_bf16 a[212:215], v[60:63], a[136:139], a[212:215]\n"
  "v_exp_f32 v0, v0\n"
  "v_mfma_f32_16x16x32_bf16 a[196:199], v[40:43], a[140:143], a[196:199]\n"
  "v_cvt_pk_bf16_f32 v64, v64, v65\n"
  "v_cvt_pk_bf16_f32 v65, v66, v67\n"
  "v_mfma_f32_16x16x32_bf16 a[196:199], v[44:47], a[136:139], a[196:199]\n"
  "v_fma_f32 v1, v1, s73, v223\n"
  "v_fma_f32 v2, v2, s73, v223\n"
  "v_mfma_f32_16x16x32_bf16 a[180:183], v[24:27], a[140:143], a[180:183]\n"
  "ds_read_b64_tr_b16 a[132:133], v219 offset:0\n"
  "v_fma_f32 v3, v3, s73, v223\n"
  "v_mfma_f32_16x16x32_bf16 a[180:183], v[28:31], a[136:139], a[180:183]\n"
  "ds_read_b64_tr_b16 a[134:135], v220 offset:0\n"
  "v_add_f32 v252, v252, v0\n"
  "v_mfma_f32_16x16x32_bf16 a[164:167], v[8:11], a[140:143], a[164:167]\n"
  "ds_read_b64_tr_b16 a[128:129], v219 offset:1024\n"
  "v_fma_f32 v16, v16, s73, v224\n"
  "v_mfma_f32_16x16x32_bf16 a[164:167], v[12:15], a[136:139], a[164:167]\n"
  "ds_read_b64_tr_b16 a[130:131], v220 offset:1024\n"
  "v_fma_f32 v17, v17, s73, v224\n"
  "v_mfma_f32_16x16x32_bf16 v[84:87], a[156:159], a[152:155], v[84:87]\n"
  "v_exp_f32 v1, v1\n"
  "v_mfma_f32_16x16x32_bf16 v[84:87], a[160:163], a[152:155], v[84:87]\n"
  "s_waitcnt lgkmcnt(4)\n"
  "v_exp_f32 v2, v2\n"
  "v_mfma_f32_16x16x32_bf16 a[168:171], v[8:11], a[120:123], a[168:171]\n"
  "v_exp_f32 v3, v3\n"
  "v_mfma_f32_16x16x32_bf16 a[168:171], v[12:15], a[124:127], a[168:171]\n"
  "v_exp_f32 v16, v16\n"
  "v_mfma_f32_16x16x32_bf16 a[184:187], v[24:27], a[120:123], a[184:187]\n"
  "ds_read_b128 a[140:143], v235 offset:0\n"
  "v_add_f32 v252, v252, v1\n"
  "v_mfma_f32_16x16x32_bf16 a[184:187], v[28:31], a[124:127], a[184:187]\n"
  "ds_read_b128 a[136:139], v235 offset:64\n"
  "v_add_f32 v252, v252, v2\n"
  "v_mfma_f32_16x16x32_bf16 a[200:203], v[40:43], a[120:123], a[200:203]\n"
  "ds_read_b128 a[148:151], v235 offset:1024\n"
  "v_add_f32 v252, v252, v3\n"
  "v_mfma_f32_16x16x32_bf16 a[200:203], v[44:47], a[124:127], a[200:203]\n"
  "ds_read_b128 a[144:147], v235 offset:1088\n"
  "v_fma_f32 v18, v18, s73, v224\n"
  "v_mfma_f32_16x16x32_bf16 a[216:219], v[56:59], a[120:123], a[216:219]\n"
  "v_exp_f32 v17, v17\n"
  "v_mfma_f32_16x16x32_bf16 a[216:219], v[60:63], a[124:127], a[216:219]\n"
  "v_exp_f32 v18, v18\n"
  "v_mfma_f32_16x16x32_bf16 a[232:235], a[156:159], a[120:123], a[232:235]\n"
  "ds_read_b128 a[120:123], v235 offset:2048\n"
  "v_fma_f32 v19, v19, s73, v224\n"
  "v_mfma_f32_16x16x32_bf16 a[232:235], a[160:163], a[124:127], a[232:235]\n"
  "s_waitcnt lgkmcnt(5)\n"
  "ds_read_b128 a[124:127], v235 offset:2112\n"
  "v_add_f32 v253, v253, v16\n"
  "v_mfma_f32_16x16x32_bf16 a[220:223], v[56:59], a[132:135], a[220:223]\n"
  "v_exp_f32 v19, v19\n"
  "v_mfma_f32_16x16x32_bf16 a[220:223], v[60:63], a[128:131], a[220:223]\n"
  "ds_write_b64 v231, v[64:65] offset:0\n"
  "v_mfma_f32_16x16x32_bf16 a[204:207], v[40:43], a[132:135], a[204:207]\n"
  "v_add_f32 v253, v253, v17\n"
  "v_add_f32 v253, v253, v18\n"
  "v_mfma_f32_16x16x32_bf16 a[204:207], v[44:47], a[128:131], a[204:207]\n"
  "buffer_store_dwordx4 a[252:255], v228, s[52:55], s56 offen offset:256 nt\n"
  "v_add_f32 v253, v253, v19\n"
  "v_fma_f32 v32, v32, s73, v225\n"
  "v_fma_f32 v33, v33, s73, v225\n"
  "v_mfma_f32_16x16x32_bf16 a[188:191], v[24:27], a[132:135], a[188:191]\n"
  "ds_read_b64_tr_b16 a[252:253], v230 offset:10080\n"
  "ds_read_b64_tr_b16 a[254:255], v230 offset:10112\n"
  "v_mfma_f32_16x16x32_bf16 a[188:191], v[28:31], a[128:131], a[188:191]\n"
  "v_exp_f32 v32, v32\n"
  "v_mfma_f32_16x16x32_bf16 a[172:175], v[8:11], a[132:135], a[172:175]\n"
  "v_exp_f32 v33, v33\n"
  "v_mfma_f32_16x16x32_bf16 a[172:175], v[12:15], a[128:131], a[172:175]\n"
  "s_waitcnt lgkmcnt(7)\n"
  "v_fma_f32 v34, v34, s73, v225\n"
  "v_fma_f32 v35, v35, s73, v225\n"
  "v_mfma_f32_16x16x32_bf16 v[4:7], a[140:143], a[0:3], v[248:251]\n"
  "v_exp_f32 v34, v34\n"
  "v_mfma_f32_16x16x32_bf16 v[4:7], a[136:139], a[4:7], v[4:7]\n"
  "s_waitcnt lgkmcnt(5)\n"
  "v_exp_f32 v35, v35\n"
  "v_mfma_f32_16x16x32_bf16 v[4:7], a[148:151], a[8:11], v[4:7]\n"
  "v_add_f32 v254, v254, v32\n"
  "v_add_f32 v254, v254, v33\n"
  "v_mfma_f32_16x16x32_bf16 v[4:7], a[144:147], a[12:15], v[4:7]\n"
  "s_waitcnt lgkmcnt(3)\n"
  "v_add_f32 v254, v254, v34\n"
  "v_add_f32 v254, v254, v35\n"
  "v_mfma_f32_16x16x32_bf16 v[4:7], a[120:123], a[16:19], v[4:7]\n"
  "v_fma_f32 v48, v48, s73, v226\n"
  "v_fma_f32 v49, v49, s73, v226\n"
  "v_mfma_f32_16x16x32_bf16 v[4:7], a[124:127], a[20:23], v[4:7]\n"
  "buffer_store_dwordx4 v[244:247], v228, s[52:55], s80 offen offset:128 nt\n"
  "v_exp_f32 v48, v48\n"
  "v_fma_f32 v50, v50, s73, v226\n"
  "v_mfma_f32_16x16x32_bf16 v[20:23], a[124:127], a[44:47], v[248:251]\n"
  "v_exp_f32 v49, v49\n"
  "v_mfma_f32_16x16x32_bf16 v[20:23], a[120:123], a[40:43], v[20:23]\n"
  "v_exp_f32 v50, v50\n"
  "v_mfma_f32_16x16x32_bf16 v[20:23], a[144:147], a[36:39], v[20:23]\n"
  "ds_read_b64_tr_b16 a[132:133], v219 offset:16\n"
  "v_fma_f32 v51, v51, s73, v226\n"
  "v_mfma_f32_16x16x32_bf16 v[20:23], a[148:151], a[32:35], v[20:23]\n"
  "ds_read_b64_tr_b16 a[134:135], v220 offset:16\n"
  "v_add_f32 v255, v255, v48\n"
  "v_mfma_f32_16x16x32_bf16 v[20:23], a[136:139], a[28:31], v[20:23]\n"
  "ds_read_b64_tr_b16 a[128:129], v219 offset:1040\n"
  "v_add_f32 v255, v255, v49\n"
  "v_mfma_f32_16x16x32_bf16 v[20:23], a[140:143], a[24:27], v[20:23]\n"
  "s_waitcnt lgkmcnt(3)\n"
  "buffer_store_dwordx4 a[252:255], v228, s[52:55], s80 offen offset:3200 nt\n"
  "ds_read_b64_tr_b16 a[130:131], v220 offset:1040\n"
  "v_exp_f32 v51, v51\n"
  "v_mfma_f32_16x16x32_bf16 v[36:39], a[140:143], a[48:51], v[248:251]\n"
  "v_add_f32 v255, v255, v50\n"
  "v_add_f32 v255, v255, v51\n"
  "v_mfma_f32_16x16x32_bf16 v[36:39], a[136:139], a[52:55], v[36:39]\n"
  "v_fma_f32 v4, v4, s73, v223\n"
  "v_fma_f32 v5, v5, s73, v223\n"
  "v_mfma_f32_16x16x32_bf16 v[36:39], a[148:151], a[56:59], v[36:39]\n"
  "v_exp_f32 v4, v4\n"
  "v_mfma_f32_16x16x32_bf16 v[36:39], a[144:147], a[60:63], v[36:39]\n"
  "v_exp_f32 v5, v5\n"
  "v_mfma_f32_16x16x32_bf16 v[36:39], a[120:123], a[64:67], v[36:39]\n"
  "v_fma_f32 v20, v20, s73, v224\n"
  "v_fma_f32 v21, v21, s73, v224\n"
  "v_mfma_f32_16x16x32_bf16 v[36:39], a[124:127], a[68:71], v[36:39]\n"
  "v_exp_f32 v20, v20\n"
  "v_mfma_f32_16x16x32_bf16 v[52:55], a[124:127], a[92:95], v[248:251]\n"
  "ds_read_b64_tr_b16 a[124:125], v217 offset:2048\n"
  "v_fma_f32 v22, v22, s73, v224\n"
  "v_mfma_f32_16x16x32_bf16 v[52:55], a[120:123], a[88:91], v[52:55]\n"
  "ds_read_b64_tr_b16 a[126:127], v218 offset:2048\n"
  "v_fma_f32 v23, v23, s73, v224\n"
  "v_mfma_f32_16x16x32_bf16 v[52:55], a[144:147], a[84:87], v[52:55]\n"
  "ds_read_b64_tr_b16 a[120:121], v217 offset:2064\n"
  "v_fma_f32 v36, v36, s73, v225\n"
  "v_mfma_f32_16x16x32_bf16 v[52:55], a[148:151], a[80:83], v[52:55]\n"
  "ds_read_b64_tr_b16 a[122:123], v218 offset:2064\n"
  "v_fma_f32 v37, v37, s73, v225\n"
  "v_mfma_f32_16x16x32_bf16 v[52:55], a[136:139], a[76:79], v[52:55]\n"
  "v_exp_f32 v36, v36\n"
  "v_mfma_f32_16x16x32_bf16 v[52:55], a[140:143], a[72:75], v[52:55]\n"
  "s_waitcnt lgkmcnt(4)\n"
  "v_exp_f32 v37, v37\n"
  "v_mfma_f32_16x16x32_bf16 a[176:179], v[8:11], a[132:135], a[176:179]\n"
  "v_cvt_pk_bf16_f32 v8, v0, v1\n"
  "v_cvt_pk_bf16_f32 v9, v2, v3\n"
  "v_mfma_f32_16x16x32_bf16 a[176:179], v[12:15], a[128:131], a[176:179]\n"
  "v_exp_f32 v21, v21\n"
  "v_mfma_f32_16x16x32_bf16 a[192:195], v[24:27], a[132:135], a[192:195]\n"
  "ds_read_b64_tr_b16 a[136:137], v219 offset:2048\n"
  "v_cvt_pk_bf16_f32 v24, v16, v17\n"
  "v_mfma_f32_16x16x32_bf16 a[192:195], v[28:31], a[128:131], a[192:195]\n"
  "ds_read_b64_tr_b16 a[138:139], v220 offset:2048\n"
  "v_cvt_pk_bf16_f32 v25, v18, v19\n"
  "v_mfma_f32_16x16x32_bf16 a[208:211], v[40:43], a[132:135], a[208:211]\n"
  "ds_read_b64_tr_b16 a[140:141], v219 offset:2064\n"
  "v_cvt_pk_bf16_f32 v40, v32, v33\n"
  "v_mfma_f32_16x16x32_bf16 a[208:211], v[44:47], a[128:131], a[208:211]\n"
  "ds_read_b64_tr_b16 a[142:143], v220 offset:2064\n"
  "v_cvt_pk_bf16_f32 v41, v34, v35\n"
  "v_mfma_f32_16x16x32_bf16 a[224:227], v[56:59], a[132:135], a[224:227]\n"
  "v_exp_f32 v22, v22\n"
  "v_mfma_f32_16x16x32_bf16 a[224:227], v[60:63], a[128:131], a[224:227]\n"
  "s_waitcnt lgkmcnt(4)\n"
  "v_exp_f32 v23, v23\n"
  "v_mfma_f32_16x16x32_bf16 a[244:247], a[160:163], a[124:127], a[244:247]\n"
  "v_cvt_pk_bf16_f32 v56, v48, v49\n"
  "v_cvt_pk_bf16_f32 v57, v50, v51\n"
  "v_mfma_f32_16x16x32_bf16 a[248:251], a[160:163], a[120:123], a[248:251]\n"
  "v_fma_f32 v52, v52, s73, v226\n"
  "v_fma_f32 v53, v53, s73, v226\n"
  "v_mfma_f32_16x16x32_bf16 v[200:203], v[60:63], a[124:127], v[200:203]\n"
  "v_exp_f32 v52, v52\n"
  "v_mfma_f32_16x16x32_bf16 v[204:207], v[60:63], a[120:123], v[204:207]\n"
  "v_exp_f32 v53, v53\n"
  "v_mfma_f32_16x16x32_bf16 v[168:171], v[44:47], a[124:127], v[168:171]\n"
  "v_fma_f32 v54, v54, s73, v226\n"
  "v_fma_f32 v55, v55, s73, v226\n"
  "v_mfma_f32_16x16x32_bf16 v[172:175], v[44:47], a[120:123], v[172:175]\n"
  "s_waitcnt vmcnt(13) lgkmcnt(0)\n"
  "s_barrier\n"
  "ds_read_b128 a[132:135], v216 offset:12352\n"
  "v_mfma_f32_16x16x32_bf16 v[136:139], v[28:31], a[124:127], v[136:139]\n"
  "ds_read_b64 a[156:157], v231 offset:0\n"
  "v_cvt_pk_bf16_f32 v58, v52, v53\n"
  "v_mfma_f32_16x16x32_bf16 v[140:143], v[28:31], a[120:123], v[140:143]\n"
  "ds_read_b64 a[158:159], v232 offset:0\n"
  "v_fma_f32 v38, v38, s73, v225\n"
  "v_mfma_f32_16x16x32_bf16 v[104:107], v[12:15], a[124:127], v[104:107]\n"
  "ds_read_b128 a[128:131], v216 offset:12416\n"
  "v_fma_f32 v39, v39, s73, v225\n"
  "v_mfma_f32_16x16x32_bf16 v[108:111], v[12:15], a[120:123], v[108:111]\n"
  "ds_read_b128 a[124:127], v216 offset:13376\n"
  "v_cvt_pk_bf16_f32 v42, v36, v37\n"
  "v_mfma_f32_16x16x32_bf16 v[112:115], v[12:15], a[136:139], v[112:115]\n"
  "ds_read_b128 a[120:123], v216 offset:13440\n"
  "v_cvt_pk_bf16_f32 v26, v20, v21\n"
  "v_mfma_f32_16x16x32_bf16 v[116:119], v[12:15], a[140:143], v[116:119]\n"
  "s_mov_b32 m0, s66\n"
  "ds_read_b128 a[144:147], v216 offset:14400\n"
  "v_cvt_pk_bf16_f32 v27, v22, v23\n"
  "v_mfma_f32_16x16x32_bf16 v[144:147], v[28:31], a[136:139], v[144:147]\n"
  "ds_read_b128 a[148:151], v216 offset:14464\n"
  "v_fma_f32 v6, v6, s73, v223\n"
  "v_mfma_f32_16x16x32_bf16 v[148:151], v[28:31], a[140:143], v[148:151]\n"
  "v_exp_f32 v54, v54\n"
  "v_mfma_f32_16x16x32_bf16 v[176:179], v[44:47], a[136:139], v[176:179]\n"
  "v_exp_f32 v55, v55\n"
  "v_mfma_f32_16x16x32_bf16 v[180:183], v[44:47], a[140:143], v[180:183]\n"
  "buffer_load_dwordx4 v221, s[40:43], 0 offen offset:0 lds\n"
  "v_mfma_f32_16x16x32_bf16 v[208:211], v[60:63], a[136:139], v[208:211]\n"
  "s_addk_i32 m0, 896\n"
  "v_exp_f32 v38, v38\n"
  "v_mfma_f32_16x16x32_bf16 v[212:215], v[60:63], a[140:143], v[212:215]\n"
  "s_waitcnt lgkmcnt(4)\n"
  "v_exp_f32 v39, v39\n"
  "v_mfma_f32_16x16x32_bf16 v[64:67], a[132:135], a[96:99], v[248:251]\n"
  "s_add_u32 s71, s92, s81\n"
  "v_exp_f32 v6, v6\n"
  "v_mfma_f32_16x16x32_bf16 v[64:67], a[128:131], a[100:103], v[64:67]\n"
  "s_waitcnt lgkmcnt(2)\n"
  "buffer_load_dwordx4 v221, s[40:43], 0 offen offset:128 lds\n"
  "v_cvt_pk_bf16_f32 v59, v54, v55\n"
  "v_mfma_f32_16x16x32_bf16 v[64:67], a[124:127], a[104:107], v[64:67]\n"
  "s_addk_i32 m0, 896\n"
  "v_cvt_pk_bf16_f32 v43, v38, v39\n"
  "v_fma_f32 v7, v7, s73, v223\n"
  "v_mfma_f32_16x16x32_bf16 v[64:67], a[120:123], a[108:111], v[64:67]\n"
  "s_waitcnt lgkmcnt(0)\n"
  "v_exp_f32 v7, v7\n"
  "v_mfma_f32_16x16x32_bf16 v[64:67], a[144:147], a[112:115], v[64:67]\n"
  "v_cvt_pk_bf16_f32 v10, v4, v5\n"
  "v_cvt_pk_bf16_f32 v11, v6, v7\n"
  "v_mfma_f32_16x16x32_bf16 v[64:67], a[148:151], a[116:119], v[64:67]\n"
  "buffer_load_dwordx4 v221, s[40:43], 0 offen offset:256 lds\n"
  "v_add_f32 v252, v252, v4\n"
  "v_mfma_f32_16x16x32_bf16 v[0:3], a[148:151], a[20:23], v[248:251]\n"
  "s_add_u32 s40, s40, s31\n"
  "v_add_f32 v252, v252, v5\n"
  "v_add_f32 v252, v252, v6\n"
  "v_mfma_f32_16x16x32_bf16 v[0:3], a[144:147], a[16:19], v[0:3]\n"
  "s_addc_u32 s41, s41, 0\n"
  "v_add_f32 v252, v252, v7\n"
  "v_add_f32 v253, v253, v20\n"
  "v_mfma_f32_16x16x32_bf16 v[0:3], a[120:123], a[12:15], v[0:3]\n"
  "v_add_f32 v253, v253, v21\n"
  "v_add_f32 v253, v253, v22\n"
  "v_mfma_f32_16x16x32_bf16 v[0:3], a[124:127], a[8:11], v[0:3]\n"
  "s_add_u32 m0, s58, -128\n"
  "v_add_f32 v253, v253, v23\n"
  "v_add_f32 v254, v254, v36\n"
  "v_mfma_f32_16x16x32_bf16 v[0:3], a[128:131], a[4:7], v[0:3]\n"
  "v_add_f32 v254, v254, v37\n"
  "v_add_f32 v254, v254, v38\n"
  "v_mfma_f32_16x16x32_bf16 v[0:3], a[132:135], a[0:3], v[0:3]\n"
  "v_add_f32 v254, v254, v39\n"
  "v_bfe_u32 v237, v231, 1, 8\n"
  "v_mfma_f32_16x16x32_bf16 v[16:19], a[132:135], a[24:27], v[248:251]\n"
  "v_add_u32 v237, s29, v237\n"
  "v_add_f32 v255, v255, v52\n"
  "v_mfma_f32_16x16x32_bf16 v[16:19], a[128:131], a[28:31], v[16:19]\n"
  "buffer_load_dwordx4 v221, s[32:35], 0 offen offset:128 lds\n"
  "ds_read_b32 v236, v237\n"
  "v_mfma_f32_16x16x32_bf16 v[16:19], a[124:127], a[32:35], v[16:19]\n"
  "s_addk_i32 m0, 1024\n"
  "ds_read_b64_tr_b16 a[136:137], v217 offset:12352\n"
  "v_add_f32 v255, v255, v53\n"
  "v_mfma_f32_16x16x32_bf16 v[16:19], a[120:123], a[36:39], v[16:19]\n"
  "s_mov_b64 s[32:33], s[44:45]\n"
  "ds_read_b64_tr_b16 a[138:139], v218 offset:12352\n"
  "v_add_f32 v255, v255, v54\n"
  "v_mfma_f32_16x16x32_bf16 v[16:19], a[144:147], a[40:43], v[16:19]\n"
  "ds_read_b64_tr_b16 a[140:141], v217 offset:13376\n"
  "v_add_f32 v255, v255, v55\n"
  "v_mfma_f32_16x16x32_bf16 v[16:19], a[148:151], a[44:47], v[16:19]\n"
  "buffer_load_dwordx4 v221, s[44:47], 0 offen offset:128 lds\n"
  "v_fma_f32 v64, v64, s73, v227\n"
  "v_mfma_f32_16x16x32_bf16 v[32:35], a[148:151], a[68:71], v[248:251]\n"
  "s_addk_i32 m0, 896\n"
  "ds_read_b64_tr_b16 a[142:143], v218 offset:13376\n"
  "v_fma_f32 v65, v65, s73, v227\n"
  "v_mfma_f32_16x16x32_bf16 v[32:35], a[144:147], a[64:67], v[32:35]\n"
  "s_waitcnt lgkmcnt(4)\n"
  "v_exp_f32 v64, v64\n"
  "v_mfma_f32_16x16x32_bf16 v[32:35], a[120:123], a[60:63], v[32:35]\n"
  "v_exp_f32 v65, v65\n"
  "v_mfma_f32_16x16x32_bf16 v[32:35], a[124:127], a[56:59], v[32:35]\n"
  "buffer_load_dwordx4 v221, s[44:47], 0 offen offset:256 lds\n"
  "v_fma_f32 v66, v66, s73, v227\n"
  "v_mfma_f32_16x16x32_bf16 v[32:35], a[128:131], a[52:55], v[32:35]\n"
  "s_add_u32 s44, s44, s31\n"
  "v_exp_f32 v66, v66\n"
  "v_mfma_f32_16x16x32_bf16 v[32:35], a[132:135], a[48:51], v[32:35]\n"
  "s_addc_u32 s45, s45, 0\n"
  "v_fma_f32 v67, v67, s73, v227\n"
  "v_cvt_pk_bf16_f32 v64, v64, v65\n"
  "v_mfma_f32_16x16x32_bf16 v[48:51], a[132:135], a[72:75], v[248:251]\n"
  "ds_read_b64_tr_b16 a[132:133], v217 offset:12368\n"
  "v_fma_f32 v0, v0, s73, v223\n"
  "v_mfma_f32_16x16x32_bf16 v[48:51], a[128:131], a[76:79], v[48:51]\n"
  "ds_read_b64_tr_b16 a[134:135], v218 offset:12368\n"
  "v_fma_f32 v1, v1, s73, v223\n"
  "v_mfma_f32_16x16x32_bf16 v[48:51], a[124:127], a[80:83], v[48:51]\n"
  "ds_read_b64_tr_b16 a[128:129], v217 offset:13392\n"
  "v_fma_f32 v2, v2, s73, v223\n"
  "v_mfma_f32_16x16x32_bf16 v[48:51], a[120:123], a[84:87], v[48:51]\n"
  "s_add_u32 m0, s72, 19904\n"
  "ds_read_b64_tr_b16 a[130:131], v218 offset:13392\n"
  "v_mfma_f32_16x16x32_bf16 v[48:51], a[144:147], a[88:91], v[48:51]\n"
  "v_exp_f32 v67, v67\n"
  "v_mfma_f32_16x16x32_bf16 v[48:51], a[148:151], a[92:95], v[48:51]\n"
  "s_waitcnt lgkmcnt(4)\n"
  "v_exp_f32 v0, v0\n"
  "v_mfma_f32_16x16x32_bf16 a[236:239], a[160:163], a[136:139], a[236:239]\n"
  "v_exp_f32 v1, v1\n"
  "v_mfma_f32_16x16x32_bf16 a[236:239], a[156:159], a[140:143], a[236:239]\n"
  "buffer_load_dwordx4 v221, s[48:51], 0 offen offset:256 nt lds\n"
  "v_cvt_pk_bf16_f32 v65, v66, v67\n"
  "v_mfma_f32_16x16x32_bf16 v[184:187], v[60:63], a[136:139], v[184:187]\n"
  "s_add_u32 s48, s84, s71\n"
  "v_exp_f32 v2, v2\n"
  "v_mfma_f32_16x16x32_bf16 v[184:187], v[56:59], a[140:143], v[184:187]\n"
  "s_addc_u32 s49, s85, 0\n"
  "v_fma_f32 v3, v3, s73, v223\n"
  "v_add_f32 v252, v252, v0\n"
  "v_mfma_f32_16x16x32_bf16 v[152:155], v[44:47], a[136:139], v[152:155]\n"
  "s_sub_i32 s71, s38, s81\n"
  "ds_read_b64_tr_b16 a[124:125], v219 offset:12352\n"
  "v_add_f32 v252, v252, v1\n"
  "v_mfma_f32_16x16x32_bf16 v[152:155], v[40:43], a[140:143], v[152:155]\n"
  "s_add_u32 m0, s72, 8832\n"
  "ds_read_b64_tr_b16 a[126:127], v220 offset:12352\n"
  "v_add_f32 v252, v252, v2\n"
  "v_mfma_f32_16x16x32_bf16 v[120:123], v[28:31], a[136:139], v[120:123]\n"
  "s_max_i32 s50, s71, 0\n"
  "ds_read_b64_tr_b16 a[120:121], v219 offset:13376\n"
  "v_fma_f32 v16, v16, s73, v224\n"
  "v_mfma_f32_16x16x32_bf16 v[120:123], v[24:27], a[140:143], v[120:123]\n"
  "ds_read_b64_tr_b16 a[122:123], v220 offset:13376\n"
  "v_fma_f32 v17, v17, s73, v224\n"
  "v_mfma_f32_16x16x32_bf16 v[88:91], v[12:15], a[136:139], v[88:91]\n"
  "v_exp_f32 v3, v3\n"
  "v_mfma_f32_16x16x32_bf16 v[88:91], v[8:11], a[140:143], v[88:91]\n"
  "s_waitcnt lgkmcnt(4)\n"
  "buffer_load_dwordx4 v221, s[48:51], 0 offen offset:128 nt lds\n"
  "v_add_f32 v252, v252, v3\n"
  "v_mfma_f32_16x16x32_bf16 v[92:95], v[12:15], a[132:135], v[92:95]\n"
  "v_exp_f32 v16, v16\n"
  "v_mfma_f32_16x16x32_bf16 v[92:95], v[8:11], a[128:131], v[92:95]\n"
  "v_exp_f32 v17, v17\n"
  "v_mfma_f32_16x16x32_bf16 v[124:127], v[28:31], a[132:135], v[124:127]\n"
  "ds_read_b128 a[136:139], v235 offset:12352\n"
  "v_fma_f32 v18, v18, s73, v224\n"
  "v_mfma_f32_16x16x32_bf16 v[124:127], v[24:27], a[128:131], v[124:127]\n"
  "s_add_u32 m0, s72, 6848\n"
  "ds_read_b128 a[140:143], v235 offset:12416\n"
  "v_add_f32 v253, v253, v16\n"
  "v_mfma_f32_16x16x32_bf16 v[156:159], v[44:47], a[132:135], v[156:159]\n"
  "ds_read_b128 a[144:147], v235 offset:13376\n"
  "v_fma_f32 v19, v19, s73, v224\n"
  "v_mfma_f32_16x16x32_bf16 v[156:159], v[40:43], a[128:131], v[156:159]\n"
  "ds_read_b128 a[148:151], v235 offset:13440\n"
  "v_add_f32 v253, v253, v17\n"
  "v_mfma_f32_16x16x32_bf16 v[188:191], v[60:63], a[132:135], v[188:191]\n"
  "v_exp_f32 v18, v18\n"
  "v_mfma_f32_16x16x32_bf16 v[188:191], v[56:59], a[128:131], v[188:191]\n"
  "buffer_load_dwordx4 v221, s[48:51], 0 offen offset:3200 nt lds\n"
  "v_add_f32 v253, v253, v18\n"
  "v_mfma_f32_16x16x32_bf16 a[240:243], a[160:163], a[132:135], a[240:243]\n"
  "ds_read_b128 a[132:135], v235 offset:14400\n"
  "v_fma_f32 v32, v32, s73, v225\n"
  "v_mfma_f32_16x16x32_bf16 a[240:243], a[156:159], a[128:131], a[240:243]\n"
  "s_waitcnt lgkmcnt(5)\n"
  "ds_read_b128 a[128:131], v235 offset:14464\n"
  "v_fma_f32 v33, v33, s73, v225\n"
  "v_mfma_f32_16x16x32_bf16 v[192:195], v[60:63], a[124:127], v[192:195]\n"
  "v_exp_f32 v19, v19\n"
  "v_mfma_f32_16x16x32_bf16 v[192:195], v[56:59], a[120:123], v[192:195]\n"
  "ds_write_b64 v231, v[64:65] offset:2048\n"
  "v_mfma_f32_16x16x32_bf16 v[160:163], v[44:47], a[124:127], v[160:163]\n"
  "v_exp_f32 v32, v32\n"
  "v_mfma_f32_16x16x32_bf16 v[160:163], v[40:43], a[120:123], v[160:163]\n"
  "v_exp_f32 v33, v33\n"
  "v_mfma_f32_16x16x32_bf16 v[128:131], v[28:31], a[124:127], v[128:131]\n"
  "v_add_f32 v253, v253, v19\n"
  "v_fma_f32 v34, v34, s73, v225\n"
  "v_mfma_f32_16x16x32_bf16 v[128:131], v[24:27], a[120:123], v[128:131]\n"
  "v_exp_f32 v34, v34\n"
  "v_mfma_f32_16x16x32_bf16 v[96:99], v[12:15], a[124:127], v[96:99]\n"
  "v_fma_f32 v35, v35, s73, v225\n"
  "v_add_f32 v254, v254, v32\n"
  "v_mfma_f32_16x16x32_bf16 v[96:99], v[8:11], a[120:123], v[96:99]\n"
  "s_waitcnt lgkmcnt(5)\n"
  "v_exp_f32 v35, v35\n"
  "v_mfma_f32_16x16x32_bf16 v[4:7], a[136:139], a[0:3], v[248:251]\n"
  "v_add_f32 v254, v254, v33\n"
  "v_add_f32 v254, v254, v34\n"
  "v_mfma_f32_16x16x32_bf16 v[4:7], a[140:143], a[4:7], v[4:7]\n"
  "s_waitcnt lgkmcnt(3)\n"
  "v_add_f32 v254, v254, v35\n"
  "v_fma_f32 v48, v48, s73, v226\n"
  "v_mfma_f32_16x16x32_bf16 v[4:7], a[144:147], a[8:11], v[4:7]\n"
  "v_exp_f32 v48, v48\n"
  "v_mfma_f32_16x16x32_bf16 v[4:7], a[148:151], a[12:15], v[4:7]\n"
  "s_waitcnt lgkmcnt(1)\n"
  "v_fma_f32 v49, v49, s73, v226\n"
  "v_fma_f32 v50, v50, s73, v226\n"
  "v_mfma_f32_16x16x32_bf16 v[4:7], a[132:135], a[16:19], v[4:7]\n"
  "v_exp_f32 v49, v49\n"
  "v_mfma_f32_16x16x32_bf16 v[4:7], a[128:131], a[20:23], v[4:7]\n"
  "v_exp_f32 v50, v50\n"
  "v_mfma_f32_16x16x32_bf16 v[20:23], a[128:131], a[44:47], v[248:251]\n"
  "v_fma_f32 v51, v51, s73, v226\n"
  "v_add_f32 v255, v255, v48\n"
  "v_mfma_f32_16x16x32_bf16 v[20:23], a[132:135], a[40:43], v[20:23]\n"
  "v_exp_f32 v51, v51\n"
  "v_mfma_f32_16x16x32_bf16 v[20:23], a[148:151], a[36:39], v[20:23]\n"
  "ds_read_b64_tr_b16 a[124:125], v219 offset:12368\n"
  "v_add_f32 v255, v255, v49\n"
  "v_mfma_f32_16x16x32_bf16 v[20:23], a[144:147], a[32:35], v[20:23]\n"
  "ds_read_b64_tr_b16 a[126:127], v220 offset:12368\n"
  "v_add_f32 v255, v255, v50\n"
  "v_mfma_f32_16x16x32_bf16 v[20:23], a[140:143], a[28:31], v[20:23]\n"
  "ds_read_b64_tr_b16 a[120:121], v219 offset:13392\n"
  "v_add_f32 v255, v255, v51\n"
  "v_mfma_f32_16x16x32_bf16 v[20:23], a[136:139], a[24:27], v[20:23]\n"
  "ds_read_b64_tr_b16 a[122:123], v220 offset:13392\n"
  "v_fma_f32 v4, v4, s73, v223\n"
  "v_mfma_f32_16x16x32_bf16 v[36:39], a[136:139], a[48:51], v[248:251]\n"
  "v_exp_f32 v4, v4\n"
  "v_mfma_f32_16x16x32_bf16 v[36:39], a[140:143], a[52:55], v[36:39]\n"
  "v_fma_f32 v5, v5, s73, v223\n"
  "v_fma_f32 v6, v6, s73, v223\n"
  "v_mfma_f32_16x16x32_bf16 v[36:39], a[144:147], a[56:59], v[36:39]\n"
  "v_exp_f32 v5, v5\n"
  "v_mfma_f32_16x16x32_bf16 v[36:39], a[148:151], a[60:63], v[36:39]\n"
  "v_exp_f32 v6, v6\n"
  "v_mfma_f32_16x16x32_bf16 v[36:39], a[132:135], a[64:67], v[36:39]\n"
  "v_fma_f32 v20, v20, s73, v224\n"
  "v_fma_f32 v21, v21, s73, v224\n"
  "v_mfma_f32_16x16x32_bf16 v[36:39], a[128:131], a[68:71], v[36:39]\n"
  "v_exp_f32 v20, v20\n"
  "v_mfma_f32_16x16x32_bf16 v[52:55], a[128:131], a[92:95], v[248:251]\n"
  "ds_read_b64_tr_b16 a[128:129], v217 offset:14400\n"
  "v_fma_f32 v22, v22, s73, v224\n"
  "v_mfma_f32_16x16x32_bf16 v[52:55], a[132:135], a[88:91], v[52:55]\n"
  "ds_read_b64_tr_b16 a[130:131], v218 offset:14400\n"
  "v_fma_f32 v23, v23, s73, v224\n"
  "v_mfma_f32_16x16x32_bf16 v[52:55], a[148:151], a[84:87], v[52:55]\n"
  "ds_read_b64_tr_b16 a[132:133], v217 offset:14416\n"
  "v_fma_f32 v36, v36, s73, v225\n"
  "v_mfma_f32_16x16x32_bf16 v[52:55], a[144:147], a[80:83], v[52:55]\n"
  "ds_read_b64_tr_b16 a[134:135], v218 offset:14416\n"
  "v_fma_f32 v37, v37, s73, v225\n"
  "v_mfma_f32_16x16x32_bf16 v[52:55], a[140:143], a[76:79], v[52:55]\n"
  "v_exp_f32 v36, v36\n"
  "v_mfma_f32_16x16x32_bf16 v[52:55], a[136:139], a[72:75], v[52:55]\n"
  "s_waitcnt lgkmcnt(4)\n"
  "v_exp_f32 v37, v37\n"
  "v_mfma_f32_16x16x32_bf16 v[100:103], v[12:15], a[124:127], v[100:103]\n"
  "v_cvt_pk_bf16_f32 v12, v0, v1\n"
  "v_cvt_pk_bf16_f32 v13, v2, v3\n"
  "v_mfma_f32_16x16x32_bf16 v[100:103], v[8:11], a[120:123], v[100:103]\n"
  "v_exp_f32 v21, v21\n"
  "v_mfma_f32_16x16x32_bf16 v[132:135], v[28:31], a[124:127], v[132:135]\n"
  "ds_read_b64_tr_b16 a[140:141], v219 offset:14400\n"
  "v_cvt_pk_bf16_f32 v28, v16, v17\n"
  "v_mfma_f32_16x16x32_bf16 v[132:135], v[24:27], a[120:123], v[132:135]\n"
  "ds_read_b64_tr_b16 a[142:143], v220 offset:14400\n"
  "v_cvt_pk_bf16_f32 v29, v18, v19\n"
  "v_mfma_f32_16x16x32_bf16 v[164:167], v[44:47], a[124:127], v[164:167]\n"
  "ds_read_b64_tr_b16 a[136:137], v219 offset:14416\n"
  "v_cvt_pk_bf16_f32 v44, v32, v33\n"
  "v_mfma_f32_16x16x32_bf16 v[164:167], v[40:43], a[120:123], v[164:167]\n"
  "ds_read_b64_tr_b16 a[138:139], v220 offset:14416\n"
  "v_cvt_pk_bf16_f32 v45, v34, v35\n"
  "v_mfma_f32_16x16x32_bf16 v[196:199], v[60:63], a[124:127], v[196:199]\n"
  "v_exp_f32 v22, v22\n"
  "v_mfma_f32_16x16x32_bf16 v[196:199], v[56:59], a[120:123], v[196:199]\n"
  "s_waitcnt lgkmcnt(4)\n"
  "v_exp_f32 v23, v23\n"
  "v_mfma_f32_16x16x32_bf16 a[244:247], a[156:159], a[128:131], a[244:247]\n"
  "v_cvt_pk_bf16_f32 v60, v48, v49\n"
  "v_cvt_pk_bf16_f32 v61, v50, v51\n"
  "v_mfma_f32_16x16x32_bf16 a[248:251], a[156:159], a[132:135], a[248:251]\n"
  "v_fma_f32 v52, v52, s73, v226\n"
  "v_fma_f32 v53, v53, s73, v226\n"
  "v_mfma_f32_16x16x32_bf16 v[200:203], v[56:59], a[128:131], v[200:203]\n"
  "v_exp_f32 v52, v52\n"
  "v_mfma_f32_16x16x32_bf16 v[204:207], v[56:59], a[132:135], v[204:207]\n"
  "v_exp_f32 v53, v53\n"
  "v_mfma_f32_16x16x32_bf16 v[168:171], v[40:43], a[128:131], v[168:171]\n"
  "v_fma_f32 v54, v54, s73, v226\n"
  "v_fma_f32 v55, v55, s73, v226\n"
  "v_mfma_f32_16x16x32_bf16 v[172:175], v[40:43], a[132:135], v[172:175]\n"
  "s_waitcnt vmcnt(12) lgkmcnt(0)\n"
  "s_barrier\n"
  "ds_read_b128 a[124:127], v216 offset:24704\n"
  "v_mfma_f32_16x16x32_bf16 v[136:139], v[24:27], a[128:131], v[136:139]\n"
  "ds_read_b64 a[160:161], v231 offset:2048\n"
  "v_cvt_pk_bf16_f32 v62, v52, v53\n"
  "v_mfma_f32_16x16x32_bf16 v[140:143], v[24:27], a[132:135], v[140:143]\n"
  "ds_read_b64 a[162:163], v232 offset:2048\n"
  "v_fma_f32 v38, v38, s73, v225\n"
  "v_mfma_f32_16x16x32_bf16 v[104:107], v[8:11], a[128:131], v[104:107]\n"
  "ds_read_b128 a[120:123], v216 offset:24768\n"
  "v_fma_f32 v39, v39, s73, v225\n"
  "v_mfma_f32_16x16x32_bf16 v[108:111], v[8:11], a[132:135], v[108:111]\n"
  "ds_read_b128 a[128:131], v216 offset:25728\n"
  "v_cvt_pk_bf16_f32 v46, v36, v37\n"
  "v_mfma_f32_16x16x32_bf16 v[112:115], v[8:11], a[140:143], v[112:115]\n"
  "ds_read_b128 a[132:135], v216 offset:25792\n"
  "v_cvt_pk_bf16_f32 v30, v20, v21\n"
  "v_mfma_f32_16x16x32_bf16 v[116:119], v[8:11], a[136:139], v[116:119]\n"
  "s_mov_b32 m0, s67\n"
  "ds_read_b128 a[148:151], v216 offset:26752\n"
  "v_cvt_pk_bf16_f32 v31, v22, v23\n"
  "v_mfma_f32_16x16x32_bf16 v[144:147], v[24:27], a[140:143], v[144:147]\n"
  "ds_read_b128 a[144:147], v216 offset:26816\n"
  "v_fma_f32 v7, v7, s73, v223\n"
  "v_mfma_f32_16x16x32_bf16 v[148:151], v[24:27], a[136:139], v[148:151]\n"
  "v_exp_f32 v54, v54\n"
  "v_mfma_f32_16x16x32_bf16 v[176:179], v[40:43], a[140:143], v[176:179]\n"
  "v_exp_f32 v55, v55\n"
  "v_mfma_f32_16x16x32_bf16 v[180:183], v[40:43], a[136:139], v[180:183]\n"
  "buffer_load_dwordx4 v221, s[40:43], 0 offen offset:0 lds\n"
  "v_mfma_f32_16x16x32_bf16 v[208:211], v[56:59], a[140:143], v[208:211]\n"
  "s_addk_i32 m0, 896\n"
  "v_exp_f32 v38, v38\n"
  "v_mfma_f32_16x16x32_bf16 v[212:215], v[56:59], a[136:139], v[212:215]\n"
  "s_waitcnt lgkmcnt(4)\n"
  "v_exp_f32 v39, v39\n"
  "v_mfma_f32_16x16x32_bf16 v[64:67], a[124:127], a[96:99], v[248:251]\n"
  "v_exp_f32 v7, v7\n"
  "v_mfma_f32_16x16x32_bf16 v[64:67], a[120:123], a[100:103], v[64:67]\n"
  "s_waitcnt lgkmcnt(2)\n"
  "buffer_load_dwordx4 v221, s[40:43], 0 offen offset:128 lds\n"
  "v_cvt_pk_bf16_f32 v63, v54, v55\n"
  "v_mfma_f32_16x16x32_bf16 v[64:67], a[128:131], a[104:107], v[64:67]\n"
  "s_addk_i32 m0, 896\n"
  "v_cvt_pk_bf16_f32 v47, v38, v39\n"
  "v_cvt_pk_bf16_f32 v14, v4, v5\n"
  "v_mfma_f32_16x16x32_bf16 v[64:67], a[132:135], a[108:111], v[64:67]\n"
  "s_waitcnt lgkmcnt(0)\n"
  "v_cvt_pk_bf16_f32 v15, v6, v7\n"
  "v_add_f32 v252, v252, v4\n"
  "v_mfma_f32_16x16x32_bf16 v[64:67], a[148:151], a[112:115], v[64:67]\n"
  "v_add_f32 v252, v252, v5\n"
  "v_add_f32 v252, v252, v6\n"
  "v_mfma_f32_16x16x32_bf16 v[64:67], a[144:147], a[116:119], v[64:67]\n"
  "buffer_load_dwordx4 v221, s[40:43], 0 offen offset:256 lds\n"
  "v_add_f32 v252, v252, v7\n"
  "v_mfma_f32_16x16x32_bf16 v[0:3], a[144:147], a[20:23], v[248:251]\n"
  "s_add_u32 s40, s40, s31\n"
  "v_add_f32 v253, v253, v20\n"
  "v_add_f32 v253, v253, v21\n"
  "v_mfma_f32_16x16x32_bf16 v[0:3], a[148:151], a[16:19], v[0:3]\n"
  "s_addc_u32 s41, s41, 0\n"
  "v_add_f32 v253, v253, v22\n"
  "v_add_f32 v253, v253, v23\n"
  "v_mfma_f32_16x16x32_bf16 v[0:3], a[132:135], a[12:15], v[0:3]\n"
  "v_add_f32 v254, v254, v36\n"
  "v_add_f32 v254, v254, v37\n"
  "v_mfma_f32_16x16x32_bf16 v[0:3], a[128:131], a[8:11], v[0:3]\n"
  "s_mov_b32 m0, s59\n"
  "v_add_f32 v254, v254, v38\n"
  "v_add_f32 v254, v254, v39\n"
  "v_mfma_f32_16x16x32_bf16 v[0:3], a[120:123], a[4:7], v[0:3]\n"
  "v_add_f32 v255, v255, v52\n"
  "v_add_f32 v255, v255, v53\n"
  "v_mfma_f32_16x16x32_bf16 v[0:3], a[124:127], a[0:3], v[0:3]\n"
  "v_add_f32 v255, v255, v54\n"
  "v_add_f32 v255, v255, v55\n"
  "v_mfma_f32_16x16x32_bf16 v[16:19], a[124:127], a[24:27], v[248:251]\n"
  "ds_read_b64_tr_b16 v[244:245], v230 offset:11200\n"
  "ds_read_b64_tr_b16 v[246:247], v230 offset:11232\n"
  "v_mfma_f32_16x16x32_bf16 v[16:19], a[120:123], a[28:31], v[16:19]\n"
  "buffer_load_dwordx4 v221, s[32:35], 0 offen offset:0 lds\n"
  "ds_read_b64_tr_b16 a[252:253], v230 offset:12320\n"
  "v_mfma_f32_16x16x32_bf16 v[16:19], a[128:131], a[32:35], v[16:19]\n"
  "s_addk_i32 m0, 1024\n"
  "ds_read_b64_tr_b16 a[140:141], v217 offset:24704\n"
  "ds_read_b64_tr_b16 a[254:255], v230 offset:12352\n"
  "v_mfma_f32_16x16x32_bf16 v[16:19], a[132:135], a[36:39], v[16:19]\n"
  "s_mov_b64 s[32:33], s[44:45]\n"
  "ds_read_b64_tr_b16 a[142:143], v218 offset:24704\n"
  "v_fma_f32 v64, v64, s73, v227\n"
  "v_mfma_f32_16x16x32_bf16 v[16:19], a[148:151], a[40:43], v[16:19]\n"
  "ds_read_b64_tr_b16 a[136:137], v217 offset:25728\n"
  "v_fma_f32 v65, v65, s73, v227\n"
  "v_mfma_f32_16x16x32_bf16 v[16:19], a[144:147], a[44:47], v[16:19]\n"
  "buffer_load_dwordx4 v221, s[44:47], 0 offen offset:0 lds\n"
  "v_fma_f32 v66, v66, s73, v227\n"
  "v_mfma_f32_16x16x32_bf16 v[32:35], a[144:147], a[68:71], v[248:251]\n"
  "s_addk_i32 m0, 768\n"
  "ds_read_b64_tr_b16 a[138:139], v218 offset:25728\n"
  "v_fma_f32 v67, v67, s73, v227\n"
  "v_mfma_f32_16x16x32_bf16 v[32:35], a[148:151], a[64:67], v[32:35]\n"
  "v_exp_f32 v64, v64\n"
  "v_mfma_f32_16x16x32_bf16 v[32:35], a[132:135], a[60:63], v[32:35]\n"
  "v_exp_f32 v65, v65\n"
  "v_mfma_f32_16x16x32_bf16 v[32:35], a[128:131], a[56:59], v[32:35]\n"
  "buffer_load_dwordx4 v221, s[44:47], 0 offen offset:256 lds\n"
  "v_cvt_pk_bf16_f32 v64, v64, v65\n"
  "v_mfma_f32_16x16x32_bf16 v[32:35], a[120:123], a[52:55], v[32:35]\n"
  "s_add_u32 s44, s44, s31\n"
  "v_exp_f32 v66, v66\n"
  "v_mfma_f32_16x16x32_bf16 v[32:35], a[124:127], a[48:51], v[32:35]\n"
  "s_addc_u32 s45, s45, 0\n"
  "v_exp_f32 v67, v67\n"
  "v_mfma_f32_16x16x32_bf16 v[48:51], a[124:127], a[72:75], v[248:251]\n"
  "v_cvt_pk_bf16_f32 v65, v66, v67\n"
  "v_fma_f32 v0, v0, s73, v223\n"
  "v_mfma_f32_16x16x32_bf16 v[48:51], a[120:123], a[76:79], v[48:51]\n"
  "v_exp_f32 v0, v0\n"
  "v_mfma_f32_16x16x32_bf16 v[48:51], a[128:131], a[80:83], v[48:51]\n"
  "ds_read_b64_tr_b16 a[124:125], v217 offset:24720\n"
  "v_fma_f32 v1, v1, s73, v223\n"
  "v_mfma_f32_16x16x32_bf16 v[48:51], a[132:135], a[84:87], v[48:51]\n"
  "ds_read_b64_tr_b16 a[126:127], v218 offset:24720\n"
  "v_fma_f32 v2, v2, s73, v223\n"
  "v_mfma_f32_16x16x32_bf16 v[48:51], a[148:151], a[88:91], v[48:51]\n"
  "ds_read_b64_tr_b16 a[120:121], v217 offset:25744\n"
  "v_fma_f32 v3, v3, s73, v223\n"
  "v_mfma_f32_16x16x32_bf16 v[48:51], a[144:147], a[92:95], v[48:51]\n"
  "s_waitcnt lgkmcnt(3)\n"
  "ds_read_b64_tr_b16 a[122:123], v218 offset:25744\n"
  "v_add_f32 v252, v252, v0\n"
  "v_mfma_f32_16x16x32_bf16 a[228:231], a[156:159], a[140:143], a[228:231]\n"
  "v_exp_f32 v1, v1\n"
  "v_mfma_f32_16x16x32_bf16 a[228:231], a[160:163], a[136:139], a[228:231]\n"
  "v_exp_f32 v2, v2\n"
  "v_mfma_f32_16x16x32_bf16 a[212:215], v[56:59], a[140:143], a[212:215]\n"
  "v_exp_f32 v3, v3\n"
  "v_mfma_f32_16x16x32_bf16 a[212:215], v[60:63], a[136:139], a[212:215]\n"
  "v_add_f32 v252, v252, v1\n"
  "v_add_f32 v252, v252, v2\n"
  "v_mfma_f32_16x16x32_bf16 a[196:199], v[40:43], a[140:143], a[196:199]\n"
  "v_add_f32 v252, v252, v3\n"
  "v_fma_f32 v16, v16, s73, v224\n"
  "v_mfma_f32_16x16x32_bf16 a[196:199], v[44:47], a[136:139], a[196:199]\n"
  "v_exp_f32 v16, v16\n"
  "v_mfma_f32_16x16x32_bf16 a[180:183], v[24:27], a[140:143], a[180:183]\n"
  "ds_read_b64_tr_b16 a[128:129], v219 offset:24704\n"
  "v_fma_f32 v17, v17, s73, v224\n"
  "v_mfma_f32_16x16x32_bf16 a[180:183], v[28:31], a[136:139], a[180:183]\n"
  "ds_read_b64_tr_b16 a[130:131], v220 offset:24704\n"
  "v_fma_f32 v18, v18, s73, v224\n"
  "v_mfma_f32_16x16x32_bf16 a[164:167], v[8:11], a[140:143], a[164:167]\n"
  "ds_read_b64_tr_b16 a[132:133], v219 offset:25728\n"
  "v_fma_f32 v19, v19, s73, v224\n"
  "v_mfma_f32_16x16x32_bf16 a[164:167], v[12:15], a[136:139], a[164:167]\n"
  "ds_read_b64_tr_b16 a[134:135], v220 offset:25728\n"
  "v_add_f32 v253, v253, v16\n"
  "v_mfma_f32_16x16x32_bf16 v[84:87], a[156:159], a[152:155], v[84:87]\n"
  "v_exp_f32 v17, v17\n"
  "v_mfma_f32_16x16x32_bf16 v[84:87], a[160:163], a[152:155], v[84:87]\n"
  "s_waitcnt lgkmcnt(4)\n"
  "v_exp_f32 v18, v18\n"
  "v_mfma_f32_16x16x32_bf16 a[168:171], v[8:11], a[124:127], a[168:171]\n"
  "v_exp_f32 v19, v19\n"
  "v_mfma_f32_16x16x32_bf16 a[168:171], v[12:15], a[120:123], a[168:171]\n"
  "v_add_f32 v253, v253, v17\n"
  "v_add_f32 v253, v253, v18\n"
  "v_mfma_f32_16x16x32_bf16 a[184:187], v[24:27], a[124:127], a[184:187]\n"
  "ds_read_b128 a[140:143], v235 offset:24704\n"
  "v_add_f32 v253, v253, v19\n"
  "v_mfma_f32_16x16x32_bf16 a[184:187], v[28:31], a[120:123], a[184:187]\n"
  "ds_read_b128 a[136:139], v235 offset:24768\n"
  "v_fma_f32 v32, v32, s73, v225\n"
  "v_mfma_f32_16x16x32_bf16 a[200:203], v[40:43], a[124:127], a[200:203]\n"
  "ds_read_b128 a[148:151], v235 offset:25728\n"
  "v_fma_f32 v33, v33, s73, v225\n"
  "v_mfma_f32_16x16x32_bf16 a[200:203], v[44:47], a[120:123], a[200:203]\n"
  "ds_read_b128 a[144:147], v235 offset:25792\n"
  "v_fma_f32 v34, v34, s73, v225\n"
  "v_mfma_f32_16x16x32_bf16 a[216:219], v[56:59], a[124:127], a[216:219]\n"
  "v_exp_f32 v32, v32\n"
  "v_mfma_f32_16x16x32_bf16 a[216:219], v[60:63], a[120:123], a[216:219]\n"
  "v_exp_f32 v33, v33\n"
  "v_mfma_f32_16x16x32_bf16 a[232:235], a[156:159], a[124:127], a[232:235]\n"
  "ds_read_b128 a[124:127], v235 offset:26752\n"
  "v_fma_f32 v35, v35, s73, v225\n"
  "v_mfma_f32_16x16x32_bf16 a[232:235], a[160:163], a[120:123], a[232:235]\n"
  "s_waitcnt lgkmcnt(5)\n"
  "ds_read_b128 a[120:123], v235 offset:26816\n"
  "v_add_f32 v254, v254, v32\n"
  "v_mfma_f32_16x16x32_bf16 a[220:223], v[56:59], a[128:131], a[220:223]\n"
  "v_exp_f32_e64 v34, v34\n"
  "v_mfma_f32_16x16x32_bf16 a[220:223], v[60:63], a[132:135], a[220:223]\n"
  "ds_write_b64 v231, v[64:65] offset:0\n"
  "v_mfma_f32_16x16x32_bf16 a[204:207], v[40:43], a[128:131], a[204:207]\n"
  "v_exp_f32 v35, v35\n"
  "v_mfma_f32_16x16x32_bf16 a[204:207], v[44:47], a[132:135], a[204:207]\n"
  "buffer_store_dwordx4 v[244:247], v228, s[52:55], s81 offen offset:128 nt\n"
  "v_add_f32 v254, v254, v33\n"
  "v_add_f32 v254, v254, v34\n"
  "v_add_f32 v254, v254, v35\n"
  "v_mfma_f32_16x16x32_bf16 a[188:191], v[24:27], a[128:131], a[188:191]\n"
  "ds_read_b64_tr_b16 v[244:245], v230 offset:13440\n"
  "ds_read_b64_tr_b16 v[246:247], v230 offset:13472\n"
  "v_mfma_f32_16x16x32_bf16 a[188:191], v[28:31], a[132:135], a[188:191]\n"
  "v_fma_f32 v48, v48, s73, v226\n"
  "v_fma_f32 v49, v49, s73, v226\n"
  "v_mfma_f32_16x16x32_bf16 a[172:175], v[8:11], a[128:131], a[172:175]\n"
  "v_exp_f32 v48, v48\n"
  "v_mfma_f32_16x16x32_bf16 a[172:175], v[12:15], a[132:135], a[172:175]\n"
  "s_waitcnt lgkmcnt(7)\n"
  "v_exp_f32 v49, v49\n"
  "v_mfma_f32_16x16x32_bf16 v[4:7], a[140:143], a[0:3], v[248:251]\n"
  "v_fma_f32 v50, v50, s73, v226\n"
  "v_fma_f32 v51, v51, s73, v226\n"
  "v_mfma_f32_16x16x32_bf16 v[4:7], a[136:139], a[4:7], v[4:7]\n"
  "s_waitcnt lgkmcnt(5)\n"
  "v_exp_f32 v50, v50\n"
  "v_mfma_f32_16x16x32_bf16 v[4:7], a[148:151], a[8:11], v[4:7]\n"
  "v_exp_f32 v51, v51\n"
  "v_mfma_f32_16x16x32_bf16 v[4:7], a[144:147], a[12:15], v[4:7]\n"
  "s_waitcnt lgkmcnt(3)\n"
  "v_add_f32 v255, v255, v48\n"
  "v_add_f32 v255, v255, v49\n"
  "v_mfma_f32_16x16x32_bf16 v[4:7], a[124:127], a[16:19], v[4:7]\n"
  "v_add_f32 v255, v255, v50\n"
  "v_add_f32_e64 v255, v255, v51\n"
  "v_mfma_f32_16x16x32_bf16 v[4:7], a[120:123], a[20:23], v[4:7]\n"
  "buffer_store_dwordx4 a[252:255], v228, s[52:55], s81 offen offset:3200 nt\n"
  "v_and_b32 v236, 0x7fff7fff, v236\n"
  "v_lshlrev_b32 v237, 16, v236\n"
  "v_max_u32 v236, v236, v237\n"
  "v_mfma_f32_16x16x32_bf16 v[20:23], a[120:123], a[44:47], v[248:251]\n"
  "ds_read_b64_tr_b16 a[252:253], v230 offset:14560\n"
  "ds_read_b64_tr_b16 a[254:255], v230 offset:14592\n"
  "v_mfma_f32_16x16x32_bf16 v[20:23], a[124:127], a[40:43], v[20:23]\n"
  "v_max_u32_dpp v236, v236, v236 quad_perm:[1,0,3,2] row_mask:0xf bank_mask:0xf\n"
  "v_mfma_f32_16x16x32_bf16 v[20:23], a[144:147], a[36:39], v[20:23]\n"
  "ds_read_b64_tr_b16 a[128:129], v219 offset:24720\n"
  "v_fma_f32 v4, v4, s73, v223\n"
  "v_mfma_f32_16x16x32_bf16 v[20:23], a[148:151], a[32:35], v[20:23]\n"
  "ds_read_b64_tr_b16 a[130:131], v220 offset:24720\n"
  "v_fma_f32 v5, v5, s73, v223\n"
  "v_mfma_f32_16x16x32_bf16 v[20:23], a[136:139], a[28:31], v[20:23]\n"
  "ds_read_b64_tr_b16 a[132:133], v219 offset:25744\n"
  "v_fma_f32 v6, v6, s73, v223\n"
  "v_mfma_f32_16x16x32_bf16 v[20:23], a[140:143], a[24:27], v[20:23]\n"
  "s_waitcnt lgkmcnt(5)\n"
  "buffer_store_dwordx4 v[244:247], v228, s[52:55], s82 offen offset:128 nt\n"
  "ds_read_b64_tr_b16 a[134:135], v220 offset:25744\n"
  "v_exp_f32 v4, v4\n"
  "v_mfma_f32_16x16x32_bf16 v[36:39], a[140:143], a[48:51], v[248:251]\n"
  "v_exp_f32 v5, v5\n"
  "v_mfma_f32_16x16x32_bf16 v[36:39], a[136:139], a[52:55], v[36:39]\n"
  "v_exp_f32 v6, v6\n"
  "v_mfma_f32_16x16x32_bf16 v[36:39], a[148:151], a[56:59], v[36:39]\n"
  "v_fma_f32 v20, v20, s73, v224\n"
  "v_fma_f32 v21, v21, s73, v224\n"
  "v_mfma_f32_16x16x32_bf16 v[36:39], a[144:147], a[60:63], v[36:39]\n"
  "v_exp_f32 v20, v20\n"
  "v_mfma_f32_16x16x32_bf16 v[36:39], a[124:127], a[64:67], v[36:39]\n"
  "v_exp_f32 v21, v21\n"
  "v_mfma_f32_16x16x32_bf16 v[36:39], a[120:123], a[68:71], v[36:39]\n"
  "v_fma_f32 v22, v22, s73, v224\n"
  "v_fma_f32 v23, v23, s73, v224\n"
  "v_mfma_f32_16x16x32_bf16 v[52:55], a[120:123], a[92:95], v[248:251]\n"
  "ds_read_b64_tr_b16 a[120:121], v217 offset:26752\n"
  "v_fma_f32 v7, v7, s73, v223\n"
  "v_mfma_f32_16x16x32_bf16 v[52:55], a[124:127], a[88:91], v[52:55]\n"
  "ds_read_b64_tr_b16 a[122:123], v218 offset:26752\n"
  "v_add_f32 v252, v252, v4\n"
  "v_mfma_f32_16x16x32_bf16 v[52:55], a[144:147], a[84:87], v[52:55]\n"
  "ds_read_b64_tr_b16 a[124:125], v217 offset:26768\n"
  "v_fma_f32 v36, v36, s73, v225\n"
  "v_mfma_f32_16x16x32_bf16 v[52:55], a[148:151], a[80:83], v[52:55]\n"
  "ds_read_b64_tr_b16 a[126:127], v218 offset:26768\n"
  "v_fma_f32 v37, v37, s73, v225\n"
  "v_mfma_f32_16x16x32_bf16 v[52:55], a[136:139], a[76:79], v[52:55]\n"
  "v_exp_f32 v36, v36\n"
  "v_mfma_f32_16x16x32_bf16 v[52:55], a[140:143], a[72:75], v[52:55]\n"
  "s_waitcnt lgkmcnt(4)\n"
  "v_exp_f32 v37, v37\n"
  "v_mfma_f32_16x16x32_bf16 a[176:179], v[8:11], a[128:131], a[176:179]\n"
  "v_cvt_pk_bf16_f32 v8, v0, v1\n"
  "v_cvt_pk_bf16_f32 v9, v2, v3\n"
  "v_mfma_f32_16x16x32_bf16 a[176:179], v[12:15], a[132:135], a[176:179]\n"
  "v_exp_f32 v22, v22\n"
  "v_mfma_f32_16x16x32_bf16 a[192:195], v[24:27], a[128:131], a[192:195]\n"
  "ds_read_b64_tr_b16 a[136:137], v219 offset:26752\n"
  "v_cvt_pk_bf16_f32 v24, v16, v17\n"
  "v_mfma_f32_16x16x32_bf16 a[192:195], v[28:31], a[132:135], a[192:195]\n"
  "ds_read_b64_tr_b16 a[138:139], v220 offset:26752\n"
  "v_cvt_pk_bf16_f32 v25, v18, v19\n"
  "v_mfma_f32_16x16x32_bf16 a[208:211], v[40:43], a[128:131], a[208:211]\n"
  "ds_read_b64_tr_b16 a[140:141], v219 offset:26768\n"
  "v_cvt_pk_bf16_f32 v40, v32, v33\n"
  "v_mfma_f32_16x16x32_bf16 a[208:211], v[44:47], a[132:135], a[208:211]\n"
  "ds_read_b64_tr_b16 a[142:143], v220 offset:26768\n"
  "v_cvt_pk_bf16_f32 v41, v34, v35\n"
  "v_mfma_f32_16x16x32_bf16 a[224:227], v[56:59], a[128:131], a[224:227]\n"
  "v_exp_f32 v23, v23\n"
  "v_mfma_f32_16x16x32_bf16 a[224:227], v[60:63], a[132:135], a[224:227]\n"
  "s_waitcnt lgkmcnt(4)\n"
  "v_exp_f32 v7, v7\n"
  "v_mfma_f32_16x16x32_bf16 a[244:247], a[160:163], a[120:123], a[244:247]\n"
  "v_cvt_pk_bf16_f32 v56, v48, v49\n"
  "v_cvt_pk_bf16_f32 v57, v50, v51\n"
  "v_mfma_f32_16x16x32_bf16 a[248:251], a[160:163], a[124:127], a[248:251]\n"
  "v_fma_f32 v52, v52, s73, v226\n"
  "v_fma_f32 v53, v53, s73, v226\n"
  "v_mfma_f32_16x16x32_bf16 v[200:203], v[60:63], a[120:123], v[200:203]\n"
  "v_exp_f32 v52, v52\n"
  "v_mfma_f32_16x16x32_bf16 v[204:207], v[60:63], a[124:127], v[204:207]\n"
  "v_exp_f32 v53, v53\n"
  "v_mfma_f32_16x16x32_bf16 v[168:171], v[44:47], a[120:123], v[168:171]\n"
  "v_fma_f32 v54, v54, s73, v226\n"
  "v_fma_f32 v55, v55, s73, v226\n"
  "v_mfma_f32_16x16x32_bf16 v[172:175], v[44:47], a[124:127], v[172:175]\n"
  "s_waitcnt vmcnt(12) lgkmcnt(0)\n"
  "s_barrier\n"
  "ds_read_b128 a[128:131], v216 offset:0\n"
  "v_mfma_f32_16x16x32_bf16 v[136:139], v[28:31], a[120:123], v[136:139]\n"
  "ds_read_b64 a[156:157], v231 offset:0\n"
  "v_cvt_pk_bf16_f32 v58, v52, v53\n"
  "v_mfma_f32_16x16x32_bf16 v[140:143], v[28:31], a[124:127], v[140:143]\n"
  "ds_read_b64 a[158:159], v232 offset:0\n"
  "v_fma_f32 v38, v38, s73, v225\n"
  "v_mfma_f32_16x16x32_bf16 v[104:107], v[12:15], a[120:123], v[104:107]\n"
  "ds_read_b128 a[132:135], v216 offset:64\n"
  "v_fma_f32 v39, v39, s73, v225\n"
  "v_mfma_f32_16x16x32_bf16 v[108:111], v[12:15], a[124:127], v[108:111]\n"
  "ds_read_b128 a[120:123], v216 offset:1024\n"
  "v_cvt_pk_bf16_f32 v42, v36, v37\n"
  "v_mfma_f32_16x16x32_bf16 v[112:115], v[12:15], a[136:139], v[112:115]\n"
  "ds_read_b128 a[124:127], v216 offset:1088\n"
  "v_cvt_pk_bf16_f32 v26, v20, v21\n"
  "v_mfma_f32_16x16x32_bf16 v[116:119], v[12:15], a[140:143], v[116:119]\n"
  "s_mov_b32 m0, s68\n"
  "ds_read_b128 a[144:147], v216 offset:2048\n"
  "v_cvt_pk_bf16_f32 v27, v22, v23\n"
  "v_mfma_f32_16x16x32_bf16 v[144:147], v[28:31], a[136:139], v[144:147]\n"
  "ds_read_b128 a[148:151], v216 offset:2112\n"
  "v_cvt_pk_bf16_f32 v10, v4, v5\n"
  "v_mfma_f32_16x16x32_bf16 v[148:151], v[28:31], a[140:143], v[148:151]\n"
  "v_exp_f32 v54, v54\n"
  "v_mfma_f32_16x16x32_bf16 v[176:179], v[44:47], a[136:139], v[176:179]\n"
  "v_exp_f32 v55, v55\n"
  "v_mfma_f32_16x16x32_bf16 v[180:183], v[44:47], a[140:143], v[180:183]\n"
  "buffer_load_dwordx4 v221, s[40:43], 0 offen offset:0 lds\n"
  "v_mfma_f32_16x16x32_bf16 v[208:211], v[60:63], a[136:139], v[208:211]\n"
  "s_addk_i32 m0, 896\n"
  "v_exp_f32 v38, v38\n"
  "v_mfma_f32_16x16x32_bf16 v[212:215], v[60:63], a[140:143], v[212:215]\n"
  "s_waitcnt lgkmcnt(4)\n"
  "v_exp_f32 v39, v39\n"
  "v_mfma_f32_16x16x32_bf16 v[64:67], a[128:131], a[96:99], v[248:251]\n"
  "v_cvt_pk_bf16_f32 v59, v54, v55\n"
  "v_cvt_pk_bf16_f32 v43, v38, v39\n"
  "v_mfma_f32_16x16x32_bf16 v[64:67], a[132:135], a[100:103], v[64:67]\n"
  "s_waitcnt lgkmcnt(2)\n"
  "buffer_load_dwordx4 v221, s[40:43], 0 offen offset:128 lds\n"
  "v_cvt_pk_bf16_f32 v11, v6, v7\n"
  "v_mfma_f32_16x16x32_bf16 v[64:67], a[120:123], a[104:107], v[64:67]\n"
  "s_addk_i32 m0, 896\n"
  "v_add_f32 v252, v252, v5\n"
  "v_add_f32 v252, v252, v6\n"
  "v_mfma_f32_16x16x32_bf16 v[64:67], a[124:127], a[108:111], v[64:67]\n"
  "s_waitcnt lgkmcnt(0)\n"
  "v_add_f32 v252, v252, v7\n"
  "v_add_f32 v253, v253, v20\n"
  "v_mfma_f32_16x16x32_bf16 v[64:67], a[144:147], a[112:115], v[64:67]\n"
  "v_add_f32 v253, v253, v21\n"
  "v_add_f32 v253, v253, v22\n"
  "v_mfma_f32_16x16x32_bf16 v[64:67], a[148:151], a[116:119], v[64:67]\n"
  "buffer_load_dwordx4 v221, s[40:43], 0 offen offset:256 lds\n"
  "v_add_f32 v253, v253, v23\n"
  "v_mfma_f32_16x16x32_bf16 v[0:3], a[148:151], a[20:23], v[248:251]\n"
  "s_add_u32 s40, s40, s31\n"
  "v_add_f32 v254, v254, v36\n"
  "v_add_f32 v254, v254, v37\n"
  "v_mfma_f32_16x16x32_bf16 v[0:3], a[144:147], a[16:19], v[0:3]\n"
  "s_addc_u32 s41, s41, 0\n"
  "v_add_f32 v254, v254, v38\n"
  "v_add_f32 v254, v254, v39\n"
  "v_mfma_f32_16x16x32_bf16 v[0:3], a[124:127], a[12:15], v[0:3]\n"
  "v_add_f32 v255, v255, v52\n"
  "v_add_f32 v255, v255, v53\n"
  "v_mfma_f32_16x16x32_bf16 v[0:3], a[120:123], a[8:11], v[0:3]\n"
  "s_add_u32 m0, s60, -128\n"
  "v_add_f32 v255, v255, v54\n"
  "v_add_f32 v255, v255, v55\n"
  "v_mfma_f32_16x16x32_bf16 v[0:3], a[132:135], a[4:7], v[0:3]\n"
  "ds_read_b64_tr_b16 v[244:245], v230 offset:15680\n"
  "ds_read_b64_tr_b16 v[246:247], v230 offset:15712\n"
  "v_mfma_f32_16x16x32_bf16 v[0:3], a[128:131], a[0:3], v[0:3]\n"
  "v_fma_f32 v64, v64, s73, v227\n"
  "v_fma_f32 v65, v65, s73, v227\n"
  "v_mfma_f32_16x16x32_bf16 v[16:19], a[128:131], a[24:27], v[248:251]\n"
  "v_exp_f32 v64, v64\n"
  "v_mfma_f32_16x16x32_bf16 v[16:19], a[132:135], a[28:31], v[16:19]\n"
  "buffer_load_dwordx4 v221, s[32:35], 0 offen offset:128 lds\n"
  "v_fma_f32 v66, v66, s73, v227\n"
  "v_mfma_f32_16x16x32_bf16 v[16:19], a[120:123], a[32:35], v[16:19]\n"
  "s_addk_i32 m0, 1024\n"
  "ds_read_b64_tr_b16 a[136:137], v217 offset:0\n"
  "v_fma_f32 v67, v67, s73, v227\n"
  "v_mfma_f32_16x16x32_bf16 v[16:19], a[124:127], a[36:39], v[16:19]\n"
  "s_mov_b64 s[32:33], s[44:45]\n"
  "ds_read_b64_tr_b16 a[138:139], v218 offset:0\n"
  "v_fma_f32 v0, v0, s73, v223\n"
  "v_mfma_f32_16x16x32_bf16 v[16:19], a[144:147], a[40:43], v[16:19]\n"
  "ds_read_b64_tr_b16 a[140:141], v217 offset:1024\n"
  "v_fma_f32 v1, v1, s73, v223\n"
  "v_mfma_f32_16x16x32_bf16 v[16:19], a[148:151], a[44:47], v[16:19]\n"
  "buffer_load_dwordx4 v221, s[44:47], 0 offen offset:128 lds\n"
  "v_fma_f32 v2, v2, s73, v223\n"
  "v_mfma_f32_16x16x32_bf16 v[32:35], a[148:151], a[68:71], v[248:251]\n"
  "s_addk_i32 m0, 896\n"
  "ds_read_b64_tr_b16 a[142:143], v218 offset:1024\n"
  "v_fma_f32 v3, v3, s73, v223\n"
  "v_mfma_f32_16x16x32_bf16 v[32:35], a[144:147], a[64:67], v[32:35]\n"
  "v_exp_f32 v65, v65\n"
  "v_mfma_f32_16x16x32_bf16 v[32:35], a[124:127], a[60:63], v[32:35]\n"
  "v_exp_f32 v66, v66\n"
  "v_mfma_f32_16x16x32_bf16 v[32:35], a[120:123], a[56:59], v[32:35]\n"
  "buffer_load_dwordx4 v221, s[44:47], 0 offen offset:256 lds\n"
  "v_cvt_pk_bf16_f32 v64, v64, v65\n"
  "v_mfma_f32_16x16x32_bf16 v[32:35], a[132:135], a[52:55], v[32:35]\n"
  "s_add_u32 s44, s44, s31\n"
  "v_exp_f32 v67, v67\n"
  "v_mfma_f32_16x16x32_bf16 v[32:35], a[128:131], a[48:51], v[32:35]\n"
  "s_addc_u32 s45, s45, 0\n"
  "v_exp_f32 v0, v0\n"
  "v_mfma_f32_16x16x32_bf16 v[48:51], a[128:131], a[72:75], v[248:251]\n"
  "ds_read_b64_tr_b16 a[128:129], v217 offset:16\n"
  "v_cvt_pk_bf16_f32 v65, v66, v67\n"
  "v_mfma_f32_16x16x32_bf16 v[48:51], a[132:135], a[76:79], v[48:51]\n"
  "ds_read_b64_tr_b16 a[130:131], v218 offset:16\n"
  "v_add_f32 v252, v252, v0\n"
  "v_mfma_f32_16x16x32_bf16 v[48:51], a[120:123], a[80:83], v[48:51]\n"
  "ds_read_b64_tr_b16 a[132:133], v217 offset:1040\n"
  "v_fma_f32 v16, v16, s73, v224\n"
  "v_mfma_f32_16x16x32_bf16 v[48:51], a[124:127], a[84:87], v[48:51]\n"
  "s_add_u32 m0, s72, 11200\n"
  "ds_read_b64_tr_b16 a[134:135], v218 offset:1040\n"
  "v_mfma_f32_16x16x32_bf16 v[48:51], a[144:147], a[88:91], v[48:51]\n"
  "v_exp_f32 v1, v1\n"
  "v_mfma_f32_16x16x32_bf16 v[48:51], a[148:151], a[92:95], v[48:51]\n"
  "s_waitcnt lgkmcnt(4)\n"
  "v_exp_f32 v2, v2\n"
  "v_mfma_f32_16x16x32_bf16 a[236:239], a[160:163], a[136:139], a[236:239]\n"
  "v_exp_f32 v3, v3\n"
  "v_mfma_f32_16x16x32_bf16 a[236:239], a[156:159], a[140:143], a[236:239]\n"
  "buffer_load_dwordx4 v221, s[48:51], 0 offen offset:0 nt lds\n"
  "v_add_f32 v252, v252, v1\n"
  "v_mfma_f32_16x16x32_bf16 v[184:187], v[60:63], a[136:139], v[184:187]\n"
  "v_exp_f32 v16, v16\n"
  "v_mfma_f32_16x16x32_bf16 v[184:187], v[56:59], a[140:143], v[184:187]\n"
  "v_add_f32 v252, v252, v2\n"
  "v_add_f32 v252, v252, v3\n"
  "v_mfma_f32_16x16x32_bf16 v[152:155], v[44:47], a[136:139], v[152:155]\n"
  "ds_read_b64_tr_b16 a[120:121], v219 offset:0\n"
  "v_fma_f32 v17, v17, s73, v224\n"
  "v_mfma_f32_16x16x32_bf16 v[152:155], v[40:43], a[140:143], v[152:155]\n"
  "s_add_u32 m0, s72, 9216\n"
  "ds_read_b64_tr_b16 a[122:123], v220 offset:0\n"
  "v_add_f32 v253, v253, v16\n"
  "v_mfma_f32_16x16x32_bf16 v[120:123], v[28:31], a[136:139], v[120:123]\n"
  "ds_read_b64_tr_b16 a[124:125], v219 offset:1024\n"
  "v_fma_f32 v18, v18, s73, v224\n"
  "v_mfma_f32_16x16x32_bf16 v[120:123], v[24:27], a[140:143], v[120:123]\n"
  "ds_read_b64_tr_b16 a[126:127], v220 offset:1024\n"
  "v_fma_f32 v19, v19, s73, v224\n"
  "v_mfma_f32_16x16x32_bf16 v[88:91], v[12:15], a[136:139], v[88:91]\n"
  "v_exp_f32 v17, v17\n"
  "v_mfma_f32_16x16x32_bf16 v[88:91], v[8:11], a[140:143], v[88:91]\n"
  "s_waitcnt lgkmcnt(4)\n"
  "buffer_load_dwordx4 v221, s[48:51], 0 offen offset:3072 nt lds\n"
  "v_add_f32 v253, v253, v17\n"
  "v_mfma_f32_16x16x32_bf16 v[92:95], v[12:15], a[128:131], v[92:95]\n"
  "v_exp_f32 v18, v18\n"
  "v_mfma_f32_16x16x32_bf16 v[92:95], v[8:11], a[132:135], v[92:95]\n"
  "v_exp_f32 v19, v19\n"
  "v_mfma_f32_16x16x32_bf16 v[124:127], v[28:31], a[128:131], v[124:127]\n"
  "ds_read_b128 a[136:139], v235 offset:0\n"
  "v_add_f32 v253, v253, v18\n"
  "v_mfma_f32_16x16x32_bf16 v[124:127], v[24:27], a[132:135], v[124:127]\n"
  "ds_read_b128 a[140:143], v235 offset:64\n"
  "v_add_f32 v253, v253, v19\n"
  "v_mfma_f32_16x16x32_bf16 v[156:159], v[44:47], a[128:131], v[156:159]\n"
  "ds_read_b128 a[144:147], v235 offset:1024\n"
  "v_fma_f32 v32, v32, s73, v225\n"
  "v_mfma_f32_16x16x32_bf16 v[156:159], v[40:43], a[132:135], v[156:159]\n"
  "ds_read_b128 a[148:151], v235 offset:1088\n"
  "v_fma_f32 v33, v33, s73, v225\n"
  "v_mfma_f32_16x16x32_bf16 v[188:191], v[60:63], a[128:131], v[188:191]\n"
  "v_exp_f32 v32, v32\n"
  "v_mfma_f32_16x16x32_bf16 v[188:191], v[56:59], a[132:135], v[188:191]\n"
  "v_exp_f32 v33, v33\n"
  "v_mfma_f32_16x16x32_bf16 a[240:243], a[160:163], a[128:131], a[240:243]\n"
  "ds_read_b128 a[128:131], v235 offset:2048\n"
  "v_fma_f32 v34, v34, s73, v225\n"
  "v_mfma_f32_16x16x32_bf16 a[240:243], a[156:159], a[132:135], a[240:243]\n"
  "s_waitcnt lgkmcnt(5)\n"
  "ds_read_b128 a[132:135], v235 offset:2112\n"
  "v_fma_f32 v35, v35, s73, v225\n"
  "v_mfma_f32_16x16x32_bf16 v[192:195], v[60:63], a[120:123], v[192:195]\n"
  "v_exp_f32 v34, v34\n"
  "v_mfma_f32_16x16x32_bf16 v[192:195], v[56:59], a[124:127], v[192:195]\n"
  "ds_write_b64 v231, v[64:65] offset:2048\n"
  "v_mfma_f32_16x16x32_bf16 v[160:163], v[44:47], a[120:123], v[160:163]\n"
  "v_exp_f32 v35, v35\n"
  "v_mfma_f32_16x16x32_bf16 v[160:163], v[40:43], a[124:127], v[160:163]\n"
  "v_add_f32 v254, v254, v32\n"
  "v_add_f32 v254, v254, v33\n"
  "v_mfma_f32_16x16x32_bf16 v[128:131], v[28:31], a[120:123], v[128:131]\n"
  "v_add_f32 v254, v254, v34\n"
  "v_add_f32_e64 v254, v254, v35\n"
  "v_mfma_f32_16x16x32_bf16 v[128:131], v[24:27], a[124:127], v[128:131]\n"
  "buffer_store_dwordx4 a[252:255], v228, s[52:55], s82 offen offset:3200 nt\n"
  "v_max_u32_dpp v236, v236, v236 quad_perm:[2,3,0,1] row_mask:0xf bank_mask:0xf\n"
  "v_add_u32_e64 v236, -1, v236\n"
  "v_fma_f32 v48, v48, s73, v226\n"
  "v_mfma_f32_16x16x32_bf16 v[96:99], v[12:15], a[120:123], v[96:99]\n"
  "ds_read_b64_tr_b16 a[252:253], v230 offset:16800\n"
  "ds_read_b64_tr_b16 a[254:255], v230 offset:16832\n"
  "v_mfma_f32_16x16x32_bf16 v[96:99], v[8:11], a[124:127], v[96:99]\n"
  "s_waitcnt lgkmcnt(7)\n"
  "v_exp_f32 v48, v48\n"
  "v_mfma_f32_16x16x32_bf16 v[4:7], a[136:139], a[0:3], v[248:251]\n"
  "v_min_u32_dpp v236, v236, v236 row_shr:4 row_mask:0xf bank_mask:0xf\n"
  "v_fma_f32 v49, v49, s73, v226\n"
  "v_mfma_f32_16x16x32_bf16 v[4:7], a[140:143], a[4:7], v[4:7]\n"
  "s_waitcnt lgkmcnt(5)\n"
  "v_exp_f32 v49, v49\n"
  "v_mfma_f32_16x16x32_bf16 v[4:7], a[144:147], a[8:11], v[4:7]\n"
  "v_min_u32_dpp v236, v236, v236 row_shr:8 row_mask:0xf bank_mask:0xf\n"
  "v_fma_f32 v50, v50, s73, v226\n"
  "v_mfma_f32_16x16x32_bf16 v[4:7], a[148:151], a[12:15], v[4:7]\n"
  "s_waitcnt lgkmcnt(3)\n"
  "v_exp_f32 v50, v50\n"
  "v_mfma_f32_16x16x32_bf16 v[4:7], a[128:131], a[16:19], v[4:7]\n"
  "v_min_u32_dpp v236, v236, v236 row_bcast:15 row_mask:0xa bank_mask:0xf\n"
  "v_fma_f32 v51, v51, s73, v226\n"
  "v_mfma_f32_16x16x32_bf16 v[4:7], a[132:135], a[20:23], v[4:7]\n"
  "buffer_store_dwordx4 v[244:247], v228, s[52:55], s83 offen offset:128 nt\n"
  "v_min_u32_dpp v236, v236, v236 row_bcast:31 row_mask:0xc bank_mask:0xf\n"
  "v_exp_f32 v51, v51\n"
  "v_mfma_f32_16x16x32_bf16 v[20:23], a[132:135], a[44:47], v[248:251]\n"
  "v_readlane_b32 s65, v236, 63\n"
  "v_mfma_f32_16x16x32_bf16 v[20:23], a[128:131], a[40:43], v[20:23]\n"
  "v_add_f32 v255, v255, v48\n"
  "v_add_f32 v255, v255, v49\n"
  "v_mfma_f32_16x16x32_bf16 v[20:23], a[148:151], a[36:39], v[20:23]\n"
  "s_add_u32 s65, s65, 1\n"
  "ds_read_b64_tr_b16 a[120:121], v219 offset:16\n"
  "v_add_f32 v255, v255, v50\n"
  "v_mfma_f32_16x16x32_bf16 v[20:23], a[144:147], a[32:35], v[20:23]\n"
  "s_lshr_b32 s65, s65, 23\n"
  "ds_read_b64_tr_b16 a[122:123], v220 offset:16\n"
  "v_add_f32_e64 v255, v255, v51\n"
  "v_mfma_f32_16x16x32_bf16 v[20:23], a[140:143], a[28:31], v[20:23]\n"
  "s_sub_i32 s65, 0x77, s65\n"
  "ds_read_b64_tr_b16 a[124:125], v219 offset:1040\n"
  "v_fma_f32 v4, v4, s73, v223\n"
  "v_mfma_f32_16x16x32_bf16 v[20:23], a[136:139], a[24:27], v[20:23]\n"
  "s_and_b32 s65, s65, -4\n"
  "s_waitcnt lgkmcnt(3)\n"
  "buffer_store_dwordx4 a[252:255], v228, s[52:55], s83 offen offset:3200 nt\n"
  "ds_read_b64_tr_b16 a[126:127], v220 offset:1040\n"
  "v_exp_f32 v4, v4\n"
  "v_mfma_f32_16x16x32_bf16 v[36:39], a[136:139], a[48:51], v[248:251]\n"
  "s_max_i32 s65, s65, 0xffffffc4\n"
  "v_cvt_f32_i32 v236, s65\n"
  "v_add_f32 v252, v252, v4\n"
  "v_mfma_f32_16x16x32_bf16 v[36:39], a[140:143], a[52:55], v[36:39]\n"
  "s_add_u32 s65, s65, 0xab\n"
  "v_readfirstlane_b32 s64, v236\n"
  "v_mfma_f32_16x16x32_bf16 v[36:39], a[144:147], a[56:59], v[36:39]\n"
  "v_fma_f32 v20, v20, s73, v224\n"
  "v_fma_f32 v21, v21, s73, v224\n"
  "v_mfma_f32_16x16x32_bf16 v[36:39], a[148:151], a[60:63], v[36:39]\n"
  "s_cmpk_gt_u32 s65, 0xd3\n"
  "s_cselect_b32 s65, 0, s65\n"
  "v_exp_f32 v20, v20\n"
  "v_mfma_f32_16x16x32_bf16 v[36:39], a[128:131], a[64:67], v[36:39]\n"
  "s_lshl_b32 s65, s65, 23\n"
  "v_exp_f32 v21, v21\n"
  "v_mfma_f32_16x16x32_bf16 v[36:39], a[132:135], a[68:71], v[36:39]\n"
  "v_fma_f32 v22, v22, s73, v224\n"
  "v_fma_f32 v23, v23, s73, v224\n"
  "v_mfma_f32_16x16x32_bf16 v[52:55], a[132:135], a[92:95], v[248:251]\n"
  "ds_read_b64_tr_b16 a[132:133], v217 offset:2048\n"
  "v_fma_f32 v5, v5, s73, v223\n"
  "v_mfma_f32_16x16x32_bf16 v[52:55], a[128:131], a[88:91], v[52:55]\n"
  "ds_read_b64_tr_b16 a[134:135], v218 offset:2048\n"
  "v_fma_f32 v6, v6, s73, v223\n"
  "v_mfma_f32_16x16x32_bf16 v[52:55], a[148:151], a[84:87], v[52:55]\n"
  "ds_read_b64_tr_b16 a[128:129], v217 offset:2064\n"
  "v_fma_f32 v36, v36, s73, v225\n"
  "v_mfma_f32_16x16x32_bf16 v[52:55], a[144:147], a[80:83], v[52:55]\n"
  "ds_read_b64_tr_b16 a[130:131], v218 offset:2064\n"
  "v_fma_f32 v37, v37, s73, v225\n"
  "v_mfma_f32_16x16x32_bf16 v[52:55], a[140:143], a[76:79], v[52:55]\n"
  "v_exp_f32 v36, v36\n"
  "v_mfma_f32_16x16x32_bf16 v[52:55], a[136:139], a[72:75], v[52:55]\n"
  "s_waitcnt lgkmcnt(4)\n"
  "v_exp_f32 v37, v37\n"
  "v_mfma_f32_16x16x32_bf16 v[100:103], v[12:15], a[120:123], v[100:103]\n"
  "v_cvt_pk_bf16_f32 v12, v0, v1\n"
  "v_cvt_pk_bf16_f32 v13, v2, v3\n"
  "v_mfma_f32_16x16x32_bf16 v[100:103], v[8:11], a[124:127], v[100:103]\n"
  "v_exp_f32 v22, v22\n"
  "v_mfma_f32_16x16x32_bf16 v[132:135], v[28:31], a[120:123], v[132:135]\n"
  "ds_read_b64_tr_b16 a[140:141], v219 offset:2048\n"
  "v_cvt_pk_bf16_f32 v28, v16, v17\n"
  "v_mfma_f32_16x16x32_bf16 v[132:135], v[24:27], a[124:127], v[132:135]\n"
  "ds_read_b64_tr_b16 a[142:143], v220 offset:2048\n"
  "v_cvt_pk_bf16_f32 v29, v18, v19\n"
  "v_mfma_f32_16x16x32_bf16 v[164:167], v[44:47], a[120:123], v[164:167]\n"
  "ds_read_b64_tr_b16 a[136:137], v219 offset:2064\n"
  "v_cvt_pk_bf16_f32 v44, v32, v33\n"
  "v_mfma_f32_16x16x32_bf16 v[164:167], v[40:43], a[124:127], v[164:167]\n"
  "ds_read_b64_tr_b16 a[138:139], v220 offset:2064\n"
  "v_cvt_pk_bf16_f32 v45, v34, v35\n"
  "v_mfma_f32_16x16x32_bf16 v[196:199], v[60:63], a[120:123], v[196:199]\n"
  "v_exp_f32 v23, v23\n"
  "v_mfma_f32_16x16x32_bf16 v[196:199], v[56:59], a[124:127], v[196:199]\n"
  "s_waitcnt lgkmcnt(4)\n"
  "v_exp_f32 v5, v5\n"
  "v_mfma_f32_16x16x32_bf16 a[244:247], a[156:159], a[132:135], a[244:247]\n"
  "v_exp_f32 v6, v6\n"
  "v_mfma_f32_16x16x32_bf16 a[248:251], a[156:159], a[128:131], a[248:251]\n"
  "v_cvt_pk_bf16_f32 v60, v48, v49\n"
  "v_cvt_pk_bf16_f32 v61, v50, v51\n"
  "v_mfma_f32_16x16x32_bf16 v[200:203], v[56:59], a[132:135], v[200:203]\n"
  "v_fma_f32 v52, v52, s73, v226\n"
  "v_fma_f32 v53, v53, s73, v226\n"
  "v_mfma_f32_16x16x32_bf16 v[204:207], v[56:59], a[128:131], v[204:207]\n"
  "v_exp_f32 v52, v52\n"
  "v_mfma_f32_16x16x32_bf16 v[168:171], v[40:43], a[132:135], v[168:171]\n"
  "v_exp_f32_e64 v53, v53\n"
  "v_mfma_f32_16x16x32_bf16 v[172:175], v[40:43], a[128:131], v[172:175]\n"
  "s_waitcnt vmcnt(14) lgkmcnt(0)\n"
  "s_barrier\n"
  "ds_read_b128 a[120:123], v216 offset:12352\n"
  "v_mfma_f32_16x16x32_bf16 v[136:139], v[24:27], a[132:135], v[136:139]\n"
  "ds_read_b64 a[160:161], v231 offset:2048\n"
  "v_fma_f32 v54, v54, s73, v226\n"
  "v_mfma_f32_16x16x32_bf16 v[140:143], v[24:27], a[128:131], v[140:143]\n"
  "ds_read_b64 a[162:163], v232 offset:2048\n"
  "v_fma_f32 v55, v55, s73, v226\n"
  "v_mfma_f32_16x16x32_bf16 v[104:107], v[8:11], a[132:135], v[104:107]\n"
  "ds_read_b128 a[124:127], v216 offset:12416\n"
  "v_cvt_pk_bf16_f32 v62, v52, v53\n"
  "v_mfma_f32_16x16x32_bf16 v[108:111], v[8:11], a[128:131], v[108:111]\n"
  "ds_read_b128 a[132:135], v216 offset:13376\n"
  "v_fma_f32 v38, v38, s73, v225\n"
  "v_mfma_f32_16x16x32_bf16 v[112:115], v[8:11], a[140:143], v[112:115]\n"
  "s_add_u32 s71, s92, s82\n"
  "ds_read_b128 a[128:131], v216 offset:13440\n"
  "v_fma_f32 v39, v39, s73, v225\n"
  "v_mfma_f32_16x16x32_bf16 v[116:119], v[8:11], a[136:139], v[116:119]\n"
  "s_mov_b32 m0, s66\n"
  "ds_read_b128 a[148:151], v216 offset:14400\n"
  "v_cvt_pk_bf16_f32 v46, v36, v37\n"
  "v_mfma_f32_16x16x32_bf16 v[144:147], v[24:27], a[140:143], v[144:147]\n"
  "s_add_u32 s48, s84, s71\n"
  "ds_read_b128 a[144:147], v216 offset:14464\n"
  "v_cvt_pk_bf16_f32 v30, v20, v21\n"
  "v_mfma_f32_16x16x32_bf16 v[148:151], v[24:27], a[136:139], v[148:151]\n"
  "s_addc_u32 s49, s85, 0\n"
  "v_exp_f32 v54, v54\n"
  "v_mfma_f32_16x16x32_bf16 v[176:179], v[40:43], a[140:143], v[176:179]\n"
  "s_sub_i32 s71, s38, s82\n"
  "v_exp_f32 v55, v55\n"
  "v_mfma_f32_16x16x32_bf16 v[180:183], v[40:43], a[136:139], v[180:183]\n"
  "buffer_load_dwordx4 v221, s[40:43], 0 offen offset:0 lds\n"
  "v_mfma_f32_16x16x32_bf16 v[208:211], v[56:59], a[140:143], v[208:211]\n"
  "s_addk_i32 m0, 896\n"
  "v_exp_f32 v38, v38\n"
  "v_mfma_f32_16x16x32_bf16 v[212:215], v[56:59], a[136:139], v[212:215]\n"
  "s_waitcnt lgkmcnt(4)\n"
  "v_exp_f32 v39, v39\n"
  "v_mfma_f32_16x16x32_bf16 v[64:67], a[120:123], a[96:99], v[248:251]\n"
  "s_max_i32 s50, s71, 0\n"
  "v_cvt_pk_bf16_f32 v63, v54, v55\n"
  "v_cvt_pk_bf16_f32 v47, v38, v39\n"
  "v_mfma_f32_16x16x32_bf16 v[64:67], a[124:127], a[100:103], v[64:67]\n"
  "s_waitcnt lgkmcnt(2)\n"
  "buffer_load_dwordx4 v221, s[40:43], 0 offen offset:128 lds\n"
  "v_cvt_pk_bf16_f32 v31, v22, v23\n"
  "v_mfma_f32_16x16x32_bf16 v[64:67], a[132:135], a[104:107], v[64:67]\n"
  "s_addk_i32 m0, 896\n"
  "v_fma_f32 v7, v7, s73, v223\n"
  "v_cvt_pk_bf16_f32 v14, v4, v5\n"
  "v_mfma_f32_16x16x32_bf16 v[64:67], a[128:131], a[108:111], v[64:67]\n"
  "s_waitcnt lgkmcnt(0)\n"
  "v_exp_f32 v7, v7\n"
  "v_mfma_f32_16x16x32_bf16 v[64:67], a[148:151], a[112:115], v[64:67]\n"
  "v_cvt_pk_bf16_f32 v15, v6, v7\n"
  "v_add_f32 v252, v252, v5\n"
  "v_mfma_f32_16x16x32_bf16 v[64:67], a[144:147], a[116:119], v[64:67]\n"
  "buffer_load_dwordx4 v221, s[40:43], 0 offen offset:256 lds\n"
  "v_add_f32 v252, v252, v6\n"
  "v_mfma_f32_16x16x32_bf16 v[0:3], a[144:147], a[20:23], v[248:251]\n"
  "s_add_u32 s40, s40, s31\n"
  "v_add_f32 v252, v252, v7\n"
  "v_add_f32 v253, v253, v20\n"
  "v_mfma_f32_16x16x32_bf16 v[0:3], a[148:151], a[16:19], v[0:3]\n"
  "s_addc_u32 s41, s41, 0\n"
  "v_bfe_u32 v236, v231, 3, 6\n"
  "v_lshlrev_b32 v236, 7, v236\n"
  "v_mfma_f32_16x16x32_bf16 v[0:3], a[128:131], a[12:15], v[0:3]\n"
  "s_cmp_eq_u32 s76, s24\n"
  "s_cselect_b32 s22, s25, 0\n"
  "v_add_f32 v253, v253, v21\n"
  "v_add_f32 v253, v253, v22\n"
  "v_mfma_f32_16x16x32_bf16 v[0:3], a[132:135], a[8:11], v[0:3]\n"
  "s_mov_b32 m0, s58\n"
  "v_add_f32 v253, v253, v23\n"
  "v_add_f32 v254, v254, v36\n"
  "v_mfma_f32_16x16x32_bf16 v[0:3], a[124:127], a[4:7], v[0:3]\n"
  "v_add_f32 v254, v254, v37\n"
  "v_add_f32 v254, v254, v38\n"
  "v_mfma_f32_16x16x32_bf16 v[0:3], a[120:123], a[0:3], v[0:3]\n"
  "v_add_f32 v254, v254, v39\n"
  "v_add_f32 v255, v255, v52\n"
  "v_mfma_f32_16x16x32_bf16 v[16:19], a[120:123], a[24:27], v[248:251]\n"
  "v_add_f32 v255, v255, v53\n"
  "v_add_f32 v255, v255, v54\n"
  "v_mfma_f32_16x16x32_bf16 v[16:19], a[124:127], a[28:31], v[16:19]\n"
  "buffer_load_dwordx4 v221, s[32:35], 0 offen offset:0 lds\n"
  "v_add_f32 v255, v255, v55\n"
  "v_mfma_f32_16x16x32_bf16 v[16:19], a[132:135], a[32:35], v[16:19]\n"
  "s_addk_i32 m0, 1024\n"
  "ds_read_b64_tr_b16 a[140:141], v217 offset:12352\n"
  "v_fma_f32 v64, v64, s73, v227\n"
  "v_mfma_f32_16x16x32_bf16 v[16:19], a[128:131], a[36:39], v[16:19]\n"
  "s_mov_b64 s[32:33], s[44:45]\n"
  "ds_read_b64_tr_b16 a[142:143], v218 offset:12352\n"
  "v_fma_f32 v65, v65, s73, v227\n"
  "v_mfma_f32_16x16x32_bf16 v[16:19], a[148:151], a[40:43], v[16:19]\n"
  "ds_read_b64_tr_b16 a[136:137], v217 offset:13376\n"
  "v_fma_f32 v66, v66, s73, v227\n"
  "v_mfma_f32_16x16x32_bf16 v[16:19], a[144:147], a[44:47], v[16:19]\n"
  "buffer_load_dwordx4 v221, s[44:47], 0 offen offset:0 lds\n"
  "v_fma_f32 v67, v67, s73, v227\n"
  "v_mfma_f32_16x16x32_bf16 v[32:35], a[144:147], a[68:71], v[248:251]\n"
  "s_addk_i32 m0, 768\n"
  "ds_read_b64_tr_b16 a[138:139], v218 offset:13376\n"
  "v_fma_f32 v0, v0, s73, v223\n"
  "v_mfma_f32_16x16x32_bf16 v[32:35], a[148:151], a[64:67], v[32:35]\n"
  "v_exp_f32 v64, v64\n"
  "v_mfma_f32_16x16x32_bf16 v[32:35], a[128:131], a[60:63], v[32:35]\n"
  "v_exp_f32 v65, v65\n"
  "v_mfma_f32_16x16x32_bf16 v[32:35], a[132:135], a[56:59], v[32:35]\n"
  "buffer_load_dwordx4 v221, s[44:47], 0 offen offset:256 lds\n"
  "v_cvt_pk_bf16_f32 v64, v64, v65\n"
  "v_mfma_f32_16x16x32_bf16 v[32:35], a[124:127], a[52:55], v[32:35]\n"
  "s_add_u32 s44, s44, s31\n"
  "v_exp_f32 v66, v66\n"
  "v_mfma_f32_16x16x32_bf16 v[32:35], a[120:123], a[48:51], v[32:35]\n"
  "s_addc_u32 s45, s45, 0\n"
  "v_exp_f32 v67, v67\n"
  "v_mfma_f32_16x16x32_bf16 v[48:51], a[120:123], a[72:75], v[248:251]\n"
  "s_mov_b32 m0, s29\n"
  "v_exp_f32 v0, v0\n"
  "v_mfma_f32_16x16x32_bf16 v[48:51], a[124:127], a[76:79], v[48:51]\n"
  "v_cvt_pk_bf16_f32 v65, v66, v67\n"
  "v_fma_f32 v1, v1, s73, v223\n"
  "v_mfma_f32_16x16x32_bf16 v[48:51], a[132:135], a[80:83], v[48:51]\n"
  "ds_read_b64_tr_b16 a[120:121], v217 offset:12368\n"
  "v_fma_f32 v2, v2, s73, v223\n"
  "v_mfma_f32_16x16x32_bf16 v[48:51], a[128:131], a[84:87], v[48:51]\n"
  "buffer_load_dword v236, s[20:23], 0 offen lds\n"
  "v_fma_f32 v3, v3, s73, v223\n"
  "v_mfma_f32_16x16x32_bf16 v[48:51], a[148:151], a[88:91], v[48:51]\n"
  "s_add_u32 m0, s72, 13440\n"
  "ds_read_b64_tr_b16 a[122:123], v218 offset:12368\n"
  "v_add_f32 v252, v252, v0\n"
  "v_mfma_f32_16x16x32_bf16 v[48:51], a[144:147], a[92:95], v[48:51]\n"
  "s_waitcnt lgkmcnt(2)\n"
  "ds_read_b64_tr_b16 a[124:125], v217 offset:13392\n"
  "v_fma_f32 v16, v16, s73, v224\n"
  "v_mfma_f32_16x16x32_bf16 a[228:231], a[156:159], a[140:143], a[228:231]\n"
  "ds_read_b64_tr_b16 a[126:127], v218 offset:13392\n"
  "v_fma_f32 v17, v17, s73, v224\n"
  "v_mfma_f32_16x16x32_bf16 a[228:231], a[160:163], a[136:139], a[228:231]\n"
  "buffer_load_dwordx4 v221, s[48:51], 0 offen offset:0 nt lds\n"
  "v_fma_f32 v18, v18, s73, v224\n"
  "v_mfma_f32_16x16x32_bf16 a[212:215], v[56:59], a[140:143], a[212:215]\n"
  "v_exp_f32 v1, v1\n"
  "v_mfma_f32_16x16x32_bf16 a[212:215], v[60:63], a[136:139], a[212:215]\n"
  "v_exp_f32 v2, v2\n"
  "v_mfma_f32_16x16x32_bf16 a[196:199], v[40:43], a[140:143], a[196:199]\n"
  "v_exp_f32 v3, v3\n"
  "v_mfma_f32_16x16x32_bf16 a[196:199], v[44:47], a[136:139], a[196:199]\n"
  "s_add_u32 m0, s72, 11456\n"
  "v_exp_f32 v16, v16\n"
  "v_mfma_f32_16x16x32_bf16 a[180:183], v[24:27], a[140:143], a[180:183]\n"
  "ds_read_b64_tr_b16 a[132:133], v219 offset:12352\n"
  "v_add_f32 v252, v252, v1\n"
  "v_mfma_f32_16x16x32_bf16 a[180:183], v[28:31], a[136:139], a[180:183]\n"
  "ds_read_b64_tr_b16 a[134:135], v220 offset:12352\n"
  "v_add_f32 v252, v252, v2\n"
  "v_mfma_f32_16x16x32_bf16 a[164:167], v[8:11], a[140:143], a[164:167]\n"
  "ds_read_b64_tr_b16 a[128:129], v219 offset:13376\n"
  "v_add_f32 v252, v252, v3\n"
  "v_mfma_f32_16x16x32_bf16 a[164:167], v[12:15], a[136:139], a[164:167]\n"
  "buffer_load_dwordx4 v221, s[48:51], 0 offen offset:3072 nt lds\n"
  "v_fma_f32 v19, v19, s73, v224\n"
  "v_mfma_f32_16x16x32_bf16 v[84:87], a[156:159], a[152:155], v[84:87]\n"
  "ds_read_b64_tr_b16 a[130:131], v220 offset:13376\n"
  "v_add_f32 v253, v253, v16\n"
  "v_mfma_f32_16x16x32_bf16 v[84:87], a[160:163], a[152:155], v[84:87]\n"
  "s_waitcnt lgkmcnt(4)\n"
  "v_exp_f32 v17, v17\n"
  "v_mfma_f32_16x16x32_bf16 a[168:171], v[8:11], a[120:123], a[168:171]\n"
  "v_exp_f32 v18, v18\n"
  "v_mfma_f32_16x16x32_bf16 a[168:171], v[12:15], a[124:127], a[168:171]\n"
  "s_add_u32 m0, s72, 15552\n"
  "v_exp_f32 v19, v19\n"
  "v_mfma_f32_16x16x32_bf16 a[184:187], v[24:27], a[120:123], a[184:187]\n"
  "ds_read_b128 a[140:143], v235 offset:12352\n"
  "v_add_f32 v253, v253, v17\n"
  "v_mfma_f32_16x16x32_bf16 a[184:187], v[28:31], a[124:127], a[184:187]\n"
  "ds_read_b128 a[136:139], v235 offset:12416\n"
  "v_add_f32 v253, v253, v18\n"
  "v_mfma_f32_16x16x32_bf16 a[200:203], v[40:43], a[120:123], a[200:203]\n"
  "ds_read_b128 a[148:151], v235 offset:13376\n"
  "v_add_f32 v253, v253, v19\n"
  "v_mfma_f32_16x16x32_bf16 a[200:203], v[44:47], a[124:127], a[200:203]\n"
  "buffer_load_dwordx4 v221, s[48:51], 0 offen offset:128 nt lds\n"
  "v_fma_f32 v32, v32, s73, v225\n"
  "v_mfma_f32_16x16x32_bf16 a[216:219], v[56:59], a[120:123], a[216:219]\n"
  "ds_read_b128 a[144:147], v235 offset:13440\n"
  "v_fma_f32 v33, v33, s73, v225\n"
  "v_mfma_f32_16x16x32_bf16 a[216:219], v[60:63], a[124:127], a[216:219]\n"
  "v_exp_f32 v32, v32\n"
  "v_mfma_f32_16x16x32_bf16 a[232:235], a[156:159], a[120:123], a[232:235]\n"
  "ds_read_b128 a[120:123], v235 offset:14400\n"
  "v_fma_f32 v34, v34, s73, v225\n"
  "v_mfma_f32_16x16x32_bf16 a[232:235], a[160:163], a[124:127], a[232:235]\n"
  "s_waitcnt lgkmcnt(5)\n"
  "ds_read_b128 a[124:127], v235 offset:14464\n"
  "v_fma_f32 v35, v35, s73, v225\n"
  "v_mfma_f32_16x16x32_bf16 a[220:223], v[56:59], a[132:135], a[220:223]\n"
  "s_add_u32 m0, s72, 13568\n"
  "v_exp_f32 v33, v33\n"
  "v_mfma_f32_16x16x32_bf16 a[220:223], v[60:63], a[128:131], a[220:223]\n"
  "ds_write_b64 v231, v[64:65] offset:0\n"
  "v_mfma_f32_16x16x32_bf16 a[204:207], v[40:43], a[132:135], a[204:207]\n"
  "v_exp_f32 v34, v34\n"
  "v_mfma_f32_16x16x32_bf16 a[204:207], v[44:47], a[128:131], a[204:207]\n"
  "buffer_load_dwordx4 v221, s[48:51], 0 offen offset:3200 nt lds\n"
  "v_add_f32 v254, v254, v32\n"
  "v_mfma_f32_16x16x32_bf16 a[188:191], v[24:27], a[132:135], a[188:191]\n"
  "v_exp_f32 v35, v35\n"
  "v_mfma_f32_16x16x32_bf16 a[188:191], v[28:31], a[128:131], a[188:191]\n"
  "v_add_f32 v254, v254, v33\n"
  "v_add_f32 v254, v254, v34\n"
  "v_mfma_f32_16x16x32_bf16 a[172:175], v[8:11], a[132:135], a[172:175]\n"
  "v_add_f32 v254, v254, v35\n"
  "v_fma_f32 v48, v48, s73, v226\n"
  "v_mfma_f32_16x16x32_bf16 a[172:175], v[12:15], a[128:131], a[172:175]\n"
  "s_waitcnt lgkmcnt(5)\n"
  "v_exp_f32 v48, v48\n"
  "v_mfma_f32_16x16x32_bf16 v[4:7], a[140:143], a[0:3], v[248:251]\n"
  "v_fma_f32 v49, v49, s73, v226\n"
  "v_fma_f32 v50, v50, s73, v226\n"
  "v_mfma_f32_16x16x32_bf16 v[4:7], a[136:139], a[4:7], v[4:7]\n"
  "s_waitcnt lgkmcnt(3)\n"
  "v_exp_f32 v49, v49\n"
  "v_mfma_f32_16x16x32_bf16 v[4:7], a[148:151], a[8:11], v[4:7]\n"
  "v_exp_f32 v50, v50\n"
  "v_mfma_f32_16x16x32_bf16 v[4:7], a[144:147], a[12:15], v[4:7]\n"
  "s_waitcnt lgkmcnt(1)\n"
  "v_fma_f32 v51, v51, s73, v226\n"
  "v_add_f32 v255, v255, v48\n"
  "v_mfma_f32_16x16x32_bf16 v[4:7], a[120:123], a[16:19], v[4:7]\n"
  "v_exp_f32 v51, v51\n"
  "v_mfma_f32_16x16x32_bf16 v[4:7], a[124:127], a[20:23], v[4:7]\n"
  "v_add_f32 v255, v255, v49\n"
  "v_add_f32 v255, v255, v50\n"
  "v_mfma_f32_16x16x32_bf16 v[20:23], a[124:127], a[44:47], v[248:251]\n"
  "v_add_f32 v255, v255, v51\n"
  "v_mfma_f32_16x16x32_bf16 v[20:23], a[120:123], a[40:43], v[20:23]\n"
  "v_mfma_f32_16x16x32_bf16 v[20:23], a[144:147], a[36:39], v[20:23]\n"
  "ds_read_b64_tr_b16 a[132:133], v219 offset:12368\n"
  "v_fma_f32 v4, v4, s73, v223\n"
  "v_mfma_f32_16x16x32_bf16 v[20:23], a[148:151], a[32:35], v[20:23]\n"
  "ds_read_b64_tr_b16 a[134:135], v220 offset:12368\n"
  "v_fma_f32 v5, v5, s73, v223\n"
  "v_mfma_f32_16x16x32_bf16 v[20:23], a[136:139], a[28:31], v[20:23]\n"
  "ds_read_b64_tr_b16 a[128:129], v219 offset:13392\n"
  "v_fma_f32 v6, v6, s73, v223\n"
  "v_mfma_f32_16x16x32_bf16 v[20:23], a[140:143], a[24:27], v[20:23]\n"
  "ds_read_b64_tr_b16 a[130:131], v220 offset:13392\n"
  "v_fma_f32 v7, v7, s73, v223\n"
  "v_mfma_f32_16x16x32_bf16 v[36:39], a[140:143], a[48:51], v[248:251]\n"
  "v_exp_f32 v4, v4\n"
  "v_mfma_f32_16x16x32_bf16 v[36:39], a[136:139], a[52:55], v[36:39]\n"
  "v_exp_f32 v5, v5\n"
  "v_mfma_f32_16x16x32_bf16 v[36:39], a[148:151], a[56:59], v[36:39]\n"
  "v_exp_f32 v6, v6\n"
  "v_mfma_f32_16x16x32_bf16 v[36:39], a[144:147], a[60:63], v[36:39]\n"
  "v_exp_f32 v7, v7\n"
  "v_mfma_f32_16x16x32_bf16 v[36:39], a[120:123], a[64:67], v[36:39]\n"
  "v_fma_f32 v20, v20, s73, v224\n"
  "v_fma_f32 v21, v21, s73, v224\n"
  "v_mfma_f32_16x16x32_bf16 v[36:39], a[124:127], a[68:71], v[36:39]\n"
  "v_exp_f32 v20, v20\n"
  "v_mfma_f32_16x16x32_bf16 v[52:55], a[124:127], a[92:95], v[248:251]\n"
  "ds_read_b64_tr_b16 a[124:125], v217 offset:14400\n"
  "v_fma_f32 v22, v22, s73, v224\n"
  "v_mfma_f32_16x16x32_bf16 v[52:55], a[120:123], a[88:91], v[52:55]\n"
  "ds_read_b64_tr_b16 a[126:127], v218 offset:14400\n"
  "v_fma_f32 v23, v23, s73, v224\n"
  "v_mfma_f32_16x16x32_bf16 v[52:55], a[144:147], a[84:87], v[52:55]\n"
  "ds_read_b64_tr_b16 a[120:121], v217 offset:14416\n"
  "v_fma_f32 v36, v36, s73, v225\n"
  "v_mfma_f32_16x16x32_bf16 v[52:55], a[148:151], a[80:83], v[52:55]\n"
  "ds_read_b64_tr_b16 a[122:123], v218 offset:14416\n"
  "v_fma_f32 v37, v37, s73, v225\n"
  "v_mfma_f32_16x16x32_bf16 v[52:55], a[136:139], a[76:79], v[52:55]\n"
  "v_exp_f32 v36, v36\n"
  "v_mfma_f32_16x16x32_bf16 v[52:55], a[140:143], a[72:75], v[52:55]\n"
  "s_waitcnt lgkmcnt(4)\n"
  "v_exp_f32 v37, v37\n"
  "v_mfma_f32_16x16x32_bf16 a[176:179], v[8:11], a[132:135], a[176:179]\n"
  "v_cvt_pk_bf16_f32 v8, v0, v1\n"
  "v_cvt_pk_bf16_f32 v9, v2, v3\n"
  "v_mfma_f32_16x16x32_bf16 a[176:179], v[12:15], a[128:131], a[176:179]\n"
  "v_exp_f32 v21, v21\n"
  "v_mfma_f32_16x16x32_bf16 a[192:195], v[24:27], a[132:135], a[192:195]\n"
  "ds_read_b64_tr_b16 a[136:137], v219 offset:14400\n"
  "v_cvt_pk_bf16_f32 v24, v16, v17\n"
  "v_mfma_f32_16x16x32_bf16 a[192:195], v[28:31], a[128:131], a[192:195]\n"
  "ds_read_b64_tr_b16 a[138:139], v220 offset:14400\n"
  "v_cvt_pk_bf16_f32 v25, v18, v19\n"
  "v_mfma_f32_16x16x32_bf16 a[208:211], v[40:43], a[132:135], a[208:211]\n"
  "ds_read_b64_tr_b16 a[140:141], v219 offset:14416\n"
  "v_cvt_pk_bf16_f32 v40, v32, v33\n"
  "v_mfma_f32_16x16x32_bf16 a[208:211], v[44:47], a[128:131], a[208:211]\n"
  "ds_read_b64_tr_b16 a[142:143], v220 offset:14416\n"
  "v_cvt_pk_bf16_f32 v41, v34, v35\n"
  "v_mfma_f32_16x16x32_bf16 a[224:227], v[56:59], a[132:135], a[224:227]\n"
  "v_exp_f32 v22, v22\n"
  "v_mfma_f32_16x16x32_bf16 a[224:227], v[60:63], a[128:131], a[224:227]\n"
  "s_waitcnt lgkmcnt(4)\n"
  "v_exp_f32 v23, v23\n"
  "v_mfma_f32_16x16x32_bf16 a[244:247], a[160:163], a[124:127], a[244:247]\n"
  "v_cvt_pk_bf16_f32 v56, v48, v49\n"
  "v_cvt_pk_bf16_f32 v57, v50, v51\n"
  "v_mfma_f32_16x16x32_bf16 a[248:251], a[160:163], a[120:123], a[248:251]\n"
  "v_fma_f32 v52, v52, s73, v226\n"
  "v_fma_f32 v53, v53, s73, v226\n"
  "v_mfma_f32_16x16x32_bf16 v[200:203], v[60:63], a[124:127], v[200:203]\n"
  "v_exp_f32 v52, v52\n"
  "v_mfma_f32_16x16x32_bf16 v[204:207], v[60:63], a[120:123], v[204:207]\n"
  "v_exp_f32_e64 v53, v53\n"
  "v_mfma_f32_16x16x32_bf16 v[168:171], v[44:47], a[124:127], v[168:171]\n"
  "v_fma_f32 v54, v54, s73, v226\n"
  "v_fma_f32 v55, v55, s73, v226\n"
  "v_mfma_f32_16x16x32_bf16 v[172:175], v[44:47], a[120:123], v[172:175]\n"
  "s_waitcnt vmcnt(16) lgkmcnt(0)\n"
  "s_barrier\n"
  "ds_read_b128 a[132:135], v216 offset:24704\n"
  "v_mfma_f32_16x16x32_bf16 v[136:139], v[28:31], a[124:127], v[136:139]\n"
  "ds_read_b64 a[156:157], v231 offset:0\n"
  "v_cvt_pk_bf16_f32 v58, v52, v53\n"
  "v_mfma_f32_16x16x32_bf16 v[140:143], v[28:31], a[120:123], v[140:143]\n"
  "ds_read_b64 a[158:159], v232 offset:0\n"
  "v_fma_f32 v38, v38, s73, v225\n"
  "v_mfma_f32_16x16x32_bf16 v[104:107], v[12:15], a[124:127], v[104:107]\n"
  "ds_read_b128 a[128:131], v216 offset:24768\n"
  "v_fma_f32 v39, v39, s73, v225\n"
  "v_mfma_f32_16x16x32_bf16 v[108:111], v[12:15], a[120:123], v[108:111]\n"
  "ds_read_b128 a[124:127], v216 offset:25728\n"
  "v_cvt_pk_bf16_f32 v42, v36, v37\n"
  "v_mfma_f32_16x16x32_bf16 v[112:115], v[12:15], a[136:139], v[112:115]\n"
  "ds_read_b128 a[120:123], v216 offset:25792\n"
  "v_cvt_pk_bf16_f32 v26, v20, v21\n"
  "v_mfma_f32_16x16x32_bf16 v[116:119], v[12:15], a[140:143], v[116:119]\n"
  "s_mov_b32 m0, s67\n"
  "ds_read_b128 a[144:147], v216 offset:26752\n"
  "v_cvt_pk_bf16_f32 v27, v22, v23\n"
  "v_mfma_f32_16x16x32_bf16 v[144:147], v[28:31], a[136:139], v[144:147]\n"
  "ds_read_b128 a[148:151], v216 offset:26816\n"
  "v_cvt_pk_bf16_f32 v10, v4, v5\n"
  "v_mfma_f32_16x16x32_bf16 v[148:151], v[28:31], a[140:143], v[148:151]\n"
  "v_exp_f32 v54, v54\n"
  "v_mfma_f32_16x16x32_bf16 v[176:179], v[44:47], a[136:139], v[176:179]\n"
  "v_exp_f32 v55, v55\n"
  "v_mfma_f32_16x16x32_bf16 v[180:183], v[44:47], a[140:143], v[180:183]\n"
  "buffer_load_dwordx4 v221, s[40:43], 0 offen offset:0 lds\n"
  "v_mfma_f32_16x16x32_bf16 v[208:211], v[60:63], a[136:139], v[208:211]\n"
  "s_addk_i32 m0, 896\n"
  "v_exp_f32 v38, v38\n"
  "v_mfma_f32_16x16x32_bf16 v[212:215], v[60:63], a[140:143], v[212:215]\n"
  "s_waitcnt lgkmcnt(4)\n"
  "v_exp_f32 v39, v39\n"
  "v_mfma_f32_16x16x32_bf16 v[64:67], a[132:135], a[96:99], v[248:251]\n"
  "v_cvt_pk_bf16_f32 v59, v54, v55\n"
  "v_cvt_pk_bf16_f32 v43, v38, v39\n"
  "v_mfma_f32_16x16x32_bf16 v[64:67], a[128:131], a[100:103], v[64:67]\n"
  "s_waitcnt lgkmcnt(2)\n"
  "buffer_load_dwordx4 v221, s[40:43], 0 offen offset:128 lds\n"
  "v_cvt_pk_bf16_f32 v11, v6, v7\n"
  "v_mfma_f32_16x16x32_bf16 v[64:67], a[124:127], a[104:107], v[64:67]\n"
  "s_addk_i32 m0, 896\n"
  "v_add_f32 v252, v252, v4\n"
  "v_add_f32 v252, v252, v5\n"
  "v_mfma_f32_16x16x32_bf16 v[64:67], a[120:123], a[108:111], v[64:67]\n"
  "s_waitcnt lgkmcnt(0)\n"
  "v_add_f32 v252, v252, v6\n"
  "v_add_f32 v252, v252, v7\n"
  "v_mfma_f32_16x16x32_bf16 v[64:67], a[144:147], a[112:115], v[64:67]\n"
  "v_add_f32 v253, v253, v20\n"
  "v_add_f32 v253, v253, v21\n"
  "v_mfma_f32_16x16x32_bf16 v[64:67], a[148:151], a[116:119], v[64:67]\n"
  "buffer_load_dwordx4 v221, s[40:43], 0 offen offset:256 lds\n"
  "v_add_f32 v253, v253, v22\n"
  "v_mfma_f32_16x16x32_bf16 v[0:3], a[148:151], a[20:23], v[248:251]\n"
  "s_add_u32 s40, s86, s96\n"
  "v_add_f32 v253, v253, v23\n"
  "v_add_f32 v254, v254, v36\n"
  "v_mfma_f32_16x16x32_bf16 v[0:3], a[144:147], a[16:19], v[0:3]\n"
  "s_addc_u32 s41, s87, 0\n"
  "v_add_f32 v254, v254, v37\n"
  "v_add_f32 v254, v254, v38\n"
  "v_mfma_f32_16x16x32_bf16 v[0:3], a[120:123], a[12:15], v[0:3]\n"
  "s_mov_b32 s42, s30\n"
  "v_add_f32 v254, v254, v39\n"
  "v_add_f32 v255, v255, v52\n"
  "v_mfma_f32_16x16x32_bf16 v[0:3], a[124:127], a[8:11], v[0:3]\n"
  "s_add_u32 m0, s59, -128\n"
  "v_add_f32 v255, v255, v53\n"
  "v_add_f32 v255, v255, v54\n"
  "v_mfma_f32_16x16x32_bf16 v[0:3], a[128:131], a[4:7], v[0:3]\n"
  "v_add_f32 v255, v255, v55\n"
  "v_fma_f32 v64, v64, s73, v227\n"
  "v_mfma_f32_16x16x32_bf16 v[0:3], a[132:135], a[0:3], v[0:3]\n"
  "v_exp_f32 v64, v64\n"
  "v_mfma_f32_16x16x32_bf16 v[16:19], a[132:135], a[24:27], v[248:251]\n"
  "v_fma_f32 v65, v65, s73, v227\n"
  "v_fma_f32 v66, v66, s73, v227\n"
  "v_mfma_f32_16x16x32_bf16 v[16:19], a[128:131], a[28:31], v[16:19]\n"
  "buffer_load_dwordx4 v221, s[32:35], 0 offen offset:128 lds\n"
  "v_fma_f32 v67, v67, s73, v227\n"
  "v_mfma_f32_16x16x32_bf16 v[16:19], a[124:127], a[32:35], v[16:19]\n"
  "s_addk_i32 m0, 1024\n"
  "ds_read_b64_tr_b16 a[136:137], v217 offset:24704\n"
  "v_fma_f32 v0, v0, s73, v223\n"
  "v_mfma_f32_16x16x32_bf16 v[16:19], a[120:123], a[36:39], v[16:19]\n"
  "s_mov_b64 s[32:33], s[44:45]\n"
  "ds_read_b64_tr_b16 a[138:139], v218 offset:24704\n"
  "v_fma_f32 v1, v1, s73, v223\n"
  "v_mfma_f32_16x16x32_bf16 v[16:19], a[144:147], a[40:43], v[16:19]\n"
  "ds_read_b64_tr_b16 a[140:141], v217 offset:25728\n"
  "v_fma_f32 v2, v2, s73, v223\n"
  "v_mfma_f32_16x16x32_bf16 v[16:19], a[148:151], a[44:47], v[16:19]\n"
  "buffer_load_dwordx4 v221, s[44:47], 0 offen offset:128 lds\n"
  "v_fma_f32 v3, v3, s73, v223\n"
  "v_mfma_f32_16x16x32_bf16 v[32:35], a[148:151], a[68:71], v[248:251]\n"
  "s_addk_i32 m0, 896\n"
  "ds_read_b64_tr_b16 a[142:143], v218 offset:25728\n"
  "v_mfma_f32_16x16x32_bf16 v[32:35], a[144:147], a[64:67], v[32:35]\n"
  "v_exp_f32 v65, v65\n"
  "v_mfma_f32_16x16x32_bf16 v[32:35], a[120:123], a[60:63], v[32:35]\n"
  "v_exp_f32 v66, v66\n"
  "v_mfma_f32_16x16x32_bf16 v[32:35], a[124:127], a[56:59], v[32:35]\n"
  "buffer_load_dwordx4 v221, s[44:47], 0 offen offset:256 lds\n"
  "v_cvt_pk_bf16_f32 v64, v64, v65\n"
  "v_mfma_f32_16x16x32_bf16 v[32:35], a[128:131], a[52:55], v[32:35]\n"
  "s_add_u32 s44, s44, s31\n"
  "v_exp_f32 v67, v67\n"
  "v_mfma_f32_16x16x32_bf16 v[32:35], a[132:135], a[48:51], v[32:35]\n"
  "s_addc_u32 s45, s45, 0\n"
  "v_exp_f32 v0, v0\n"
  "v_mfma_f32_16x16x32_bf16 v[48:51], a[132:135], a[72:75], v[248:251]\n"
  "ds_read_b64_tr_b16 a[132:133], v217 offset:24720\n"
  "v_cvt_pk_bf16_f32 v65, v66, v67\n"
  "v_mfma_f32_16x16x32_bf16 v[48:51], a[128:131], a[76:79], v[48:51]\n"
  "ds_read_b64_tr_b16 a[134:135], v218 offset:24720\n"
  "v_add_f32 v252, v252, v0\n"
  "v_mfma_f32_16x16x32_bf16 v[48:51], a[124:127], a[80:83], v[48:51]\n"
  "ds_read_b64_tr_b16 a[128:129], v217 offset:25744\n"
  "v_fma_f32 v16, v16, s73, v224\n"
  "v_mfma_f32_16x16x32_bf16 v[48:51], a[120:123], a[84:87], v[48:51]\n"
  "ds_read_b64_tr_b16 a[130:131], v218 offset:25744\n"
  "v_fma_f32 v17, v17, s73, v224\n"
  "v_mfma_f32_16x16x32_bf16 v[48:51], a[144:147], a[88:91], v[48:51]\n"
  "v_exp_f32 v1, v1\n"
  "v_mfma_f32_16x16x32_bf16 v[48:51], a[148:151], a[92:95], v[48:51]\n"
  "s_waitcnt lgkmcnt(4)\n"
  "v_exp_f32 v2, v2\n"
  "v_mfma_f32_16x16x32_bf16 a[236:239], a[160:163], a[136:139], a[236:239]\n"
  "v_exp_f32 v3, v3\n"
  "v_mfma_f32_16x16x32_bf16 a[236:239], a[156:159], a[140:143], a[236:239]\n"
  "v_exp_f32 v16, v16\n"
  "v_mfma_f32_16x16x32_bf16 v[184:187], v[60:63], a[136:139], v[184:187]\n"
  "v_exp_f32 v17, v17\n"
  "v_mfma_f32_16x16x32_bf16 v[184:187], v[56:59], a[140:143], v[184:187]\n"
  "v_add_f32 v252, v252, v1\n"
  "v_add_f32 v252, v252, v2\n"
  "v_mfma_f32_16x16x32_bf16 v[152:155], v[44:47], a[136:139], v[152:155]\n"
  "ds_read_b64_tr_b16 a[124:125], v219 offset:24704\n"
  "v_add_f32 v252, v252, v3\n"
  "v_mfma_f32_16x16x32_bf16 v[152:155], v[40:43], a[140:143], v[152:155]\n"
  "ds_read_b64_tr_b16 a[126:127], v220 offset:24704\n"
  "v_fma_f32 v18, v18, s73, v224\n"
  "v_mfma_f32_16x16x32_bf16 v[120:123], v[28:31], a[136:139], v[120:123]\n"
  "ds_read_b64_tr_b16 a[120:121], v219 offset:25728\n"
  "v_fma_f32 v19, v19, s73, v224\n"
  "v_mfma_f32_16x16x32_bf16 v[120:123], v[24:27], a[140:143], v[120:123]\n"
  "ds_read_b64_tr_b16 a[122:123], v220 offset:25728\n"
  "v_fma_f32 v32, v32, s73, v225\n"
  "v_mfma_f32_16x16x32_bf16 v[88:91], v[12:15], a[136:139], v[88:91]\n"
  "v_exp_f32 v18, v18\n"
  "v_mfma_f32_16x16x32_bf16 v[88:91], v[8:11], a[140:143], v[88:91]\n"
  "s_waitcnt lgkmcnt(4)\n"
  "v_exp_f32 v19, v19\n"
  "v_mfma_f32_16x16x32_bf16 v[92:95], v[12:15], a[132:135], v[92:95]\n"
  "v_exp_f32 v32, v32\n"
  "v_mfma_f32_16x16x32_bf16 v[92:95], v[8:11], a[128:131], v[92:95]\n"
  "v_fma_f32 v33, v33, s73, v225\n"
  "v_fma_f32 v34, v34, s73, v225\n"
  "v_mfma_f32_16x16x32_bf16 v[124:127], v[28:31], a[132:135], v[124:127]\n"
  "ds_read_b128 a[136:139], v235 offset:24704\n"
  "v_fma_f32 v35, v35, s73, v225\n"
  "v_mfma_f32_16x16x32_bf16 v[124:127], v[24:27], a[128:131], v[124:127]\n"
  "ds_read_b128 a[140:143], v235 offset:24768\n"
  "v_fma_f32 v48, v48, s73, v226\n"
  "v_mfma_f32_16x16x32_bf16 v[156:159], v[44:47], a[132:135], v[156:159]\n"
  "ds_read_b128 a[144:147], v235 offset:25728\n"
  "v_fma_f32 v49, v49, s73, v226\n"
  "v_mfma_f32_16x16x32_bf16 v[156:159], v[40:43], a[128:131], v[156:159]\n"
  "ds_read_b128 a[148:151], v235 offset:25792\n"
  "v_fma_f32 v50, v50, s73, v226\n"
  "v_mfma_f32_16x16x32_bf16 v[188:191], v[60:63], a[132:135], v[188:191]\n"
  "v_exp_f32 v33, v33\n"
  "v_mfma_f32_16x16x32_bf16 v[188:191], v[56:59], a[128:131], v[188:191]\n"
  "v_exp_f32 v34, v34\n"
  "v_mfma_f32_16x16x32_bf16 a[240:243], a[160:163], a[132:135], a[240:243]\n"
  "ds_read_b128 a[132:135], v235 offset:26752\n"
  "v_fma_f32 v51, v51, s73, v226\n"
  "v_mfma_f32_16x16x32_bf16 a[240:243], a[156:159], a[128:131], a[240:243]\n"
  "s_waitcnt lgkmcnt(5)\n"
  "ds_read_b128 a[128:131], v235 offset:26816\n"
  "v_add_f32 v253, v253, v16\n"
  "v_mfma_f32_16x16x32_bf16 v[192:195], v[60:63], a[124:127], v[192:195]\n"
  "v_exp_f32 v35, v35\n"
  "v_mfma_f32_16x16x32_bf16 v[192:195], v[56:59], a[120:123], v[192:195]\n"
  "ds_write_b64 v231, v[64:65] offset:2048\n"
  "v_mfma_f32_16x16x32_bf16 v[160:163], v[44:47], a[124:127], v[160:163]\n"
  "v_exp_f32 v48, v48\n"
  "v_mfma_f32_16x16x32_bf16 v[160:163], v[40:43], a[120:123], v[160:163]\n"
  "v_exp_f32 v49, v49\n"
  "v_mfma_f32_16x16x32_bf16 v[128:131], v[28:31], a[124:127], v[128:131]\n"
  "v_exp_f32 v50, v50\n"
  "v_mfma_f32_16x16x32_bf16 v[128:131], v[24:27], a[120:123], v[128:131]\n"
  "v_exp_f32 v51, v51\n"
  "v_mfma_f32_16x16x32_bf16 v[96:99], v[12:15], a[124:127], v[96:99]\n"
  "v_add_f32 v253, v253, v17\n"
  "v_add_f32 v253, v253, v18\n"
  "v_mfma_f32_16x16x32_bf16 v[96:99], v[8:11], a[120:123], v[96:99]\n"
  "s_waitcnt lgkmcnt(5)\n"
  "v_add_f32 v253, v253, v19\n"
  "v_add_f32 v254, v254, v32\n"
  "v_mfma_f32_16x16x32_bf16 v[4:7], a[136:139], a[0:3], v[248:251]\n"
  "v_add_f32 v254, v254, v33\n"
  "v_add_f32 v254, v254, v34\n"
  "v_mfma_f32_16x16x32_bf16 v[4:7], a[140:143], a[4:7], v[4:7]\n"
  "s_waitcnt lgkmcnt(3)\n"
  "v_add_f32 v254, v254, v35\n"
  "v_add_f32 v255, v255, v48\n"
  "v_mfma_f32_16x16x32_bf16 v[4:7], a[144:147], a[8:11], v[4:7]\n"
  "v_add_f32 v255, v255, v49\n"
  "v_add_f32 v255, v255, v50\n"
  "v_mfma_f32_16x16x32_bf16 v[4:7], a[148:151], a[12:15], v[4:7]\n"
  "s_waitcnt lgkmcnt(1)\n"
  "v_add_f32 v255, v255, v51\n"
  "v_mfma_f32_16x16x32_bf16 v[4:7], a[132:135], a[16:19], v[4:7]\n"
  "v_mfma_f32_16x16x32_bf16 v[4:7], a[128:131], a[20:23], v[4:7]\n"
  "v_mfma_f32_16x16x32_bf16 v[20:23], a[128:131], a[44:47], v[248:251]\n"
  "v_mfma_f32_16x16x32_bf16 v[20:23], a[132:135], a[40:43], v[20:23]\n"
  "v_mfma_f32_16x16x32_bf16 v[20:23], a[148:151], a[36:39], v[20:23]\n"
  "ds_read_b64_tr_b16 a[124:125], v219 offset:24720\n"
  "v_fma_f32 v4, v4, s73, v223\n"
  "v_mfma_f32_16x16x32_bf16 v[20:23], a[144:147], a[32:35], v[20:23]\n"
  "ds_read_b64_tr_b16 a[126:127], v220 offset:24720\n"
  "v_fma_f32 v5, v5, s73, v223\n"
  "v_mfma_f32_16x16x32_bf16 v[20:23], a[140:143], a[28:31], v[20:23]\n"
  "ds_read_b64_tr_b16 a[120:121], v219 offset:25744\n"
  "v_fma_f32 v6, v6, s73, v223\n"
  "v_mfma_f32_16x16x32_bf16 v[20:23], a[136:139], a[24:27], v[20:23]\n"
  "ds_read_b64_tr_b16 a[122:123], v220 offset:25744\n"
  "v_fma_f32 v7, v7, s73, v223\n"
  "v_mfma_f32_16x16x32_bf16 v[36:39], a[136:139], a[48:51], v[248:251]\n"
  "v_exp_f32 v4, v4\n"
  "v_mfma_f32_16x16x32_bf16 v[36:39], a[140:143], a[52:55], v[36:39]\n"
  "v_exp_f32 v5, v5\n"
  "v_mfma_f32_16x16x32_bf16 v[36:39], a[144:147], a[56:59], v[36:39]\n"
  "v_exp_f32 v6, v6\n"
  "v_mfma_f32_16x16x32_bf16 v[36:39], a[148:151], a[60:63], v[36:39]\n"
  "v_exp_f32 v7, v7\n"
  "v_mfma_f32_16x16x32_bf16 v[36:39], a[132:135], a[64:67], v[36:39]\n"
  "v_fma_f32 v20, v20, s73, v224\n"
  "v_fma_f32 v21, v21, s73, v224\n"
  "v_mfma_f32_16x16x32_bf16 v[36:39], a[128:131], a[68:71], v[36:39]\n"
  "v_exp_f32 v20, v20\n"
  "v_mfma_f32_16x16x32_bf16 v[52:55], a[128:131], a[92:95], v[248:251]\n"
  "ds_read_b64_tr_b16 a[128:129], v217 offset:26752\n"
  "v_fma_f32 v22, v22, s73, v224\n"
  "v_mfma_f32_16x16x32_bf16 v[52:55], a[132:135], a[88:91], v[52:55]\n"
  "ds_read_b64_tr_b16 a[130:131], v218 offset:26752\n"
  "v_fma_f32 v23, v23, s73, v224\n"
  "v_mfma_f32_16x16x32_bf16 v[52:55], a[148:151], a[84:87], v[52:55]\n"
  "ds_read_b64_tr_b16 a[132:133], v217 offset:26768\n"
  "v_fma_f32 v36, v36, s73, v225\n"
  "v_mfma_f32_16x16x32_bf16 v[52:55], a[144:147], a[80:83], v[52:55]\n"
  "ds_read_b64_tr_b16 a[134:135], v218 offset:26768\n"
  "v_fma_f32 v37, v37, s73, v225\n"
  "v_mfma_f32_16x16x32_bf16 v[52:55], a[140:143], a[76:79], v[52:55]\n"
  "v_exp_f32 v21, v21\n"
  "v_mfma_f32_16x16x32_bf16 v[52:55], a[136:139], a[72:75], v[52:55]\n"
  "s_waitcnt lgkmcnt(4)\n"
  "v_exp_f32 v22, v22\n"
  "v_mfma_f32_16x16x32_bf16 v[100:103], v[12:15], a[124:127], v[100:103]\n"
  "v_exp_f32 v23, v23\n"
  "v_mfma_f32_16x16x32_bf16 v[100:103], v[8:11], a[120:123], v[100:103]\n"
  "v_exp_f32 v36, v36\n"
  "v_mfma_f32_16x16x32_bf16 v[132:135], v[28:31], a[124:127], v[132:135]\n"
  "ds_read_b64_tr_b16 a[148:149], v219 offset:26752\n"
  "v_cvt_pk_bf16_f32 v12, v0, v1\n"
  "v_mfma_f32_16x16x32_bf16 v[132:135], v[24:27], a[120:123], v[132:135]\n"
  "ds_read_b64_tr_b16 a[150:151], v220 offset:26752\n"
  "v_cvt_pk_bf16_f32 v13, v2, v3\n"
  "v_mfma_f32_16x16x32_bf16 v[164:167], v[44:47], a[124:127], v[164:167]\n"
  "ds_read_b64_tr_b16 a[144:145], v219 offset:26768\n"
  "v_fma_f32 v38, v38, s73, v225\n"
  "v_mfma_f32_16x16x32_bf16 v[164:167], v[40:43], a[120:123], v[164:167]\n"
  "ds_read_b64_tr_b16 a[146:147], v220 offset:26768\n"
  "v_fma_f32 v39, v39, s73, v225\n"
  "v_mfma_f32_16x16x32_bf16 v[196:199], v[60:63], a[124:127], v[196:199]\n"
  "v_exp_f32 v37, v37\n"
  "v_mfma_f32_16x16x32_bf16 v[196:199], v[56:59], a[120:123], v[196:199]\n"
  "s_waitcnt lgkmcnt(4)\n"
  "v_exp_f32 v38, v38\n"
  "v_mfma_f32_16x16x32_bf16 a[244:247], a[156:159], a[128:131], a[244:247]\n"
  "v_exp_f32 v39, v39\n"
  "v_mfma_f32_16x16x32_bf16 a[248:251], a[156:159], a[132:135], a[248:251]\n"
  "v_fma_f32 v52, v52, s73, v226\n"
  "v_fma_f32 v53, v53, s73, v226\n"
  "v_mfma_f32_16x16x32_bf16 v[200:203], v[56:59], a[128:131], v[200:203]\n"
  "v_exp_f32 v52, v52\n"
  "v_mfma_f32_16x16x32_bf16 v[204:207], v[56:59], a[132:135], v[204:207]\n"
  "v_exp_f32 v53, v53\n"
  "v_mfma_f32_16x16x32_bf16 v[168:171], v[40:43], a[128:131], v[168:171]\n"
  "v_fma_f32 v54, v54, s73, v226\n"
  "v_fma_f32 v55, v55, s73, v226\n"
  "v_mfma_f32_16x16x32_bf16 v[172:175], v[40:43], a[132:135], v[172:175]\n"
  "s_waitcnt vmcnt(11) lgkmcnt(0)\n"
  "s_barrier\n"
  "ds_read_b128 a[120:123], v216 offset:0\n"
  "v_mfma_f32_16x16x32_bf16 v[136:139], v[24:27], a[128:131], v[136:139]\n"
  "ds_read_b64 a[160:161], v231 offset:2048\n"
  "v_add_f32 v252, v252, v4\n"
  "v_mfma_f32_16x16x32_bf16 v[140:143], v[24:27], a[132:135], v[140:143]\n"
  "ds_read_b64 a[162:163], v232 offset:2048\n"
  "v_add_f32 v252, v252, v5\n"
  "v_mfma_f32_16x16x32_bf16 v[104:107], v[8:11], a[128:131], v[104:107]\n"
  "ds_read_b128 a[124:127], v216 offset:64\n"
  "v_add_f32 v252, v252, v6\n"
  "v_mfma_f32_16x16x32_bf16 v[108:111], v[8:11], a[132:135], v[108:111]\n"
  "ds_read_b128 a[128:131], v216 offset:1024\n"
  "v_add_f32 v252, v252, v7\n"
  "v_mfma_f32_16x16x32_bf16 v[112:115], v[8:11], a[148:151], v[112:115]\n"
  "ds_read_b128 a[132:135], v216 offset:1088\n"
  "v_add_f32 v253, v253, v20\n"
  "v_mfma_f32_16x16x32_bf16 v[116:119], v[8:11], a[144:147], v[116:119]\n"
  "s_mov_b32 m0, s68\n"
  "ds_read_b128 a[136:139], v216 offset:2048\n"
  "v_add_f32 v253, v253, v21\n"
  "v_mfma_f32_16x16x32_bf16 v[144:147], v[24:27], a[148:151], v[144:147]\n"
  "ds_read_b128 a[140:143], v216 offset:2112\n"
  "v_add_f32 v253, v253, v22\n"
  "v_mfma_f32_16x16x32_bf16 v[148:151], v[24:27], a[144:147], v[148:151]\n"
  "v_exp_f32 v54, v54\n"
  "v_mfma_f32_16x16x32_bf16 v[176:179], v[40:43], a[148:151], v[176:179]\n"
  "v_exp_f32 v55, v55\n"
  "v_mfma_f32_16x16x32_bf16 v[180:183], v[40:43], a[144:147], v[180:183]\n"
  "buffer_load_dwordx4 v221, s[40:43], 0 offen offset:0 lds\n"
  "v_mfma_f32_16x16x32_bf16 v[208:211], v[56:59], a[148:151], v[208:211]\n"
  "s_addk_i32 m0, 896\n"
  "v_add_f32 v253, v253, v23\n"
  "v_add_f32 v254, v254, v36\n"
  "v_mfma_f32_16x16x32_bf16 v[212:215], v[56:59], a[144:147], v[212:215]\n"
  "s_waitcnt lgkmcnt(4)\n"
  "v_add_f32 v254, v254, v37\n"
  "v_add_f32 v254, v254, v38\n"
  "v_mfma_f32_16x16x32_bf16 v[64:67], a[120:123], a[96:99], v[248:251]\n"
  "v_add_f32 v254, v254, v39\n"
  "v_add_f32 v255, v255, v52\n"
  "v_mfma_f32_16x16x32_bf16 v[64:67], a[124:127], a[100:103], v[64:67]\n"
  "s_waitcnt lgkmcnt(2)\n"
  "buffer_load_dwordx4 v221, s[40:43], 0 offen offset:128 lds\n"
  "v_add_f32 v255, v255, v53\n"
  "v_mfma_f32_16x16x32_bf16 v[64:67], a[128:131], a[104:107], v[64:67]\n"
  "s_addk_i32 m0, 896\n"
  "v_add_f32 v255, v255, v54\n"
  "v_add_f32 v255, v255, v55\n"
  "v_mfma_f32_16x16x32_bf16 v[64:67], a[132:135], a[108:111], v[64:67]\n"
  "s_waitcnt lgkmcnt(0)\n"
  "v_cvt_pk_bf16_f32 v14, v4, v5\n"
  "v_cvt_pk_bf16_f32 v15, v6, v7\n"
  "v_mfma_f32_16x16x32_bf16 v[64:67], a[136:139], a[112:115], v[64:67]\n"
  "v_cvt_pk_bf16_f32 v28, v16, v17\n"
  "v_cvt_pk_bf16_f32 v29, v18, v19\n"
  "v_mfma_f32_16x16x32_bf16 v[64:67], a[140:143], a[116:119], v[64:67]\n"
  "buffer_load_dwordx4 v221, s[40:43], 0 offen offset:256 lds\n"
  "v_cvt_pk_bf16_f32 v30, v20, v21\n"
  "v_mfma_f32_16x16x32_bf16 v[0:3], a[140:143], a[20:23], v[248:251]\n"
  "v_cvt_pk_bf16_f32 v31, v22, v23\n"
  "v_cvt_pk_bf16_f32 v44, v32, v33\n"
  "v_mfma_f32_16x16x32_bf16 v[0:3], a[136:139], a[16:19], v[0:3]\n"
  "v_cvt_pk_bf16_f32 v45, v34, v35\n"
  "v_cvt_pk_bf16_f32 v46, v36, v37\n"
  "v_mfma_f32_16x16x32_bf16 v[0:3], a[132:135], a[12:15], v[0:3]\n"
  "v_cvt_pk_bf16_f32 v47, v38, v39\n"
  "v_cvt_pk_bf16_f32 v60, v48, v49\n"
  "v_mfma_f32_16x16x32_bf16 v[0:3], a[128:131], a[8:11], v[0:3]\n"
  "v_cvt_pk_bf16_f32 v61, v50, v51\n"
  "v_cvt_pk_bf16_f32 v62, v52, v53\n"
  "v_mfma_f32_16x16x32_bf16 v[0:3], a[124:127], a[4:7], v[0:3]\n"
  "v_cvt_pk_bf16_f32 v63, v54, v55\n"
  "v_mfma_f32_16x16x32_bf16 v[0:3], a[120:123], a[0:3], v[0:3]\n"
  "s_add_u32 s40, s40, 24576\n"
  "s_addc_u32 s41, s41, 0\n"
  "s_branch L_BIG_%=\n"
  ".p2align 5\n"
  "L_TAIL_%=:\n"
  "ds_read_b64_tr_b16 a[120:121], v217 offset:26752\n"
  "ds_read_b64_tr_b16 a[122:123], v218 offset:26752\n"
  "v_exp_f32_e64 v55, v55\n"
  "v_cvt_pk_bf16_f32 v45, v34, v35\n"
  "ds_read_b64_tr_b16 a[124:125], v217 offset:26768\n"
  "ds_read_b64_tr_b16 a[126:127], v218 offset:26768\n"
  "v_cvt_pk_bf16_f32 v46, v36, v37\n"
  "v_cvt_pk_bf16_f32 v47, v38, v39\n"
  "v_cvt_pk_bf16_f32 v60, v48, v49\n"
  "s_waitcnt lgkmcnt(8)\n"
  "ds_read_b64_tr_b16 a[128:129], v219 offset:26752\n"
  "ds_read_b64_tr_b16 a[130:131], v220 offset:26752\n"
  "v_cvt_pk_bf16_f32 v61, v50, v51\n"
  "v_cvt_pk_bf16_f32 v62, v52, v53\n"
  "v_cvt_pk_bf16_f32 v63, v54, v55\n"
  "v_mfma_f32_16x16x32_bf16 v[84:87], a[156:159], a[152:155], v[84:87]\n"
  "s_nop 0\n"
  "ds_read_b64_tr_b16 a[132:133], v219 offset:26768\n"
  "ds_read_b64_tr_b16 a[134:135], v220 offset:26768\n"
  "v_add_u32 v233, s61, v229\n"
  "s_mov_b32 exec_lo, 0\n"
  "v_add_u32 v233, s69, v233\n"
  "s_mov_b32 exec_lo, -1\n"
  "v_mfma_f32_16x16x32_bf16 v[84:87], a[160:163], a[152:155], v[84:87]\n"
  "ds_read_b64_tr_b16 a[136:137], v217 offset:24704\n"
  "ds_read_b64_tr_b16 a[138:139], v218 offset:24704\n"
  "v_mfma_f32_16x16x32_bf16 v[68:71], v[12:15], a[152:155], v[68:71]\n"
  "ds_read_b64_tr_b16 a[140:141], v217 offset:25728\n"
  "ds_read_b64_tr_b16 a[142:143], v218 offset:25728\n"
  "v_mfma_f32_16x16x32_bf16 v[72:75], v[28:31], a[152:155], v[72:75]\n"
  "ds_read_b64_tr_b16 a[144:145], v217 offset:24720\n"
  "ds_read_b64_tr_b16 a[146:147], v218 offset:24720\n"
  "v_mfma_f32_16x16x32_bf16 v[76:79], v[44:47], a[152:155], v[76:79]\n"
  "ds_read_b64_tr_b16 a[148:149], v217 offset:25744\n"
  "ds_read_b64_tr_b16 a[150:151], v218 offset:25744\n"
  "v_mfma_f32_16x16x32_bf16 v[80:83], v[60:63], a[152:155], v[80:83]\n"
  "s_waitcnt lgkmcnt(12)\n"
  "v_maximum3_f32 v236, v68, v69, v70\n"
  "v_rcp_f32 v68, v68\n"
  "v_rcp_f32_e64 v69, v69\n"
  "v_mfma_f32_16x16x32_bf16 a[244:247], a[160:163], a[120:123], a[244:247]\n"
  "v_maximum3_f32 v236, v236, v71, v72\n"
  "v_rcp_f32 v70, v70\n"
  "v_rcp_f32 v71, v71\n"
  "v_mfma_f32_16x16x32_bf16 a[248:251], a[160:163], a[124:127], a[248:251]\n"
  "v_maximum3_f32 v236, v236, v73, v74\n"
  "v_rcp_f32 v72, v72\n"
  "v_rcp_f32 v73, v73\n"
  "v_mfma_f32_16x16x32_bf16 v[104:107], v[12:15], a[120:123], v[104:107]\n"
  "v_maximum3_f32 v236, v236, v75, v76\n"
  "v_rcp_f32 v74, v74\n"
  "v_rcp_f32 v75, v75\n"
  "v_mfma_f32_16x16x32_bf16 v[108:111], v[12:15], a[124:127], v[108:111]\n"
  "s_waitcnt lgkmcnt(8)\n"
  "v_maximum3_f32 v236, v236, v77, v78\n"
  "v_rcp_f32 v76, v76\n"
  "v_rcp_f32 v77, v77\n"
  "v_mfma_f32_16x16x32_bf16 v[112:115], v[12:15], a[128:131], v[112:115]\n"
  "v_maximum3_f32 v236, v236, v79, v80\n"
  "v_rcp_f32 v78, v78\n"
  "v_rcp_f32_e64 v79, v79\n"
  "v_mfma_f32_16x16x32_bf16 v[116:119], v[12:15], a[132:135], v[116:119]\n"
  "v_maximum3_f32 v236, v236, v81, v82\n"
  "v_rcp_f32 v80, v80\n"
  "v_rcp_f32 v81, v81\n"
  "v_pk_fma_f32 v[104:105], v[104:105], v[68:69], v[68:69] neg_lo:[0,0,1] neg_hi:[0,0,1]\n"
  "v_pk_fma_f32 v[106:107], v[106:107], v[70:71], v[70:71] neg_lo:[0,0,1] neg_hi:[0,0,1]\n"
  "v_mfma_f32_16x16x32_bf16 v[136:139], v[28:31], a[120:123], v[136:139]\n"
  "v_maximum3_f32 v236, v236, v83, v84\n"
  "v_rcp_f32 v82, v82\n"
  "v_rcp_f32 v83, v83\n"
  "v_pk_fma_f32 v[108:109], v[108:109], v[68:69], v[68:69] neg_lo:[0,0,1] neg_hi:[0,0,1]\n"
  "v_pk_fma_f32 v[110:111], v[110:111], v[70:71], v[70:71] neg_lo:[0,0,1] neg_hi:[0,0,1]\n"
  "v_mfma_f32_16x16x32_bf16 v[140:143], v[28:31], a[124:127], v[140:143]\n"
  "v_cvt_pk_bf16_f32 v104, v104, v108\n"
  "v_cvt_pk_bf16_f32 v108, v105, v109\n"
  "v_cvt_pk_bf16_f32 v106, v106, v110\n"
  "v_cvt_pk_bf16_f32 v110, v107, v111\n"
  "v_maximum3_f32 v236, v236, v85, v86\n"
  "v_pk_fma_f32 v[112:113], v[112:113], v[68:69], v[68:69] neg_lo:[0,0,1] neg_hi:[0,0,1]\n"
  "v_pk_fma_f32 v[114:115], v[114:115], v[70:71], v[70:71] neg_lo:[0,0,1] neg_hi:[0,0,1]\n"
  "v_mfma_f32_16x16x32_bf16 v[144:147], v[28:31], a[128:131], v[144:147]\n"
  "v_maximum3_f32 v236, v236, v87, v87\n"
  "v_rcp_f32_e64 v84, v84\n"
  "v_rcp_f32_e64 v85, v85\n"
  "v_pk_fma_f32 v[116:117], v[116:117], v[68:69], v[68:69] neg_lo:[0,0,1] neg_hi:[0,0,1]\n"
  "v_pk_fma_f32 v[118:119], v[118:119], v[70:71], v[70:71] neg_lo:[0,0,1] neg_hi:[0,0,1]\n"
  "v_mfma_f32_16x16x32_bf16 v[148:151], v[28:31], a[132:135], v[148:151]\n"
  "v_cvt_pk_bf16_f32 v105, v112, v116\n"
  "v_cvt_pk_bf16_f32 v109, v113, v117\n"
  "v_cvt_pk_bf16_f32 v107, v114, v118\n"
  "v_cvt_pk_bf16_f32 v111, v115, v119\n"
  "v_cmp_ngt_f32_e64 vcc, s75, v236\n"
  "v_pk_fma_f32 v[136:137], v[136:137], v[72:73], v[72:73] neg_lo:[0,0,1] neg_hi:[0,0,1]\n"
  "v_pk_fma_f32 v[138:139], v[138:139], v[74:75], v[74:75] neg_lo:[0,0,1] neg_hi:[0,0,1]\n"
  "v_mfma_f32_16x16x32_bf16 v[168:171], v[44:47], a[120:123], v[168:171]\n"
  "ds_write_b64 v229, v[104:105] offset:8960\n"
  "v_rcp_f32_e64 v86, v86\n"
  "v_pk_fma_f32 v[140:141], v[140:141], v[72:73], v[72:73] neg_lo:[0,0,1] neg_hi:[0,0,1]\n"
  "v_pk_fma_f32 v[142:143], v[142:143], v[74:75], v[74:75] neg_lo:[0,0,1] neg_hi:[0,0,1]\n"
  "v_mfma_f32_16x16x32_bf16 v[172:175], v[44:47], a[124:127], v[172:175]\n"
  "s_cmp_lg_u64 vcc, 0\n"
  "s_cselect_b32 s79, s77, 0\n"
  "s_or_b32 s78, s78, s79\n"
  "s_lshl_b32 s77, s77, 1\n"
  "ds_write_b64 v229, v[108:109] offset:9248\n"
  "v_cvt_pk_bf16_f32 v136, v136, v140\n"
  "v_cvt_pk_bf16_f32 v140, v137, v141\n"
  "v_pk_fma_f32 v[144:145], v[144:145], v[72:73], v[72:73] neg_lo:[0,0,1] neg_hi:[0,0,1]\n"
  "v_pk_fma_f32 v[146:147], v[146:147], v[74:75], v[74:75] neg_lo:[0,0,1] neg_hi:[0,0,1]\n"
  "v_mfma_f32_16x16x32_bf16 v[176:179], v[44:47], a[128:131], v[176:179]\n"
  "ds_write_b64 v229, v[106:107] offset:9536\n"
  "v_cvt_pk_bf16_f32 v138, v138, v142\n"
  "v_cvt_pk_bf16_f32 v142, v139, v143\n"
  "v_pk_fma_f32 v[148:149], v[148:149], v[72:73], v[72:73] neg_lo:[0,0,1] neg_hi:[0,0,1]\n"
  "v_pk_fma_f32 v[150:151], v[150:151], v[74:75], v[74:75] neg_lo:[0,0,1] neg_hi:[0,0,1]\n"
  "v_mfma_f32_16x16x32_bf16 v[180:183], v[44:47], a[132:135], v[180:183]\n"
  "ds_write_b64 v229, v[110:111] offset:9824\n"
  "v_cvt_pk_bf16_f32 v137, v144, v148\n"
  "v_cvt_pk_bf16_f32 v141, v145, v149\n"
  "v_pk_fma_f32 v[168:169], v[168:169], v[76:77], v[76:77] neg_lo:[0,0,1] neg_hi:[0,0,1]\n"
  "v_pk_fma_f32 v[170:171], v[170:171], v[78:79], v[78:79] neg_lo:[0,0,1] neg_hi:[0,0,1]\n"
  "v_mfma_f32_16x16x32_bf16 v[200:203], v[60:63], a[120:123], v[200:203]\n"
  "ds_read_b64_tr_b16 a[120:121], v219 offset:24704\n"
  "ds_read_b64_tr_b16 a[122:123], v220 offset:24704\n"
  "ds_write_b64 v229, v[136:137] offset:11200\n"
  "v_pk_fma_f32 v[172:173], v[172:173], v[76:77], v[76:77] neg_lo:[0,0,1] neg_hi:[0,0,1]\n"
  "v_pk_fma_f32 v[174:175], v[174:175], v[78:79], v[78:79] neg_lo:[0,0,1] neg_hi:[0,0,1]\n"
  "v_mfma_f32_16x16x32_bf16 v[204:207], v[60:63], a[124:127], v[204:207]\n"
  "ds_read_b64_tr_b16 a[124:125], v219 offset:25728\n"
  "ds_read_b64_tr_b16 a[126:127], v220 offset:25728\n"
  "ds_write_b64 v229, v[140:141] offset:11488\n"
  "v_pk_fma_f32 v[176:177], v[176:177], v[76:77], v[76:77] neg_lo:[0,0,1] neg_hi:[0,0,1]\n"
  "v_pk_fma_f32 v[178:179], v[178:179], v[78:79], v[78:79] neg_lo:[0,0,1] neg_hi:[0,0,1]\n"
  "v_mfma_f32_16x16x32_bf16 v[208:211], v[60:63], a[128:131], v[208:211]\n"
  "ds_read_b64_tr_b16 a[128:129], v219 offset:24720\n"
  "ds_read_b64_tr_b16 a[130:131], v220 offset:24720\n"
  "ds_read_b64_tr_b16 v[244:245], v230 offset:8960\n"
  "ds_read_b64_tr_b16 v[246:247], v230 offset:8992\n"
  "v_cvt_pk_bf16_f32 v139, v146, v150\n"
  "v_pk_fma_f32 v[180:181], v[180:181], v[76:77], v[76:77] neg_lo:[0,0,1] neg_hi:[0,0,1]\n"
  "v_pk_fma_f32 v[182:183], v[182:183], v[78:79], v[78:79] neg_lo:[0,0,1] neg_hi:[0,0,1]\n"
  "v_mfma_f32_16x16x32_bf16 v[212:215], v[60:63], a[132:135], v[212:215]\n"
  "s_nop 0\n"
  "s_waitcnt lgkmcnt(15)\n"
  "ds_read_b64_tr_b16 a[132:133], v219 offset:25744\n"
  "ds_read_b64_tr_b16 a[134:135], v220 offset:25744\n"
  "ds_write_b64 v229, v[138:139] offset:11776\n"
  "v_pk_fma_f32 v[200:201], v[200:201], v[80:81], v[80:81] neg_lo:[0,0,1] neg_hi:[0,0,1]\n"
  "v_pk_fma_f32 v[202:203], v[202:203], v[82:83], v[82:83] neg_lo:[0,0,1] neg_hi:[0,0,1]\n"
  "v_mfma_f32_16x16x32_bf16 a[228:231], a[156:159], a[136:139], a[228:231]\n"
  "ds_read_b64_tr_b16 a[252:253], v230 offset:10080\n"
  "ds_read_b64_tr_b16 a[254:255], v230 offset:10112\n"
  "v_cvt_pk_bf16_f32 v143, v147, v151\n"
  "v_rcp_f32 v87, v87\n"
  "v_pk_fma_f32 v[204:205], v[204:205], v[80:81], v[80:81] neg_lo:[0,0,1] neg_hi:[0,0,1]\n"
  "v_pk_fma_f32 v[206:207], v[206:207], v[82:83], v[82:83] neg_lo:[0,0,1] neg_hi:[0,0,1]\n"
  "v_mfma_f32_16x16x32_bf16 a[228:231], a[160:163], a[140:143], a[228:231]\n"
  "s_waitcnt lgkmcnt(15)\n"
  "ds_write_b64 v229, v[142:143] offset:12064\n"
  "v_cvt_pk_bf16_f32 v168, v168, v172\n"
  "v_cvt_pk_bf16_f32 v172, v169, v173\n"
  "v_pk_fma_f32 v[208:209], v[208:209], v[80:81], v[80:81] neg_lo:[0,0,1] neg_hi:[0,0,1]\n"
  "v_pk_fma_f32 v[210:211], v[210:211], v[82:83], v[82:83] neg_lo:[0,0,1] neg_hi:[0,0,1]\n"
  "v_mfma_f32_16x16x32_bf16 a[232:235], a[156:159], a[144:147], a[232:235]\n"
  "v_cvt_pk_bf16_f32 v169, v176, v180\n"
  "v_cvt_pk_bf16_f32 v173, v177, v181\n"
  "v_cvt_pk_bf16_f32 v170, v170, v174\n"
  "v_cvt_pk_bf16_f32 v174, v171, v175\n"
  "v_cvt_pk_bf16_f32 v171, v178, v182\n"
  "v_pk_fma_f32 v[212:213], v[212:213], v[80:81], v[80:81] neg_lo:[0,0,1] neg_hi:[0,0,1]\n"
  "v_pk_fma_f32 v[214:215], v[214:215], v[82:83], v[82:83] neg_lo:[0,0,1] neg_hi:[0,0,1]\n"
  "v_mfma_f32_16x16x32_bf16 a[232:235], a[160:163], a[148:151], a[232:235]\n"
  "ds_read_b64_tr_b16 v[0:1], v230 offset:11200\n"
  "ds_read_b64_tr_b16 v[2:3], v230 offset:11232\n"
  "ds_write_b64 v229, v[168:169] offset:13440\n"
  "v_mfma_f32_16x16x32_bf16 a[164:167], v[8:11], a[136:139], a[164:167]\n"
  "s_waitcnt lgkmcnt(9)\n"
  "buffer_store_dwordx4 v[244:247], v228, s[52:55], s80 offen offset:256 nt\n"
  "ds_write_b64 v229, v[172:173] offset:13728\n"
  "v_cvt_pk_bf16_f32 v175, v179, v183\n"
  "v_mfma_f32_16x16x32_bf16 a[164:167], v[12:15], a[140:143], a[164:167]\n"
  "ds_read_b64_tr_b16 v[4:5], v230 offset:12320\n"
  "ds_read_b64_tr_b16 v[6:7], v230 offset:12352\n"
  "ds_write_b64 v229, v[170:171] offset:14016\n"
  "v_mfma_f32_16x16x32_bf16 a[168:171], v[8:11], a[144:147], a[168:171]\n"
  "s_waitcnt lgkmcnt(8)\n"
  "buffer_store_dwordx4 a[252:255], v228, s[52:55], s80 offen offset:3328 nt\n"
  "ds_write_b64 v229, v[174:175] offset:14304\n"
  "v_cvt_pk_bf16_f32 v200, v200, v204\n"
  "v_mfma_f32_16x16x32_bf16 a[168:171], v[12:15], a[148:151], a[168:171]\n"
  "v_cvt_pk_bf16_f32 v204, v201, v205\n"
  "v_cvt_pk_bf16_f32 v201, v208, v212\n"
  "v_cvt_pk_bf16_f32 v205, v209, v213\n"
  "v_cvt_pk_bf16_f32 v202, v202, v206\n"
  "v_cvt_pk_bf16_f32 v206, v203, v207\n"
  "v_mfma_f32_16x16x32_bf16 a[180:183], v[24:27], a[136:139], a[180:183]\n"
  "ds_read_b64_tr_b16 v[16:17], v230 offset:13440\n"
  "ds_read_b64_tr_b16 v[18:19], v230 offset:13472\n"
  "ds_write_b64 v229, v[200:201] offset:15680\n"
  "v_mfma_f32_16x16x32_bf16 a[180:183], v[28:31], a[140:143], a[180:183]\n"
  "s_waitcnt lgkmcnt(9)\n"
  "buffer_store_dwordx4 v[0:3], v228, s[52:55], s81 offen offset:256 nt\n"
  "ds_write_b64 v229, v[204:205] offset:15968\n"
  "v_cvt_pk_bf16_f32 v203, v210, v214\n"
  "v_mfma_f32_16x16x32_bf16 a[184:187], v[24:27], a[144:147], a[184:187]\n"
  "ds_read_b64_tr_b16 v[20:21], v230 offset:14560\n"
  "ds_write_b64 v229, v[202:203] offset:16256\n"
  "v_cvt_pk_bf16_f32 v207, v211, v215\n"
  "v_mfma_f32_16x16x32_bf16 a[184:187], v[28:31], a[148:151], a[184:187]\n"
  "s_waitcnt lgkmcnt(8)\n"
  "buffer_store_dwordx4 v[4:7], v228, s[52:55], s81 offen offset:3328 nt\n"
  "ds_read_b64_tr_b16 v[22:23], v230 offset:14592\n"
  "ds_write_b64 v229, v[206:207] offset:16544\n"
  "v_mfma_f32_16x16x32_bf16 a[196:199], v[40:43], a[136:139], a[196:199]\n"
  "v_accvgpr_read_b32 v104, a164\n"
  "v_accvgpr_read_b32 v105, a165\n"
  "v_accvgpr_read_b32 v108, a168\n"
  "v_accvgpr_read_b32 v109, a169\n"
  "v_accvgpr_read_b32 v106, a166\n"
  "v_pk_fma_f32 v[104:105], v[104:105], v[68:69], v[68:69] neg_lo:[0,0,1] neg_hi:[0,0,1]\n"
  "v_pk_fma_f32 v[108:109], v[108:109], v[68:69], v[68:69] neg_lo:[0,0,1] neg_hi:[0,0,1]\n"
  "v_mfma_f32_16x16x32_bf16 a[196:199], v[44:47], a[140:143], a[196:199]\n"
  "ds_read_b64_tr_b16 v[244:245], v230 offset:15680\n"
  "ds_read_b64_tr_b16 v[246:247], v230 offset:15712\n"
  "v_accvgpr_read_b32 v107, a167\n"
  "v_accvgpr_read_b32 v110, a170\n"
  "v_accvgpr_read_b32 v111, a171\n"
  "v_pk_fma_f32 v[106:107], v[106:107], v[70:71], v[70:71] neg_lo:[0,0,1] neg_hi:[0,0,1]\n"
  "v_pk_fma_f32 v[110:111], v[110:111], v[70:71], v[70:71] neg_lo:[0,0,1] neg_hi:[0,0,1]\n"
  "v_mfma_f32_16x16x32_bf16 a[200:203], v[40:43], a[144:147], a[200:203]\n"
  "s_nop 0\n"
  "s_waitcnt lgkmcnt(8)\n"
  "buffer_store_dwordx4 v[16:19], v228, s[52:55], s82 offen offset:256 nt\n"
  "v_cvt_pk_bf16_f32 v104, v104, v108\n"
  "v_cvt_pk_bf16_f32 v108, v105, v109\n"
  "v_cvt_pk_bf16_f32 v106, v106, v110\n"
  "v_cvt_pk_bf16_f32 v110, v107, v111\n"
  "v_mfma_f32_16x16x32_bf16 a[200:203], v[44:47], a[148:151], a[200:203]\n"
  "ds_read_b64_tr_b16 a[252:253], v230 offset:16800\n"
  "ds_read_b64_tr_b16 a[254:255], v230 offset:16832\n"
  "v_accvgpr_read_b32 v136, a180\n"
  "v_accvgpr_read_b32 v137, a181\n"
  "v_accvgpr_read_b32 v140, a184\n"
  "v_pk_fma_f32 v[136:137], v[136:137], v[72:73], v[72:73] neg_lo:[0,0,1] neg_hi:[0,0,1]\n"
  "v_mfma_f32_16x16x32_bf16 a[212:215], v[56:59], a[136:139], a[212:215]\n"
  "v_accvgpr_read_b32 v141, a185\n"
  "v_accvgpr_read_b32 v138, a182\n"
  "v_accvgpr_read_b32 v139, a183\n"
  "v_accvgpr_read_b32 v142, a186\n"
  "v_accvgpr_read_b32 v143, a187\n"
  "v_pk_fma_f32 v[140:141], v[140:141], v[72:73], v[72:73] neg_lo:[0,0,1] neg_hi:[0,0,1]\n"
  "v_pk_fma_f32 v[138:139], v[138:139], v[74:75], v[74:75] neg_lo:[0,0,1] neg_hi:[0,0,1]\n"
  "v_pk_fma_f32 v[142:143], v[142:143], v[74:75], v[74:75] neg_lo:[0,0,1] neg_hi:[0,0,1]\n"
  "v_mfma_f32_16x16x32_bf16 a[212:215], v[60:63], a[140:143], a[212:215]\n"
  "s_nop 0\n"
  "s_waitcnt lgkmcnt(5)\n"
  "buffer_store_dwordx4 v[20:23], v228, s[52:55], s82 offen offset:3328 nt\n"
  "v_cvt_pk_bf16_f32 v136, v136, v140\n"
  "v_cvt_pk_bf16_f32 v140, v137, v141\n"
  "v_cvt_pk_bf16_f32 v138, v138, v142\n"
  "v_cvt_pk_bf16_f32 v142, v139, v143\n"
  "v_mfma_f32_16x16x32_bf16 a[216:219], v[56:59], a[144:147], a[216:219]\n"
  "v_accvgpr_read_b32 v168, a196\n"
  "v_accvgpr_read_b32 v169, a197\n"
  "v_accvgpr_read_b32 v172, a200\n"
  "v_accvgpr_read_b32 v173, a201\n"
  "v_accvgpr_read_b32 v170, a198\n"
  "v_pk_fma_f32 v[168:169], v[168:169], v[76:77], v[76:77] neg_lo:[0,0,1] neg_hi:[0,0,1]\n"
  "v_pk_fma_f32 v[172:173], v[172:173], v[76:77], v[76:77] neg_lo:[0,0,1] neg_hi:[0,0,1]\n"
  "v_mfma_f32_16x16x32_bf16 a[216:219], v[60:63], a[148:151], a[216:219]\n"
  "s_nop 0\n"
  "s_waitcnt lgkmcnt(2)\n"
  "buffer_store_dwordx4 v[244:247], v228, s[52:55], s83 offen offset:256 nt\n"
  "v_accvgpr_read_b32 v171, a199\n"
  "v_accvgpr_read_b32 v174, a202\n"
  "v_accvgpr_read_b32 v175, a203\n"
  "v_cvt_pk_bf16_f32 v168, v168, v172\n"
  "v_pk_fma_f32 v[170:171], v[170:171], v[78:79], v[78:79] neg_lo:[0,0,1] neg_hi:[0,0,1]\n"
  "v_pk_fma_f32 v[174:175], v[174:175], v[78:79], v[78:79] neg_lo:[0,0,1] neg_hi:[0,0,1]\n"
  "v_mfma_f32_16x16x32_bf16 a[172:175], v[8:11], a[120:123], a[172:175]\n"
  "s_waitcnt vmcnt(10)\n"
  "s_barrier\n"
  "ds_read_b64_tr_b16 a[136:137], v217 offset:0\n"
  "ds_read_b64_tr_b16 a[138:139], v218 offset:0\n"
  "v_cvt_pk_bf16_f32 v172, v169, v173\n"
  "v_mfma_f32_16x16x32_bf16 a[172:175], v[12:15], a[124:127], a[172:175]\n"
  "s_nop 0\n"
  "s_waitcnt lgkmcnt(2)\n"
  "buffer_store_dwordx4 a[252:255], v228, s[52:55], s83 offen offset:3328 nt\n"
  "ds_read_b64_tr_b16 a[140:141], v217 offset:16\n"
  "ds_read_b64_tr_b16 a[142:143], v218 offset:16\n"
  "v_cvt_pk_bf16_f32 v170, v170, v174\n"
  "v_cvt_pk_bf16_f32 v174, v171, v175\n"
  "v_mfma_f32_16x16x32_bf16 a[176:179], v[8:11], a[128:131], a[176:179]\n"
  "ds_read_b64_tr_b16 a[144:145], v219 offset:0\n"
  "ds_read_b64_tr_b16 a[146:147], v220 offset:0\n"
  "v_accvgpr_read_b32 v240, a244\n"
  "v_accvgpr_read_b32 v241, a245\n"
  "v_accvgpr_read_b32 v242, a248\n"
  "v_pk_fma_f32 v[240:241], v[240:241], v[84:85], v[84:85] neg_lo:[0,0,1] neg_hi:[0,0,1]\n"
  "v_mfma_f32_16x16x32_bf16 a[176:179], v[12:15], a[132:135], a[176:179]\n"
  "ds_read_b64_tr_b16 a[148:149], v219 offset:16\n"
  "ds_read_b64_tr_b16 a[150:151], v220 offset:16\n"
  "v_accvgpr_read_b32 v243, a249\n"
  "v_accvgpr_read_b32 v236, a246\n"
  "v_accvgpr_read_b32 v237, a247\n"
  "v_pk_fma_f32 v[242:243], v[242:243], v[84:85], v[84:85] neg_lo:[0,0,1] neg_hi:[0,0,1]\n"
  "v_pk_fma_f32 v[236:237], v[236:237], v[86:87], v[86:87] neg_lo:[0,0,1] neg_hi:[0,0,1]\n"
  "v_mfma_f32_16x16x32_bf16 a[188:191], v[24:27], a[120:123], a[188:191]\n"
  "v_accvgpr_read_b32 v112, a172\n"
  "v_accvgpr_read_b32 v113, a173\n"
  "v_accvgpr_read_b32 v114, a174\n"
  "v_accvgpr_read_b32 v115, a175\n"
  "v_accvgpr_read_b32 v238, a250\n"
  "v_pk_fma_f32 v[112:113], v[112:113], v[68:69], v[68:69] neg_lo:[0,0,1] neg_hi:[0,0,1]\n"
  "v_pk_fma_f32 v[114:115], v[114:115], v[70:71], v[70:71] neg_lo:[0,0,1] neg_hi:[0,0,1]\n"
  "v_mfma_f32_16x16x32_bf16 a[188:191], v[28:31], a[124:127], a[188:191]\n"
  "v_accvgpr_read_b32 v239, a251\n"
  "v_accvgpr_read_b32 v200, a212\n"
  "v_accvgpr_read_b32 v201, a213\n"
  "v_accvgpr_read_b32 v204, a216\n"
  "v_accvgpr_read_b32 v205, a217\n"
  "v_pk_fma_f32 v[238:239], v[238:239], v[86:87], v[86:87] neg_lo:[0,0,1] neg_hi:[0,0,1]\n"
  "v_pk_fma_f32 v[200:201], v[200:201], v[80:81], v[80:81] neg_lo:[0,0,1] neg_hi:[0,0,1]\n"
  "v_pk_fma_f32 v[204:205], v[204:205], v[80:81], v[80:81] neg_lo:[0,0,1] neg_hi:[0,0,1]\n"
  "v_mfma_f32_16x16x32_bf16 a[192:195], v[24:27], a[128:131], a[192:195]\n"
  "v_accvgpr_read_b32 v116, a176\n"
  "v_accvgpr_read_b32 v117, a177\n"
  "v_accvgpr_read_b32 v118, a178\n"
  "v_accvgpr_read_b32 v119, a179\n"
  "v_accvgpr_read_b32 v202, a214\n"
  "v_pk_fma_f32 v[116:117], v[116:117], v[68:69], v[68:69] neg_lo:[0,0,1] neg_hi:[0,0,1]\n"
  "v_pk_fma_f32 v[118:119], v[118:119], v[70:71], v[70:71] neg_lo:[0,0,1] neg_hi:[0,0,1]\n"
  "v_mfma_f32_16x16x32_bf16 a[192:195], v[28:31], a[132:135], a[192:195]\n"
  "v_cvt_pk_bf16_f32 v105, v112, v116\n"
  "v_cvt_pk_bf16_f32 v109, v113, v117\n"
  "v_cvt_pk_bf16_f32 v107, v114, v118\n"
  "v_cvt_pk_bf16_f32 v111, v115, v119\n"
  "v_accvgpr_read_b32 v203, a215\n"
  "v_pk_fma_f32 v[202:203], v[202:203], v[82:83], v[82:83] neg_lo:[0,0,1] neg_hi:[0,0,1]\n"
  "v_mfma_f32_16x16x32_bf16 a[204:207], v[40:43], a[120:123], a[204:207]\n"
  "ds_write_b64 v229, v[104:105] offset:0\n"
  "v_accvgpr_read_b32 v144, a188\n"
  "v_accvgpr_read_b32 v145, a189\n"
  "v_pk_fma_f32 v[144:145], v[144:145], v[72:73], v[72:73] neg_lo:[0,0,1] neg_hi:[0,0,1]\n"
  "v_mfma_f32_16x16x32_bf16 a[204:207], v[44:47], a[124:127], a[204:207]\n"
  "ds_write_b64 v229, v[108:109] offset:288\n"
  "v_accvgpr_read_b32 v146, a190\n"
  "v_accvgpr_read_b32 v147, a191\n"
  "v_pk_fma_f32 v[146:147], v[146:147], v[74:75], v[74:75] neg_lo:[0,0,1] neg_hi:[0,0,1]\n"
  "v_mfma_f32_16x16x32_bf16 a[208:211], v[40:43], a[128:131], a[208:211]\n"
  "ds_write_b64 v229, v[106:107] offset:576\n"
  "ds_write_b64 v229, v[110:111] offset:864\n"
  "v_mfma_f32_16x16x32_bf16 a[208:211], v[44:47], a[132:135], a[208:211]\n"
  "v_accvgpr_read_b32 v148, a192\n"
  "v_accvgpr_read_b32 v149, a193\n"
  "v_accvgpr_read_b32 v150, a194\n"
  "v_accvgpr_read_b32 v151, a195\n"
  "v_accvgpr_read_b32 v206, a218\n"
  "v_pk_fma_f32 v[148:149], v[148:149], v[72:73], v[72:73] neg_lo:[0,0,1] neg_hi:[0,0,1]\n"
  "v_pk_fma_f32 v[150:151], v[150:151], v[74:75], v[74:75] neg_lo:[0,0,1] neg_hi:[0,0,1]\n"
  "v_mfma_f32_16x16x32_bf16 a[220:223], v[56:59], a[120:123], a[220:223]\n"
  "ds_read_b64_tr_b16 v[0:1], v230 offset:0\n"
  "ds_read_b64_tr_b16 v[2:3], v230 offset:32\n"
  "v_cvt_pk_bf16_f32 v137, v144, v148\n"
  "v_cvt_pk_bf16_f32 v141, v145, v149\n"
  "v_cvt_pk_bf16_f32 v139, v146, v150\n"
  "v_mfma_f32_16x16x32_bf16 a[220:223], v[60:63], a[124:127], a[220:223]\n"
  "ds_read_b64_tr_b16 v[4:5], v230 offset:1120\n"
  "ds_write_b64 v229, v[136:137] offset:2240\n"
  "v_cvt_pk_bf16_f32 v143, v147, v151\n"
  "v_mfma_f32_16x16x32_bf16 a[224:227], v[56:59], a[128:131], a[224:227]\n"
  "ds_read_b64_tr_b16 v[6:7], v230 offset:1152\n"
  "ds_write_b64 v229, v[140:141] offset:2528\n"
  "v_accvgpr_read_b32 v176, a204\n"
  "v_mfma_f32_16x16x32_bf16 a[224:227], v[60:63], a[132:135], a[224:227]\n"
  "s_nop 0\n"
  "s_waitcnt lgkmcnt(14)\n"
  "ds_write_b64 v229, v[138:139] offset:2816\n"
  "ds_write_b64 v229, v[142:143] offset:3104\n"
  "v_mfma_f32_16x16x32_bf16 a[236:239], a[160:163], a[136:139], a[236:239]\n"
  "v_accvgpr_read_b32 v177, a205\n"
  "v_accvgpr_read_b32 v180, a208\n"
  "v_accvgpr_read_b32 v181, a209\n"
  "v_accvgpr_read_b32 v178, a206\n"
  "v_accvgpr_read_b32 v179, a207\n"
  "v_pk_fma_f32 v[176:177], v[176:177], v[76:77], v[76:77] neg_lo:[0,0,1] neg_hi:[0,0,1]\n"
  "v_pk_fma_f32 v[180:181], v[180:181], v[76:77], v[76:77] neg_lo:[0,0,1] neg_hi:[0,0,1]\n"
  "v_pk_fma_f32 v[178:179], v[178:179], v[78:79], v[78:79] neg_lo:[0,0,1] neg_hi:[0,0,1]\n"
  "v_mfma_f32_16x16x32_bf16 a[240:243], a[160:163], a[140:143], a[240:243]\n"
  "ds_read_b64_tr_b16 v[16:17], v230 offset:2240\n"
  "ds_read_b64_tr_b16 v[18:19], v230 offset:2272\n"
  "v_accvgpr_read_b32 v182, a210\n"
  "v_accvgpr_read_b32 v183, a211\n"
  "v_cvt_pk_bf16_f32 v169, v176, v180\n"
  "v_pk_fma_f32 v[182:183], v[182:183], v[78:79], v[78:79] neg_lo:[0,0,1] neg_hi:[0,0,1]\n"
  "v_mfma_f32_16x16x32_bf16 v[88:91], v[12:15], a[136:139], v[88:91]\n"
  "s_waitcnt lgkmcnt(8)\n"
  "buffer_store_dwordx4 v[0:3], v228, s[52:55], s80 offen offset:0 nt\n"
  "ds_write_b64 v229, v[168:169] offset:4480\n"
  "v_cvt_pk_bf16_f32 v173, v177, v181\n"
  "v_mfma_f32_16x16x32_bf16 v[92:95], v[12:15], a[140:143], v[92:95]\n"
  "ds_write_b64 v229, v[172:173] offset:4768\n"
  "v_cvt_pk_bf16_f32 v171, v178, v182\n"
  "v_cvt_pk_bf16_f32 v175, v179, v183\n"
  "v_mfma_f32_16x16x32_bf16 v[96:99], v[12:15], a[144:147], v[96:99]\n"
  "s_waitcnt lgkmcnt(7)\n"
  "buffer_store_dwordx4 v[4:7], v228, s[52:55], s80 offen offset:3072 nt\n"
  "ds_read_b64_tr_b16 v[20:21], v230 offset:3360\n"
  "ds_read_b64_tr_b16 v[22:23], v230 offset:3392\n"
  "ds_write_b64 v229, v[170:171] offset:5056\n"
  "ds_write_b64 v229, v[174:175] offset:5344\n"
  "v_mfma_f32_16x16x32_bf16 v[100:103], v[12:15], a[148:151], v[100:103]\n"
  "v_accvgpr_read_b32 v208, a220\n"
  "v_accvgpr_read_b32 v209, a221\n"
  "v_accvgpr_read_b32 v212, a224\n"
  "v_accvgpr_read_b32 v213, a225\n"
  "v_accvgpr_read_b32 v207, a219\n"
  "v_pk_fma_f32 v[208:209], v[208:209], v[80:81], v[80:81] neg_lo:[0,0,1] neg_hi:[0,0,1]\n"
  "v_pk_fma_f32 v[212:213], v[212:213], v[80:81], v[80:81] neg_lo:[0,0,1] neg_hi:[0,0,1]\n"
  "v_pk_fma_f32 v[206:207], v[206:207], v[82:83], v[82:83] neg_lo:[0,0,1] neg_hi:[0,0,1]\n"
  "v_pk_fma_f32 v[88:89], v[88:89], v[68:69], v[68:69] neg_lo:[0,0,1] neg_hi:[0,0,1]\n"
  "v_mfma_f32_16x16x32_bf16 v[120:123], v[28:31], a[136:139], v[120:123]\n"
  "ds_read_b64_tr_b16 v[244:245], v230 offset:4480\n"
  "ds_read_b64_tr_b16 v[246:247], v230 offset:4512\n"
  "v_accvgpr_read_b32 v210, a222\n"
  "v_accvgpr_read_b32 v211, a223\n"
  "v_accvgpr_read_b32 v214, a226\n"
  "v_pk_fma_f32 v[210:211], v[210:211], v[82:83], v[82:83] neg_lo:[0,0,1] neg_hi:[0,0,1]\n"
  "v_pk_fma_f32 v[92:93], v[92:93], v[68:69], v[68:69] neg_lo:[0,0,1] neg_hi:[0,0,1]\n"
  "v_pk_fma_f32 v[90:91], v[90:91], v[70:71], v[70:71] neg_lo:[0,0,1] neg_hi:[0,0,1]\n"
  "v_pk_fma_f32 v[94:95], v[94:95], v[70:71], v[70:71] neg_lo:[0,0,1] neg_hi:[0,0,1]\n"
  "v_mfma_f32_16x16x32_bf16 v[124:127], v[28:31], a[140:143], v[124:127]\n"
  "s_nop 0\n"
  "s_waitcnt lgkmcnt(8)\n"
  "buffer_store_dwordx4 v[16:19], v228, s[52:55], s81 offen offset:0 nt\n"
  "v_accvgpr_read_b32 v215, a227\n"
  "v_cvt_pk_bf16_f32 v240, v240, v242\n"
  "v_cvt_pk_bf16_f32 v241, v241, v243\n"
  "v_pk_fma_f32 v[214:215], v[214:215], v[82:83], v[82:83] neg_lo:[0,0,1] neg_hi:[0,0,1]\n"
  "v_pk_fma_f32 v[96:97], v[96:97], v[68:69], v[68:69] neg_lo:[0,0,1] neg_hi:[0,0,1]\n"
  "v_pk_fma_f32 v[98:99], v[98:99], v[70:71], v[70:71] neg_lo:[0,0,1] neg_hi:[0,0,1]\n"
  "v_mfma_f32_16x16x32_bf16 v[128:131], v[28:31], a[144:147], v[128:131]\n"
  "v_cvt_pk_bf16_f32 v236, v236, v238\n"
  "v_cvt_pk_bf16_f32 v237, v237, v239\n"
  "v_cvt_pk_bf16_f32 v200, v200, v204\n"
  "v_cvt_pk_bf16_f32 v204, v201, v205\n"
  "v_cvt_pk_bf16_f32 v201, v208, v212\n"
  "v_cvt_pk_bf16_f32 v205, v209, v213\n"
  "v_cvt_pk_bf16_f32 v202, v202, v206\n"
  "v_cvt_pk_bf16_f32 v206, v203, v207\n"
  "v_cvt_pk_bf16_f32 v203, v210, v214\n"
  "v_cvt_pk_bf16_f32 v207, v211, v215\n"
  "v_pk_fma_f32 v[100:101], v[100:101], v[68:69], v[68:69] neg_lo:[0,0,1] neg_hi:[0,0,1]\n"
  "v_pk_fma_f32 v[102:103], v[102:103], v[70:71], v[70:71] neg_lo:[0,0,1] neg_hi:[0,0,1]\n"
  "v_mfma_f32_16x16x32_bf16 v[132:135], v[28:31], a[148:151], v[132:135]\n"
  "ds_read_b64_tr_b16 a[252:253], v230 offset:5600\n"
  "ds_read_b64_tr_b16 a[254:255], v230 offset:5632\n"
  "ds_write_b32 v233, v240 offset:20160\n"
  "ds_write_b32 v233, v241 offset:20448\n"
  "ds_write_b32 v233, v236 offset:20736\n"
  "ds_write_b32 v233, v237 offset:21024\n"
  "ds_write_b64 v229, v[200:201] offset:6720\n"
  "ds_write_b64 v229, v[204:205] offset:7008\n"
  "ds_write_b64 v229, v[202:203] offset:7296\n"
  "ds_write_b64 v229, v[206:207] offset:7584\n"
  "v_pk_fma_f32 v[120:121], v[120:121], v[72:73], v[72:73] neg_lo:[0,0,1] neg_hi:[0,0,1]\n"
  "v_pk_fma_f32 v[122:123], v[122:123], v[74:75], v[74:75] neg_lo:[0,0,1] neg_hi:[0,0,1]\n"
  "v_mfma_f32_16x16x32_bf16 v[152:155], v[44:47], a[136:139], v[152:155]\n"
  "s_nop 0\n"
  "s_waitcnt lgkmcnt(14)\n"
  "buffer_store_dwordx4 v[20:23], v228, s[52:55], s81 offen offset:3072 nt\n"
  "v_accvgpr_read_b32 v240, a228\n"
  "v_accvgpr_read_b32 v241, a229\n"
  "v_accvgpr_read_b32 v242, a232\n"
  "v_accvgpr_read_b32 v243, a233\n"
  "v_accvgpr_read_b32 v236, a230\n"
  "v_accvgpr_read_b32 v237, a231\n"
  "v_accvgpr_read_b32 v238, a234\n"
  "v_accvgpr_read_b32 v239, a235\n"
  "v_pk_fma_f32 v[240:241], v[240:241], v[84:85], v[84:85] neg_lo:[0,0,1] neg_hi:[0,0,1]\n"
  "v_pk_fma_f32 v[242:243], v[242:243], v[84:85], v[84:85] neg_lo:[0,0,1] neg_hi:[0,0,1]\n"
  "v_pk_fma_f32 v[236:237], v[236:237], v[86:87], v[86:87] neg_lo:[0,0,1] neg_hi:[0,0,1]\n"
  "v_pk_fma_f32 v[238:239], v[238:239], v[86:87], v[86:87] neg_lo:[0,0,1] neg_hi:[0,0,1]\n"
  "v_mfma_f32_16x16x32_bf16 v[156:159], v[44:47], a[140:143], v[156:159]\n"
  "ds_read_b64_tr_b16 v[0:1], v230 offset:6720\n"
  "ds_read_b64_tr_b16 v[2:3], v230 offset:6752\n"
  "v_cvt_pk_bf16_f32 v240, v240, v242\n"
  "v_cvt_pk_bf16_f32 v241, v241, v243\n"
  "v_cvt_pk_bf16_f32 v236, v236, v238\n"
  "v_cvt_pk_bf16_f32 v237, v237, v239\n"
  "v_pk_fma_f32 v[124:125], v[124:125], v[72:73], v[72:73] neg_lo:[0,0,1] neg_hi:[0,0,1]\n"
  "v_pk_fma_f32 v[128:129], v[128:129], v[72:73], v[72:73] neg_lo:[0,0,1] neg_hi:[0,0,1]\n"
  "v_pk_fma_f32 v[126:127], v[126:127], v[74:75], v[74:75] neg_lo:[0,0,1] neg_hi:[0,0,1]\n"
  "v_pk_fma_f32 v[130:131], v[130:131], v[74:75], v[74:75] neg_lo:[0,0,1] neg_hi:[0,0,1]\n"
  "v_mfma_f32_16x16x32_bf16 v[160:163], v[44:47], a[144:147], v[160:163]\n"
  "s_nop 0\n"
  "s_waitcnt lgkmcnt(12)\n"
  "buffer_store_dwordx4 v[244:247], v228, s[52:55], s82 offen offset:0 nt\n"
  "ds_write_b32 v233, v240 offset:17920\n"
  "ds_write_b32 v233, v241 offset:18208\n"
  "ds_write_b32 v233, v236 offset:18496\n"
  "ds_write_b32 v233, v237 offset:18784\n"
  "v_pk_fma_f32 v[132:133], v[132:133], v[72:73], v[72:73] neg_lo:[0,0,1] neg_hi:[0,0,1]\n"
  "v_pk_fma_f32 v[134:135], v[134:135], v[74:75], v[74:75] neg_lo:[0,0,1] neg_hi:[0,0,1]\n"
  "v_mfma_f32_16x16x32_bf16 v[164:167], v[44:47], a[148:151], v[164:167]\n"
  "v_accvgpr_read_b32 v240, a236\n"
  "v_accvgpr_read_b32 v241, a237\n"
  "v_accvgpr_read_b32 v242, a240\n"
  "v_accvgpr_read_b32 v243, a241\n"
  "v_accvgpr_read_b32 v236, a238\n"
  "v_accvgpr_read_b32 v237, a239\n"
  "v_accvgpr_read_b32 v238, a242\n"
  "v_accvgpr_read_b32 v239, a243\n"
  "v_cvt_pk_bf16_f32 v88, v88, v92\n"
  "v_cvt_pk_bf16_f32 v92, v89, v93\n"
  "v_cvt_pk_bf16_f32 v89, v96, v100\n"
  "v_cvt_pk_bf16_f32 v93, v97, v101\n"
  "v_cvt_pk_bf16_f32 v90, v90, v94\n"
  "v_cvt_pk_bf16_f32 v94, v91, v95\n"
  "v_cvt_pk_bf16_f32 v91, v98, v102\n"
  "v_cvt_pk_bf16_f32 v95, v99, v103\n"
  "v_pk_fma_f32 v[240:241], v[240:241], v[84:85], v[84:85] neg_lo:[0,0,1] neg_hi:[0,0,1]\n"
  "v_pk_fma_f32 v[242:243], v[242:243], v[84:85], v[84:85] neg_lo:[0,0,1] neg_hi:[0,0,1]\n"
  "v_pk_fma_f32 v[236:237], v[236:237], v[86:87], v[86:87] neg_lo:[0,0,1] neg_hi:[0,0,1]\n"
  "v_pk_fma_f32 v[238:239], v[238:239], v[86:87], v[86:87] neg_lo:[0,0,1] neg_hi:[0,0,1]\n"
  "v_mfma_f32_16x16x32_bf16 v[184:187], v[60:63], a[136:139], v[184:187]\n"
  "ds_read_b64_tr_b16 v[4:5], v230 offset:7840\n"
  "ds_read_b64_tr_b16 v[6:7], v230 offset:7872\n"
  "ds_write_b64 v229, v[88:89] offset:8960\n"
  "ds_write_b64 v229, v[92:93] offset:9248\n"
  "ds_write_b64 v229, v[90:91] offset:9536\n"
  "ds_write_b64 v229, v[94:95] offset:9824\n"
  "v_cvt_pk_bf16_f32 v240, v240, v242\n"
  "v_cvt_pk_bf16_f32 v241, v241, v243\n"
  "v_cvt_pk_bf16_f32 v236, v236, v238\n"
  "v_cvt_pk_bf16_f32 v237, v237, v239\n"
  "v_pk_fma_f32 v[152:153], v[152:153], v[76:77], v[76:77] neg_lo:[0,0,1] neg_hi:[0,0,1]\n"
  "v_pk_fma_f32 v[156:157], v[156:157], v[76:77], v[76:77] neg_lo:[0,0,1] neg_hi:[0,0,1]\n"
  "v_pk_fma_f32 v[154:155], v[154:155], v[78:79], v[78:79] neg_lo:[0,0,1] neg_hi:[0,0,1]\n"
  "v_pk_fma_f32 v[158:159], v[158:159], v[78:79], v[78:79] neg_lo:[0,0,1] neg_hi:[0,0,1]\n"
  "v_mfma_f32_16x16x32_bf16 v[188:191], v[60:63], a[140:143], v[188:191]\n"
  "s_nop 0\n"
  "s_waitcnt lgkmcnt(15)\n"
  "buffer_store_dwordx4 a[252:255], v228, s[52:55], s82 offen offset:3072 nt\n"
  "ds_write_b32 v233, v240 offset:19040\n"
  "ds_write_b32 v233, v241 offset:19328\n"
  "ds_write_b32 v233, v236 offset:19616\n"
  "ds_write_b32 v233, v237 offset:19904\n"
  "v_pk_fma_f32 v[160:161], v[160:161], v[76:77], v[76:77] neg_lo:[0,0,1] neg_hi:[0,0,1]\n"
  "v_pk_fma_f32 v[162:163], v[162:163], v[78:79], v[78:79] neg_lo:[0,0,1] neg_hi:[0,0,1]\n"
  "v_mfma_f32_16x16x32_bf16 v[192:195], v[60:63], a[144:147], v[192:195]\n"
  "ds_read_b64_tr_b16 v[16:17], v230 offset:8960\n"
  "ds_read_b64_tr_b16 v[18:19], v230 offset:8992\n"
  "ds_read_b64_tr_b16 v[20:21], v230 offset:10080\n"
  "ds_read_b64_tr_b16 v[22:23], v230 offset:10112\n"
  "v_cvt_pk_bf16_f32 v120, v120, v124\n"
  "v_pk_fma_f32 v[164:165], v[164:165], v[76:77], v[76:77] neg_lo:[0,0,1] neg_hi:[0,0,1]\n"
  "v_pk_fma_f32 v[166:167], v[166:167], v[78:79], v[78:79] neg_lo:[0,0,1] neg_hi:[0,0,1]\n"
  "v_mfma_f32_16x16x32_bf16 v[196:199], v[60:63], a[148:151], v[196:199]\n"
  "s_waitcnt lgkmcnt(4)\n"
  "s_barrier\n"
  "buffer_store_dwordx4 v[0:3], v228, s[52:55], s83 offen offset:0 nt\n"
  "v_cvt_pk_bf16_f32 v124, v121, v125\n"
  "v_cvt_pk_bf16_f32 v121, v128, v132\n"
  "v_pk_fma_f32 v[184:185], v[184:185], v[80:81], v[80:81] neg_lo:[0,0,1] neg_hi:[0,0,1]\n"
  "v_pk_fma_f32 v[186:187], v[186:187], v[82:83], v[82:83] neg_lo:[0,0,1] neg_hi:[0,0,1]\n"
  "ds_read_b64_tr_b16 v[244:245], v230 offset:17920\n"
  "ds_read_b64_tr_b16 v[246:247], v230 offset:17952\n"
  "ds_write_b64 v229, v[120:121] offset:11200\n"
  "v_pk_fma_f32 v[188:189], v[188:189], v[80:81], v[80:81] neg_lo:[0,0,1] neg_hi:[0,0,1]\n"
  "v_pk_fma_f32 v[190:191], v[190:191], v[82:83], v[82:83] neg_lo:[0,0,1] neg_hi:[0,0,1]\n"
  "ds_read_b64_tr_b16 a[252:253], v230 offset:19040\n"
  "ds_read_b64_tr_b16 a[254:255], v230 offset:19072\n"
  "ds_read_b64_tr_b16 v[0:1], v230 offset:20160\n"
  "ds_read_b64_tr_b16 v[2:3], v230 offset:20192\n"
  "v_cvt_pk_bf16_f32 v125, v129, v133\n"
  "v_pk_fma_f32 v[192:193], v[192:193], v[80:81], v[80:81] neg_lo:[0,0,1] neg_hi:[0,0,1]\n"
  "v_pk_fma_f32 v[194:195], v[194:195], v[82:83], v[82:83] neg_lo:[0,0,1] neg_hi:[0,0,1]\n"
  "buffer_store_dwordx4 v[4:7], v228, s[52:55], s83 offen offset:3072 nt\n"
  "ds_write_b64 v229, v[124:125] offset:11488\n"
  "v_cvt_pk_bf16_f32 v122, v122, v126\n"
  "v_pk_fma_f32 v[196:197], v[196:197], v[80:81], v[80:81] neg_lo:[0,0,1] neg_hi:[0,0,1]\n"
  "v_pk_fma_f32 v[198:199], v[198:199], v[82:83], v[82:83] neg_lo:[0,0,1] neg_hi:[0,0,1]\n"
  "v_cvt_pk_bf16_f32 v126, v123, v127\n"
  "v_cvt_pk_bf16_f32 v123, v130, v134\n"
  "v_cvt_pk_bf16_f32 v127, v131, v135\n"
  "v_cvt_pk_bf16_f32 v152, v152, v156\n"
  "v_cvt_pk_bf16_f32 v156, v153, v157\n"
  "s_waitcnt lgkmcnt(10)\n"
  "buffer_store_dwordx4 v[16:19], v228, s[52:55], s80 offen offset:128 nt\n"
  "ds_write_b64 v229, v[122:123] offset:11776\n"
  "v_cvt_pk_bf16_f32 v153, v160, v164\n"
  "ds_write_b64 v229, v[126:127] offset:12064\n"
  "v_cvt_pk_bf16_f32 v157, v161, v165\n"
  "v_cvt_pk_bf16_f32 v154, v154, v158\n"
  "s_waitcnt lgkmcnt(10)\n"
  "buffer_store_dwordx4 v[20:23], v228, s[52:55], s80 offen offset:3200 nt\n"
  "ds_write_b64 v229, v[152:153] offset:13440\n"
  "v_cvt_pk_bf16_f32 v158, v155, v159\n"
  "ds_read_b64_tr_b16 v[4:5], v230 offset:11200\n"
  "ds_read_b64_tr_b16 v[6:7], v230 offset:11232\n"
  "ds_write_b64 v229, v[156:157] offset:13728\n"
  "s_waitcnt lgkmcnt(12)\n"
  "buffer_store_dwordx4 v[244:247], v228, s[52:55], s56 offen offset:0 nt\n"
  "ds_read_b64_tr_b16 v[16:17], v230 offset:12320\n"
  "ds_read_b64_tr_b16 v[18:19], v230 offset:12352\n"
  "v_cvt_pk_bf16_f32 v155, v162, v166\n"
  "v_cvt_pk_bf16_f32 v159, v163, v167\n"
  "ds_write_b64 v229, v[154:155] offset:14016\n"
  "v_cvt_pk_bf16_f32 v184, v184, v188\n"
  "v_cvt_pk_bf16_f32 v188, v185, v189\n"
  "s_waitcnt lgkmcnt(12)\n"
  "buffer_store_dwordx4 a[252:255], v228, s[52:55], s56 offen offset:128 nt\n"
  "ds_write_b64 v229, v[158:159] offset:14304\n"
  "v_cvt_pk_bf16_f32 v185, v192, v196\n"
  "ds_write_b64 v229, v[184:185] offset:15680\n"
  "v_cvt_pk_bf16_f32 v189, v193, v197\n"
  "v_cvt_pk_bf16_f32 v186, v186, v190\n"
  "s_waitcnt lgkmcnt(12)\n"
  "buffer_store_dwordx4 v[0:3], v228, s[52:55], s56 offen offset:256 nt\n"
  "ds_read_b64_tr_b16 v[20:21], v230 offset:13440\n"
  "ds_write_b64 v229, v[188:189] offset:15968\n"
  "ds_read_b64_tr_b16 v[22:23], v230 offset:13472\n"
  "ds_read_b64_tr_b16 v[244:245], v230 offset:14560\n"
  "ds_read_b64_tr_b16 v[246:247], v230 offset:14592\n"
  "v_cvt_pk_bf16_f32 v190, v187, v191\n"
  "v_cvt_pk_bf16_f32 v187, v194, v198\n"
  "s_waitcnt lgkmcnt(11)\n"
  "buffer_store_dwordx4 v[4:7], v228, s[52:55], s81 offen offset:128 nt\n"
  "ds_write_b64 v229, v[186:187] offset:16256\n"
  "v_cvt_pk_bf16_f32 v191, v195, v199\n"
  "ds_write_b64 v229, v[190:191] offset:16544\n"
  "s_waitcnt lgkmcnt(10)\n"
  "buffer_store_dwordx4 v[16:19], v228, s[52:55], s81 offen offset:3200 nt\n"
  "ds_read_b64_tr_b16 a[252:253], v230 offset:15680\n"
  "ds_read_b64_tr_b16 a[254:255], v230 offset:15712\n"
  "ds_read_b64_tr_b16 v[0:1], v230 offset:16800\n"
  "ds_read_b64_tr_b16 v[2:3], v230 offset:16832\n"
  "s_waitcnt lgkmcnt(8)\n"
  "buffer_store_dwordx4 v[20:23], v228, s[52:55], s82 offen offset:128 nt\n"
  "s_waitcnt lgkmcnt(6)\n"
  "buffer_store_dwordx4 v[244:247], v228, s[52:55], s82 offen offset:3200 nt\n"
  "s_waitcnt lgkmcnt(2)\n"
  "buffer_store_dwordx4 a[252:255], v228, s[52:55], s83 offen offset:128 nt\n"
  "s_waitcnt lgkmcnt(0)\n"
  "buffer_store_dwordx4 v[0:3], v228, s[52:55], s83 offen offset:3200 nt\n"
  "L_END_%=:\n"
  "s_mov_b32 %0, s78\n"
  : "=&s"(bad)
  : "s"((unsigned long long)__builtin_amdgcn_kernarg_segment_ptr()), "s"(wg), "s"(tid0), "s"((unsigned)(size_t)lds)
  : "v0", "v1", "v2", "v3", "v4", "v5", "v6", "v7", "v8", "v9", "v10", "v11", "v12", "v13", "v14", "v15", "v16", "v17", "v18", "v19", "v20", "v21", "v22", "v23", "v24", "v25", "v26", "v27", "v28", "v29", "v30", "v31", "v32", "v33", "v34", "v35", "v36", "v37", "v38", "v39", "v40", "v41", "v42", "v43", "v44", "v45", "v46", "v47", "v48", "v49", "v50", "v51", "v52", "v53", "v54", "v55", "v56", "v57", "v58", "v59", "v60", "v61", "v62", "v63", "v64", "v65", "v66", "v67", "v68", "v69", "v70", "v71", "v72", "v73", "v74", "v75", "v76", "v77", "v78", "v79", "v80", "v81", "v82", "v83", "v84", "v85", "v86", "v87", "v88", "v89", "v90", "v91", "v92", "v93", "v94", "v95", "v96", "v97", "v98", "v99", "v100", "v101", "v102", "v103", "v104", "v105", "v106", "v107", "v108", "v109", "v110", "v111", "v112", "v113", "v114", "v115", "v116", "v117", "v118", "v119", "v120", "v121", "v122", "v123", "v124", "v125", "v126", "v127", "v128", "v129", "v130", "v131", "v132", "v133", "v134", "v135", "v136", "v137", "v138", "v139", "v140", "v141", "v142", "v143", "v144", "v145", "v146", "v147", "v148", "v149", "v150", "v151", "v152", "v153", "v154", "v155", "v156", "v157", "v158", "v159", "v160", "v161", "v162", "v163", "v164", "v165", "v166", "v167", "v168", "v169", "v170", "v171", "v172", "v173", "v174", "v175", "v176", "v177", "v178", "v179", "v180", "v181", "v182", "v183", "v184", "v185", "v186", "v187", "v188", "v189", "v190", "v191", "v192", "v193", "v194", "v195", "v196", "v197", "v198", "v199", "v200", "v201", "v202", "v203", "v204", "v205", "v206", "v207", "v208", "v209", "v210", "v211", "v212", "v213", "v214", "v215", "v216", "v217", "v218", "v219", "v220", "v221", "v222", "v223", "v224", "v225", "v226", "v227", "v228", "v229", "v230", "v231", "v232", "v233", "v234", "v235", "v236", "v237", "v238", "v239", "v240", "v241", "v242", "v243", "v244", "v245", "v246", "v247", "v248", "v249", "v250", "v251", "v252", "v253", "v254", "v255", "a0", "a1", "a2", "a3", "a4", "a5", "a6", "a7", "a8", "a9", "a10", "a11", "a12", "a13", "a14", "a15", "a16", "a17", "a18", "a19", "a20", "a21", "a22", "a23", "a24", "a25", "a26", "a27", "a28", "a29", "a30", "a31", "a32", "a33", "a34", "a35", "a36", "a37", "a38", "a39", "a40", "a41", "a42", "a43", "a44", "a45", "a46", "a47", "a48", "a49", "a50", "a51", "a52", "a53", "a54", "a55", "a56", "a57", "a58", "a59", "a60", "a61", "a62", "a63", "a64", "a65", "a66", "a67", "a68", "a69", "a70", "a71", "a72", "a73", "a74", "a75", "a76", "a77", "a78", "a79", "a80", "a81", "a82", "a83", "a84", "a85", "a86", "a87", "a88", "a89", "a90", "a91", "a92", "a93", "a94", "a95", "a96", "a97", "a98", "a99", "a100", "a101", "a102", "a103", "a104", "a105", "a106", "a107", "a108", "a109", "a110", "a111", "a112", "a113", "a114", "a115", "a116", "a117", "a118", "a119", "a120", "a121", "a122", "a123", "a124", "a125", "a126", "a127", "a128", "a129", "a130", "a131", "a132", "a133", "a134", "a135", "a136", "a137", "a138", "a139", "a140", "a141", "a142", "a143", "a144", "a145", "a146", "a147", "a148", "a149", "a150", "a151", "a152", "a153", "a154", "a155", "a156", "a157", "a158", "a159", "a160", "a161", "a162", "a163", "a164", "a165", "a166", "a167", "a168", "a169", "a170", "a171", "a172", "a173", "a174", "a175", "a176", "a177", "a178", "a179", "a180", "a181", "a182", "a183", "a184", "a185", "a186", "a187", "a188", "a189", "a190", "a191", "a192", "a193", "a194", "a195", "a196", "a197", "a198", "a199", "a200", "a201", "a202", "a203", "a204", "a205", "a206", "a207", "a208", "a209", "a210", "a211", "a212", "a213", "a214", "a215", "a216", "a217", "a218", "a219", "a220", "a221", "a222", "a223", "a224", "a225", "a226", "a227", "a228", "a229", "a230", "a231", "a232", "a233", "a234", "a235", "a236", "a237", "a238", "a239", "a240", "a241", "a242", "a243", "a244", "a245", "a246", "a247", "a248", "a249", "a250", "a251", "a252", "a253", "a254", "a255", "s20", "s21", "s22", "s23", "s24", "s25", "s26", "s27", "s28", "s29", "s30", "s31", "s32", "s33", "s34", "s35", "s36", "s37", "s38", "s39", "s40", "s41", "s42", "s43", "s44", "s45", "s46", "s47", "s48", "s49", "s50", "s51", "s52", "s53", "s54", "s55", "s56", "s57", "s58", "s59", "s60", "s61", "s62", "s63", "s64", "s65", "s66", "s67", "s68", "s69", "s70", "s71", "s72", "s73", "s74", "s75", "s76", "s77", "s78", "s79", "s80", "s81", "s82", "s83", "s84", "s85", "s86", "s87", "s88", "s89", "s90", "s91", "s92", "s93", "s94", "s95", "s96", "s97", "s98", "s99", "s100", "s101", "vcc", "scc", "memory");
  if (bad) {
    unsigned long long ka = (unsigned long long)__builtin_amdgcn_kernarg_segment_ptr();
    asm volatile("s_waitcnt vmcnt(0)" : "+s"(ka) :: "memory");
    const unsigned short* const* arg = (const unsigned short* const*)ka;
    const int xcd = wg & 7, slot = wg >> 3;
    constexpr int HB = 0;
    auto head_of = [&](int lh) { return lh + (lh >> HB) * (7 << HB) + (xcd << HB); };
#pragma unroll 1
    for (int it = 0; it < 6; ++it) {
      if (!((bad >> it) & 1u)) continue;
      const int p = slot + 32 * it, head = head_of(p / 3), r0 = 288 * (p % 3);
#pragma unroll 1
      for (int Y = 0; Y < 5; ++Y) {
        const int row = r0 + (Y < 4 ? 64 * wid + 16 * Y : 256 + 16 * (wid >> 1)), lo = Y < 4 ? 0 : 8 * (wid & 1);
        if (row < SQ) exact_tile(arg[0], arg[1], arg[2], (unsigned short*)arg[3], head, row, lo, Y < 4 ? 16 : lo + 8);
      }
    }
  }
}

extern "C" void kernel_launch(void* const* d_in, const int* in_sizes, int n_in,
                              void* d_out, int out_size, void* d_ws, size_t ws_size,
                              hipStream_t stream) {
  (void)hipFuncSetAttribute((const void*)attn_fwd_g, hipFuncAttributeMaxDynamicSharedMemorySize, LDS_BYTES);
  hipLaunchKernelGGL(attn_fwd_g, dim3(256), dim3(256), LDS_BYTES, stream, (const unsigned short*)d_in[0], (const unsigned short*)d_in[1],
                     (const unsigned short*)d_in[2], (unsigned short*)d_out);
}
```

```cpp
#include <hip/hip_runtime.h>
#include <hip/hip_bf16.h>

typedef __bf16   bf16x8 __attribute__((ext_vector_type(8)));
typedef __bf16   bf16x2 __attribute__((ext_vector_type(2)));
typedef float    f32x4  __attribute__((ext_vector_type(4)));
typedef float    f32x2  __attribute__((ext_vector_type(2)));
typedef unsigned u32x4  __attribute__((ext_vector_type(4)));
typedef unsigned u32x2  __attribute__((ext_vector_type(2)));
typedef unsigned short u16x8 __attribute__((ext_vector_type(8)));

constexpr int SQ = 848, SK = 512, DH = 192, ROWB = 384;
constexpr int LDS_BYTES = 163840;
constexpr float CSC = 0.07216878364870322f * 1.4426950408889634f;

#define DI __device__ __forceinline__
DI unsigned cvtpk(float lo, float hi) { f32x2 v = {lo, hi}; bf16x2 b = __builtin_convertvector(v, bf16x2); return __builtin_bit_cast(unsigned, b); }
DI float xmax(float x) {
  auto r = __builtin_amdgcn_permlane32_swap(__float_as_uint(x), __float_as_uint(x), false, false);
  x = fmaxf(__uint_as_float(r[0]), __uint_as_float(r[1]));
  auto q = __builtin_amdgcn_permlane16_swap(__float_as_uint(x), __float_as_uint(x), false, false);
  return fmaxf(__uint_as_float(q[0]), __uint_as_float(q[1]));
}
DI float xsum(float x) {
  auto r = __builtin_amdgcn_permlane32_swap(__float_as_uint(x), __float_as_uint(x), false, false);
  x = __uint_as_float(r[0]) + __uint_as_float(r[1]);
  auto q = __builtin_amdgcn_permlane16_swap(__float_as_uint(x), __float_as_uint(x), false, false);
  return __uint_as_float(q[0]) + __uint_as_float(q[1]);
}
DI f32x4 score_tile(const unsigned short* krow, const bf16x8 (&qf)[6]) {
  f32x4 acc = {0.f, 0.f, 0.f, 0.f};
#pragma unroll
  for (int s = 0; s < 6; ++s) acc = __builtin_amdgcn_mfma_f32_16x16x32_bf16(*(const bf16x8*)(krow + 32 * s), qf[s], acc, 0, 0, 0);
  return acc;
}
DI void exact_tile(const unsigned short* Q, const unsigned short* K, const unsigned short* V, unsigned short* O, int head, int row0, int lo, int hi) {
  const unsigned lane = __builtin_amdgcn_mbcnt_hi(~0u, __builtin_amdgcn_mbcnt_lo(~0u, 0u));
  const int c = lane & 15, g4 = lane >> 4;
  const int qrow = min(row0 + c, SQ - 1);
  bf16x8 qf[6];
#pragma unroll
  for (int s = 0; s < 6; ++s) qf[s] = *(const bf16x8*)(Q + ((size_t)head * SQ + qrow) * DH + 32 * s + 8 * g4);
  const unsigned short* Kh = K + ((size_t)head * SK + c) * DH + 8 * g4;
  float m = -__builtin_huge_valf();
#pragma unroll 1
  for (int kb = 0; kb < SK / 16; ++kb) {
    const f32x4 a = score_tile(Kh + (size_t)kb * 16 * DH, qf);
    m = fmaxf(fmaxf(m, fmaxf(a[0], a[1])), fmaxf(a[2], a[3]));
  }
  const float mc = -xmax(m) * CSC;
  float l = 0.f;
  f32x4 o[12];
#pragma unroll
  for (int dt = 0; dt < 12; ++dt) o[dt] = f32x4{0.f, 0.f, 0.f, 0.f};
#pragma unroll 1
  for (int j = 0; j < SK / 32; ++j) {
    u32x4 p;
#pragma unroll
    for (int kt = 0; kt < 2; ++kt) {
      f32x4 a = score_tile(Kh + (size_t)(32 * j + 16 * kt) * DH, qf);
#pragma unroll
      for (int r = 0; r < 4; ++r) { a[r] = __builtin_amdgcn_exp2f(fmaf(a[r], CSC, mc)); l += a[r]; }
      p[2 * kt] = cvtpk(a[0], a[1]); p[2 * kt + 1] = cvtpk(a[2], a[3]);
    }
    const unsigned short* Vj = V + ((size_t)head * SK + 32 * j + 4 * g4) * DH + c;
#pragma unroll
    for (int dt = 0; dt < 12; ++dt) {
      u16x8 vf;
#pragma unroll
      for (int i = 0; i < 8; ++i) vf[i] = Vj[(16 * (i >> 2) + (i & 3)) * DH + 16 * dt];
      o[dt] = __builtin_amdgcn_mfma_f32_16x16x32_bf16(__builtin_bit_cast(bf16x8, vf), __builtin_bit_cast(bf16x8, p), o[dt], 0, 0, 0);
    }
  }
  const float inv = 1.0f / xsum(l);
  if (row0 + c < SQ && c >= lo && c < hi) {
#pragma unroll
    for (int dt = 0; dt < 12; ++dt)
      *(u32x2*)(O + ((size_t)head * SQ + qrow) * DH + 16 * dt + 4 * g4) = u32x2{cvtpk(o[dt][0] * inv, o[dt][1] * inv), cvtpk(o[dt][2] * inv, o[dt][3] * inv)};
  }
}

__global__ __launch_bounds__(256, 1)
void attn_fwd_g(const unsigned short* __restrict__ Qg, const unsigned short* __restrict__ Kg,
                const unsigned short* __restrict__ Vg, unsigned short* __restrict__ Og) {
  extern __shared__ __attribute__((aligned(512))) char lds[];
  const int tid0 = __builtin_amdgcn_readfirstlane(threadIdx.x), wg = blockIdx.x, wid = tid0 >> 6;
  unsigned bad;
  asm volatile(
  ".p2align 4\n"
  "s_load_dwordx8 s[84:91], %1, 0\n"
  "L_PC_%=:\n"
  "s_getpc_b64 s[52:53]\n"
  "s_lshr_b32 s79, %2, 3\n"
  "s_lshl_b32 s79, s79, 15\n"
  "s_add_u32 s52, s52, s79\n"
  "s_addc_u32 s53, s53, 0\n"
  "s_sub_i32 s54, L_END_%=-L_PC_%=, s79\n"
  "s_max_i32 s54, s54, 0\n"
  "s_mov_b32 s55, 0x20000\n"
  "s_add_u32 m0, %4, 2048\n"
  "v_mbcnt_lo_u32_b32 v0, -1, 0\n"
  "v_mbcnt_hi_u32_b32 v0, -1, v0\n"
  "v_add_lshl_u32 v1, v0, %3, 7\n"
  "buffer_load_dword v1, s[52:55], 0 offen lds\n"
  "s_bitcmp1_b32 %2, 0\n"
  "s_cbranch_scc1 2\n"
  "s_waitcnt vmcnt(0) lgkmcnt(0)\n"
  "s_sleep 110\n"
  "s_lshr_b32 s38, %3, 6\n"
  "s_lshr_b32 s79, %2, 3\n"
  "s_mul_i32 s100, s79, 43\n"
  "s_lshr_b32 s100, s100, 7\n"
  "s_mul_i32 s71, s100, 3\n"
  "s_sub_u32 s95, s79, s71\n"
  "s_and_b32 s101, %2, 7\n"
  "s_lshl_b32 s101, s101, 0\n"
  "s_lshr_b32 s79, s100, 0\n"
  "s_mul_i32 s79, s79, 7\n"
  "s_add_u32 s79, s79, s100\n"
  "s_add_u32 s79, s79, s101\n"
  "s_mul_i32 s37, s79, 196608\n"
  "s_mul_i32 s92, s79, 325632\n"
  "s_mul_i32 s79, s95, 110592\n"
  "s_add_u32 s92, s92, s79\n"
  "s_mul_i32 s79, s38, 3072\n"
  "s_add_u32 s96, s79, s37\n"
  "s_lshr_b32 s71, s38, 1\n"
  "s_lshl_b32 s71, s71, 6\n"
  "s_add_u32 s79, s79, s71\n"
  "s_add_u32 s79, s79, %4\n"
  "s_add_u32 s66, s79, 41152\n"
  "s_add_u32 s67, s79, 53504\n"
  "s_add_u32 s68, s79, 65856\n"
  "s_mul_i32 s79, s38, 3088\n"
  "s_add_u32 s79, s79, %4\n"
  "s_add_u32 s58, s79, 4096\n"
  "s_add_u32 s59, s79, 16448\n"
  "s_add_u32 s60, s79, 28800\n"
  "s_mul_i32 s72, s38, 21312\n"
  "s_add_u32 s72, s72, %4\n"
  "s_add_u32 s72, s72, 78208\n"
  "s_movk_i32 s42, 0xc00\n"
  "s_movk_i32 s31, 12288\n"
  "s_mov_b32 s43, 0x20000\n"
  "s_mov_b32 s51, 0x20000\n"
  "s_bitcmp1_b32 s95, 1\n"
  "s_movk_i32 s93, 288\n"
  "s_cmovk_i32 s93, 272\n"
  "s_mul_i32 s93, s93, 384\n"
  "s_mov_b32 s50, s93\n"
  "s_mul_i32 s80, s38, 24576\n"
  "s_add_u32 s81, s80, 6144\n"
  "v_lshrrev_b32 v1, 3, v0\n"
  "v_and_b32 v2, 2, v1\n"
  "v_and_b32 v3, 7, v0\n"
  "v_xor_b32 v2, v2, v3\n"
  "v_mul_u32_u24 v221, 0x180, v1\n"
  "v_lshl_add_u32 v221, v2, 4, v221\n"
  "s_waitcnt lgkmcnt(0)\n"
  "s_add_u32 s40, s86, s96\n"
  "s_addc_u32 s41, s87, 0\n"
  "s_add_u32 s48, s84, s92\n"
  "s_addc_u32 s49, s85, 0\n"
  "s_mov_b32 m0, s68\n"
  "s_nop 0\n"
  "buffer_load_dwordx4 v221, s[40:43], 0 offen offset:0 lds\n"
  "s_addk_i32 m0, 896\n"
  "s_nop 0\n"
  "buffer_load_dwordx4 v221, s[40:43], 0 offen offset:128 lds\n"
  "s_addk_i32 m0, 896\n"
  "s_nop 0\n"
  "buffer_load_dwordx4 v221, s[40:43], 0 offen offset:256 lds\n"
  "s_add_u32 s40, s40, s31\n"
  "s_addc_u32 s41, s41, 0\n"
  "s_add_u32 s44, s88, s96\n"
  "s_addc_u32 s45, s89, 0\n"
  "s_mov_b64 s[62:63], s[90:91]\n"
  "s_mov_b32 s90, 100663296\n"
  "s_mov_b32 s91, 0x20000\n"
  "s_add_u32 s29, %4, 163456\n"
  "v_mbcnt_lo_u32_b32 v4, -1, 0\n"
  "v_mbcnt_hi_u32_b32 v4, -1, v4\n"
  "v_mul_u32_u24 v4, 0x18c, v4\n"
  "s_mov_b32 m0, s29\n"
  "s_nop 0\n"
  "buffer_load_dword v4, s[88:91], s37 offen lds\n"
  "s_mul_i32 s56, s38, 3072\n"
  "s_add_u32 s56, s56, 98304\n"
  "s_add_u32 m0, s72, 17920\n"
  "s_nop 0\n"
  "buffer_load_dwordx4 v221, s[48:51], s56 offen offset:0  lds\n"
  "s_add_u32 s82, s80, 12288\n"
  "s_add_u32 s83, s80, 18432\n"
  "s_mov_b64 s[46:47], s[42:43]\n"
  "s_mov_b64 s[34:35], s[42:43]\n"
  "s_bitcmp1_b32 s38, 0\n"
  "s_movk_i32 s70, 512\n"
  "s_cmovk_i32 s70, -512\n"
  "s_movk_i32 s97, 0\n"
  "s_cmovk_i32 s97, -21312\n"
  "s_movk_i32 s61, 0\n"
  "s_cmovk_i32 s61, -21308\n"
  "s_movk_i32 s69, 20196\n"
  "s_cmovk_i32 s69, 20188\n"
  "s_add_u32 m0, s72, 18912\n"
  "s_nop 0\n"
  "buffer_load_dwordx4 v221, s[48:51], s56 offen offset:128  lds\n"
  "s_mov_b32 s98, 0xff00ff00\n"
  "s_mov_b32 s99, s98\n"
  "s_mov_b32 s73, 0x3dd53b94\n"
  "s_mov_b32 s74, 0xbdd53b94\n"
  "s_mov_b32 s75, 0x53800000\n"
  "s_mov_b32 s76, -1\n"
  "s_add_u32 m0, s72, 19904\n"
  "s_nop 0\n"
  "buffer_load_dwordx4 v221, s[48:51], s56 offen offset:256  lds\n"
  "s_mov_b32 s77, 1\n"
  "s_mov_b32 s78, 0\n"
  "s_mov_b32 s57, s92\n"
  "s_mul_i32 s39, s38, 3072\n"
  "s_mov_b32 s54, 0\n"
  "s_add_u32 m0, s58, 1792\n"
  "s_nop 0\n"
  "buffer_load_dwordx4 v221, s[48:51], s80 offen offset:256  lds\n"
  "s_mov_b32 s55, 0x20000\n"
  "L_PC2_%=:\n"
  "s_getpc_b64 s[20:21]\n"
  "s_lshl_b32 s24, s38, 13\n"
  "s_add_u32 s24, s24, L_BIG_%=-L_PC2_%=-4\n"
  "s_cmp_lt_u32 s38, 2\n"
  "s_cselect_b32 s24, s24, L_TAIL_%=-L_PC2_%=-4\n"
  "s_sub_u32 s25, L_END_%=-L_PC2_%=-4, s24\n"
  "s_add_u32 s20, s20, s24\n"
  "s_addc_u32 s21, s21, 0\n"
  "s_mov_b32 s23, 0x20000\n"
  "s_lshr_b32 s24, s38, 1\n"
  "s_mul_i32 s24, s24, s38\n"
  "s_mul_i32 s24, s24, 5\n"
  "s_lshr_b32 s24, s24, 1\n"
  "v_lshrrev_b32 v1, 4, v0\n"
  "v_and_b32 v2, 2, v0\n"
  "v_xor_b32_e64 v1, v1, v2\n"
  "s_add_u32 m0, s60, -1280\n"
  "s_nop 0\n"
  "buffer_load_dwordx4 v221, s[48:51], s80 offen offset:3328  lds\n"
  "v_lshlrev_b32 v1, 4, v1\n"
  "v_and_b32 v2, 7, v0\n"
  "v_lshl_add_u32 v72, v2, 7, v1\n"
  "v_bfe_u32 v3, v0, 3, 1\n"
  "v_mul_u32_u24 v2, 0x440, v3\n"
  "s_add_u32 m0, s66, -128\n"
  "s_nop 0\n"
  "buffer_load_dwordx4 v221, s[48:51], s80 offen offset:128  lds\n"
  "v_add3_u32 v222, v72, v2, s72\n"
  "v_mul_u32_u24 v2, 0x6080, v3\n"
  "v_add3_u32 v74, v72, v2, s58\n"
  "v_lshl_add_u32 v72, v3, 10, v72\n"
  "v_add_u32 v73, s66, v72\n"
  "s_add_u32 m0, s66, -2176\n"
  "s_nop 0\n"
  "buffer_load_dwordx4 v221, s[48:51], s80 offen offset:3200  lds\n"
  "v_add_u32 v72, s58, v72\n"
  "v_lshl_add_u32 v1, v3, 6, v1\n"
  "v_and_b32 v2, 3, v0\n"
  "v_lshl_add_u32 v1, v2, 7, v1\n"
  "v_bfe_u32 v2, v0, 2, 2\n"
  "s_add_u32 m0, s58, 0\n"
  "s_nop 0\n"
  "buffer_load_dwordx4 v221, s[48:51], s80 offen offset:0  lds\n"
  "v_mul_u32_u24 v2, 0xc00, v2\n"
  "s_and_b32 s79, s38, 1\n"
  "s_lshl_b32 s79, s79, 9\n"
  "s_add_u32 s79, s79, %4\n"
  "s_add_u32 s71, s79, 41152\n"
  "s_add_u32 m0, s58, -2048\n"
  "s_nop 0\n"
  "buffer_load_dwordx4 v221, s[48:51], s80 offen offset:3072  lds\n"
  "v_add3_u32 v216, v1, v2, s71\n"
  "v_add_u32 v235, s70, v216\n"
  "v_add_u32 v233, s97, v222\n"
  "s_mov_b64 exec, s[98:99]\n"
  "v_add_u32 v233, 0x4f00, v233\n"
  "s_mov_b64 exec, -1\n"
  "v_lshrrev_b32_e64 v1, 4, v0\n"
  "v_mul_u32_u24 v1, 0xc10, v1\n"
  "s_mov_b32 m0, s67\n"
  "s_nop 0\n"
  "buffer_load_dwordx4 v221, s[40:43], 0 offen offset:0 lds\n"
  "v_bfe_u32 v2, v0, 2, 2\n"
  "v_lshl_add_u32 v1, v2, 7, v1\n"
  "v_bfe_u32 v2, v0, 1, 1\n"
  "v_lshl_add_u32 v1, v2, 6, v1\n"
  "v_and_b32_e64 v2, 1, v0\n"
  "s_addk_i32 m0, 896\n"
  "s_nop 0\n"
  "buffer_load_dwordx4 v221, s[40:43], 0 offen offset:128 lds\n"
  "v_lshl_add_u32 v1, v2, 3, v1\n"
  "s_add_u32 s71, s79, 4096\n"
  "v_add_u32 v1, s71, v1\n"
  "v_lshrrev_b32 v2, 3, v0\n"
  "v_xor_b32_e64 v2, s38, v2\n"
  "s_addk_i32 m0, 896\n"
  "s_nop 0\n"
  "buffer_load_dwordx4 v221, s[40:43], 0 offen offset:256 lds\n"
  "s_add_u32 s40, s40, s31\n"
  "s_addc_u32 s41, s41, 0\n"
  "v_and_b32_e64 v2, 1, v2\n"
  "v_lshl_add_u32 v217, v2, 5, v1\n"
  "v_xor_b32_e64 v2, 1, v2\n"
  "v_lshl_add_u32 v219, v2, 5, v1\n"
  "v_add_u32_e64 v218, s70, v217\n"
  "s_add_u32 m0, s59, 896\n"
  "s_nop 0\n"
  "buffer_load_dwordx4 v221, s[44:47], 0 offen offset:128 lds\n"
  "v_add_u32_e64 v220, s70, v219\n"
  "s_lshl_b32 s71, s38, 9\n"
  "s_add_u32 s71, s71, %4\n"
  "s_add_u32 s71, s71, 0\n"
  "v_lshl_add_u32 v231, v0, 3, s71\n"
  "s_addk_i32 m0, 896\n"
  "s_nop 0\n"
  "buffer_load_dwordx4 v221, s[44:47], 0 offen offset:256 lds\n"
  "s_mov_b64 s[32:33], s[44:45]\n"
  "s_add_u32 s44, s44, s31\n"
  "s_addc_u32 s45, s45, 0\n"
  "v_add_u32 v232, s70, v231\n"
  "v_mov_b32 v248, 0x43400000\n"
  "v_mov_b32 v249, 0x43400000\n"
  "v_mov_b32 v250, 0x43400000\n"
  "v_mov_b32 v251, 0x43400000\n"
  "s_add_u32 m0, s72, 0\n"
  "s_nop 0\n"
  "buffer_load_dwordx4 v221, s[48:51], s81 offen offset:0  lds\n"
  "v_mov_b32 v1, 0x3f803f80\n"
  "v_accvgpr_write_b32 a152, v1\n"
  "v_accvgpr_write_b32 a153, v1\n"
  "v_accvgpr_write_b32 a154, v1\n"
  "v_accvgpr_write_b32 a155, v1\n"
  "s_add_u32 m0, s72, -1984\n"
  "s_nop 0\n"
  "buffer_load_dwordx4 v221, s[48:51], s81 offen offset:3072  lds\n"
  "v_bfe_u32 v1, v0, 3, 1\n"
  "v_lshrrev_b32 v230, 4, v0\n"
  "v_lshl_add_u32 v1, v1, 2, v230\n"
  "v_mul_u32_u24 v228, 0x180, v1\n"
  "s_and_b32 s71, s38, 1\n"
  "s_add_u32 m0, s72, 2112\n"
  "s_nop 0\n"
  "buffer_load_dwordx4 v221, s[48:51], s81 offen offset:128  lds\n"
  "s_lshl_b32 s71, s71, 1\n"
  "v_and_b32 v1, 7, v0\n"
  "v_xor_b32_e64 v1, s71, v1\n"
  "v_lshl_add_u32 v228, v1, 4, v228\n"
  "v_mul_u32_u24 v230, 0x120, v230\n"
  "s_add_u32 m0, s72, 128\n"
  "s_nop 0\n"
  "buffer_load_dwordx4 v221, s[48:51], s81 offen offset:3200  lds\n"
  "v_and_b32 v1, 3, v0\n"
  "v_lshl_add_u32 v230, v1, 6, v230\n"
  "v_bfe_u32 v1, v0, 2, 2\n"
  "v_lshl_add_u32 v230, v1, 3, v230\n"
  "v_add_u32_e64 v230, s72, v230\n"
  "s_add_u32 m0, s60, -256\n"
  "s_nop 0\n"
  "buffer_load_dwordx4 v221, s[48:51], s81 offen offset:256  lds\n"
  "v_and_b32 v1, 31, v0\n"
  "v_lshlrev_b32_e64 v229, 3, v1\n"
  "v_lshrrev_b32_e64 v1, 5, v0\n"
  "v_mul_u32_u24 v1, 0x460, v1\n"
  "v_add3_u32 v229, v1, v229, s72\n"
  "s_waitcnt vmcnt(16)\n"
  "s_barrier\n"
  "ds_read_b128 a[164:167], v216 offset:24704\n"
  "ds_read_b128 a[168:171], v216 offset:24768\n"
  "ds_read_b128 a[172:175], v216 offset:25728\n"
  "ds_read_b128 a[176:179], v216 offset:25792\n"
  "ds_read_b128 a[180:183], v216 offset:26752\n"
  "ds_read_b128 a[184:187], v216 offset:26816\n"
  "ds_read_b128 a[96:99], v233 offset:17920\n"
  "ds_read_b128 a[100:103], v233 offset:17984\n"
  "ds_read_b128 a[104:107], v233 offset:19040\n"
  "ds_read_b128 a[108:111], v233 offset:19104\n"
  "ds_read_b128 a[112:115], v233 offset:20160\n"
  "ds_read_b128 a[116:119], v233 offset:20224\n"
  "v_bfe_u32 v5, v231, 1, 8\n"
  "v_add_u32_e64 v5, s29, v5\n"
  "ds_read_b128 a[188:191], v235 offset:24704\n"
  "ds_read_b128 a[192:195], v235 offset:24768\n"
  "ds_read_b128 a[196:199], v235 offset:25728\n"
  "ds_read_b128 a[200:203], v235 offset:25792\n"
  "ds_read_b128 a[204:207], v235 offset:26752\n"
  "ds_read_b128 a[208:211], v235 offset:26816\n"
  "ds_read_b32 v4, v5\n"
  "v_bfe_u32 v234, v231, 1, 6\n"
  "s_waitcnt lgkmcnt(12)\n"
  "v_mfma_f32_16x16x32_bf16 v[64:67], a[164:167], a[96:99], v[248:251]\n"
  "s_waitcnt lgkmcnt(11)\n"
  "v_mfma_f32_16x16x32_bf16 v[64:67], a[168:171], a[100:103], v[64:67]\n"
  "s_add_u32 m0, s60, -2304\n"
  "s_nop 0\n"
  "buffer_load_dwordx4 v221, s[48:51], s81 offen offset:3328  lds\n"
  "s_waitcnt lgkmcnt(10)\n"
  "v_mfma_f32_16x16x32_bf16 v[64:67], a[172:175], a[104:107], v[64:67]\n"
  "s_waitcnt lgkmcnt(9)\n"
  "v_mfma_f32_16x16x32_bf16 v[64:67], a[176:179], a[108:111], v[64:67]\n"
  "s_waitcnt lgkmcnt(8)\n"
  "v_mfma_f32_16x16x32_bf16 v[64:67], a[180:183], a[112:115], v[64:67]\n"
  "s_add_u32 m0, s72, 8704\n"
  "s_nop 0\n"
  "buffer_load_dwordx4 v221, s[48:51], s82 offen offset:256  lds\n"
  "s_waitcnt lgkmcnt(7)\n"
  "v_mfma_f32_16x16x32_bf16 v[64:67], a[184:187], a[116:119], v[64:67]\n"
  "s_waitcnt lgkmcnt(0)\n"
  "v_and_b32 v4, 0x7fff7fff, v4\n"
  "v_lshlrev_b32_e64 v5, 16, v4\n"
  "v_mfma_f32_16x16x32_bf16 v[240:243], a[188:191], a[96:99], v[248:251]\n"
  "s_waitcnt vmcnt(12)\n"
  "ds_read_b128 a[20:23], v74 offset:2112\n"
  "ds_read_b128 a[16:19], v74 offset:2048\n"
  "ds_read_b128 a[12:15], v73 offset:64\n"
  "v_mfma_f32_16x16x32_bf16 v[240:243], a[192:195], a[100:103], v[240:243]\n"
  "s_nop 0\n"
  "s_add_u32 m0, s72, 6720\n"
  "s_nop 0\n"
  "buffer_load_dwordx4 v221, s[48:51], s82 offen offset:3328  lds\n"
  "ds_read_b128 a[8:11], v73 offset:0\n"
  "ds_read_b128 a[4:7], v72 offset:64\n"
  "ds_read_b128 a[0:3], v72 offset:0\n"
  "v_max_u32 v4, v4, v5\n"
  "v_mfma_f32_16x16x32_bf16 v[240:243], a[196:199], a[104:107], v[240:243]\n"
  "s_nop 0\n"
  "v_max_u32_dpp v4, v4, v4 quad_perm:[1,0,3,2] row_mask:0xf bank_mask:0xf\n"
  "v_max3_f32 v227, v64, v65, v66\n"
  "v_mfma_f32_16x16x32_bf16 v[240:243], a[200:203], a[108:111], v[240:243]\n"
  "v_max_u32_dpp v4, v4, v4 quad_perm:[2,3,0,1] row_mask:0xf bank_mask:0xf\n"
  "v_add_u32 v4, -1, v4\n"
  "v_mfma_f32_16x16x32_bf16 v[240:243], a[204:207], a[112:115], v[240:243]\n"
  "s_add_u32 m0, s72, 6592\n"
  "s_nop 0\n"
  "buffer_load_dwordx4 v221, s[48:51], s82 offen offset:128  lds\n"
  "v_min_u32_dpp v4, v4, v4 row_shr:4 row_mask:0xf bank_mask:0xf\n"
  "v_mfma_f32_16x16x32_bf16 v[240:243], a[208:211], a[116:119], v[240:243]\n"
  "s_waitcnt lgkmcnt(5)\n"
  "v_min_u32_dpp v4, v4, v4 row_shr:8 row_mask:0xf bank_mask:0xf\n"
  "v_mfma_f32_16x16x32_bf16 v[0:3], a[184:187], a[20:23], v[248:251]\n"
  "s_waitcnt lgkmcnt(4)\n"
  "v_min_u32_dpp v4, v4, v4 row_bcast:15 row_mask:0xa bank_mask:0xf\n"
  "v_mfma_f32_16x16x32_bf16 v[0:3], a[180:183], a[16:19], v[0:3]\n"
  "s_add_u32 m0, s72, 4608\n"
  "s_nop 0\n"
  "buffer_load_dwordx4 v221, s[48:51], s82 offen offset:3200  lds\n"
  "s_waitcnt lgkmcnt(3)\n"
  "v_min_u32_dpp v4, v4, v4 row_bcast:31 row_mask:0xc bank_mask:0xf\n"
  "v_mfma_f32_16x16x32_bf16 v[0:3], a[176:179], a[12:15], v[0:3]\n"
  "s_waitcnt lgkmcnt(2)\n"
  "v_readlane_b32 s65, v4, 63\n"
  "v_mfma_f32_16x16x32_bf16 v[0:3], a[172:175], a[8:11], v[0:3]\n"
  "s_waitcnt lgkmcnt(1)\n"
  "v_max3_f32 v227, v227, v67, v240\n"
  "v_max3_f32 v227, v227, v241, v242\n"
  "v_mfma_f32_16x16x32_bf16 v[0:3], a[168:171], a[4:7], v[0:3]\n"
  "s_add_u32 s65, s65, 1\n"
  "s_lshr_b32 s65, s65, 23\n"
  "s_sub_i32 s65, 0x77, s65\n"
  "s_and_b32 s65, s65, -4\n"
  "s_max_i32 s65, s65, 0xffffffc4\n"
  "s_add_u32 m0, s72, 4480\n"
  "s_nop 0\n"
  "buffer_load_dwordx4 v221, s[48:51], s82 offen offset:0  lds\n"
  "s_waitcnt lgkmcnt(0)\n"
  "v_cvt_f32_i32 v4, s65\n"
  "v_mfma_f32_16x16x32_bf16 v[0:3], a[164:167], a[0:3], v[0:3]\n"
  "s_nop 0\n"
  "v_readfirstlane_b32 s64, v4\n"
  "v_max_f32 v227, v227, v243\n"
  "v_mul_f32 v227, s74, v227\n"
  "v_add_f32 v227, s64, v227\n"
  "v_mfma_f32_16x16x32_bf16 v[4:7], a[208:211], a[20:23], v[248:251]\n"
  "s_add_u32 s65, s65, 0xab\n"
  "ds_bpermute_b32 v227, v234, v227\n"
  "v_mfma_f32_16x16x32_bf16 v[4:7], a[204:207], a[16:19], v[4:7]\n"
  "s_add_u32 m0, s72, 2496\n"
  "s_nop 0\n"
  "buffer_load_dwordx4 v221, s[48:51], s82 offen offset:3072  lds\n"
  "v_mfma_f32_16x16x32_bf16 v[4:7], a[200:203], a[12:15], v[4:7]\n"
  "s_cmpk_gt_u32 s65, 0xd3\n"
  "s_cselect_b32 s65, 0, s65\n"
  "v_max3_f32 v223, v0, v1, v2\n"
  "v_max_f32 v223, v223, v3\n"
  "v_mfma_f32_16x16x32_bf16 v[4:7], a[196:199], a[8:11], v[4:7]\n"
  "s_lshl_b32 s65, s65, 23\n"
  "v_mul_f32 v223, s74, v223\n"
  "v_add_f32_e64 v223, s64, v223\n"
  "v_mfma_f32_16x16x32_bf16 v[4:7], a[192:195], a[4:7], v[4:7]\n"
  "s_add_u32 m0, s72, 11200\n"
  "s_nop 0\n"
  "buffer_load_dwordx4 v221, s[48:51], s83 offen offset:0  lds\n"
  "v_mfma_f32_16x16x32_bf16 v[4:7], a[188:191], a[0:3], v[4:7]\n"
  "s_mov_b32 s75, s65\n"
  "ds_bpermute_b32 v223, v234, v223\n"
  "s_waitcnt lgkmcnt(1)\n"
  "v_fma_f32 v64, v64, s73, v227\n"
  "v_exp_f32_e64 v64, v64\n"
  "v_fma_f32 v65, v65, s73, v227\n"
  "v_exp_f32_e64 v65, v65\n"
  "v_fma_f32 v66, v66, s73, v227\n"
  "v_exp_f32_e64 v66, v66\n"
  "v_fma_f32 v67, v67, s73, v227\n"
  "v_exp_f32_e64 v67, v67\n"
  "v_cvt_pk_bf16_f32 v64, v64, v65\n"
  "v_cvt_pk_bf16_f32 v65, v66, v67\n"
  "ds_write_b64 v231, v[64:65] offset:0\n"
  "s_waitcnt vmcnt(13) lgkmcnt(0)\n"
  "s_barrier\n"
  "ds_read_b128 a[212:215], v216 offset:12352\n"
  "ds_read_b128 a[216:219], v216 offset:12416\n"
  "ds_read_b128 a[220:223], v216 offset:13376\n"
  "ds_read_b128 a[224:227], v216 offset:13440\n"
  "ds_read_b128 a[228:231], v216 offset:14400\n"
  "ds_read_b128 a[232:235], v216 offset:14464\n"
  "ds_read_b64 a[156:157], v231 offset:0\n"
  "ds_read_b64 a[158:159], v232 offset:0\n"
  "s_waitcnt lgkmcnt(7)\n"
  "v_mfma_f32_16x16x32_bf16 v[64:67], a[212:215], a[96:99], v[248:251]\n"
  "s_add_u32 m0, s72, 9216\n"
  "s_nop 0\n"
  "buffer_load_dwordx4 v221, s[48:51], s83 offen offset:3072  lds\n"
  "ds_read_b64_tr_b16 a[144:145], v217 offset:14400\n"
  "ds_read_b64_tr_b16 a[146:147], v218 offset:14400\n"
  "s_waitcnt lgkmcnt(8)\n"
  "v_mfma_f32_16x16x32_bf16 v[64:67], a[216:219], a[100:103], v[64:67]\n"
  "ds_read_b64_tr_b16 a[148:149], v217 offset:14416\n"
  "ds_read_b64_tr_b16 a[150:151], v218 offset:14416\n"
  "s_waitcnt lgkmcnt(9)\n"
  "v_mfma_f32_16x16x32_bf16 v[64:67], a[220:223], a[104:107], v[64:67]\n"
  "ds_read_b64_tr_b16 v[76:77], v217 offset:13376\n"
  "ds_read_b64_tr_b16 v[78:79], v218 offset:13376\n"
  "s_waitcnt lgkmcnt(10)\n"
  "v_mfma_f32_16x16x32_bf16 v[64:67], a[224:227], a[108:111], v[64:67]\n"
  "s_add_u32 m0, s72, 13312\n"
  "s_nop 0\n"
  "buffer_load_dwordx4 v221, s[48:51], s83 offen offset:128  lds\n"
  "ds_read_b64_tr_b16 v[80:81], v217 offset:13392\n"
  "ds_read_b64_tr_b16 v[82:83], v218 offset:13392\n"
  "s_waitcnt lgkmcnt(11)\n"
  "v_mfma_f32_16x16x32_bf16 v[64:67], a[228:231], a[112:115], v[64:67]\n"
  "ds_read_b128 a[120:123], v235 offset:12352\n"
  "ds_read_b128 a[124:127], v235 offset:12416\n"
  "s_waitcnt lgkmcnt(12)\n"
  "v_mfma_f32_16x16x32_bf16 v[64:67], a[232:235], a[116:119], v[64:67]\n"
  "ds_read_b128 a[128:131], v235 offset:13376\n"
  "ds_read_b128 a[132:135], v235 offset:13440\n"
  "ds_read_b128 a[136:139], v235 offset:14400\n"
  "ds_read_b128 a[140:143], v235 offset:14464\n"
  "s_waitcnt lgkmcnt(12)\n"
  "v_mfma_f32_16x16x32_bf16 a[244:247], a[156:159], a[144:147], 1.0\n"
  "s_add_u32 m0, s72, 11328\n"
  "s_nop 0\n"
  "buffer_load_dwordx4 v221, s[48:51], s83 offen offset:3200  lds\n"
  "ds_read_b64_tr_b16 v[84:85], v219 offset:14400\n"
  "ds_read_b64_tr_b16 v[86:87], v220 offset:14400\n"
  "s_waitcnt lgkmcnt(12)\n"
  "v_mfma_f32_16x16x32_bf16 a[248:251], a[156:159], a[148:151], 1.0\n"
  "ds_read_b64_tr_b16 v[244:245], v219 offset:14416\n"
  "ds_read_b64_tr_b16 v[246:247], v220 offset:14416\n"
  "s_waitcnt lgkmcnt(12)\n"
  "v_mfma_f32_16x16x32_bf16 a[236:239], a[156:159], v[76:79], 1.0\n"
  "ds_read_b64_tr_b16 a[252:253], v219 offset:13376\n"
  "ds_read_b64_tr_b16 a[254:255], v220 offset:13376\n"
  "s_waitcnt lgkmcnt(12)\n"
  "v_fma_f32 v0, v0, s73, v223\n"
  "v_exp_f32 v0, v0\n"
  "v_fma_f32 v1, v1, s73, v223\n"
  "v_exp_f32_e64 v1, v1\n"
  "v_fma_f32 v2, v2, s73, v223\n"
  "v_exp_f32_e64 v2, v2\n"
  "v_fma_f32 v3, v3, s73, v223\n"
  "v_exp_f32 v3, v3\n"
  "v_mfma_f32_16x16x32_bf16 a[240:243], a[156:159], v[80:83], 1.0\n"
  "s_add_u32 m0, s72, 15424\n"
  "s_nop 0\n"
  "buffer_load_dwordx4 v221, s[48:51], s83 offen offset:256  lds\n"
  "ds_read_b64_tr_b16 v[236:237], v219 offset:13392\n"
  "ds_read_b64_tr_b16 v[238:239], v220 offset:13392\n"
  "v_cvt_pk_bf16_f32 v8, v0, v1\n"
  "v_cvt_pk_bf16_f32 v9, v2, v3\n"
  "v_mfma_f32_16x16x32_bf16 v[0:3], a[232:235], a[20:23], v[248:251]\n"
  "v_fma_f32 v4, v4, s73, v223\n"
  "v_fma_f32 v5, v5, s73, v223\n"
  "v_mfma_f32_16x16x32_bf16 v[0:3], a[228:231], a[16:19], v[0:3]\n"
  "v_exp_f32 v4, v4\n"
  "v_mfma_f32_16x16x32_bf16 v[0:3], a[224:227], a[12:15], v[0:3]\n"
  "s_add_u32 m0, s72, 13440\n"
  "s_nop 0\n"
  "buffer_load_dwordx4 v221, s[48:51], s83 offen offset:3328  lds\n"
  "v_mfma_f32_16x16x32_bf16 v[0:3], a[220:223], a[8:11], v[0:3]\n"
  "v_exp_f32 v5, v5\n"
  "v_mfma_f32_16x16x32_bf16 v[0:3], a[216:219], a[4:7], v[0:3]\n"
  "v_fma_f32 v6, v6, s73, v223\n"
  "v_exp_f32 v6, v6\n"
  "v_fma_f32 v7, v7, s73, v223\n"
  "v_exp_f32 v7, v7\n"
  "v_mfma_f32_16x16x32_bf16 v[0:3], a[212:215], a[0:3], v[0:3]\n"
  "s_mov_b32 m0, s66\n"
  "s_nop 0\n"
  "buffer_load_dwordx4 v221, s[40:43], 0 offen offset:0 lds\n"
  "s_waitcnt lgkmcnt(13)\n"
  "v_cvt_pk_bf16_f32 v10, v4, v5\n"
  "v_cvt_pk_bf16_f32 v11, v6, v7\n"
  "v_mfma_f32_16x16x32_bf16 v[4:7], a[120:123], a[0:3], v[248:251]\n"
  "s_waitcnt vmcnt(13)\n"
  "ds_read_b128 a[24:27], v222 offset:0\n"
  "ds_read_b128 a[28:31], v222 offset:64\n"
  "ds_read_b128 a[32:35], v222 offset:2240\n"
  "s_waitcnt lgkmcnt(15)\n"
  "v_mfma_f32_16x16x32_bf16 v[4:7], a[124:127], a[4:7], v[4:7]\n"
  "ds_read_b128 a[36:39], v222 offset:2304\n"
  "ds_read_b128 a[40:43], v72 offset:24704\n"
  "ds_read_b128 a[44:47], v72 offset:24768\n"
  "s_waitcnt lgkmcnt(15)\n"
  "v_mfma_f32_16x16x32_bf16 v[4:7], a[128:131], a[8:11], v[4:7]\n"
  "s_addk_i32 m0, 896\n"
  "s_nop 0\n"
  "buffer_load_dwordx4 v221, s[40:43], 0 offen offset:128 lds\n"
  "v_mfma_f32_16x16x32_bf16 v[4:7], a[132:135], a[12:15], v[4:7]\n"
  "v_fma_f32 v64, v64, s73, v227\n"
  "v_fma_f32 v65, v65, s73, v227\n"
  "v_mfma_f32_16x16x32_bf16 v[4:7], a[136:139], a[16:19], v[4:7]\n"
  "s_waitcnt lgkmcnt(14)\n"
  "v_exp_f32 v64, v64\n"
  "v_mfma_f32_16x16x32_bf16 v[4:7], a[140:143], a[20:23], v[4:7]\n"
  "s_addk_i32 m0, 896\n"
  "s_nop 0\n"
  "buffer_load_dwordx4 v221, s[40:43], 0 offen offset:256 lds\n"
  "s_add_u32 s40, s40, s31\n"
  "s_addc_u32 s41, s41, 0\n"
  "s_waitcnt lgkmcnt(5)\n"
  "v_mfma_f32_16x16x32_bf16 v[16:19], a[164:167], a[24:27], v[248:251]\n"
  "s_waitcnt lgkmcnt(4)\n"
  "v_exp_f32 v65, v65\n"
  "v_mfma_f32_16x16x32_bf16 v[16:19], a[168:171], a[28:31], v[16:19]\n"
  "s_waitcnt lgkmcnt(3)\n"
  "v_fma_f32 v66, v66, s73, v227\n"
  "v_fma_f32 v67, v67, s73, v227\n"
  "v_mfma_f32_16x16x32_bf16 v[16:19], a[172:175], a[32:35], v[16:19]\n"
  "s_mov_b32 m0, s58\n"
  "s_nop 0\n"
  "buffer_load_dwordx4 v221, s[32:35], 0 offen offset:0 lds\n"
  "s_waitcnt lgkmcnt(2)\n"
  "v_mfma_f32_16x16x32_bf16 v[16:19], a[176:179], a[36:39], v[16:19]\n"
  "s_waitcnt lgkmcnt(1)\n"
  "v_exp_f32 v66, v66\n"
  "v_mfma_f32_16x16x32_bf16 v[16:19], a[180:183], a[40:43], v[16:19]\n"
  "s_waitcnt lgkmcnt(0)\n"
  "v_exp_f32 v67, v67\n"
  "v_mfma_f32_16x16x32_bf16 v[16:19], a[184:187], a[44:47], v[16:19]\n"
  "s_addk_i32 m0, 1024\n"
  "s_nop 0\n"
  "buffer_load_dwordx4 v221, s[44:47], 0 offen offset:0 lds\n"
  "v_mfma_f32_16x16x32_bf16 v[20:23], a[188:191], a[24:27], v[248:251]\n"
  "v_cvt_pk_bf16_f32 v64, v64, v65\n"
  "v_cvt_pk_bf16_f32 v65, v66, v67\n"
  "v_mfma_f32_16x16x32_bf16 v[20:23], a[192:195], a[28:31], v[20:23]\n"
  "v_fma_f32 v0, v0, s73, v223\n"
  "v_fma_f32 v1, v1, s73, v223\n"
  "v_mfma_f32_16x16x32_bf16 v[20:23], a[196:199], a[32:35], v[20:23]\n"
  "s_addk_i32 m0, 768\n"
  "s_nop 0\n"
  "buffer_load_dwordx4 v221, s[44:47], 0 offen offset:256 lds\n"
  "s_mov_b64 s[32:33], s[44:45]\n"
  "s_add_u32 s44, s44, s31\n"
  "s_addc_u32 s45, s45, 0\n"
  "v_mfma_f32_16x16x32_bf16 v[20:23], a[200:203], a[36:39], v[20:23]\n"
  "v_max3_f32 v224, v16, v17, v18\n"
  "v_max_f32 v224, v224, v19\n"
  "v_mfma_f32_16x16x32_bf16 v[20:23], a[204:207], a[40:43], v[20:23]\n"
  "v_mul_f32 v224, s74, v224\n"
  "v_add_f32 v224, s64, v224\n"
  "v_mfma_f32_16x16x32_bf16 v[20:23], a[208:211], a[44:47], v[20:23]\n"
  "ds_bpermute_b32 v224, v234, v224\n"
  "v_mfma_f32_16x16x32_bf16 v[104:107], v[8:11], a[144:147], 1.0\n"
  "v_exp_f32 v0, v0\n"
  "v_mfma_f32_16x16x32_bf16 v[108:111], v[8:11], a[148:151], 1.0\n"
  "v_exp_f32 v1, v1\n"
  "v_mfma_f32_16x16x32_bf16 v[112:115], v[8:11], v[84:87], 1.0\n"
  "v_fma_f32 v2, v2, s73, v223\n"
  "v_fma_f32 v3, v3, s73, v223\n"
  "v_mfma_f32_16x16x32_bf16 v[116:119], v[8:11], v[244:247], 1.0\n"
  "v_exp_f32 v2, v2\n"
  "v_mfma_f32_16x16x32_bf16 v[88:91], v[8:11], v[76:79], 1.0\n"
  "v_exp_f32 v3, v3\n"
  "v_mfma_f32_16x16x32_bf16 v[92:95], v[8:11], v[80:83], 1.0\n"
  "s_waitcnt lgkmcnt(0)\n"
  "v_fma_f32 v16, v16, s73, v224\n"
  "v_fma_f32 v17, v17, s73, v224\n"
  "v_mfma_f32_16x16x32_bf16 v[96:99], v[8:11], a[252:255], 1.0\n"
  "v_exp_f32 v16, v16\n"
  "v_exp_f32 v17, v17\n"
  "v_fma_f32 v18, v18, s73, v224\n"
  "v_exp_f32 v18, v18\n"
  "v_fma_f32 v19, v19, s73, v224\n"
  "v_exp_f32_e64 v19, v19\n"
  "v_mfma_f32_16x16x32_bf16 v[100:103], v[8:11], v[236:239], 1.0\n"
  "v_cvt_pk_bf16_f32 v24, v16, v17\n"
  "v_cvt_pk_bf16_f32 v25, v18, v19\n"
  "v_mfma_f32_16x16x32_bf16 v[16:19], a[212:215], a[24:27], v[248:251]\n"
  "v_fma_f32 v20, v20, s73, v224\n"
  "v_fma_f32 v21, v21, s73, v224\n"
  "v_mfma_f32_16x16x32_bf16 v[16:19], a[216:219], a[28:31], v[16:19]\n"
  "v_exp_f32 v20, v20\n"
  "v_mfma_f32_16x16x32_bf16 v[16:19], a[220:223], a[32:35], v[16:19]\n"
  "v_exp_f32 v21, v21\n"
  "v_mfma_f32_16x16x32_bf16 v[16:19], a[224:227], a[36:39], v[16:19]\n"
  "v_fma_f32 v22, v22, s73, v224\n"
  "v_fma_f32 v23, v23, s73, v224\n"
  "v_mfma_f32_16x16x32_bf16 v[16:19], a[228:231], a[40:43], v[16:19]\n"
  "v_exp_f32 v22, v22\n"
  "v_exp_f32_e64 v23, v23\n"
  "v_mfma_f32_16x16x32_bf16 v[16:19], a[232:235], a[44:47], v[16:19]\n"
  "v_cvt_pk_bf16_f32 v26, v20, v21\n"
  "v_cvt_pk_bf16_f32 v27, v22, v23\n"
  "v_mfma_f32_16x16x32_bf16 v[20:23], a[140:143], a[44:47], v[248:251]\n"
  "s_waitcnt vmcnt(12)\n"
  "ds_read_b128 a[68:71], v222 offset:9024\n"
  "ds_read_b128 a[64:67], v222 offset:8960\n"
  "ds_read_b128 a[60:63], v222 offset:6784\n"
  "v_mfma_f32_16x16x32_bf16 v[20:23], a[136:139], a[40:43], v[20:23]\n"
  "ds_read_b128 a[56:59], v222 offset:6720\n"
  "ds_read_b128 a[52:55], v222 offset:4544\n"
  "ds_read_b128 a[48:51], v222 offset:4480\n"
  "v_mfma_f32_16x16x32_bf16 v[20:23], a[132:135], a[36:39], v[20:23]\n"
  "v_fma_f32 v4, v4, s73, v223\n"
  "v_fma_f32 v5, v5, s73, v223\n"
  "v_mfma_f32_16x16x32_bf16 v[20:23], a[128:131], a[32:35], v[20:23]\n"
  "v_exp_f32 v4, v4\n"
  "v_mfma_f32_16x16x32_bf16 v[20:23], a[124:127], a[28:31], v[20:23]\n"
  "v_exp_f32 v5, v5\n"
  "v_mfma_f32_16x16x32_bf16 v[20:23], a[120:123], a[24:27], v[20:23]\n"
  "s_waitcnt lgkmcnt(5)\n"
  "v_fma_f32 v6, v6, s73, v223\n"
  "v_fma_f32 v7, v7, s73, v223\n"
  "v_mfma_f32_16x16x32_bf16 v[32:35], a[184:187], a[68:71], v[248:251]\n"
  "s_waitcnt lgkmcnt(4)\n"
  "v_exp_f32 v6, v6\n"
  "v_mfma_f32_16x16x32_bf16 v[32:35], a[180:183], a[64:67], v[32:35]\n"
  "s_waitcnt lgkmcnt(3)\n"
  "v_exp_f32 v7, v7\n"
  "v_mfma_f32_16x16x32_bf16 v[32:35], a[176:179], a[60:63], v[32:35]\n"
  "s_waitcnt lgkmcnt(2)\n"
  "v_fma_f32 v16, v16, s73, v224\n"
  "v_fma_f32 v17, v17, s73, v224\n"
  "v_mfma_f32_16x16x32_bf16 v[32:35], a[172:175], a[56:59], v[32:35]\n"
  "s_waitcnt lgkmcnt(1)\n"
  "v_exp_f32 v16, v16\n"
  "v_mfma_f32_16x16x32_bf16 v[32:35], a[168:171], a[52:55], v[32:35]\n"
  "s_waitcnt lgkmcnt(0)\n"
  "v_exp_f32 v17, v17\n"
  "v_mfma_f32_16x16x32_bf16 v[32:35], a[164:167], a[48:51], v[32:35]\n"
  "v_fma_f32 v18, v18, s73, v224\n"
  "v_fma_f32 v19, v19, s73, v224\n"
  "v_mfma_f32_16x16x32_bf16 v[36:39], a[208:211], a[68:71], v[248:251]\n"
  "v_exp_f32 v18, v18\n"
  "v_mfma_f32_16x16x32_bf16 v[36:39], a[204:207], a[64:67], v[36:39]\n"
  "v_exp_f32 v19, v19\n"
  "v_mfma_f32_16x16x32_bf16 v[36:39], a[200:203], a[60:63], v[36:39]\n"
  "v_max3_f32 v225, v32, v33, v34\n"
  "v_max_f32 v225, v225, v35\n"
  "v_mfma_f32_16x16x32_bf16 v[36:39], a[196:199], a[56:59], v[36:39]\n"
  "v_mul_f32 v225, s74, v225\n"
  "v_add_f32 v225, s64, v225\n"
  "v_mfma_f32_16x16x32_bf16 v[36:39], a[192:195], a[52:55], v[36:39]\n"
  "ds_bpermute_b32 v225, v234, v225\n"
  "v_mfma_f32_16x16x32_bf16 v[36:39], a[188:191], a[48:51], v[36:39]\n"
  "v_fma_f32 v20, v20, s73, v224\n"
  "v_fma_f32 v21, v21, s73, v224\n"
  "v_mfma_f32_16x16x32_bf16 v[136:139], v[24:27], a[144:147], 1.0\n"
  "v_exp_f32 v20, v20\n"
  "v_mfma_f32_16x16x32_bf16 v[140:143], v[24:27], a[148:151], 1.0\n"
  "v_exp_f32 v21, v21\n"
  "v_mfma_f32_16x16x32_bf16 v[144:147], v[24:27], v[84:87], 1.0\n"
  "v_fma_f32 v22, v22, s73, v224\n"
  "v_fma_f32 v23, v23, s73, v224\n"
  "v_mfma_f32_16x16x32_bf16 v[148:151], v[24:27], v[244:247], 1.0\n"
  "v_exp_f32 v22, v22\n"
  "v_mfma_f32_16x16x32_bf16 v[120:123], v[24:27], v[76:79], 1.0\n"
  "s_waitcnt lgkmcnt(0)\n"
  "v_fma_f32 v32, v32, s73, v225\n"
  "v_fma_f32 v33, v33, s73, v225\n"
  "v_mfma_f32_16x16x32_bf16 v[124:127], v[24:27], v[80:83], 1.0\n"
  "v_exp_f32 v32, v32\n"
  "v_mfma_f32_16x16x32_bf16 v[128:131], v[24:27], a[252:255], 1.0\n"
  "v_exp_f32 v33, v33\n"
  "v_fma_f32 v34, v34, s73, v225\n"
  "v_exp_f32 v34, v34\n"
  "v_fma_f32 v35, v35, s73, v225\n"
  "v_exp_f32_e64 v35, v35\n"
  "v_mfma_f32_16x16x32_bf16 v[132:135], v[24:27], v[236:239], 1.0\n"
  "v_cvt_pk_bf16_f32 v40, v32, v33\n"
  "v_cvt_pk_bf16_f32 v41, v34, v35\n"
  "v_mfma_f32_16x16x32_bf16 v[32:35], a[232:235], a[68:71], v[248:251]\n"
  "v_fma_f32 v36, v36, s73, v225\n"
  "v_fma_f32 v37, v37, s73, v225\n"
  "v_mfma_f32_16x16x32_bf16 v[32:35], a[228:231], a[64:67], v[32:35]\n"
  "v_exp_f32 v36, v36\n"
  "v_mfma_f32_16x16x32_bf16 v[32:35], a[224:227], a[60:63], v[32:35]\n"
  "v_exp_f32 v37, v37\n"
  "v_mfma_f32_16x16x32_bf16 v[32:35], a[220:223], a[56:59], v[32:35]\n"
  "v_fma_f32 v38, v38, s73, v225\n"
  "v_fma_f32 v39, v39, s73, v225\n"
  "v_mfma_f32_16x16x32_bf16 v[32:35], a[216:219], a[52:55], v[32:35]\n"
  "v_exp_f32 v38, v38\n"
  "v_exp_f32_e64 v39, v39\n"
  "v_mfma_f32_16x16x32_bf16 v[32:35], a[212:215], a[48:51], v[32:35]\n"
  "v_cvt_pk_bf16_f32 v42, v36, v37\n"
  "v_cvt_pk_bf16_f32 v43, v38, v39\n"
  "v_mfma_f32_16x16x32_bf16 v[36:39], a[120:123], a[48:51], v[248:251]\n"
  "s_waitcnt vmcnt(6)\n"
  "ds_read_b128 a[72:75], v222 offset:11200\n"
  "ds_read_b128 a[76:79], v222 offset:11264\n"
  "ds_read_b128 a[80:83], v222 offset:13440\n"
  "v_mfma_f32_16x16x32_bf16 v[36:39], a[124:127], a[52:55], v[36:39]\n"
  "ds_read_b128 a[84:87], v222 offset:13504\n"
  "ds_read_b128 a[88:91], v222 offset:15680\n"
  "ds_read_b128 a[92:95], v222 offset:15744\n"
  "v_mfma_f32_16x16x32_bf16 v[36:39], a[128:131], a[56:59], v[36:39]\n"
  "v_exp_f32 v23, v23\n"
  "v_mfma_f32_16x16x32_bf16 v[36:39], a[132:135], a[60:63], v[36:39]\n"
  "v_fma_f32 v32, v32, s73, v225\n"
  "v_fma_f32 v33, v33, s73, v225\n"
  "v_mfma_f32_16x16x32_bf16 v[36:39], a[136:139], a[64:67], v[36:39]\n"
  "v_exp_f32 v32, v32\n"
  "v_mfma_f32_16x16x32_bf16 v[36:39], a[140:143], a[68:71], v[36:39]\n"
  "s_waitcnt lgkmcnt(5)\n"
  "v_exp_f32 v33, v33\n"
  "v_mfma_f32_16x16x32_bf16 v[48:51], a[164:167], a[72:75], v[248:251]\n"
  "s_waitcnt lgkmcnt(4)\n"
  "v_fma_f32 v34, v34, s73, v225\n"
  "v_fma_f32 v35, v35, s73, v225\n"
  "v_mfma_f32_16x16x32_bf16 v[48:51], a[168:171], a[76:79], v[48:51]\n"
  "s_waitcnt lgkmcnt(3)\n"
  "v_exp_f32 v34, v34\n"
  "v_mfma_f32_16x16x32_bf16 v[48:51], a[172:175], a[80:83], v[48:51]\n"
  "s_waitcnt lgkmcnt(2)\n"
  "v_exp_f32 v35, v35\n"
  "v_mfma_f32_16x16x32_bf16 v[48:51], a[176:179], a[84:87], v[48:51]\n"
  "s_waitcnt lgkmcnt(1)\n"
  "v_fma_f32 v36, v36, s73, v225\n"
  "v_fma_f32 v37, v37, s73, v225\n"
  "v_mfma_f32_16x16x32_bf16 v[48:51], a[180:183], a[88:91], v[48:51]\n"
  "s_waitcnt lgkmcnt(0)\n"
  "v_exp_f32 v36, v36\n"
  "v_mfma_f32_16x16x32_bf16 v[48:51], a[184:187], a[92:95], v[48:51]\n"
  "v_exp_f32 v37, v37\n"
  "v_mfma_f32_16x16x32_bf16 v[52:55], a[188:191], a[72:75], v[248:251]\n"
  "v_fma_f32 v38, v38, s73, v225\n"
  "v_fma_f32 v39, v39, s73, v225\n"
  "v_mfma_f32_16x16x32_bf16 v[52:55], a[192:195], a[76:79], v[52:55]\n"
  "v_exp_f32 v38, v38\n"
  "v_mfma_f32_16x16x32_bf16 v[52:55], a[196:199], a[80:83], v[52:55]\n"
  "v_max3_f32 v226, v48, v49, v50\n"
  "v_max_f32 v226, v226, v51\n"
  "v_mfma_f32_16x16x32_bf16 v[52:55], a[200:203], a[84:87], v[52:55]\n"
  "v_mul_f32 v226, s74, v226\n"
  "v_add_f32 v226, s64, v226\n"
  "v_mfma_f32_16x16x32_bf16 v[52:55], a[204:207], a[88:91], v[52:55]\n"
  "ds_bpermute_b32 v226, v234, v226\n"
  "v_mfma_f32_16x16x32_bf16 v[52:55], a[208:211], a[92:95], v[52:55]\n"
  "v_exp_f32 v39, v39\n"
  "v_mfma_f32_16x16x32_bf16 v[168:171], v[40:43], a[144:147], 1.0\n"
  "ds_write_b64 v231, v[64:65] offset:2048\n"
  "v_mfma_f32_16x16x32_bf16 v[172:175], v[40:43], a[148:151], 1.0\n"
  "v_cvt_pk_bf16_f32 v12, v0, v1\n"
  "v_cvt_pk_bf16_f32 v13, v2, v3\n"
  "v_mfma_f32_16x16x32_bf16 v[176:179], v[40:43], v[84:87], 1.0\n"
  "v_cvt_pk_bf16_f32 v14, v4, v5\n"
  "v_cvt_pk_bf16_f32 v15, v6, v7\n"
  "v_mfma_f32_16x16x32_bf16 v[180:183], v[40:43], v[244:247], 1.0\n"
  "v_cvt_pk_bf16_f32 v28, v16, v17\n"
  "v_cvt_pk_bf16_f32 v29, v18, v19\n"
  "v_mfma_f32_16x16x32_bf16 v[152:155], v[40:43], v[76:79], 1.0\n"
  "s_waitcnt lgkmcnt(1)\n"
  "v_fma_f32 v48, v48, s73, v226\n"
  "v_fma_f32 v49, v49, s73, v226\n"
  "v_mfma_f32_16x16x32_bf16 v[156:159], v[40:43], v[80:83], 1.0\n"
  "v_exp_f32 v48, v48\n"
  "v_mfma_f32_16x16x32_bf16 v[160:163], v[40:43], a[252:255], 1.0\n"
  "v_exp_f32 v49, v49\n"
  "v_fma_f32 v50, v50, s73, v226\n"
  "v_exp_f32 v50, v50\n"
  "v_fma_f32 v51, v51, s73, v226\n"
  "v_exp_f32 v51, v51\n"
  "v_mfma_f32_16x16x32_bf16 v[164:167], v[40:43], v[236:239], 1.0\n"
  "v_cvt_pk_bf16_f32 v56, v48, v49\n"
  "v_cvt_pk_bf16_f32 v57, v50, v51\n"
  "v_mfma_f32_16x16x32_bf16 v[48:51], a[212:215], a[72:75], v[248:251]\n"
  "v_fma_f32 v52, v52, s73, v226\n"
  "v_fma_f32 v53, v53, s73, v226\n"
  "v_mfma_f32_16x16x32_bf16 v[48:51], a[216:219], a[76:79], v[48:51]\n"
  "v_exp_f32 v52, v52\n"
  "v_mfma_f32_16x16x32_bf16 v[48:51], a[220:223], a[80:83], v[48:51]\n"
  "v_exp_f32 v53, v53\n"
  "v_mfma_f32_16x16x32_bf16 v[48:51], a[224:227], a[84:87], v[48:51]\n"
  "v_fma_f32 v54, v54, s73, v226\n"
  "v_fma_f32 v55, v55, s73, v226\n"
  "v_mfma_f32_16x16x32_bf16 v[48:51], a[228:231], a[88:91], v[48:51]\n"
  "v_exp_f32 v54, v54\n"
  "v_exp_f32_e64 v55, v55\n"
  "v_mfma_f32_16x16x32_bf16 v[48:51], a[232:235], a[92:95], v[48:51]\n"
  "v_cvt_pk_bf16_f32 v58, v52, v53\n"
  "v_cvt_pk_bf16_f32 v59, v54, v55\n"
  "v_mfma_f32_16x16x32_bf16 v[52:55], a[140:143], a[92:95], v[248:251]\n"
  "v_cvt_pk_bf16_f32 v30, v20, v21\n"
  "v_cvt_pk_bf16_f32 v31, v22, v23\n"
  "v_mfma_f32_16x16x32_bf16 v[52:55], a[136:139], a[88:91], v[52:55]\n"
  "v_cvt_pk_bf16_f32 v44, v32, v33\n"
  "v_cvt_pk_bf16_f32 v45, v34, v35\n"
  "v_mfma_f32_16x16x32_bf16 v[52:55], a[132:135], a[84:87], v[52:55]\n"
  "v_fma_f32 v48, v48, s73, v226\n"
  "v_fma_f32 v49, v49, s73, v226\n"
  "v_mfma_f32_16x16x32_bf16 v[52:55], a[128:131], a[80:83], v[52:55]\n"
  "v_exp_f32 v48, v48\n"
  "v_mfma_f32_16x16x32_bf16 v[52:55], a[124:127], a[76:79], v[52:55]\n"
  "v_exp_f32 v49, v49\n"
  "v_mfma_f32_16x16x32_bf16 v[52:55], a[120:123], a[72:75], v[52:55]\n"
  "v_fma_f32 v50, v50, s73, v226\n"
  "v_fma_f32 v51, v51, s73, v226\n"
  "v_mfma_f32_16x16x32_bf16 v[200:203], v[56:59], a[144:147], 1.0\n"
  "v_exp_f32 v50, v50\n"
  "v_mfma_f32_16x16x32_bf16 v[204:207], v[56:59], a[148:151], 1.0\n"
  "v_exp_f32 v51, v51\n"
  "v_mfma_f32_16x16x32_bf16 v[208:211], v[56:59], v[84:87], 1.0\n"
  "v_fma_f32 v52, v52, s73, v226\n"
  "v_fma_f32 v53, v53, s73, v226\n"
  "v_mfma_f32_16x16x32_bf16 v[212:215], v[56:59], v[244:247], 1.0\n"
  "v_exp_f32 v52, v52\n"
  "v_mfma_f32_16x16x32_bf16 v[184:187], v[56:59], v[76:79], 1.0\n"
  "v_exp_f32 v53, v53\n"
  "v_mfma_f32_16x16x32_bf16 v[188:191], v[56:59], v[80:83], 1.0\n"
  "v_fma_f32 v54, v54, s73, v226\n"
  "v_fma_f32 v55, v55, s73, v226\n"
  "v_mfma_f32_16x16x32_bf16 v[192:195], v[56:59], a[252:255], 1.0\n"
  "v_exp_f32_e64 v54, v54\n"
  "v_mfma_f32_16x16x32_bf16 v[196:199], v[56:59], v[236:239], 1.0\n"
  "v_exp_f32_e64 v55, v55\n"
  "v_cvt_pk_bf16_f32 v46, v36, v37\n"
  "v_cvt_pk_bf16_f32 v47, v38, v39\n"
  "v_cvt_pk_bf16_f32 v60, v48, v49\n"
  "v_cvt_pk_bf16_f32 v61, v50, v51\n"
  "v_cvt_pk_bf16_f32 v62, v52, v53\n"
  "v_cvt_pk_bf16_f32 v63, v54, v55\n"
  "s_add_i32 s76, s76, 1\n"
  "s_mov_b32 m0, s67\n"
  "s_waitcnt vmcnt(0) lgkmcnt(0)\n"
  "s_barrier\n"
  "ds_read_b128 a[124:127], v216 offset:0\n"
  "ds_read_b64 a[160:161], v231 offset:2048\n"
  "ds_read_b64 a[162:163], v232 offset:2048\n"
  "buffer_load_dwordx4 v221, s[40:43], 0 offen offset:0 lds\n"
  "ds_read_b128 a[120:123], v216 offset:64\n"
  "s_addk_i32 m0, 896\n"
  "ds_read_b128 a[140:143], v216 offset:1024\n"
  "ds_read_b128 a[136:139], v216 offset:1088\n"
  "buffer_load_dwordx4 v221, s[40:43], 0 offen offset:128 lds\n"
  "s_addk_i32 m0, 896\n"
  "ds_read_b128 a[144:147], v216 offset:2048\n"
  "ds_read_b128 a[148:151], v216 offset:2112\n"
  "buffer_load_dwordx4 v221, s[40:43], 0 offen offset:256 lds\n"
  "s_waitcnt lgkmcnt(4)\n"
  "v_mfma_f32_16x16x32_bf16 v[64:67], a[124:127], a[96:99], v[248:251]\n"
  "s_add_u32 m0, s59, -128\n"
  "v_mfma_f32_16x16x32_bf16 v[64:67], a[120:123], a[100:103], v[64:67]\n"
  "s_waitcnt lgkmcnt(2)\n"
  "v_mfma_f32_16x16x32_bf16 v[64:67], a[140:143], a[104:107], v[64:67]\n"
  "buffer_load_dwordx4 v221, s[32:35], 0 offen offset:128 lds\n"
  "v_mfma_f32_16x16x32_bf16 v[64:67], a[136:139], a[108:111], v[64:67]\n"
  "s_waitcnt lgkmcnt(0)\n"
  "v_mfma_f32_16x16x32_bf16 v[64:67], a[144:147], a[112:115], v[64:67]\n"
  "s_addk_i32 m0, 1024\n"
  "v_mfma_f32_16x16x32_bf16 v[64:67], a[148:151], a[116:119], v[64:67]\n"
  "s_mov_b64 s[32:33], s[44:45]\n"
  "v_mfma_f32_16x16x32_bf16 v[0:3], a[148:151], a[20:23], v[248:251]\n"
  "buffer_load_dwordx4 v221, s[44:47], 0 offen offset:128 lds\n"
  "v_mfma_f32_16x16x32_bf16 v[0:3], a[144:147], a[16:19], v[0:3]\n"
  "s_addk_i32 m0, 896\n"
  "v_mfma_f32_16x16x32_bf16 v[0:3], a[136:139], a[12:15], v[0:3]\n"
  "s_add_u32 s40, s40, s31\n"
  "v_mfma_f32_16x16x32_bf16 v[0:3], a[140:143], a[8:11], v[0:3]\n"
  "s_addc_u32 s41, s41, 0\n"
  "v_mfma_f32_16x16x32_bf16 v[0:3], a[120:123], a[4:7], v[0:3]\n"
  "buffer_load_dwordx4 v221, s[44:47], 0 offen offset:256 lds\n"
  "v_mfma_f32_16x16x32_bf16 v[0:3], a[124:127], a[0:3], v[0:3]\n"
  "s_add_u32 s44, s44, s31\n"
  "v_mfma_f32_16x16x32_bf16 v[16:19], a[124:127], a[24:27], v[248:251]\n"
  "s_addc_u32 s45, s45, 0\n"
  "v_mfma_f32_16x16x32_bf16 v[16:19], a[120:123], a[28:31], v[16:19]\n"
  "v_mfma_f32_16x16x32_bf16 v[16:19], a[140:143], a[32:35], v[16:19]\n"
  "ds_read_b128 a[132:135], v235 offset:0\n"
  "v_mfma_f32_16x16x32_bf16 v[16:19], a[136:139], a[36:39], v[16:19]\n"
  "ds_read_b128 a[128:131], v235 offset:64\n"
  "v_mfma_f32_16x16x32_bf16 v[16:19], a[144:147], a[40:43], v[16:19]\n"
  "v_mfma_f32_16x16x32_bf16 v[16:19], a[148:151], a[44:47], v[16:19]\n"
  "v_mfma_f32_16x16x32_bf16 v[32:35], a[148:151], a[68:71], v[248:251]\n"
  "v_mfma_f32_16x16x32_bf16 v[32:35], a[144:147], a[64:67], v[32:35]\n"
  "v_mfma_f32_16x16x32_bf16 v[32:35], a[136:139], a[60:63], v[32:35]\n"
  "v_mfma_f32_16x16x32_bf16 v[32:35], a[140:143], a[56:59], v[32:35]\n"
  "v_mfma_f32_16x16x32_bf16 v[32:35], a[120:123], a[52:55], v[32:35]\n"
  "v_mfma_f32_16x16x32_bf16 v[32:35], a[124:127], a[48:51], v[32:35]\n"
  "v_mfma_f32_16x16x32_bf16 v[48:51], a[124:127], a[72:75], v[248:251]\n"
  "ds_read_b128 a[124:127], v235 offset:1024\n"
  "v_mfma_f32_16x16x32_bf16 v[48:51], a[120:123], a[76:79], v[48:51]\n"
  "ds_read_b128 a[120:123], v235 offset:1088\n"
  "v_mfma_f32_16x16x32_bf16 v[48:51], a[140:143], a[80:83], v[48:51]\n"
  "ds_read_b128 a[140:143], v235 offset:2048\n"
  "v_mfma_f32_16x16x32_bf16 v[48:51], a[136:139], a[84:87], v[48:51]\n"
  "ds_read_b128 a[136:139], v235 offset:2112\n"
  "v_mfma_f32_16x16x32_bf16 v[48:51], a[144:147], a[88:91], v[48:51]\n"
  "v_mfma_f32_16x16x32_bf16 v[48:51], a[148:151], a[92:95], v[48:51]\n"
  "s_waitcnt lgkmcnt(4)\n"
  "v_mfma_f32_16x16x32_bf16 v[4:7], a[132:135], a[0:3], v[248:251]\n"
  "v_mfma_f32_16x16x32_bf16 v[4:7], a[128:131], a[4:7], v[4:7]\n"
  "s_waitcnt lgkmcnt(2)\n"
  "v_mfma_f32_16x16x32_bf16 v[4:7], a[124:127], a[8:11], v[4:7]\n"
  "v_mfma_f32_16x16x32_bf16 v[4:7], a[120:123], a[12:15], v[4:7]\n"
  "s_waitcnt lgkmcnt(0)\n"
  "v_mfma_f32_16x16x32_bf16 v[4:7], a[140:143], a[16:19], v[4:7]\n"
  "v_mfma_f32_16x16x32_bf16 v[4:7], a[136:139], a[20:23], v[4:7]\n"
  "v_mfma_f32_16x16x32_bf16 v[20:23], a[136:139], a[44:47], v[248:251]\n"
  "v_mfma_f32_16x16x32_bf16 v[20:23], a[140:143], a[40:43], v[20:23]\n"
  "v_mfma_f32_16x16x32_bf16 v[20:23], a[120:123], a[36:39], v[20:23]\n"
  "ds_read_b64_tr_b16 a[144:145], v217 offset:2048\n"
  "v_mfma_f32_16x16x32_bf16 v[20:23], a[124:127], a[32:35], v[20:23]\n"
  "ds_read_b64_tr_b16 a[146:147], v218 offset:2048\n"
  "v_mfma_f32_16x16x32_bf16 v[20:23], a[128:131], a[28:31], v[20:23]\n"
  "ds_read_b64_tr_b16 a[148:149], v217 offset:2064\n"
  "v_mfma_f32_16x16x32_bf16 v[20:23], a[132:135], a[24:27], v[20:23]\n"
  "ds_read_b64_tr_b16 a[150:151], v218 offset:2064\n"
  "v_mfma_f32_16x16x32_bf16 v[36:39], a[132:135], a[48:51], v[248:251]\n"
  "v_mfma_f32_16x16x32_bf16 v[36:39], a[128:131], a[52:55], v[36:39]\n"
  "v_mfma_f32_16x16x32_bf16 v[36:39], a[124:127], a[56:59], v[36:39]\n"
  "v_mfma_f32_16x16x32_bf16 v[36:39], a[120:123], a[60:63], v[36:39]\n"
  "v_mfma_f32_16x16x32_bf16 v[36:39], a[140:143], a[64:67], v[36:39]\n"
  "v_mfma_f32_16x16x32_bf16 v[36:39], a[136:139], a[68:71], v[36:39]\n"
  "v_mfma_f32_16x16x32_bf16 v[52:55], a[136:139], a[92:95], v[248:251]\n"
  "ds_read_b64_tr_b16 a[136:137], v219 offset:2048\n"
  "v_mfma_f32_16x16x32_bf16 v[52:55], a[140:143], a[88:91], v[52:55]\n"
  "ds_read_b64_tr_b16 a[138:139], v220 offset:2048\n"
  "v_mfma_f32_16x16x32_bf16 v[52:55], a[120:123], a[84:87], v[52:55]\n"
  "ds_read_b64_tr_b16 a[140:141], v219 offset:2064\n"
  "v_mfma_f32_16x16x32_bf16 v[52:55], a[124:127], a[80:83], v[52:55]\n"
  "ds_read_b64_tr_b16 a[142:143], v220 offset:2064\n"
  "v_fma_f32 v64, v64, s73, v227\n"
  "v_mfma_f32_16x16x32_bf16 v[52:55], a[128:131], a[76:79], v[52:55]\n"
  "v_exp_f32 v64, v64\n"
  "v_mfma_f32_16x16x32_bf16 v[52:55], a[132:135], a[72:75], v[52:55]\n"
  "s_waitcnt lgkmcnt(4)\n"
  "v_fma_f32 v65, v65, s73, v227\n"
  "v_fma_f32 v66, v66, s73, v227\n"
  "v_mfma_f32_16x16x32_bf16 v[104:107], v[12:15], a[144:147], v[104:107]\n"
  "v_exp_f32 v65, v65\n"
  "v_mfma_f32_16x16x32_bf16 v[108:111], v[12:15], a[148:151], v[108:111]\n"
  "v_mfma_f32_16x16x32_bf16 v[136:139], v[28:31], a[144:147], v[136:139]\n"
  "ds_read_b64_tr_b16 a[120:121], v217 offset:0\n"
  "v_mfma_f32_16x16x32_bf16 v[140:143], v[28:31], a[148:151], v[140:143]\n"
  "ds_read_b64_tr_b16 a[122:123], v218 offset:0\n"
  "v_mfma_f32_16x16x32_bf16 v[168:171], v[44:47], a[144:147], v[168:171]\n"
  "ds_read_b64_tr_b16 a[124:125], v217 offset:1024\n"
  "v_mfma_f32_16x16x32_bf16 v[172:175], v[44:47], a[148:151], v[172:175]\n"
  "ds_read_b64_tr_b16 a[126:127], v218 offset:1024\n"
  "v_mfma_f32_16x16x32_bf16 v[200:203], v[60:63], a[144:147], v[200:203]\n"
  "v_mfma_f32_16x16x32_bf16 v[204:207], v[60:63], a[148:151], v[204:207]\n"
  "v_exp_f32_e64 v66, v66\n"
  "v_mfma_f32_16x16x32_bf16 a[244:247], a[160:163], a[144:147], a[244:247]\n"
  "v_mfma_f32_16x16x32_bf16 a[248:251], a[160:163], a[148:151], a[248:251]\n"
  "s_waitcnt lgkmcnt(4)\n"
  "v_mfma_f32_16x16x32_bf16 v[208:211], v[60:63], a[136:139], v[208:211]\n"
  "v_mfma_f32_16x16x32_bf16 v[212:215], v[60:63], a[140:143], v[212:215]\n"
  "v_mfma_f32_16x16x32_bf16 v[176:179], v[44:47], a[136:139], v[176:179]\n"
  "ds_read_b64_tr_b16 a[128:129], v217 offset:16\n"
  "v_mfma_f32_16x16x32_bf16 v[180:183], v[44:47], a[140:143], v[180:183]\n"
  "ds_read_b64_tr_b16 a[130:131], v218 offset:16\n"
  "v_mfma_f32_16x16x32_bf16 v[144:147], v[28:31], a[136:139], v[144:147]\n"
  "ds_read_b64_tr_b16 a[132:133], v217 offset:1040\n"
  "v_mfma_f32_16x16x32_bf16 v[148:151], v[28:31], a[140:143], v[148:151]\n"
  "ds_read_b64_tr_b16 a[134:135], v218 offset:1040\n"
  "v_mfma_f32_16x16x32_bf16 v[112:115], v[12:15], a[136:139], v[112:115]\n"
  "v_mfma_f32_16x16x32_bf16 v[116:119], v[12:15], a[140:143], v[116:119]\n"
  "s_waitcnt lgkmcnt(4)\n"
  "v_mfma_f32_16x16x32_bf16 a[164:167], v[8:11], a[120:123], 1.0\n"
  "v_fma_f32 v67, v67, s73, v227\n"
  "v_mfma_f32_16x16x32_bf16 a[164:167], v[12:15], a[124:127], a[164:167]\n"
  "v_exp_f32 v67, v67\n"
  "v_mfma_f32_16x16x32_bf16 a[180:183], v[24:27], a[120:123], 1.0\n"
  "v_mfma_f32_16x16x32_bf16 a[180:183], v[28:31], a[124:127], a[180:183]\n"
  "v_cvt_pk_bf16_f32 v64, v64, v65\n"
  "v_cvt_pk_bf16_f32 v65, v66, v67\n"
  "v_mfma_f32_16x16x32_bf16 a[196:199], v[40:43], a[120:123], 1.0\n"
  "s_mov_b32 m0, s68\n"
  "ds_read_b64_tr_b16 a[136:137], v219 offset:0\n"
  "v_fma_f32 v0, v0, s73, v223\n"
  "v_mfma_f32_16x16x32_bf16 a[196:199], v[44:47], a[124:127], a[196:199]\n"
  "ds_read_b64_tr_b16 a[138:139], v220 offset:0\n"
  "v_fma_f32 v1, v1, s73, v223\n"
  "v_mfma_f32_16x16x32_bf16 a[212:215], v[56:59], a[120:123], 1.0\n"
  "ds_read_b64_tr_b16 a[140:141], v219 offset:1024\n"
  "v_fma_f32 v2, v2, s73, v223\n"
  "v_mfma_f32_16x16x32_bf16 a[212:215], v[60:63], a[124:127], a[212:215]\n"
  "ds_read_b64_tr_b16 a[142:143], v220 offset:1024\n"
  "v_fma_f32 v3, v3, s73, v223\n"
  "v_mfma_f32_16x16x32_bf16 a[228:231], a[156:159], a[120:123], 1.0\n"
  "buffer_load_dwordx4 v221, s[40:43], 0 offen offset:0 lds\n"
  "v_mfma_f32_16x16x32_bf16 a[228:231], a[160:163], a[124:127], a[228:231]\n"
  "s_waitcnt lgkmcnt(4)\n"
  "v_mfma_f32_16x16x32_bf16 a[232:235], a[156:159], a[128:131], 1.0\n"
  "s_addk_i32 m0, 896\n"
  "v_mfma_f32_16x16x32_bf16 a[232:235], a[160:163], a[132:135], a[232:235]\n"
  "ds_write_b64 v231, v[64:65] offset:0\n"
  "v_mfma_f32_16x16x32_bf16 a[216:219], v[56:59], a[128:131], 1.0\n"
  "buffer_load_dwordx4 v221, s[40:43], 0 offen offset:128 lds\n"
  "v_fma_f32 v16, v16, s73, v224\n"
  "v_mfma_f32_16x16x32_bf16 a[216:219], v[60:63], a[132:135], a[216:219]\n"
  "s_addk_i32 m0, 896\n"
  "ds_read_b64_tr_b16 a[120:121], v219 offset:16\n"
  "v_fma_f32 v17, v17, s73, v224\n"
  "v_mfma_f32_16x16x32_bf16 a[200:203], v[40:43], a[128:131], 1.0\n"
  "ds_read_b64_tr_b16 a[122:123], v220 offset:16\n"
  "v_fma_f32 v18, v18, s73, v224\n"
  "v_mfma_f32_16x16x32_bf16 a[200:203], v[44:47], a[132:135], a[200:203]\n"
  "s_nop 0\n"
  "ds_read_b64_tr_b16 a[124:125], v219 offset:1040\n"
  "v_fma_f32 v19, v19, s73, v224\n"
  "v_mfma_f32_16x16x32_bf16 a[184:187], v[24:27], a[128:131], 1.0\n"
  "buffer_load_dwordx4 v221, s[40:43], 0 offen offset:256 lds\n"
  "v_fma_f32 v32, v32, s73, v225\n"
  "v_mfma_f32_16x16x32_bf16 a[184:187], v[28:31], a[132:135], a[184:187]\n"
  "s_mov_b32 m0, s60\n"
  "ds_read_b64_tr_b16 a[126:127], v220 offset:1040\n"
  "v_fma_f32 v33, v33, s73, v225\n"
  "v_mfma_f32_16x16x32_bf16 a[168:171], v[8:11], a[128:131], 1.0\n"
  "s_add_u32 s28, s96, 184448\n"
  "v_exp_f32 v0, v0\n"
  "v_mfma_f32_16x16x32_bf16 a[168:171], v[12:15], a[132:135], a[168:171]\n"
  "s_waitcnt lgkmcnt(5)\n"
  "v_exp_f32 v1, v1\n"
  "v_mfma_f32_16x16x32_bf16 a[172:175], v[8:11], a[136:139], 1.0\n"
  "buffer_load_dwordx4 v221, s[32:35], 0 offen offset:0 lds\n"
  "v_add_f32 v252, v0, v1\n"
  "v_mfma_f32_16x16x32_bf16 a[172:175], v[12:15], a[140:143], a[172:175]\n"
  "s_addk_i32 m0, 1024\n"
  "v_exp_f32 v2, v2\n"
  "v_mfma_f32_16x16x32_bf16 a[188:191], v[24:27], a[136:139], 1.0\n"
  "s_add_u32 s40, s40, s31\n"
  "v_exp_f32 v3, v3\n"
  "v_mfma_f32_16x16x32_bf16 a[188:191], v[28:31], a[140:143], a[188:191]\n"
  "s_addc_u32 s41, s41, 0\n"
  "v_exp_f32 v16, v16\n"
  "v_mfma_f32_16x16x32_bf16 a[204:207], v[40:43], a[136:139], 1.0\n"
  "buffer_load_dwordx4 v221, s[44:47], 0 offen offset:0 lds\n"
  "v_add_f32 v252, v252, v2\n"
  "v_mfma_f32_16x16x32_bf16 a[204:207], v[44:47], a[140:143], a[204:207]\n"
  "s_addk_i32 m0, 768\n"
  "v_exp_f32 v17, v17\n"
  "v_mfma_f32_16x16x32_bf16 a[220:223], v[56:59], a[136:139], 1.0\n"
  "s_mov_b64 s[32:33], s[44:45]\n"
  "v_exp_f32 v18, v18\n"
  "v_mfma_f32_16x16x32_bf16 a[220:223], v[60:63], a[140:143], a[220:223]\n"
  "s_waitcnt lgkmcnt(0)\n"
  "v_exp_f32 v19, v19\n"
  "v_mfma_f32_16x16x32_bf16 a[224:227], v[56:59], a[120:123], 1.0\n"
  "buffer_load_dwordx4 v221, s[44:47], 0 offen offset:256 lds\n"
  "v_add_f32_e64 v252, v252, v3\n"
  "v_mfma_f32_16x16x32_bf16 a[224:227], v[60:63], a[124:127], a[224:227]\n"
  "s_add_u32 s44, s44, s31\n"
  "v_exp_f32 v32, v32\n"
  "v_mfma_f32_16x16x32_bf16 a[208:211], v[40:43], a[120:123], 1.0\n"
  "s_addc_u32 s45, s45, 0\n"
  "v_exp_f32 v33, v33\n"
  "v_mfma_f32_16x16x32_bf16 a[208:211], v[44:47], a[124:127], a[208:211]\n"
  "s_cmp_eq_u32 s95, 0\n"
  "s_cselect_b32 s95, 3, s95\n"
  "s_subb_u32 s100, s100, -11\n"
  "v_add_f32 v253, v16, v17\n"
  "v_add_f32 v253, v253, v18\n"
  "v_mfma_f32_16x16x32_bf16 a[192:195], v[24:27], a[120:123], 1.0\n"
  "s_sub_u32 s95, s95, 1\n"
  "v_add_f32 v253, v253, v19\n"
  "v_fma_f32 v34, v34, s73, v225\n"
  "v_mfma_f32_16x16x32_bf16 a[192:195], v[28:31], a[124:127], a[192:195]\n"
  "s_waitcnt vmcnt(6)\n"
  "s_barrier\n"
  "ds_read_b128 a[128:131], v216 offset:12352\n"
  "v_mfma_f32_16x16x32_bf16 a[176:179], v[8:11], a[120:123], 1.0\n"
  "s_lshr_b32 s79, s100, 0\n"
  "ds_read_b128 a[132:135], v216 offset:12416\n"
  "v_fma_f32 v35, v35, s73, v225\n"
  "v_mfma_f32_16x16x32_bf16 a[176:179], v[12:15], a[124:127], a[176:179]\n"
  "s_mul_i32 s79, s79, 7\n"
  "ds_read_b128 a[120:123], v216 offset:13376\n"
  "v_add_f32 v254, v32, v33\n"
  "v_mfma_f32_16x16x32_bf16 v[68:71], v[8:11], a[152:155], 0\n"
  "s_add_u32 s79, s79, s100\n"
  "ds_read_b128 a[124:127], v216 offset:13440\n"
  "v_cvt_pk_bf16_f32 v8, v0, v1\n"
  "v_mfma_f32_16x16x32_bf16 v[68:71], v[12:15], a[152:155], v[68:71]\n"
  "s_add_u32 s79, s79, s101\n"
  "ds_read_b128 a[136:139], v216 offset:14400\n"
  "v_cvt_pk_bf16_f32 v9, v2, v3\n"
  "v_mfma_f32_16x16x32_bf16 v[72:75], v[24:27], a[152:155], 0\n"
  "ds_read_b128 a[140:143], v216 offset:14464\n"
  "v_cvt_pk_bf16_f32 v24, v16, v17\n"
  "v_mfma_f32_16x16x32_bf16 v[72:75], v[28:31], a[152:155], v[72:75]\n"
  "s_mov_b32 m0, s66\n"
  "v_exp_f32 v34, v34\n"
  "v_mfma_f32_16x16x32_bf16 v[76:79], v[40:43], a[152:155], 0\n"
  "s_min_u32 s79, s79, 0x1ff\n"
  "v_exp_f32 v35, v35\n"
  "v_mfma_f32_16x16x32_bf16 v[76:79], v[44:47], a[152:155], v[76:79]\n"
  "s_mul_i32 s96, s79, 196608\n"
  "v_cvt_pk_bf16_f32 v25, v18, v19\n"
  "v_add_f32 v254, v254, v34\n"
  "v_mfma_f32_16x16x32_bf16 v[80:83], v[56:59], a[152:155], 0\n"
  "s_mul_i32 s92, s79, 325632\n"
  "v_add_f32 v254, v254, v35\n"
  "v_cvt_pk_bf16_f32 v40, v32, v33\n"
  "v_mfma_f32_16x16x32_bf16 v[80:83], v[60:63], a[152:155], v[80:83]\n"
  "buffer_load_dwordx4 v221, s[40:43], 0 offen offset:0 lds\n"
  "v_mfma_f32_16x16x32_bf16 v[84:87], a[156:159], a[152:155], 0\n"
  "s_add_u32 s96, s96, s39\n"
  "ds_read_b64 a[156:157], v231 offset:0\n"
  "v_cvt_pk_bf16_f32 v41, v34, v35\n"
  "v_mfma_f32_16x16x32_bf16 v[84:87], a[160:163], a[152:155], v[84:87]\n"
  "s_branch L_CUT_%=\n"
  ".p2align 5\n"
  "L_BIG_%=:\n"
  "v_mfma_f32_16x16x32_bf16 v[16:19], a[120:123], a[24:27], v[248:251]\n"
  "s_mov_b32 m0, s60\n"
  "v_fma_f32 v64, v64, s73, v227\n"
  "v_fma_f32 v65, v65, s73, v227\n"
  "v_mfma_f32_16x16x32_bf16 v[16:19], a[124:127], a[28:31], v[16:19]\n"
  "buffer_load_dwordx4 v221, s[32:35], 0 offen offset:0 lds\n"
  "v_fma_f32 v66, v66, s73, v227\n"
  "v_mfma_f32_16x16x32_bf16 v[16:19], a[128:131], a[32:35], v[16:19]\n"
  "s_addk_i32 m0, 1024\n"
  "ds_read_b64_tr_b16 a[144:145], v217 offset:0\n"
  "v_fma_f32 v67, v67, s73, v227\n"
  "v_mfma_f32_16x16x32_bf16 v[16:19], a[132:135], a[36:39], v[16:19]\n"
  "s_mov_b64 s[36:37], s[44:45]\n"
  "ds_read_b64_tr_b16 a[146:147], v218 offset:0\n"
  "v_fma_f32 v0, v0, s73, v223\n"
  "v_mfma_f32_16x16x32_bf16 v[16:19], a[136:139], a[40:43], v[16:19]\n"
  "ds_read_b64_tr_b16 a[148:149], v217 offset:1024\n"
  "v_fma_f32 v1, v1, s73, v223\n"
  "v_mfma_f32_16x16x32_bf16 v[16:19], a[140:143], a[44:47], v[16:19]\n"
  "buffer_load_dwordx4 v221, s[44:47], 0 offen offset:0 lds\n"
  "v_fma_f32 v2, v2, s73, v223\n"
  "v_mfma_f32_16x16x32_bf16 v[32:35], a[140:143], a[68:71], v[248:251]\n"
  "s_addk_i32 m0, 768\n"
  "ds_read_b64_tr_b16 a[150:151], v218 offset:1024\n"
  "v_fma_f32 v3, v3, s73, v223\n"
  "v_mfma_f32_16x16x32_bf16 v[32:35], a[136:139], a[64:67], v[32:35]\n"
  "s_add_u32 s32, s88, s28\n"
  "v_exp_f32 v64, v64\n"
  "v_mfma_f32_16x16x32_bf16 v[32:35], a[132:135], a[60:63], v[32:35]\n"
  "s_addc_u32 s33, s89, 0\n"
  "v_exp_f32 v65, v65\n"
  "v_mfma_f32_16x16x32_bf16 v[32:35], a[128:131], a[56:59], v[32:35]\n"
  "buffer_load_dwordx4 v221, s[44:47], 0 offen offset:256 lds\n"
  "v_cvt_pk_bf16_f32 v64, v64, v65\n"
  "v_mfma_f32_16x16x32_bf16 v[32:35], a[124:127], a[52:55], v[32:35]\n"
  "s_add_u32 s44, s32, s31\n"
  "v_exp_f32 v66, v66\n"
  "v_mfma_f32_16x16x32_bf16 v[32:35], a[120:123], a[48:51], v[32:35]\n"
  "s_addc_u32 s45, s33, 0\n"
  "v_exp_f32 v67, v67\n"
  "v_mfma_f32_16x16x32_bf16 v[48:51], a[120:123], a[72:75], v[248:251]\n"
  "s_mov_b32 s46, s30\n"
  "v_exp_f32 v0, v0\n"
  "v_mfma_f32_16x16x32_bf16 v[48:51], a[124:127], a[76:79], v[48:51]\n"
  "v_exp_f32 v1, v1\n"
  "v_mfma_f32_16x16x32_bf16 v[48:51], a[128:131], a[80:83], v[48:51]\n"
  "ds_read_b64_tr_b16 a[120:121], v217 offset:16\n"
  "v_cvt_pk_bf16_f32 v65, v66, v67\n"
  "v_mfma_f32_16x16x32_bf16 v[48:51], a[132:135], a[84:87], v[48:51]\n"
  "ds_read_b64_tr_b16 a[122:123], v218 offset:16\n"
  "v_add_f32 v252, v252, v0\n"
  "v_mfma_f32_16x16x32_bf16 v[48:51], a[136:139], a[88:91], v[48:51]\n"
  "ds_read_b64_tr_b16 a[124:125], v217 offset:1040\n"
  "v_add_f32 v252, v252, v1\n"
  "v_mfma_f32_16x16x32_bf16 v[48:51], a[140:143], a[92:95], v[48:51]\n"
  "s_waitcnt lgkmcnt(3)\n"
  "ds_read_b64_tr_b16 a[126:127], v218 offset:1040\n"
  "v_fma_f32 v16, v16, s73, v224\n"
  "v_mfma_f32_16x16x32_bf16 a[228:231], a[156:159], a[144:147], a[228:231]\n"
  "v_exp_f32 v2, v2\n"
  "v_mfma_f32_16x16x32_bf16 a[228:231], a[160:163], a[148:151], a[228:231]\n"
  "v_exp_f32 v3, v3\n"
  "v_mfma_f32_16x16x32_bf16 a[212:215], v[56:59], a[144:147], a[212:215]\n"
  "v_exp_f32 v16, v16\n"
  "v_mfma_f32_16x16x32_bf16 a[212:215], v[60:63], a[148:151], a[212:215]\n"
  "v_add_f32 v252, v252, v2\n"
  "v_add_f32 v252, v252, v3\n"
  "v_mfma_f32_16x16x32_bf16 a[196:199], v[40:43], a[144:147], a[196:199]\n"
  "v_fma_f32 v17, v17, s73, v224\n"
  "v_fma_f32 v18, v18, s73, v224\n"
  "v_mfma_f32_16x16x32_bf16 a[196:199], v[44:47], a[148:151], a[196:199]\n"
  "v_exp_f32 v17, v17\n"
  "v_mfma_f32_16x16x32_bf16 a[180:183], v[24:27], a[144:147], a[180:183]\n"
  "ds_read_b64_tr_b16 a[128:129], v219 offset:0\n"
  "v_fma_f32 v19, v19, s73, v224\n"
  "v_mfma_f32_16x16x32_bf16 a[180:183], v[28:31], a[148:151], a[180:183]\n"
  "s_waitcnt vmcnt(14)\n"
  "ds_read_b64_tr_b16 a[130:131], v220 offset:0\n"
  "v_add_f32 v253, v253, v16\n"
  "v_mfma_f32_16x16x32_bf16 a[164:167], v[8:11], a[144:147], a[164:167]\n"
  "s_waitcnt vmcnt(12)\n"
  "ds_read_b64_tr_b16 a[132:133], v219 offset:1024\n"
  "v_add_f32 v253, v253, v17\n"
  "v_mfma_f32_16x16x32_bf16 a[164:167], v[12:15], a[148:151], a[164:167]\n"
  "ds_read_b64_tr_b16 a[134:135], v220 offset:1024\n"
  "v_fma_f32 v32, v32, s73, v225\n"
  "v_mfma_f32_16x16x32_bf16 v[84:87], a[156:159], a[152:155], v[84:87]\n"
  "v_exp_f32 v18, v18\n"
  "v_mfma_f32_16x16x32_bf16 v[84:87], a[160:163], a[152:155], v[84:87]\n"
  "s_waitcnt lgkmcnt(4)\n"
  "v_exp_f32 v19, v19\n"
  "v_mfma_f32_16x16x32_bf16 a[168:171], v[8:11], a[120:123], a[168:171]\n"
  "v_exp_f32 v32, v32\n"
  "v_mfma_f32_16x16x32_bf16 a[168:171], v[12:15], a[124:127], a[168:171]\n"
  "v_add_f32 v253, v253, v18\n"
  "v_add_f32 v253, v253, v19\n"
  "v_mfma_f32_16x16x32_bf16 a[184:187], v[24:27], a[120:123], a[184:187]\n"
  "ds_read_b128 a[136:139], v235 offset:0\n"
  "v_fma_f32 v33, v33, s73, v225\n"
  "v_mfma_f32_16x16x32_bf16 a[184:187], v[28:31], a[124:127], a[184:187]\n"
  "ds_read_b128 a[140:143], v235 offset:64\n"
  "v_fma_f32 v34, v34, s73, v225\n"
  "v_mfma_f32_16x16x32_bf16 a[200:203], v[40:43], a[120:123], a[200:203]\n"
  "ds_read_b128 a[144:147], v235 offset:1024\n"
  "v_fma_f32 v35, v35, s73, v225\n"
  "v_mfma_f32_16x16x32_bf16 a[200:203], v[44:47], a[124:127], a[200:203]\n"
  "ds_read_b128 a[148:151], v235 offset:1088\n"
  "v_add_f32 v254, v254, v32\n"
  "v_mfma_f32_16x16x32_bf16 a[216:219], v[56:59], a[120:123], a[216:219]\n"
  "v_exp_f32 v33, v33\n"
  "v_mfma_f32_16x16x32_bf16 a[216:219], v[60:63], a[124:127], a[216:219]\n"
  "v_exp_f32 v34, v34\n"
  "v_mfma_f32_16x16x32_bf16 a[232:235], a[156:159], a[120:123], a[232:235]\n"
  "ds_read_b128 a[120:123], v235 offset:2048\n"
  "v_add_f32 v254, v254, v33\n"
  "v_mfma_f32_16x16x32_bf16 a[232:235], a[160:163], a[124:127], a[232:235]\n"
  "s_waitcnt lgkmcnt(5)\n"
  "ds_read_b128 a[124:127], v235 offset:2112\n"
  "v_add_f32 v254, v254, v34\n"
  "v_mfma_f32_16x16x32_bf16 a[220:223], v[56:59], a[128:131], a[220:223]\n"
  "v_exp_f32 v35, v35\n"
  "v_mfma_f32_16x16x32_bf16 a[220:223], v[60:63], a[132:135], a[220:223]\n"
  "ds_write_b64 v231, v[64:65] offset:0\n"
  "v_mfma_f32_16x16x32_bf16 a[204:207], v[40:43], a[128:131], a[204:207]\n"
  "v_add_f32 v254, v254, v35\n"
  "v_fma_f32 v48, v48, s73, v226\n"
  "v_mfma_f32_16x16x32_bf16 a[204:207], v[44:47], a[132:135], a[204:207]\n"
  "v_exp_f32 v48, v48\n"
  "v_mfma_f32_16x16x32_bf16 a[188:191], v[24:27], a[128:131], a[188:191]\n"
  "v_fma_f32 v49, v49, s73, v226\n"
  "v_fma_f32 v50, v50, s73, v226\n"
  "v_mfma_f32_16x16x32_bf16 a[188:191], v[28:31], a[132:135], a[188:191]\n"
  "v_exp_f32 v49, v49\n"
  "v_mfma_f32_16x16x32_bf16 a[172:175], v[8:11], a[128:131], a[172:175]\n"
  "v_exp_f32 v50, v50\n"
  "v_mfma_f32_16x16x32_bf16 a[172:175], v[12:15], a[132:135], a[172:175]\n"
  "s_waitcnt lgkmcnt(5)\n"
  "v_fma_f32 v51, v51, s73, v226\n"
  "v_add_f32 v255, v255, v48\n"
  "v_mfma_f32_16x16x32_bf16 v[4:7], a[136:139], a[0:3], v[248:251]\n"
  "v_exp_f32 v51, v51\n"
  "v_mfma_f32_16x16x32_bf16 v[4:7], a[140:143], a[4:7], v[4:7]\n"
  "s_waitcnt lgkmcnt(3)\n"
  "v_add_f32 v255, v255, v49\n"
  "v_add_f32 v255, v255, v50\n"
  "v_mfma_f32_16x16x32_bf16 v[4:7], a[144:147], a[8:11], v[4:7]\n"
  "v_add_f32 v255, v255, v51\n"
  "v_mov_b32 v237, 0\n"
  "v_mfma_f32_16x16x32_bf16 v[4:7], a[148:151], a[12:15], v[4:7]\n"
  "s_waitcnt lgkmcnt(1)\n"
  "v_mov_b32 v238, 0\n"
  "v_mov_b32 v239, 0\n"
  "v_mfma_f32_16x16x32_bf16 v[4:7], a[120:123], a[16:19], v[4:7]\n"
  "v_mov_b32 v241, 0\n"
  "v_mov_b32 v242, 0\n"
  "v_mfma_f32_16x16x32_bf16 v[4:7], a[124:127], a[20:23], v[4:7]\n"
  "v_mov_b32 v243, 0\n"
  "v_bfe_u32 v234, v231, 1, 6\n"
  "v_mfma_f32_16x16x32_bf16 v[20:23], a[124:127], a[44:47], v[248:251]\n"
  "v_mfma_f32_16x16x32_bf16 v[20:23], a[120:123], a[40:43], v[20:23]\n"
  "v_mfma_f32_16x16x32_bf16 v[20:23], a[148:151], a[36:39], v[20:23]\n"
  "ds_read_b64_tr_b16 a[128:129], v219 offset:16\n"
  "v_fma_f32 v4, v4, s73, v223\n"
  "v_mfma_f32_16x16x32_bf16 v[20:23], a[144:147], a[32:35], v[20:23]\n"
  "ds_read_b64_tr_b16 a[130:131], v220 offset:16\n"
  "v_fma_f32 v5, v5, s73, v223\n"
  "v_mfma_f32_16x16x32_bf16 v[20:23], a[140:143], a[28:31], v[20:23]\n"
  "ds_read_b64_tr_b16 a[132:133], v219 offset:1040\n"
  "v_fma_f32 v6, v6, s73, v223\n"
  "v_mfma_f32_16x16x32_bf16 v[20:23], a[136:139], a[24:27], v[20:23]\n"
  "ds_read_b64_tr_b16 a[134:135], v220 offset:1040\n"
  "v_fma_f32 v7, v7, s73, v223\n"
  "v_mfma_f32_16x16x32_bf16 v[36:39], a[136:139], a[48:51], v[248:251]\n"
  "v_exp_f32 v4, v4\n"
  "v_mfma_f32_16x16x32_bf16 v[36:39], a[140:143], a[52:55], v[36:39]\n"
  "v_exp_f32 v5, v5\n"
  "v_mfma_f32_16x16x32_bf16 v[36:39], a[144:147], a[56:59], v[36:39]\n"
  "v_exp_f32 v6, v6\n"
  "v_mfma_f32_16x16x32_bf16 v[36:39], a[148:151], a[60:63], v[36:39]\n"
  "v_exp_f32 v7, v7\n"
  "v_mfma_f32_16x16x32_bf16 v[36:39], a[120:123], a[64:67], v[36:39]\n"
  "v_add_f32 v252, v252, v4\n"
  "v_add_f32 v252, v252, v5\n"
  "v_mfma_f32_16x16x32_bf16 v[36:39], a[124:127], a[68:71], v[36:39]\n"
  "v_add_f32 v252, v252, v6\n"
  "v_add_f32 v252, v252, v7\n"
  "v_mfma_f32_16x16x32_bf16 v[52:55], a[124:127], a[92:95], v[248:251]\n"
  "ds_read_b64_tr_b16 a[124:125], v217 offset:2048\n"
  "v_fma_f32 v20, v20, s73, v224\n"
  "v_mfma_f32_16x16x32_bf16 v[52:55], a[120:123], a[88:91], v[52:55]\n"
  "ds_read_b64_tr_b16 a[126:127], v218 offset:2048\n"
  "v_fma_f32 v21, v21, s73, v224\n"
  "v_mfma_f32_16x16x32_bf16 v[52:55], a[148:151], a[84:87], v[52:55]\n"
  "ds_read_b64_tr_b16 a[120:121], v217 offset:2064\n"
  "v_fma_f32 v22, v22, s73, v224\n"
  "v_mfma_f32_16x16x32_bf16 v[52:55], a[144:147], a[80:83], v[52:55]\n"
  "ds_read_b64_tr_b16 a[122:123], v218 offset:2064\n"
  "v_fma_f32 v23, v23, s73, v224\n"
  "v_mfma_f32_16x16x32_bf16 v[52:55], a[140:143], a[76:79], v[52:55]\n"
  "v_exp_f32 v20, v20\n"
  "v_mfma_f32_16x16x32_bf16 v[52:55], a[136:139], a[72:75], v[52:55]\n"
  "s_waitcnt lgkmcnt(4)\n"
  "v_exp_f32 v21, v21\n"
  "v_mfma_f32_16x16x32_bf16 a[176:179], v[8:11], a[128:131], a[176:179]\n"
  "v_cvt_pk_bf16_f32 v8, v0, v1\n"
  "v_cvt_pk_bf16_f32 v9, v2, v3\n"
  "v_mfma_f32_16x16x32_bf16 a[176:179], v[12:15], a[132:135], a[176:179]\n"
  "v_exp_f32 v22, v22\n"
  "v_mfma_f32_16x16x32_bf16 a[192:195], v[24:27], a[128:131], a[192:195]\n"
  "ds_read_b64_tr_b16 a[140:141], v219 offset:2048\n"
  "v_cvt_pk_bf16_f32 v24, v16, v17\n"
  "v_mfma_f32_16x16x32_bf16 a[192:195], v[28:31], a[132:135], a[192:195]\n"
  "ds_read_b64_tr_b16 a[142:143], v220 offset:2048\n"
  "v_cvt_pk_bf16_f32 v25, v18, v19\n"
  "v_mfma_f32_16x16x32_bf16 a[208:211], v[40:43], a[128:131], a[208:211]\n"
  "ds_read_b64_tr_b16 a[136:137], v219 offset:2064\n"
  "v_cvt_pk_bf16_f32 v40, v32, v33\n"
  "v_mfma_f32_16x16x32_bf16 a[208:211], v[44:47], a[132:135], a[208:211]\n"
  "ds_read_b64_tr_b16 a[138:139], v220 offset:2064\n"
  "v_cvt_pk_bf16_f32 v41, v34, v35\n"
  "v_mfma_f32_16x16x32_bf16 a[224:227], v[56:59], a[128:131], a[224:227]\n"
  "v_exp_f32 v23, v23\n"
  "v_mfma_f32_16x16x32_bf16 a[224:227], v[60:63], a[132:135], a[224:227]\n"
  "s_waitcnt lgkmcnt(4)\n"
  "v_cvt_pk_bf16_f32 v56, v48, v49\n"
  "v_cvt_pk_bf16_f32 v57, v50, v51\n"
  "v_mfma_f32_16x16x32_bf16 a[244:247], a[160:163], a[124:127], a[244:247]\n"
  "v_cvt_pk_bf16_f32 v10, v4, v5\n"
  "v_cvt_pk_bf16_f32 v11, v6, v7\n"
  "v_mfma_f32_16x16x32_bf16 a[248:251], a[160:163], a[120:123], a[248:251]\n"
  "v_add_f32 v253, v253, v20\n"
  "v_add_f32 v253, v253, v21\n"
  "v_mfma_f32_16x16x32_bf16 v[200:203], v[60:63], a[124:127], v[200:203]\n"
  "v_add_f32 v253, v253, v22\n"
  "v_add_f32 v253, v253, v23\n"
  "v_mfma_f32_16x16x32_bf16 v[204:207], v[60:63], a[120:123], v[204:207]\n"
  "v_cvt_pk_bf16_f32 v26, v20, v21\n"
  "v_cvt_pk_bf16_f32 v27, v22, v23\n"
  "v_mfma_f32_16x16x32_bf16 v[168:171], v[44:47], a[124:127], v[168:171]\n"
  "v_fma_f32 v36, v36, s73, v225\n"
  "v_fma_f32 v37, v37, s73, v225\n"
  "v_mfma_f32_16x16x32_bf16 v[172:175], v[44:47], a[120:123], v[172:175]\n"
  "s_waitcnt vmcnt(6) lgkmcnt(0)\n"
  "s_barrier\n"
  "ds_read_b128 a[128:131], v216 offset:12352\n"
  "v_mfma_f32_16x16x32_bf16 v[136:139], v[28:31], a[124:127], v[136:139]\n"
  "ds_read_b64 a[156:157], v231 offset:0\n"
  "v_fma_f32 v38, v38, s73, v225\n"
  "v_mfma_f32_16x16x32_bf16 v[140:143], v[28:31], a[120:123], v[140:143]\n"
  "ds_read_b64 a[158:159], v232 offset:0\n"
  "v_fma_f32 v39, v39, s73, v225\n"
  "v_mfma_f32_16x16x32_bf16 v[104:107], v[12:15], a[124:127], v[104:107]\n"
  "ds_read_b128 a[132:135], v216 offset:12416\n"
  "v_fma_f32 v52, v52, s73, v226\n"
  "v_mfma_f32_16x16x32_bf16 v[108:111], v[12:15], a[120:123], v[108:111]\n"
  "ds_read_b128 a[124:127], v216 offset:13376\n"
  "v_fma_f32 v53, v53, s73, v226\n"
  "v_mfma_f32_16x16x32_bf16 v[112:115], v[12:15], a[140:143], v[112:115]\n"
  "ds_read_b128 a[120:123], v216 offset:13440\n"
  "v_fma_f32 v54, v54, s73, v226\n"
  "v_mfma_f32_16x16x32_bf16 v[116:119], v[12:15], a[136:139], v[116:119]\n"
  "s_mov_b32 m0, s66\n"
  "ds_read_b128 a[148:151], v216 offset:14400\n"
  "v_fma_f32 v55, v55, s73, v226\n"
  "v_mfma_f32_16x16x32_bf16 v[144:147], v[28:31], a[140:143], v[144:147]\n"
  "ds_read_b128 a[144:147], v216 offset:14464\n"
  "v_and_b32 v236, 0xffff0000, v252\n"
  "v_mfma_f32_16x16x32_bf16 v[148:151], v[28:31], a[136:139], v[148:151]\n"
  "v_exp_f32 v36, v36\n"
  "v_mfma_f32_16x16x32_bf16 v[176:179], v[44:47], a[140:143], v[176:179]\n"
  "v_exp_f32 v37, v37\n"
  "v_mfma_f32_16x16x32_bf16 v[180:183], v[44:47], a[136:139], v[180:183]\n"
  "buffer_load_dwordx4 v221, s[40:43], 0 offen offset:0 lds\n"
  "v_mfma_f32_16x16x32_bf16 v[208:211], v[60:63], a[140:143], v[208:211]\n"
  "s_addk_i32 m0, 896\n"
  "v_exp_f32 v38, v38\n"
  "v_mfma_f32_16x16x32_bf16 v[212:215], v[60:63], a[136:139], v[212:215]\n"
  "s_waitcnt lgkmcnt(4)\n"
  "v_exp_f32 v39, v39\n"
  "v_mfma_f32_16x16x32_bf16 v[64:67], a[128:131], a[96:99], v[248:251]\n"
  "v_exp_f32 v52, v52\n"
  "v_mfma_f32_16x16x32_bf16 v[64:67], a[132:135], a[100:103], v[64:67]\n"
  "s_waitcnt lgkmcnt(2)\n"
  "buffer_load_dwordx4 v221, s[40:43], 0 offen offset:128 lds\n"
  "v_add_f32 v254, v254, v36\n"
  "v_mfma_f32_16x16x32_bf16 v[64:67], a[124:127], a[104:107], v[64:67]\n"
  "s_addk_i32 m0, 896\n"
  "v_exp_f32 v53, v53\n"
  "v_mfma_f32_16x16x32_bf16 v[64:67], a[120:123], a[108:111], v[64:67]\n"
  "s_waitcnt lgkmcnt(0)\n"
  "v_exp_f32 v54, v54\n"
  "v_mfma_f32_16x16x32_bf16 v[64:67], a[148:151], a[112:115], v[64:67]\n"
  "v_exp_f32 v55, v55\n"
  "v_mfma_f32_16x16x32_bf16 v[64:67], a[144:147], a[116:119], v[64:67]\n"
  "buffer_load_dwordx4 v221, s[40:43], 0 offen offset:256 lds\n"
  "v_add_f32 v254, v254, v37\n"
  "v_mfma_f32_16x16x32_bf16 v[0:3], a[144:147], a[20:23], v[248:251]\n"
  "s_add_u32 s40, s40, -12288\n"
  "v_add_f32 v254, v254, v38\n"
  "v_add_f32 v254, v254, v39\n"
  "v_mfma_f32_16x16x32_bf16 v[0:3], a[148:151], a[16:19], v[0:3]\n"
  "s_addc_u32 s41, s41, -1\n"
  "v_cvt_pk_bf16_f32 v42, v36, v37\n"
  "v_cvt_pk_bf16_f32 v43, v38, v39\n"
  "v_mfma_f32_16x16x32_bf16 v[0:3], a[120:123], a[12:15], v[0:3]\n"
  "v_add_f32 v255, v255, v52\n"
  "v_add_f32 v255, v255, v53\n"
  "v_mfma_f32_16x16x32_bf16 v[0:3], a[124:127], a[8:11], v[0:3]\n"
  "s_mov_b32 m0, s58\n"
  "v_add_f32 v255, v255, v54\n"
  "v_add_f32 v255, v255, v55\n"
  "v_mfma_f32_16x16x32_bf16 v[0:3], a[132:135], a[4:7], v[0:3]\n"
  "v_cvt_pk_bf16_f32 v58, v52, v53\n"
  "v_cvt_pk_bf16_f32 v59, v54, v55\n"
  "v_mfma_f32_16x16x32_bf16 v[0:3], a[128:131], a[0:3], v[0:3]\n"
  "v_sub_f32 v252, v252, v236\n"
  "v_or_b32_sdwa v236, v236, v252 dst_sel:DWORD dst_unused:UNUSED_PAD src0_sel:DWORD src1_sel:WORD_1\n"
  "v_mfma_f32_16x16x32_bf16 v[16:19], a[128:131], a[24:27], v[248:251]\n"
  "s_add_u32 s71, s92, s83\n"
  "v_and_b32 v240, 0xffff0000, v253\n"
  "v_sub_f32 v253, v253, v240\n"
  "v_mfma_f32_16x16x32_bf16 v[16:19], a[132:135], a[28:31], v[16:19]\n"
  "buffer_load_dwordx4 v221, s[32:35], 0 offen offset:0 lds\n"
  "v_or_b32_sdwa v240, v240, v253 dst_sel:DWORD dst_unused:UNUSED_PAD src0_sel:DWORD src1_sel:WORD_1\n"
  "v_mfma_f32_16x16x32_bf16 v[16:19], a[124:127], a[32:35], v[16:19]\n"
  "s_addk_i32 m0, 1024\n"
  "ds_read_b128 a[140:143], v235 offset:12352\n"
  "v_fma_f32 v64, v64, s73, v227\n"
  "v_mfma_f32_16x16x32_bf16 v[16:19], a[120:123], a[36:39], v[16:19]\n"
  "s_mov_b64 s[32:33], s[36:37]\n"
  "ds_read_b128 a[136:139], v235 offset:12416\n"
  "v_fma_f32 v65, v65, s73, v227\n"
  "v_mfma_f32_16x16x32_bf16 v[16:19], a[148:151], a[40:43], v[16:19]\n"
  "v_exp_f32 v64, v64\n"
  "v_mfma_f32_16x16x32_bf16 v[16:19], a[144:147], a[44:47], v[16:19]\n"
  "buffer_load_dwordx4 v221, s[44:47], 0 offen offset:0 lds\n"
  "v_fma_f32 v66, v66, s73, v227\n"
  "v_mfma_f32_16x16x32_bf16 v[32:35], a[144:147], a[68:71], v[248:251]\n"
  "s_addk_i32 m0, 768\n"
  "v_exp_f32 v65, v65\n"
  "v_mfma_f32_16x16x32_bf16 v[32:35], a[148:151], a[64:67], v[32:35]\n"
  "v_exp_f32 v66, v66\n"
  "v_mfma_f32_16x16x32_bf16 v[32:35], a[120:123], a[60:63], v[32:35]\n"
  "v_fma_f32 v67, v67, s73, v227\n"
  "v_cvt_pk_bf16_f32 v64, v64, v65\n"
  "v_mfma_f32_16x16x32_bf16 v[32:35], a[124:127], a[56:59], v[32:35]\n"
  "buffer_load_dwordx4 v221, s[44:47], 0 offen offset:256 lds\n"
  "v_fma_f32 v0, v0, s73, v223\n"
  "v_mfma_f32_16x16x32_bf16 v[32:35], a[132:135], a[52:55], v[32:35]\n"
  "s_add_u32 s44, s44, -12288\n"
  "v_exp_f32 v67, v67\n"
  "v_mfma_f32_16x16x32_bf16 v[32:35], a[128:131], a[48:51], v[32:35]\n"
  "s_addc_u32 s45, s45, -1\n"
  "v_exp_f32 v0, v0\n"
  "v_mfma_f32_16x16x32_bf16 v[48:51], a[128:131], a[72:75], v[248:251]\n"
  "ds_read_b128 a[128:131], v235 offset:13376\n"
  "v_cvt_pk_bf16_f32 v65, v66, v67\n"
  "v_mfma_f32_16x16x32_bf16 v[48:51], a[132:135], a[76:79], v[48:51]\n"
  "ds_read_b128 a[132:135], v235 offset:13440\n"
  "v_fma_f32 v1, v1, s73, v223\n"
  "v_mfma_f32_16x16x32_bf16 v[48:51], a[124:127], a[80:83], v[48:51]\n"
  "ds_read_b128 a[124:127], v235 offset:14400\n"
  "v_fma_f32 v2, v2, s73, v223\n"
  "v_mfma_f32_16x16x32_bf16 v[48:51], a[120:123], a[84:87], v[48:51]\n"
  "ds_read_b128 a[120:123], v235 offset:14464\n"
  "v_fma_f32 v3, v3, s73, v223\n"
  "v_mfma_f32_16x16x32_bf16 v[48:51], a[148:151], a[88:91], v[48:51]\n"
  "v_exp_f32 v1, v1\n"
  "v_mfma_f32_16x16x32_bf16 v[48:51], a[144:147], a[92:95], v[48:51]\n"
  "s_waitcnt lgkmcnt(4)\n"
  "v_exp_f32 v2, v2\n"
  "v_mfma_f32_16x16x32_bf16 v[4:7], a[140:143], a[0:3], v[248:251]\n"
  "ds_read_b128 a[0:3], v222 offset:0\n"
  "v_fma_f32 v16, v16, s73, v224\n"
  "v_mfma_f32_16x16x32_bf16 v[4:7], a[136:139], a[4:7], v[4:7]\n"
  "s_waitcnt lgkmcnt(3)\n"
  "ds_read_b128 a[4:7], v222 offset:64\n"
  "v_fma_f32 v17, v17, s73, v224\n"
  "v_mfma_f32_16x16x32_bf16 v[4:7], a[128:131], a[8:11], v[4:7]\n"
  "ds_read_b128 a[8:11], v222 offset:2240\n"
  "v_fma_f32 v18, v18, s73, v224\n"
  "v_mfma_f32_16x16x32_bf16 v[4:7], a[132:135], a[12:15], v[4:7]\n"
  "s_waitcnt lgkmcnt(3)\n"
  "ds_read_b128 a[12:15], v222 offset:2304\n"
  "v_fma_f32 v19, v19, s73, v224\n"
  "v_mfma_f32_16x16x32_bf16 v[4:7], a[124:127], a[16:19], v[4:7]\n"
  "ds_read_b128 a[16:19], v222 offset:4480\n"
  "v_fma_f32 v32, v32, s73, v225\n"
  "v_mfma_f32_16x16x32_bf16 v[4:7], a[120:123], a[20:23], v[4:7]\n"
  "ds_read_b128 a[20:23], v222 offset:4544\n"
  "v_fma_f32 v33, v33, s73, v225\n"
  "v_mfma_f32_16x16x32_bf16 v[20:23], a[120:123], a[44:47], v[248:251]\n"
  "ds_read_b128 a[44:47], v222 offset:6784\n"
  "v_fma_f32 v34, v34, s73, v225\n"
  "v_mfma_f32_16x16x32_bf16 v[20:23], a[124:127], a[40:43], v[20:23]\n"
  "ds_read_b128 a[40:43], v222 offset:6720\n"
  "v_fma_f32 v35, v35, s73, v225\n"
  "v_mfma_f32_16x16x32_bf16 v[20:23], a[132:135], a[36:39], v[20:23]\n"
  "ds_read_b64_tr_b16 a[148:149], v217 offset:12352\n"
  "v_fma_f32 v4, v4, s73, v223\n"
  "v_mfma_f32_16x16x32_bf16 v[20:23], a[128:131], a[32:35], v[20:23]\n"
  "ds_read_b64_tr_b16 a[150:151], v218 offset:12352\n"
  "v_fma_f32 v5, v5, s73, v223\n"
  "v_mfma_f32_16x16x32_bf16 v[20:23], a[136:139], a[28:31], v[20:23]\n"
  "s_add_u32 m0, s72, -256\n"
  "ds_read_b64_tr_b16 a[144:145], v217 offset:13376\n"
  "v_mfma_f32_16x16x32_bf16 v[20:23], a[140:143], a[24:27], v[20:23]\n"
  "s_add_u32 s52, s62, s57\n"
  "ds_read_b64_tr_b16 a[146:147], v218 offset:13376\n"
  "v_fma_f32 v6, v6, s73, v223\n"
  "v_mfma_f32_16x16x32_bf16 v[36:39], a[140:143], a[48:51], v[248:251]\n"
  "s_addc_u32 s53, s63, 0\n"
  "v_exp_f32 v3, v3\n"
  "v_mfma_f32_16x16x32_bf16 v[36:39], a[136:139], a[52:55], v[36:39]\n"
  "s_mov_b32 s54, s93\n"
  "v_exp_f32 v4, v4\n"
  "v_mfma_f32_16x16x32_bf16 v[36:39], a[128:131], a[56:59], v[36:39]\n"
  "s_waitcnt lgkmcnt(10)\n"
  "buffer_load_dwordx4 v221, s[48:51], 0 offen offset:256  lds\n"
  "v_mfma_f32_16x16x32_bf16 v[36:39], a[132:135], a[60:63], v[36:39]\n"
  "s_add_u32 m0, s72, -2240\n"
  "v_exp_f32 v5, v5\n"
  "v_mfma_f32_16x16x32_bf16 v[36:39], a[124:127], a[64:67], v[36:39]\n"
  "s_mov_b32 s57, s92\n"
  "v_exp_f32 v6, v6\n"
  "v_mfma_f32_16x16x32_bf16 v[36:39], a[120:123], a[68:71], v[36:39]\n"
  "s_mov_b32 s93, s94\n"
  "v_exp_f32 v16, v16\n"
  "v_mfma_f32_16x16x32_bf16 v[52:55], a[120:123], a[92:95], v[248:251]\n"
  "buffer_load_dwordx4 v221, s[48:51], 0 offen offset:3328  lds\n"
  "v_fma_f32 v7, v7, s73, v223\n"
  "v_mfma_f32_16x16x32_bf16 v[52:55], a[124:127], a[88:91], v[52:55]\n"
  "s_add_u32 s48, s84, s71\n"
  "ds_read_b64_tr_b16 a[120:121], v217 offset:12368\n"
  "v_fma_f32 v20, v20, s73, v224\n"
  "v_mfma_f32_16x16x32_bf16 v[52:55], a[132:135], a[84:87], v[52:55]\n"
  "s_addc_u32 s49, s85, 0\n"
  "ds_read_b64_tr_b16 a[122:123], v218 offset:12368\n"
  "v_fma_f32 v21, v21, s73, v224\n"
  "v_mfma_f32_16x16x32_bf16 v[52:55], a[128:131], a[80:83], v[52:55]\n"
  "s_add_u32 m0, s72, 2240\n"
  "ds_read_b64_tr_b16 a[124:125], v217 offset:13392\n"
  "v_mfma_f32_16x16x32_bf16 v[52:55], a[136:139], a[76:79], v[52:55]\n"
  "s_sub_i32 s71, s38, s83\n"
  "s_max_i32 s50, s71, 0\n"
  "s_waitcnt lgkmcnt(11)\n"
  "buffer_load_dwordx4 v221, s[48:51], 0 offen offset:0 nt lds\n"
  "v_fma_f32 v22, v22, s73, v224\n"
  "v_mfma_f32_16x16x32_bf16 v[52:55], a[140:143], a[72:75], v[52:55]\n"
  "s_waitcnt lgkmcnt(3)\n"
  "ds_read_b64_tr_b16 a[126:127], v218 offset:13392\n"
  "v_fma_f32 v23, v23, s73, v224\n"
  "v_mfma_f32_16x16x32_bf16 a[236:239], a[160:163], a[148:151], a[236:239]\n"
  "v_exp_f32 v7, v7\n"
  "v_mfma_f32_16x16x32_bf16 a[236:239], a[156:159], a[144:147], a[236:239]\n"
  "s_add_u32 m0, s72, 256\n"
  "ds_write_b64 v231, v[64:65] offset:2048\n"
  "v_mfma_f32_16x16x32_bf16 v[184:187], v[60:63], a[148:151], v[184:187]\n"
  "buffer_load_dwordx4 v221, s[48:51], 0 offen offset:3072 nt lds\n"
  "v_fma_f32 v36, v36, s73, v225\n"
  "v_mfma_f32_16x16x32_bf16 v[184:187], v[56:59], a[144:147], v[184:187]\n"
  "s_add_u32 m0, s72, 4352\n"
  "v_exp_f32 v17, v17\n"
  "v_mfma_f32_16x16x32_bf16 v[152:155], v[44:47], a[148:151], v[152:155]\n"
  "v_exp_f32 v18, v18\n"
  "v_mfma_f32_16x16x32_bf16 v[152:155], v[40:43], a[144:147], v[152:155]\n"
  "ds_read_b64_tr_b16 a[132:133], v219 offset:12352\n"
  "v_fma_f32 v37, v37, s73, v225\n"
  "v_mfma_f32_16x16x32_bf16 v[120:123], v[28:31], a[148:151], v[120:123]\n"
  "buffer_load_dwordx4 v221, s[48:51], 0 offen offset:128 nt lds\n"
  "v_fma_f32 v38, v38, s73, v225\n"
  "v_mfma_f32_16x16x32_bf16 v[120:123], v[24:27], a[144:147], v[120:123]\n"
  "s_add_u32 m0, s72, 2368\n"
  "ds_read_b64_tr_b16 a[134:135], v220 offset:12352\n"
  "v_mfma_f32_16x16x32_bf16 v[88:91], v[12:15], a[148:151], v[88:91]\n"
  "ds_read_b64_tr_b16 a[128:129], v219 offset:13376\n"
  "v_fma_f32 v39, v39, s73, v225\n"
  "v_mfma_f32_16x16x32_bf16 v[88:91], v[8:11], a[144:147], v[88:91]\n"
  "s_waitcnt lgkmcnt(4)\n"
  "ds_read_b64_tr_b16 a[130:131], v220 offset:13376\n"
  "v_fma_f32 v48, v48, s73, v226\n"
  "v_mfma_f32_16x16x32_bf16 v[92:95], v[12:15], a[120:123], v[92:95]\n"
  "buffer_load_dwordx4 v221, s[48:51], 0 offen offset:3200 nt lds\n"
  "v_fma_f32 v49, v49, s73, v226\n"
  "v_mfma_f32_16x16x32_bf16 v[92:95], v[8:11], a[124:127], v[92:95]\n"
  "s_add_u32 m0, s72, 6464\n"
  "v_exp_f32 v19, v19\n"
  "v_mfma_f32_16x16x32_bf16 v[124:127], v[28:31], a[120:123], v[124:127]\n"
  "v_exp_f32 v20, v20\n"
  "v_mfma_f32_16x16x32_bf16 v[124:127], v[24:27], a[124:127], v[124:127]\n"
  "v_exp_f32 v21, v21\n"
  "v_mfma_f32_16x16x32_bf16 v[156:159], v[44:47], a[120:123], v[156:159]\n"
  "buffer_load_dwordx4 v221, s[48:51], 0 offen offset:256 nt lds\n"
  "v_fma_f32 v50, v50, s73, v226\n"
  "v_mfma_f32_16x16x32_bf16 v[156:159], v[40:43], a[124:127], v[156:159]\n"
  "s_add_u32 m0, s72, 4480\n"
  "ds_read_b64_tr_b16 a[136:137], v219 offset:12368\n"
  "v_mfma_f32_16x16x32_bf16 v[188:191], v[60:63], a[120:123], v[188:191]\n"
  "ds_read_b64_tr_b16 a[138:139], v220 offset:12368\n"
  "v_fma_f32 v51, v51, s73, v226\n"
  "v_mfma_f32_16x16x32_bf16 v[188:191], v[56:59], a[124:127], v[188:191]\n"
  "ds_read_b64_tr_b16 a[140:141], v219 offset:13392\n"
  "v_fma_f32 v52, v52, s73, v226\n"
  "v_mfma_f32_16x16x32_bf16 a[240:243], a[160:163], a[120:123], a[240:243]\n"
  "buffer_load_dwordx4 v221, s[48:51], 0 offen offset:3328 nt lds\n"
  "v_fma_f32 v53, v53, s73, v226\n"
  "v_mfma_f32_16x16x32_bf16 a[240:243], a[156:159], a[124:127], a[240:243]\n"
  "ds_read_b64_tr_b16 a[142:143], v220 offset:13392\n"
  "v_fma_f32 v54, v54, s73, v226\n"
  "v_mfma_f32_16x16x32_bf16 v[68:71], v[236:239], a[152:155], v[68:71]\n"
  "s_waitcnt lgkmcnt(4)\n"
  "v_exp_f32 v22, v22\n"
  "v_mfma_f32_16x16x32_bf16 v[192:195], v[60:63], a[132:135], v[192:195]\n"
  "v_exp_f32 v23, v23\n"
  "v_mfma_f32_16x16x32_bf16 v[192:195], v[56:59], a[128:131], v[192:195]\n"
  "v_exp_f32 v32, v32\n"
  "v_mfma_f32_16x16x32_bf16 v[160:163], v[44:47], a[132:135], v[160:163]\n"
  "ds_read_b64_tr_b16 a[120:121], v217 offset:14400\n"
  "v_and_b32 v236, 0xffff0000, v254\n"
  "v_mfma_f32_16x16x32_bf16 v[160:163], v[40:43], a[128:131], v[160:163]\n"
  "ds_read_b64_tr_b16 a[122:123], v218 offset:14400\n"
  "v_sub_f32 v254, v254, v236\n"
  "v_mfma_f32_16x16x32_bf16 v[128:131], v[28:31], a[132:135], v[128:131]\n"
  "ds_read_b64_tr_b16 a[124:125], v217 offset:14416\n"
  "v_or_b32_sdwa v236, v236, v254 dst_sel:DWORD dst_unused:UNUSED_PAD src0_sel:DWORD src1_sel:WORD_1\n"
  "v_mfma_f32_16x16x32_bf16 v[128:131], v[24:27], a[128:131], v[128:131]\n"
  "ds_read_b64_tr_b16 a[126:127], v218 offset:14416\n"
  "v_fma_f32 v55, v55, s73, v226\n"
  "v_mfma_f32_16x16x32_bf16 v[96:99], v[12:15], a[132:135], v[96:99]\n"
  "v_exp_f32 v33, v33\n"
  "v_mfma_f32_16x16x32_bf16 v[96:99], v[8:11], a[128:131], v[96:99]\n"
  "v_exp_f32 v34, v34\n"
  "v_mfma_f32_16x16x32_bf16 v[72:75], v[240:243], a[152:155], v[72:75]\n"
  "s_waitcnt lgkmcnt(4)\n"
  "v_exp_f32 v35, v35\n"
  "v_mfma_f32_16x16x32_bf16 v[100:103], v[12:15], a[136:139], v[100:103]\n"
  "v_exp_f32 v36, v36\n"
  "v_mfma_f32_16x16x32_bf16 v[100:103], v[8:11], a[140:143], v[100:103]\n"
  "v_exp_f32 v37, v37\n"
  "v_mfma_f32_16x16x32_bf16 v[132:135], v[28:31], a[136:139], v[132:135]\n"
  "v_exp_f32 v38, v38\n"
  "v_mfma_f32_16x16x32_bf16 v[132:135], v[24:27], a[140:143], v[132:135]\n"
  "v_exp_f32 v39, v39\n"
  "v_mfma_f32_16x16x32_bf16 v[164:167], v[44:47], a[136:139], v[164:167]\n"
  "ds_read_b64_tr_b16 a[132:133], v219 offset:14400\n"
  "v_and_b32 v240, 0xffff0000, v255\n"
  "v_mfma_f32_16x16x32_bf16 v[164:167], v[40:43], a[140:143], v[164:167]\n"
  "ds_read_b64_tr_b16 a[134:135], v220 offset:14400\n"
  "v_sub_f32 v255, v255, v240\n"
  "v_mfma_f32_16x16x32_bf16 v[196:199], v[60:63], a[136:139], v[196:199]\n"
  "ds_read_b64_tr_b16 a[128:129], v219 offset:14416\n"
  "v_or_b32_sdwa v240, v240, v255 dst_sel:DWORD dst_unused:UNUSED_PAD src0_sel:DWORD src1_sel:WORD_1\n"
  "v_mfma_f32_16x16x32_bf16 v[196:199], v[56:59], a[140:143], v[196:199]\n"
  "ds_read_b64_tr_b16 a[130:131], v220 offset:14416\n"
  "v_cvt_pk_bf16_f32 v12, v0, v1\n"
  "v_mfma_f32_16x16x32_bf16 v[76:79], v[236:239], a[152:155], v[76:79]\n"
  "s_waitcnt lgkmcnt(4)\n"
  "v_exp_f32 v48, v48\n"
  "v_mfma_f32_16x16x32_bf16 a[244:247], a[156:159], a[120:123], a[244:247]\n"
  "v_exp_f32 v49, v49\n"
  "v_mfma_f32_16x16x32_bf16 a[248:251], a[156:159], a[124:127], a[248:251]\n"
  "v_exp_f32_e64 v50, v50\n"
  "v_mfma_f32_16x16x32_bf16 v[200:203], v[56:59], a[120:123], v[200:203]\n"
  "v_add_u32 v233, s97, v222\n"
  "s_mov_b64 exec, s[98:99]\n"
  "v_add_u32 v233, 0x4f00, v233\n"
  "s_mov_b64 exec, -1\n"
  "v_mfma_f32_16x16x32_bf16 v[204:207], v[56:59], a[124:127], v[204:207]\n"
  "ds_read_b128 a[96:99], v233 offset:17920\n"
  "ds_read_b128 a[100:103], v233 offset:17984\n"
  "ds_read_b128 a[104:107], v233 offset:19040\n"
  "ds_read_b128 a[108:111], v233 offset:19104\n"
  "ds_read_b128 a[112:115], v233 offset:20160\n"
  "ds_read_b128 a[116:119], v233 offset:20224\n"
  "v_mfma_f32_16x16x32_bf16 v[168:171], v[40:43], a[120:123], v[168:171]\n"
  "v_exp_f32 v51, v51\n"
  "v_mfma_f32_16x16x32_bf16 v[172:175], v[40:43], a[124:127], v[172:175]\n"
  "s_mov_b32 m0, s67\n"
  "v_exp_f32 v52, v52\n"
  "v_mfma_f32_16x16x32_bf16 v[136:139], v[24:27], a[120:123], v[136:139]\n"
  "s_waitcnt vmcnt(14) lgkmcnt(0)\n"
  "s_barrier\n"
  "v_mfma_f32_16x16x32_bf16 v[140:143], v[24:27], a[124:127], v[140:143]\n"
  "ds_read_b64 a[160:161], v231 offset:2048\n"
  "v_cvt_pk_bf16_f32 v13, v2, v3\n"
  "v_mfma_f32_16x16x32_bf16 v[104:107], v[8:11], a[120:123], v[104:107]\n"
  "ds_read_b64 a[162:163], v232 offset:2048\n"
  "v_cvt_pk_bf16_f32 v14, v4, v5\n"
  "v_mfma_f32_16x16x32_bf16 v[108:111], v[8:11], a[124:127], v[108:111]\n"
  "buffer_load_dwordx4 v221, s[40:43], 0 offen offset:0 lds\n"
  "v_mfma_f32_16x16x32_bf16 v[80:83], v[240:243], a[152:155], v[80:83]\n"
  "ds_read_b64_tr_b16 a[120:121], v217 offset:26752\n"
  "v_cvt_pk_bf16_f32 v15, v6, v7\n"
  "v_mfma_f32_16x16x32_bf16 v[112:115], v[8:11], a[132:135], v[112:115]\n"
  "s_addk_i32 m0, 896\n"
  "ds_read_b64_tr_b16 a[122:123], v218 offset:26752\n"
  "v_cvt_pk_bf16_f32 v28, v16, v17\n"
  "v_mfma_f32_16x16x32_bf16 v[116:119], v[8:11], a[128:131], v[116:119]\n"
  "ds_read_b64_tr_b16 a[124:125], v217 offset:26768\n"
  "v_cvt_pk_bf16_f32 v29, v18, v19\n"
  "v_mfma_f32_16x16x32_bf16 v[144:147], v[24:27], a[132:135], v[144:147]\n"
  "buffer_load_dwordx4 v221, s[40:43], 0 offen offset:128 lds\n"
  "v_cvt_pk_bf16_f32 v30, v20, v21\n"
  "v_mfma_f32_16x16x32_bf16 v[148:151], v[24:27], a[128:131], v[148:151]\n"
  "s_addk_i32 m0, 896\n"
  "ds_read_b64_tr_b16 a[126:127], v218 offset:26768\n"
  "v_cvt_pk_bf16_f32 v31, v22, v23\n"
  "v_mfma_f32_16x16x32_bf16 v[176:179], v[40:43], a[132:135], v[176:179]\n"
  "v_exp_f32 v53, v53\n"
  "v_mfma_f32_16x16x32_bf16 v[180:183], v[40:43], a[128:131], v[180:183]\n"
  "v_exp_f32 v54, v54\n"
  "v_mfma_f32_16x16x32_bf16 v[208:211], v[56:59], a[132:135], v[208:211]\n"
  "buffer_load_dwordx4 v221, s[40:43], 0 offen offset:256 lds\n"
  "v_cvt_pk_bf16_f32 v44, v32, v33\n"
  "v_mfma_f32_16x16x32_bf16 v[212:215], v[56:59], a[128:131], v[212:215]\n"
  "s_cmp_eq_i32 s76, 5\n"
  "s_cbranch_scc1 L_TAIL_%=\n"
  "v_exp_f32 v55, v55\n"
  "v_mfma_f32_16x16x32_bf16 v[68:71], v[12:15], a[152:155], v[68:71]\n"
  "ds_read_b64_tr_b16 a[132:133], v219 offset:26752\n"
  "v_cvt_pk_bf16_f32 v45, v34, v35\n"
  "v_cvt_pk_bf16_f32 v46, v36, v37\n"
  "v_cvt_pk_bf16_f32 v47, v38, v39\n"
  "v_mfma_f32_16x16x32_bf16 v[72:75], v[28:31], a[152:155], v[72:75]\n"
  "s_add_u32 m0, s59, -128\n"
  "ds_read_b64_tr_b16 a[134:135], v220 offset:26752\n"
  "v_cvt_pk_bf16_f32 v60, v48, v49\n"
  "v_cvt_pk_bf16_f32 v61, v50, v51\n"
  "v_cvt_pk_bf16_f32 v62, v52, v53\n"
  "v_cvt_pk_bf16_f32 v63, v54, v55\n"
  "v_mfma_f32_16x16x32_bf16 v[76:79], v[44:47], a[152:155], v[76:79]\n"
  "buffer_load_dwordx4 v221, s[32:35], 0 offen offset:128 lds\n"
  "ds_read_b128 a[24:27], v222 offset:11200\n"
  "v_mfma_f32_16x16x32_bf16 v[80:83], v[60:63], a[152:155], v[80:83]\n"
  "s_addk_i32 m0, 1024\n"
  "ds_read_b64_tr_b16 a[128:129], v219 offset:26768\n"
  "ds_read_b128 a[28:31], v222 offset:11264\n"
  "v_mfma_f32_16x16x32_bf16 v[84:87], a[156:159], a[152:155], v[84:87]\n"
  "s_waitcnt lgkmcnt(9)\n"
  "ds_read_b64_tr_b16 a[130:131], v220 offset:26768\n"
  "ds_read_b128 a[32:35], v222 offset:8960\n"
  "v_mfma_f32_16x16x32_bf16 v[84:87], a[160:163], a[152:155], v[84:87]\n"
  "s_waitcnt lgkmcnt(7)\n"
  "ds_read_b128 a[36:39], v222 offset:9024\n"
  "ds_read_b128 a[48:51], v222 offset:13440\n"
  "v_mfma_f32_16x16x32_bf16 a[244:247], a[160:163], a[120:123], a[244:247]\n"
  "buffer_load_dwordx4 v221, s[44:47], 0 offen offset:128 lds\n"
  "ds_read_b128 a[52:55], v222 offset:13504\n"
  "v_mfma_f32_16x16x32_bf16 a[248:251], a[160:163], a[124:127], a[248:251]\n"
  "s_addk_i32 m0, 896\n"
  "ds_read_b128 a[56:59], v222 offset:15680\n"
  "ds_read_b128 a[60:63], v222 offset:15744\n"
  "v_mfma_f32_16x16x32_bf16 v[200:203], v[60:63], a[120:123], v[200:203]\n"
  "ds_read_b64_tr_b16 a[136:137], v217 offset:24704\n"
  "v_maximum3_f32 v236, v68, v69, v70\n"
  "v_mfma_f32_16x16x32_bf16 v[204:207], v[60:63], a[124:127], v[204:207]\n"
  "ds_read_b64_tr_b16 a[138:139], v218 offset:24704\n"
  "v_maximum3_f32 v236, v236, v71, v72\n"
  "v_mfma_f32_16x16x32_bf16 v[168:171], v[44:47], a[120:123], v[168:171]\n"
  "buffer_load_dwordx4 v221, s[44:47], 0 offen offset:256 lds\n"
  "v_maximum3_f32 v236, v236, v73, v74\n"
  "v_mfma_f32_16x16x32_bf16 v[172:175], v[44:47], a[124:127], v[172:175]\n"
  "ds_read_b64_tr_b16 a[140:141], v217 offset:25728\n"
  "v_maximum3_f32 v236, v236, v75, v76\n"
  "v_mfma_f32_16x16x32_bf16 v[136:139], v[28:31], a[120:123], v[136:139]\n"
  "ds_read_b64_tr_b16 a[142:143], v218 offset:25728\n"
  "v_maximum3_f32 v236, v236, v77, v78\n"
  "v_mfma_f32_16x16x32_bf16 v[140:143], v[28:31], a[124:127], v[140:143]\n"
  "v_maximum3_f32 v236, v236, v79, v80\n"
  "v_maximum3_f32 v236, v236, v81, v82\n"
  "v_mfma_f32_16x16x32_bf16 v[104:107], v[12:15], a[120:123], v[104:107]\n"
  "v_maximum3_f32 v236, v236, v83, v84\n"
  "v_maximum3_f32 v236, v236, v85, v86\n"
  "v_mfma_f32_16x16x32_bf16 v[108:111], v[12:15], a[124:127], v[108:111]\n"
  "s_waitcnt lgkmcnt(10)\n"
  "v_rcp_f32 v84, v84\n"
  "v_mfma_f32_16x16x32_bf16 v[112:115], v[12:15], a[132:135], v[112:115]\n"
  "s_add_u32 s40, s40, 24576\n"
  "v_rcp_f32 v85, v85\n"
  "v_mfma_f32_16x16x32_bf16 v[116:119], v[12:15], a[128:131], v[116:119]\n"
  "s_addc_u32 s41, s41, 0\n"
  "v_accvgpr_read_b32 v240, a244\n"
  "v_accvgpr_read_b32 v241, a245\n"
  "v_mfma_f32_16x16x32_bf16 v[144:147], v[28:31], a[132:135], v[144:147]\n"
  "ds_read_b64_tr_b16 a[120:121], v217 offset:24720\n"
  "v_accvgpr_read_b32 v242, a248\n"
  "v_mfma_f32_16x16x32_bf16 v[148:151], v[28:31], a[128:131], v[148:151]\n"
  "ds_read_b64_tr_b16 a[122:123], v218 offset:24720\n"
  "v_accvgpr_read_b32 v243, a249\n"
  "v_mfma_f32_16x16x32_bf16 v[176:179], v[44:47], a[132:135], v[176:179]\n"
  "ds_read_b64_tr_b16 a[124:125], v217 offset:25744\n"
  "v_maximum3_f32 v236, v236, v87, v87\n"
  "v_mfma_f32_16x16x32_bf16 v[180:183], v[44:47], a[128:131], v[180:183]\n"
  "ds_read_b64_tr_b16 a[126:127], v218 offset:25744\n"
  "v_cmp_ngt_f32 vcc, s75, v236\n"
  "v_mfma_f32_16x16x32_bf16 v[208:211], v[60:63], a[132:135], v[208:211]\n"
  "s_mov_b32 s75, s65\n"
  "v_rcp_f32 v68, v68\n"
  "v_mfma_f32_16x16x32_bf16 v[212:215], v[60:63], a[128:131], v[212:215]\n"
  "s_waitcnt lgkmcnt(4)\n"
  "v_rcp_f32 v69, v69\n"
  "v_mfma_f32_16x16x32_bf16 a[228:231], a[156:159], a[136:139], a[228:231]\n"
  "s_add_u32 s32, s44, s31\n"
  "v_rcp_f32 v70, v70\n"
  "v_mfma_f32_16x16x32_bf16 a[228:231], a[160:163], a[140:143], a[228:231]\n"
  "s_addc_u32 s33, s45, 0\n"
  "v_rcp_f32 v71, v71\n"
  "v_mfma_f32_16x16x32_bf16 a[212:215], v[56:59], a[136:139], a[212:215]\n"
  "s_add_u32 s44, s44, 24576\n"
  "v_rcp_f32 v72, v72\n"
  "v_mfma_f32_16x16x32_bf16 a[212:215], v[60:63], a[140:143], a[212:215]\n"
  "s_addc_u32 s45, s45, 0\n"
  "v_rcp_f32 v73, v73\n"
  "v_mfma_f32_16x16x32_bf16 a[196:199], v[40:43], a[136:139], a[196:199]\n"
  "ds_read_b128 a[132:135], v216 offset:24704\n"
  "v_accvgpr_read_b32 v236, a246\n"
  "v_mfma_f32_16x16x32_bf16 a[196:199], v[44:47], a[140:143], a[196:199]\n"
  "ds_read_b128 a[128:131], v216 offset:24768\n"
  "v_accvgpr_read_b32 v237, a247\n"
  "v_mfma_f32_16x16x32_bf16 a[180:183], v[24:27], a[136:139], a[180:183]\n"
  "ds_read_b128 a[148:151], v216 offset:25728\n"
  "v_accvgpr_read_b32 v238, a250\n"
  "v_mfma_f32_16x16x32_bf16 a[180:183], v[28:31], a[140:143], a[180:183]\n"
  "ds_read_b128 a[144:147], v216 offset:25792\n"
  "v_add_u32 v233, s61, v229\n"
  "s_mov_b32 exec_lo, 0\n"
  "v_add_u32 v233, s69, v233\n"
  "s_mov_b32 exec_lo, -1\n"
  "v_rcp_f32_e64 v74, v74\n"
  "v_pk_fma_f32 v[240:241], v[240:241], v[84:85], v[84:85] neg_lo:[0,0,1] neg_hi:[0,0,1]\n"
  "v_pk_fma_f32 v[242:243], v[242:243], v[84:85], v[84:85] neg_lo:[0,0,1] neg_hi:[0,0,1]\n"
  "v_pk_fma_f32 v[104:105], v[104:105], v[68:69], v[68:69] neg_lo:[0,0,1] neg_hi:[0,0,1]\n"
  "v_pk_fma_f32 v[108:109], v[108:109], v[68:69], v[68:69] neg_lo:[0,0,1] neg_hi:[0,0,1]\n"
  "v_mfma_f32_16x16x32_bf16 a[164:167], v[8:11], a[136:139], a[164:167]\n"
  "s_nop 0\n"
  "ds_read_b128 a[136:139], v216 offset:26752\n"
  "v_cvt_pk_bf16_f32 v240, v240, v242\n"
  "v_mfma_f32_16x16x32_bf16 a[164:167], v[12:15], a[140:143], a[164:167]\n"
  "s_waitcnt lgkmcnt(5)\n"
  "ds_read_b128 a[140:143], v216 offset:26816\n"
  "v_cvt_pk_bf16_f32 v241, v241, v243\n"
  "v_cvt_pk_bf16_f32 v104, v104, v108\n"
  "v_rcp_f32_e64 v75, v75\n"
  "v_pk_fma_f32 v[112:113], v[112:113], v[68:69], v[68:69] neg_lo:[0,0,1] neg_hi:[0,0,1]\n"
  "v_pk_fma_f32 v[116:117], v[116:117], v[68:69], v[68:69] neg_lo:[0,0,1] neg_hi:[0,0,1]\n"
  "v_pk_fma_f32 v[106:107], v[106:107], v[70:71], v[70:71] neg_lo:[0,0,1] neg_hi:[0,0,1]\n"
  "v_pk_fma_f32 v[110:111], v[110:111], v[70:71], v[70:71] neg_lo:[0,0,1] neg_hi:[0,0,1]\n"
  "v_mfma_f32_16x16x32_bf16 a[168:171], v[8:11], a[120:123], a[168:171]\n"
  "s_cmp_lg_u64 vcc, 0\n"
  "s_cselect_b32 s79, s77, 0\n"
  "s_or_b32 s78, s78, s79\n"
  "s_lshl_b32 s77, s77, 1\n"
  "v_rcp_f32 v76, v76\n"
  "v_mfma_f32_16x16x32_bf16 a[168:171], v[12:15], a[124:127], a[168:171]\n"
  "v_cvt_pk_bf16_f32 v108, v105, v109\n"
  "v_rcp_f32 v77, v77\n"
  "v_rcp_f32_e64 v78, v78\n"
  "v_pk_fma_f32 v[114:115], v[114:115], v[70:71], v[70:71] neg_lo:[0,0,1] neg_hi:[0,0,1]\n"
  "v_pk_fma_f32 v[118:119], v[118:119], v[70:71], v[70:71] neg_lo:[0,0,1] neg_hi:[0,0,1]\n"
  "v_pk_fma_f32 v[136:137], v[136:137], v[72:73], v[72:73] neg_lo:[0,0,1] neg_hi:[0,0,1]\n"
  "v_pk_fma_f32 v[140:141], v[140:141], v[72:73], v[72:73] neg_lo:[0,0,1] neg_hi:[0,0,1]\n"
  "v_mfma_f32_16x16x32_bf16 a[184:187], v[24:27], a[120:123], a[184:187]\n"
  "v_rcp_f32_e64 v79, v79\n"
  "v_mfma_f32_16x16x32_bf16 a[184:187], v[28:31], a[124:127], a[184:187]\n"
  "v_cvt_pk_bf16_f32 v105, v112, v116\n"
  "v_rcp_f32_e64 v80, v80\n"
  "v_rcp_f32_e64 v81, v81\n"
  "v_pk_fma_f32 v[144:145], v[144:145], v[72:73], v[72:73] neg_lo:[0,0,1] neg_hi:[0,0,1]\n"
  "v_pk_fma_f32 v[148:149], v[148:149], v[72:73], v[72:73] neg_lo:[0,0,1] neg_hi:[0,0,1]\n"
  "v_pk_fma_f32 v[138:139], v[138:139], v[74:75], v[74:75] neg_lo:[0,0,1] neg_hi:[0,0,1]\n"
  "v_pk_fma_f32 v[142:143], v[142:143], v[74:75], v[74:75] neg_lo:[0,0,1] neg_hi:[0,0,1]\n"
  "v_mfma_f32_16x16x32_bf16 a[200:203], v[40:43], a[120:123], a[200:203]\n"
  "v_rcp_f32_e64 v82, v82\n"
  "v_mfma_f32_16x16x32_bf16 a[200:203], v[44:47], a[124:127], a[200:203]\n"
  "ds_write_b32 v233, v240 offset:20160\n"
  "v_cvt_pk_bf16_f32 v109, v113, v117\n"
  "v_rcp_f32_e64 v83, v83\n"
  "v_pk_fma_f32 v[146:147], v[146:147], v[74:75], v[74:75] neg_lo:[0,0,1] neg_hi:[0,0,1]\n"
  "v_pk_fma_f32 v[150:151], v[150:151], v[74:75], v[74:75] neg_lo:[0,0,1] neg_hi:[0,0,1]\n"
  "v_pk_fma_f32 v[168:169], v[168:169], v[76:77], v[76:77] neg_lo:[0,0,1] neg_hi:[0,0,1]\n"
  "v_pk_fma_f32 v[172:173], v[172:173], v[76:77], v[76:77] neg_lo:[0,0,1] neg_hi:[0,0,1]\n"
  "v_mfma_f32_16x16x32_bf16 a[216:219], v[56:59], a[120:123], a[216:219]\n"
  "ds_write_b32 v233, v241 offset:20448\n"
  "v_mfma_f32_16x16x32_bf16 a[216:219], v[60:63], a[124:127], a[216:219]\n"
  "ds_write_b64 v229, v[104:105] offset:8960\n"
  "v_cvt_pk_bf16_f32 v106, v106, v110\n"
  "v_cvt_pk_bf16_f32 v110, v107, v111\n"
  "v_pk_fma_f32 v[176:177], v[176:177], v[76:77], v[76:77] neg_lo:[0,0,1] neg_hi:[0,0,1]\n"
  "v_pk_fma_f32 v[180:181], v[180:181], v[76:77], v[76:77] neg_lo:[0,0,1] neg_hi:[0,0,1]\n"
  "v_pk_fma_f32 v[170:171], v[170:171], v[78:79], v[78:79] neg_lo:[0,0,1] neg_hi:[0,0,1]\n"
  "v_pk_fma_f32 v[174:175], v[174:175], v[78:79], v[78:79] neg_lo:[0,0,1] neg_hi:[0,0,1]\n"
  "v_mfma_f32_16x16x32_bf16 a[232:235], a[156:159], a[120:123], a[232:235]\n"
  "s_nop 0\n"
  "v_cvt_pk_bf16_f32 v107, v114, v118\n"
  "v_cvt_pk_bf16_f32 v111, v115, v119\n"
  "v_mfma_f32_16x16x32_bf16 a[232:235], a[160:163], a[124:127], a[232:235]\n"
  "s_waitcnt lgkmcnt(7)\n"
  "ds_write_b64 v229, v[108:109] offset:9248\n"
  "v_cvt_pk_bf16_f32 v136, v136, v140\n"
  "v_cvt_pk_bf16_f32 v140, v137, v141\n"
  "v_pk_fma_f32 v[178:179], v[178:179], v[78:79], v[78:79] neg_lo:[0,0,1] neg_hi:[0,0,1]\n"
  "v_pk_fma_f32 v[182:183], v[182:183], v[78:79], v[78:79] neg_lo:[0,0,1] neg_hi:[0,0,1]\n"
  "v_pk_fma_f32 v[200:201], v[200:201], v[80:81], v[80:81] neg_lo:[0,0,1] neg_hi:[0,0,1]\n"
  "v_pk_fma_f32 v[204:205], v[204:205], v[80:81], v[80:81] neg_lo:[0,0,1] neg_hi:[0,0,1]\n"
  "v_mfma_f32_16x16x32_bf16 v[64:67], a[132:135], a[96:99], v[248:251]\n"
  "v_cvt_pk_bf16_f32 v137, v144, v148\n"
  "v_cvt_pk_bf16_f32 v141, v145, v149\n"
  "v_mfma_f32_16x16x32_bf16 v[64:67], a[128:131], a[100:103], v[64:67]\n"
  "s_waitcnt lgkmcnt(6)\n"
  "v_cvt_pk_bf16_f32 v138, v138, v142\n"
  "v_cvt_pk_bf16_f32 v142, v139, v143\n"
  "v_mfma_f32_16x16x32_bf16 v[64:67], a[148:151], a[104:107], v[64:67]\n"
  "s_waitcnt vmcnt(12)\n"
  "ds_read_b128 a[64:67], v222 offset:0\n"
  "ds_read_b128 a[68:71], v222 offset:64\n"
  "v_mfma_f32_16x16x32_bf16 v[64:67], a[144:147], a[108:111], v[64:67]\n"
  "s_waitcnt lgkmcnt(6)\n"
  "v_cvt_pk_bf16_f32 v139, v146, v150\n"
  "v_cvt_pk_bf16_f32 v143, v147, v151\n"
  "v_mfma_f32_16x16x32_bf16 v[64:67], a[136:139], a[112:115], v[64:67]\n"
  "s_nop 0\n"
  "v_cvt_pk_bf16_f32 v168, v168, v172\n"
  "v_cvt_pk_bf16_f32 v172, v169, v173\n"
  "v_mfma_f32_16x16x32_bf16 v[64:67], a[140:143], a[116:119], v[64:67]\n"
  "ds_write_b64 v229, v[106:107] offset:9536\n"
  "v_cvt_pk_bf16_f32 v169, v176, v180\n"
  "v_cvt_pk_bf16_f32 v173, v177, v181\n"
  "v_pk_fma_f32 v[208:209], v[208:209], v[80:81], v[80:81] neg_lo:[0,0,1] neg_hi:[0,0,1]\n"
  "v_pk_fma_f32 v[212:213], v[212:213], v[80:81], v[80:81] neg_lo:[0,0,1] neg_hi:[0,0,1]\n"
  "v_pk_fma_f32 v[202:203], v[202:203], v[82:83], v[82:83] neg_lo:[0,0,1] neg_hi:[0,0,1]\n"
  "v_pk_fma_f32 v[206:207], v[206:207], v[82:83], v[82:83] neg_lo:[0,0,1] neg_hi:[0,0,1]\n"
  "v_mfma_f32_16x16x32_bf16 v[0:3], a[140:143], a[20:23], v[248:251]\n"
  "v_cvt_pk_bf16_f32 v170, v170, v174\n"
  "v_cvt_pk_bf16_f32 v174, v171, v175\n"
  "v_mfma_f32_16x16x32_bf16 v[0:3], a[136:139], a[16:19], v[0:3]\n"
  "v_cvt_pk_bf16_f32 v171, v178, v182\n"
  "v_cvt_pk_bf16_f32 v175, v179, v183\n"
  "v_mfma_f32_16x16x32_bf16 v[0:3], a[144:147], a[12:15], v[0:3]\n"
  "s_waitcnt vmcnt(10)\n"
  "ds_read_b128 a[72:75], v222 offset:2240\n"
  "ds_read_b128 a[76:79], v222 offset:2304\n"
  "v_mfma_f32_16x16x32_bf16 v[0:3], a[148:151], a[8:11], v[0:3]\n"
  "s_waitcnt vmcnt(8)\n"
  "ds_read_b128 a[80:83], v222 offset:4480\n"
  "ds_read_b128 a[84:87], v222 offset:4544\n"
  "v_mfma_f32_16x16x32_bf16 v[0:3], a[128:131], a[4:7], v[0:3]\n"
  "s_waitcnt vmcnt(6)\n"
  "ds_read_b128 a[88:91], v222 offset:6720\n"
  "ds_read_b128 a[92:95], v222 offset:6784\n"
  "v_mfma_f32_16x16x32_bf16 v[0:3], a[132:135], a[0:3], v[0:3]\n"
  "s_nop 0\n"
  "ds_write_b64 v229, v[110:111] offset:9824\n"
  "v_max3_f32 v227, v64, v65, v66\n"
  "v_cvt_pk_bf16_f32 v200, v200, v204\n"
  "v_pk_fma_f32 v[210:211], v[210:211], v[82:83], v[82:83] neg_lo:[0,0,1] neg_hi:[0,0,1]\n"
  "v_pk_fma_f32 v[214:215], v[214:215], v[82:83], v[82:83] neg_lo:[0,0,1] neg_hi:[0,0,1]\n"
  "v_mfma_f32_16x16x32_bf16 v[16:19], a[132:135], a[24:27], v[248:251]\n"
  "v_rcp_f32 v86, v86\n"
  "v_mfma_f32_16x16x32_bf16 v[16:19], a[128:131], a[28:31], v[16:19]\n"
  "ds_read_b64_tr_b16 a[252:253], v230 offset:8960\n"
  "ds_read_b64_tr_b16 a[254:255], v230 offset:8992\n"
  "v_mfma_f32_16x16x32_bf16 v[16:19], a[148:151], a[32:35], v[16:19]\n"
  "ds_read_b128 a[120:123], v235 offset:24704\n"
  "v_max3_f32 v223, v0, v1, v2\n"
  "v_mfma_f32_16x16x32_bf16 v[16:19], a[144:147], a[36:39], v[16:19]\n"
  "ds_read_b128 a[124:127], v235 offset:24768\n"
  "v_max_f32 v223, v223, v3\n"
  "v_mfma_f32_16x16x32_bf16 v[16:19], a[136:139], a[40:43], v[16:19]\n"
  "v_mul_f32 v223, s74, v223\n"
  "v_add_f32 v223, s64, v223\n"
  "v_mfma_f32_16x16x32_bf16 v[16:19], a[140:143], a[44:47], v[16:19]\n"
  "s_waitcnt lgkmcnt(12)\n"
  "ds_bpermute_b32 v223, v234, v223\n"
  "ds_write_b64 v229, v[136:137] offset:11200\n"
  "v_mfma_f32_16x16x32_bf16 v[32:35], a[140:143], a[68:71], v[248:251]\n"
  "ds_read_b64_tr_b16 v[244:245], v230 offset:10080\n"
  "ds_read_b64_tr_b16 v[246:247], v230 offset:10112\n"
  "v_mfma_f32_16x16x32_bf16 v[32:35], a[136:139], a[64:67], v[32:35]\n"
  "v_rcp_f32 v87, v87\n"
  "v_mfma_f32_16x16x32_bf16 v[32:35], a[144:147], a[60:63], v[32:35]\n"
  "v_max3_f32 v224, v16, v17, v18\n"
  "v_max_f32 v224, v224, v19\n"
  "v_mfma_f32_16x16x32_bf16 v[32:35], a[148:151], a[56:59], v[32:35]\n"
  "v_mul_f32 v224, s74, v224\n"
  "v_add_f32_e64 v224, s64, v224\n"
  "v_mfma_f32_16x16x32_bf16 v[32:35], a[128:131], a[52:55], v[32:35]\n"
  "ds_bpermute_b32 v224, v234, v224\n"
  "v_mfma_f32_16x16x32_bf16 v[32:35], a[132:135], a[48:51], v[32:35]\n"
  "s_waitcnt lgkmcnt(10)\n"
  "s_waitcnt lgkmcnt(7)\n"
  "buffer_store_dwordx4 a[252:255], v228, s[52:55], s80 offen offset:256 nt\n"
  "ds_write_b64 v229, v[140:141] offset:11488\n"
  "v_cvt_pk_bf16_f32 v204, v201, v205\n"
  "v_cvt_pk_bf16_f32 v201, v208, v212\n"
  "v_cvt_pk_bf16_f32 v205, v209, v213\n"
  "v_pk_fma_f32 v[236:237], v[236:237], v[86:87], v[86:87] neg_lo:[0,0,1] neg_hi:[0,0,1]\n"
  "v_mfma_f32_16x16x32_bf16 v[48:51], a[132:135], a[72:75], v[248:251]\n"
  "s_waitcnt lgkmcnt(5)\n"
  "ds_read_b128 a[132:135], v235 offset:25728\n"
  "v_fma_f32 v0, v0, s73, v223\n"
  "v_mfma_f32_16x16x32_bf16 v[48:51], a[128:131], a[76:79], v[48:51]\n"
  "ds_read_b128 a[128:131], v235 offset:25792\n"
  "v_fma_f32 v1, v1, s73, v223\n"
  "v_mfma_f32_16x16x32_bf16 v[48:51], a[148:151], a[80:83], v[48:51]\n"
  "ds_read_b128 a[148:151], v235 offset:26752\n"
  "v_fma_f32 v2, v2, s73, v223\n"
  "v_mfma_f32_16x16x32_bf16 v[48:51], a[144:147], a[84:87], v[48:51]\n"
  "ds_read_b128 a[144:147], v235 offset:26816\n"
  "v_fma_f32 v3, v3, s73, v223\n"
  "v_mfma_f32_16x16x32_bf16 v[48:51], a[136:139], a[88:91], v[48:51]\n"
  "s_waitcnt lgkmcnt(5)\n"
  "v_exp_f32 v0, v0\n"
  "v_mfma_f32_16x16x32_bf16 v[48:51], a[140:143], a[92:95], v[48:51]\n"
  "buffer_store_dwordx4 v[244:247], v228, s[52:55], s80 offen offset:3328 nt\n"
  "ds_write_b64 v229, v[138:139] offset:11776\n"
  "v_exp_f32 v1, v1\n"
  "v_max3_f32 v225, v32, v33, v34\n"
  "v_mfma_f32_16x16x32_bf16 v[240:243], a[120:123], a[96:99], v[248:251]\n"
  "v_exp_f32 v2, v2\n"
  "v_mfma_f32_16x16x32_bf16 v[240:243], a[124:127], a[100:103], v[240:243]\n"
  "s_waitcnt lgkmcnt(3)\n"
  "v_exp_f32 v3, v3\n"
  "v_mfma_f32_16x16x32_bf16 v[240:243], a[132:135], a[104:107], v[240:243]\n"
  "v_max_f32 v225, v225, v35\n"
  "v_mul_f32 v225, s74, v225\n"
  "v_mfma_f32_16x16x32_bf16 v[240:243], a[128:131], a[108:111], v[240:243]\n"
  "s_waitcnt lgkmcnt(1)\n"
  "v_add_f32 v225, s64, v225\n"
  "v_fma_f32 v16, v16, s73, v224\n"
  "v_mfma_f32_16x16x32_bf16 v[240:243], a[148:151], a[112:115], v[240:243]\n"
  "v_exp_f32 v16, v16\n"
  "v_mfma_f32_16x16x32_bf16 v[240:243], a[144:147], a[116:119], v[240:243]\n"
  "ds_bpermute_b32 v225, v234, v225\n"
  "ds_write_b64 v229, v[142:143] offset:12064\n"
  "v_mfma_f32_16x16x32_bf16 v[4:7], a[144:147], a[20:23], v[248:251]\n"
  "v_fma_f32 v17, v17, s73, v224\n"
  "v_fma_f32 v18, v18, s73, v224\n"
  "v_mfma_f32_16x16x32_bf16 v[4:7], a[148:151], a[16:19], v[4:7]\n"
  "v_exp_f32 v17, v17\n"
  "v_mfma_f32_16x16x32_bf16 v[4:7], a[128:131], a[12:15], v[4:7]\n"
  "v_max3_f32 v227, v227, v67, v240\n"
  "v_max3_f32 v227, v227, v241, v242\n"
  "v_mfma_f32_16x16x32_bf16 v[4:7], a[132:135], a[8:11], v[4:7]\n"
  "v_max_f32 v227, v227, v243\n"
  "v_mul_f32 v227, s74, v227\n"
  "v_mfma_f32_16x16x32_bf16 v[4:7], a[124:127], a[4:7], v[4:7]\n"
  "v_add_f32_e64 v227, s64, v227\n"
  "v_fma_f32 v19, v19, s73, v224\n"
  "v_mfma_f32_16x16x32_bf16 v[4:7], a[120:123], a[0:3], v[4:7]\n"
  "s_waitcnt lgkmcnt(1)\n"
  "ds_bpermute_b32 v227, v234, v227\n"
  "ds_write_b64 v229, v[168:169] offset:13440\n"
  "v_mfma_f32_16x16x32_bf16 v[20:23], a[120:123], a[24:27], v[248:251]\n"
  "v_exp_f32 v18, v18\n"
  "v_mfma_f32_16x16x32_bf16 v[20:23], a[124:127], a[28:31], v[20:23]\n"
  "v_exp_f32 v19, v19\n"
  "v_mfma_f32_16x16x32_bf16 v[20:23], a[132:135], a[32:35], v[20:23]\n"
  "ds_read_b64_tr_b16 a[136:137], v219 offset:24704\n"
  "v_max3_f32 v226, v48, v49, v50\n"
  "v_mfma_f32_16x16x32_bf16 v[20:23], a[128:131], a[36:39], v[20:23]\n"
  "ds_read_b64_tr_b16 a[138:139], v220 offset:24704\n"
  "v_max_f32 v226, v226, v51\n"
  "v_mfma_f32_16x16x32_bf16 v[20:23], a[148:151], a[40:43], v[20:23]\n"
  "ds_read_b64_tr_b16 a[140:141], v219 offset:25728\n"
  "v_mul_f32_e64 v226, s74, v226\n"
  "v_mfma_f32_16x16x32_bf16 v[20:23], a[144:147], a[44:47], v[20:23]\n"
  "s_waitcnt lgkmcnt(4)\n"
  "ds_read_b64_tr_b16 a[142:143], v220 offset:25728\n"
  "ds_write_b64 v229, v[172:173] offset:13728\n"
  "v_fma_f32 v64, v64, s73, v227\n"
  "v_mfma_f32_16x16x32_bf16 v[36:39], a[144:147], a[68:71], v[248:251]\n"
  "v_exp_f32 v64, v64\n"
  "v_mfma_f32_16x16x32_bf16 v[36:39], a[148:151], a[64:67], v[36:39]\n"
  "v_fma_f32 v65, v65, s73, v227\n"
  "v_fma_f32 v66, v66, s73, v227\n"
  "v_mfma_f32_16x16x32_bf16 v[36:39], a[128:131], a[60:63], v[36:39]\n"
  "v_exp_f32 v65, v65\n"
  "v_mfma_f32_16x16x32_bf16 v[36:39], a[132:135], a[56:59], v[36:39]\n"
  "v_exp_f32_e64 v66, v66\n"
  "v_mfma_f32_16x16x32_bf16 v[36:39], a[124:127], a[52:55], v[36:39]\n"
  "v_fma_f32 v67, v67, s73, v227\n"
  "v_cvt_pk_bf16_f32 v64, v64, v65\n"
  "v_mfma_f32_16x16x32_bf16 v[36:39], a[120:123], a[48:51], v[36:39]\n"
  "ds_write_b64 v229, v[170:171] offset:14016\n"
  "v_exp_f32_e64 v67, v67\n"
  "v_mfma_f32_16x16x32_bf16 v[52:55], a[120:123], a[72:75], v[248:251]\n"
  "ds_read_b64_tr_b16 a[120:121], v219 offset:24720\n"
  "v_cvt_pk_bf16_f32 v65, v66, v67\n"
  "v_mfma_f32_16x16x32_bf16 v[52:55], a[124:127], a[76:79], v[52:55]\n"
  "ds_read_b64_tr_b16 a[122:123], v220 offset:24720\n"
  "v_add_f32 v226, s64, v226\n"
  "v_mfma_f32_16x16x32_bf16 v[52:55], a[132:135], a[80:83], v[52:55]\n"
  "ds_read_b64_tr_b16 a[124:125], v219 offset:25744\n"
  "v_fma_f32 v32, v32, s73, v225\n"
  "v_mfma_f32_16x16x32_bf16 v[52:55], a[128:131], a[84:87], v[52:55]\n"
  "ds_read_b64_tr_b16 a[126:127], v220 offset:25744\n"
  "v_fma_f32 v33, v33, s73, v225\n"
  "v_mfma_f32_16x16x32_bf16 v[52:55], a[148:151], a[88:91], v[52:55]\n"
  "v_exp_f32_e64 v32, v32\n"
  "v_mfma_f32_16x16x32_bf16 v[52:55], a[144:147], a[92:95], v[52:55]\n"
  "s_waitcnt lgkmcnt(6)\n"
  "ds_read_b64_tr_b16 a[252:253], v230 offset:11200\n"
  "ds_read_b64_tr_b16 a[254:255], v230 offset:11232\n"
  "ds_write_b64 v231, v[64:65] offset:0\n"
  "v_mfma_f32_16x16x32_bf16 a[220:223], v[56:59], a[136:139], a[220:223]\n"
  "v_exp_f32_e64 v33, v33\n"
  "v_mfma_f32_16x16x32_bf16 a[220:223], v[60:63], a[140:143], a[220:223]\n"
  "ds_bpermute_b32 v226, v234, v226\n"
  "ds_write_b64 v229, v[174:175] offset:14304\n"
  "v_mfma_f32_16x16x32_bf16 a[204:207], v[40:43], a[136:139], a[204:207]\n"
  "v_fma_f32 v34, v34, s73, v225\n"
  "v_fma_f32 v35, v35, s73, v225\n"
  "v_mfma_f32_16x16x32_bf16 a[204:207], v[44:47], a[140:143], a[204:207]\n"
  "s_waitcnt vmcnt(2) lgkmcnt(2)\n"
  "s_barrier\n"
  "ds_read_b64_tr_b16 a[132:133], v217 offset:12352\n"
  "ds_read_b64_tr_b16 v[244:245], v230 offset:12320\n"
  "ds_read_b64_tr_b16 v[246:247], v230 offset:12352\n"
  "v_mfma_f32_16x16x32_bf16 a[188:191], v[24:27], a[136:139], a[188:191]\n"
  "ds_read_b64 a[156:157], v231 offset:0\n"
  "v_fma_f32 v4, v4, s73, v223\n"
  "v_mfma_f32_16x16x32_bf16 a[188:191], v[28:31], a[140:143], a[188:191]\n"
  "buffer_store_dwordx4 a[252:255], v228, s[52:55], s81 offen offset:256 nt\n"
  "ds_read_b64 a[158:159], v232 offset:0\n"
  "ds_write_b64 v229, v[200:201] offset:15680\n"
  "v_exp_f32_e64 v34, v34\n"
  "v_mfma_f32_16x16x32_bf16 a[172:175], v[8:11], a[136:139], a[172:175]\n"
  "ds_read_b64_tr_b16 a[134:135], v218 offset:12352\n"
  "v_fma_f32 v5, v5, s73, v223\n"
  "v_mfma_f32_16x16x32_bf16 a[172:175], v[12:15], a[140:143], a[172:175]\n"
  "ds_read_b64_tr_b16 a[128:129], v217 offset:12368\n"
  "ds_write_b64 v229, v[204:205] offset:15968\n"
  "v_fma_f32 v6, v6, s73, v223\n"
  "v_mfma_f32_16x16x32_bf16 a[176:179], v[8:11], a[120:123], a[176:179]\n"
  "s_waitcnt lgkmcnt(10)\n"
  "ds_read_b64_tr_b16 a[130:131], v218 offset:12368\n"
  "v_cvt_pk_bf16_f32 v8, v0, v1\n"
  "v_mfma_f32_16x16x32_bf16 a[176:179], v[12:15], a[124:127], a[176:179]\n"
  "s_nop 0\n"
  "ds_read_b64_tr_b16 a[252:253], v230 offset:13440\n"
  "ds_read_b64_tr_b16 a[254:255], v230 offset:13472\n"
  "v_cvt_pk_bf16_f32 v9, v2, v3\n"
  "v_fma_f32 v48, v48, s73, v226\n"
  "v_cvt_pk_bf16_f32 v202, v202, v206\n"
  "v_mfma_f32_16x16x32_bf16 a[192:195], v[24:27], a[120:123], a[192:195]\n"
  "ds_read_b128 a[136:139], v216 offset:12352\n"
  "v_cvt_pk_bf16_f32 v24, v16, v17\n"
  "v_mfma_f32_16x16x32_bf16 a[192:195], v[28:31], a[124:127], a[192:195]\n"
  "s_waitcnt lgkmcnt(10)\n"
  "buffer_store_dwordx4 v[244:247], v228, s[52:55], s81 offen offset:3328 nt\n"
  "ds_read_b128 a[140:143], v216 offset:12416\n"
  "v_cvt_pk_bf16_f32 v25, v18, v19\n"
  "v_exp_f32 v35, v35\n"
  "v_cvt_pk_bf16_f32 v206, v203, v207\n"
  "v_cvt_pk_bf16_f32 v203, v210, v214\n"
  "v_mfma_f32_16x16x32_bf16 a[208:211], v[40:43], a[120:123], a[208:211]\n"
  "ds_read_b128 a[148:151], v216 offset:13376\n"
  "v_cvt_pk_bf16_f32 v40, v32, v33\n"
  "v_mfma_f32_16x16x32_bf16 a[208:211], v[44:47], a[124:127], a[208:211]\n"
  "ds_read_b128 a[144:147], v216 offset:13440\n"
  "ds_write_b64 v229, v[202:203] offset:16256\n"
  "v_cvt_pk_bf16_f32 v41, v34, v35\n"
  "v_mfma_f32_16x16x32_bf16 a[224:227], v[56:59], a[120:123], a[224:227]\n"
  "s_nop 0\n"
  "ds_read_b128 a[120:123], v216 offset:14400\n"
  "v_fma_f32 v49, v49, s73, v226\n"
  "v_mfma_f32_16x16x32_bf16 a[224:227], v[60:63], a[124:127], a[224:227]\n"
  "L_JOIN_%=:\n"
  "s_waitcnt lgkmcnt(8)\n"
  "ds_read_b128 a[124:127], v216 offset:14464\n"
  "ds_read_b64_tr_b16 v[244:245], v230 offset:14560\n"
  "ds_read_b64_tr_b16 v[246:247], v230 offset:14592\n"
  "v_fma_f32 v50, v50, s73, v226\n"
  "v_cvt_pk_bf16_f32 v207, v211, v215\n"
  "v_mfma_f32_16x16x32_bf16 a[236:239], a[160:163], a[132:135], a[236:239]\n"
  "s_add_i32 s76, s76, 1\n"
  "v_exp_f32_e64 v48, v48\n"
  "v_mfma_f32_16x16x32_bf16 a[240:243], a[160:163], a[128:131], a[240:243]\n"
  "s_waitcnt lgkmcnt(9)\n"
  "buffer_store_dwordx4 a[252:255], v228, s[52:55], s82 offen offset:256 nt\n"
  "ds_write_b64 v229, v[206:207] offset:16544\n"
  "v_exp_f32 v49, v49\n"
  "v_fma_f32 v51, v51, s73, v226\n"
  "v_mfma_f32_16x16x32_bf16 v[184:187], v[60:63], a[132:135], v[184:187]\n"
  "v_exp_f32 v50, v50\n"
  "v_accvgpr_read_b32 v239, a251\n"
  "v_accvgpr_read_b32 v136, a180\n"
  "v_accvgpr_read_b32 v137, a181\n"
  "v_pk_fma_f32 v[136:137], v[136:137], v[72:73], v[72:73] neg_lo:[0,0,1] neg_hi:[0,0,1]\n"
  "v_pk_fma_f32 v[238:239], v[238:239], v[86:87], v[86:87] neg_lo:[0,0,1] neg_hi:[0,0,1]\n"
  "v_mfma_f32_16x16x32_bf16 v[188:191], v[60:63], a[128:131], v[188:191]\n"
  "v_exp_f32_e64 v51, v51\n"
  "v_mfma_f32_16x16x32_bf16 v[152:155], v[44:47], a[132:135], v[152:155]\n"
  "ds_read_b64_tr_b16 a[252:253], v230 offset:15680\n"
  "ds_read_b64_tr_b16 a[254:255], v230 offset:15712\n"
  "v_cvt_pk_bf16_f32 v56, v48, v49\n"
  "v_cvt_pk_bf16_f32 v57, v50, v51\n"
  "v_accvgpr_read_b32 v140, a184\n"
  "v_mfma_f32_16x16x32_bf16 v[156:159], v[44:47], a[128:131], v[156:159]\n"
  "v_exp_f32_e64 v4, v4\n"
  "v_accvgpr_read_b32 v141, a185\n"
  "v_accvgpr_read_b32 v144, a188\n"
  "v_accvgpr_read_b32 v145, a189\n"
  "v_pk_fma_f32 v[140:141], v[140:141], v[72:73], v[72:73] neg_lo:[0,0,1] neg_hi:[0,0,1]\n"
  "v_pk_fma_f32 v[144:145], v[144:145], v[72:73], v[72:73] neg_lo:[0,0,1] neg_hi:[0,0,1]\n"
  "v_pk_fma_f32 v[184:185], v[184:185], v[80:81], v[80:81] neg_lo:[0,0,1] neg_hi:[0,0,1]\n"
  "v_pk_fma_f32 v[186:187], v[186:187], v[82:83], v[82:83] neg_lo:[0,0,1] neg_hi:[0,0,1]\n"
  "v_mfma_f32_16x16x32_bf16 v[120:123], v[28:31], a[132:135], v[120:123]\n"
  "v_exp_f32_e64 v5, v5\n"
  "v_mfma_f32_16x16x32_bf16 v[124:127], v[28:31], a[128:131], v[124:127]\n"
  "s_waitcnt lgkmcnt(3)\n"
  "buffer_store_dwordx4 v[244:247], v228, s[52:55], s82 offen offset:3328 nt\n"
  "v_exp_f32 v6, v6\n"
  "v_fma_f32 v7, v7, s73, v223\n"
  "v_cvt_pk_bf16_f32 v10, v4, v5\n"
  "v_accvgpr_read_b32 v148, a192\n"
  "v_accvgpr_read_b32 v149, a193\n"
  "v_pk_fma_f32 v[148:149], v[148:149], v[72:73], v[72:73] neg_lo:[0,0,1] neg_hi:[0,0,1]\n"
  "v_pk_fma_f32 v[188:189], v[188:189], v[80:81], v[80:81] neg_lo:[0,0,1] neg_hi:[0,0,1]\n"
  "v_pk_fma_f32 v[190:191], v[190:191], v[82:83], v[82:83] neg_lo:[0,0,1] neg_hi:[0,0,1]\n"
  "v_pk_fma_f32 v[152:153], v[152:153], v[76:77], v[76:77] neg_lo:[0,0,1] neg_hi:[0,0,1]\n"
  "v_mfma_f32_16x16x32_bf16 v[88:91], v[12:15], a[132:135], v[88:91]\n"
  "ds_read_b64_tr_b16 v[244:245], v230 offset:16800\n"
  "ds_read_b64_tr_b16 v[246:247], v230 offset:16832\n"
  "v_exp_f32_e64 v7, v7\n"
  "v_accvgpr_read_b32 v138, a182\n"
  "v_pk_fma_f32 v[156:157], v[156:157], v[76:77], v[76:77] neg_lo:[0,0,1] neg_hi:[0,0,1]\n"
  "v_pk_fma_f32 v[154:155], v[154:155], v[78:79], v[78:79] neg_lo:[0,0,1] neg_hi:[0,0,1]\n"
  "v_pk_fma_f32 v[158:159], v[158:159], v[78:79], v[78:79] neg_lo:[0,0,1] neg_hi:[0,0,1]\n"
  "v_mfma_f32_16x16x32_bf16 v[92:95], v[12:15], a[128:131], v[92:95]\n"
  "v_cvt_pk_bf16_f32 v11, v6, v7\n"
  "v_fma_f32 v20, v20, s73, v224\n"
  "v_accvgpr_read_b32 v139, a183\n"
  "v_accvgpr_read_b32 v142, a186\n"
  "v_accvgpr_read_b32 v143, a187\n"
  "v_pk_fma_f32 v[138:139], v[138:139], v[74:75], v[74:75] neg_lo:[0,0,1] neg_hi:[0,0,1]\n"
  "v_pk_fma_f32 v[142:143], v[142:143], v[74:75], v[74:75] neg_lo:[0,0,1] neg_hi:[0,0,1]\n"
  "v_pk_fma_f32 v[120:121], v[120:121], v[72:73], v[72:73] neg_lo:[0,0,1] neg_hi:[0,0,1]\n"
  "v_pk_fma_f32 v[122:123], v[122:123], v[74:75], v[74:75] neg_lo:[0,0,1] neg_hi:[0,0,1]\n"
  "v_mfma_f32_16x16x32_bf16 v[64:67], a[136:139], a[96:99], v[248:251]\n"
  "v_exp_f32 v20, v20\n"
  "v_mfma_f32_16x16x32_bf16 v[64:67], a[140:143], a[100:103], v[64:67]\n"
  "v_fma_f32 v21, v21, s73, v224\n"
  "v_fma_f32 v22, v22, s73, v224\n"
  "v_mfma_f32_16x16x32_bf16 v[64:67], a[148:151], a[104:107], v[64:67]\n"
  "v_exp_f32 v21, v21\n"
  "v_mfma_f32_16x16x32_bf16 v[64:67], a[144:147], a[108:111], v[64:67]\n"
  "v_exp_f32_e64 v22, v22\n"
  "v_mfma_f32_16x16x32_bf16 v[64:67], a[120:123], a[112:115], v[64:67]\n"
  "v_fma_f32 v23, v23, s73, v224\n"
  "v_cvt_pk_bf16_f32 v26, v20, v21\n"
  "v_mfma_f32_16x16x32_bf16 v[64:67], a[124:127], a[116:119], v[64:67]\n"
  "s_waitcnt lgkmcnt(2)\n"
  "buffer_store_dwordx4 a[252:255], v228, s[52:55], s83 offen offset:256 nt\n"
  "v_exp_f32 v23, v23\n"
  "v_fma_f32 v36, v36, s73, v225\n"
  "v_mfma_f32_16x16x32_bf16 v[0:3], a[124:127], a[20:23], v[248:251]\n"
  "v_exp_f32 v36, v36\n"
  "v_mfma_f32_16x16x32_bf16 v[0:3], a[120:123], a[16:19], v[0:3]\n"
  "v_cvt_pk_bf16_f32 v27, v22, v23\n"
  "v_fma_f32 v37, v37, s73, v225\n"
  "v_mfma_f32_16x16x32_bf16 v[0:3], a[144:147], a[12:15], v[0:3]\n"
  "v_exp_f32 v37, v37\n"
  "v_mfma_f32_16x16x32_bf16 v[0:3], a[148:151], a[8:11], v[0:3]\n"
  "v_fma_f32 v38, v38, s73, v225\n"
  "v_fma_f32 v39, v39, s73, v225\n"
  "v_mfma_f32_16x16x32_bf16 v[0:3], a[140:143], a[4:7], v[0:3]\n"
  "v_exp_f32 v38, v38\n"
  "v_mfma_f32_16x16x32_bf16 v[0:3], a[136:139], a[0:3], v[0:3]\n"
  "s_waitcnt lgkmcnt(0)\n"
  "buffer_store_dwordx4 v[244:247], v228, s[52:55], s83 offen offset:3328 nt\n"
  "v_exp_f32_e64 v39, v39\n"
  "v_cvt_pk_bf16_f32 v42, v36, v37\n"
  "v_fma_f32 v52, v52, s73, v226\n"
  "v_accvgpr_read_b32 v146, a190\n"
  "v_accvgpr_read_b32 v147, a191\n"
  "v_pk_fma_f32 v[146:147], v[146:147], v[74:75], v[74:75] neg_lo:[0,0,1] neg_hi:[0,0,1]\n"
  "v_pk_fma_f32 v[124:125], v[124:125], v[72:73], v[72:73] neg_lo:[0,0,1] neg_hi:[0,0,1]\n"
  "v_pk_fma_f32 v[126:127], v[126:127], v[74:75], v[74:75] neg_lo:[0,0,1] neg_hi:[0,0,1]\n"
  "v_pk_fma_f32 v[88:89], v[88:89], v[68:69], v[68:69] neg_lo:[0,0,1] neg_hi:[0,0,1]\n"
  "v_mfma_f32_16x16x32_bf16 v[16:19], a[136:139], a[24:27], v[248:251]\n"
  "v_exp_f32_e64 v52, v52\n"
  "v_mfma_f32_16x16x32_bf16 v[16:19], a[140:143], a[28:31], v[16:19]\n"
  "v_cvt_pk_bf16_f32 v43, v38, v39\n"
  "v_fma_f32 v53, v53, s73, v226\n"
  "v_mfma_f32_16x16x32_bf16 v[16:19], a[148:151], a[32:35], v[16:19]\n"
  "ds_read_b64_tr_b16 a[132:133], v219 offset:12352\n"
  "v_fma_f32 v54, v54, s73, v226\n"
  "v_mfma_f32_16x16x32_bf16 v[16:19], a[144:147], a[36:39], v[16:19]\n"
  "ds_read_b64_tr_b16 a[134:135], v220 offset:12352\n"
  "v_fma_f32 v55, v55, s73, v226\n"
  "v_mfma_f32_16x16x32_bf16 v[16:19], a[120:123], a[40:43], v[16:19]\n"
  "ds_read_b64_tr_b16 a[128:129], v219 offset:12368\n"
  "v_accvgpr_read_b32 v150, a194\n"
  "v_mfma_f32_16x16x32_bf16 v[16:19], a[124:127], a[44:47], v[16:19]\n"
  "ds_read_b64_tr_b16 a[130:131], v220 offset:12368\n"
  "v_accvgpr_read_b32 v151, a195\n"
  "v_cvt_pk_bf16_f32 v136, v136, v140\n"
  "v_cvt_pk_bf16_f32 v140, v137, v141\n"
  "v_cvt_pk_bf16_f32 v137, v144, v148\n"
  "v_pk_fma_f32 v[150:151], v[150:151], v[74:75], v[74:75] neg_lo:[0,0,1] neg_hi:[0,0,1]\n"
  "v_pk_fma_f32 v[92:93], v[92:93], v[68:69], v[68:69] neg_lo:[0,0,1] neg_hi:[0,0,1]\n"
  "v_pk_fma_f32 v[90:91], v[90:91], v[70:71], v[70:71] neg_lo:[0,0,1] neg_hi:[0,0,1]\n"
  "v_pk_fma_f32 v[94:95], v[94:95], v[70:71], v[70:71] neg_lo:[0,0,1] neg_hi:[0,0,1]\n"
  "v_mfma_f32_16x16x32_bf16 v[32:35], a[124:127], a[68:71], v[248:251]\n"
  "v_exp_f32 v53, v53\n"
  "v_mfma_f32_16x16x32_bf16 v[32:35], a[120:123], a[64:67], v[32:35]\n"
  "v_exp_f32 v54, v54\n"
  "v_mfma_f32_16x16x32_bf16 v[32:35], a[144:147], a[60:63], v[32:35]\n"
  "v_exp_f32 v55, v55\n"
  "v_mfma_f32_16x16x32_bf16 v[32:35], a[148:151], a[56:59], v[32:35]\n"
  "v_cvt_pk_bf16_f32 v58, v52, v53\n"
  "v_cvt_pk_bf16_f32 v59, v54, v55\n"
  "v_mfma_f32_16x16x32_bf16 v[32:35], a[140:143], a[52:55], v[32:35]\n"
  "v_cvt_pk_bf16_f32 v138, v138, v142\n"
  "v_cvt_pk_bf16_f32 v142, v139, v143\n"
  "v_mfma_f32_16x16x32_bf16 v[32:35], a[136:139], a[48:51], v[32:35]\n"
  "v_cvt_pk_bf16_f32 v139, v146, v150\n"
  "v_accvgpr_read_b32 v104, a164\n"
  "v_mfma_f32_16x16x32_bf16 v[48:51], a[136:139], a[72:75], v[248:251]\n"
  "ds_read_b128 a[136:139], v235 offset:12352\n"
  "v_accvgpr_read_b32 v105, a165\n"
  "v_mfma_f32_16x16x32_bf16 v[48:51], a[140:143], a[76:79], v[48:51]\n"
  "ds_read_b128 a[140:143], v235 offset:12416\n"
  "v_accvgpr_read_b32 v108, a168\n"
  "v_mfma_f32_16x16x32_bf16 v[48:51], a[148:151], a[80:83], v[48:51]\n"
  "ds_read_b128 a[148:151], v235 offset:13376\n"
  "v_accvgpr_read_b32 v109, a169\n"
  "v_mfma_f32_16x16x32_bf16 v[48:51], a[144:147], a[84:87], v[48:51]\n"
  "ds_read_b128 a[144:147], v235 offset:13440\n"
  "v_accvgpr_read_b32 v112, a172\n"
  "v_mfma_f32_16x16x32_bf16 v[48:51], a[120:123], a[88:91], v[48:51]\n"
  "ds_read_b128 a[120:123], v235 offset:14400\n"
  "v_accvgpr_read_b32 v113, a173\n"
  "v_mfma_f32_16x16x32_bf16 v[48:51], a[124:127], a[92:95], v[48:51]\n"
  "s_waitcnt lgkmcnt(5)\n"
  "ds_read_b128 a[124:127], v235 offset:14464\n"
  "ds_write_b64 v229, v[136:137] offset:2240\n"
  "v_accvgpr_read_b32 v116, a176\n"
  "v_pk_fma_f32 v[104:105], v[104:105], v[68:69], v[68:69] neg_lo:[0,0,1] neg_hi:[0,0,1]\n"
  "v_pk_fma_f32 v[108:109], v[108:109], v[68:69], v[68:69] neg_lo:[0,0,1] neg_hi:[0,0,1]\n"
  "v_pk_fma_f32 v[112:113], v[112:113], v[68:69], v[68:69] neg_lo:[0,0,1] neg_hi:[0,0,1]\n"
  "v_mfma_f32_16x16x32_bf16 v[96:99], v[12:15], a[132:135], v[96:99]\n"
  "ds_write_b64 v229, v[138:139] offset:2816\n"
  "v_accvgpr_read_b32 v117, a177\n"
  "v_accvgpr_read_b32 v106, a166\n"
  "v_pk_fma_f32 v[116:117], v[116:117], v[68:69], v[68:69] neg_lo:[0,0,1] neg_hi:[0,0,1]\n"
  "v_mfma_f32_16x16x32_bf16 v[100:103], v[12:15], a[128:131], v[100:103]\n"
  "v_accvgpr_read_b32 v107, a167\n"
  "v_accvgpr_read_b32 v110, a170\n"
  "v_mfma_f32_16x16x32_bf16 v[128:131], v[28:31], a[132:135], v[128:131]\n"
  "v_accvgpr_read_b32 v111, a171\n"
  "v_accvgpr_read_b32 v114, a174\n"
  "v_accvgpr_read_b32 v115, a175\n"
  "v_accvgpr_read_b32 v118, a178\n"
  "v_accvgpr_read_b32 v119, a179\n"
  "v_pk_fma_f32 v[106:107], v[106:107], v[70:71], v[70:71] neg_lo:[0,0,1] neg_hi:[0,0,1]\n"
  "v_pk_fma_f32 v[110:111], v[110:111], v[70:71], v[70:71] neg_lo:[0,0,1] neg_hi:[0,0,1]\n"
  "v_pk_fma_f32 v[114:115], v[114:115], v[70:71], v[70:71] neg_lo:[0,0,1] neg_hi:[0,0,1]\n"
  "v_pk_fma_f32 v[118:119], v[118:119], v[70:71], v[70:71] neg_lo:[0,0,1] neg_hi:[0,0,1]\n"
  "v_mfma_f32_16x16x32_bf16 v[132:135], v[28:31], a[128:131], v[132:135]\n"
  "v_cvt_pk_bf16_f32 v141, v145, v149\n"
  "v_cvt_pk_bf16_f32 v143, v147, v151\n"
  "v_cvt_pk_bf16_f32 v104, v104, v108\n"
  "v_cvt_pk_bf16_f32 v108, v105, v109\n"
  "v_cvt_pk_bf16_f32 v105, v112, v116\n"
  "v_pk_fma_f32 v[96:97], v[96:97], v[68:69], v[68:69] neg_lo:[0,0,1] neg_hi:[0,0,1]\n"
  "v_pk_fma_f32 v[98:99], v[98:99], v[70:71], v[70:71] neg_lo:[0,0,1] neg_hi:[0,0,1]\n"
  "v_mfma_f32_16x16x32_bf16 v[160:163], v[44:47], a[132:135], v[160:163]\n"
  "ds_write_b64 v229, v[140:141] offset:2528\n"
  "v_cvt_pk_bf16_f32 v109, v113, v117\n"
  "v_cvt_pk_bf16_f32 v106, v106, v110\n"
  "v_pk_fma_f32 v[100:101], v[100:101], v[68:69], v[68:69] neg_lo:[0,0,1] neg_hi:[0,0,1]\n"
  "v_pk_fma_f32 v[102:103], v[102:103], v[70:71], v[70:71] neg_lo:[0,0,1] neg_hi:[0,0,1]\n"
  "v_mfma_f32_16x16x32_bf16 v[164:167], v[44:47], a[128:131], v[164:167]\n"
  "v_cvt_pk_bf16_f32 v110, v107, v111\n"
  "v_cvt_pk_bf16_f32 v107, v114, v118\n"
  "v_mfma_f32_16x16x32_bf16 v[192:195], v[60:63], a[132:135], v[192:195]\n"
  "ds_write_b64 v229, v[142:143] offset:3104\n"
  "v_cvt_pk_bf16_f32 v111, v115, v119\n"
  "v_cvt_pk_bf16_f32 v236, v236, v238\n"
  "v_pk_fma_f32 v[128:129], v[128:129], v[72:73], v[72:73] neg_lo:[0,0,1] neg_hi:[0,0,1]\n"
  "v_pk_fma_f32 v[132:133], v[132:133], v[72:73], v[72:73] neg_lo:[0,0,1] neg_hi:[0,0,1]\n"
  "v_pk_fma_f32 v[130:131], v[130:131], v[74:75], v[74:75] neg_lo:[0,0,1] neg_hi:[0,0,1]\n"
  "v_pk_fma_f32 v[134:135], v[134:135], v[74:75], v[74:75] neg_lo:[0,0,1] neg_hi:[0,0,1]\n"
  "v_mfma_f32_16x16x32_bf16 v[196:199], v[60:63], a[128:131], v[196:199]\n"
  "s_nop 0\n"
  "s_waitcnt lgkmcnt(8)\n"
  "ds_write_b64 v229, v[104:105] offset:0\n"
  "v_cvt_pk_bf16_f32 v237, v237, v239\n"
  "v_cvt_pk_bf16_f32 v184, v184, v188\n"
  "v_pk_fma_f32 v[160:161], v[160:161], v[76:77], v[76:77] neg_lo:[0,0,1] neg_hi:[0,0,1]\n"
  "v_pk_fma_f32 v[162:163], v[162:163], v[78:79], v[78:79] neg_lo:[0,0,1] neg_hi:[0,0,1]\n"
  "v_mfma_f32_16x16x32_bf16 v[4:7], a[136:139], a[0:3], v[248:251]\n"
  "ds_write_b32 v233, v236 offset:20736\n"
  "v_mfma_f32_16x16x32_bf16 v[4:7], a[140:143], a[4:7], v[4:7]\n"
  "s_waitcnt lgkmcnt(8)\n"
  "ds_write_b32 v233, v237 offset:21024\n"
  "v_mfma_f32_16x16x32_bf16 v[4:7], a[148:151], a[8:11], v[4:7]\n"
  "v_cvt_pk_bf16_f32 v188, v185, v189\n"
  "v_cvt_pk_bf16_f32 v186, v186, v190\n"
  "v_mfma_f32_16x16x32_bf16 v[4:7], a[144:147], a[12:15], v[4:7]\n"
  "s_waitcnt lgkmcnt(7)\n"
  "v_cvt_pk_bf16_f32 v190, v187, v191\n"
  "v_accvgpr_read_b32 v240, a236\n"
  "v_mfma_f32_16x16x32_bf16 v[4:7], a[120:123], a[16:19], v[4:7]\n"
  "v_accvgpr_read_b32 v241, a237\n"
  "v_accvgpr_read_b32 v236, a238\n"
  "v_mfma_f32_16x16x32_bf16 v[4:7], a[124:127], a[20:23], v[4:7]\n"
  "ds_write_b64 v229, v[108:109] offset:288\n"
  "v_mfma_f32_16x16x32_bf16 v[20:23], a[124:127], a[44:47], v[248:251]\n"
  "v_accvgpr_read_b32 v237, a239\n"
  "v_accvgpr_read_b32 v242, a240\n"
  "v_mfma_f32_16x16x32_bf16 v[20:23], a[120:123], a[40:43], v[20:23]\n"
  "v_accvgpr_read_b32 v243, a241\n"
  "v_accvgpr_read_b32 v238, a242\n"
  "v_mfma_f32_16x16x32_bf16 v[20:23], a[144:147], a[36:39], v[20:23]\n"
  "ds_read_b64_tr_b16 a[132:133], v217 offset:14400\n"
  "v_accvgpr_read_b32 v239, a243\n"
  "v_mfma_f32_16x16x32_bf16 v[20:23], a[148:151], a[32:35], v[20:23]\n"
  "ds_read_b64_tr_b16 a[134:135], v218 offset:14400\n"
  "v_fma_f32 v64, v64, s73, v227\n"
  "v_mfma_f32_16x16x32_bf16 v[20:23], a[140:143], a[28:31], v[20:23]\n"
  "ds_read_b64_tr_b16 a[128:129], v217 offset:14416\n"
  "v_fma_f32 v65, v65, s73, v227\n"
  "v_mfma_f32_16x16x32_bf16 v[20:23], a[136:139], a[24:27], v[20:23]\n"
  "ds_read_b64_tr_b16 a[130:131], v218 offset:14416\n"
  "ds_write_b64 v229, v[106:107] offset:576\n"
  "v_fma_f32 v66, v66, s73, v227\n"
  "v_pk_fma_f32 v[192:193], v[192:193], v[80:81], v[80:81] neg_lo:[0,0,1] neg_hi:[0,0,1]\n"
  "v_pk_fma_f32 v[196:197], v[196:197], v[80:81], v[80:81] neg_lo:[0,0,1] neg_hi:[0,0,1]\n"
  "v_pk_fma_f32 v[194:195], v[194:195], v[82:83], v[82:83] neg_lo:[0,0,1] neg_hi:[0,0,1]\n"
  "v_pk_fma_f32 v[198:199], v[198:199], v[82:83], v[82:83] neg_lo:[0,0,1] neg_hi:[0,0,1]\n"
  "v_mfma_f32_16x16x32_bf16 v[36:39], a[136:139], a[48:51], v[248:251]\n"
  "v_exp_f32 v64, v64\n"
  "v_mfma_f32_16x16x32_bf16 v[36:39], a[140:143], a[52:55], v[36:39]\n"
  "v_exp_f32 v65, v65\n"
  "v_mfma_f32_16x16x32_bf16 v[36:39], a[148:151], a[56:59], v[36:39]\n"
  "v_exp_f32_e64 v66, v66\n"
  "v_mfma_f32_16x16x32_bf16 v[36:39], a[144:147], a[60:63], v[36:39]\n"
  "v_cvt_pk_bf16_f32 v185, v192, v196\n"
  "v_cvt_pk_bf16_f32 v187, v194, v198\n"
  "v_mfma_f32_16x16x32_bf16 v[36:39], a[120:123], a[64:67], v[36:39]\n"
  "v_cvt_pk_bf16_f32 v189, v193, v197\n"
  "v_cvt_pk_bf16_f32 v191, v195, v199\n"
  "v_mfma_f32_16x16x32_bf16 v[36:39], a[124:127], a[68:71], v[36:39]\n"
  "ds_write_b64 v229, v[110:111] offset:864\n"
  "v_fma_f32 v67, v67, s73, v227\n"
  "v_cvt_pk_bf16_f32 v152, v152, v156\n"
  "v_pk_fma_f32 v[164:165], v[164:165], v[76:77], v[76:77] neg_lo:[0,0,1] neg_hi:[0,0,1]\n"
  "v_pk_fma_f32 v[166:167], v[166:167], v[78:79], v[78:79] neg_lo:[0,0,1] neg_hi:[0,0,1]\n"
  "v_pk_fma_f32 v[240:241], v[240:241], v[84:85], v[84:85] neg_lo:[0,0,1] neg_hi:[0,0,1]\n"
  "v_pk_fma_f32 v[242:243], v[242:243], v[84:85], v[84:85] neg_lo:[0,0,1] neg_hi:[0,0,1]\n"
  "v_mfma_f32_16x16x32_bf16 v[52:55], a[124:127], a[92:95], v[248:251]\n"
  "ds_read_b64_tr_b16 a[124:125], v219 offset:14400\n"
  "v_cvt_pk_bf16_f32 v156, v153, v157\n"
  "v_mfma_f32_16x16x32_bf16 v[52:55], a[120:123], a[88:91], v[52:55]\n"
  "ds_read_b64_tr_b16 a[126:127], v220 offset:14400\n"
  "ds_read_b64_tr_b16 a[252:253], v230 offset:0\n"
  "v_mfma_f32_16x16x32_bf16 v[52:55], a[144:147], a[84:87], v[52:55]\n"
  "ds_read_b64_tr_b16 a[120:121], v219 offset:14416\n"
  "ds_read_b64_tr_b16 a[254:255], v230 offset:32\n"
  "v_mfma_f32_16x16x32_bf16 v[52:55], a[148:151], a[80:83], v[52:55]\n"
  "ds_read_b64_tr_b16 a[122:123], v220 offset:14416\n"
  "ds_read_b64_tr_b16 v[244:245], v230 offset:1120\n"
  "v_mfma_f32_16x16x32_bf16 v[52:55], a[140:143], a[76:79], v[52:55]\n"
  "v_exp_f32 v67, v67\n"
  "v_mfma_f32_16x16x32_bf16 v[52:55], a[136:139], a[72:75], v[52:55]\n"
  "s_waitcnt lgkmcnt(9)\n"
  "ds_read_b64_tr_b16 v[246:247], v230 offset:1152\n"
  "v_cvt_pk_bf16_f32 v153, v160, v164\n"
  "v_mfma_f32_16x16x32_bf16 a[244:247], a[156:159], a[132:135], 1.0\n"
  "ds_write_b64 v229, v[184:185] offset:15680\n"
  "v_cvt_pk_bf16_f32 v154, v154, v158\n"
  "v_cvt_pk_bf16_f32 v158, v155, v159\n"
  "v_pk_fma_f32 v[236:237], v[236:237], v[86:87], v[86:87] neg_lo:[0,0,1] neg_hi:[0,0,1]\n"
  "v_pk_fma_f32 v[238:239], v[238:239], v[86:87], v[86:87] neg_lo:[0,0,1] neg_hi:[0,0,1]\n"
  "v_mfma_f32_16x16x32_bf16 a[248:251], a[156:159], a[128:131], 1.0\n"
  "ds_write_b64 v229, v[186:187] offset:16256\n"
  "v_cvt_pk_bf16_f32 v155, v162, v166\n"
  "v_cvt_pk_bf16_f32 v64, v64, v65\n"
  "v_mfma_f32_16x16x32_bf16 v[200:203], v[56:59], a[132:135], 1.0\n"
  "s_nop 0\n"
  "s_waitcnt lgkmcnt(5)\n"
  "buffer_store_dwordx4 a[252:255], v228, s[52:55], s80 offen offset:0 nt\n"
  "ds_read_b64_tr_b16 a[140:141], v217 offset:13376\n"
  "ds_write_b64 v229, v[188:189] offset:15968\n"
  "v_cvt_pk_bf16_f32 v65, v66, v67\n"
  "v_cvt_pk_bf16_f32 v157, v161, v165\n"
  "v_mfma_f32_16x16x32_bf16 v[204:207], v[56:59], a[128:131], 1.0\n"
  "ds_read_b64_tr_b16 a[142:143], v218 offset:13376\n"
  "ds_read_b64_tr_b16 a[252:253], v230 offset:2240\n"
  "v_mfma_f32_16x16x32_bf16 v[168:171], v[40:43], a[132:135], 1.0\n"
  "ds_read_b64_tr_b16 a[136:137], v217 offset:13392\n"
  "ds_read_b64_tr_b16 a[254:255], v230 offset:2272\n"
  "ds_write_b64 v229, v[190:191] offset:16544\n"
  "v_mfma_f32_16x16x32_bf16 v[172:175], v[40:43], a[128:131], 1.0\n"
  "ds_read_b64_tr_b16 a[138:139], v218 offset:13392\n"
  "ds_write_b64 v229, v[152:153] offset:13440\n"
  "v_cvt_pk_bf16_f32 v159, v163, v167\n"
  "v_mfma_f32_16x16x32_bf16 v[136:139], v[24:27], a[132:135], 1.0\n"
  "ds_write_b64 v229, v[154:155] offset:14016\n"
  "v_mfma_f32_16x16x32_bf16 v[140:143], v[24:27], a[128:131], 1.0\n"
  "ds_write_b64 v231, v[64:65] offset:2048\n"
  "v_cvt_pk_bf16_f32 v120, v120, v124\n"
  "v_cvt_pk_bf16_f32 v124, v121, v125\n"
  "v_mfma_f32_16x16x32_bf16 v[104:107], v[8:11], a[132:135], 1.0\n"
  "s_nop 0\n"
  "s_waitcnt lgkmcnt(13)\n"
  "buffer_store_dwordx4 v[244:247], v228, s[52:55], s80 offen offset:3072 nt\n"
  "ds_write_b64 v229, v[156:157] offset:13728\n"
  "v_cvt_pk_bf16_f32 v121, v128, v132\n"
  "v_cvt_pk_bf16_f32 v122, v122, v126\n"
  "v_cvt_pk_bf16_f32 v126, v123, v127\n"
  "v_mfma_f32_16x16x32_bf16 v[108:111], v[8:11], a[128:131], 1.0\n"
  "ds_read_b64_tr_b16 v[244:245], v230 offset:3360\n"
  "ds_read_b64_tr_b16 v[246:247], v230 offset:3392\n"
  "v_mfma_f32_16x16x32_bf16 v[112:115], v[8:11], a[124:127], 1.0\n"
  "ds_write_b64 v229, v[158:159] offset:14304\n"
  "v_cvt_pk_bf16_f32 v123, v130, v134\n"
  "v_cvt_pk_bf16_f32 v125, v129, v133\n"
  "v_mfma_f32_16x16x32_bf16 v[116:119], v[8:11], a[120:123], 1.0\n"
  "ds_write_b64 v229, v[120:121] offset:11200\n"
  "v_cvt_pk_bf16_f32 v127, v131, v135\n"
  "v_cvt_pk_bf16_f32 v88, v88, v92\n"
  "v_mfma_f32_16x16x32_bf16 v[144:147], v[24:27], a[124:127], 1.0\n"
  "ds_read_b64_tr_b16 a[132:133], v219 offset:13376\n"
  "ds_write_b64 v229, v[122:123] offset:11776\n"
  "v_cvt_pk_bf16_f32 v92, v89, v93\n"
  "v_mfma_f32_16x16x32_bf16 v[148:151], v[24:27], a[120:123], 1.0\n"
  "s_nop 0\n"
  "ds_read_b64_tr_b16 a[134:135], v220 offset:13376\n"
  "v_cvt_pk_bf16_f32 v89, v96, v100\n"
  "v_mfma_f32_16x16x32_bf16 v[176:179], v[40:43], a[124:127], 1.0\n"
  "s_waitcnt lgkmcnt(13)\n"
  "buffer_store_dwordx4 a[252:255], v228, s[52:55], s81 offen offset:0 nt\n"
  "ds_read_b64_tr_b16 a[128:129], v219 offset:13392\n"
  "ds_write_b64 v229, v[124:125] offset:11488\n"
  "v_cvt_pk_bf16_f32 v90, v90, v94\n"
  "v_cvt_pk_bf16_f32 v94, v91, v95\n"
  "v_mfma_f32_16x16x32_bf16 v[180:183], v[40:43], a[120:123], 1.0\n"
  "ds_read_b64_tr_b16 a[130:131], v220 offset:13392\n"
  "ds_write_b64 v229, v[126:127] offset:12064\n"
  "v_cvt_pk_bf16_f32 v91, v98, v102\n"
  "v_mfma_f32_16x16x32_bf16 v[208:211], v[56:59], a[124:127], 1.0\n"
  "s_nop 0\n"
  "v_cvt_pk_bf16_f32 v93, v97, v101\n"
  "v_cvt_pk_bf16_f32 v95, v99, v103\n"
  "v_mfma_f32_16x16x32_bf16 v[212:215], v[56:59], a[120:123], 1.0\n"
  "s_waitcnt lgkmcnt(15)\n"
  "ds_write_b64 v229, v[88:89] offset:8960\n"
  "v_fma_f32 v0, v0, s73, v223\n"
  "v_fma_f32 v1, v1, s73, v223\n"
  "v_mfma_f32_16x16x32_bf16 a[236:239], a[156:159], a[140:143], 1.0\n"
  "ds_write_b64 v229, v[90:91] offset:9536\n"
  "v_exp_f32_e64 v0, v0\n"
  "v_mfma_f32_16x16x32_bf16 a[240:243], a[156:159], a[136:139], 1.0\n"
  "s_nop 0\n"
  "ds_write_b64 v229, v[92:93] offset:9248\n"
  "v_mfma_f32_16x16x32_bf16 v[184:187], v[56:59], a[140:143], 1.0\n"
  "s_waitcnt lgkmcnt(12)\n"
  "buffer_store_dwordx4 v[244:247], v228, s[52:55], s81 offen offset:3072 nt\n"
  "ds_write_b64 v229, v[94:95] offset:9824\n"
  "v_exp_f32_e64 v1, v1\n"
  "v_fma_f32 v2, v2, s73, v223\n"
  "v_mfma_f32_16x16x32_bf16 v[188:191], v[56:59], a[136:139], 1.0\n"
  "v_exp_f32 v2, v2\n"
  "v_cvt_pk_bf16_f32 v240, v240, v242\n"
  "v_cvt_pk_bf16_f32 v241, v241, v243\n"
  "v_cvt_pk_bf16_f32 v236, v236, v238\n"
  "v_mfma_f32_16x16x32_bf16 v[152:155], v[40:43], a[140:143], 1.0\n"
  "s_mov_b32 m0, s67\n"
  "ds_write_b32 v233, v240 offset:19040\n"
  "v_fma_f32 v3, v3, s73, v223\n"
  "v_cvt_pk_bf16_f32 v12, v0, v1\n"
  "v_cvt_pk_bf16_f32 v237, v237, v239\n"
  "v_mfma_f32_16x16x32_bf16 v[156:159], v[40:43], a[136:139], 1.0\n"
  "s_waitcnt lgkmcnt(6)\n"
  "s_barrier\n"
  "ds_read_b128 a[124:127], v216 offset:0\n"
  "v_mfma_f32_16x16x32_bf16 v[120:123], v[24:27], a[140:143], 1.0\n"
  "ds_read_b64 a[160:161], v231 offset:2048\n"
  "ds_write_b32 v233, v241 offset:19328\n"
  "v_fma_f32 v16, v16, s73, v224\n"
  "v_accvgpr_read_b32 v240, a196\n"
  "v_mfma_f32_16x16x32_bf16 v[124:127], v[24:27], a[136:139], 1.0\n"
  "ds_read_b64 a[162:163], v232 offset:2048\n"
  "ds_write_b32 v233, v236 offset:19616\n"
  "v_fma_f32 v17, v17, s73, v224\n"
  "v_accvgpr_read_b32 v241, a197\n"
  "v_pk_fma_f32 v[240:241], v[240:241], v[76:77], v[76:77] neg_lo:[0,0,1] neg_hi:[0,0,1]\n"
  "v_mfma_f32_16x16x32_bf16 v[88:91], v[8:11], a[140:143], 1.0\n"
  "buffer_load_dwordx4 v221, s[40:43], 0 offen offset:0 lds\n"
  "v_mfma_f32_16x16x32_bf16 v[92:95], v[8:11], a[136:139], 1.0\n"
  "s_nop 0\n"
  "ds_read_b128 a[120:123], v216 offset:64\n"
  "v_fma_f32 v18, v18, s73, v224\n"
  "v_mfma_f32_16x16x32_bf16 v[96:99], v[8:11], a[132:135], 1.0\n"
  "s_addk_i32 m0, 896\n"
  "ds_read_b128 a[140:143], v216 offset:1024\n"
  "ds_write_b32 v233, v237 offset:19904\n"
  "v_fma_f32 v19, v19, s73, v224\n"
  "v_accvgpr_read_b32 v242, a200\n"
  "v_mfma_f32_16x16x32_bf16 v[100:103], v[8:11], a[128:131], 1.0\n"
  "ds_read_b128 a[136:139], v216 offset:1088\n"
  "v_fma_f32 v32, v32, s73, v225\n"
  "v_accvgpr_read_b32 v243, a201\n"
  "v_accvgpr_read_b32 v236, a204\n"
  "v_accvgpr_read_b32 v237, a205\n"
  "v_pk_fma_f32 v[242:243], v[242:243], v[76:77], v[76:77] neg_lo:[0,0,1] neg_hi:[0,0,1]\n"
  "v_pk_fma_f32 v[236:237], v[236:237], v[76:77], v[76:77] neg_lo:[0,0,1] neg_hi:[0,0,1]\n"
  "v_mfma_f32_16x16x32_bf16 v[128:131], v[24:27], a[132:135], 1.0\n"
  "buffer_load_dwordx4 v221, s[40:43], 0 offen offset:128 lds\n"
  "v_fma_f32 v33, v33, s73, v225\n"
  "v_mfma_f32_16x16x32_bf16 v[132:135], v[24:27], a[128:131], 1.0\n"
  "s_addk_i32 m0, 896\n"
  "ds_read_b128 a[144:147], v216 offset:2048\n"
  "v_fma_f32 v34, v34, s73, v225\n"
  "v_mfma_f32_16x16x32_bf16 v[160:163], v[40:43], a[132:135], 1.0\n"
  "s_nop 0\n"
  "ds_read_b128 a[148:151], v216 offset:2112\n"
  "v_fma_f32 v35, v35, s73, v225\n"
  "v_accvgpr_read_b32 v238, a208\n"
  "v_accvgpr_read_b32 v239, a209\n"
  "v_cvt_pk_bf16_f32 v240, v240, v242\n"
  "v_pk_fma_f32 v[238:239], v[238:239], v[76:77], v[76:77] neg_lo:[0,0,1] neg_hi:[0,0,1]\n"
  "v_mfma_f32_16x16x32_bf16 v[164:167], v[40:43], a[128:131], 1.0\n"
  "v_exp_f32_e64 v3, v3\n"
  "v_cvt_pk_bf16_f32 v242, v241, v243\n"
  "v_cvt_pk_bf16_f32 v241, v236, v238\n"
  "v_cvt_pk_bf16_f32 v243, v237, v239\n"
  "v_mfma_f32_16x16x32_bf16 v[192:195], v[56:59], a[132:135], 1.0\n"
  "buffer_load_dwordx4 v221, s[40:43], 0 offen offset:256 lds\n"
  "v_cvt_pk_bf16_f32 v13, v2, v3\n"
  "v_mfma_f32_16x16x32_bf16 v[196:199], v[56:59], a[128:131], 1.0\n"
  "s_waitcnt lgkmcnt(5)\n"
  "v_exp_f32 v16, v16\n"
  "v_accvgpr_read_b32 v236, a198\n"
  "v_accvgpr_read_b32 v237, a199\n"
  "v_accvgpr_read_b32 v238, a202\n"
  "v_pk_fma_f32 v[236:237], v[236:237], v[78:79], v[78:79] neg_lo:[0,0,1] neg_hi:[0,0,1]\n"
  "v_mfma_f32_16x16x32_bf16 v[64:67], a[124:127], a[96:99], v[248:251]\n"
  "s_add_u32 m0, s59, -128\n"
  "v_exp_f32 v17, v17\n"
  "v_mfma_f32_16x16x32_bf16 v[64:67], a[120:123], a[100:103], v[64:67]\n"
  "s_waitcnt lgkmcnt(2)\n"
  "v_exp_f32 v18, v18\n"
  "v_mfma_f32_16x16x32_bf16 v[64:67], a[140:143], a[104:107], v[64:67]\n"
  "buffer_load_dwordx4 v221, s[32:35], 0 offen offset:128 lds\n"
  "v_cvt_pk_bf16_f32 v28, v16, v17\n"
  "v_mfma_f32_16x16x32_bf16 v[64:67], a[136:139], a[108:111], v[64:67]\n"
  "s_waitcnt lgkmcnt(0)\n"
  "v_exp_f32 v19, v19\n"
  "v_mfma_f32_16x16x32_bf16 v[64:67], a[144:147], a[112:115], v[64:67]\n"
  "s_addk_i32 m0, 1024\n"
  "v_exp_f32 v32, v32\n"
  "v_mfma_f32_16x16x32_bf16 v[64:67], a[148:151], a[116:119], v[64:67]\n"
  "s_mov_b64 s[32:33], s[44:45]\n"
  "v_exp_f32 v33, v33\n"
  "v_mfma_f32_16x16x32_bf16 v[0:3], a[148:151], a[20:23], v[248:251]\n"
  "buffer_load_dwordx4 v221, s[44:47], 0 offen offset:128 lds\n"
  "v_cvt_pk_bf16_f32 v29, v18, v19\n"
  "v_mfma_f32_16x16x32_bf16 v[0:3], a[144:147], a[16:19], v[0:3]\n"
  "s_addk_i32 m0, 896\n"
  "v_exp_f32 v34, v34\n"
  "v_mfma_f32_16x16x32_bf16 v[0:3], a[136:139], a[12:15], v[0:3]\n"
  "s_add_u32 s40, s40, s31\n"
  "v_exp_f32 v35, v35\n"
  "v_mfma_f32_16x16x32_bf16 v[0:3], a[140:143], a[8:11], v[0:3]\n"
  "s_addc_u32 s41, s41, 0\n"
  "v_cvt_pk_bf16_f32 v44, v32, v33\n"
  "v_cvt_pk_bf16_f32 v45, v34, v35\n"
  "v_mfma_f32_16x16x32_bf16 v[0:3], a[120:123], a[4:7], v[0:3]\n"
  "buffer_load_dwordx4 v221, s[44:47], 0 offen offset:256 lds\n"
  "v_fma_f32 v48, v48, s73, v226\n"
  "v_mfma_f32_16x16x32_bf16 v[0:3], a[124:127], a[0:3], v[0:3]\n"
  "s_add_u32 s44, s44, s31\n"
  "v_exp_f32 v48, v48\n"
  "v_accvgpr_read_b32 v239, a203\n"
  "v_mfma_f32_16x16x32_bf16 v[16:19], a[124:127], a[24:27], v[248:251]\n"
  "s_addc_u32 s45, s45, 0\n"
  "v_fma_f32 v49, v49, s73, v226\n"
  "v_fma_f32 v50, v50, s73, v226\n"
  "v_mfma_f32_16x16x32_bf16 v[16:19], a[120:123], a[28:31], v[16:19]\n"
  "v_exp_f32 v49, v49\n"
  "v_mfma_f32_16x16x32_bf16 v[16:19], a[140:143], a[32:35], v[16:19]\n"
  "ds_read_b128 a[132:135], v235 offset:0\n"
  "v_fma_f32 v51, v51, s73, v226\n"
  "v_mfma_f32_16x16x32_bf16 v[16:19], a[136:139], a[36:39], v[16:19]\n"
  "ds_read_b128 a[128:131], v235 offset:64\n"
  "v_cvt_pk_bf16_f32 v60, v48, v49\n"
  "v_mfma_f32_16x16x32_bf16 v[16:19], a[144:147], a[40:43], v[16:19]\n"
  "v_exp_f32 v50, v50\n"
  "v_mfma_f32_16x16x32_bf16 v[16:19], a[148:151], a[44:47], v[16:19]\n"
  "v_exp_f32 v51, v51\n"
  "v_mfma_f32_16x16x32_bf16 v[32:35], a[148:151], a[68:71], v[248:251]\n"
  "v_cvt_pk_bf16_f32 v61, v50, v51\n"
  "v_fma_f32 v4, v4, s73, v223\n"
  "v_mfma_f32_16x16x32_bf16 v[32:35], a[144:147], a[64:67], v[32:35]\n"
  "v_exp_f32 v4, v4\n"
  "v_mfma_f32_16x16x32_bf16 v[32:35], a[136:139], a[60:63], v[32:35]\n"
  "v_fma_f32 v5, v5, s73, v223\n"
  "v_fma_f32 v6, v6, s73, v223\n"
  "v_mfma_f32_16x16x32_bf16 v[32:35], a[140:143], a[56:59], v[32:35]\n"
  "v_exp_f32 v5, v5\n"
  "v_mfma_f32_16x16x32_bf16 v[32:35], a[120:123], a[52:55], v[32:35]\n"
  "v_exp_f32 v6, v6\n"
  "v_mfma_f32_16x16x32_bf16 v[32:35], a[124:127], a[48:51], v[32:35]\n"
  "v_fma_f32 v7, v7, s73, v223\n"
  "v_cvt_pk_bf16_f32 v14, v4, v5\n"
  "v_mfma_f32_16x16x32_bf16 v[48:51], a[124:127], a[72:75], v[248:251]\n"
  "ds_read_b128 a[124:127], v235 offset:1024\n"
  "v_fma_f32 v20, v20, s73, v224\n"
  "v_mfma_f32_16x16x32_bf16 v[48:51], a[120:123], a[76:79], v[48:51]\n"
  "ds_read_b128 a[120:123], v235 offset:1088\n"
  "v_fma_f32 v21, v21, s73, v224\n"
  "v_mfma_f32_16x16x32_bf16 v[48:51], a[140:143], a[80:83], v[48:51]\n"
  "ds_read_b128 a[140:143], v235 offset:2048\n"
  "v_fma_f32 v22, v22, s73, v224\n"
  "v_mfma_f32_16x16x32_bf16 v[48:51], a[136:139], a[84:87], v[48:51]\n"
  "ds_read_b128 a[136:139], v235 offset:2112\n"
  "v_fma_f32 v23, v23, s73, v224\n"
  "v_mfma_f32_16x16x32_bf16 v[48:51], a[144:147], a[88:91], v[48:51]\n"
  "v_exp_f32 v7, v7\n"
  "v_mfma_f32_16x16x32_bf16 v[48:51], a[148:151], a[92:95], v[48:51]\n"
  "s_waitcnt lgkmcnt(4)\n"
  "v_cvt_pk_bf16_f32 v15, v6, v7\n"
  "v_fma_f32 v36, v36, s73, v225\n"
  "v_mfma_f32_16x16x32_bf16 v[4:7], a[132:135], a[0:3], v[248:251]\n"
  "v_exp_f32 v20, v20\n"
  "v_mfma_f32_16x16x32_bf16 v[4:7], a[128:131], a[4:7], v[4:7]\n"
  "s_waitcnt lgkmcnt(2)\n"
  "v_exp_f32 v21, v21\n"
  "v_mfma_f32_16x16x32_bf16 v[4:7], a[124:127], a[8:11], v[4:7]\n"
  "v_exp_f32 v22, v22\n"
  "v_mfma_f32_16x16x32_bf16 v[4:7], a[120:123], a[12:15], v[4:7]\n"
  "s_waitcnt lgkmcnt(0)\n"
  "v_exp_f32_e64 v23, v23\n"
  "v_mfma_f32_16x16x32_bf16 v[4:7], a[140:143], a[16:19], v[4:7]\n"
  "v_cvt_pk_bf16_f32 v30, v20, v21\n"
  "v_cvt_pk_bf16_f32 v31, v22, v23\n"
  "v_mfma_f32_16x16x32_bf16 v[4:7], a[136:139], a[20:23], v[4:7]\n"
  "v_exp_f32 v36, v36\n"
  "v_fma_f32 v37, v37, s73, v225\n"
  "v_exp_f32 v37, v37\n"
  "v_mfma_f32_16x16x32_bf16 v[20:23], a[136:139], a[44:47], v[248:251]\n"
  "v_fma_f32 v38, v38, s73, v225\n"
  "v_fma_f32 v39, v39, s73, v225\n"
  "v_mfma_f32_16x16x32_bf16 v[20:23], a[140:143], a[40:43], v[20:23]\n"
  "v_exp_f32 v38, v38\n"
  "v_mfma_f32_16x16x32_bf16 v[20:23], a[120:123], a[36:39], v[20:23]\n"
  "ds_read_b64_tr_b16 a[144:145], v217 offset:2048\n"
  "v_cvt_pk_bf16_f32 v46, v36, v37\n"
  "v_mfma_f32_16x16x32_bf16 v[20:23], a[124:127], a[32:35], v[20:23]\n"
  "ds_read_b64_tr_b16 a[146:147], v218 offset:2048\n"
  "v_fma_f32 v52, v52, s73, v226\n"
  "v_mfma_f32_16x16x32_bf16 v[20:23], a[128:131], a[28:31], v[20:23]\n"
  "ds_read_b64_tr_b16 a[148:149], v217 offset:2064\n"
  "v_exp_f32 v39, v39\n"
  "v_mfma_f32_16x16x32_bf16 v[20:23], a[132:135], a[24:27], v[20:23]\n"
  "ds_read_b64_tr_b16 a[150:151], v218 offset:2064\n"
  "ds_write_b64 v229, v[240:241] offset:4480\n"
  "v_cvt_pk_bf16_f32 v47, v38, v39\n"
  "v_exp_f32 v52, v52\n"
  "v_pk_fma_f32 v[238:239], v[238:239], v[78:79], v[78:79] neg_lo:[0,0,1] neg_hi:[0,0,1]\n"
  "v_mfma_f32_16x16x32_bf16 v[36:39], a[132:135], a[48:51], v[248:251]\n"
  "v_fma_f32 v53, v53, s73, v226\n"
  "v_fma_f32 v54, v54, s73, v226\n"
  "v_mfma_f32_16x16x32_bf16 v[36:39], a[128:131], a[52:55], v[36:39]\n"
  "v_exp_f32 v53, v53\n"
  "v_mfma_f32_16x16x32_bf16 v[36:39], a[124:127], a[56:59], v[36:39]\n"
  "v_exp_f32 v54, v54\n"
  "v_mfma_f32_16x16x32_bf16 v[36:39], a[120:123], a[60:63], v[36:39]\n"
  "v_fma_f32 v55, v55, s73, v226\n"
  "v_cvt_pk_bf16_f32 v62, v52, v53\n"
  "v_mfma_f32_16x16x32_bf16 v[36:39], a[140:143], a[64:67], v[36:39]\n"
  "v_exp_f32 v55, v55\n"
  "v_mfma_f32_16x16x32_bf16 v[36:39], a[136:139], a[68:71], v[36:39]\n"
  "v_cvt_pk_bf16_f32 v63, v54, v55\n"
  "v_accvgpr_read_b32 v240, a206\n"
  "v_mfma_f32_16x16x32_bf16 v[52:55], a[136:139], a[92:95], v[248:251]\n"
  "ds_read_b64_tr_b16 a[136:137], v219 offset:2048\n"
  "v_accvgpr_read_b32 v241, a207\n"
  "v_mfma_f32_16x16x32_bf16 v[52:55], a[140:143], a[88:91], v[52:55]\n"
  "ds_read_b64_tr_b16 a[138:139], v220 offset:2048\n"
  "v_cvt_pk_bf16_f32 v236, v236, v238\n"
  "v_mfma_f32_16x16x32_bf16 v[52:55], a[120:123], a[84:87], v[52:55]\n"
  "ds_read_b64_tr_b16 a[140:141], v219 offset:2064\n"
  "v_cvt_pk_bf16_f32 v238, v237, v239\n"
  "v_mfma_f32_16x16x32_bf16 v[52:55], a[124:127], a[80:83], v[52:55]\n"
  "ds_read_b64_tr_b16 a[142:143], v220 offset:2064\n"
  "v_fma_f32 v64, v64, s73, v227\n"
  "v_mfma_f32_16x16x32_bf16 v[52:55], a[128:131], a[76:79], v[52:55]\n"
  "v_exp_f32 v64, v64\n"
  "v_mfma_f32_16x16x32_bf16 v[52:55], a[132:135], a[72:75], v[52:55]\n"
  "s_waitcnt lgkmcnt(5)\n"
  "ds_write_b64 v229, v[242:243] offset:4768\n"
  "v_fma_f32 v65, v65, s73, v227\n"
  "v_fma_f32 v66, v66, s73, v227\n"
  "v_pk_fma_f32 v[240:241], v[240:241], v[78:79], v[78:79] neg_lo:[0,0,1] neg_hi:[0,0,1]\n"
  "v_mfma_f32_16x16x32_bf16 v[104:107], v[12:15], a[144:147], v[104:107]\n"
  "v_accvgpr_read_b32 v242, a210\n"
  "v_accvgpr_read_b32 v243, a211\n"
  "v_exp_f32_e64 v65, v65\n"
  "v_pk_fma_f32 v[242:243], v[242:243], v[78:79], v[78:79] neg_lo:[0,0,1] neg_hi:[0,0,1]\n"
  "v_mfma_f32_16x16x32_bf16 v[108:111], v[12:15], a[148:151], v[108:111]\n"
  "v_cvt_pk_bf16_f32 v237, v240, v242\n"
  "v_cvt_pk_bf16_f32 v239, v241, v243\n"
  "v_mfma_f32_16x16x32_bf16 v[136:139], v[28:31], a[144:147], v[136:139]\n"
  "ds_read_b64_tr_b16 a[120:121], v217 offset:0\n"
  "ds_write_b64 v229, v[236:237] offset:5056\n"
  "v_accvgpr_read_b32 v240, a212\n"
  "v_mfma_f32_16x16x32_bf16 v[140:143], v[28:31], a[148:151], v[140:143]\n"
  "ds_read_b64_tr_b16 a[122:123], v218 offset:0\n"
  "v_accvgpr_read_b32 v241, a213\n"
  "v_mfma_f32_16x16x32_bf16 v[168:171], v[44:47], a[144:147], v[168:171]\n"
  "ds_read_b64_tr_b16 a[124:125], v217 offset:1024\n"
  "ds_write_b64 v229, v[238:239] offset:5344\n"
  "v_accvgpr_read_b32 v242, a216\n"
  "v_pk_fma_f32 v[240:241], v[240:241], v[80:81], v[80:81] neg_lo:[0,0,1] neg_hi:[0,0,1]\n"
  "v_mfma_f32_16x16x32_bf16 v[172:175], v[44:47], a[148:151], v[172:175]\n"
  "ds_read_b64_tr_b16 a[126:127], v218 offset:1024\n"
  "v_accvgpr_read_b32 v243, a217\n"
  "v_accvgpr_read_b32 v236, a220\n"
  "v_accvgpr_read_b32 v237, a221\n"
  "v_accvgpr_read_b32 v238, a224\n"
  "v_pk_fma_f32 v[242:243], v[242:243], v[80:81], v[80:81] neg_lo:[0,0,1] neg_hi:[0,0,1]\n"
  "v_pk_fma_f32 v[236:237], v[236:237], v[80:81], v[80:81] neg_lo:[0,0,1] neg_hi:[0,0,1]\n"
  "v_mfma_f32_16x16x32_bf16 v[200:203], v[60:63], a[144:147], v[200:203]\n"
  "v_accvgpr_read_b32 v239, a225\n"
  "v_cvt_pk_bf16_f32 v240, v240, v242\n"
  "v_mfma_f32_16x16x32_bf16 v[204:207], v[60:63], a[148:151], v[204:207]\n"
  "ds_read_b64_tr_b16 a[252:253], v230 offset:4480\n"
  "ds_read_b64_tr_b16 a[254:255], v230 offset:4512\n"
  "v_cvt_pk_bf16_f32 v242, v241, v243\n"
  "v_exp_f32 v66, v66\n"
  "v_pk_fma_f32 v[238:239], v[238:239], v[80:81], v[80:81] neg_lo:[0,0,1] neg_hi:[0,0,1]\n"
  "v_mfma_f32_16x16x32_bf16 a[244:247], a[160:163], a[144:147], a[244:247]\n"
  "v_cvt_pk_bf16_f32 v241, v236, v238\n"
  "v_cvt_pk_bf16_f32 v243, v237, v239\n"
  "v_mfma_f32_16x16x32_bf16 a[248:251], a[160:163], a[148:151], a[248:251]\n"
  "s_waitcnt lgkmcnt(9)\n"
  "ds_write_b64 v229, v[240:241] offset:6720\n"
  "v_accvgpr_read_b32 v236, a214\n"
  "v_accvgpr_read_b32 v237, a215\n"
  "v_pk_fma_f32 v[236:237], v[236:237], v[82:83], v[82:83] neg_lo:[0,0,1] neg_hi:[0,0,1]\n"
  "v_mfma_f32_16x16x32_bf16 v[208:211], v[60:63], a[136:139], v[208:211]\n"
  "ds_write_b64 v229, v[242:243] offset:7008\n"
  "v_accvgpr_read_b32 v238, a218\n"
  "v_accvgpr_read_b32 v239, a219\n"
  "v_pk_fma_f32 v[238:239], v[238:239], v[82:83], v[82:83] neg_lo:[0,0,1] neg_hi:[0,0,1]\n"
  "v_mfma_f32_16x16x32_bf16 v[212:215], v[60:63], a[140:143], v[212:215]\n"
  "v_accvgpr_read_b32 v240, a222\n"
  "v_accvgpr_read_b32 v241, a223\n"
  "v_mfma_f32_16x16x32_bf16 v[176:179], v[44:47], a[136:139], v[176:179]\n"
  "ds_read_b64_tr_b16 a[128:129], v217 offset:16\n"
  "v_accvgpr_read_b32 v242, a226\n"
  "v_accvgpr_read_b32 v243, a227\n"
  "v_cvt_pk_bf16_f32 v236, v236, v238\n"
  "v_cvt_pk_bf16_f32 v238, v237, v239\n"
  "v_pk_fma_f32 v[240:241], v[240:241], v[82:83], v[82:83] neg_lo:[0,0,1] neg_hi:[0,0,1]\n"
  "v_pk_fma_f32 v[242:243], v[242:243], v[82:83], v[82:83] neg_lo:[0,0,1] neg_hi:[0,0,1]\n"
  "v_mfma_f32_16x16x32_bf16 v[180:183], v[44:47], a[140:143], v[180:183]\n"
  "ds_read_b64_tr_b16 a[130:131], v218 offset:16\n"
  "v_cvt_pk_bf16_f32 v237, v240, v242\n"
  "v_mfma_f32_16x16x32_bf16 v[144:147], v[28:31], a[136:139], v[144:147]\n"
  "ds_read_b64_tr_b16 a[132:133], v217 offset:1040\n"
  "ds_write_b64 v229, v[236:237] offset:7296\n"
  "v_cvt_pk_bf16_f32 v239, v241, v243\n"
  "v_mfma_f32_16x16x32_bf16 v[148:151], v[28:31], a[140:143], v[148:151]\n"
  "ds_read_b64_tr_b16 a[134:135], v218 offset:1040\n"
  "ds_write_b64 v229, v[238:239] offset:7584\n"
  "v_accvgpr_read_b32 v240, a228\n"
  "v_mfma_f32_16x16x32_bf16 v[112:115], v[12:15], a[136:139], v[112:115]\n"
  "s_nop 0\n"
  "v_accvgpr_read_b32 v241, a229\n"
  "v_accvgpr_read_b32 v236, a230\n"
  "v_mfma_f32_16x16x32_bf16 v[116:119], v[12:15], a[140:143], v[116:119]\n"
  "s_waitcnt lgkmcnt(10)\n"
  "v_accvgpr_read_b32 v237, a231\n"
  "v_accvgpr_read_b32 v242, a232\n"
  "v_accvgpr_read_b32 v243, a233\n"
  "v_accvgpr_read_b32 v238, a234\n"
  "v_accvgpr_read_b32 v239, a235\n"
  "v_pk_fma_f32 v[240:241], v[240:241], v[84:85], v[84:85] neg_lo:[0,0,1] neg_hi:[0,0,1]\n"
  "v_pk_fma_f32 v[242:243], v[242:243], v[84:85], v[84:85] neg_lo:[0,0,1] neg_hi:[0,0,1]\n"
  "v_pk_fma_f32 v[236:237], v[236:237], v[86:87], v[86:87] neg_lo:[0,0,1] neg_hi:[0,0,1]\n"
  "v_pk_fma_f32 v[238:239], v[238:239], v[86:87], v[86:87] neg_lo:[0,0,1] neg_hi:[0,0,1]\n"
  "v_mfma_f32_16x16x32_bf16 a[164:167], v[8:11], a[120:123], 1.0\n"
  "v_fma_f32 v67, v67, s73, v227\n"
  "v_cvt_pk_bf16_f32 v240, v240, v242\n"
  "v_mfma_f32_16x16x32_bf16 a[164:167], v[12:15], a[124:127], a[164:167]\n"
  "v_exp_f32_e64 v67, v67\n"
  "v_mfma_f32_16x16x32_bf16 a[180:183], v[24:27], a[120:123], 1.0\n"
  "ds_write_b32 v233, v240 offset:17920\n"
  "v_mfma_f32_16x16x32_bf16 a[180:183], v[28:31], a[124:127], a[180:183]\n"
  "v_cvt_pk_bf16_f32 v241, v241, v243\n"
  "v_cvt_pk_bf16_f32 v236, v236, v238\n"
  "v_cvt_pk_bf16_f32 v237, v237, v239\n"
  "v_cvt_pk_bf16_f32 v64, v64, v65\n"
  "v_cvt_pk_bf16_f32 v65, v66, v67\n"
  "v_mfma_f32_16x16x32_bf16 a[196:199], v[40:43], a[120:123], 1.0\n"
  "s_mov_b32 m0, s68\n"
  "ds_read_b64_tr_b16 a[136:137], v219 offset:0\n"
  "v_fma_f32 v0, v0, s73, v223\n"
  "v_mfma_f32_16x16x32_bf16 a[196:199], v[44:47], a[124:127], a[196:199]\n"
  "s_nop 0\n"
  "ds_read_b64_tr_b16 a[138:139], v220 offset:0\n"
  "ds_read_b64_tr_b16 v[244:245], v230 offset:5600\n"
  "ds_write_b32 v233, v241 offset:18208\n"
  "v_fma_f32 v1, v1, s73, v223\n"
  "v_mfma_f32_16x16x32_bf16 a[212:215], v[56:59], a[120:123], 1.0\n"
  "ds_read_b64_tr_b16 a[140:141], v219 offset:1024\n"
  "v_fma_f32 v2, v2, s73, v223\n"
  "v_mfma_f32_16x16x32_bf16 a[212:215], v[60:63], a[124:127], a[212:215]\n"
  "ds_read_b64_tr_b16 a[142:143], v220 offset:1024\n"
  "v_fma_f32 v3, v3, s73, v223\n"
  "v_mfma_f32_16x16x32_bf16 a[228:231], a[156:159], a[120:123], 1.0\n"
  "buffer_load_dwordx4 v221, s[40:43], 0 offen offset:0 lds\n"
  "v_mfma_f32_16x16x32_bf16 a[228:231], a[160:163], a[124:127], a[228:231]\n"
  "s_waitcnt lgkmcnt(8)\n"
  "ds_write_b32 v233, v236 offset:18496\n"
  "v_mfma_f32_16x16x32_bf16 a[232:235], a[156:159], a[128:131], 1.0\n"
  "s_addk_i32 m0, 896\n"
  "ds_write_b32 v233, v237 offset:18784\n"
  "v_mfma_f32_16x16x32_bf16 a[232:235], a[160:163], a[132:135], a[232:235]\n"
  "ds_write_b64 v231, v[64:65] offset:0\n"
  "v_mfma_f32_16x16x32_bf16 a[216:219], v[56:59], a[128:131], 1.0\n"
  "buffer_load_dwordx4 v221, s[40:43], 0 offen offset:128 lds\n"
  "v_fma_f32 v16, v16, s73, v224\n"
  "v_mfma_f32_16x16x32_bf16 a[216:219], v[60:63], a[132:135], a[216:219]\n"
  "s_addk_i32 m0, 896\n"
  "ds_read_b64_tr_b16 a[120:121], v219 offset:16\n"
  "v_fma_f32 v17, v17, s73, v224\n"
  "v_mfma_f32_16x16x32_bf16 a[200:203], v[40:43], a[128:131], 1.0\n"
  "ds_read_b64_tr_b16 a[122:123], v220 offset:16\n"
  "v_fma_f32 v18, v18, s73, v224\n"
  "v_mfma_f32_16x16x32_bf16 a[200:203], v[44:47], a[132:135], a[200:203]\n"
  "ds_read_b64_tr_b16 a[124:125], v219 offset:1040\n"
  "v_fma_f32 v19, v19, s73, v224\n"
  "v_mfma_f32_16x16x32_bf16 a[184:187], v[24:27], a[128:131], 1.0\n"
  "buffer_load_dwordx4 v221, s[40:43], 0 offen offset:256 lds\n"
  "v_fma_f32 v32, v32, s73, v225\n"
  "v_mfma_f32_16x16x32_bf16 a[184:187], v[28:31], a[132:135], a[184:187]\n"
  "s_mov_b32 m0, s60\n"
  "ds_read_b64_tr_b16 a[126:127], v220 offset:1040\n"
  "v_fma_f32 v33, v33, s73, v225\n"
  "v_mfma_f32_16x16x32_bf16 a[168:171], v[8:11], a[128:131], 1.0\n"
  "s_add_u32 s28, s96, 184448\n"
  "v_exp_f32 v0, v0\n"
  "v_mfma_f32_16x16x32_bf16 a[168:171], v[12:15], a[132:135], a[168:171]\n"
  "s_waitcnt lgkmcnt(7)\n"
  "v_exp_f32 v1, v1\n"
  "v_mfma_f32_16x16x32_bf16 a[172:175], v[8:11], a[136:139], 1.0\n"
  "buffer_load_dwordx4 v221, s[32:35], 0 offen offset:0 lds\n"
  "v_add_f32 v252, v0, v1\n"
  "v_mfma_f32_16x16x32_bf16 a[172:175], v[12:15], a[140:143], a[172:175]\n"
  "s_addk_i32 m0, 1024\n"
  "v_exp_f32 v2, v2\n"
  "v_mfma_f32_16x16x32_bf16 a[188:191], v[24:27], a[136:139], 1.0\n"
  "s_add_u32 s40, s40, s31\n"
  "v_exp_f32 v3, v3\n"
  "v_mfma_f32_16x16x32_bf16 a[188:191], v[28:31], a[140:143], a[188:191]\n"
  "s_addc_u32 s41, s41, 0\n"
  "v_exp_f32 v16, v16\n"
  "v_mfma_f32_16x16x32_bf16 a[204:207], v[40:43], a[136:139], 1.0\n"
  "buffer_load_dwordx4 v221, s[44:47], 0 offen offset:0 lds\n"
  "v_add_f32 v252, v252, v2\n"
  "v_mfma_f32_16x16x32_bf16 a[204:207], v[44:47], a[140:143], a[204:207]\n"
  "s_addk_i32 m0, 768\n"
  "v_exp_f32 v17, v17\n"
  "v_mfma_f32_16x16x32_bf16 a[220:223], v[56:59], a[136:139], 1.0\n"
  "s_mov_b64 s[32:33], s[44:45]\n"
  "v_exp_f32 v18, v18\n"
  "v_mfma_f32_16x16x32_bf16 a[220:223], v[60:63], a[140:143], a[220:223]\n"
  "s_waitcnt lgkmcnt(0)\n"
  "v_exp_f32 v19, v19\n"
  "v_mfma_f32_16x16x32_bf16 a[224:227], v[56:59], a[120:123], 1.0\n"
  "buffer_load_dwordx4 v221, s[44:47], 0 offen offset:256 lds\n"
  "v_add_f32 v252, v252, v3\n"
  "v_mfma_f32_16x16x32_bf16 a[224:227], v[60:63], a[124:127], a[224:227]\n"
  "s_add_u32 s44, s44, s31\n"
  "v_exp_f32 v32, v32\n"
  "v_mfma_f32_16x16x32_bf16 a[208:211], v[40:43], a[120:123], 1.0\n"
  "s_addc_u32 s45, s45, 0\n"
  "v_exp_f32 v33, v33\n"
  "v_mfma_f32_16x16x32_bf16 a[208:211], v[44:47], a[124:127], a[208:211]\n"
  "s_cmp_eq_u32 s95, 0\n"
  "s_cselect_b32 s95, 3, s95\n"
  "s_subb_u32 s100, s100, -11\n"
  "v_add_f32 v253, v16, v17\n"
  "v_add_f32 v253, v253, v18\n"
  "v_mfma_f32_16x16x32_bf16 a[192:195], v[24:27], a[120:123], 1.0\n"
  "s_sub_u32 s95, s95, 1\n"
  "v_add_f32 v253, v253, v19\n"
  "v_fma_f32 v34, v34, s73, v225\n"
  "v_mfma_f32_16x16x32_bf16 a[192:195], v[28:31], a[124:127], a[192:195]\n"
  "s_waitcnt vmcnt(6)\n"
  "s_barrier\n"
  "ds_read_b128 a[128:131], v216 offset:12352\n"
  "v_mfma_f32_16x16x32_bf16 a[176:179], v[8:11], a[120:123], 1.0\n"
  "s_lshr_b32 s79, s100, 0\n"
  "ds_read_b128 a[132:135], v216 offset:12416\n"
  "v_fma_f32 v35, v35, s73, v225\n"
  "v_mfma_f32_16x16x32_bf16 a[176:179], v[12:15], a[124:127], a[176:179]\n"
  "s_mul_i32 s79, s79, 7\n"
  "ds_read_b128 a[120:123], v216 offset:13376\n"
  "v_add_f32 v254, v32, v33\n"
  "v_mfma_f32_16x16x32_bf16 v[68:71], v[8:11], a[152:155], 0\n"
  "s_add_u32 s79, s79, s100\n"
  "ds_read_b128 a[124:127], v216 offset:13440\n"
  "v_cvt_pk_bf16_f32 v8, v0, v1\n"
  "v_mfma_f32_16x16x32_bf16 v[68:71], v[12:15], a[152:155], v[68:71]\n"
  "s_add_u32 s79, s79, s101\n"
  "ds_read_b128 a[136:139], v216 offset:14400\n"
  "v_cvt_pk_bf16_f32 v9, v2, v3\n"
  "v_mfma_f32_16x16x32_bf16 v[72:75], v[24:27], a[152:155], 0\n"
  "ds_read_b128 a[140:143], v216 offset:14464\n"
  "v_cvt_pk_bf16_f32 v24, v16, v17\n"
  "v_mfma_f32_16x16x32_bf16 v[72:75], v[28:31], a[152:155], v[72:75]\n"
  "s_mov_b32 m0, s66\n"
  "v_exp_f32 v34, v34\n"
  "v_mfma_f32_16x16x32_bf16 v[76:79], v[40:43], a[152:155], 0\n"
  "s_min_u32 s79, s79, 0x1ff\n"
  "v_exp_f32 v35, v35\n"
  "v_mfma_f32_16x16x32_bf16 v[76:79], v[44:47], a[152:155], v[76:79]\n"
  "s_mul_i32 s96, s79, 196608\n"
  "v_cvt_pk_bf16_f32 v25, v18, v19\n"
  "v_add_f32 v254, v254, v34\n"
  "v_mfma_f32_16x16x32_bf16 v[80:83], v[56:59], a[152:155], 0\n"
  "s_mul_i32 s92, s79, 325632\n"
  "v_add_f32 v254, v254, v35\n"
  "v_cvt_pk_bf16_f32 v40, v32, v33\n"
  "v_mfma_f32_16x16x32_bf16 v[80:83], v[60:63], a[152:155], v[80:83]\n"
  "buffer_load_dwordx4 v221, s[40:43], 0 offen offset:0 lds\n"
  "v_mfma_f32_16x16x32_bf16 v[84:87], a[156:159], a[152:155], 0\n"
  "s_add_u32 s96, s96, s39\n"
  "ds_read_b64 a[156:157], v231 offset:0\n"
  "v_cvt_pk_bf16_f32 v41, v34, v35\n"
  "v_mfma_f32_16x16x32_bf16 v[84:87], a[160:163], a[152:155], v[84:87]\n"
  "L_CUT_%=:\n"
  "s_waitcnt lgkmcnt(5)\n"
  "ds_read_b64 a[158:159], v232 offset:0\n"
  "v_fma_f32 v48, v48, s73, v226\n"
  "v_mfma_f32_16x16x32_bf16 v[64:67], a[128:131], a[96:99], v[248:251]\n"
  "s_addk_i32 m0, 896\n"
  "v_exp_f32 v48, v48\n"
  "v_mfma_f32_16x16x32_bf16 v[64:67], a[132:135], a[100:103], v[64:67]\n"
  "s_waitcnt lgkmcnt(4)\n"
  "buffer_load_dwordx4 v221, s[40:43], 0 offen offset:128 lds\n"
  "v_fma_f32 v49, v49, s73, v226\n"
  "v_mfma_f32_16x16x32_bf16 v[64:67], a[120:123], a[104:107], v[64:67]\n"
  "s_addk_i32 m0, 896\n"
  "v_exp_f32 v49, v49\n"
  "v_mfma_f32_16x16x32_bf16 v[64:67], a[124:127], a[108:111], v[64:67]\n"
  "s_waitcnt lgkmcnt(2)\n"
  "v_fma_f32 v50, v50, s73, v226\n"
  "v_fma_f32 v51, v51, s73, v226\n"
  "v_mfma_f32_16x16x32_bf16 v[64:67], a[136:139], a[112:115], v[64:67]\n"
  "s_mul_i32 s79, s95, 110592\n"
  "v_exp_f32 v50, v50\n"
  "v_mfma_f32_16x16x32_bf16 v[64:67], a[140:143], a[116:119], v[64:67]\n"
  "buffer_load_dwordx4 v221, s[40:43], 0 offen offset:256 lds\n"
  "v_add_f32 v255, v48, v49\n"
  "v_mfma_f32_16x16x32_bf16 v[0:3], a[140:143], a[20:23], v[248:251]\n"
  "s_add_u32 s92, s92, s79\n"
  "v_exp_f32 v51, v51\n"
  "v_mfma_f32_16x16x32_bf16 v[0:3], a[136:139], a[16:19], v[0:3]\n"
  "s_add_u32 s71, s92, s80\n"
  "v_add_f32 v255, v255, v50\n"
  "v_add_f32 v255, v255, v51\n"
  "v_mfma_f32_16x16x32_bf16 v[0:3], a[124:127], a[12:15], v[0:3]\n"
  "v_cvt_pk_bf16_f32 v56, v48, v49\n"
  "v_cvt_pk_bf16_f32 v57, v50, v51\n"
  "v_mfma_f32_16x16x32_bf16 v[0:3], a[120:123], a[8:11], v[0:3]\n"
  "s_add_u32 m0, s58, -128\n"
  "v_fma_f32 v52, v52, s73, v226\n"
  "v_mfma_f32_16x16x32_bf16 v[0:3], a[132:135], a[4:7], v[0:3]\n"
  "s_add_u32 s48, s84, s71\n"
  "v_exp_f32 v52, v52\n"
  "v_mfma_f32_16x16x32_bf16 v[0:3], a[128:131], a[0:3], v[0:3]\n"
  "s_addc_u32 s49, s85, 0\n"
  "v_fma_f32 v53, v53, s73, v226\n"
  "v_fma_f32 v54, v54, s73, v226\n"
  "v_mfma_f32_16x16x32_bf16 v[16:19], a[128:131], a[24:27], v[248:251]\n"
  "s_cmp_eq_u32 s95, 2\n"
  "s_cselect_b32 s94, 17, 18\n"
  "v_exp_f32 v53, v53\n"
  "v_mfma_f32_16x16x32_bf16 v[16:19], a[132:135], a[28:31], v[16:19]\n"
  "buffer_load_dwordx4 v221, s[32:35], 0 offen offset:128 lds\n"
  "v_fma_f32 v55, v55, s73, v226\n"
  "v_mfma_f32_16x16x32_bf16 v[16:19], a[120:123], a[32:35], v[16:19]\n"
  "s_addk_i32 m0, 1024\n"
  "ds_read_b64_tr_b16 a[144:145], v217 offset:12352\n"
  "v_cvt_pk_bf16_f32 v58, v52, v53\n"
  "v_mfma_f32_16x16x32_bf16 v[16:19], a[124:127], a[36:39], v[16:19]\n"
  "s_mul_i32 s94, s94, 6144\n"
  "ds_read_b64_tr_b16 a[146:147], v218 offset:12352\n"
  "v_add_f32 v255, v255, v52\n"
  "v_mfma_f32_16x16x32_bf16 v[16:19], a[136:139], a[40:43], v[16:19]\n"
  "s_cmp_lt_i32 s76, 5\n"
  "s_cselect_b32 s38, s94, 0\n"
  "s_cselect_b32 s28, s96, s28\n"
  "ds_read_b64_tr_b16 a[148:149], v217 offset:13376\n"
  "v_mfma_f32_16x16x32_bf16 v[16:19], a[140:143], a[44:47], v[16:19]\n"
  "buffer_load_dwordx4 v221, s[44:47], 0 offen offset:128 lds\n"
  "v_fma_f32 v36, v36, s73, v225\n"
  "v_mfma_f32_16x16x32_bf16 v[32:35], a[140:143], a[68:71], v[248:251]\n"
  "s_addk_i32 m0, 896\n"
  "ds_read_b64_tr_b16 a[150:151], v218 offset:13376\n"
  "v_fma_f32 v37, v37, s73, v225\n"
  "v_mfma_f32_16x16x32_bf16 v[32:35], a[136:139], a[64:67], v[32:35]\n"
  "s_sub_i32 s71, s38, s80\n"
  "v_exp_f32 v54, v54\n"
  "v_mfma_f32_16x16x32_bf16 v[32:35], a[124:127], a[60:63], v[32:35]\n"
  "s_max_i32 s50, s71, 0\n"
  "v_exp_f32 v55, v55\n"
  "v_mfma_f32_16x16x32_bf16 v[32:35], a[120:123], a[56:59], v[32:35]\n"
  "buffer_load_dwordx4 v221, s[44:47], 0 offen offset:256 lds\n"
  "v_cvt_pk_bf16_f32 v59, v54, v55\n"
  "v_mfma_f32_16x16x32_bf16 v[32:35], a[132:135], a[52:55], v[32:35]\n"
  "s_add_u32 s40, s40, s31\n"
  "v_exp_f32 v36, v36\n"
  "v_mfma_f32_16x16x32_bf16 v[32:35], a[128:131], a[48:51], v[32:35]\n"
  "s_addc_u32 s41, s41, 0\n"
  "v_exp_f32 v37, v37\n"
  "v_mfma_f32_16x16x32_bf16 v[48:51], a[128:131], a[72:75], v[248:251]\n"
  "s_mov_b64 s[32:33], s[44:45]\n"
  "ds_read_b64_tr_b16 a[128:129], v217 offset:12368\n"
  "v_fma_f32 v38, v38, s73, v225\n"
  "v_mfma_f32_16x16x32_bf16 v[48:51], a[132:135], a[76:79], v[48:51]\n"
  "ds_read_b64_tr_b16 a[130:131], v218 offset:12368\n"
  "v_fma_f32 v39, v39, s73, v225\n"
  "v_mfma_f32_16x16x32_bf16 v[48:51], a[120:123], a[80:83], v[48:51]\n"
  "ds_read_b64_tr_b16 a[132:133], v217 offset:13392\n"
  "v_cvt_pk_bf16_f32 v42, v36, v37\n"
  "v_mfma_f32_16x16x32_bf16 v[48:51], a[124:127], a[84:87], v[48:51]\n"
  "s_add_u32 m0, s72, 0\n"
  "ds_read_b64_tr_b16 a[134:135], v218 offset:13392\n"
  "v_fma_f32 v20, v20, s73, v224\n"
  "v_mfma_f32_16x16x32_bf16 v[48:51], a[136:139], a[88:91], v[48:51]\n"
  "s_min_u32 s30, s38, 0xc00\n"
  "v_exp_f32 v38, v38\n"
  "v_mfma_f32_16x16x32_bf16 v[48:51], a[140:143], a[92:95], v[48:51]\n"
  "s_waitcnt lgkmcnt(4)\n"
  "v_exp_f32 v39, v39\n"
  "v_mfma_f32_16x16x32_bf16 a[236:239], a[160:163], a[144:147], a[236:239]\n"
  "v_exp_f32 v20, v20\n"
  "v_mfma_f32_16x16x32_bf16 a[236:239], a[156:159], a[148:151], a[236:239]\n"
  "buffer_load_dwordx4 v221, s[48:51], 0 offen offset:0 nt lds\n"
  "v_cvt_pk_bf16_f32 v43, v38, v39\n"
  "v_mfma_f32_16x16x32_bf16 v[184:187], v[60:63], a[144:147], v[184:187]\n"
  "v_fma_f32 v21, v21, s73, v224\n"
  "v_fma_f32 v22, v22, s73, v224\n"
  "v_mfma_f32_16x16x32_bf16 v[184:187], v[56:59], a[148:151], v[184:187]\n"
  "s_add_u32 s44, s44, s31\n"
  "v_exp_f32 v21, v21\n"
  "v_exp_f32_e64 v22, v22\n"
  "v_fma_f32 v23, v23, s73, v224\n"
  "v_exp_f32 v23, v23\n"
  "v_mfma_f32_16x16x32_bf16 v[152:155], v[44:47], a[144:147], v[152:155]\n"
  "s_addc_u32 s45, s45, 0\n"
  "ds_read_b64_tr_b16 a[120:121], v219 offset:12352\n"
  "v_cvt_pk_bf16_f32 v26, v20, v21\n"
  "v_mfma_f32_16x16x32_bf16 v[152:155], v[40:43], a[148:151], v[152:155]\n"
  "ds_read_b64_tr_b16 a[122:123], v220 offset:12352\n"
  "v_cvt_pk_bf16_f32 v27, v22, v23\n"
  "v_fma_f32 v4, v4, s73, v223\n"
  "v_exp_f32 v4, v4\n"
  "v_fma_f32 v5, v5, s73, v223\n"
  "v_exp_f32 v5, v5\n"
  "v_fma_f32 v6, v6, s73, v223\n"
  "v_exp_f32 v6, v6\n"
  "v_fma_f32 v7, v7, s73, v223\n"
  "v_exp_f32 v7, v7\n"
  "v_mfma_f32_16x16x32_bf16 v[120:123], v[28:31], a[144:147], v[120:123]\n"
  "ds_read_b64_tr_b16 a[124:125], v219 offset:13376\n"
  "v_cvt_pk_bf16_f32 v10, v4, v5\n"
  "v_mfma_f32_16x16x32_bf16 v[120:123], v[24:27], a[148:151], v[120:123]\n"
  "ds_read_b64_tr_b16 a[126:127], v220 offset:13376\n"
  "v_cvt_pk_bf16_f32 v11, v6, v7\n"
  "v_mfma_f32_16x16x32_bf16 v[88:91], v[12:15], a[144:147], v[88:91]\n"
  "s_add_u32 m0, s72, -1984\n"
  "v_add_f32 v252, v252, v4\n"
  "v_add_f32 v252, v252, v5\n"
  "v_mfma_f32_16x16x32_bf16 v[88:91], v[8:11], a[148:151], v[88:91]\n"
  "s_waitcnt lgkmcnt(4)\n"
  "buffer_load_dwordx4 v221, s[48:51], 0 offen offset:3072 nt lds\n"
  "v_add_f32 v252, v252, v6\n"
  "v_mfma_f32_16x16x32_bf16 v[92:95], v[12:15], a[128:131], v[92:95]\n"
  "v_add_f32 v252, v252, v7\n"
  "v_add_f32 v253, v253, v20\n"
  "v_mfma_f32_16x16x32_bf16 v[92:95], v[8:11], a[132:135], v[92:95]\n"
  "v_add_f32 v253, v253, v21\n"
  "v_add_f32 v253, v253, v22\n"
  "v_mfma_f32_16x16x32_bf16 v[124:127], v[28:31], a[128:131], v[124:127]\n"
  "ds_read_b128 a[136:139], v235 offset:12352\n"
  "v_add_f32 v253, v253, v23\n"
  "v_mfma_f32_16x16x32_bf16 v[124:127], v[24:27], a[132:135], v[124:127]\n"
  "s_add_u32 m0, s72, 2112\n"
  "ds_read_b128 a[140:143], v235 offset:12416\n"
  "v_add_f32 v254, v254, v36\n"
  "v_mfma_f32_16x16x32_bf16 v[156:159], v[44:47], a[128:131], v[156:159]\n"
  "ds_read_b128 a[144:147], v235 offset:13376\n"
  "v_add_f32 v254, v254, v37\n"
  "v_mfma_f32_16x16x32_bf16 v[156:159], v[40:43], a[132:135], v[156:159]\n"
  "ds_read_b128 a[148:151], v235 offset:13440\n"
  "v_add_f32 v254, v254, v38\n"
  "v_mfma_f32_16x16x32_bf16 v[188:191], v[60:63], a[128:131], v[188:191]\n"
  "v_add_f32 v254, v254, v39\n"
  "v_add_f32 v255, v255, v53\n"
  "v_mfma_f32_16x16x32_bf16 v[188:191], v[56:59], a[132:135], v[188:191]\n"
  "buffer_load_dwordx4 v221, s[48:51], 0 offen offset:128 nt lds\n"
  "v_add_f32 v255, v255, v54\n"
  "v_mfma_f32_16x16x32_bf16 a[240:243], a[160:163], a[128:131], a[240:243]\n"
  "ds_read_b128 a[128:131], v235 offset:14400\n"
  "v_add_f32 v255, v255, v55\n"
  "v_mfma_f32_16x16x32_bf16 a[240:243], a[156:159], a[132:135], a[240:243]\n"
  "s_waitcnt lgkmcnt(5)\n"
  "ds_read_b128 a[132:135], v235 offset:14464\n"
  "v_fma_f32 v64, v64, s73, v227\n"
  "v_mfma_f32_16x16x32_bf16 v[192:195], v[60:63], a[120:123], v[192:195]\n"
  "v_exp_f32 v64, v64\n"
  "v_mfma_f32_16x16x32_bf16 v[192:195], v[56:59], a[124:127], v[192:195]\n"
  "s_add_u32 m0, s72, 128\n"
  "v_fma_f32 v65, v65, s73, v227\n"
  "v_mfma_f32_16x16x32_bf16 v[160:163], v[44:47], a[120:123], v[160:163]\n"
  "v_exp_f32 v65, v65\n"
  "v_mfma_f32_16x16x32_bf16 v[160:163], v[40:43], a[124:127], v[160:163]\n"
  "v_fma_f32 v66, v66, s73, v227\n"
  "v_fma_f32 v67, v67, s73, v227\n"
  "v_mfma_f32_16x16x32_bf16 v[128:131], v[28:31], a[120:123], v[128:131]\n"
  "v_exp_f32 v66, v66\n"
  "v_mfma_f32_16x16x32_bf16 v[128:131], v[24:27], a[124:127], v[128:131]\n"
  "buffer_load_dwordx4 v221, s[48:51], 0 offen offset:3200 nt lds\n"
  "v_cvt_pk_bf16_f32 v64, v64, v65\n"
  "v_mfma_f32_16x16x32_bf16 v[96:99], v[12:15], a[120:123], v[96:99]\n"
  "v_exp_f32 v67, v67\n"
  "v_mfma_f32_16x16x32_bf16 v[96:99], v[8:11], a[124:127], v[96:99]\n"
  "s_waitcnt lgkmcnt(4)\n"
  "v_cvt_pk_bf16_f32 v65, v66, v67\n"
  "v_fma_f32 v0, v0, s73, v223\n"
  "v_mfma_f32_16x16x32_bf16 v[4:7], a[136:139], a[0:3], v[248:251]\n"
  "v_exp_f32 v0, v0\n"
  "v_mfma_f32_16x16x32_bf16 v[4:7], a[140:143], a[4:7], v[4:7]\n"
  "s_waitcnt lgkmcnt(2)\n"
  "v_fma_f32 v1, v1, s73, v223\n"
  "v_fma_f32 v2, v2, s73, v223\n"
  "v_mfma_f32_16x16x32_bf16 v[4:7], a[144:147], a[8:11], v[4:7]\n"
  "v_exp_f32 v1, v1\n"
  "v_mfma_f32_16x16x32_bf16 v[4:7], a[148:151], a[12:15], v[4:7]\n"
  "s_waitcnt lgkmcnt(0)\n"
  "v_exp_f32 v2, v2\n"
  "v_mfma_f32_16x16x32_bf16 v[4:7], a[128:131], a[16:19], v[4:7]\n"
  "v_fma_f32 v3, v3, s73, v223\n"
  "v_add_f32 v252, v252, v0\n"
  "v_mfma_f32_16x16x32_bf16 v[4:7], a[132:135], a[20:23], v[4:7]\n"
  "ds_write_b64 v231, v[64:65] offset:2048\n"
  "v_mfma_f32_16x16x32_bf16 v[20:23], a[132:135], a[44:47], v[248:251]\n"
  "v_exp_f32 v3, v3\n"
  "v_mfma_f32_16x16x32_bf16 v[20:23], a[128:131], a[40:43], v[20:23]\n"
  "v_add_f32 v252, v252, v1\n"
  "v_add_f32 v252, v252, v2\n"
  "v_mfma_f32_16x16x32_bf16 v[20:23], a[148:151], a[36:39], v[20:23]\n"
  "ds_read_b64_tr_b16 a[120:121], v219 offset:12368\n"
  "v_add_f32 v252, v252, v3\n"
  "v_mfma_f32_16x16x32_bf16 v[20:23], a[144:147], a[32:35], v[20:23]\n"
  "ds_read_b64_tr_b16 a[122:123], v220 offset:12368\n"
  "v_fma_f32 v16, v16, s73, v224\n"
  "v_mfma_f32_16x16x32_bf16 v[20:23], a[140:143], a[28:31], v[20:23]\n"
  "ds_read_b64_tr_b16 a[124:125], v219 offset:13392\n"
  "v_fma_f32 v17, v17, s73, v224\n"
  "v_mfma_f32_16x16x32_bf16 v[20:23], a[136:139], a[24:27], v[20:23]\n"
  "ds_read_b64_tr_b16 a[126:127], v220 offset:13392\n"
  "v_fma_f32 v18, v18, s73, v224\n"
  "v_mfma_f32_16x16x32_bf16 v[36:39], a[136:139], a[48:51], v[248:251]\n"
  "v_exp_f32 v16, v16\n"
  "v_mfma_f32_16x16x32_bf16 v[36:39], a[140:143], a[52:55], v[36:39]\n"
  "v_exp_f32 v17, v17\n"
  "v_mfma_f32_16x16x32_bf16 v[36:39], a[144:147], a[56:59], v[36:39]\n"
  "v_exp_f32 v18, v18\n"
  "v_mfma_f32_16x16x32_bf16 v[36:39], a[148:151], a[60:63], v[36:39]\n"
  "v_fma_f32 v19, v19, s73, v224\n"
  "v_add_f32 v253, v253, v16\n"
  "v_mfma_f32_16x16x32_bf16 v[36:39], a[128:131], a[64:67], v[36:39]\n"
  "v_exp_f32 v19, v19\n"
  "v_mfma_f32_16x16x32_bf16 v[36:39], a[132:135], a[68:71], v[36:39]\n"
  "v_add_f32 v253, v253, v17\n"
  "v_add_f32 v253, v253, v18\n"
  "v_mfma_f32_16x16x32_bf16 v[52:55], a[132:135], a[92:95], v[248:251]\n"
  "ds_read_b64_tr_b16 a[132:133], v217 offset:14400\n"
  "v_add_f32 v253, v253, v19\n"
  "v_mfma_f32_16x16x32_bf16 v[52:55], a[128:131], a[88:91], v[52:55]\n"
  "ds_read_b64_tr_b16 a[134:135], v218 offset:14400\n"
  "v_fma_f32 v32, v32, s73, v225\n"
  "v_mfma_f32_16x16x32_bf16 v[52:55], a[148:151], a[84:87], v[52:55]\n"
  "ds_read_b64_tr_b16 a[128:129], v217 offset:14416\n"
  "v_fma_f32 v33, v33, s73, v225\n"
  "v_mfma_f32_16x16x32_bf16 v[52:55], a[144:147], a[80:83], v[52:55]\n"
  "ds_read_b64_tr_b16 a[130:131], v218 offset:14416\n"
  "v_fma_f32 v34, v34, s73, v225\n"
  "v_mfma_f32_16x16x32_bf16 v[52:55], a[140:143], a[76:79], v[52:55]\n"
  "v_exp_f32 v32, v32\n"
  "v_mfma_f32_16x16x32_bf16 v[52:55], a[136:139], a[72:75], v[52:55]\n"
  "s_waitcnt lgkmcnt(4)\n"
  "v_exp_f32 v33, v33\n"
  "v_mfma_f32_16x16x32_bf16 v[100:103], v[12:15], a[120:123], v[100:103]\n"
  "v_exp_f32 v34, v34\n"
  "v_mfma_f32_16x16x32_bf16 v[100:103], v[8:11], a[124:127], v[100:103]\n"
  "v_cvt_pk_bf16_f32 v12, v0, v1\n"
  "v_cvt_pk_bf16_f32 v13, v2, v3\n"
  "v_mfma_f32_16x16x32_bf16 v[132:135], v[28:31], a[120:123], v[132:135]\n"
  "ds_read_b64_tr_b16 a[140:141], v219 offset:14400\n"
  "v_cvt_pk_bf16_f32 v28, v16, v17\n"
  "v_mfma_f32_16x16x32_bf16 v[132:135], v[24:27], a[124:127], v[132:135]\n"
  "ds_read_b64_tr_b16 a[142:143], v220 offset:14400\n"
  "v_cvt_pk_bf16_f32 v29, v18, v19\n"
  "v_mfma_f32_16x16x32_bf16 v[164:167], v[44:47], a[120:123], v[164:167]\n"
  "ds_read_b64_tr_b16 a[136:137], v219 offset:14416\n"
  "v_fma_f32 v35, v35, s73, v225\n"
  "v_mfma_f32_16x16x32_bf16 v[164:167], v[40:43], a[124:127], v[164:167]\n"
  "ds_read_b64_tr_b16 a[138:139], v220 offset:14416\n"
  "v_add_f32 v254, v254, v32\n"
  "v_mfma_f32_16x16x32_bf16 v[196:199], v[60:63], a[120:123], v[196:199]\n"
  "v_exp_f32 v35, v35\n"
  "v_mfma_f32_16x16x32_bf16 v[196:199], v[56:59], a[124:127], v[196:199]\n"
  "s_waitcnt lgkmcnt(4)\n"
  "v_add_f32 v254, v254, v33\n"
  "v_add_f32 v254, v254, v34\n"
  "v_mfma_f32_16x16x32_bf16 a[244:247], a[156:159], a[132:135], a[244:247]\n"
  "v_add_f32 v254, v254, v35\n"
  "v_cvt_pk_bf16_f32 v44, v32, v33\n"
  "v_mfma_f32_16x16x32_bf16 a[248:251], a[156:159], a[128:131], a[248:251]\n"
  "v_cvt_pk_bf16_f32 v45, v34, v35\n"
  "v_fma_f32 v48, v48, s73, v226\n"
  "v_mfma_f32_16x16x32_bf16 v[200:203], v[56:59], a[132:135], v[200:203]\n"
  "v_exp_f32 v48, v48\n"
  "v_mfma_f32_16x16x32_bf16 v[204:207], v[56:59], a[128:131], v[204:207]\n"
  "v_fma_f32 v49, v49, s73, v226\n"
  "v_fma_f32 v50, v50, s73, v226\n"
  "v_mfma_f32_16x16x32_bf16 v[168:171], v[40:43], a[132:135], v[168:171]\n"
  "v_exp_f32 v49, v49\n"
  "v_mfma_f32_16x16x32_bf16 v[172:175], v[40:43], a[128:131], v[172:175]\n"
  "s_waitcnt vmcnt(10) lgkmcnt(0)\n"
  "s_barrier\n"
  "ds_read_b128 a[120:123], v216 offset:24704\n"
  "v_mfma_f32_16x16x32_bf16 v[136:139], v[24:27], a[132:135], v[136:139]\n"
  "ds_read_b64 a[160:161], v231 offset:2048\n"
  "v_fma_f32 v51, v51, s73, v226\n"
  "v_mfma_f32_16x16x32_bf16 v[140:143], v[24:27], a[128:131], v[140:143]\n"
  "ds_read_b64 a[162:163], v232 offset:2048\n"
  "v_add_f32 v255, v255, v48\n"
  "v_mfma_f32_16x16x32_bf16 v[104:107], v[8:11], a[132:135], v[104:107]\n"
  "ds_read_b128 a[124:127], v216 offset:24768\n"
  "v_add_f32 v255, v255, v49\n"
  "v_mfma_f32_16x16x32_bf16 v[108:111], v[8:11], a[128:131], v[108:111]\n"
  "ds_read_b128 a[132:135], v216 offset:25728\n"
  "v_cvt_pk_bf16_f32 v60, v48, v49\n"
  "v_mfma_f32_16x16x32_bf16 v[112:115], v[8:11], a[140:143], v[112:115]\n"
  "ds_read_b128 a[128:131], v216 offset:25792\n"
  "v_fma_f32 v52, v52, s73, v226\n"
  "v_mfma_f32_16x16x32_bf16 v[116:119], v[8:11], a[136:139], v[116:119]\n"
  "s_mov_b32 m0, s67\n"
  "ds_read_b128 a[148:151], v216 offset:26752\n"
  "v_fma_f32 v53, v53, s73, v226\n"
  "v_mfma_f32_16x16x32_bf16 v[144:147], v[24:27], a[140:143], v[144:147]\n"
  "ds_read_b128 a[144:147], v216 offset:26816\n"
  "v_fma_f32 v54, v54, s73, v226\n"
  "v_mfma_f32_16x16x32_bf16 v[148:151], v[24:27], a[136:139], v[148:151]\n"
  "v_exp_f32 v50, v50\n"
  "v_mfma_f32_16x16x32_bf16 v[176:179], v[40:43], a[140:143], v[176:179]\n"
  "v_exp_f32 v51, v51\n"
  "v_mfma_f32_16x16x32_bf16 v[180:183], v[40:43], a[136:139], v[180:183]\n"
  "buffer_load_dwordx4 v221, s[40:43], 0 offen offset:0 lds\n"
  "v_mfma_f32_16x16x32_bf16 v[208:211], v[56:59], a[140:143], v[208:211]\n"
  "s_addk_i32 m0, 896\n"
  "v_exp_f32 v52, v52\n"
  "v_mfma_f32_16x16x32_bf16 v[212:215], v[56:59], a[136:139], v[212:215]\n"
  "s_waitcnt lgkmcnt(4)\n"
  "v_exp_f32 v53, v53\n"
  "v_mfma_f32_16x16x32_bf16 v[64:67], a[120:123], a[96:99], v[248:251]\n"
  "v_exp_f32 v54, v54\n"
  "v_mfma_f32_16x16x32_bf16 v[64:67], a[124:127], a[100:103], v[64:67]\n"
  "s_waitcnt lgkmcnt(2)\n"
  "buffer_load_dwordx4 v221, s[40:43], 0 offen offset:128 lds\n"
  "v_add_f32 v255, v255, v50\n"
  "v_mfma_f32_16x16x32_bf16 v[64:67], a[132:135], a[104:107], v[64:67]\n"
  "s_addk_i32 m0, 896\n"
  "v_add_f32 v255, v255, v51\n"
  "v_cvt_pk_bf16_f32 v61, v50, v51\n"
  "v_mfma_f32_16x16x32_bf16 v[64:67], a[128:131], a[108:111], v[64:67]\n"
  "s_waitcnt lgkmcnt(0)\n"
  "v_fma_f32 v55, v55, s73, v226\n"
  "v_cvt_pk_bf16_f32 v62, v52, v53\n"
  "v_mfma_f32_16x16x32_bf16 v[64:67], a[148:151], a[112:115], v[64:67]\n"
  "v_exp_f32 v55, v55\n"
  "v_mfma_f32_16x16x32_bf16 v[64:67], a[144:147], a[116:119], v[64:67]\n"
  "buffer_load_dwordx4 v221, s[40:43], 0 offen offset:256 lds\n"
  "v_cvt_pk_bf16_f32 v63, v54, v55\n"
  "v_mfma_f32_16x16x32_bf16 v[0:3], a[144:147], a[20:23], v[248:251]\n"
  "v_fma_f32 v36, v36, s73, v225\n"
  "v_fma_f32 v37, v37, s73, v225\n"
  "v_mfma_f32_16x16x32_bf16 v[0:3], a[148:151], a[16:19], v[0:3]\n"
  "s_add_u32 s40, s40, s31\n"
  "v_exp_f32 v36, v36\n"
  "v_mfma_f32_16x16x32_bf16 v[0:3], a[128:131], a[12:15], v[0:3]\n"
  "s_addc_u32 s41, s41, 0\n"
  "v_exp_f32 v37, v37\n"
  "v_mfma_f32_16x16x32_bf16 v[0:3], a[132:135], a[8:11], v[0:3]\n"
  "s_mov_b32 m0, s59\n"
  "v_fma_f32 v38, v38, s73, v225\n"
  "v_fma_f32 v39, v39, s73, v225\n"
  "v_mfma_f32_16x16x32_bf16 v[0:3], a[124:127], a[4:7], v[0:3]\n"
  "v_exp_f32 v38, v38\n"
  "v_mfma_f32_16x16x32_bf16 v[0:3], a[120:123], a[0:3], v[0:3]\n"
  "v_exp_f32 v39, v39\n"
  "v_mfma_f32_16x16x32_bf16 v[16:19], a[120:123], a[24:27], v[248:251]\n"
  "v_cvt_pk_bf16_f32 v46, v36, v37\n"
  "v_cvt_pk_bf16_f32 v47, v38, v39\n"
  "v_mfma_f32_16x16x32_bf16 v[16:19], a[124:127], a[28:31], v[16:19]\n"
  "buffer_load_dwordx4 v221, s[32:35], 0 offen offset:0 lds\n"
  "v_fma_f32 v20, v20, s73, v224\n"
  "v_mfma_f32_16x16x32_bf16 v[16:19], a[132:135], a[32:35], v[16:19]\n"
  "s_addk_i32 m0, 1024\n"
  "ds_read_b64_tr_b16 a[140:141], v217 offset:24704\n"
  "v_fma_f32 v21, v21, s73, v224\n"
  "v_mfma_f32_16x16x32_bf16 v[16:19], a[128:131], a[36:39], v[16:19]\n"
  "s_mov_b64 s[32:33], s[44:45]\n"
  "ds_read_b64_tr_b16 a[142:143], v218 offset:24704\n"
  "v_fma_f32 v22, v22, s73, v224\n"
  "v_mfma_f32_16x16x32_bf16 v[16:19], a[148:151], a[40:43], v[16:19]\n"
  "ds_read_b64_tr_b16 a[136:137], v217 offset:25728\n"
  "v_fma_f32 v23, v23, s73, v224\n"
  "v_mfma_f32_16x16x32_bf16 v[16:19], a[144:147], a[44:47], v[16:19]\n"
  "buffer_load_dwordx4 v221, s[44:47], 0 offen offset:0 lds\n"
  "v_fma_f32 v4, v4, s73, v223\n"
  "v_mfma_f32_16x16x32_bf16 v[32:35], a[144:147], a[68:71], v[248:251]\n"
  "s_addk_i32 m0, 768\n"
  "ds_read_b64_tr_b16 a[138:139], v218 offset:25728\n"
  "v_fma_f32 v5, v5, s73, v223\n"
  "v_mfma_f32_16x16x32_bf16 v[32:35], a[148:151], a[64:67], v[32:35]\n"
  "v_exp_f32 v20, v20\n"
  "v_mfma_f32_16x16x32_bf16 v[32:35], a[128:131], a[60:63], v[32:35]\n"
  "v_exp_f32 v21, v21\n"
  "v_mfma_f32_16x16x32_bf16 v[32:35], a[132:135], a[56:59], v[32:35]\n"
  "buffer_load_dwordx4 v221, s[44:47], 0 offen offset:256 lds\n"
  "v_cvt_pk_bf16_f32 v30, v20, v21\n"
  "v_mfma_f32_16x16x32_bf16 v[32:35], a[124:127], a[52:55], v[32:35]\n"
  "s_add_u32 s44, s44, s31\n"
  "v_exp_f32 v22, v22\n"
  "v_mfma_f32_16x16x32_bf16 v[32:35], a[120:123], a[48:51], v[32:35]\n"
  "s_addc_u32 s45, s45, 0\n"
  "v_exp_f32 v23, v23\n"
  "v_mfma_f32_16x16x32_bf16 v[48:51], a[120:123], a[72:75], v[248:251]\n"
  "v_exp_f32 v4, v4\n"
  "v_mfma_f32_16x16x32_bf16 v[48:51], a[124:127], a[76:79], v[48:51]\n"
  "v_exp_f32 v5, v5\n"
  "v_mfma_f32_16x16x32_bf16 v[48:51], a[132:135], a[80:83], v[48:51]\n"
  "ds_read_b64_tr_b16 a[120:121], v217 offset:24720\n"
  "v_cvt_pk_bf16_f32 v31, v22, v23\n"
  "v_mfma_f32_16x16x32_bf16 v[48:51], a[128:131], a[84:87], v[48:51]\n"
  "ds_read_b64_tr_b16 a[122:123], v218 offset:24720\n"
  "v_fma_f32 v6, v6, s73, v223\n"
  "v_mfma_f32_16x16x32_bf16 v[48:51], a[148:151], a[88:91], v[48:51]\n"
  "ds_read_b64_tr_b16 a[124:125], v217 offset:25744\n"
  "v_fma_f32 v7, v7, s73, v223\n"
  "v_mfma_f32_16x16x32_bf16 v[48:51], a[144:147], a[92:95], v[48:51]\n"
  "s_waitcnt lgkmcnt(3)\n"
  "ds_read_b64_tr_b16 a[126:127], v218 offset:25744\n"
  "v_cvt_pk_bf16_f32 v14, v4, v5\n"
  "v_mfma_f32_16x16x32_bf16 a[228:231], a[156:159], a[140:143], a[228:231]\n"
  "v_exp_f32 v6, v6\n"
  "v_mfma_f32_16x16x32_bf16 a[228:231], a[160:163], a[136:139], a[228:231]\n"
  "v_exp_f32 v7, v7\n"
  "v_mfma_f32_16x16x32_bf16 a[212:215], v[56:59], a[140:143], a[212:215]\n"
  "v_cvt_pk_bf16_f32 v15, v6, v7\n"
  "v_add_f32 v252, v252, v4\n"
  "v_mfma_f32_16x16x32_bf16 a[212:215], v[60:63], a[136:139], a[212:215]\n"
  "v_add_f32 v252, v252, v5\n"
  "v_add_f32 v252, v252, v6\n"
  "v_mfma_f32_16x16x32_bf16 a[196:199], v[40:43], a[140:143], a[196:199]\n"
  "v_add_f32 v252, v252, v7\n"
  "v_add_f32 v253, v253, v20\n"
  "v_mfma_f32_16x16x32_bf16 a[196:199], v[44:47], a[136:139], a[196:199]\n"
  "v_add_f32 v253, v253, v21\n"
  "v_add_f32 v253, v253, v22\n"
  "v_mfma_f32_16x16x32_bf16 a[180:183], v[24:27], a[140:143], a[180:183]\n"
  "ds_read_b64_tr_b16 a[132:133], v219 offset:24704\n"
  "v_add_f32 v253, v253, v23\n"
  "v_mfma_f32_16x16x32_bf16 a[180:183], v[28:31], a[136:139], a[180:183]\n"
  "ds_read_b64_tr_b16 a[134:135], v220 offset:24704\n"
  "v_add_f32 v254, v254, v36\n"
  "v_mfma_f32_16x16x32_bf16 a[164:167], v[8:11], a[140:143], a[164:167]\n"
  "ds_read_b64_tr_b16 a[128:129], v219 offset:25728\n"
  "v_add_f32 v254, v254, v37\n"
  "v_mfma_f32_16x16x32_bf16 a[164:167], v[12:15], a[136:139], a[164:167]\n"
  "ds_read_b64_tr_b16 a[130:131], v220 offset:25728\n"
  "v_add_f32 v254, v254, v38\n"
  "v_mfma_f32_16x16x32_bf16 v[84:87], a[156:159], a[152:155], v[84:87]\n"
  "v_add_f32 v254, v254, v39\n"
  "v_add_f32 v255, v255, v52\n"
  "v_mfma_f32_16x16x32_bf16 v[84:87], a[160:163], a[152:155], v[84:87]\n"
  "s_waitcnt lgkmcnt(4)\n"
  "v_add_f32 v255, v255, v53\n"
  "v_add_f32 v255, v255, v54\n"
  "v_mfma_f32_16x16x32_bf16 a[168:171], v[8:11], a[120:123], a[168:171]\n"
  "ds_read_b64_tr_b16 v[246:247], v230 offset:5632\n"
  "v_add_f32 v255, v255, v55\n"
  "v_mfma_f32_16x16x32_bf16 a[168:171], v[12:15], a[124:127], a[168:171]\n"
  "v_fma_f32 v64, v64, s73, v227\n"
  "v_fma_f32 v65, v65, s73, v227\n"
  "v_mfma_f32_16x16x32_bf16 a[184:187], v[24:27], a[120:123], a[184:187]\n"
  "ds_read_b128 a[140:143], v235 offset:24704\n"
  "v_fma_f32 v66, v66, s73, v227\n"
  "v_mfma_f32_16x16x32_bf16 a[184:187], v[28:31], a[124:127], a[184:187]\n"
  "ds_read_b128 a[136:139], v235 offset:24768\n"
  "v_fma_f32 v67, v67, s73, v227\n"
  "v_mfma_f32_16x16x32_bf16 a[200:203], v[40:43], a[120:123], a[200:203]\n"
  "ds_read_b128 a[148:151], v235 offset:25728\n"
  "v_fma_f32 v0, v0, s73, v223\n"
  "v_mfma_f32_16x16x32_bf16 a[200:203], v[44:47], a[124:127], a[200:203]\n"
  "ds_read_b128 a[144:147], v235 offset:25792\n"
  "v_fma_f32 v1, v1, s73, v223\n"
  "v_mfma_f32_16x16x32_bf16 a[216:219], v[56:59], a[120:123], a[216:219]\n"
  "v_exp_f32 v64, v64\n"
  "v_mfma_f32_16x16x32_bf16 a[216:219], v[60:63], a[124:127], a[216:219]\n"
  "v_exp_f32 v65, v65\n"
  "v_mfma_f32_16x16x32_bf16 a[232:235], a[156:159], a[120:123], a[232:235]\n"
  "ds_read_b128 a[120:123], v235 offset:26752\n"
  "v_cvt_pk_bf16_f32 v64, v64, v65\n"
  "v_mfma_f32_16x16x32_bf16 a[232:235], a[160:163], a[124:127], a[232:235]\n"
  "s_waitcnt lgkmcnt(6)\n"
  "ds_read_b128 a[124:127], v235 offset:26816\n"
  "v_fma_f32 v2, v2, s73, v223\n"
  "v_mfma_f32_16x16x32_bf16 a[220:223], v[56:59], a[132:135], a[220:223]\n"
  "v_exp_f32 v66, v66\n"
  "v_mfma_f32_16x16x32_bf16 a[220:223], v[60:63], a[128:131], a[220:223]\n"
  "v_exp_f32 v67, v67\n"
  "v_mfma_f32_16x16x32_bf16 a[204:207], v[40:43], a[132:135], a[204:207]\n"
  "v_exp_f32 v0, v0\n"
  "v_mfma_f32_16x16x32_bf16 a[204:207], v[44:47], a[128:131], a[204:207]\n"
  "buffer_store_dwordx4 a[252:255], v228, s[52:55], s82 offen offset:0 nt\n"
  "v_cvt_pk_bf16_f32 v65, v66, v67\n"
  "v_exp_f32 v1, v1\n"
  "v_mfma_f32_16x16x32_bf16 a[188:191], v[24:27], a[132:135], a[188:191]\n"
  "ds_read_b64_tr_b16 a[252:253], v230 offset:6720\n"
  "ds_read_b64_tr_b16 a[254:255], v230 offset:6752\n"
  "v_mfma_f32_16x16x32_bf16 a[188:191], v[28:31], a[128:131], a[188:191]\n"
  "ds_write_b64 v231, v[64:65] offset:0\n"
  "v_mfma_f32_16x16x32_bf16 a[172:175], v[8:11], a[132:135], a[172:175]\n"
  "v_exp_f32 v2, v2\n"
  "v_mfma_f32_16x16x32_bf16 a[172:175], v[12:15], a[128:131], a[172:175]\n"
  "s_waitcnt lgkmcnt(7)\n"
  "v_fma_f32 v3, v3, s73, v223\n"
  "v_add_f32 v252, v252, v0\n"
  "v_mfma_f32_16x16x32_bf16 v[4:7], a[140:143], a[0:3], v[248:251]\n"
  "v_exp_f32 v3, v3\n"
  "v_mfma_f32_16x16x32_bf16 v[4:7], a[136:139], a[4:7], v[4:7]\n"
  "s_waitcnt lgkmcnt(5)\n"
  "v_add_f32 v252, v252, v1\n"
  "v_add_f32 v252, v252, v2\n"
  "v_mfma_f32_16x16x32_bf16 v[4:7], a[148:151], a[8:11], v[4:7]\n"
  "v_add_f32 v252, v252, v3\n"
  "v_fma_f32 v16, v16, s73, v224\n"
  "v_mfma_f32_16x16x32_bf16 v[4:7], a[144:147], a[12:15], v[4:7]\n"
  "s_waitcnt lgkmcnt(3)\n"
  "v_exp_f32_e64 v16, v16\n"
  "v_mfma_f32_16x16x32_bf16 v[4:7], a[120:123], a[16:19], v[4:7]\n"
  "v_fma_f32 v17, v17, s73, v224\n"
  "v_fma_f32 v18, v18, s73, v224\n"
  "v_mfma_f32_16x16x32_bf16 v[4:7], a[124:127], a[20:23], v[4:7]\n"
  "buffer_store_dwordx4 v[244:247], v228, s[52:55], s82 offen offset:3072 nt\n"
  "v_exp_f32 v17, v17\n"
  "v_fma_f32 v19, v19, s73, v224\n"
  "v_mfma_f32_16x16x32_bf16 v[20:23], a[124:127], a[44:47], v[248:251]\n"
  "v_exp_f32 v18, v18\n"
  "v_mfma_f32_16x16x32_bf16 v[20:23], a[120:123], a[40:43], v[20:23]\n"
  "v_exp_f32 v19, v19\n"
  "v_mfma_f32_16x16x32_bf16 v[20:23], a[144:147], a[36:39], v[20:23]\n"
  "ds_read_b64_tr_b16 a[132:133], v219 offset:24720\n"
  "v_add_f32 v253, v253, v16\n"
  "v_mfma_f32_16x16x32_bf16 v[20:23], a[148:151], a[32:35], v[20:23]\n"
  "ds_read_b64_tr_b16 a[134:135], v220 offset:24720\n"
  "v_add_f32 v253, v253, v17\n"
  "v_mfma_f32_16x16x32_bf16 v[20:23], a[136:139], a[28:31], v[20:23]\n"
  "ds_read_b64_tr_b16 a[128:129], v219 offset:25744\n"
  "v_add_f32_e64 v253, v253, v18\n"
  "v_mfma_f32_16x16x32_bf16 v[20:23], a[140:143], a[24:27], v[20:23]\n"
  "s_waitcnt lgkmcnt(4)\n"
  "buffer_store_dwordx4 a[252:255], v228, s[52:55], s83 offen offset:0 nt\n"
  "ds_read_b64_tr_b16 a[130:131], v220 offset:25744\n"
  "v_add_f32 v253, v253, v19\n"
  "v_fma_f32 v32, v32, s73, v225\n"
  "v_mfma_f32_16x16x32_bf16 v[36:39], a[140:143], a[48:51], v[248:251]\n"
  "v_exp_f32 v32, v32\n"
  "v_mfma_f32_16x16x32_bf16 v[36:39], a[136:139], a[52:55], v[36:39]\n"
  "v_fma_f32 v33, v33, s73, v225\n"
  "v_fma_f32 v34, v34, s73, v225\n"
  "v_mfma_f32_16x16x32_bf16 v[36:39], a[148:151], a[56:59], v[36:39]\n"
  "v_exp_f32 v33, v33\n"
  "v_mfma_f32_16x16x32_bf16 v[36:39], a[144:147], a[60:63], v[36:39]\n"
  "v_exp_f32 v34, v34\n"
  "v_mfma_f32_16x16x32_bf16 v[36:39], a[120:123], a[64:67], v[36:39]\n"
  "v_fma_f32 v35, v35, s73, v225\n"
  "v_add_f32 v254, v254, v32\n"
  "v_mfma_f32_16x16x32_bf16 v[36:39], a[124:127], a[68:71], v[36:39]\n"
  "v_exp_f32 v35, v35\n"
  "v_mfma_f32_16x16x32_bf16 v[52:55], a[124:127], a[92:95], v[248:251]\n"
  "ds_read_b64_tr_b16 a[124:125], v217 offset:26752\n"
  "v_add_f32 v254, v254, v33\n"
  "v_mfma_f32_16x16x32_bf16 v[52:55], a[120:123], a[88:91], v[52:55]\n"
  "ds_read_b64_tr_b16 a[126:127], v218 offset:26752\n"
  "v_add_f32 v254, v254, v34\n"
  "v_mfma_f32_16x16x32_bf16 v[52:55], a[144:147], a[84:87], v[52:55]\n"
  "ds_read_b64_tr_b16 a[120:121], v217 offset:26768\n"
  "v_add_f32 v254, v254, v35\n"
  "v_mfma_f32_16x16x32_bf16 v[52:55], a[148:151], a[80:83], v[52:55]\n"
  "ds_read_b64_tr_b16 a[122:123], v218 offset:26768\n"
  "v_fma_f32 v48, v48, s73, v226\n"
  "v_mfma_f32_16x16x32_bf16 v[52:55], a[136:139], a[76:79], v[52:55]\n"
  "v_exp_f32 v48, v48\n"
  "v_mfma_f32_16x16x32_bf16 v[52:55], a[140:143], a[72:75], v[52:55]\n"
  "s_waitcnt lgkmcnt(4)\n"
  "v_fma_f32 v49, v49, s73, v226\n"
  "v_fma_f32 v50, v50, s73, v226\n"
  "v_mfma_f32_16x16x32_bf16 a[176:179], v[8:11], a[132:135], a[176:179]\n"
  "v_exp_f32 v49, v49\n"
  "v_mfma_f32_16x16x32_bf16 a[176:179], v[12:15], a[128:131], a[176:179]\n"
  "v_exp_f32 v50, v50\n"
  "v_mfma_f32_16x16x32_bf16 a[192:195], v[24:27], a[132:135], a[192:195]\n"
  "ds_read_b64_tr_b16 a[136:137], v219 offset:26752\n"
  "v_cvt_pk_bf16_f32 v8, v0, v1\n"
  "v_mfma_f32_16x16x32_bf16 a[192:195], v[28:31], a[128:131], a[192:195]\n"
  "ds_read_b64_tr_b16 a[138:139], v220 offset:26752\n"
  "v_cvt_pk_bf16_f32 v9, v2, v3\n"
  "v_mfma_f32_16x16x32_bf16 a[208:211], v[40:43], a[132:135], a[208:211]\n"
  "ds_read_b64_tr_b16 a[140:141], v219 offset:26768\n"
  "v_cvt_pk_bf16_f32 v24, v16, v17\n"
  "v_mfma_f32_16x16x32_bf16 a[208:211], v[44:47], a[128:131], a[208:211]\n"
  "ds_read_b64_tr_b16 a[142:143], v220 offset:26768\n"
  "v_cvt_pk_bf16_f32 v25, v18, v19\n"
  "v_mfma_f32_16x16x32_bf16 a[224:227], v[56:59], a[132:135], a[224:227]\n"
  "v_cvt_pk_bf16_f32 v40, v32, v33\n"
  "v_cvt_pk_bf16_f32 v41, v34, v35\n"
  "v_mfma_f32_16x16x32_bf16 a[224:227], v[60:63], a[128:131], a[224:227]\n"
  "s_waitcnt lgkmcnt(4)\n"
  "v_fma_f32 v51, v51, s73, v226\n"
  "v_add_f32 v255, v255, v48\n"
  "v_mfma_f32_16x16x32_bf16 a[244:247], a[160:163], a[124:127], a[244:247]\n"
  "v_exp_f32 v51, v51\n"
  "v_mfma_f32_16x16x32_bf16 a[248:251], a[160:163], a[120:123], a[248:251]\n"
  "v_add_f32 v255, v255, v49\n"
  "v_add_f32 v255, v255, v50\n"
  "v_mfma_f32_16x16x32_bf16 v[200:203], v[60:63], a[124:127], v[200:203]\n"
  "v_add_f32 v255, v255, v51\n"
  "v_cvt_pk_bf16_f32 v56, v48, v49\n"
  "v_mfma_f32_16x16x32_bf16 v[204:207], v[60:63], a[120:123], v[204:207]\n"
  "v_cvt_pk_bf16_f32 v57, v50, v51\n"
  "v_fma_f32 v52, v52, s73, v226\n"
  "v_mfma_f32_16x16x32_bf16 v[168:171], v[44:47], a[124:127], v[168:171]\n"
  "v_exp_f32 v52, v52\n"
  "v_mfma_f32_16x16x32_bf16 v[172:175], v[44:47], a[120:123], v[172:175]\n"
  "s_waitcnt vmcnt(13) lgkmcnt(0)\n"
  "s_barrier\n"
  "ds_read_b128 a[132:135], v216 offset:0\n"
  "v_mfma_f32_16x16x32_bf16 v[136:139], v[28:31], a[124:127], v[136:139]\n"
  "ds_read_b64 a[156:157], v231 offset:0\n"
  "v_fma_f32 v53, v53, s73, v226\n"
  "v_mfma_f32_16x16x32_bf16 v[140:143], v[28:31], a[120:123], v[140:143]\n"
  "ds_read_b64 a[158:159], v232 offset:0\n"
  "v_fma_f32 v54, v54, s73, v226\n"
  "v_mfma_f32_16x16x32_bf16 v[104:107], v[12:15], a[124:127], v[104:107]\n"
  "ds_read_b128 a[128:131], v216 offset:64\n"
  "v_fma_f32 v55, v55, s73, v226\n"
  "v_mfma_f32_16x16x32_bf16 v[108:111], v[12:15], a[120:123], v[108:111]\n"
  "ds_read_b128 a[124:127], v216 offset:1024\n"
  "v_fma_f32 v36, v36, s73, v225\n"
  "v_mfma_f32_16x16x32_bf16 v[112:115], v[12:15], a[136:139], v[112:115]\n"
  "ds_read_b128 a[120:123], v216 offset:1088\n"
  "v_fma_f32 v37, v37, s73, v225\n"
  "v_mfma_f32_16x16x32_bf16 v[116:119], v[12:15], a[140:143], v[116:119]\n"
  "s_mov_b32 m0, s68\n"
  "ds_read_b128 a[144:147], v216 offset:2048\n"
  "v_fma_f32 v38, v38, s73, v225\n"
  "v_mfma_f32_16x16x32_bf16 v[144:147], v[28:31], a[136:139], v[144:147]\n"
  "ds_read_b128 a[148:151], v216 offset:2112\n"
  "v_fma_f32 v39, v39, s73, v225\n"
  "v_mfma_f32_16x16x32_bf16 v[148:151], v[28:31], a[140:143], v[148:151]\n"
  "v_exp_f32 v53, v53\n"
  "v_mfma_f32_16x16x32_bf16 v[176:179], v[44:47], a[136:139], v[176:179]\n"
  "v_exp_f32 v54, v54\n"
  "v_mfma_f32_16x16x32_bf16 v[180:183], v[44:47], a[140:143], v[180:183]\n"
  "buffer_load_dwordx4 v221, s[40:43], 0 offen offset:0 lds\n"
  "v_mfma_f32_16x16x32_bf16 v[208:211], v[60:63], a[136:139], v[208:211]\n"
  "s_addk_i32 m0, 896\n"
  "v_exp_f32 v55, v55\n"
  "v_mfma_f32_16x16x32_bf16 v[212:215], v[60:63], a[140:143], v[212:215]\n"
  "s_waitcnt lgkmcnt(4)\n"
  "v_exp_f32 v36, v36\n"
  "v_mfma_f32_16x16x32_bf16 v[64:67], a[132:135], a[96:99], v[248:251]\n"
  "v_exp_f32 v37, v37\n"
  "v_mfma_f32_16x16x32_bf16 v[64:67], a[128:131], a[100:103], v[64:67]\n"
  "s_waitcnt lgkmcnt(2)\n"
  "buffer_load_dwordx4 v221, s[40:43], 0 offen offset:128 lds\n"
  "v_cvt_pk_bf16_f32 v58, v52, v53\n"
  "v_mfma_f32_16x16x32_bf16 v[64:67], a[124:127], a[104:107], v[64:67]\n"
  "s_addk_i32 m0, 896\n"
  "v_exp_f32 v38, v38\n"
  "v_mfma_f32_16x16x32_bf16 v[64:67], a[120:123], a[108:111], v[64:67]\n"
  "s_waitcnt lgkmcnt(0)\n"
  "v_exp_f32 v39, v39\n"
  "v_mfma_f32_16x16x32_bf16 v[64:67], a[144:147], a[112:115], v[64:67]\n"
  "v_cvt_pk_bf16_f32 v59, v54, v55\n"
  "v_cvt_pk_bf16_f32 v42, v36, v37\n"
  "v_mfma_f32_16x16x32_bf16 v[64:67], a[148:151], a[116:119], v[64:67]\n"
  "buffer_load_dwordx4 v221, s[40:43], 0 offen offset:256 lds\n"
  "v_cvt_pk_bf16_f32 v43, v38, v39\n"
  "v_mfma_f32_16x16x32_bf16 v[0:3], a[148:151], a[20:23], v[248:251]\n"
  "v_fma_f32 v20, v20, s73, v224\n"
  "v_fma_f32 v21, v21, s73, v224\n"
  "v_mfma_f32_16x16x32_bf16 v[0:3], a[144:147], a[16:19], v[0:3]\n"
  "s_add_u32 s40, s40, s31\n"
  "v_exp_f32 v20, v20\n"
  "v_mfma_f32_16x16x32_bf16 v[0:3], a[120:123], a[12:15], v[0:3]\n"
  "s_addc_u32 s41, s41, 0\n"
  "v_exp_f32 v21, v21\n"
  "v_mfma_f32_16x16x32_bf16 v[0:3], a[124:127], a[8:11], v[0:3]\n"
  "s_add_u32 m0, s60, -128\n"
  "v_fma_f32 v22, v22, s73, v224\n"
  "v_add_f32 v253, v253, v20\n"
  "v_mfma_f32_16x16x32_bf16 v[0:3], a[128:131], a[4:7], v[0:3]\n"
  "v_exp_f32 v22, v22\n"
  "v_mfma_f32_16x16x32_bf16 v[0:3], a[132:135], a[0:3], v[0:3]\n"
  "v_fma_f32 v23, v23, s73, v224\n"
  "v_cvt_pk_bf16_f32 v26, v20, v21\n"
  "v_mfma_f32_16x16x32_bf16 v[16:19], a[132:135], a[24:27], v[248:251]\n"
  "v_exp_f32 v23, v23\n"
  "v_mfma_f32_16x16x32_bf16 v[16:19], a[128:131], a[28:31], v[16:19]\n"
  "buffer_load_dwordx4 v221, s[32:35], 0 offen offset:128 lds\n"
  "v_cvt_pk_bf16_f32 v27, v22, v23\n"
  "v_mfma_f32_16x16x32_bf16 v[16:19], a[124:127], a[32:35], v[16:19]\n"
  "s_addk_i32 m0, 1024\n"
  "ds_read_b64_tr_b16 a[136:137], v217 offset:0\n"
  "v_fma_f32 v4, v4, s73, v223\n"
  "v_mfma_f32_16x16x32_bf16 v[16:19], a[120:123], a[36:39], v[16:19]\n"
  "s_mov_b64 s[32:33], s[44:45]\n"
  "ds_read_b64_tr_b16 a[138:139], v218 offset:0\n"
  "v_fma_f32 v5, v5, s73, v223\n"
  "v_mfma_f32_16x16x32_bf16 v[16:19], a[144:147], a[40:43], v[16:19]\n"
  "ds_read_b64_tr_b16 a[140:141], v217 offset:1024\n"
  "v_fma_f32 v6, v6, s73, v223\n"
  "v_mfma_f32_16x16x32_bf16 v[16:19], a[148:151], a[44:47], v[16:19]\n"
  "buffer_load_dwordx4 v221, s[44:47], 0 offen offset:128 lds\n"
  "v_fma_f32 v7, v7, s73, v223\n"
  "v_mfma_f32_16x16x32_bf16 v[32:35], a[148:151], a[68:71], v[248:251]\n"
  "s_addk_i32 m0, 896\n"
  "ds_read_b64_tr_b16 a[142:143], v218 offset:1024\n"
  "v_add_f32 v253, v253, v21\n"
  "v_mfma_f32_16x16x32_bf16 v[32:35], a[144:147], a[64:67], v[32:35]\n"
  "v_exp_f32 v4, v4\n"
  "v_mfma_f32_16x16x32_bf16 v[32:35], a[120:123], a[60:63], v[32:35]\n"
  "v_exp_f32 v5, v5\n"
  "v_mfma_f32_16x16x32_bf16 v[32:35], a[124:127], a[56:59], v[32:35]\n"
  "buffer_load_dwordx4 v221, s[44:47], 0 offen offset:256 lds\n"
  "v_cvt_pk_bf16_f32 v10, v4, v5\n"
  "v_mfma_f32_16x16x32_bf16 v[32:35], a[128:131], a[52:55], v[32:35]\n"
  "s_add_u32 s44, s44, s31\n"
  "v_exp_f32 v6, v6\n"
  "v_mfma_f32_16x16x32_bf16 v[32:35], a[132:135], a[48:51], v[32:35]\n"
  "s_addc_u32 s45, s45, 0\n"
  "v_exp_f32 v7, v7\n"
  "v_mfma_f32_16x16x32_bf16 v[48:51], a[132:135], a[72:75], v[248:251]\n"
  "ds_read_b64_tr_b16 a[132:133], v217 offset:16\n"
  "v_cvt_pk_bf16_f32 v11, v6, v7\n"
  "v_mfma_f32_16x16x32_bf16 v[48:51], a[128:131], a[76:79], v[48:51]\n"
  "ds_read_b64_tr_b16 a[134:135], v218 offset:16\n"
  "v_add_f32 v252, v252, v4\n"
  "v_mfma_f32_16x16x32_bf16 v[48:51], a[124:127], a[80:83], v[48:51]\n"
  "ds_read_b64_tr_b16 a[128:129], v217 offset:1040\n"
  "v_add_f32 v252, v252, v5\n"
  "v_mfma_f32_16x16x32_bf16 v[48:51], a[120:123], a[84:87], v[48:51]\n"
  "s_add_u32 m0, s72, 4224\n"
  "ds_read_b64_tr_b16 a[130:131], v218 offset:1040\n"
  "v_add_f32 v252, v252, v6\n"
  "v_mfma_f32_16x16x32_bf16 v[48:51], a[144:147], a[88:91], v[48:51]\n"
  "v_add_f32 v252, v252, v7\n"
  "v_add_f32 v253, v253, v22\n"
  "v_mfma_f32_16x16x32_bf16 v[48:51], a[148:151], a[92:95], v[48:51]\n"
  "s_waitcnt lgkmcnt(4)\n"
  "v_add_f32 v253, v253, v23\n"
  "v_add_f32 v254, v254, v36\n"
  "v_mfma_f32_16x16x32_bf16 a[236:239], a[160:163], a[136:139], a[236:239]\n"
  "v_add_f32 v254, v254, v37\n"
  "v_add_f32 v254, v254, v38\n"
  "v_mfma_f32_16x16x32_bf16 a[236:239], a[156:159], a[140:143], a[236:239]\n"
  "buffer_load_dwordx4 v221, s[48:51], 0 offen offset:256 nt lds\n"
  "v_add_f32 v254, v254, v39\n"
  "v_mfma_f32_16x16x32_bf16 v[184:187], v[60:63], a[136:139], v[184:187]\n"
  "v_add_f32 v255, v255, v52\n"
  "v_add_f32 v255, v255, v53\n"
  "v_mfma_f32_16x16x32_bf16 v[184:187], v[56:59], a[140:143], v[184:187]\n"
  "v_add_f32 v255, v255, v54\n"
  "v_add_f32 v255, v255, v55\n"
  "v_mfma_f32_16x16x32_bf16 v[152:155], v[44:47], a[136:139], v[152:155]\n"
  "ds_read_b64_tr_b16 a[124:125], v219 offset:0\n"
  "v_fma_f32 v64, v64, s73, v227\n"
  "v_mfma_f32_16x16x32_bf16 v[152:155], v[40:43], a[140:143], v[152:155]\n"
  "s_add_u32 m0, s72, 2240\n"
  "ds_read_b64_tr_b16 a[126:127], v220 offset:0\n"
  "v_mfma_f32_16x16x32_bf16 v[120:123], v[28:31], a[136:139], v[120:123]\n"
  "ds_read_b64_tr_b16 a[120:121], v219 offset:1024\n"
  "v_fma_f32 v65, v65, s73, v227\n"
  "v_mfma_f32_16x16x32_bf16 v[120:123], v[24:27], a[140:143], v[120:123]\n"
  "ds_read_b64_tr_b16 a[122:123], v220 offset:1024\n"
  "v_fma_f32 v66, v66, s73, v227\n"
  "v_mfma_f32_16x16x32_bf16 v[88:91], v[12:15], a[136:139], v[88:91]\n"
  "v_exp_f32 v64, v64\n"
  "v_mfma_f32_16x16x32_bf16 v[88:91], v[8:11], a[140:143], v[88:91]\n"
  "s_waitcnt lgkmcnt(4)\n"
  "buffer_load_dwordx4 v221, s[48:51], 0 offen offset:3328 nt lds\n"
  "v_fma_f32 v67, v67, s73, v227\n"
  "v_mfma_f32_16x16x32_bf16 v[92:95], v[12:15], a[132:135], v[92:95]\n"
  "v_exp_f32 v65, v65\n"
  "v_mfma_f32_16x16x32_bf16 v[92:95], v[8:11], a[128:131], v[92:95]\n"
  "v_exp_f32 v66, v66\n"
  "v_mfma_f32_16x16x32_bf16 v[124:127], v[28:31], a[132:135], v[124:127]\n"
  "ds_read_b128 a[136:139], v235 offset:0\n"
  "v_cvt_pk_bf16_f32 v64, v64, v65\n"
  "v_mfma_f32_16x16x32_bf16 v[124:127], v[24:27], a[128:131], v[124:127]\n"
  "ds_read_b128 a[140:143], v235 offset:64\n"
  "v_fma_f32 v0, v0, s73, v223\n"
  "v_mfma_f32_16x16x32_bf16 v[156:159], v[44:47], a[132:135], v[156:159]\n"
  "ds_read_b128 a[144:147], v235 offset:1024\n"
  "v_fma_f32 v1, v1, s73, v223\n"
  "v_mfma_f32_16x16x32_bf16 v[156:159], v[40:43], a[128:131], v[156:159]\n"
  "ds_read_b128 a[148:151], v235 offset:1088\n"
  "v_fma_f32 v2, v2, s73, v223\n"
  "v_mfma_f32_16x16x32_bf16 v[188:191], v[60:63], a[132:135], v[188:191]\n"
  "v_exp_f32 v67, v67\n"
  "v_mfma_f32_16x16x32_bf16 v[188:191], v[56:59], a[128:131], v[188:191]\n"
  "v_exp_f32 v0, v0\n"
  "v_mfma_f32_16x16x32_bf16 a[240:243], a[160:163], a[132:135], a[240:243]\n"
  "ds_read_b128 a[132:135], v235 offset:2048\n"
  "v_cvt_pk_bf16_f32 v65, v66, v67\n"
  "v_mfma_f32_16x16x32_bf16 a[240:243], a[156:159], a[128:131], a[240:243]\n"
  "s_waitcnt lgkmcnt(5)\n"
  "ds_read_b128 a[128:131], v235 offset:2112\n"
  "v_fma_f32 v3, v3, s73, v223\n"
  "v_mfma_f32_16x16x32_bf16 v[192:195], v[60:63], a[124:127], v[192:195]\n"
  "v_exp_f32_e64 v1, v1\n"
  "v_mfma_f32_16x16x32_bf16 v[192:195], v[56:59], a[120:123], v[192:195]\n"
  "ds_write_b64 v231, v[64:65] offset:2048\n"
  "v_mfma_f32_16x16x32_bf16 v[160:163], v[44:47], a[124:127], v[160:163]\n"
  "v_exp_f32 v2, v2\n"
  "v_mfma_f32_16x16x32_bf16 v[160:163], v[40:43], a[120:123], v[160:163]\n"
  "v_exp_f32 v3, v3\n"
  "v_mfma_f32_16x16x32_bf16 v[128:131], v[28:31], a[124:127], v[128:131]\n"
  "v_add_f32 v252, v252, v0\n"
  "v_add_f32 v252, v252, v1\n"
  "v_mfma_f32_16x16x32_bf16 v[128:131], v[24:27], a[120:123], v[128:131]\n"
  "v_add_f32 v252, v252, v2\n"
  "v_add_f32 v252, v252, v3\n"
  "v_mfma_f32_16x16x32_bf16 v[96:99], v[12:15], a[124:127], v[96:99]\n"
  "v_fma_f32 v16, v16, s73, v224\n"
  "v_fma_f32 v17, v17, s73, v224\n"
  "v_mfma_f32_16x16x32_bf16 v[96:99], v[8:11], a[120:123], v[96:99]\n"
  "s_waitcnt lgkmcnt(5)\n"
  "v_exp_f32 v16, v16\n"
  "v_mfma_f32_16x16x32_bf16 v[4:7], a[136:139], a[0:3], v[248:251]\n"
  "v_exp_f32 v17, v17\n"
  "v_mfma_f32_16x16x32_bf16 v[4:7], a[140:143], a[4:7], v[4:7]\n"
  "s_waitcnt lgkmcnt(3)\n"
  "v_fma_f32 v18, v18, s73, v224\n"
  "v_fma_f32 v19, v19, s73, v224\n"
  "v_mfma_f32_16x16x32_bf16 v[4:7], a[144:147], a[8:11], v[4:7]\n"
  "v_exp_f32 v18, v18\n"
  "v_mfma_f32_16x16x32_bf16 v[4:7], a[148:151], a[12:15], v[4:7]\n"
  "s_waitcnt lgkmcnt(1)\n"
  "v_exp_f32 v19, v19\n"
  "v_mfma_f32_16x16x32_bf16 v[4:7], a[132:135], a[16:19], v[4:7]\n"
  "v_add_f32 v253, v253, v16\n"
  "v_add_f32 v253, v253, v17\n"
  "v_mfma_f32_16x16x32_bf16 v[4:7], a[128:131], a[20:23], v[4:7]\n"
  "v_add_f32 v253, v253, v18\n"
  "v_add_f32 v253, v253, v19\n"
  "v_mfma_f32_16x16x32_bf16 v[20:23], a[128:131], a[44:47], v[248:251]\n"
  "v_fma_f32 v32, v32, s73, v225\n"
  "v_fma_f32 v33, v33, s73, v225\n"
  "v_mfma_f32_16x16x32_bf16 v[20:23], a[132:135], a[40:43], v[20:23]\n"
  "v_exp_f32 v32, v32\n"
  "v_mfma_f32_16x16x32_bf16 v[20:23], a[148:151], a[36:39], v[20:23]\n"
  "ds_read_b64_tr_b16 a[124:125], v219 offset:16\n"
  "v_fma_f32 v34, v34, s73, v225\n"
  "v_mfma_f32_16x16x32_bf16 v[20:23], a[144:147], a[32:35], v[20:23]\n"
  "ds_read_b64_tr_b16 a[126:127], v220 offset:16\n"
  "v_fma_f32 v35, v35, s73, v225\n"
  "v_mfma_f32_16x16x32_bf16 v[20:23], a[140:143], a[28:31], v[20:23]\n"
  "ds_read_b64_tr_b16 a[120:121], v219 offset:1040\n"
  "v_add_f32 v254, v254, v32\n"
  "v_mfma_f32_16x16x32_bf16 v[20:23], a[136:139], a[24:27], v[20:23]\n"
  "ds_read_b64_tr_b16 a[122:123], v220 offset:1040\n"
  "v_fma_f32 v48, v48, s73, v226\n"
  "v_mfma_f32_16x16x32_bf16 v[36:39], a[136:139], a[48:51], v[248:251]\n"
  "v_exp_f32 v33, v33\n"
  "v_mfma_f32_16x16x32_bf16 v[36:39], a[140:143], a[52:55], v[36:39]\n"
  "v_exp_f32 v34, v34\n"
  "v_mfma_f32_16x16x32_bf16 v[36:39], a[144:147], a[56:59], v[36:39]\n"
  "v_exp_f32 v35, v35\n"
  "v_mfma_f32_16x16x32_bf16 v[36:39], a[148:151], a[60:63], v[36:39]\n"
  "v_exp_f32 v48, v48\n"
  "v_mfma_f32_16x16x32_bf16 v[36:39], a[132:135], a[64:67], v[36:39]\n"
  "v_add_f32 v254, v254, v33\n"
  "v_add_f32 v254, v254, v34\n"
  "v_mfma_f32_16x16x32_bf16 v[36:39], a[128:131], a[68:71], v[36:39]\n"
  "v_add_f32 v254, v254, v35\n"
  "v_fma_f32 v49, v49, s73, v226\n"
  "v_mfma_f32_16x16x32_bf16 v[52:55], a[128:131], a[92:95], v[248:251]\n"
  "ds_read_b64_tr_b16 a[128:129], v217 offset:2048\n"
  "v_fma_f32 v50, v50, s73, v226\n"
  "v_mfma_f32_16x16x32_bf16 v[52:55], a[132:135], a[88:91], v[52:55]\n"
  "ds_read_b64_tr_b16 a[130:131], v218 offset:2048\n"
  "v_fma_f32 v51, v51, s73, v226\n"
  "v_mfma_f32_16x16x32_bf16 v[52:55], a[148:151], a[84:87], v[52:55]\n"
  "ds_read_b64_tr_b16 a[132:133], v217 offset:2064\n"
  "v_add_f32 v255, v255, v48\n"
  "v_mfma_f32_16x16x32_bf16 v[52:55], a[144:147], a[80:83], v[52:55]\n"
  "ds_read_b64_tr_b16 a[134:135], v218 offset:2064\n"
  "v_fma_f32 v36, v36, s73, v225\n"
  "v_mfma_f32_16x16x32_bf16 v[52:55], a[140:143], a[76:79], v[52:55]\n"
  "v_exp_f32 v49, v49\n"
  "v_mfma_f32_16x16x32_bf16 v[52:55], a[136:139], a[72:75], v[52:55]\n"
  "s_waitcnt lgkmcnt(4)\n"
  "v_exp_f32 v50, v50\n"
  "v_mfma_f32_16x16x32_bf16 v[100:103], v[12:15], a[124:127], v[100:103]\n"
  "v_exp_f32 v51, v51\n"
  "v_mfma_f32_16x16x32_bf16 v[100:103], v[8:11], a[120:123], v[100:103]\n"
  "v_cvt_pk_bf16_f32 v12, v0, v1\n"
  "v_cvt_pk_bf16_f32 v13, v2, v3\n"
  "v_mfma_f32_16x16x32_bf16 v[132:135], v[28:31], a[124:127], v[132:135]\n"
  "ds_read_b64_tr_b16 a[140:141], v219 offset:2048\n"
  "v_cvt_pk_bf16_f32 v28, v16, v17\n"
  "v_mfma_f32_16x16x32_bf16 v[132:135], v[24:27], a[120:123], v[132:135]\n"
  "ds_read_b64_tr_b16 a[142:143], v220 offset:2048\n"
  "v_cvt_pk_bf16_f32 v29, v18, v19\n"
  "v_mfma_f32_16x16x32_bf16 v[164:167], v[44:47], a[124:127], v[164:167]\n"
  "ds_read_b64_tr_b16 a[136:137], v219 offset:2064\n"
  "v_cvt_pk_bf16_f32 v44, v32, v33\n"
  "v_mfma_f32_16x16x32_bf16 v[164:167], v[40:43], a[120:123], v[164:167]\n"
  "ds_read_b64_tr_b16 a[138:139], v220 offset:2064\n"
  "v_cvt_pk_bf16_f32 v45, v34, v35\n"
  "v_mfma_f32_16x16x32_bf16 v[196:199], v[60:63], a[124:127], v[196:199]\n"
  "v_exp_f32 v36, v36\n"
  "v_mfma_f32_16x16x32_bf16 v[196:199], v[56:59], a[120:123], v[196:199]\n"
  "s_waitcnt lgkmcnt(4)\n"
  "v_add_f32 v255, v255, v49\n"
  "v_add_f32 v255, v255, v50\n"
  "v_mfma_f32_16x16x32_bf16 a[244:247], a[156:159], a[128:131], a[244:247]\n"
  "v_add_f32 v255, v255, v51\n"
  "v_cvt_pk_bf16_f32 v60, v48, v49\n"
  "v_mfma_f32_16x16x32_bf16 a[248:251], a[156:159], a[132:135], a[248:251]\n"
  "v_cvt_pk_bf16_f32 v61, v50, v51\n"
  "v_fma_f32 v52, v52, s73, v226\n"
  "v_mfma_f32_16x16x32_bf16 v[200:203], v[56:59], a[128:131], v[200:203]\n"
  "v_exp_f32 v52, v52\n"
  "v_mfma_f32_16x16x32_bf16 v[204:207], v[56:59], a[132:135], v[204:207]\n"
  "v_fma_f32 v53, v53, s73, v226\n"
  "v_fma_f32 v54, v54, s73, v226\n"
  "v_mfma_f32_16x16x32_bf16 v[168:171], v[40:43], a[128:131], v[168:171]\n"
  "v_exp_f32 v53, v53\n"
  "v_mfma_f32_16x16x32_bf16 v[172:175], v[40:43], a[132:135], v[172:175]\n"
  "s_waitcnt vmcnt(11) lgkmcnt(0)\n"
  "s_barrier\n"
  "ds_read_b128 a[124:127], v216 offset:12352\n"
  "v_mfma_f32_16x16x32_bf16 v[136:139], v[24:27], a[128:131], v[136:139]\n"
  "ds_read_b64 a[160:161], v231 offset:2048\n"
  "v_fma_f32 v55, v55, s73, v226\n"
  "v_mfma_f32_16x16x32_bf16 v[140:143], v[24:27], a[132:135], v[140:143]\n"
  "ds_read_b64 a[162:163], v232 offset:2048\n"
  "v_cvt_pk_bf16_f32 v62, v52, v53\n"
  "v_mfma_f32_16x16x32_bf16 v[104:107], v[8:11], a[128:131], v[104:107]\n"
  "ds_read_b128 a[120:123], v216 offset:12416\n"
  "v_fma_f32 v37, v37, s73, v225\n"
  "v_mfma_f32_16x16x32_bf16 v[108:111], v[8:11], a[132:135], v[108:111]\n"
  "ds_read_b128 a[128:131], v216 offset:13376\n"
  "v_fma_f32 v38, v38, s73, v225\n"
  "v_mfma_f32_16x16x32_bf16 v[112:115], v[8:11], a[140:143], v[112:115]\n"
  "ds_read_b128 a[132:135], v216 offset:13440\n"
  "v_fma_f32 v39, v39, s73, v225\n"
  "v_mfma_f32_16x16x32_bf16 v[116:119], v[8:11], a[136:139], v[116:119]\n"
  "s_mov_b32 m0, s66\n"
  "ds_read_b128 a[148:151], v216 offset:14400\n"
  "v_fma_f32 v20, v20, s73, v224\n"
  "v_mfma_f32_16x16x32_bf16 v[144:147], v[24:27], a[140:143], v[144:147]\n"
  "ds_read_b128 a[144:147], v216 offset:14464\n"
  "v_fma_f32 v21, v21, s73, v224\n"
  "v_mfma_f32_16x16x32_bf16 v[148:151], v[24:27], a[136:139], v[148:151]\n"
  "v_exp_f32 v54, v54\n"
  "v_mfma_f32_16x16x32_bf16 v[176:179], v[40:43], a[140:143], v[176:179]\n"
  "v_exp_f32 v55, v55\n"
  "v_mfma_f32_16x16x32_bf16 v[180:183], v[40:43], a[136:139], v[180:183]\n"
  "buffer_load_dwordx4 v221, s[40:43], 0 offen offset:0 lds\n"
  "v_mfma_f32_16x16x32_bf16 v[208:211], v[56:59], a[140:143], v[208:211]\n"
  "s_addk_i32 m0, 896\n"
  "v_exp_f32 v37, v37\n"
  "v_mfma_f32_16x16x32_bf16 v[212:215], v[56:59], a[136:139], v[212:215]\n"
  "s_waitcnt lgkmcnt(4)\n"
  "v_exp_f32 v38, v38\n"
  "v_mfma_f32_16x16x32_bf16 v[64:67], a[124:127], a[96:99], v[248:251]\n"
  "v_exp_f32 v39, v39\n"
  "v_mfma_f32_16x16x32_bf16 v[64:67], a[120:123], a[100:103], v[64:67]\n"
  "s_waitcnt lgkmcnt(2)\n"
  "buffer_load_dwordx4 v221, s[40:43], 0 offen offset:128 lds\n"
  "v_cvt_pk_bf16_f32 v63, v54, v55\n"
  "v_mfma_f32_16x16x32_bf16 v[64:67], a[128:131], a[104:107], v[64:67]\n"
  "s_addk_i32 m0, 896\n"
  "v_exp_f32 v20, v20\n"
  "v_mfma_f32_16x16x32_bf16 v[64:67], a[132:135], a[108:111], v[64:67]\n"
  "s_waitcnt lgkmcnt(0)\n"
  "v_exp_f32 v21, v21\n"
  "v_mfma_f32_16x16x32_bf16 v[64:67], a[148:151], a[112:115], v[64:67]\n"
  "v_cvt_pk_bf16_f32 v46, v36, v37\n"
  "v_cvt_pk_bf16_f32 v47, v38, v39\n"
  "v_mfma_f32_16x16x32_bf16 v[64:67], a[144:147], a[116:119], v[64:67]\n"
  "buffer_load_dwordx4 v221, s[40:43], 0 offen offset:256 lds\n"
  "v_fma_f32 v22, v22, s73, v224\n"
  "v_mfma_f32_16x16x32_bf16 v[0:3], a[144:147], a[20:23], v[248:251]\n"
  "s_add_u32 s40, s40, s31\n"
  "v_exp_f32 v22, v22\n"
  "v_mfma_f32_16x16x32_bf16 v[0:3], a[148:151], a[16:19], v[0:3]\n"
  "s_addc_u32 s41, s41, 0\n"
  "v_mbcnt_lo_u32_b32 v236, -1, 0\n"
  "v_mbcnt_hi_u32_b32 v236, -1, v236\n"
  "v_mfma_f32_16x16x32_bf16 v[0:3], a[132:135], a[12:15], v[0:3]\n"
  "s_sub_u32 s79, s96, s39\n"
  "v_mul_u32_u24 v236, 0x18c, v236\n"
  "v_fma_f32 v23, v23, s73, v224\n"
  "v_mfma_f32_16x16x32_bf16 v[0:3], a[128:131], a[8:11], v[0:3]\n"
  "s_mov_b32 m0, s58\n"
  "v_exp_f32 v23, v23\n"
  "v_mfma_f32_16x16x32_bf16 v[0:3], a[120:123], a[4:7], v[0:3]\n"
  "v_cvt_pk_bf16_f32 v30, v20, v21\n"
  "v_cvt_pk_bf16_f32 v31, v22, v23\n"
  "v_mfma_f32_16x16x32_bf16 v[0:3], a[124:127], a[0:3], v[0:3]\n"
  "v_fma_f32 v4, v4, s73, v223\n"
  "v_fma_f32 v5, v5, s73, v223\n"
  "v_mfma_f32_16x16x32_bf16 v[16:19], a[124:127], a[24:27], v[248:251]\n"
  "v_exp_f32 v4, v4\n"
  "v_mfma_f32_16x16x32_bf16 v[16:19], a[120:123], a[28:31], v[16:19]\n"
  "buffer_load_dwordx4 v221, s[32:35], 0 offen offset:0 lds\n"
  "v_fma_f32 v6, v6, s73, v223\n"
  "v_mfma_f32_16x16x32_bf16 v[16:19], a[128:131], a[32:35], v[16:19]\n"
  "s_addk_i32 m0, 1024\n"
  "ds_read_b64_tr_b16 a[140:141], v217 offset:12352\n"
  "v_fma_f32 v7, v7, s73, v223\n"
  "v_mfma_f32_16x16x32_bf16 v[16:19], a[132:135], a[36:39], v[16:19]\n"
  "s_mov_b64 s[32:33], s[44:45]\n"
  "ds_read_b64_tr_b16 a[142:143], v218 offset:12352\n"
  "v_add_f32 v252, v252, v4\n"
  "v_mfma_f32_16x16x32_bf16 v[16:19], a[148:151], a[40:43], v[16:19]\n"
  "ds_read_b64_tr_b16 a[136:137], v217 offset:13376\n"
  "v_add_f32 v253, v253, v20\n"
  "v_mfma_f32_16x16x32_bf16 v[16:19], a[144:147], a[44:47], v[16:19]\n"
  "buffer_load_dwordx4 v221, s[44:47], 0 offen offset:0 lds\n"
  "v_add_f32 v253, v253, v21\n"
  "v_mfma_f32_16x16x32_bf16 v[32:35], a[144:147], a[68:71], v[248:251]\n"
  "s_addk_i32 m0, 768\n"
  "ds_read_b64_tr_b16 a[138:139], v218 offset:13376\n"
  "v_add_f32 v253, v253, v22\n"
  "v_mfma_f32_16x16x32_bf16 v[32:35], a[148:151], a[64:67], v[32:35]\n"
  "v_exp_f32 v5, v5\n"
  "v_mfma_f32_16x16x32_bf16 v[32:35], a[132:135], a[60:63], v[32:35]\n"
  "v_exp_f32 v6, v6\n"
  "v_mfma_f32_16x16x32_bf16 v[32:35], a[128:131], a[56:59], v[32:35]\n"
  "buffer_load_dwordx4 v221, s[44:47], 0 offen offset:256 lds\n"
  "v_cvt_pk_bf16_f32 v14, v4, v5\n"
  "v_mfma_f32_16x16x32_bf16 v[32:35], a[120:123], a[52:55], v[32:35]\n"
  "s_add_u32 s44, s44, s31\n"
  "v_exp_f32 v7, v7\n"
  "v_mfma_f32_16x16x32_bf16 v[32:35], a[124:127], a[48:51], v[32:35]\n"
  "s_addc_u32 s45, s45, 0\n"
  "v_cvt_pk_bf16_f32 v15, v6, v7\n"
  "v_add_f32 v252, v252, v5\n"
  "v_mfma_f32_16x16x32_bf16 v[48:51], a[124:127], a[72:75], v[248:251]\n"
  "s_mov_b32 m0, s29\n"
  "v_add_f32 v252, v252, v6\n"
  "v_add_f32 v252, v252, v7\n"
  "v_mfma_f32_16x16x32_bf16 v[48:51], a[120:123], a[76:79], v[48:51]\n"
  "v_add_f32 v253, v253, v23\n"
  "v_add_f32 v254, v254, v36\n"
  "v_mfma_f32_16x16x32_bf16 v[48:51], a[128:131], a[80:83], v[48:51]\n"
  "ds_read_b64_tr_b16 a[124:125], v217 offset:12368\n"
  "v_add_f32 v254, v254, v37\n"
  "v_mfma_f32_16x16x32_bf16 v[48:51], a[132:135], a[84:87], v[48:51]\n"
  "buffer_load_dword v236, s[88:91], s79 offen lds\n"
  "v_add_f32 v254, v254, v38\n"
  "v_mfma_f32_16x16x32_bf16 v[48:51], a[148:151], a[88:91], v[48:51]\n"
  "ds_read_b64_tr_b16 a[126:127], v218 offset:12368\n"
  "v_add_f32 v254, v254, v39\n"
  "v_mfma_f32_16x16x32_bf16 v[48:51], a[144:147], a[92:95], v[48:51]\n"
  "s_waitcnt lgkmcnt(2)\n"
  "ds_read_b64_tr_b16 a[120:121], v217 offset:13392\n"
  "v_add_f32 v255, v255, v52\n"
  "v_mfma_f32_16x16x32_bf16 a[228:231], a[156:159], a[140:143], a[228:231]\n"
  "ds_read_b64_tr_b16 a[122:123], v218 offset:13392\n"
  "v_add_f32 v255, v255, v53\n"
  "v_mfma_f32_16x16x32_bf16 a[228:231], a[160:163], a[136:139], a[228:231]\n"
  "v_add_f32 v255, v255, v54\n"
  "v_add_f32 v255, v255, v55\n"
  "v_mfma_f32_16x16x32_bf16 a[212:215], v[56:59], a[140:143], a[212:215]\n"
  "ds_read_b64_tr_b16 v[244:245], v230 offset:7840\n"
  "ds_read_b64_tr_b16 v[246:247], v230 offset:7872\n"
  "v_mfma_f32_16x16x32_bf16 a[212:215], v[60:63], a[136:139], a[212:215]\n"
  "ds_read_b64_tr_b16 a[252:253], v230 offset:17920\n"
  "ds_read_b64_tr_b16 a[254:255], v230 offset:17952\n"
  "v_mfma_f32_16x16x32_bf16 a[196:199], v[40:43], a[140:143], a[196:199]\n"
  "v_fma_f32 v64, v64, s73, v227\n"
  "v_fma_f32 v65, v65, s73, v227\n"
  "v_mfma_f32_16x16x32_bf16 a[196:199], v[44:47], a[136:139], a[196:199]\n"
  "v_exp_f32 v64, v64\n"
  "v_mfma_f32_16x16x32_bf16 a[180:183], v[24:27], a[140:143], a[180:183]\n"
  "ds_read_b64_tr_b16 a[128:129], v219 offset:12352\n"
  "v_fma_f32 v66, v66, s73, v227\n"
  "v_mfma_f32_16x16x32_bf16 a[180:183], v[28:31], a[136:139], a[180:183]\n"
  "ds_read_b64_tr_b16 a[130:131], v220 offset:12352\n"
  "v_fma_f32 v67, v67, s73, v227\n"
  "v_mfma_f32_16x16x32_bf16 a[164:167], v[8:11], a[140:143], a[164:167]\n"
  "ds_read_b64_tr_b16 a[132:133], v219 offset:13376\n"
  "v_fma_f32 v0, v0, s73, v223\n"
  "v_mfma_f32_16x16x32_bf16 a[164:167], v[12:15], a[136:139], a[164:167]\n"
  "ds_read_b64_tr_b16 a[134:135], v220 offset:13376\n"
  "v_fma_f32 v1, v1, s73, v223\n"
  "v_mfma_f32_16x16x32_bf16 v[84:87], a[156:159], a[152:155], v[84:87]\n"
  "v_exp_f32 v65, v65\n"
  "v_mfma_f32_16x16x32_bf16 v[84:87], a[160:163], a[152:155], v[84:87]\n"
  "s_waitcnt lgkmcnt(8)\n"
  "v_exp_f32 v66, v66\n"
  "v_mfma_f32_16x16x32_bf16 a[168:171], v[8:11], a[124:127], a[168:171]\n"
  "v_exp_f32 v67, v67\n"
  "v_mfma_f32_16x16x32_bf16 a[168:171], v[12:15], a[120:123], a[168:171]\n"
  "v_exp_f32 v0, v0\n"
  "v_mfma_f32_16x16x32_bf16 a[184:187], v[24:27], a[124:127], a[184:187]\n"
  "ds_read_b128 a[140:143], v235 offset:12352\n"
  "v_cvt_pk_bf16_f32 v64, v64, v65\n"
  "v_mfma_f32_16x16x32_bf16 a[184:187], v[28:31], a[120:123], a[184:187]\n"
  "ds_read_b128 a[136:139], v235 offset:12416\n"
  "v_cvt_pk_bf16_f32 v65, v66, v67\n"
  "v_mfma_f32_16x16x32_bf16 a[200:203], v[40:43], a[124:127], a[200:203]\n"
  "ds_read_b128 a[148:151], v235 offset:13376\n"
  "v_fma_f32 v2, v2, s73, v223\n"
  "v_mfma_f32_16x16x32_bf16 a[200:203], v[44:47], a[120:123], a[200:203]\n"
  "ds_read_b128 a[144:147], v235 offset:13440\n"
  "v_fma_f32 v3, v3, s73, v223\n"
  "v_mfma_f32_16x16x32_bf16 a[216:219], v[56:59], a[124:127], a[216:219]\n"
  "v_exp_f32 v1, v1\n"
  "v_mfma_f32_16x16x32_bf16 a[216:219], v[60:63], a[120:123], a[216:219]\n"
  "v_exp_f32 v2, v2\n"
  "v_mfma_f32_16x16x32_bf16 a[232:235], a[156:159], a[124:127], a[232:235]\n"
  "ds_read_b128 a[124:127], v235 offset:14400\n"
  "v_add_f32 v252, v252, v0\n"
  "v_mfma_f32_16x16x32_bf16 a[232:235], a[160:163], a[120:123], a[232:235]\n"
  "s_waitcnt lgkmcnt(5)\n"
  "ds_read_b128 a[120:123], v235 offset:14464\n"
  "v_add_f32 v252, v252, v1\n"
  "v_mfma_f32_16x16x32_bf16 a[220:223], v[56:59], a[128:131], a[220:223]\n"
  "v_exp_f32 v3, v3\n"
  "v_mfma_f32_16x16x32_bf16 a[220:223], v[60:63], a[132:135], a[220:223]\n"
  "ds_write_b64 v231, v[64:65] offset:0\n"
  "v_mfma_f32_16x16x32_bf16 a[204:207], v[40:43], a[128:131], a[204:207]\n"
  "v_add_f32 v252, v252, v2\n"
  "v_add_f32 v252, v252, v3\n"
  "v_mfma_f32_16x16x32_bf16 a[204:207], v[44:47], a[132:135], a[204:207]\n"
  "buffer_store_dwordx4 v[244:247], v228, s[52:55], s83 offen offset:3072 nt\n"
  "v_fma_f32 v16, v16, s73, v224\n"
  "v_exp_f32 v16, v16\n"
  "v_mfma_f32_16x16x32_bf16 a[188:191], v[24:27], a[128:131], a[188:191]\n"
  "ds_read_b64_tr_b16 v[244:245], v230 offset:19040\n"
  "ds_read_b64_tr_b16 v[246:247], v230 offset:19072\n"
  "v_mfma_f32_16x16x32_bf16 a[188:191], v[28:31], a[132:135], a[188:191]\n"
  "v_fma_f32 v17, v17, s73, v224\n"
  "v_fma_f32 v18, v18, s73, v224\n"
  "v_mfma_f32_16x16x32_bf16 a[172:175], v[8:11], a[128:131], a[172:175]\n"
  "v_exp_f32 v17, v17\n"
  "v_mfma_f32_16x16x32_bf16 a[172:175], v[12:15], a[132:135], a[172:175]\n"
  "s_waitcnt lgkmcnt(7)\n"
  "v_exp_f32 v18, v18\n"
  "v_mfma_f32_16x16x32_bf16 v[4:7], a[140:143], a[0:3], v[248:251]\n"
  "v_fma_f32 v19, v19, s73, v224\n"
  "v_add_f32 v253, v253, v16\n"
  "v_mfma_f32_16x16x32_bf16 v[4:7], a[136:139], a[4:7], v[4:7]\n"
  "s_waitcnt lgkmcnt(5)\n"
  "v_exp_f32 v19, v19\n"
  "v_mfma_f32_16x16x32_bf16 v[4:7], a[148:151], a[8:11], v[4:7]\n"
  "v_add_f32 v253, v253, v17\n"
  "v_add_f32 v253, v253, v18\n"
  "v_mfma_f32_16x16x32_bf16 v[4:7], a[144:147], a[12:15], v[4:7]\n"
  "s_waitcnt lgkmcnt(3)\n"
  "v_add_f32 v253, v253, v19\n"
  "v_fma_f32 v32, v32, s73, v225\n"
  "v_mfma_f32_16x16x32_bf16 v[4:7], a[124:127], a[16:19], v[4:7]\n"
  "v_exp_f32 v32, v32\n"
  "v_mfma_f32_16x16x32_bf16 v[4:7], a[120:123], a[20:23], v[4:7]\n"
  "buffer_store_dwordx4 a[252:255], v228, s[52:55], s56 offen offset:0 nt\n"
  "v_fma_f32 v33, v33, s73, v225\n"
  "v_exp_f32 v33, v33\n"
  "v_mfma_f32_16x16x32_bf16 v[20:23], a[120:123], a[44:47], v[248:251]\n"
  "v_fma_f32 v34, v34, s73, v225\n"
  "v_fma_f32 v35, v35, s73, v225\n"
  "v_mfma_f32_16x16x32_bf16 v[20:23], a[124:127], a[40:43], v[20:23]\n"
  "v_exp_f32 v34, v34\n"
  "v_mfma_f32_16x16x32_bf16 v[20:23], a[144:147], a[36:39], v[20:23]\n"
  "ds_read_b64_tr_b16 a[128:129], v219 offset:12368\n"
  "v_add_f32 v254, v254, v32\n"
  "v_mfma_f32_16x16x32_bf16 v[20:23], a[148:151], a[32:35], v[20:23]\n"
  "ds_read_b64_tr_b16 a[130:131], v220 offset:12368\n"
  "v_add_f32 v254, v254, v33\n"
  "v_mfma_f32_16x16x32_bf16 v[20:23], a[136:139], a[28:31], v[20:23]\n"
  "ds_read_b64_tr_b16 a[132:133], v219 offset:13392\n"
  "v_add_f32 v254, v254, v34\n"
  "v_mfma_f32_16x16x32_bf16 v[20:23], a[140:143], a[24:27], v[20:23]\n"
  "s_waitcnt lgkmcnt(3)\n"
  "buffer_store_dwordx4 v[244:247], v228, s[52:55], s56 offen offset:128 nt\n"
  "ds_read_b64_tr_b16 a[134:135], v220 offset:13392\n"
  "v_exp_f32 v35, v35\n"
  "v_mfma_f32_16x16x32_bf16 v[36:39], a[140:143], a[48:51], v[248:251]\n"
  "v_add_f32 v254, v254, v35\n"
  "v_fma_f32 v48, v48, s73, v226\n"
  "v_mfma_f32_16x16x32_bf16 v[36:39], a[136:139], a[52:55], v[36:39]\n"
  "v_exp_f32 v48, v48\n"
  "v_mfma_f32_16x16x32_bf16 v[36:39], a[148:151], a[56:59], v[36:39]\n"
  "v_fma_f32 v49, v49, s73, v226\n"
  "v_fma_f32 v50, v50, s73, v226\n"
  "v_mfma_f32_16x16x32_bf16 v[36:39], a[144:147], a[60:63], v[36:39]\n"
  "v_exp_f32 v49, v49\n"
  "v_mfma_f32_16x16x32_bf16 v[36:39], a[124:127], a[64:67], v[36:39]\n"
  "v_exp_f32 v50, v50\n"
  "v_mfma_f32_16x16x32_bf16 v[36:39], a[120:123], a[68:71], v[36:39]\n"
  "v_fma_f32 v51, v51, s73, v226\n"
  "v_add_f32 v255, v255, v48\n"
  "v_mfma_f32_16x16x32_bf16 v[52:55], a[120:123], a[92:95], v[248:251]\n"
  "ds_read_b64_tr_b16 a[120:121], v217 offset:14400\n"
  "v_add_f32 v255, v255, v49\n"
  "v_mfma_f32_16x16x32_bf16 v[52:55], a[124:127], a[88:91], v[52:55]\n"
  "ds_read_b64_tr_b16 a[122:123], v218 offset:14400\n"
  "v_add_f32 v255, v255, v50\n"
  "v_mfma_f32_16x16x32_bf16 v[52:55], a[144:147], a[84:87], v[52:55]\n"
  "ds_read_b64_tr_b16 a[124:125], v217 offset:14416\n"
  "v_fma_f32 v36, v36, s73, v225\n"
  "v_mfma_f32_16x16x32_bf16 v[52:55], a[148:151], a[80:83], v[52:55]\n"
  "ds_read_b64_tr_b16 a[126:127], v218 offset:14416\n"
  "v_fma_f32 v37, v37, s73, v225\n"
  "v_mfma_f32_16x16x32_bf16 v[52:55], a[136:139], a[76:79], v[52:55]\n"
  "v_exp_f32 v51, v51\n"
  "v_mfma_f32_16x16x32_bf16 v[52:55], a[140:143], a[72:75], v[52:55]\n"
  "s_waitcnt lgkmcnt(4)\n"
  "v_exp_f32 v36, v36\n"
  "v_mfma_f32_16x16x32_bf16 a[176:179], v[8:11], a[128:131], a[176:179]\n"
  "v_cvt_pk_bf16_f32 v8, v0, v1\n"
  "v_cvt_pk_bf16_f32 v9, v2, v3\n"
  "v_mfma_f32_16x16x32_bf16 a[176:179], v[12:15], a[132:135], a[176:179]\n"
  "v_exp_f32 v37, v37\n"
  "v_mfma_f32_16x16x32_bf16 a[192:195], v[24:27], a[128:131], a[192:195]\n"
  "ds_read_b64_tr_b16 a[136:137], v219 offset:14400\n"
  "v_cvt_pk_bf16_f32 v24, v16, v17\n"
  "v_mfma_f32_16x16x32_bf16 a[192:195], v[28:31], a[132:135], a[192:195]\n"
  "ds_read_b64_tr_b16 a[138:139], v220 offset:14400\n"
  "v_cvt_pk_bf16_f32 v25, v18, v19\n"
  "v_mfma_f32_16x16x32_bf16 a[208:211], v[40:43], a[128:131], a[208:211]\n"
  "ds_read_b64_tr_b16 a[140:141], v219 offset:14416\n"
  "v_cvt_pk_bf16_f32 v40, v32, v33\n"
  "v_mfma_f32_16x16x32_bf16 a[208:211], v[44:47], a[132:135], a[208:211]\n"
  "ds_read_b64_tr_b16 a[142:143], v220 offset:14416\n"
  "v_cvt_pk_bf16_f32 v41, v34, v35\n"
  "v_mfma_f32_16x16x32_bf16 a[224:227], v[56:59], a[128:131], a[224:227]\n"
  "v_add_f32 v255, v255, v51\n"
  "v_cvt_pk_bf16_f32 v56, v48, v49\n"
  "v_mfma_f32_16x16x32_bf16 a[224:227], v[60:63], a[132:135], a[224:227]\n"
  "s_waitcnt lgkmcnt(4)\n"
  "v_cvt_pk_bf16_f32 v57, v50, v51\n"
  "v_fma_f32 v52, v52, s73, v226\n"
  "v_mfma_f32_16x16x32_bf16 a[244:247], a[160:163], a[120:123], a[244:247]\n"
  "v_exp_f32 v52, v52\n"
  "v_mfma_f32_16x16x32_bf16 a[248:251], a[160:163], a[124:127], a[248:251]\n"
  "v_fma_f32 v53, v53, s73, v226\n"
  "v_fma_f32 v54, v54, s73, v226\n"
  "v_mfma_f32_16x16x32_bf16 v[200:203], v[60:63], a[120:123], v[200:203]\n"
  "v_exp_f32 v53, v53\n"
  "v_mfma_f32_16x16x32_bf16 v[204:207], v[60:63], a[124:127], v[204:207]\n"
  "v_exp_f32_e64 v54, v54\n"
  "v_mfma_f32_16x16x32_bf16 v[168:171], v[44:47], a[120:123], v[168:171]\n"
  "v_fma_f32 v55, v55, s73, v226\n"
  "v_cvt_pk_bf16_f32 v58, v52, v53\n"
  "v_mfma_f32_16x16x32_bf16 v[172:175], v[44:47], a[124:127], v[172:175]\n"
  "s_waitcnt vmcnt(12) lgkmcnt(0)\n"
  "s_barrier\n"
  "ds_read_b128 a[128:131], v216 offset:24704\n"
  "v_mfma_f32_16x16x32_bf16 v[136:139], v[28:31], a[120:123], v[136:139]\n"
  "ds_read_b64 a[156:157], v231 offset:0\n"
  "v_fma_f32 v38, v38, s73, v225\n"
  "v_mfma_f32_16x16x32_bf16 v[140:143], v[28:31], a[124:127], v[140:143]\n"
  "ds_read_b64 a[158:159], v232 offset:0\n"
  "v_fma_f32 v39, v39, s73, v225\n"
  "v_mfma_f32_16x16x32_bf16 v[104:107], v[12:15], a[120:123], v[104:107]\n"
  "ds_read_b128 a[132:135], v216 offset:24768\n"
  "v_cvt_pk_bf16_f32 v42, v36, v37\n"
  "v_mfma_f32_16x16x32_bf16 v[108:111], v[12:15], a[124:127], v[108:111]\n"
  "ds_read_b128 a[120:123], v216 offset:25728\n"
  "v_fma_f32 v20, v20, s73, v224\n"
  "v_mfma_f32_16x16x32_bf16 v[112:115], v[12:15], a[136:139], v[112:115]\n"
  "s_add_u32 s71, s92, s81\n"
  "ds_read_b128 a[124:127], v216 offset:25792\n"
  "v_fma_f32 v21, v21, s73, v224\n"
  "v_mfma_f32_16x16x32_bf16 v[116:119], v[12:15], a[140:143], v[116:119]\n"
  "s_mov_b32 m0, s67\n"
  "ds_read_b128 a[144:147], v216 offset:26752\n"
  "v_fma_f32 v22, v22, s73, v224\n"
  "v_mfma_f32_16x16x32_bf16 v[144:147], v[28:31], a[136:139], v[144:147]\n"
  "s_add_u32 s48, s84, s71\n"
  "ds_read_b128 a[148:151], v216 offset:26816\n"
  "v_fma_f32 v23, v23, s73, v224\n"
  "v_mfma_f32_16x16x32_bf16 v[148:151], v[28:31], a[140:143], v[148:151]\n"
  "s_addc_u32 s49, s85, 0\n"
  "v_exp_f32 v55, v55\n"
  "v_mfma_f32_16x16x32_bf16 v[176:179], v[44:47], a[136:139], v[176:179]\n"
  "s_sub_i32 s71, s38, s81\n"
  "v_exp_f32 v38, v38\n"
  "v_mfma_f32_16x16x32_bf16 v[180:183], v[44:47], a[140:143], v[180:183]\n"
  "buffer_load_dwordx4 v221, s[40:43], 0 offen offset:0 lds\n"
  "v_mfma_f32_16x16x32_bf16 v[208:211], v[60:63], a[136:139], v[208:211]\n"
  "s_addk_i32 m0, 896\n"
  "v_exp_f32 v39, v39\n"
  "v_mfma_f32_16x16x32_bf16 v[212:215], v[60:63], a[140:143], v[212:215]\n"
  "s_waitcnt lgkmcnt(4)\n"
  "v_exp_f32 v20, v20\n"
  "v_mfma_f32_16x16x32_bf16 v[64:67], a[128:131], a[96:99], v[248:251]\n"
  "s_max_i32 s50, s71, 0\n"
  "v_exp_f32 v21, v21\n"
  "v_mfma_f32_16x16x32_bf16 v[64:67], a[132:135], a[100:103], v[64:67]\n"
  "s_waitcnt lgkmcnt(2)\n"
  "buffer_load_dwordx4 v221, s[40:43], 0 offen offset:128 lds\n"
  "v_cvt_pk_bf16_f32 v59, v54, v55\n"
  "v_mfma_f32_16x16x32_bf16 v[64:67], a[120:123], a[104:107], v[64:67]\n"
  "s_addk_i32 m0, 896\n"
  "v_exp_f32 v22, v22\n"
  "v_mfma_f32_16x16x32_bf16 v[64:67], a[124:127], a[108:111], v[64:67]\n"
  "s_waitcnt lgkmcnt(0)\n"
  "v_exp_f32 v23, v23\n"
  "v_mfma_f32_16x16x32_bf16 v[64:67], a[144:147], a[112:115], v[64:67]\n"
  "v_cvt_pk_bf16_f32 v43, v38, v39\n"
  "v_cvt_pk_bf16_f32 v26, v20, v21\n"
  "v_mfma_f32_16x16x32_bf16 v[64:67], a[148:151], a[116:119], v[64:67]\n"
  "buffer_load_dwordx4 v221, s[40:43], 0 offen offset:256 lds\n"
  "v_cvt_pk_bf16_f32 v27, v22, v23\n"
  "v_mfma_f32_16x16x32_bf16 v[0:3], a[148:151], a[20:23], v[248:251]\n"
  "v_fma_f32 v4, v4, s73, v223\n"
  "v_fma_f32 v5, v5, s73, v223\n"
  "v_mfma_f32_16x16x32_bf16 v[0:3], a[144:147], a[16:19], v[0:3]\n"
  "s_add_u32 s71, s92, s56\n"
  "v_exp_f32 v4, v4\n"
  "v_mfma_f32_16x16x32_bf16 v[0:3], a[124:127], a[12:15], v[0:3]\n"
  "s_add_u32 s40, s40, s31\n"
  "v_exp_f32 v5, v5\n"
  "v_mfma_f32_16x16x32_bf16 v[0:3], a[120:123], a[8:11], v[0:3]\n"
  "s_addc_u32 s41, s41, 0\n"
  "v_fma_f32 v6, v6, s73, v223\n"
  "v_fma_f32 v7, v7, s73, v223\n"
  "v_mfma_f32_16x16x32_bf16 v[0:3], a[132:135], a[4:7], v[0:3]\n"
  "s_add_u32 m0, s59, -128\n"
  "v_exp_f32 v6, v6\n"
  "v_mfma_f32_16x16x32_bf16 v[0:3], a[128:131], a[0:3], v[0:3]\n"
  "v_exp_f32 v7, v7\n"
  "v_mfma_f32_16x16x32_bf16 v[16:19], a[128:131], a[24:27], v[248:251]\n"
  "v_cvt_pk_bf16_f32 v10, v4, v5\n"
  "v_cvt_pk_bf16_f32 v11, v6, v7\n"
  "v_mfma_f32_16x16x32_bf16 v[16:19], a[132:135], a[28:31], v[16:19]\n"
  "buffer_load_dwordx4 v221, s[32:35], 0 offen offset:128 lds\n"
  "v_add_f32 v252, v252, v4\n"
  "v_mfma_f32_16x16x32_bf16 v[16:19], a[120:123], a[32:35], v[16:19]\n"
  "s_addk_i32 m0, 1024\n"
  "ds_read_b64_tr_b16 a[136:137], v217 offset:24704\n"
  "v_add_f32 v252, v252, v5\n"
  "v_mfma_f32_16x16x32_bf16 v[16:19], a[124:127], a[36:39], v[16:19]\n"
  "s_mov_b64 s[32:33], s[44:45]\n"
  "ds_read_b64_tr_b16 a[138:139], v218 offset:24704\n"
  "v_add_f32 v252, v252, v6\n"
  "v_mfma_f32_16x16x32_bf16 v[16:19], a[144:147], a[40:43], v[16:19]\n"
  "ds_read_b64_tr_b16 a[140:141], v217 offset:25728\n"
  "v_add_f32 v252, v252, v7\n"
  "v_mfma_f32_16x16x32_bf16 v[16:19], a[148:151], a[44:47], v[16:19]\n"
  "buffer_load_dwordx4 v221, s[44:47], 0 offen offset:128 lds\n"
  "v_add_f32 v253, v253, v20\n"
  "v_mfma_f32_16x16x32_bf16 v[32:35], a[148:151], a[68:71], v[248:251]\n"
  "s_addk_i32 m0, 896\n"
  "ds_read_b64_tr_b16 a[142:143], v218 offset:25728\n"
  "v_add_f32 v253, v253, v21\n"
  "v_mfma_f32_16x16x32_bf16 v[32:35], a[144:147], a[64:67], v[32:35]\n"
  "v_add_f32 v253, v253, v22\n"
  "v_add_f32 v253, v253, v23\n"
  "v_mfma_f32_16x16x32_bf16 v[32:35], a[124:127], a[60:63], v[32:35]\n"
  "v_add_f32 v254, v254, v36\n"
  "v_add_f32 v254, v254, v37\n"
  "v_mfma_f32_16x16x32_bf16 v[32:35], a[120:123], a[56:59], v[32:35]\n"
  "buffer_load_dwordx4 v221, s[44:47], 0 offen offset:256 lds\n"
  "v_add_f32 v254, v254, v38\n"
  "v_mfma_f32_16x16x32_bf16 v[32:35], a[132:135], a[52:55], v[32:35]\n"
  "s_add_u32 s44, s44, s31\n"
  "v_add_f32 v254, v254, v39\n"
  "v_add_f32 v255, v255, v52\n"
  "v_mfma_f32_16x16x32_bf16 v[32:35], a[128:131], a[48:51], v[32:35]\n"
  "s_addc_u32 s45, s45, 0\n"
  "v_add_f32 v255, v255, v53\n"
  "v_add_f32 v255, v255, v54\n"
  "v_mfma_f32_16x16x32_bf16 v[48:51], a[128:131], a[72:75], v[248:251]\n"
  "ds_read_b64_tr_b16 a[128:129], v217 offset:24720\n"
  "v_add_f32 v255, v255, v55\n"
  "v_mfma_f32_16x16x32_bf16 v[48:51], a[132:135], a[76:79], v[48:51]\n"
  "ds_read_b64_tr_b16 a[130:131], v218 offset:24720\n"
  "v_fma_f32 v64, v64, s73, v227\n"
  "v_mfma_f32_16x16x32_bf16 v[48:51], a[120:123], a[80:83], v[48:51]\n"
  "ds_read_b64_tr_b16 a[132:133], v217 offset:25744\n"
  "v_fma_f32 v65, v65, s73, v227\n"
  "v_mfma_f32_16x16x32_bf16 v[48:51], a[124:127], a[84:87], v[48:51]\n"
  "s_add_u32 m0, s72, 6464\n"
  "ds_read_b64_tr_b16 a[134:135], v218 offset:25744\n"
  "v_mfma_f32_16x16x32_bf16 v[48:51], a[144:147], a[88:91], v[48:51]\n"
  "v_exp_f32 v64, v64\n"
  "v_mfma_f32_16x16x32_bf16 v[48:51], a[148:151], a[92:95], v[48:51]\n"
  "s_waitcnt lgkmcnt(4)\n"
  "v_exp_f32 v65, v65\n"
  "v_mfma_f32_16x16x32_bf16 a[236:239], a[160:163], a[136:139], a[236:239]\n"
  "v_fma_f32 v66, v66, s73, v227\n"
  "v_fma_f32 v67, v67, s73, v227\n"
  "v_mfma_f32_16x16x32_bf16 a[236:239], a[156:159], a[140:143], a[236:239]\n"
  "buffer_load_dwordx4 v221, s[48:51], 0 offen offset:256 nt lds\n"
  "v_cvt_pk_bf16_f32 v64, v64, v65\n"
  "v_mfma_f32_16x16x32_bf16 v[184:187], v[60:63], a[136:139], v[184:187]\n"
  "v_exp_f32 v66, v66\n"
  "v_mfma_f32_16x16x32_bf16 v[184:187], v[56:59], a[140:143], v[184:187]\n"
  "v_exp_f32 v67, v67\n"
  "v_mfma_f32_16x16x32_bf16 v[152:155], v[44:47], a[136:139], v[152:155]\n"
  "ds_read_b64_tr_b16 a[120:121], v219 offset:24704\n"
  "v_cvt_pk_bf16_f32 v65, v66, v67\n"
  "v_mfma_f32_16x16x32_bf16 v[152:155], v[40:43], a[140:143], v[152:155]\n"
  "s_add_u32 m0, s72, 4480\n"
  "ds_read_b64_tr_b16 a[122:123], v220 offset:24704\n"
  "v_mfma_f32_16x16x32_bf16 v[120:123], v[28:31], a[136:139], v[120:123]\n"
  "ds_read_b64_tr_b16 a[124:125], v219 offset:25728\n"
  "v_fma_f32 v0, v0, s73, v223\n"
  "v_mfma_f32_16x16x32_bf16 v[120:123], v[24:27], a[140:143], v[120:123]\n"
  "ds_read_b64_tr_b16 a[126:127], v220 offset:25728\n"
  "v_fma_f32 v1, v1, s73, v223\n"
  "v_mfma_f32_16x16x32_bf16 v[88:91], v[12:15], a[136:139], v[88:91]\n"
  "v_exp_f32 v0, v0\n"
  "v_mfma_f32_16x16x32_bf16 v[88:91], v[8:11], a[140:143], v[88:91]\n"
  "s_waitcnt lgkmcnt(4)\n"
  "buffer_load_dwordx4 v221, s[48:51], 0 offen offset:3328 nt lds\n"
  "v_fma_f32 v2, v2, s73, v223\n"
  "v_mfma_f32_16x16x32_bf16 v[92:95], v[12:15], a[128:131], v[92:95]\n"
  "s_add_u32 s48, s84, s71\n"
  "v_exp_f32 v1, v1\n"
  "v_mfma_f32_16x16x32_bf16 v[92:95], v[8:11], a[132:135], v[92:95]\n"
  "s_addc_u32 s49, s85, 0\n"
  "v_exp_f32 v2, v2\n"
  "v_mfma_f32_16x16x32_bf16 v[124:127], v[28:31], a[128:131], v[124:127]\n"
  "s_sub_i32 s71, s38, s56\n"
  "ds_read_b128 a[136:139], v235 offset:24704\n"
  "v_fma_f32 v3, v3, s73, v223\n"
  "v_mfma_f32_16x16x32_bf16 v[124:127], v[24:27], a[132:135], v[124:127]\n"
  "s_add_u32 m0, s72, 17920\n"
  "ds_read_b128 a[140:143], v235 offset:24768\n"
  "v_add_f32 v252, v252, v0\n"
  "v_mfma_f32_16x16x32_bf16 v[156:159], v[44:47], a[128:131], v[156:159]\n"
  "s_max_i32 s50, s71, 0\n"
  "ds_read_b128 a[144:147], v235 offset:25728\n"
  "v_add_f32 v252, v252, v1\n"
  "v_mfma_f32_16x16x32_bf16 v[156:159], v[40:43], a[132:135], v[156:159]\n"
  "ds_read_b128 a[148:151], v235 offset:25792\n"
  "v_add_f32 v252, v252, v2\n"
  "v_mfma_f32_16x16x32_bf16 v[188:191], v[60:63], a[128:131], v[188:191]\n"
  "v_exp_f32 v3, v3\n"
  "v_mfma_f32_16x16x32_bf16 v[188:191], v[56:59], a[132:135], v[188:191]\n"
  "buffer_load_dwordx4 v221, s[48:51], 0 offen offset:0 nt lds\n"
  "v_add_f32 v252, v252, v3\n"
  "v_mfma_f32_16x16x32_bf16 a[240:243], a[160:163], a[128:131], a[240:243]\n"
  "ds_read_b128 a[128:131], v235 offset:26752\n"
  "v_fma_f32 v16, v16, s73, v224\n"
  "v_mfma_f32_16x16x32_bf16 a[240:243], a[156:159], a[132:135], a[240:243]\n"
  "s_waitcnt lgkmcnt(5)\n"
  "ds_read_b128 a[132:135], v235 offset:26816\n"
  "v_fma_f32 v17, v17, s73, v224\n"
  "v_mfma_f32_16x16x32_bf16 v[192:195], v[60:63], a[120:123], v[192:195]\n"
  "v_exp_f32 v16, v16\n"
  "v_mfma_f32_16x16x32_bf16 v[192:195], v[56:59], a[124:127], v[192:195]\n"
  "s_add_u32 m0, s72, 18912\n"
  "ds_write_b64 v231, v[64:65] offset:2048\n"
  "v_mfma_f32_16x16x32_bf16 v[160:163], v[44:47], a[120:123], v[160:163]\n"
  "v_exp_f32 v17, v17\n"
  "v_mfma_f32_16x16x32_bf16 v[160:163], v[40:43], a[124:127], v[160:163]\n"
  "v_fma_f32 v18, v18, s73, v224\n"
  "v_fma_f32 v19, v19, s73, v224\n"
  "v_mfma_f32_16x16x32_bf16 v[128:131], v[28:31], a[120:123], v[128:131]\n"
  "v_exp_f32 v18, v18\n"
  "v_mfma_f32_16x16x32_bf16 v[128:131], v[24:27], a[124:127], v[128:131]\n"
  "buffer_load_dwordx4 v221, s[48:51], 0 offen offset:128 nt lds\n"
  "v_add_f32 v253, v253, v16\n"
  "v_mfma_f32_16x16x32_bf16 v[96:99], v[12:15], a[120:123], v[96:99]\n"
  "v_exp_f32 v19, v19\n"
  "v_mfma_f32_16x16x32_bf16 v[96:99], v[8:11], a[124:127], v[96:99]\n"
  "s_waitcnt lgkmcnt(5)\n"
  "v_add_f32 v253, v253, v17\n"
  "v_add_f32 v253, v253, v18\n"
  "v_mfma_f32_16x16x32_bf16 v[4:7], a[136:139], a[0:3], v[248:251]\n"
  "v_add_f32 v253, v253, v19\n"
  "v_fma_f32 v32, v32, s73, v225\n"
  "v_mfma_f32_16x16x32_bf16 v[4:7], a[140:143], a[4:7], v[4:7]\n"
  "s_waitcnt lgkmcnt(3)\n"
  "v_exp_f32 v32, v32\n"
  "v_mfma_f32_16x16x32_bf16 v[4:7], a[144:147], a[8:11], v[4:7]\n"
  "v_fma_f32 v33, v33, s73, v225\n"
  "v_fma_f32 v34, v34, s73, v225\n"
  "v_mfma_f32_16x16x32_bf16 v[4:7], a[148:151], a[12:15], v[4:7]\n"
  "s_waitcnt lgkmcnt(1)\n"
  "v_exp_f32 v33, v33\n"
  "v_mfma_f32_16x16x32_bf16 v[4:7], a[128:131], a[16:19], v[4:7]\n"
  "v_exp_f32 v34, v34\n"
  "v_mfma_f32_16x16x32_bf16 v[4:7], a[132:135], a[20:23], v[4:7]\n"
  "v_fma_f32 v35, v35, s73, v225\n"
  "v_add_f32 v254, v254, v32\n"
  "v_mfma_f32_16x16x32_bf16 v[20:23], a[132:135], a[44:47], v[248:251]\n"
  "v_exp_f32 v35, v35\n"
  "v_mfma_f32_16x16x32_bf16 v[20:23], a[128:131], a[40:43], v[20:23]\n"
  "v_add_f32 v254, v254, v33\n"
  "v_add_f32 v254, v254, v34\n"
  "v_mfma_f32_16x16x32_bf16 v[20:23], a[148:151], a[36:39], v[20:23]\n"
  "ds_read_b64_tr_b16 a[120:121], v219 offset:24720\n"
  "v_add_f32 v254, v254, v35\n"
  "v_mfma_f32_16x16x32_bf16 v[20:23], a[144:147], a[32:35], v[20:23]\n"
  "ds_read_b64_tr_b16 a[122:123], v220 offset:24720\n"
  "v_fma_f32 v48, v48, s73, v226\n"
  "v_mfma_f32_16x16x32_bf16 v[20:23], a[140:143], a[28:31], v[20:23]\n"
  "ds_read_b64_tr_b16 a[124:125], v219 offset:25744\n"
  "v_fma_f32 v49, v49, s73, v226\n"
  "v_mfma_f32_16x16x32_bf16 v[20:23], a[136:139], a[24:27], v[20:23]\n"
  "ds_read_b64_tr_b16 a[126:127], v220 offset:25744\n"
  "v_fma_f32 v50, v50, s73, v226\n"
  "v_mfma_f32_16x16x32_bf16 v[36:39], a[136:139], a[48:51], v[248:251]\n"
  "v_exp_f32 v48, v48\n"
  "v_mfma_f32_16x16x32_bf16 v[36:39], a[140:143], a[52:55], v[36:39]\n"
  "v_exp_f32 v49, v49\n"
  "v_mfma_f32_16x16x32_bf16 v[36:39], a[144:147], a[56:59], v[36:39]\n"
  "v_exp_f32 v50, v50\n"
  "v_mfma_f32_16x16x32_bf16 v[36:39], a[148:151], a[60:63], v[36:39]\n"
  "v_fma_f32 v51, v51, s73, v226\n"
  "v_add_f32 v255, v255, v48\n"
  "v_mfma_f32_16x16x32_bf16 v[36:39], a[128:131], a[64:67], v[36:39]\n"
  "v_exp_f32 v51, v51\n"
  "v_mfma_f32_16x16x32_bf16 v[36:39], a[132:135], a[68:71], v[36:39]\n"
  "v_add_f32 v255, v255, v49\n"
  "v_add_f32 v255, v255, v50\n"
  "v_mfma_f32_16x16x32_bf16 v[52:55], a[132:135], a[92:95], v[248:251]\n"
  "ds_read_b64_tr_b16 a[132:133], v217 offset:26752\n"
  "v_add_f32 v255, v255, v51\n"
  "v_mfma_f32_16x16x32_bf16 v[52:55], a[128:131], a[88:91], v[52:55]\n"
  "ds_read_b64_tr_b16 a[134:135], v218 offset:26752\n"
  "v_fma_f32 v20, v20, s73, v224\n"
  "v_mfma_f32_16x16x32_bf16 v[52:55], a[148:151], a[84:87], v[52:55]\n"
  "ds_read_b64_tr_b16 a[128:129], v217 offset:26768\n"
  "v_fma_f32 v36, v36, s73, v225\n"
  "v_mfma_f32_16x16x32_bf16 v[52:55], a[144:147], a[80:83], v[52:55]\n"
  "ds_read_b64_tr_b16 a[130:131], v218 offset:26768\n"
  "v_fma_f32 v37, v37, s73, v225\n"
  "v_mfma_f32_16x16x32_bf16 v[52:55], a[140:143], a[76:79], v[52:55]\n"
  "v_exp_f32 v36, v36\n"
  "v_mfma_f32_16x16x32_bf16 v[52:55], a[136:139], a[72:75], v[52:55]\n"
  "s_waitcnt lgkmcnt(4)\n"
  "v_exp_f32 v37, v37\n"
  "v_mfma_f32_16x16x32_bf16 v[100:103], v[12:15], a[120:123], v[100:103]\n"
  "v_cvt_pk_bf16_f32 v12, v0, v1\n"
  "v_cvt_pk_bf16_f32 v13, v2, v3\n"
  "v_mfma_f32_16x16x32_bf16 v[100:103], v[8:11], a[124:127], v[100:103]\n"
  "v_exp_f32 v20, v20\n"
  "v_mfma_f32_16x16x32_bf16 v[132:135], v[28:31], a[120:123], v[132:135]\n"
  "ds_read_b64_tr_b16 a[140:141], v219 offset:26752\n"
  "v_cvt_pk_bf16_f32 v28, v16, v17\n"
  "v_mfma_f32_16x16x32_bf16 v[132:135], v[24:27], a[124:127], v[132:135]\n"
  "ds_read_b64_tr_b16 a[142:143], v220 offset:26752\n"
  "v_cvt_pk_bf16_f32 v29, v18, v19\n"
  "v_mfma_f32_16x16x32_bf16 v[164:167], v[44:47], a[120:123], v[164:167]\n"
  "ds_read_b64_tr_b16 a[136:137], v219 offset:26768\n"
  "v_cvt_pk_bf16_f32 v44, v32, v33\n"
  "v_mfma_f32_16x16x32_bf16 v[164:167], v[40:43], a[124:127], v[164:167]\n"
  "ds_read_b64_tr_b16 a[138:139], v220 offset:26768\n"
  "v_cvt_pk_bf16_f32 v45, v34, v35\n"
  "v_mfma_f32_16x16x32_bf16 v[196:199], v[60:63], a[120:123], v[196:199]\n"
  "v_cvt_pk_bf16_f32 v60, v48, v49\n"
  "v_cvt_pk_bf16_f32 v61, v50, v51\n"
  "v_mfma_f32_16x16x32_bf16 v[196:199], v[56:59], a[124:127], v[196:199]\n"
  "s_waitcnt lgkmcnt(4)\n"
  "v_fma_f32 v52, v52, s73, v226\n"
  "v_fma_f32 v53, v53, s73, v226\n"
  "v_mfma_f32_16x16x32_bf16 a[244:247], a[156:159], a[132:135], a[244:247]\n"
  "v_exp_f32 v52, v52\n"
  "v_mfma_f32_16x16x32_bf16 a[248:251], a[156:159], a[128:131], a[248:251]\n"
  "v_exp_f32 v53, v53\n"
  "v_mfma_f32_16x16x32_bf16 v[200:203], v[56:59], a[132:135], v[200:203]\n"
  "v_fma_f32 v54, v54, s73, v226\n"
  "v_fma_f32 v55, v55, s73, v226\n"
  "v_mfma_f32_16x16x32_bf16 v[204:207], v[56:59], a[128:131], v[204:207]\n"
  "v_exp_f32 v54, v54\n"
  "v_mfma_f32_16x16x32_bf16 v[168:171], v[40:43], a[132:135], v[168:171]\n"
  "v_exp_f32_e64 v55, v55\n"
  "v_mfma_f32_16x16x32_bf16 v[172:175], v[40:43], a[128:131], v[172:175]\n"
  "s_waitcnt vmcnt(14) lgkmcnt(0)\n"
  "s_barrier\n"
  "ds_read_b128 a[120:123], v216 offset:0\n"
  "v_mfma_f32_16x16x32_bf16 v[136:139], v[24:27], a[132:135], v[136:139]\n"
  "ds_read_b64 a[160:161], v231 offset:2048\n"
  "v_cvt_pk_bf16_f32 v62, v52, v53\n"
  "v_mfma_f32_16x16x32_bf16 v[140:143], v[24:27], a[128:131], v[140:143]\n"
  "ds_read_b64 a[162:163], v232 offset:2048\n"
  "v_cvt_pk_bf16_f32 v63, v54, v55\n"
  "v_mfma_f32_16x16x32_bf16 v[104:107], v[8:11], a[132:135], v[104:107]\n"
  "ds_read_b128 a[124:127], v216 offset:64\n"
  "v_fma_f32 v38, v38, s73, v225\n"
  "v_mfma_f32_16x16x32_bf16 v[108:111], v[8:11], a[128:131], v[108:111]\n"
  "ds_read_b128 a[132:135], v216 offset:1024\n"
  "v_fma_f32 v39, v39, s73, v225\n"
  "v_mfma_f32_16x16x32_bf16 v[112:115], v[8:11], a[140:143], v[112:115]\n"
  "ds_read_b128 a[128:131], v216 offset:1088\n"
  "v_cvt_pk_bf16_f32 v46, v36, v37\n"
  "v_mfma_f32_16x16x32_bf16 v[116:119], v[8:11], a[136:139], v[116:119]\n"
  "s_mov_b32 m0, s68\n"
  "ds_read_b128 a[148:151], v216 offset:2048\n"
  "v_fma_f32 v21, v21, s73, v224\n"
  "v_mfma_f32_16x16x32_bf16 v[144:147], v[24:27], a[140:143], v[144:147]\n"
  "ds_read_b128 a[144:147], v216 offset:2112\n"
  "v_fma_f32 v22, v22, s73, v224\n"
  "v_mfma_f32_16x16x32_bf16 v[148:151], v[24:27], a[136:139], v[148:151]\n"
  "v_exp_f32 v38, v38\n"
  "v_mfma_f32_16x16x32_bf16 v[176:179], v[40:43], a[140:143], v[176:179]\n"
  "v_exp_f32 v39, v39\n"
  "v_mfma_f32_16x16x32_bf16 v[180:183], v[40:43], a[136:139], v[180:183]\n"
  "buffer_load_dwordx4 v221, s[40:43], 0 offen offset:0 lds\n"
  "v_mfma_f32_16x16x32_bf16 v[208:211], v[56:59], a[140:143], v[208:211]\n"
  "s_addk_i32 m0, 896\n"
  "v_exp_f32 v21, v21\n"
  "v_mfma_f32_16x16x32_bf16 v[212:215], v[56:59], a[136:139], v[212:215]\n"
  "s_waitcnt lgkmcnt(4)\n"
  "v_exp_f32 v22, v22\n"
  "v_mfma_f32_16x16x32_bf16 v[64:67], a[120:123], a[96:99], v[248:251]\n"
  "v_cvt_pk_bf16_f32 v47, v38, v39\n"
  "v_fma_f32 v23, v23, s73, v224\n"
  "v_mfma_f32_16x16x32_bf16 v[64:67], a[124:127], a[100:103], v[64:67]\n"
  "s_waitcnt lgkmcnt(2)\n"
  "buffer_load_dwordx4 v221, s[40:43], 0 offen offset:128 lds\n"
  "v_cvt_pk_bf16_f32 v30, v20, v21\n"
  "v_mfma_f32_16x16x32_bf16 v[64:67], a[132:135], a[104:107], v[64:67]\n"
  "s_addk_i32 m0, 896\n"
  "v_exp_f32 v23, v23\n"
  "v_mfma_f32_16x16x32_bf16 v[64:67], a[128:131], a[108:111], v[64:67]\n"
  "s_waitcnt lgkmcnt(0)\n"
  "v_cvt_pk_bf16_f32 v31, v22, v23\n"
  "v_fma_f32 v4, v4, s73, v223\n"
  "v_mfma_f32_16x16x32_bf16 v[64:67], a[148:151], a[112:115], v[64:67]\n"
  "v_exp_f32 v4, v4\n"
  "v_mfma_f32_16x16x32_bf16 v[64:67], a[144:147], a[116:119], v[64:67]\n"
  "buffer_load_dwordx4 v221, s[40:43], 0 offen offset:256 lds\n"
  "v_fma_f32 v5, v5, s73, v223\n"
  "v_mfma_f32_16x16x32_bf16 v[0:3], a[144:147], a[20:23], v[248:251]\n"
  "s_add_u32 s40, s40, s31\n"
  "v_exp_f32 v5, v5\n"
  "v_mfma_f32_16x16x32_bf16 v[0:3], a[148:151], a[16:19], v[0:3]\n"
  "s_addc_u32 s41, s41, 0\n"
  "v_fma_f32 v6, v6, s73, v223\n"
  "v_fma_f32 v7, v7, s73, v223\n"
  "v_mfma_f32_16x16x32_bf16 v[0:3], a[128:131], a[12:15], v[0:3]\n"
  "v_exp_f32 v6, v6\n"
  "v_mfma_f32_16x16x32_bf16 v[0:3], a[132:135], a[8:11], v[0:3]\n"
  "s_mov_b32 m0, s60\n"
  "v_exp_f32 v7, v7\n"
  "v_mfma_f32_16x16x32_bf16 v[0:3], a[124:127], a[4:7], v[0:3]\n"
  "v_cvt_pk_bf16_f32 v14, v4, v5\n"
  "v_cvt_pk_bf16_f32 v15, v6, v7\n"
  "v_mfma_f32_16x16x32_bf16 v[0:3], a[120:123], a[0:3], v[0:3]\n"
  "v_add_f32 v252, v252, v4\n"
  "v_add_f32 v252, v252, v5\n"
  "v_mfma_f32_16x16x32_bf16 v[16:19], a[120:123], a[24:27], v[248:251]\n"
  "v_add_f32 v252, v252, v6\n"
  "v_add_f32 v252, v252, v7\n"
  "v_mfma_f32_16x16x32_bf16 v[16:19], a[124:127], a[28:31], v[16:19]\n"
  "buffer_load_dwordx4 v221, s[32:35], 0 offen offset:0 lds\n"
  "v_add_f32 v253, v253, v20\n"
  "v_mfma_f32_16x16x32_bf16 v[16:19], a[132:135], a[32:35], v[16:19]\n"
  "s_addk_i32 m0, 1024\n"
  "ds_read_b64_tr_b16 a[140:141], v217 offset:0\n"
  "v_add_f32 v253, v253, v21\n"
  "v_mfma_f32_16x16x32_bf16 v[16:19], a[128:131], a[36:39], v[16:19]\n"
  "s_mov_b64 s[32:33], s[44:45]\n"
  "ds_read_b64_tr_b16 a[142:143], v218 offset:0\n"
  "v_add_f32 v253, v253, v22\n"
  "v_mfma_f32_16x16x32_bf16 v[16:19], a[148:151], a[40:43], v[16:19]\n"
  "ds_read_b64_tr_b16 a[136:137], v217 offset:1024\n"
  "v_add_f32 v253, v253, v23\n"
  "v_mfma_f32_16x16x32_bf16 v[16:19], a[144:147], a[44:47], v[16:19]\n"
  "buffer_load_dwordx4 v221, s[44:47], 0 offen offset:0 lds\n"
  "v_add_f32 v254, v254, v36\n"
  "v_mfma_f32_16x16x32_bf16 v[32:35], a[144:147], a[68:71], v[248:251]\n"
  "s_addk_i32 m0, 768\n"
  "ds_read_b64_tr_b16 a[138:139], v218 offset:1024\n"
  "v_add_f32 v254, v254, v37\n"
  "v_mfma_f32_16x16x32_bf16 v[32:35], a[148:151], a[64:67], v[32:35]\n"
  "v_add_f32 v254, v254, v38\n"
  "v_add_f32 v254, v254, v39\n"
  "v_mfma_f32_16x16x32_bf16 v[32:35], a[128:131], a[60:63], v[32:35]\n"
  "v_add_f32 v255, v255, v52\n"
  "v_add_f32 v255, v255, v53\n"
  "v_mfma_f32_16x16x32_bf16 v[32:35], a[132:135], a[56:59], v[32:35]\n"
  "buffer_load_dwordx4 v221, s[44:47], 0 offen offset:256 lds\n"
  "v_add_f32 v255, v255, v54\n"
  "v_mfma_f32_16x16x32_bf16 v[32:35], a[124:127], a[52:55], v[32:35]\n"
  "s_add_u32 s44, s44, s31\n"
  "ds_read_b64_tr_b16 a[252:253], v230 offset:20160\n"
  "v_add_f32 v255, v255, v55\n"
  "v_mfma_f32_16x16x32_bf16 v[32:35], a[120:123], a[48:51], v[32:35]\n"
  "s_addc_u32 s45, s45, 0\n"
  "ds_read_b64_tr_b16 a[254:255], v230 offset:20192\n"
  "ds_read_b64_tr_b16 v[244:245], v230 offset:8960\n"
  "v_mfma_f32_16x16x32_bf16 v[48:51], a[120:123], a[72:75], v[248:251]\n"
  "ds_read_b64_tr_b16 v[246:247], v230 offset:8992\n"
  "v_fma_f32 v64, v64, s73, v227\n"
  "v_mfma_f32_16x16x32_bf16 v[48:51], a[124:127], a[76:79], v[48:51]\n"
  "v_exp_f32 v64, v64\n"
  "v_mfma_f32_16x16x32_bf16 v[48:51], a[132:135], a[80:83], v[48:51]\n"
  "ds_read_b64_tr_b16 a[120:121], v217 offset:16\n"
  "v_fma_f32 v65, v65, s73, v227\n"
  "v_mfma_f32_16x16x32_bf16 v[48:51], a[128:131], a[84:87], v[48:51]\n"
  "ds_read_b64_tr_b16 a[122:123], v218 offset:16\n"
  "v_fma_f32 v66, v66, s73, v227\n"
  "v_mfma_f32_16x16x32_bf16 v[48:51], a[148:151], a[88:91], v[48:51]\n"
  "ds_read_b64_tr_b16 a[124:125], v217 offset:1040\n"
  "v_fma_f32 v67, v67, s73, v227\n"
  "v_mfma_f32_16x16x32_bf16 v[48:51], a[144:147], a[92:95], v[48:51]\n"
  "s_waitcnt lgkmcnt(7)\n"
  "ds_read_b64_tr_b16 a[126:127], v218 offset:1040\n"
  "v_fma_f32 v0, v0, s73, v223\n"
  "v_mfma_f32_16x16x32_bf16 a[228:231], a[156:159], a[140:143], a[228:231]\n"
  "v_exp_f32 v65, v65\n"
  "v_mfma_f32_16x16x32_bf16 a[228:231], a[160:163], a[136:139], a[228:231]\n"
  "v_exp_f32 v66, v66\n"
  "v_mfma_f32_16x16x32_bf16 a[212:215], v[56:59], a[140:143], a[212:215]\n"
  "v_exp_f32 v67, v67\n"
  "v_mfma_f32_16x16x32_bf16 a[212:215], v[60:63], a[136:139], a[212:215]\n"
  "v_exp_f32 v0, v0\n"
  "v_mfma_f32_16x16x32_bf16 a[196:199], v[40:43], a[140:143], a[196:199]\n"
  "v_cvt_pk_bf16_f32 v64, v64, v65\n"
  "v_cvt_pk_bf16_f32 v65, v66, v67\n"
  "v_mfma_f32_16x16x32_bf16 a[196:199], v[44:47], a[136:139], a[196:199]\n"
  "v_fma_f32 v1, v1, s73, v223\n"
  "v_fma_f32 v2, v2, s73, v223\n"
  "v_mfma_f32_16x16x32_bf16 a[180:183], v[24:27], a[140:143], a[180:183]\n"
  "ds_read_b64_tr_b16 a[132:133], v219 offset:0\n"
  "v_fma_f32 v3, v3, s73, v223\n"
  "v_mfma_f32_16x16x32_bf16 a[180:183], v[28:31], a[136:139], a[180:183]\n"
  "ds_read_b64_tr_b16 a[134:135], v220 offset:0\n"
  "v_add_f32 v252, v252, v0\n"
  "v_mfma_f32_16x16x32_bf16 a[164:167], v[8:11], a[140:143], a[164:167]\n"
  "ds_read_b64_tr_b16 a[128:129], v219 offset:1024\n"
  "v_fma_f32 v16, v16, s73, v224\n"
  "v_mfma_f32_16x16x32_bf16 a[164:167], v[12:15], a[136:139], a[164:167]\n"
  "ds_read_b64_tr_b16 a[130:131], v220 offset:1024\n"
  "v_fma_f32 v17, v17, s73, v224\n"
  "v_mfma_f32_16x16x32_bf16 v[84:87], a[156:159], a[152:155], v[84:87]\n"
  "v_exp_f32 v1, v1\n"
  "v_mfma_f32_16x16x32_bf16 v[84:87], a[160:163], a[152:155], v[84:87]\n"
  "s_waitcnt lgkmcnt(4)\n"
  "v_exp_f32 v2, v2\n"
  "v_mfma_f32_16x16x32_bf16 a[168:171], v[8:11], a[120:123], a[168:171]\n"
  "v_exp_f32 v3, v3\n"
  "v_mfma_f32_16x16x32_bf16 a[168:171], v[12:15], a[124:127], a[168:171]\n"
  "v_exp_f32 v16, v16\n"
  "v_mfma_f32_16x16x32_bf16 a[184:187], v[24:27], a[120:123], a[184:187]\n"
  "ds_read_b128 a[140:143], v235 offset:0\n"
  "v_add_f32 v252, v252, v1\n"
  "v_mfma_f32_16x16x32_bf16 a[184:187], v[28:31], a[124:127], a[184:187]\n"
  "ds_read_b128 a[136:139], v235 offset:64\n"
  "v_add_f32 v252, v252, v2\n"
  "v_mfma_f32_16x16x32_bf16 a[200:203], v[40:43], a[120:123], a[200:203]\n"
  "ds_read_b128 a[148:151], v235 offset:1024\n"
  "v_add_f32 v252, v252, v3\n"
  "v_mfma_f32_16x16x32_bf16 a[200:203], v[44:47], a[124:127], a[200:203]\n"
  "ds_read_b128 a[144:147], v235 offset:1088\n"
  "v_fma_f32 v18, v18, s73, v224\n"
  "v_mfma_f32_16x16x32_bf16 a[216:219], v[56:59], a[120:123], a[216:219]\n"
  "v_exp_f32 v17, v17\n"
  "v_mfma_f32_16x16x32_bf16 a[216:219], v[60:63], a[124:127], a[216:219]\n"
  "v_exp_f32 v18, v18\n"
  "v_mfma_f32_16x16x32_bf16 a[232:235], a[156:159], a[120:123], a[232:235]\n"
  "ds_read_b128 a[120:123], v235 offset:2048\n"
  "v_fma_f32 v19, v19, s73, v224\n"
  "v_mfma_f32_16x16x32_bf16 a[232:235], a[160:163], a[124:127], a[232:235]\n"
  "s_waitcnt lgkmcnt(5)\n"
  "ds_read_b128 a[124:127], v235 offset:2112\n"
  "v_add_f32 v253, v253, v16\n"
  "v_mfma_f32_16x16x32_bf16 a[220:223], v[56:59], a[132:135], a[220:223]\n"
  "v_exp_f32 v19, v19\n"
  "v_mfma_f32_16x16x32_bf16 a[220:223], v[60:63], a[128:131], a[220:223]\n"
  "ds_write_b64 v231, v[64:65] offset:0\n"
  "v_mfma_f32_16x16x32_bf16 a[204:207], v[40:43], a[132:135], a[204:207]\n"
  "v_add_f32 v253, v253, v17\n"
  "v_add_f32 v253, v253, v18\n"
  "v_mfma_f32_16x16x32_bf16 a[204:207], v[44:47], a[128:131], a[204:207]\n"
  "buffer_store_dwordx4 a[252:255], v228, s[52:55], s56 offen offset:256 nt\n"
  "v_add_f32 v253, v253, v19\n"
  "v_fma_f32 v32, v32, s73, v225\n"
  "v_fma_f32 v33, v33, s73, v225\n"
  "v_mfma_f32_16x16x32_bf16 a[188:191], v[24:27], a[132:135], a[188:191]\n"
  "ds_read_b64_tr_b16 a[252:253], v230 offset:10080\n"
  "ds_read_b64_tr_b16 a[254:255], v230 offset:10112\n"
  "v_mfma_f32_16x16x32_bf16 a[188:191], v[28:31], a[128:131], a[188:191]\n"
  "v_exp_f32 v32, v32\n"
  "v_mfma_f32_16x16x32_bf16 a[172:175], v[8:11], a[132:135], a[172:175]\n"
  "v_exp_f32 v33, v33\n"
  "v_mfma_f32_16x16x32_bf16 a[172:175], v[12:15], a[128:131], a[172:175]\n"
  "s_waitcnt lgkmcnt(7)\n"
  "v_fma_f32 v34, v34, s73, v225\n"
  "v_fma_f32 v35, v35, s73, v225\n"
  "v_mfma_f32_16x16x32_bf16 v[4:7], a[140:143], a[0:3], v[248:251]\n"
  "v_exp_f32 v34, v34\n"
  "v_mfma_f32_16x16x32_bf16 v[4:7], a[136:139], a[4:7], v[4:7]\n"
  "s_waitcnt lgkmcnt(5)\n"
  "v_exp_f32 v35, v35\n"
  "v_mfma_f32_16x16x32_bf16 v[4:7], a[148:151], a[8:11], v[4:7]\n"
  "v_add_f32 v254, v254, v32\n"
  "v_add_f32 v254, v254, v33\n"
  "v_mfma_f32_16x16x32_bf16 v[4:7], a[144:147], a[12:15], v[4:7]\n"
  "s_waitcnt lgkmcnt(3)\n"
  "v_add_f32 v254, v254, v34\n"
  "v_add_f32 v254, v254, v35\n"
  "v_mfma_f32_16x16x32_bf16 v[4:7], a[120:123], a[16:19], v[4:7]\n"
  "v_fma_f32 v48, v48, s73, v226\n"
  "v_fma_f32 v49, v49, s73, v226\n"
  "v_mfma_f32_16x16x32_bf16 v[4:7], a[124:127], a[20:23], v[4:7]\n"
  "buffer_store_dwordx4 v[244:247], v228, s[52:55], s80 offen offset:128 nt\n"
  "v_exp_f32 v48, v48\n"
  "v_fma_f32 v50, v50, s73, v226\n"
  "v_mfma_f32_16x16x32_bf16 v[20:23], a[124:127], a[44:47], v[248:251]\n"
  "v_exp_f32 v49, v49\n"
  "v_mfma_f32_16x16x32_bf16 v[20:23], a[120:123], a[40:43], v[20:23]\n"
  "v_exp_f32 v50, v50\n"
  "v_mfma_f32_16x16x32_bf16 v[20:23], a[144:147], a[36:39], v[20:23]\n"
  "ds_read_b64_tr_b16 a[132:133], v219 offset:16\n"
  "v_fma_f32 v51, v51, s73, v226\n"
  "v_mfma_f32_16x16x32_bf16 v[20:23], a[148:151], a[32:35], v[20:23]\n"
  "ds_read_b64_tr_b16 a[134:135], v220 offset:16\n"
  "v_add_f32 v255, v255, v48\n"
  "v_mfma_f32_16x16x32_bf16 v[20:23], a[136:139], a[28:31], v[20:23]\n"
  "ds_read_b64_tr_b16 a[128:129], v219 offset:1040\n"
  "v_add_f32 v255, v255, v49\n"
  "v_mfma_f32_16x16x32_bf16 v[20:23], a[140:143], a[24:27], v[20:23]\n"
  "s_waitcnt lgkmcnt(3)\n"
  "buffer_store_dwordx4 a[252:255], v228, s[52:55], s80 offen offset:3200 nt\n"
  "ds_read_b64_tr_b16 a[130:131], v220 offset:1040\n"
  "v_exp_f32 v51, v51\n"
  "v_mfma_f32_16x16x32_bf16 v[36:39], a[140:143], a[48:51], v[248:251]\n"
  "v_add_f32 v255, v255, v50\n"
  "v_add_f32 v255, v255, v51\n"
  "v_mfma_f32_16x16x32_bf16 v[36:39], a[136:139], a[52:55], v[36:39]\n"
  "v_fma_f32 v4, v4, s73, v223\n"
  "v_fma_f32 v5, v5, s73, v223\n"
  "v_mfma_f32_16x16x32_bf16 v[36:39], a[148:151], a[56:59], v[36:39]\n"
  "v_exp_f32 v4, v4\n"
  "v_mfma_f32_16x16x32_bf16 v[36:39], a[144:147], a[60:63], v[36:39]\n"
  "v_exp_f32 v5, v5\n"
  "v_mfma_f32_16x16x32_bf16 v[36:39], a[120:123], a[64:67], v[36:39]\n"
  "v_fma_f32 v20, v20, s73, v224\n"
  "v_fma_f32 v21, v21, s73, v224\n"
  "v_mfma_f32_16x16x32_bf16 v[36:39], a[124:127], a[68:71], v[36:39]\n"
  "v_exp_f32 v20, v20\n"
  "v_mfma_f32_16x16x32_bf16 v[52:55], a[124:127], a[92:95], v[248:251]\n"
  "ds_read_b64_tr_b16 a[124:125], v217 offset:2048\n"
  "v_fma_f32 v22, v22, s73, v224\n"
  "v_mfma_f32_16x16x32_bf16 v[52:55], a[120:123], a[88:91], v[52:55]\n"
  "ds_read_b64_tr_b16 a[126:127], v218 offset:2048\n"
  "v_fma_f32 v23, v23, s73, v224\n"
  "v_mfma_f32_16x16x32_bf16 v[52:55], a[144:147], a[84:87], v[52:55]\n"
  "ds_read_b64_tr_b16 a[120:121], v217 offset:2064\n"
  "v_fma_f32 v36, v36, s73, v225\n"
  "v_mfma_f32_16x16x32_bf16 v[52:55], a[148:151], a[80:83], v[52:55]\n"
  "ds_read_b64_tr_b16 a[122:123], v218 offset:2064\n"
  "v_fma_f32 v37, v37, s73, v225\n"
  "v_mfma_f32_16x16x32_bf16 v[52:55], a[136:139], a[76:79], v[52:55]\n"
  "v_exp_f32 v36, v36\n"
  "v_mfma_f32_16x16x32_bf16 v[52:55], a[140:143], a[72:75], v[52:55]\n"
  "s_waitcnt lgkmcnt(4)\n"
  "v_exp_f32 v37, v37\n"
  "v_mfma_f32_16x16x32_bf16 a[176:179], v[8:11], a[132:135], a[176:179]\n"
  "v_cvt_pk_bf16_f32 v8, v0, v1\n"
  "v_cvt_pk_bf16_f32 v9, v2, v3\n"
  "v_mfma_f32_16x16x32_bf16 a[176:179], v[12:15], a[128:131], a[176:179]\n"
  "v_exp_f32 v21, v21\n"
  "v_mfma_f32_16x16x32_bf16 a[192:195], v[24:27], a[132:135], a[192:195]\n"
  "ds_read_b64_tr_b16 a[136:137], v219 offset:2048\n"
  "v_cvt_pk_bf16_f32 v24, v16, v17\n"
  "v_mfma_f32_16x16x32_bf16 a[192:195], v[28:31], a[128:131], a[192:195]\n"
  "ds_read_b64_tr_b16 a[138:139], v220 offset:2048\n"
  "v_cvt_pk_bf16_f32 v25, v18, v19\n"
  "v_mfma_f32_16x16x32_bf16 a[208:211], v[40:43], a[132:135], a[208:211]\n"
  "ds_read_b64_tr_b16 a[140:141], v219 offset:2064\n"
  "v_cvt_pk_bf16_f32 v40, v32, v33\n"
  "v_mfma_f32_16x16x32_bf16 a[208:211], v[44:47], a[128:131], a[208:211]\n"
  "ds_read_b64_tr_b16 a[142:143], v220 offset:2064\n"
  "v_cvt_pk_bf16_f32 v41, v34, v35\n"
  "v_mfma_f32_16x16x32_bf16 a[224:227], v[56:59], a[132:135], a[224:227]\n"
  "v_exp_f32 v22, v22\n"
  "v_mfma_f32_16x16x32_bf16 a[224:227], v[60:63], a[128:131], a[224:227]\n"
  "s_waitcnt lgkmcnt(4)\n"
  "v_exp_f32 v23, v23\n"
  "v_mfma_f32_16x16x32_bf16 a[244:247], a[160:163], a[124:127], a[244:247]\n"
  "v_cvt_pk_bf16_f32 v56, v48, v49\n"
  "v_cvt_pk_bf16_f32 v57, v50, v51\n"
  "v_mfma_f32_16x16x32_bf16 a[248:251], a[160:163], a[120:123], a[248:251]\n"
  "v_fma_f32 v52, v52, s73, v226\n"
  "v_fma_f32 v53, v53, s73, v226\n"
  "v_mfma_f32_16x16x32_bf16 v[200:203], v[60:63], a[124:127], v[200:203]\n"
  "v_exp_f32 v52, v52\n"
  "v_mfma_f32_16x16x32_bf16 v[204:207], v[60:63], a[120:123], v[204:207]\n"
  "v_exp_f32 v53, v53\n"
  "v_mfma_f32_16x16x32_bf16 v[168:171], v[44:47], a[124:127], v[168:171]\n"
  "v_fma_f32 v54, v54, s73, v226\n"
  "v_fma_f32 v55, v55, s73, v226\n"
  "v_mfma_f32_16x16x32_bf16 v[172:175], v[44:47], a[120:123], v[172:175]\n"
  "s_waitcnt vmcnt(13) lgkmcnt(0)\n"
  "s_barrier\n"
  "ds_read_b128 a[132:135], v216 offset:12352\n"
  "v_mfma_f32_16x16x32_bf16 v[136:139], v[28:31], a[124:127], v[136:139]\n"
  "ds_read_b64 a[156:157], v231 offset:0\n"
  "v_cvt_pk_bf16_f32 v58, v52, v53\n"
  "v_mfma_f32_16x16x32_bf16 v[140:143], v[28:31], a[120:123], v[140:143]\n"
  "ds_read_b64 a[158:159], v232 offset:0\n"
  "v_fma_f32 v38, v38, s73, v225\n"
  "v_mfma_f32_16x16x32_bf16 v[104:107], v[12:15], a[124:127], v[104:107]\n"
  "ds_read_b128 a[128:131], v216 offset:12416\n"
  "v_fma_f32 v39, v39, s73, v225\n"
  "v_mfma_f32_16x16x32_bf16 v[108:111], v[12:15], a[120:123], v[108:111]\n"
  "ds_read_b128 a[124:127], v216 offset:13376\n"
  "v_cvt_pk_bf16_f32 v42, v36, v37\n"
  "v_mfma_f32_16x16x32_bf16 v[112:115], v[12:15], a[136:139], v[112:115]\n"
  "ds_read_b128 a[120:123], v216 offset:13440\n"
  "v_cvt_pk_bf16_f32 v26, v20, v21\n"
  "v_mfma_f32_16x16x32_bf16 v[116:119], v[12:15], a[140:143], v[116:119]\n"
  "s_mov_b32 m0, s66\n"
  "ds_read_b128 a[144:147], v216 offset:14400\n"
  "v_cvt_pk_bf16_f32 v27, v22, v23\n"
  "v_mfma_f32_16x16x32_bf16 v[144:147], v[28:31], a[136:139], v[144:147]\n"
  "ds_read_b128 a[148:151], v216 offset:14464\n"
  "v_fma_f32 v6, v6, s73, v223\n"
  "v_mfma_f32_16x16x32_bf16 v[148:151], v[28:31], a[140:143], v[148:151]\n"
  "v_exp_f32 v54, v54\n"
  "v_mfma_f32_16x16x32_bf16 v[176:179], v[44:47], a[136:139], v[176:179]\n"
  "v_exp_f32 v55, v55\n"
  "v_mfma_f32_16x16x32_bf16 v[180:183], v[44:47], a[140:143], v[180:183]\n"
  "buffer_load_dwordx4 v221, s[40:43], 0 offen offset:0 lds\n"
  "v_mfma_f32_16x16x32_bf16 v[208:211], v[60:63], a[136:139], v[208:211]\n"
  "s_addk_i32 m0, 896\n"
  "v_exp_f32 v38, v38\n"
  "v_mfma_f32_16x16x32_bf16 v[212:215], v[60:63], a[140:143], v[212:215]\n"
  "s_waitcnt lgkmcnt(4)\n"
  "v_exp_f32 v39, v39\n"
  "v_mfma_f32_16x16x32_bf16 v[64:67], a[132:135], a[96:99], v[248:251]\n"
  "s_add_u32 s71, s92, s81\n"
  "v_exp_f32 v6, v6\n"
  "v_mfma_f32_16x16x32_bf16 v[64:67], a[128:131], a[100:103], v[64:67]\n"
  "s_waitcnt lgkmcnt(2)\n"
  "buffer_load_dwordx4 v221, s[40:43], 0 offen offset:128 lds\n"
  "v_cvt_pk_bf16_f32 v59, v54, v55\n"
  "v_mfma_f32_16x16x32_bf16 v[64:67], a[124:127], a[104:107], v[64:67]\n"
  "s_addk_i32 m0, 896\n"
  "v_cvt_pk_bf16_f32 v43, v38, v39\n"
  "v_fma_f32 v7, v7, s73, v223\n"
  "v_mfma_f32_16x16x32_bf16 v[64:67], a[120:123], a[108:111], v[64:67]\n"
  "s_waitcnt lgkmcnt(0)\n"
  "v_exp_f32 v7, v7\n"
  "v_mfma_f32_16x16x32_bf16 v[64:67], a[144:147], a[112:115], v[64:67]\n"
  "v_cvt_pk_bf16_f32 v10, v4, v5\n"
  "v_cvt_pk_bf16_f32 v11, v6, v7\n"
  "v_mfma_f32_16x16x32_bf16 v[64:67], a[148:151], a[116:119], v[64:67]\n"
  "buffer_load_dwordx4 v221, s[40:43], 0 offen offset:256 lds\n"
  "v_add_f32 v252, v252, v4\n"
  "v_mfma_f32_16x16x32_bf16 v[0:3], a[148:151], a[20:23], v[248:251]\n"
  "s_add_u32 s40, s40, s31\n"
  "v_add_f32 v252, v252, v5\n"
  "v_add_f32 v252, v252, v6\n"
  "v_mfma_f32_16x16x32_bf16 v[0:3], a[144:147], a[16:19], v[0:3]\n"
  "s_addc_u32 s41, s41, 0\n"
  "v_add_f32 v252, v252, v7\n"
  "v_add_f32 v253, v253, v20\n"
  "v_mfma_f32_16x16x32_bf16 v[0:3], a[120:123], a[12:15], v[0:3]\n"
  "v_add_f32 v253, v253, v21\n"
  "v_add_f32 v253, v253, v22\n"
  "v_mfma_f32_16x16x32_bf16 v[0:3], a[124:127], a[8:11], v[0:3]\n"
  "s_add_u32 m0, s58, -128\n"
  "v_add_f32 v253, v253, v23\n"
  "v_add_f32 v254, v254, v36\n"
  "v_mfma_f32_16x16x32_bf16 v[0:3], a[128:131], a[4:7], v[0:3]\n"
  "v_add_f32 v254, v254, v37\n"
  "v_add_f32 v254, v254, v38\n"
  "v_mfma_f32_16x16x32_bf16 v[0:3], a[132:135], a[0:3], v[0:3]\n"
  "v_add_f32 v254, v254, v39\n"
  "v_bfe_u32 v237, v231, 1, 8\n"
  "v_mfma_f32_16x16x32_bf16 v[16:19], a[132:135], a[24:27], v[248:251]\n"
  "v_add_u32 v237, s29, v237\n"
  "v_add_f32 v255, v255, v52\n"
  "v_mfma_f32_16x16x32_bf16 v[16:19], a[128:131], a[28:31], v[16:19]\n"
  "buffer_load_dwordx4 v221, s[32:35], 0 offen offset:128 lds\n"
  "ds_read_b32 v236, v237\n"
  "v_mfma_f32_16x16x32_bf16 v[16:19], a[124:127], a[32:35], v[16:19]\n"
  "s_addk_i32 m0, 1024\n"
  "ds_read_b64_tr_b16 a[136:137], v217 offset:12352\n"
  "v_add_f32 v255, v255, v53\n"
  "v_mfma_f32_16x16x32_bf16 v[16:19], a[120:123], a[36:39], v[16:19]\n"
  "s_mov_b64 s[32:33], s[44:45]\n"
  "ds_read_b64_tr_b16 a[138:139], v218 offset:12352\n"
  "v_add_f32 v255, v255, v54\n"
  "v_mfma_f32_16x16x32_bf16 v[16:19], a[144:147], a[40:43], v[16:19]\n"
  "ds_read_b64_tr_b16 a[140:141], v217 offset:13376\n"
  "v_add_f32 v255, v255, v55\n"
  "v_mfma_f32_16x16x32_bf16 v[16:19], a[148:151], a[44:47], v[16:19]\n"
  "buffer_load_dwordx4 v221, s[44:47], 0 offen offset:128 lds\n"
  "v_fma_f32 v64, v64, s73, v227\n"
  "v_mfma_f32_16x16x32_bf16 v[32:35], a[148:151], a[68:71], v[248:251]\n"
  "s_addk_i32 m0, 896\n"
  "ds_read_b64_tr_b16 a[142:143], v218 offset:13376\n"
  "v_fma_f32 v65, v65, s73, v227\n"
  "v_mfma_f32_16x16x32_bf16 v[32:35], a[144:147], a[64:67], v[32:35]\n"
  "s_waitcnt lgkmcnt(4)\n"
  "v_exp_f32 v64, v64\n"
  "v_mfma_f32_16x16x32_bf16 v[32:35], a[120:123], a[60:63], v[32:35]\n"
  "v_exp_f32 v65, v65\n"
  "v_mfma_f32_16x16x32_bf16 v[32:35], a[124:127], a[56:59], v[32:35]\n"
  "buffer_load_dwordx4 v221, s[44:47], 0 offen offset:256 lds\n"
  "v_fma_f32 v66, v66, s73, v227\n"
  "v_mfma_f32_16x16x32_bf16 v[32:35], a[128:131], a[52:55], v[32:35]\n"
  "s_add_u32 s44, s44, s31\n"
  "v_exp_f32 v66, v66\n"
  "v_mfma_f32_16x16x32_bf16 v[32:35], a[132:135], a[48:51], v[32:35]\n"
  "s_addc_u32 s45, s45, 0\n"
  "v_fma_f32 v67, v67, s73, v227\n"
  "v_cvt_pk_bf16_f32 v64, v64, v65\n"
  "v_mfma_f32_16x16x32_bf16 v[48:51], a[132:135], a[72:75], v[248:251]\n"
  "ds_read_b64_tr_b16 a[132:133], v217 offset:12368\n"
  "v_fma_f32 v0, v0, s73, v223\n"
  "v_mfma_f32_16x16x32_bf16 v[48:51], a[128:131], a[76:79], v[48:51]\n"
  "ds_read_b64_tr_b16 a[134:135], v218 offset:12368\n"
  "v_fma_f32 v1, v1, s73, v223\n"
  "v_mfma_f32_16x16x32_bf16 v[48:51], a[124:127], a[80:83], v[48:51]\n"
  "ds_read_b64_tr_b16 a[128:129], v217 offset:13392\n"
  "v_fma_f32 v2, v2, s73, v223\n"
  "v_mfma_f32_16x16x32_bf16 v[48:51], a[120:123], a[84:87], v[48:51]\n"
  "s_add_u32 m0, s72, 19904\n"
  "ds_read_b64_tr_b16 a[130:131], v218 offset:13392\n"
  "v_mfma_f32_16x16x32_bf16 v[48:51], a[144:147], a[88:91], v[48:51]\n"
  "v_exp_f32 v67, v67\n"
  "v_mfma_f32_16x16x32_bf16 v[48:51], a[148:151], a[92:95], v[48:51]\n"
  "s_waitcnt lgkmcnt(4)\n"
  "v_exp_f32 v0, v0\n"
  "v_mfma_f32_16x16x32_bf16 a[236:239], a[160:163], a[136:139], a[236:239]\n"
  "v_exp_f32 v1, v1\n"
  "v_mfma_f32_16x16x32_bf16 a[236:239], a[156:159], a[140:143], a[236:239]\n"
  "buffer_load_dwordx4 v221, s[48:51], 0 offen offset:256 nt lds\n"
  "v_cvt_pk_bf16_f32 v65, v66, v67\n"
  "v_mfma_f32_16x16x32_bf16 v[184:187], v[60:63], a[136:139], v[184:187]\n"
  "s_add_u32 s48, s84, s71\n"
  "v_exp_f32 v2, v2\n"
  "v_mfma_f32_16x16x32_bf16 v[184:187], v[56:59], a[140:143], v[184:187]\n"
  "s_addc_u32 s49, s85, 0\n"
  "v_fma_f32 v3, v3, s73, v223\n"
  "v_add_f32 v252, v252, v0\n"
  "v_mfma_f32_16x16x32_bf16 v[152:155], v[44:47], a[136:139], v[152:155]\n"
  "s_sub_i32 s71, s38, s81\n"
  "ds_read_b64_tr_b16 a[124:125], v219 offset:12352\n"
  "v_add_f32 v252, v252, v1\n"
  "v_mfma_f32_16x16x32_bf16 v[152:155], v[40:43], a[140:143], v[152:155]\n"
  "s_add_u32 m0, s72, 8832\n"
  "ds_read_b64_tr_b16 a[126:127], v220 offset:12352\n"
  "v_add_f32 v252, v252, v2\n"
  "v_mfma_f32_16x16x32_bf16 v[120:123], v[28:31], a[136:139], v[120:123]\n"
  "s_max_i32 s50, s71, 0\n"
  "ds_read_b64_tr_b16 a[120:121], v219 offset:13376\n"
  "v_fma_f32 v16, v16, s73, v224\n"
  "v_mfma_f32_16x16x32_bf16 v[120:123], v[24:27], a[140:143], v[120:123]\n"
  "ds_read_b64_tr_b16 a[122:123], v220 offset:13376\n"
  "v_fma_f32 v17, v17, s73, v224\n"
  "v_mfma_f32_16x16x32_bf16 v[88:91], v[12:15], a[136:139], v[88:91]\n"
  "v_exp_f32 v3, v3\n"
  "v_mfma_f32_16x16x32_bf16 v[88:91], v[8:11], a[140:143], v[88:91]\n"
  "s_waitcnt lgkmcnt(4)\n"
  "buffer_load_dwordx4 v221, s[48:51], 0 offen offset:128 nt lds\n"
  "v_add_f32 v252, v252, v3\n"
  "v_mfma_f32_16x16x32_bf16 v[92:95], v[12:15], a[132:135], v[92:95]\n"
  "v_exp_f32 v16, v16\n"
  "v_mfma_f32_16x16x32_bf16 v[92:95], v[8:11], a[128:131], v[92:95]\n"
  "v_exp_f32 v17, v17\n"
  "v_mfma_f32_16x16x32_bf16 v[124:127], v[28:31], a[132:135], v[124:127]\n"
  "ds_read_b128 a[136:139], v235 offset:12352\n"
  "v_fma_f32 v18, v18, s73, v224\n"
  "v_mfma_f32_16x16x32_bf16 v[124:127], v[24:27], a[128:131], v[124:127]\n"
  "s_add_u32 m0, s72, 6848\n"
  "ds_read_b128 a[140:143], v235 offset:12416\n"
  "v_add_f32 v253, v253, v16\n"
  "v_mfma_f32_16x16x32_bf16 v[156:159], v[44:47], a[132:135], v[156:159]\n"
  "ds_read_b128 a[144:147], v235 offset:13376\n"
  "v_fma_f32 v19, v19, s73, v224\n"
  "v_mfma_f32_16x16x32_bf16 v[156:159], v[40:43], a[128:131], v[156:159]\n"
  "ds_read_b128 a[148:151], v235 offset:13440\n"
  "v_add_f32 v253, v253, v17\n"
  "v_mfma_f32_16x16x32_bf16 v[188:191], v[60:63], a[132:135], v[188:191]\n"
  "v_exp_f32 v18, v18\n"
  "v_mfma_f32_16x16x32_bf16 v[188:191], v[56:59], a[128:131], v[188:191]\n"
  "buffer_load_dwordx4 v221, s[48:51], 0 offen offset:3200 nt lds\n"
  "v_add_f32 v253, v253, v18\n"
  "v_mfma_f32_16x16x32_bf16 a[240:243], a[160:163], a[132:135], a[240:243]\n"
  "ds_read_b128 a[132:135], v235 offset:14400\n"
  "v_fma_f32 v32, v32, s73, v225\n"
  "v_mfma_f32_16x16x32_bf16 a[240:243], a[156:159], a[128:131], a[240:243]\n"
  "s_waitcnt lgkmcnt(5)\n"
  "ds_read_b128 a[128:131], v235 offset:14464\n"
  "v_fma_f32 v33, v33, s73, v225\n"
  "v_mfma_f32_16x16x32_bf16 v[192:195], v[60:63], a[124:127], v[192:195]\n"
  "v_exp_f32 v19, v19\n"
  "v_mfma_f32_16x16x32_bf16 v[192:195], v[56:59], a[120:123], v[192:195]\n"
  "ds_write_b64 v231, v[64:65] offset:2048\n"
  "v_mfma_f32_16x16x32_bf16 v[160:163], v[44:47], a[124:127], v[160:163]\n"
  "v_exp_f32 v32, v32\n"
  "v_mfma_f32_16x16x32_bf16 v[160:163], v[40:43], a[120:123], v[160:163]\n"
  "v_exp_f32 v33, v33\n"
  "v_mfma_f32_16x16x32_bf16 v[128:131], v[28:31], a[124:127], v[128:131]\n"
  "v_add_f32 v253, v253, v19\n"
  "v_fma_f32 v34, v34, s73, v225\n"
  "v_mfma_f32_16x16x32_bf16 v[128:131], v[24:27], a[120:123], v[128:131]\n"
  "v_exp_f32 v34, v34\n"
  "v_mfma_f32_16x16x32_bf16 v[96:99], v[12:15], a[124:127], v[96:99]\n"
  "v_fma_f32 v35, v35, s73, v225\n"
  "v_add_f32 v254, v254, v32\n"
  "v_mfma_f32_16x16x32_bf16 v[96:99], v[8:11], a[120:123], v[96:99]\n"
  "s_waitcnt lgkmcnt(5)\n"
  "v_exp_f32 v35, v35\n"
  "v_mfma_f32_16x16x32_bf16 v[4:7], a[136:139], a[0:3], v[248:251]\n"
  "v_add_f32 v254, v254, v33\n"
  "v_add_f32 v254, v254, v34\n"
  "v_mfma_f32_16x16x32_bf16 v[4:7], a[140:143], a[4:7], v[4:7]\n"
  "s_waitcnt lgkmcnt(3)\n"
  "v_add_f32 v254, v254, v35\n"
  "v_fma_f32 v48, v48, s73, v226\n"
  "v_mfma_f32_16x16x32_bf16 v[4:7], a[144:147], a[8:11], v[4:7]\n"
  "v_exp_f32 v48, v48\n"
  "v_mfma_f32_16x16x32_bf16 v[4:7], a[148:151], a[12:15], v[4:7]\n"
  "s_waitcnt lgkmcnt(1)\n"
  "v_fma_f32 v49, v49, s73, v226\n"
  "v_fma_f32 v50, v50, s73, v226\n"
  "v_mfma_f32_16x16x32_bf16 v[4:7], a[132:135], a[16:19], v[4:7]\n"
  "v_exp_f32 v49, v49\n"
  "v_mfma_f32_16x16x32_bf16 v[4:7], a[128:131], a[20:23], v[4:7]\n"
  "v_exp_f32 v50, v50\n"
  "v_mfma_f32_16x16x32_bf16 v[20:23], a[128:131], a[44:47], v[248:251]\n"
  "v_fma_f32 v51, v51, s73, v226\n"
  "v_add_f32 v255, v255, v48\n"
  "v_mfma_f32_16x16x32_bf16 v[20:23], a[132:135], a[40:43], v[20:23]\n"
  "v_exp_f32 v51, v51\n"
  "v_mfma_f32_16x16x32_bf16 v[20:23], a[148:151], a[36:39], v[20:23]\n"
  "ds_read_b64_tr_b16 a[124:125], v219 offset:12368\n"
  "v_add_f32 v255, v255, v49\n"
  "v_mfma_f32_16x16x32_bf16 v[20:23], a[144:147], a[32:35], v[20:23]\n"
  "ds_read_b64_tr_b16 a[126:127], v220 offset:12368\n"
  "v_add_f32 v255, v255, v50\n"
  "v_mfma_f32_16x16x32_bf16 v[20:23], a[140:143], a[28:31], v[20:23]\n"
  "ds_read_b64_tr_b16 a[120:121], v219 offset:13392\n"
  "v_add_f32 v255, v255, v51\n"
  "v_mfma_f32_16x16x32_bf16 v[20:23], a[136:139], a[24:27], v[20:23]\n"
  "ds_read_b64_tr_b16 a[122:123], v220 offset:13392\n"
  "v_fma_f32 v4, v4, s73, v223\n"
  "v_mfma_f32_16x16x32_bf16 v[36:39], a[136:139], a[48:51], v[248:251]\n"
  "v_exp_f32 v4, v4\n"
  "v_mfma_f32_16x16x32_bf16 v[36:39], a[140:143], a[52:55], v[36:39]\n"
  "v_fma_f32 v5, v5, s73, v223\n"
  "v_fma_f32 v6, v6, s73, v223\n"
  "v_mfma_f32_16x16x32_bf16 v[36:39], a[144:147], a[56:59], v[36:39]\n"
  "v_exp_f32 v5, v5\n"
  "v_mfma_f32_16x16x32_bf16 v[36:39], a[148:151], a[60:63], v[36:39]\n"
  "v_exp_f32 v6, v6\n"
  "v_mfma_f32_16x16x32_bf16 v[36:39], a[132:135], a[64:67], v[36:39]\n"
  "v_fma_f32 v20, v20, s73, v224\n"
  "v_fma_f32 v21, v21, s73, v224\n"
  "v_mfma_f32_16x16x32_bf16 v[36:39], a[128:131], a[68:71], v[36:39]\n"
  "v_exp_f32 v20, v20\n"
  "v_mfma_f32_16x16x32_bf16 v[52:55], a[128:131], a[92:95], v[248:251]\n"
  "ds_read_b64_tr_b16 a[128:129], v217 offset:14400\n"
  "v_fma_f32 v22, v22, s73, v224\n"
  "v_mfma_f32_16x16x32_bf16 v[52:55], a[132:135], a[88:91], v[52:55]\n"
  "ds_read_b64_tr_b16 a[130:131], v218 offset:14400\n"
  "v_fma_f32 v23, v23, s73, v224\n"
  "v_mfma_f32_16x16x32_bf16 v[52:55], a[148:151], a[84:87], v[52:55]\n"
  "ds_read_b64_tr_b16 a[132:133], v217 offset:14416\n"
  "v_fma_f32 v36, v36, s73, v225\n"
  "v_mfma_f32_16x16x32_bf16 v[52:55], a[144:147], a[80:83], v[52:55]\n"
  "ds_read_b64_tr_b16 a[134:135], v218 offset:14416\n"
  "v_fma_f32 v37, v37, s73, v225\n"
  "v_mfma_f32_16x16x32_bf16 v[52:55], a[140:143], a[76:79], v[52:55]\n"
  "v_exp_f32 v36, v36\n"
  "v_mfma_f32_16x16x32_bf16 v[52:55], a[136:139], a[72:75], v[52:55]\n"
  "s_waitcnt lgkmcnt(4)\n"
  "v_exp_f32 v37, v37\n"
  "v_mfma_f32_16x16x32_bf16 v[100:103], v[12:15], a[124:127], v[100:103]\n"
  "v_cvt_pk_bf16_f32 v12, v0, v1\n"
  "v_cvt_pk_bf16_f32 v13, v2, v3\n"
  "v_mfma_f32_16x16x32_bf16 v[100:103], v[8:11], a[120:123], v[100:103]\n"
  "v_exp_f32 v21, v21\n"
  "v_mfma_f32_16x16x32_bf16 v[132:135], v[28:31], a[124:127], v[132:135]\n"
  "ds_read_b64_tr_b16 a[140:141], v219 offset:14400\n"
  "v_cvt_pk_bf16_f32 v28, v16, v17\n"
  "v_mfma_f32_16x16x32_bf16 v[132:135], v[24:27], a[120:123], v[132:135]\n"
  "ds_read_b64_tr_b16 a[142:143], v220 offset:14400\n"
  "v_cvt_pk_bf16_f32 v29, v18, v19\n"
  "v_mfma_f32_16x16x32_bf16 v[164:167], v[44:47], a[124:127], v[164:167]\n"
  "ds_read_b64_tr_b16 a[136:137], v219 offset:14416\n"
  "v_cvt_pk_bf16_f32 v44, v32, v33\n"
  "v_mfma_f32_16x16x32_bf16 v[164:167], v[40:43], a[120:123], v[164:167]\n"
  "ds_read_b64_tr_b16 a[138:139], v220 offset:14416\n"
  "v_cvt_pk_bf16_f32 v45, v34, v35\n"
  "v_mfma_f32_16x16x32_bf16 v[196:199], v[60:63], a[124:127], v[196:199]\n"
  "v_exp_f32 v22, v22\n"
  "v_mfma_f32_16x16x32_bf16 v[196:199], v[56:59], a[120:123], v[196:199]\n"
  "s_waitcnt lgkmcnt(4)\n"
  "v_exp_f32 v23, v23\n"
  "v_mfma_f32_16x16x32_bf16 a[244:247], a[156:159], a[128:131], a[244:247]\n"
  "v_cvt_pk_bf16_f32 v60, v48, v49\n"
  "v_cvt_pk_bf16_f32 v61, v50, v51\n"
  "v_mfma_f32_16x16x32_bf16 a[248:251], a[156:159], a[132:135], a[248:251]\n"
  "v_fma_f32 v52, v52, s73, v226\n"
  "v_fma_f32 v53, v53, s73, v226\n"
  "v_mfma_f32_16x16x32_bf16 v[200:203], v[56:59], a[128:131], v[200:203]\n"
  "v_exp_f32 v52, v52\n"
  "v_mfma_f32_16x16x32_bf16 v[204:207], v[56:59], a[132:135], v[204:207]\n"
  "v_exp_f32 v53, v53\n"
  "v_mfma_f32_16x16x32_bf16 v[168:171], v[40:43], a[128:131], v[168:171]\n"
  "v_fma_f32 v54, v54, s73, v226\n"
  "v_fma_f32 v55, v55, s73, v226\n"
  "v_mfma_f32_16x16x32_bf16 v[172:175], v[40:43], a[132:135], v[172:175]\n"
  "s_waitcnt vmcnt(12) lgkmcnt(0)\n"
  "s_barrier\n"
  "ds_read_b128 a[124:127], v216 offset:24704\n"
  "v_mfma_f32_16x16x32_bf16 v[136:139], v[24:27], a[128:131], v[136:139]\n"
  "ds_read_b64 a[160:161], v231 offset:2048\n"
  "v_cvt_pk_bf16_f32 v62, v52, v53\n"
  "v_mfma_f32_16x16x32_bf16 v[140:143], v[24:27], a[132:135], v[140:143]\n"
  "ds_read_b64 a[162:163], v232 offset:2048\n"
  "v_fma_f32 v38, v38, s73, v225\n"
  "v_mfma_f32_16x16x32_bf16 v[104:107], v[8:11], a[128:131], v[104:107]\n"
  "ds_read_b128 a[120:123], v216 offset:24768\n"
  "v_fma_f32 v39, v39, s73, v225\n"
  "v_mfma_f32_16x16x32_bf16 v[108:111], v[8:11], a[132:135], v[108:111]\n"
  "ds_read_b128 a[128:131], v216 offset:25728\n"
  "v_cvt_pk_bf16_f32 v46, v36, v37\n"
  "v_mfma_f32_16x16x32_bf16 v[112:115], v[8:11], a[140:143], v[112:115]\n"
  "ds_read_b128 a[132:135], v216 offset:25792\n"
  "v_cvt_pk_bf16_f32 v30, v20, v21\n"
  "v_mfma_f32_16x16x32_bf16 v[116:119], v[8:11], a[136:139], v[116:119]\n"
  "s_mov_b32 m0, s67\n"
  "ds_read_b128 a[148:151], v216 offset:26752\n"
  "v_cvt_pk_bf16_f32 v31, v22, v23\n"
  "v_mfma_f32_16x16x32_bf16 v[144:147], v[24:27], a[140:143], v[144:147]\n"
  "ds_read_b128 a[144:147], v216 offset:26816\n"
  "v_fma_f32 v7, v7, s73, v223\n"
  "v_mfma_f32_16x16x32_bf16 v[148:151], v[24:27], a[136:139], v[148:151]\n"
  "v_exp_f32 v54, v54\n"
  "v_mfma_f32_16x16x32_bf16 v[176:179], v[40:43], a[140:143], v[176:179]\n"
  "v_exp_f32 v55, v55\n"
  "v_mfma_f32_16x16x32_bf16 v[180:183], v[40:43], a[136:139], v[180:183]\n"
  "buffer_load_dwordx4 v221, s[40:43], 0 offen offset:0 lds\n"
  "v_mfma_f32_16x16x32_bf16 v[208:211], v[56:59], a[140:143], v[208:211]\n"
  "s_addk_i32 m0, 896\n"
  "v_exp_f32 v38, v38\n"
  "v_mfma_f32_16x16x32_bf16 v[212:215], v[56:59], a[136:139], v[212:215]\n"
  "s_waitcnt lgkmcnt(4)\n"
  "v_exp_f32 v39, v39\n"
  "v_mfma_f32_16x16x32_bf16 v[64:67], a[124:127], a[96:99], v[248:251]\n"
  "v_exp_f32 v7, v7\n"
  "v_mfma_f32_16x16x32_bf16 v[64:67], a[120:123], a[100:103], v[64:67]\n"
  "s_waitcnt lgkmcnt(2)\n"
  "buffer_load_dwordx4 v221, s[40:43], 0 offen offset:128 lds\n"
  "v_cvt_pk_bf16_f32 v63, v54, v55\n"
  "v_mfma_f32_16x16x32_bf16 v[64:67], a[128:131], a[104:107], v[64:67]\n"
  "s_addk_i32 m0, 896\n"
  "v_cvt_pk_bf16_f32 v47, v38, v39\n"
  "v_cvt_pk_bf16_f32 v14, v4, v5\n"
  "v_mfma_f32_16x16x32_bf16 v[64:67], a[132:135], a[108:111], v[64:67]\n"
  "s_waitcnt lgkmcnt(0)\n"
  "v_cvt_pk_bf16_f32 v15, v6, v7\n"
  "v_add_f32 v252, v252, v4\n"
  "v_mfma_f32_16x16x32_bf16 v[64:67], a[148:151], a[112:115], v[64:67]\n"
  "v_add_f32 v252, v252, v5\n"
  "v_add_f32 v252, v252, v6\n"
  "v_mfma_f32_16x16x32_bf16 v[64:67], a[144:147], a[116:119], v[64:67]\n"
  "buffer_load_dwordx4 v221, s[40:43], 0 offen offset:256 lds\n"
  "v_add_f32 v252, v252, v7\n"
  "v_mfma_f32_16x16x32_bf16 v[0:3], a[144:147], a[20:23], v[248:251]\n"
  "s_add_u32 s40, s40, s31\n"
  "v_add_f32 v253, v253, v20\n"
  "v_add_f32 v253, v253, v21\n"
  "v_mfma_f32_16x16x32_bf16 v[0:3], a[148:151], a[16:19], v[0:3]\n"
  "s_addc_u32 s41, s41, 0\n"
  "v_add_f32 v253, v253, v22\n"
  "v_add_f32 v253, v253, v23\n"
  "v_mfma_f32_16x16x32_bf16 v[0:3], a[132:135], a[12:15], v[0:3]\n"
  "v_add_f32 v254, v254, v36\n"
  "v_add_f32 v254, v254, v37\n"
  "v_mfma_f32_16x16x32_bf16 v[0:3], a[128:131], a[8:11], v[0:3]\n"
  "s_mov_b32 m0, s59\n"
  "v_add_f32 v254, v254, v38\n"
  "v_add_f32 v254, v254, v39\n"
  "v_mfma_f32_16x16x32_bf16 v[0:3], a[120:123], a[4:7], v[0:3]\n"
  "v_add_f32 v255, v255, v52\n"
  "v_add_f32 v255, v255, v53\n"
  "v_mfma_f32_16x16x32_bf16 v[0:3], a[124:127], a[0:3], v[0:3]\n"
  "v_add_f32 v255, v255, v54\n"
  "v_add_f32 v255, v255, v55\n"
  "v_mfma_f32_16x16x32_bf16 v[16:19], a[124:127], a[24:27], v[248:251]\n"
  "ds_read_b64_tr_b16 v[244:245], v230 offset:11200\n"
  "ds_read_b64_tr_b16 v[246:247], v230 offset:11232\n"
  "v_mfma_f32_16x16x32_bf16 v[16:19], a[120:123], a[28:31], v[16:19]\n"
  "buffer_load_dwordx4 v221, s[32:35], 0 offen offset:0 lds\n"
  "ds_read_b64_tr_b16 a[252:253], v230 offset:12320\n"
  "v_mfma_f32_16x16x32_bf16 v[16:19], a[128:131], a[32:35], v[16:19]\n"
  "s_addk_i32 m0, 1024\n"
  "ds_read_b64_tr_b16 a[140:141], v217 offset:24704\n"
  "ds_read_b64_tr_b16 a[254:255], v230 offset:12352\n"
  "v_mfma_f32_16x16x32_bf16 v[16:19], a[132:135], a[36:39], v[16:19]\n"
  "s_mov_b64 s[32:33], s[44:45]\n"
  "ds_read_b64_tr_b16 a[142:143], v218 offset:24704\n"
  "v_fma_f32 v64, v64, s73, v227\n"
  "v_mfma_f32_16x16x32_bf16 v[16:19], a[148:151], a[40:43], v[16:19]\n"
  "ds_read_b64_tr_b16 a[136:137], v217 offset:25728\n"
  "v_fma_f32 v65, v65, s73, v227\n"
  "v_mfma_f32_16x16x32_bf16 v[16:19], a[144:147], a[44:47], v[16:19]\n"
  "buffer_load_dwordx4 v221, s[44:47], 0 offen offset:0 lds\n"
  "v_fma_f32 v66, v66, s73, v227\n"
  "v_mfma_f32_16x16x32_bf16 v[32:35], a[144:147], a[68:71], v[248:251]\n"
  "s_addk_i32 m0, 768\n"
  "ds_read_b64_tr_b16 a[138:139], v218 offset:25728\n"
  "v_fma_f32 v67, v67, s73, v227\n"
  "v_mfma_f32_16x16x32_bf16 v[32:35], a[148:151], a[64:67], v[32:35]\n"
  "v_exp_f32 v64, v64\n"
  "v_mfma_f32_16x16x32_bf16 v[32:35], a[132:135], a[60:63], v[32:35]\n"
  "v_exp_f32 v65, v65\n"
  "v_mfma_f32_16x16x32_bf16 v[32:35], a[128:131], a[56:59], v[32:35]\n"
  "buffer_load_dwordx4 v221, s[44:47], 0 offen offset:256 lds\n"
  "v_cvt_pk_bf16_f32 v64, v64, v65\n"
  "v_mfma_f32_16x16x32_bf16 v[32:35], a[120:123], a[52:55], v[32:35]\n"
  "s_add_u32 s44, s44, s31\n"
  "v_exp_f32 v66, v66\n"
  "v_mfma_f32_16x16x32_bf16 v[32:35], a[124:127], a[48:51], v[32:35]\n"
  "s_addc_u32 s45, s45, 0\n"
  "v_exp_f32 v67, v67\n"
  "v_mfma_f32_16x16x32_bf16 v[48:51], a[124:127], a[72:75], v[248:251]\n"
  "v_cvt_pk_bf16_f32 v65, v66, v67\n"
  "v_fma_f32 v0, v0, s73, v223\n"
  "v_mfma_f32_16x16x32_bf16 v[48:51], a[120:123], a[76:79], v[48:51]\n"
  "v_exp_f32 v0, v0\n"
  "v_mfma_f32_16x16x32_bf16 v[48:51], a[128:131], a[80:83], v[48:51]\n"
  "ds_read_b64_tr_b16 a[124:125], v217 offset:24720\n"
  "v_fma_f32 v1, v1, s73, v223\n"
  "v_mfma_f32_16x16x32_bf16 v[48:51], a[132:135], a[84:87], v[48:51]\n"
  "ds_read_b64_tr_b16 a[126:127], v218 offset:24720\n"
  "v_fma_f32 v2, v2, s73, v223\n"
  "v_mfma_f32_16x16x32_bf16 v[48:51], a[148:151], a[88:91], v[48:51]\n"
  "ds_read_b64_tr_b16 a[120:121], v217 offset:25744\n"
  "v_fma_f32 v3, v3, s73, v223\n"
  "v_mfma_f32_16x16x32_bf16 v[48:51], a[144:147], a[92:95], v[48:51]\n"
  "s_waitcnt lgkmcnt(3)\n"
  "ds_read_b64_tr_b16 a[122:123], v218 offset:25744\n"
  "v_add_f32 v252, v252, v0\n"
  "v_mfma_f32_16x16x32_bf16 a[228:231], a[156:159], a[140:143], a[228:231]\n"
  "v_exp_f32 v1, v1\n"
  "v_mfma_f32_16x16x32_bf16 a[228:231], a[160:163], a[136:139], a[228:231]\n"
  "v_exp_f32 v2, v2\n"
  "v_mfma_f32_16x16x32_bf16 a[212:215], v[56:59], a[140:143], a[212:215]\n"
  "v_exp_f32 v3, v3\n"
  "v_mfma_f32_16x16x32_bf16 a[212:215], v[60:63], a[136:139], a[212:215]\n"
  "v_add_f32 v252, v252, v1\n"
  "v_add_f32 v252, v252, v2\n"
  "v_mfma_f32_16x16x32_bf16 a[196:199], v[40:43], a[140:143], a[196:199]\n"
  "v_add_f32 v252, v252, v3\n"
  "v_fma_f32 v16, v16, s73, v224\n"
  "v_mfma_f32_16x16x32_bf16 a[196:199], v[44:47], a[136:139], a[196:199]\n"
  "v_exp_f32 v16, v16\n"
  "v_mfma_f32_16x16x32_bf16 a[180:183], v[24:27], a[140:143], a[180:183]\n"
  "ds_read_b64_tr_b16 a[128:129], v219 offset:24704\n"
  "v_fma_f32 v17, v17, s73, v224\n"
  "v_mfma_f32_16x16x32_bf16 a[180:183], v[28:31], a[136:139], a[180:183]\n"
  "ds_read_b64_tr_b16 a[130:131], v220 offset:24704\n"
  "v_fma_f32 v18, v18, s73, v224\n"
  "v_mfma_f32_16x16x32_bf16 a[164:167], v[8:11], a[140:143], a[164:167]\n"
  "ds_read_b64_tr_b16 a[132:133], v219 offset:25728\n"
  "v_fma_f32 v19, v19, s73, v224\n"
  "v_mfma_f32_16x16x32_bf16 a[164:167], v[12:15], a[136:139], a[164:167]\n"
  "ds_read_b64_tr_b16 a[134:135], v220 offset:25728\n"
  "v_add_f32 v253, v253, v16\n"
  "v_mfma_f32_16x16x32_bf16 v[84:87], a[156:159], a[152:155], v[84:87]\n"
  "v_exp_f32 v17, v17\n"
  "v_mfma_f32_16x16x32_bf16 v[84:87], a[160:163], a[152:155], v[84:87]\n"
  "s_waitcnt lgkmcnt(4)\n"
  "v_exp_f32 v18, v18\n"
  "v_mfma_f32_16x16x32_bf16 a[168:171], v[8:11], a[124:127], a[168:171]\n"
  "v_exp_f32 v19, v19\n"
  "v_mfma_f32_16x16x32_bf16 a[168:171], v[12:15], a[120:123], a[168:171]\n"
  "v_add_f32 v253, v253, v17\n"
  "v_add_f32 v253, v253, v18\n"
  "v_mfma_f32_16x16x32_bf16 a[184:187], v[24:27], a[124:127], a[184:187]\n"
  "ds_read_b128 a[140:143], v235 offset:24704\n"
  "v_add_f32 v253, v253, v19\n"
  "v_mfma_f32_16x16x32_bf16 a[184:187], v[28:31], a[120:123], a[184:187]\n"
  "ds_read_b128 a[136:139], v235 offset:24768\n"
  "v_fma_f32 v32, v32, s73, v225\n"
  "v_mfma_f32_16x16x32_bf16 a[200:203], v[40:43], a[124:127], a[200:203]\n"
  "ds_read_b128 a[148:151], v235 offset:25728\n"
  "v_fma_f32 v33, v33, s73, v225\n"
  "v_mfma_f32_16x16x32_bf16 a[200:203], v[44:47], a[120:123], a[200:203]\n"
  "ds_read_b128 a[144:147], v235 offset:25792\n"
  "v_fma_f32 v34, v34, s73, v225\n"
  "v_mfma_f32_16x16x32_bf16 a[216:219], v[56:59], a[124:127], a[216:219]\n"
  "v_exp_f32 v32, v32\n"
  "v_mfma_f32_16x16x32_bf16 a[216:219], v[60:63], a[120:123], a[216:219]\n"
  "v_exp_f32 v33, v33\n"
  "v_mfma_f32_16x16x32_bf16 a[232:235], a[156:159], a[124:127], a[232:235]\n"
  "ds_read_b128 a[124:127], v235 offset:26752\n"
  "v_fma_f32 v35, v35, s73, v225\n"
  "v_mfma_f32_16x16x32_bf16 a[232:235], a[160:163], a[120:123], a[232:235]\n"
  "s_waitcnt lgkmcnt(5)\n"
  "ds_read_b128 a[120:123], v235 offset:26816\n"
  "v_add_f32 v254, v254, v32\n"
  "v_mfma_f32_16x16x32_bf16 a[220:223], v[56:59], a[128:131], a[220:223]\n"
  "v_exp_f32_e64 v34, v34\n"
  "v_mfma_f32_16x16x32_bf16 a[220:223], v[60:63], a[132:135], a[220:223]\n"
  "ds_write_b64 v231, v[64:65] offset:0\n"
  "v_mfma_f32_16x16x32_bf16 a[204:207], v[40:43], a[128:131], a[204:207]\n"
  "v_exp_f32 v35, v35\n"
  "v_mfma_f32_16x16x32_bf16 a[204:207], v[44:47], a[132:135], a[204:207]\n"
  "buffer_store_dwordx4 v[244:247], v228, s[52:55], s81 offen offset:128 nt\n"
  "v_add_f32 v254, v254, v33\n"
  "v_add_f32 v254, v254, v34\n"
  "v_add_f32 v254, v254, v35\n"
  "v_mfma_f32_16x16x32_bf16 a[188:191], v[24:27], a[128:131], a[188:191]\n"
  "ds_read_b64_tr_b16 v[244:245], v230 offset:13440\n"
  "ds_read_b64_tr_b16 v[246:247], v230 offset:13472\n"
  "v_mfma_f32_16x16x32_bf16 a[188:191], v[28:31], a[132:135], a[188:191]\n"
  "v_fma_f32 v48, v48, s73, v226\n"
  "v_fma_f32 v49, v49, s73, v226\n"
  "v_mfma_f32_16x16x32_bf16 a[172:175], v[8:11], a[128:131], a[172:175]\n"
  "v_exp_f32 v48, v48\n"
  "v_mfma_f32_16x16x32_bf16 a[172:175], v[12:15], a[132:135], a[172:175]\n"
  "s_waitcnt lgkmcnt(7)\n"
  "v_exp_f32 v49, v49\n"
  "v_mfma_f32_16x16x32_bf16 v[4:7], a[140:143], a[0:3], v[248:251]\n"
  "v_fma_f32 v50, v50, s73, v226\n"
  "v_fma_f32 v51, v51, s73, v226\n"
  "v_mfma_f32_16x16x32_bf16 v[4:7], a[136:139], a[4:7], v[4:7]\n"
  "s_waitcnt lgkmcnt(5)\n"
  "v_exp_f32 v50, v50\n"
  "v_mfma_f32_16x16x32_bf16 v[4:7], a[148:151], a[8:11], v[4:7]\n"
  "v_exp_f32 v51, v51\n"
  "v_mfma_f32_16x16x32_bf16 v[4:7], a[144:147], a[12:15], v[4:7]\n"
  "s_waitcnt lgkmcnt(3)\n"
  "v_add_f32 v255, v255, v48\n"
  "v_add_f32 v255, v255, v49\n"
  "v_mfma_f32_16x16x32_bf16 v[4:7], a[124:127], a[16:19], v[4:7]\n"
  "v_add_f32 v255, v255, v50\n"
  "v_add_f32_e64 v255, v255, v51\n"
  "v_mfma_f32_16x16x32_bf16 v[4:7], a[120:123], a[20:23], v[4:7]\n"
  "buffer_store_dwordx4 a[252:255], v228, s[52:55], s81 offen offset:3200 nt\n"
  "v_and_b32 v236, 0x7fff7fff, v236\n"
  "v_lshlrev_b32 v237, 16, v236\n"
  "v_max_u32 v236, v236, v237\n"
  "v_mfma_f32_16x16x32_bf16 v[20:23], a[120:123], a[44:47], v[248:251]\n"
  "ds_read_b64_tr_b16 a[252:253], v230 offset:14560\n"
  "ds_read_b64_tr_b16 a[254:255], v230 offset:14592\n"
  "v_mfma_f32_16x16x32_bf16 v[20:23], a[124:127], a[40:43], v[20:23]\n"
  "v_max_u32_dpp v236, v236, v236 quad_perm:[1,0,3,2] row_mask:0xf bank_mask:0xf\n"
  "v_mfma_f32_16x16x32_bf16 v[20:23], a[144:147], a[36:39], v[20:23]\n"
  "ds_read_b64_tr_b16 a[128:129], v219 offset:24720\n"
  "v_fma_f32 v4, v4, s73, v223\n"
  "v_mfma_f32_16x16x32_bf16 v[20:23], a[148:151], a[32:35], v[20:23]\n"
  "ds_read_b64_tr_b16 a[130:131], v220 offset:24720\n"
  "v_fma_f32 v5, v5, s73, v223\n"
  "v_mfma_f32_16x16x32_bf16 v[20:23], a[136:139], a[28:31], v[20:23]\n"
  "ds_read_b64_tr_b16 a[132:133], v219 offset:25744\n"
  "v_fma_f32 v6, v6, s73, v223\n"
  "v_mfma_f32_16x16x32_bf16 v[20:23], a[140:143], a[24:27], v[20:23]\n"
  "s_waitcnt lgkmcnt(5)\n"
  "buffer_store_dwordx4 v[244:247], v228, s[52:55], s82 offen offset:128 nt\n"
  "ds_read_b64_tr_b16 a[134:135], v220 offset:25744\n"
  "v_exp_f32 v4, v4\n"
  "v_mfma_f32_16x16x32_bf16 v[36:39], a[140:143], a[48:51], v[248:251]\n"
  "v_exp_f32 v5, v5\n"
  "v_mfma_f32_16x16x32_bf16 v[36:39], a[136:139], a[52:55], v[36:39]\n"
  "v_exp_f32 v6, v6\n"
  "v_mfma_f32_16x16x32_bf16 v[36:39], a[148:151], a[56:59], v[36:39]\n"
  "v_fma_f32 v20, v20, s73, v224\n"
  "v_fma_f32 v21, v21, s73, v224\n"
  "v_mfma_f32_16x16x32_bf16 v[36:39], a[144:147], a[60:63], v[36:39]\n"
  "v_exp_f32 v20, v20\n"
  "v_mfma_f32_16x16x32_bf16 v[36:39], a[124:127], a[64:67], v[36:39]\n"
  "v_exp_f32 v21, v21\n"
  "v_mfma_f32_16x16x32_bf16 v[36:39], a[120:123], a[68:71], v[36:39]\n"
  "v_fma_f32 v22, v22, s73, v224\n"
  "v_fma_f32 v23, v23, s73, v224\n"
  "v_mfma_f32_16x16x32_bf16 v[52:55], a[120:123], a[92:95], v[248:251]\n"
  "ds_read_b64_tr_b16 a[120:121], v217 offset:26752\n"
  "v_fma_f32 v7, v7, s73, v223\n"
  "v_mfma_f32_16x16x32_bf16 v[52:55], a[124:127], a[88:91], v[52:55]\n"
  "ds_read_b64_tr_b16 a[122:123], v218 offset:26752\n"
  "v_add_f32 v252, v252, v4\n"
  "v_mfma_f32_16x16x32_bf16 v[52:55], a[144:147], a[84:87], v[52:55]\n"
  "ds_read_b64_tr_b16 a[124:125], v217 offset:26768\n"
  "v_fma_f32 v36, v36, s73, v225\n"
  "v_mfma_f32_16x16x32_bf16 v[52:55], a[148:151], a[80:83], v[52:55]\n"
  "ds_read_b64_tr_b16 a[126:127], v218 offset:26768\n"
  "v_fma_f32 v37, v37, s73, v225\n"
  "v_mfma_f32_16x16x32_bf16 v[52:55], a[136:139], a[76:79], v[52:55]\n"
  "v_exp_f32 v36, v36\n"
  "v_mfma_f32_16x16x32_bf16 v[52:55], a[140:143], a[72:75], v[52:55]\n"
  "s_waitcnt lgkmcnt(4)\n"
  "v_exp_f32 v37, v37\n"
  "v_mfma_f32_16x16x32_bf16 a[176:179], v[8:11], a[128:131], a[176:179]\n"
  "v_cvt_pk_bf16_f32 v8, v0, v1\n"
  "v_cvt_pk_bf16_f32 v9, v2, v3\n"
  "v_mfma_f32_16x16x32_bf16 a[176:179], v[12:15], a[132:135], a[176:179]\n"
  "v_exp_f32 v22, v22\n"
  "v_mfma_f32_16x16x32_bf16 a[192:195], v[24:27], a[128:131], a[192:195]\n"
  "ds_read_b64_tr_b16 a[136:137], v219 offset:26752\n"
  "v_cvt_pk_bf16_f32 v24, v16, v17\n"
  "v_mfma_f32_16x16x32_bf16 a[192:195], v[28:31], a[132:135], a[192:195]\n"
  "ds_read_b64_tr_b16 a[138:139], v220 offset:26752\n"
  "v_cvt_pk_bf16_f32 v25, v18, v19\n"
  "v_mfma_f32_16x16x32_bf16 a[208:211], v[40:43], a[128:131], a[208:211]\n"
  "ds_read_b64_tr_b16 a[140:141], v219 offset:26768\n"
  "v_cvt_pk_bf16_f32 v40, v32, v33\n"
  "v_mfma_f32_16x16x32_bf16 a[208:211], v[44:47], a[132:135], a[208:211]\n"
  "ds_read_b64_tr_b16 a[142:143], v220 offset:26768\n"
  "v_cvt_pk_bf16_f32 v41, v34, v35\n"
  "v_mfma_f32_16x16x32_bf16 a[224:227], v[56:59], a[128:131], a[224:227]\n"
  "v_exp_f32 v23, v23\n"
  "v_mfma_f32_16x16x32_bf16 a[224:227], v[60:63], a[132:135], a[224:227]\n"
  "s_waitcnt lgkmcnt(4)\n"
  "v_exp_f32 v7, v7\n"
  "v_mfma_f32_16x16x32_bf16 a[244:247], a[160:163], a[120:123], a[244:247]\n"
  "v_cvt_pk_bf16_f32 v56, v48, v49\n"
  "v_cvt_pk_bf16_f32 v57, v50, v51\n"
  "v_mfma_f32_16x16x32_bf16 a[248:251], a[160:163], a[124:127], a[248:251]\n"
  "v_fma_f32 v52, v52, s73, v226\n"
  "v_fma_f32 v53, v53, s73, v226\n"
  "v_mfma_f32_16x16x32_bf16 v[200:203], v[60:63], a[120:123], v[200:203]\n"
  "v_exp_f32 v52, v52\n"
  "v_mfma_f32_16x16x32_bf16 v[204:207], v[60:63], a[124:127], v[204:207]\n"
  "v_exp_f32 v53, v53\n"
  "v_mfma_f32_16x16x32_bf16 v[168:171], v[44:47], a[120:123], v[168:171]\n"
  "v_fma_f32 v54, v54, s73, v226\n"
  "v_fma_f32 v55, v55, s73, v226\n"
  "v_mfma_f32_16x16x32_bf16 v[172:175], v[44:47], a[124:127], v[172:175]\n"
  "s_waitcnt vmcnt(12) lgkmcnt(0)\n"
  "s_barrier\n"
  "ds_read_b128 a[128:131], v216 offset:0\n"
  "v_mfma_f32_16x16x32_bf16 v[136:139], v[28:31], a[120:123], v[136:139]\n"
  "ds_read_b64 a[156:157], v231 offset:0\n"
  "v_cvt_pk_bf16_f32 v58, v52, v53\n"
  "v_mfma_f32_16x16x32_bf16 v[140:143], v[28:31], a[124:127], v[140:143]\n"
  "ds_read_b64 a[158:159], v232 offset:0\n"
  "v_fma_f32 v38, v38, s73, v225\n"
  "v_mfma_f32_16x16x32_bf16 v[104:107], v[12:15], a[120:123], v[104:107]\n"
  "ds_read_b128 a[132:135], v216 offset:64\n"
  "v_fma_f32 v39, v39, s73, v225\n"
  "v_mfma_f32_16x16x32_bf16 v[108:111], v[12:15], a[124:127], v[108:111]\n"
  "ds_read_b128 a[120:123], v216 offset:1024\n"
  "v_cvt_pk_bf16_f32 v42, v36, v37\n"
  "v_mfma_f32_16x16x32_bf16 v[112:115], v[12:15], a[136:139], v[112:115]\n"
  "ds_read_b128 a[124:127], v216 offset:1088\n"
  "v_cvt_pk_bf16_f32 v26, v20, v21\n"
  "v_mfma_f32_16x16x32_bf16 v[116:119], v[12:15], a[140:143], v[116:119]\n"
  "s_mov_b32 m0, s68\n"
  "ds_read_b128 a[144:147], v216 offset:2048\n"
  "v_cvt_pk_bf16_f32 v27, v22, v23\n"
  "v_mfma_f32_16x16x32_bf16 v[144:147], v[28:31], a[136:139], v[144:147]\n"
  "ds_read_b128 a[148:151], v216 offset:2112\n"
  "v_cvt_pk_bf16_f32 v10, v4, v5\n"
  "v_mfma_f32_16x16x32_bf16 v[148:151], v[28:31], a[140:143], v[148:151]\n"
  "v_exp_f32 v54, v54\n"
  "v_mfma_f32_16x16x32_bf16 v[176:179], v[44:47], a[136:139], v[176:179]\n"
  "v_exp_f32 v55, v55\n"
  "v_mfma_f32_16x16x32_bf16 v[180:183], v[44:47], a[140:143], v[180:183]\n"
  "buffer_load_dwordx4 v221, s[40:43], 0 offen offset:0 lds\n"
  "v_mfma_f32_16x16x32_bf16 v[208:211], v[60:63], a[136:139], v[208:211]\n"
  "s_addk_i32 m0, 896\n"
  "v_exp_f32 v38, v38\n"
  "v_mfma_f32_16x16x32_bf16 v[212:215], v[60:63], a[140:143], v[212:215]\n"
  "s_waitcnt lgkmcnt(4)\n"
  "v_exp_f32 v39, v39\n"
  "v_mfma_f32_16x16x32_bf16 v[64:67], a[128:131], a[96:99], v[248:251]\n"
  "v_cvt_pk_bf16_f32 v59, v54, v55\n"
  "v_cvt_pk_bf16_f32 v43, v38, v39\n"
  "v_mfma_f32_16x16x32_bf16 v[64:67], a[132:135], a[100:103], v[64:67]\n"
  "s_waitcnt lgkmcnt(2)\n"
  "buffer_load_dwordx4 v221, s[40:43], 0 offen offset:128 lds\n"
  "v_cvt_pk_bf16_f32 v11, v6, v7\n"
  "v_mfma_f32_16x16x32_bf16 v[64:67], a[120:123], a[104:107], v[64:67]\n"
  "s_addk_i32 m0, 896\n"
  "v_add_f32 v252, v252, v5\n"
  "v_add_f32 v252, v252, v6\n"
  "v_mfma_f32_16x16x32_bf16 v[64:67], a[124:127], a[108:111], v[64:67]\n"
  "s_waitcnt lgkmcnt(0)\n"
  "v_add_f32 v252, v252, v7\n"
  "v_add_f32 v253, v253, v20\n"
  "v_mfma_f32_16x16x32_bf16 v[64:67], a[144:147], a[112:115], v[64:67]\n"
  "v_add_f32 v253, v253, v21\n"
  "v_add_f32 v253, v253, v22\n"
  "v_mfma_f32_16x16x32_bf16 v[64:67], a[148:151], a[116:119], v[64:67]\n"
  "buffer_load_dwordx4 v221, s[40:43], 0 offen offset:256 lds\n"
  "v_add_f32 v253, v253, v23\n"
  "v_mfma_f32_16x16x32_bf16 v[0:3], a[148:151], a[20:23], v[248:251]\n"
  "s_add_u32 s40, s40, s31\n"
  "v_add_f32 v254, v254, v36\n"
  "v_add_f32 v254, v254, v37\n"
  "v_mfma_f32_16x16x32_bf16 v[0:3], a[144:147], a[16:19], v[0:3]\n"
  "s_addc_u32 s41, s41, 0\n"
  "v_add_f32 v254, v254, v38\n"
  "v_add_f32 v254, v254, v39\n"
  "v_mfma_f32_16x16x32_bf16 v[0:3], a[124:127], a[12:15], v[0:3]\n"
  "v_add_f32 v255, v255, v52\n"
  "v_add_f32 v255, v255, v53\n"
  "v_mfma_f32_16x16x32_bf16 v[0:3], a[120:123], a[8:11], v[0:3]\n"
  "s_add_u32 m0, s60, -128\n"
  "v_add_f32 v255, v255, v54\n"
  "v_add_f32 v255, v255, v55\n"
  "v_mfma_f32_16x16x32_bf16 v[0:3], a[132:135], a[4:7], v[0:3]\n"
  "ds_read_b64_tr_b16 v[244:245], v230 offset:15680\n"
  "ds_read_b64_tr_b16 v[246:247], v230 offset:15712\n"
  "v_mfma_f32_16x16x32_bf16 v[0:3], a[128:131], a[0:3], v[0:3]\n"
  "v_fma_f32 v64, v64, s73, v227\n"
  "v_fma_f32 v65, v65, s73, v227\n"
  "v_mfma_f32_16x16x32_bf16 v[16:19], a[128:131], a[24:27], v[248:251]\n"
  "v_exp_f32 v64, v64\n"
  "v_mfma_f32_16x16x32_bf16 v[16:19], a[132:135], a[28:31], v[16:19]\n"
  "buffer_load_dwordx4 v221, s[32:35], 0 offen offset:128 lds\n"
  "v_fma_f32 v66, v66, s73, v227\n"
  "v_mfma_f32_16x16x32_bf16 v[16:19], a[120:123], a[32:35], v[16:19]\n"
  "s_addk_i32 m0, 1024\n"
  "ds_read_b64_tr_b16 a[136:137], v217 offset:0\n"
  "v_fma_f32 v67, v67, s73, v227\n"
  "v_mfma_f32_16x16x32_bf16 v[16:19], a[124:127], a[36:39], v[16:19]\n"
  "s_mov_b64 s[32:33], s[44:45]\n"
  "ds_read_b64_tr_b16 a[138:139], v218 offset:0\n"
  "v_fma_f32 v0, v0, s73, v223\n"
  "v_mfma_f32_16x16x32_bf16 v[16:19], a[144:147], a[40:43], v[16:19]\n"
  "ds_read_b64_tr_b16 a[140:141], v217 offset:1024\n"
  "v_fma_f32 v1, v1, s73, v223\n"
  "v_mfma_f32_16x16x32_bf16 v[16:19], a[148:151], a[44:47], v[16:19]\n"
  "buffer_load_dwordx4 v221, s[44:47], 0 offen offset:128 lds\n"
  "v_fma_f32 v2, v2, s73, v223\n"
  "v_mfma_f32_16x16x32_bf16 v[32:35], a[148:151], a[68:71], v[248:251]\n"
  "s_addk_i32 m0, 896\n"
  "ds_read_b64_tr_b16 a[142:143], v218 offset:1024\n"
  "v_fma_f32 v3, v3, s73, v223\n"
  "v_mfma_f32_16x16x32_bf16 v[32:35], a[144:147], a[64:67], v[32:35]\n"
  "v_exp_f32 v65, v65\n"
  "v_mfma_f32_16x16x32_bf16 v[32:35], a[124:127], a[60:63], v[32:35]\n"
  "v_exp_f32 v66, v66\n"
  "v_mfma_f32_16x16x32_bf16 v[32:35], a[120:123], a[56:59], v[32:35]\n"
  "buffer_load_dwordx4 v221, s[44:47], 0 offen offset:256 lds\n"
  "v_cvt_pk_bf16_f32 v64, v64, v65\n"
  "v_mfma_f32_16x16x32_bf16 v[32:35], a[132:135], a[52:55], v[32:35]\n"
  "s_add_u32 s44, s44, s31\n"
  "v_exp_f32 v67, v67\n"
  "v_mfma_f32_16x16x32_bf16 v[32:35], a[128:131], a[48:51], v[32:35]\n"
  "s_addc_u32 s45, s45, 0\n"
  "v_exp_f32 v0, v0\n"
  "v_mfma_f32_16x16x32_bf16 v[48:51], a[128:131], a[72:75], v[248:251]\n"
  "ds_read_b64_tr_b16 a[128:129], v217 offset:16\n"
  "v_cvt_pk_bf16_f32 v65, v66, v67\n"
  "v_mfma_f32_16x16x32_bf16 v[48:51], a[132:135], a[76:79], v[48:51]\n"
  "ds_read_b64_tr_b16 a[130:131], v218 offset:16\n"
  "v_add_f32 v252, v252, v0\n"
  "v_mfma_f32_16x16x32_bf16 v[48:51], a[120:123], a[80:83], v[48:51]\n"
  "ds_read_b64_tr_b16 a[132:133], v217 offset:1040\n"
  "v_fma_f32 v16, v16, s73, v224\n"
  "v_mfma_f32_16x16x32_bf16 v[48:51], a[124:127], a[84:87], v[48:51]\n"
  "s_add_u32 m0, s72, 11200\n"
  "ds_read_b64_tr_b16 a[134:135], v218 offset:1040\n"
  "v_mfma_f32_16x16x32_bf16 v[48:51], a[144:147], a[88:91], v[48:51]\n"
  "v_exp_f32 v1, v1\n"
  "v_mfma_f32_16x16x32_bf16 v[48:51], a[148:151], a[92:95], v[48:51]\n"
  "s_waitcnt lgkmcnt(4)\n"
  "v_exp_f32 v2, v2\n"
  "v_mfma_f32_16x16x32_bf16 a[236:239], a[160:163], a[136:139], a[236:239]\n"
  "v_exp_f32 v3, v3\n"
  "v_mfma_f32_16x16x32_bf16 a[236:239], a[156:159], a[140:143], a[236:239]\n"
  "buffer_load_dwordx4 v221, s[48:51], 0 offen offset:0 nt lds\n"
  "v_add_f32 v252, v252, v1\n"
  "v_mfma_f32_16x16x32_bf16 v[184:187], v[60:63], a[136:139], v[184:187]\n"
  "v_exp_f32 v16, v16\n"
  "v_mfma_f32_16x16x32_bf16 v[184:187], v[56:59], a[140:143], v[184:187]\n"
  "v_add_f32 v252, v252, v2\n"
  "v_add_f32 v252, v252, v3\n"
  "v_mfma_f32_16x16x32_bf16 v[152:155], v[44:47], a[136:139], v[152:155]\n"
  "ds_read_b64_tr_b16 a[120:121], v219 offset:0\n"
  "v_fma_f32 v17, v17, s73, v224\n"
  "v_mfma_f32_16x16x32_bf16 v[152:155], v[40:43], a[140:143], v[152:155]\n"
  "s_add_u32 m0, s72, 9216\n"
  "ds_read_b64_tr_b16 a[122:123], v220 offset:0\n"
  "v_add_f32 v253, v253, v16\n"
  "v_mfma_f32_16x16x32_bf16 v[120:123], v[28:31], a[136:139], v[120:123]\n"
  "ds_read_b64_tr_b16 a[124:125], v219 offset:1024\n"
  "v_fma_f32 v18, v18, s73, v224\n"
  "v_mfma_f32_16x16x32_bf16 v[120:123], v[24:27], a[140:143], v[120:123]\n"
  "ds_read_b64_tr_b16 a[126:127], v220 offset:1024\n"
  "v_fma_f32 v19, v19, s73, v224\n"
  "v_mfma_f32_16x16x32_bf16 v[88:91], v[12:15], a[136:139], v[88:91]\n"
  "v_exp_f32 v17, v17\n"
  "v_mfma_f32_16x16x32_bf16 v[88:91], v[8:11], a[140:143], v[88:91]\n"
  "s_waitcnt lgkmcnt(4)\n"
  "buffer_load_dwordx4 v221, s[48:51], 0 offen offset:3072 nt lds\n"
  "v_add_f32 v253, v253, v17\n"
  "v_mfma_f32_16x16x32_bf16 v[92:95], v[12:15], a[128:131], v[92:95]\n"
  "v_exp_f32 v18, v18\n"
  "v_mfma_f32_16x16x32_bf16 v[92:95], v[8:11], a[132:135], v[92:95]\n"
  "v_exp_f32 v19, v19\n"
  "v_mfma_f32_16x16x32_bf16 v[124:127], v[28:31], a[128:131], v[124:127]\n"
  "ds_read_b128 a[136:139], v235 offset:0\n"
  "v_add_f32 v253, v253, v18\n"
  "v_mfma_f32_16x16x32_bf16 v[124:127], v[24:27], a[132:135], v[124:127]\n"
  "ds_read_b128 a[140:143], v235 offset:64\n"
  "v_add_f32 v253, v253, v19\n"
  "v_mfma_f32_16x16x32_bf16 v[156:159], v[44:47], a[128:131], v[156:159]\n"
  "ds_read_b128 a[144:147], v235 offset:1024\n"
  "v_fma_f32 v32, v32, s73, v225\n"
  "v_mfma_f32_16x16x32_bf16 v[156:159], v[40:43], a[132:135], v[156:159]\n"
  "ds_read_b128 a[148:151], v235 offset:1088\n"
  "v_fma_f32 v33, v33, s73, v225\n"
  "v_mfma_f32_16x16x32_bf16 v[188:191], v[60:63], a[128:131], v[188:191]\n"
  "v_exp_f32 v32, v32\n"
  "v_mfma_f32_16x16x32_bf16 v[188:191], v[56:59], a[132:135], v[188:191]\n"
  "v_exp_f32 v33, v33\n"
  "v_mfma_f32_16x16x32_bf16 a[240:243], a[160:163], a[128:131], a[240:243]\n"
  "ds_read_b128 a[128:131], v235 offset:2048\n"
  "v_fma_f32 v34, v34, s73, v225\n"
  "v_mfma_f32_16x16x32_bf16 a[240:243], a[156:159], a[132:135], a[240:243]\n"
  "s_waitcnt lgkmcnt(5)\n"
  "ds_read_b128 a[132:135], v235 offset:2112\n"
  "v_fma_f32 v35, v35, s73, v225\n"
  "v_mfma_f32_16x16x32_bf16 v[192:195], v[60:63], a[120:123], v[192:195]\n"
  "v_exp_f32 v34, v34\n"
  "v_mfma_f32_16x16x32_bf16 v[192:195], v[56:59], a[124:127], v[192:195]\n"
  "ds_write_b64 v231, v[64:65] offset:2048\n"
  "v_mfma_f32_16x16x32_bf16 v[160:163], v[44:47], a[120:123], v[160:163]\n"
  "v_exp_f32 v35, v35\n"
  "v_mfma_f32_16x16x32_bf16 v[160:163], v[40:43], a[124:127], v[160:163]\n"
  "v_add_f32 v254, v254, v32\n"
  "v_add_f32 v254, v254, v33\n"
  "v_mfma_f32_16x16x32_bf16 v[128:131], v[28:31], a[120:123], v[128:131]\n"
  "v_add_f32 v254, v254, v34\n"
  "v_add_f32_e64 v254, v254, v35\n"
  "v_mfma_f32_16x16x32_bf16 v[128:131], v[24:27], a[124:127], v[128:131]\n"
  "buffer_store_dwordx4 a[252:255], v228, s[52:55], s82 offen offset:3200 nt\n"
  "v_max_u32_dpp v236, v236, v236 quad_perm:[2,3,0,1] row_mask:0xf bank_mask:0xf\n"
  "v_add_u32_e64 v236, -1, v236\n"
  "v_fma_f32 v48, v48, s73, v226\n"
  "v_mfma_f32_16x16x32_bf16 v[96:99], v[12:15], a[120:123], v[96:99]\n"
  "ds_read_b64_tr_b16 a[252:253], v230 offset:16800\n"
  "ds_read_b64_tr_b16 a[254:255], v230 offset:16832\n"
  "v_mfma_f32_16x16x32_bf16 v[96:99], v[8:11], a[124:127], v[96:99]\n"
  "s_waitcnt lgkmcnt(7)\n"
  "v_exp_f32 v48, v48\n"
  "v_mfma_f32_16x16x32_bf16 v[4:7], a[136:139], a[0:3], v[248:251]\n"
  "v_min_u32_dpp v236, v236, v236 row_shr:4 row_mask:0xf bank_mask:0xf\n"
  "v_fma_f32 v49, v49, s73, v226\n"
  "v_mfma_f32_16x16x32_bf16 v[4:7], a[140:143], a[4:7], v[4:7]\n"
  "s_waitcnt lgkmcnt(5)\n"
  "v_exp_f32 v49, v49\n"
  "v_mfma_f32_16x16x32_bf16 v[4:7], a[144:147], a[8:11], v[4:7]\n"
  "v_min_u32_dpp v236, v236, v236 row_shr:8 row_mask:0xf bank_mask:0xf\n"
  "v_fma_f32 v50, v50, s73, v226\n"
  "v_mfma_f32_16x16x32_bf16 v[4:7], a[148:151], a[12:15], v[4:7]\n"
  "s_waitcnt lgkmcnt(3)\n"
  "v_exp_f32 v50, v50\n"
  "v_mfma_f32_16x16x32_bf16 v[4:7], a[128:131], a[16:19], v[4:7]\n"
  "v_min_u32_dpp v236, v236, v236 row_bcast:15 row_mask:0xa bank_mask:0xf\n"
  "v_fma_f32 v51, v51, s73, v226\n"
  "v_mfma_f32_16x16x32_bf16 v[4:7], a[132:135], a[20:23], v[4:7]\n"
  "buffer_store_dwordx4 v[244:247], v228, s[52:55], s83 offen offset:128 nt\n"
  "v_min_u32_dpp v236, v236, v236 row_bcast:31 row_mask:0xc bank_mask:0xf\n"
  "v_exp_f32 v51, v51\n"
  "v_mfma_f32_16x16x32_bf16 v[20:23], a[132:135], a[44:47], v[248:251]\n"
  "v_readlane_b32 s65, v236, 63\n"
  "v_mfma_f32_16x16x32_bf16 v[20:23], a[128:131], a[40:43], v[20:23]\n"
  "v_add_f32 v255, v255, v48\n"
  "v_add_f32 v255, v255, v49\n"
  "v_mfma_f32_16x16x32_bf16 v[20:23], a[148:151], a[36:39], v[20:23]\n"
  "s_add_u32 s65, s65, 1\n"
  "ds_read_b64_tr_b16 a[120:121], v219 offset:16\n"
  "v_add_f32 v255, v255, v50\n"
  "v_mfma_f32_16x16x32_bf16 v[20:23], a[144:147], a[32:35], v[20:23]\n"
  "s_lshr_b32 s65, s65, 23\n"
  "ds_read_b64_tr_b16 a[122:123], v220 offset:16\n"
  "v_add_f32_e64 v255, v255, v51\n"
  "v_mfma_f32_16x16x32_bf16 v[20:23], a[140:143], a[28:31], v[20:23]\n"
  "s_sub_i32 s65, 0x77, s65\n"
  "ds_read_b64_tr_b16 a[124:125], v219 offset:1040\n"
  "v_fma_f32 v4, v4, s73, v223\n"
  "v_mfma_f32_16x16x32_bf16 v[20:23], a[136:139], a[24:27], v[20:23]\n"
  "s_and_b32 s65, s65, -4\n"
  "s_waitcnt lgkmcnt(3)\n"
  "buffer_store_dwordx4 a[252:255], v228, s[52:55], s83 offen offset:3200 nt\n"
  "ds_read_b64_tr_b16 a[126:127], v220 offset:1040\n"
  "v_exp_f32 v4, v4\n"
  "v_mfma_f32_16x16x32_bf16 v[36:39], a[136:139], a[48:51], v[248:251]\n"
  "s_max_i32 s65, s65, 0xffffffc4\n"
  "v_cvt_f32_i32 v236, s65\n"
  "v_add_f32 v252, v252, v4\n"
  "v_mfma_f32_16x16x32_bf16 v[36:39], a[140:143], a[52:55], v[36:39]\n"
  "s_add_u32 s65, s65, 0xab\n"
  "v_readfirstlane_b32 s64, v236\n"
  "v_mfma_f32_16x16x32_bf16 v[36:39], a[144:147], a[56:59], v[36:39]\n"
  "v_fma_f32 v20, v20, s73, v224\n"
  "v_fma_f32 v21, v21, s73, v224\n"
  "v_mfma_f32_16x16x32_bf16 v[36:39], a[148:151], a[60:63], v[36:39]\n"
  "s_cmpk_gt_u32 s65, 0xd3\n"
  "s_cselect_b32 s65, 0, s65\n"
  "v_exp_f32 v20, v20\n"
  "v_mfma_f32_16x16x32_bf16 v[36:39], a[128:131], a[64:67], v[36:39]\n"
  "s_lshl_b32 s65, s65, 23\n"
  "v_exp_f32 v21, v21\n"
  "v_mfma_f32_16x16x32_bf16 v[36:39], a[132:135], a[68:71], v[36:39]\n"
  "v_fma_f32 v22, v22, s73, v224\n"
  "v_fma_f32 v23, v23, s73, v224\n"
  "v_mfma_f32_16x16x32_bf16 v[52:55], a[132:135], a[92:95], v[248:251]\n"
  "ds_read_b64_tr_b16 a[132:133], v217 offset:2048\n"
  "v_fma_f32 v5, v5, s73, v223\n"
  "v_mfma_f32_16x16x32_bf16 v[52:55], a[128:131], a[88:91], v[52:55]\n"
  "ds_read_b64_tr_b16 a[134:135], v218 offset:2048\n"
  "v_fma_f32 v6, v6, s73, v223\n"
  "v_mfma_f32_16x16x32_bf16 v[52:55], a[148:151], a[84:87], v[52:55]\n"
  "ds_read_b64_tr_b16 a[128:129], v217 offset:2064\n"
  "v_fma_f32 v36, v36, s73, v225\n"
  "v_mfma_f32_16x16x32_bf16 v[52:55], a[144:147], a[80:83], v[52:55]\n"
  "ds_read_b64_tr_b16 a[130:131], v218 offset:2064\n"
  "v_fma_f32 v37, v37, s73, v225\n"
  "v_mfma_f32_16x16x32_bf16 v[52:55], a[140:143], a[76:79], v[52:55]\n"
  "v_exp_f32 v36, v36\n"
  "v_mfma_f32_16x16x32_bf16 v[52:55], a[136:139], a[72:75], v[52:55]\n"
  "s_waitcnt lgkmcnt(4)\n"
  "v_exp_f32 v37, v37\n"
  "v_mfma_f32_16x16x32_bf16 v[100:103], v[12:15], a[120:123], v[100:103]\n"
  "v_cvt_pk_bf16_f32 v12, v0, v1\n"
  "v_cvt_pk_bf16_f32 v13, v2, v3\n"
  "v_mfma_f32_16x16x32_bf16 v[100:103], v[8:11], a[124:127], v[100:103]\n"
  "v_exp_f32 v22, v22\n"
  "v_mfma_f32_16x16x32_bf16 v[132:135], v[28:31], a[120:123], v[132:135]\n"
  "ds_read_b64_tr_b16 a[140:141], v219 offset:2048\n"
  "v_cvt_pk_bf16_f32 v28, v16, v17\n"
  "v_mfma_f32_16x16x32_bf16 v[132:135], v[24:27], a[124:127], v[132:135]\n"
  "ds_read_b64_tr_b16 a[142:143], v220 offset:2048\n"
  "v_cvt_pk_bf16_f32 v29, v18, v19\n"
  "v_mfma_f32_16x16x32_bf16 v[164:167], v[44:47], a[120:123], v[164:167]\n"
  "ds_read_b64_tr_b16 a[136:137], v219 offset:2064\n"
  "v_cvt_pk_bf16_f32 v44, v32, v33\n"
  "v_mfma_f32_16x16x32_bf16 v[164:167], v[40:43], a[124:127], v[164:167]\n"
  "ds_read_b64_tr_b16 a[138:139], v220 offset:2064\n"
  "v_cvt_pk_bf16_f32 v45, v34, v35\n"
  "v_mfma_f32_16x16x32_bf16 v[196:199], v[60:63], a[120:123], v[196:199]\n"
  "v_exp_f32 v23, v23\n"
  "v_mfma_f32_16x16x32_bf16 v[196:199], v[56:59], a[124:127], v[196:199]\n"
  "s_waitcnt lgkmcnt(4)\n"
  "v_exp_f32 v5, v5\n"
  "v_mfma_f32_16x16x32_bf16 a[244:247], a[156:159], a[132:135], a[244:247]\n"
  "v_exp_f32 v6, v6\n"
  "v_mfma_f32_16x16x32_bf16 a[248:251], a[156:159], a[128:131], a[248:251]\n"
  "v_cvt_pk_bf16_f32 v60, v48, v49\n"
  "v_cvt_pk_bf16_f32 v61, v50, v51\n"
  "v_mfma_f32_16x16x32_bf16 v[200:203], v[56:59], a[132:135], v[200:203]\n"
  "v_fma_f32 v52, v52, s73, v226\n"
  "v_fma_f32 v53, v53, s73, v226\n"
  "v_mfma_f32_16x16x32_bf16 v[204:207], v[56:59], a[128:131], v[204:207]\n"
  "v_exp_f32 v52, v52\n"
  "v_mfma_f32_16x16x32_bf16 v[168:171], v[40:43], a[132:135], v[168:171]\n"
  "v_exp_f32_e64 v53, v53\n"
  "v_mfma_f32_16x16x32_bf16 v[172:175], v[40:43], a[128:131], v[172:175]\n"
  "s_waitcnt vmcnt(14) lgkmcnt(0)\n"
  "s_barrier\n"
  "ds_read_b128 a[120:123], v216 offset:12352\n"
  "v_mfma_f32_16x16x32_bf16 v[136:139], v[24:27], a[132:135], v[136:139]\n"
  "ds_read_b64 a[160:161], v231 offset:2048\n"
  "v_fma_f32 v54, v54, s73, v226\n"
  "v_mfma_f32_16x16x32_bf16 v[140:143], v[24:27], a[128:131], v[140:143]\n"
  "ds_read_b64 a[162:163], v232 offset:2048\n"
  "v_fma_f32 v55, v55, s73, v226\n"
  "v_mfma_f32_16x16x32_bf16 v[104:107], v[8:11], a[132:135], v[104:107]\n"
  "ds_read_b128 a[124:127], v216 offset:12416\n"
  "v_cvt_pk_bf16_f32 v62, v52, v53\n"
  "v_mfma_f32_16x16x32_bf16 v[108:111], v[8:11], a[128:131], v[108:111]\n"
  "ds_read_b128 a[132:135], v216 offset:13376\n"
  "v_fma_f32 v38, v38, s73, v225\n"
  "v_mfma_f32_16x16x32_bf16 v[112:115], v[8:11], a[140:143], v[112:115]\n"
  "s_add_u32 s71, s92, s82\n"
  "ds_read_b128 a[128:131], v216 offset:13440\n"
  "v_fma_f32 v39, v39, s73, v225\n"
  "v_mfma_f32_16x16x32_bf16 v[116:119], v[8:11], a[136:139], v[116:119]\n"
  "s_mov_b32 m0, s66\n"
  "ds_read_b128 a[148:151], v216 offset:14400\n"
  "v_cvt_pk_bf16_f32 v46, v36, v37\n"
  "v_mfma_f32_16x16x32_bf16 v[144:147], v[24:27], a[140:143], v[144:147]\n"
  "s_add_u32 s48, s84, s71\n"
  "ds_read_b128 a[144:147], v216 offset:14464\n"
  "v_cvt_pk_bf16_f32 v30, v20, v21\n"
  "v_mfma_f32_16x16x32_bf16 v[148:151], v[24:27], a[136:139], v[148:151]\n"
  "s_addc_u32 s49, s85, 0\n"
  "v_exp_f32 v54, v54\n"
  "v_mfma_f32_16x16x32_bf16 v[176:179], v[40:43], a[140:143], v[176:179]\n"
  "s_sub_i32 s71, s38, s82\n"
  "v_exp_f32 v55, v55\n"
  "v_mfma_f32_16x16x32_bf16 v[180:183], v[40:43], a[136:139], v[180:183]\n"
  "buffer_load_dwordx4 v221, s[40:43], 0 offen offset:0 lds\n"
  "v_mfma_f32_16x16x32_bf16 v[208:211], v[56:59], a[140:143], v[208:211]\n"
  "s_addk_i32 m0, 896\n"
  "v_exp_f32 v38, v38\n"
  "v_mfma_f32_16x16x32_bf16 v[212:215], v[56:59], a[136:139], v[212:215]\n"
  "s_waitcnt lgkmcnt(4)\n"
  "v_exp_f32 v39, v39\n"
  "v_mfma_f32_16x16x32_bf16 v[64:67], a[120:123], a[96:99], v[248:251]\n"
  "s_max_i32 s50, s71, 0\n"
  "v_cvt_pk_bf16_f32 v63, v54, v55\n"
  "v_cvt_pk_bf16_f32 v47, v38, v39\n"
  "v_mfma_f32_16x16x32_bf16 v[64:67], a[124:127], a[100:103], v[64:67]\n"
  "s_waitcnt lgkmcnt(2)\n"
  "buffer_load_dwordx4 v221, s[40:43], 0 offen offset:128 lds\n"
  "v_cvt_pk_bf16_f32 v31, v22, v23\n"
  "v_mfma_f32_16x16x32_bf16 v[64:67], a[132:135], a[104:107], v[64:67]\n"
  "s_addk_i32 m0, 896\n"
  "v_fma_f32 v7, v7, s73, v223\n"
  "v_cvt_pk_bf16_f32 v14, v4, v5\n"
  "v_mfma_f32_16x16x32_bf16 v[64:67], a[128:131], a[108:111], v[64:67]\n"
  "s_waitcnt lgkmcnt(0)\n"
  "v_exp_f32 v7, v7\n"
  "v_mfma_f32_16x16x32_bf16 v[64:67], a[148:151], a[112:115], v[64:67]\n"
  "v_cvt_pk_bf16_f32 v15, v6, v7\n"
  "v_add_f32 v252, v252, v5\n"
  "v_mfma_f32_16x16x32_bf16 v[64:67], a[144:147], a[116:119], v[64:67]\n"
  "buffer_load_dwordx4 v221, s[40:43], 0 offen offset:256 lds\n"
  "v_add_f32 v252, v252, v6\n"
  "v_mfma_f32_16x16x32_bf16 v[0:3], a[144:147], a[20:23], v[248:251]\n"
  "s_add_u32 s40, s40, s31\n"
  "v_add_f32 v252, v252, v7\n"
  "v_add_f32 v253, v253, v20\n"
  "v_mfma_f32_16x16x32_bf16 v[0:3], a[148:151], a[16:19], v[0:3]\n"
  "s_addc_u32 s41, s41, 0\n"
  "v_bfe_u32 v236, v231, 3, 6\n"
  "v_lshlrev_b32 v236, 7, v236\n"
  "v_mfma_f32_16x16x32_bf16 v[0:3], a[128:131], a[12:15], v[0:3]\n"
  "s_cmp_eq_u32 s76, s24\n"
  "s_cselect_b32 s22, s25, 0\n"
  "v_add_f32 v253, v253, v21\n"
  "v_add_f32 v253, v253, v22\n"
  "v_mfma_f32_16x16x32_bf16 v[0:3], a[132:135], a[8:11], v[0:3]\n"
  "s_mov_b32 m0, s58\n"
  "v_add_f32 v253, v253, v23\n"
  "v_add_f32 v254, v254, v36\n"
  "v_mfma_f32_16x16x32_bf16 v[0:3], a[124:127], a[4:7], v[0:3]\n"
  "v_add_f32 v254, v254, v37\n"
  "v_add_f32 v254, v254, v38\n"
  "v_mfma_f32_16x16x32_bf16 v[0:3], a[120:123], a[0:3], v[0:3]\n"
  "v_add_f32 v254, v254, v39\n"
  "v_add_f32 v255, v255, v52\n"
  "v_mfma_f32_16x16x32_bf16 v[16:19], a[120:123], a[24:27], v[248:251]\n"
  "v_add_f32 v255, v255, v53\n"
  "v_add_f32 v255, v255, v54\n"
  "v_mfma_f32_16x16x32_bf16 v[16:19], a[124:127], a[28:31], v[16:19]\n"
  "buffer_load_dwordx4 v221, s[32:35], 0 offen offset:0 lds\n"
  "v_add_f32 v255, v255, v55\n"
  "v_mfma_f32_16x16x32_bf16 v[16:19], a[132:135], a[32:35], v[16:19]\n"
  "s_addk_i32 m0, 1024\n"
  "ds_read_b64_tr_b16 a[140:141], v217 offset:12352\n"
  "v_fma_f32 v64, v64, s73, v227\n"
  "v_mfma_f32_16x16x32_bf16 v[16:19], a[128:131], a[36:39], v[16:19]\n"
  "s_mov_b64 s[32:33], s[44:45]\n"
  "ds_read_b64_tr_b16 a[142:143], v218 offset:12352\n"
  "v_fma_f32 v65, v65, s73, v227\n"
  "v_mfma_f32_16x16x32_bf16 v[16:19], a[148:151], a[40:43], v[16:19]\n"
  "ds_read_b64_tr_b16 a[136:137], v217 offset:13376\n"
  "v_fma_f32 v66, v66, s73, v227\n"
  "v_mfma_f32_16x16x32_bf16 v[16:19], a[144:147], a[44:47], v[16:19]\n"
  "buffer_load_dwordx4 v221, s[44:47], 0 offen offset:0 lds\n"
  "v_fma_f32 v67, v67, s73, v227\n"
  "v_mfma_f32_16x16x32_bf16 v[32:35], a[144:147], a[68:71], v[248:251]\n"
  "s_addk_i32 m0, 768\n"
  "ds_read_b64_tr_b16 a[138:139], v218 offset:13376\n"
  "v_fma_f32 v0, v0, s73, v223\n"
  "v_mfma_f32_16x16x32_bf16 v[32:35], a[148:151], a[64:67], v[32:35]\n"
  "v_exp_f32 v64, v64\n"
  "v_mfma_f32_16x16x32_bf16 v[32:35], a[128:131], a[60:63], v[32:35]\n"
  "v_exp_f32 v65, v65\n"
  "v_mfma_f32_16x16x32_bf16 v[32:35], a[132:135], a[56:59], v[32:35]\n"
  "buffer_load_dwordx4 v221, s[44:47], 0 offen offset:256 lds\n"
  "v_cvt_pk_bf16_f32 v64, v64, v65\n"
  "v_mfma_f32_16x16x32_bf16 v[32:35], a[124:127], a[52:55], v[32:35]\n"
  "s_add_u32 s44, s44, s31\n"
  "v_exp_f32 v66, v66\n"
  "v_mfma_f32_16x16x32_bf16 v[32:35], a[120:123], a[48:51], v[32:35]\n"
  "s_addc_u32 s45, s45, 0\n"
  "v_exp_f32 v67, v67\n"
  "v_mfma_f32_16x16x32_bf16 v[48:51], a[120:123], a[72:75], v[248:251]\n"
  "s_mov_b32 m0, s29\n"
  "v_exp_f32 v0, v0\n"
  "v_mfma_f32_16x16x32_bf16 v[48:51], a[124:127], a[76:79], v[48:51]\n"
  "v_cvt_pk_bf16_f32 v65, v66, v67\n"
  "v_fma_f32 v1, v1, s73, v223\n"
  "v_mfma_f32_16x16x32_bf16 v[48:51], a[132:135], a[80:83], v[48:51]\n"
  "ds_read_b64_tr_b16 a[120:121], v217 offset:12368\n"
  "v_fma_f32 v2, v2, s73, v223\n"
  "v_mfma_f32_16x16x32_bf16 v[48:51], a[128:131], a[84:87], v[48:51]\n"
  "buffer_load_dword v236, s[20:23], 0 offen lds\n"
  "v_fma_f32 v3, v3, s73, v223\n"
  "v_mfma_f32_16x16x32_bf16 v[48:51], a[148:151], a[88:91], v[48:51]\n"
  "s_add_u32 m0, s72, 13440\n"
  "ds_read_b64_tr_b16 a[122:123], v218 offset:12368\n"
  "v_add_f32 v252, v252, v0\n"
  "v_mfma_f32_16x16x32_bf16 v[48:51], a[144:147], a[92:95], v[48:51]\n"
  "s_waitcnt lgkmcnt(2)\n"
  "ds_read_b64_tr_b16 a[124:125], v217 offset:13392\n"
  "v_fma_f32 v16, v16, s73, v224\n"
  "v_mfma_f32_16x16x32_bf16 a[228:231], a[156:159], a[140:143], a[228:231]\n"
  "ds_read_b64_tr_b16 a[126:127], v218 offset:13392\n"
  "v_fma_f32 v17, v17, s73, v224\n"
  "v_mfma_f32_16x16x32_bf16 a[228:231], a[160:163], a[136:139], a[228:231]\n"
  "buffer_load_dwordx4 v221, s[48:51], 0 offen offset:0 nt lds\n"
  "v_fma_f32 v18, v18, s73, v224\n"
  "v_mfma_f32_16x16x32_bf16 a[212:215], v[56:59], a[140:143], a[212:215]\n"
  "v_exp_f32 v1, v1\n"
  "v_mfma_f32_16x16x32_bf16 a[212:215], v[60:63], a[136:139], a[212:215]\n"
  "v_exp_f32 v2, v2\n"
  "v_mfma_f32_16x16x32_bf16 a[196:199], v[40:43], a[140:143], a[196:199]\n"
  "v_exp_f32 v3, v3\n"
  "v_mfma_f32_16x16x32_bf16 a[196:199], v[44:47], a[136:139], a[196:199]\n"
  "s_add_u32 m0, s72, 11456\n"
  "v_exp_f32 v16, v16\n"
  "v_mfma_f32_16x16x32_bf16 a[180:183], v[24:27], a[140:143], a[180:183]\n"
  "ds_read_b64_tr_b16 a[132:133], v219 offset:12352\n"
  "v_add_f32 v252, v252, v1\n"
  "v_mfma_f32_16x16x32_bf16 a[180:183], v[28:31], a[136:139], a[180:183]\n"
  "ds_read_b64_tr_b16 a[134:135], v220 offset:12352\n"
  "v_add_f32 v252, v252, v2\n"
  "v_mfma_f32_16x16x32_bf16 a[164:167], v[8:11], a[140:143], a[164:167]\n"
  "ds_read_b64_tr_b16 a[128:129], v219 offset:13376\n"
  "v_add_f32 v252, v252, v3\n"
  "v_mfma_f32_16x16x32_bf16 a[164:167], v[12:15], a[136:139], a[164:167]\n"
  "buffer_load_dwordx4 v221, s[48:51], 0 offen offset:3072 nt lds\n"
  "v_fma_f32 v19, v19, s73, v224\n"
  "v_mfma_f32_16x16x32_bf16 v[84:87], a[156:159], a[152:155], v[84:87]\n"
  "ds_read_b64_tr_b16 a[130:131], v220 offset:13376\n"
  "v_add_f32 v253, v253, v16\n"
  "v_mfma_f32_16x16x32_bf16 v[84:87], a[160:163], a[152:155], v[84:87]\n"
  "s_waitcnt lgkmcnt(4)\n"
  "v_exp_f32 v17, v17\n"
  "v_mfma_f32_16x16x32_bf16 a[168:171], v[8:11], a[120:123], a[168:171]\n"
  "v_exp_f32 v18, v18\n"
  "v_mfma_f32_16x16x32_bf16 a[168:171], v[12:15], a[124:127], a[168:171]\n"
  "s_add_u32 m0, s72, 15552\n"
  "v_exp_f32 v19, v19\n"
  "v_mfma_f32_16x16x32_bf16 a[184:187], v[24:27], a[120:123], a[184:187]\n"
  "ds_read_b128 a[140:143], v235 offset:12352\n"
  "v_add_f32 v253, v253, v17\n"
  "v_mfma_f32_16x16x32_bf16 a[184:187], v[28:31], a[124:127], a[184:187]\n"
  "ds_read_b128 a[136:139], v235 offset:12416\n"
  "v_add_f32 v253, v253, v18\n"
  "v_mfma_f32_16x16x32_bf16 a[200:203], v[40:43], a[120:123], a[200:203]\n"
  "ds_read_b128 a[148:151], v235 offset:13376\n"
  "v_add_f32 v253, v253, v19\n"
  "v_mfma_f32_16x16x32_bf16 a[200:203], v[44:47], a[124:127], a[200:203]\n"
  "buffer_load_dwordx4 v221, s[48:51], 0 offen offset:128 nt lds\n"
  "v_fma_f32 v32, v32, s73, v225\n"
  "v_mfma_f32_16x16x32_bf16 a[216:219], v[56:59], a[120:123], a[216:219]\n"
  "ds_read_b128 a[144:147], v235 offset:13440\n"
  "v_fma_f32 v33, v33, s73, v225\n"
  "v_mfma_f32_16x16x32_bf16 a[216:219], v[60:63], a[124:127], a[216:219]\n"
  "v_exp_f32 v32, v32\n"
  "v_mfma_f32_16x16x32_bf16 a[232:235], a[156:159], a[120:123], a[232:235]\n"
  "ds_read_b128 a[120:123], v235 offset:14400\n"
  "v_fma_f32 v34, v34, s73, v225\n"
  "v_mfma_f32_16x16x32_bf16 a[232:235], a[160:163], a[124:127], a[232:235]\n"
  "s_waitcnt lgkmcnt(5)\n"
  "ds_read_b128 a[124:127], v235 offset:14464\n"
  "v_fma_f32 v35, v35, s73, v225\n"
  "v_mfma_f32_16x16x32_bf16 a[220:223], v[56:59], a[132:135], a[220:223]\n"
  "s_add_u32 m0, s72, 13568\n"
  "v_exp_f32 v33, v33\n"
  "v_mfma_f32_16x16x32_bf16 a[220:223], v[60:63], a[128:131], a[220:223]\n"
  "ds_write_b64 v231, v[64:65] offset:0\n"
  "v_mfma_f32_16x16x32_bf16 a[204:207], v[40:43], a[132:135], a[204:207]\n"
  "v_exp_f32 v34, v34\n"
  "v_mfma_f32_16x16x32_bf16 a[204:207], v[44:47], a[128:131], a[204:207]\n"
  "buffer_load_dwordx4 v221, s[48:51], 0 offen offset:3200 nt lds\n"
  "v_add_f32 v254, v254, v32\n"
  "v_mfma_f32_16x16x32_bf16 a[188:191], v[24:27], a[132:135], a[188:191]\n"
  "v_exp_f32 v35, v35\n"
  "v_mfma_f32_16x16x32_bf16 a[188:191], v[28:31], a[128:131], a[188:191]\n"
  "v_add_f32 v254, v254, v33\n"
  "v_add_f32 v254, v254, v34\n"
  "v_mfma_f32_16x16x32_bf16 a[172:175], v[8:11], a[132:135], a[172:175]\n"
  "v_add_f32 v254, v254, v35\n"
  "v_fma_f32 v48, v48, s73, v226\n"
  "v_mfma_f32_16x16x32_bf16 a[172:175], v[12:15], a[128:131], a[172:175]\n"
  "s_waitcnt lgkmcnt(5)\n"
  "v_exp_f32 v48, v48\n"
  "v_mfma_f32_16x16x32_bf16 v[4:7], a[140:143], a[0:3], v[248:251]\n"
  "v_fma_f32 v49, v49, s73, v226\n"
  "v_fma_f32 v50, v50, s73, v226\n"
  "v_mfma_f32_16x16x32_bf16 v[4:7], a[136:139], a[4:7], v[4:7]\n"
  "s_waitcnt lgkmcnt(3)\n"
  "v_exp_f32 v49, v49\n"
  "v_mfma_f32_16x16x32_bf16 v[4:7], a[148:151], a[8:11], v[4:7]\n"
  "v_exp_f32 v50, v50\n"
  "v_mfma_f32_16x16x32_bf16 v[4:7], a[144:147], a[12:15], v[4:7]\n"
  "s_waitcnt lgkmcnt(1)\n"
  "v_fma_f32 v51, v51, s73, v226\n"
  "v_add_f32 v255, v255, v48\n"
  "v_mfma_f32_16x16x32_bf16 v[4:7], a[120:123], a[16:19], v[4:7]\n"
  "v_exp_f32 v51, v51\n"
  "v_mfma_f32_16x16x32_bf16 v[4:7], a[124:127], a[20:23], v[4:7]\n"
  "v_add_f32 v255, v255, v49\n"
  "v_add_f32 v255, v255, v50\n"
  "v_mfma_f32_16x16x32_bf16 v[20:23], a[124:127], a[44:47], v[248:251]\n"
  "v_add_f32 v255, v255, v51\n"
  "v_mfma_f32_16x16x32_bf16 v[20:23], a[120:123], a[40:43], v[20:23]\n"
  "v_mfma_f32_16x16x32_bf16 v[20:23], a[144:147], a[36:39], v[20:23]\n"
  "ds_read_b64_tr_b16 a[132:133], v219 offset:12368\n"
  "v_fma_f32 v4, v4, s73, v223\n"
  "v_mfma_f32_16x16x32_bf16 v[20:23], a[148:151], a[32:35], v[20:23]\n"
  "ds_read_b64_tr_b16 a[134:135], v220 offset:12368\n"
  "v_fma_f32 v5, v5, s73, v223\n"
  "v_mfma_f32_16x16x32_bf16 v[20:23], a[136:139], a[28:31], v[20:23]\n"
  "ds_read_b64_tr_b16 a[128:129], v219 offset:13392\n"
  "v_fma_f32 v6, v6, s73, v223\n"
  "v_mfma_f32_16x16x32_bf16 v[20:23], a[140:143], a[24:27], v[20:23]\n"
  "ds_read_b64_tr_b16 a[130:131], v220 offset:13392\n"
  "v_fma_f32 v7, v7, s73, v223\n"
  "v_mfma_f32_16x16x32_bf16 v[36:39], a[140:143], a[48:51], v[248:251]\n"
  "v_exp_f32 v4, v4\n"
  "v_mfma_f32_16x16x32_bf16 v[36:39], a[136:139], a[52:55], v[36:39]\n"
  "v_exp_f32 v5, v5\n"
  "v_mfma_f32_16x16x32_bf16 v[36:39], a[148:151], a[56:59], v[36:39]\n"
  "v_exp_f32 v6, v6\n"
  "v_mfma_f32_16x16x32_bf16 v[36:39], a[144:147], a[60:63], v[36:39]\n"
  "v_exp_f32 v7, v7\n"
  "v_mfma_f32_16x16x32_bf16 v[36:39], a[120:123], a[64:67], v[36:39]\n"
  "v_fma_f32 v20, v20, s73, v224\n"
  "v_fma_f32 v21, v21, s73, v224\n"
  "v_mfma_f32_16x16x32_bf16 v[36:39], a[124:127], a[68:71], v[36:39]\n"
  "v_exp_f32 v20, v20\n"
  "v_mfma_f32_16x16x32_bf16 v[52:55], a[124:127], a[92:95], v[248:251]\n"
  "ds_read_b64_tr_b16 a[124:125], v217 offset:14400\n"
  "v_fma_f32 v22, v22, s73, v224\n"
  "v_mfma_f32_16x16x32_bf16 v[52:55], a[120:123], a[88:91], v[52:55]\n"
  "ds_read_b64_tr_b16 a[126:127], v218 offset:14400\n"
  "v_fma_f32 v23, v23, s73, v224\n"
  "v_mfma_f32_16x16x32_bf16 v[52:55], a[144:147], a[84:87], v[52:55]\n"
  "ds_read_b64_tr_b16 a[120:121], v217 offset:14416\n"
  "v_fma_f32 v36, v36, s73, v225\n"
  "v_mfma_f32_16x16x32_bf16 v[52:55], a[148:151], a[80:83], v[52:55]\n"
  "ds_read_b64_tr_b16 a[122:123], v218 offset:14416\n"
  "v_fma_f32 v37, v37, s73, v225\n"
  "v_mfma_f32_16x16x32_bf16 v[52:55], a[136:139], a[76:79], v[52:55]\n"
  "v_exp_f32 v36, v36\n"
  "v_mfma_f32_16x16x32_bf16 v[52:55], a[140:143], a[72:75], v[52:55]\n"
  "s_waitcnt lgkmcnt(4)\n"
  "v_exp_f32 v37, v37\n"
  "v_mfma_f32_16x16x32_bf16 a[176:179], v[8:11], a[132:135], a[176:179]\n"
  "v_cvt_pk_bf16_f32 v8, v0, v1\n"
  "v_cvt_pk_bf16_f32 v9, v2, v3\n"
  "v_mfma_f32_16x16x32_bf16 a[176:179], v[12:15], a[128:131], a[176:179]\n"
  "v_exp_f32 v21, v21\n"
  "v_mfma_f32_16x16x32_bf16 a[192:195], v[24:27], a[132:135], a[192:195]\n"
  "ds_read_b64_tr_b16 a[136:137], v219 offset:14400\n"
  "v_cvt_pk_bf16_f32 v24, v16, v17\n"
  "v_mfma_f32_16x16x32_bf16 a[192:195], v[28:31], a[128:131], a[192:195]\n"
  "ds_read_b64_tr_b16 a[138:139], v220 offset:14400\n"
  "v_cvt_pk_bf16_f32 v25, v18, v19\n"
  "v_mfma_f32_16x16x32_bf16 a[208:211], v[40:43], a[132:135], a[208:211]\n"
  "ds_read_b64_tr_b16 a[140:141], v219 offset:14416\n"
  "v_cvt_pk_bf16_f32 v40, v32, v33\n"
  "v_mfma_f32_16x16x32_bf16 a[208:211], v[44:47], a[128:131], a[208:211]\n"
  "ds_read_b64_tr_b16 a[142:143], v220 offset:14416\n"
  "v_cvt_pk_bf16_f32 v41, v34, v35\n"
  "v_mfma_f32_16x16x32_bf16 a[224:227], v[56:59], a[132:135], a[224:227]\n"
  "v_exp_f32 v22, v22\n"
  "v_mfma_f32_16x16x32_bf16 a[224:227], v[60:63], a[128:131], a[224:227]\n"
  "s_waitcnt lgkmcnt(4)\n"
  "v_exp_f32 v23, v23\n"
  "v_mfma_f32_16x16x32_bf16 a[244:247], a[160:163], a[124:127], a[244:247]\n"
  "v_cvt_pk_bf16_f32 v56, v48, v49\n"
  "v_cvt_pk_bf16_f32 v57, v50, v51\n"
  "v_mfma_f32_16x16x32_bf16 a[248:251], a[160:163], a[120:123], a[248:251]\n"
  "v_fma_f32 v52, v52, s73, v226\n"
  "v_fma_f32 v53, v53, s73, v226\n"
  "v_mfma_f32_16x16x32_bf16 v[200:203], v[60:63], a[124:127], v[200:203]\n"
  "v_exp_f32 v52, v52\n"
  "v_mfma_f32_16x16x32_bf16 v[204:207], v[60:63], a[120:123], v[204:207]\n"
  "v_exp_f32_e64 v53, v53\n"
  "v_mfma_f32_16x16x32_bf16 v[168:171], v[44:47], a[124:127], v[168:171]\n"
  "v_fma_f32 v54, v54, s73, v226\n"
  "v_fma_f32 v55, v55, s73, v226\n"
  "v_mfma_f32_16x16x32_bf16 v[172:175], v[44:47], a[120:123], v[172:175]\n"
  "s_waitcnt vmcnt(16) lgkmcnt(0)\n"
  "s_barrier\n"
  "ds_read_b128 a[132:135], v216 offset:24704\n"
  "v_mfma_f32_16x16x32_bf16 v[136:139], v[28:31], a[124:127], v[136:139]\n"
  "ds_read_b64 a[156:157], v231 offset:0\n"
  "v_cvt_pk_bf16_f32 v58, v52, v53\n"
  "v_mfma_f32_16x16x32_bf16 v[140:143], v[28:31], a[120:123], v[140:143]\n"
  "ds_read_b64 a[158:159], v232 offset:0\n"
  "v_fma_f32 v38, v38, s73, v225\n"
  "v_mfma_f32_16x16x32_bf16 v[104:107], v[12:15], a[124:127], v[104:107]\n"
  "ds_read_b128 a[128:131], v216 offset:24768\n"
  "v_fma_f32 v39, v39, s73, v225\n"
  "v_mfma_f32_16x16x32_bf16 v[108:111], v[12:15], a[120:123], v[108:111]\n"
  "ds_read_b128 a[124:127], v216 offset:25728\n"
  "v_cvt_pk_bf16_f32 v42, v36, v37\n"
  "v_mfma_f32_16x16x32_bf16 v[112:115], v[12:15], a[136:139], v[112:115]\n"
  "ds_read_b128 a[120:123], v216 offset:25792\n"
  "v_cvt_pk_bf16_f32 v26, v20, v21\n"
  "v_mfma_f32_16x16x32_bf16 v[116:119], v[12:15], a[140:143], v[116:119]\n"
  "s_mov_b32 m0, s67\n"
  "ds_read_b128 a[144:147], v216 offset:26752\n"
  "v_cvt_pk_bf16_f32 v27, v22, v23\n"
  "v_mfma_f32_16x16x32_bf16 v[144:147], v[28:31], a[136:139], v[144:147]\n"
  "ds_read_b128 a[148:151], v216 offset:26816\n"
  "v_cvt_pk_bf16_f32 v10, v4, v5\n"
  "v_mfma_f32_16x16x32_bf16 v[148:151], v[28:31], a[140:143], v[148:151]\n"
  "v_exp_f32 v54, v54\n"
  "v_mfma_f32_16x16x32_bf16 v[176:179], v[44:47], a[136:139], v[176:179]\n"
  "v_exp_f32 v55, v55\n"
  "v_mfma_f32_16x16x32_bf16 v[180:183], v[44:47], a[140:143], v[180:183]\n"
  "buffer_load_dwordx4 v221, s[40:43], 0 offen offset:0 lds\n"
  "v_mfma_f32_16x16x32_bf16 v[208:211], v[60:63], a[136:139], v[208:211]\n"
  "s_addk_i32 m0, 896\n"
  "v_exp_f32 v38, v38\n"
  "v_mfma_f32_16x16x32_bf16 v[212:215], v[60:63], a[140:143], v[212:215]\n"
  "s_waitcnt lgkmcnt(4)\n"
  "v_exp_f32 v39, v39\n"
  "v_mfma_f32_16x16x32_bf16 v[64:67], a[132:135], a[96:99], v[248:251]\n"
  "v_cvt_pk_bf16_f32 v59, v54, v55\n"
  "v_cvt_pk_bf16_f32 v43, v38, v39\n"
  "v_mfma_f32_16x16x32_bf16 v[64:67], a[128:131], a[100:103], v[64:67]\n"
  "s_waitcnt lgkmcnt(2)\n"
  "buffer_load_dwordx4 v221, s[40:43], 0 offen offset:128 lds\n"
  "v_cvt_pk_bf16_f32 v11, v6, v7\n"
  "v_mfma_f32_16x16x32_bf16 v[64:67], a[124:127], a[104:107], v[64:67]\n"
  "s_addk_i32 m0, 896\n"
  "v_add_f32 v252, v252, v4\n"
  "v_add_f32 v252, v252, v5\n"
  "v_mfma_f32_16x16x32_bf16 v[64:67], a[120:123], a[108:111], v[64:67]\n"
  "s_waitcnt lgkmcnt(0)\n"
  "v_add_f32 v252, v252, v6\n"
  "v_add_f32 v252, v252, v7\n"
  "v_mfma_f32_16x16x32_bf16 v[64:67], a[144:147], a[112:115], v[64:67]\n"
  "v_add_f32 v253, v253, v20\n"
  "v_add_f32 v253, v253, v21\n"
  "v_mfma_f32_16x16x32_bf16 v[64:67], a[148:151], a[116:119], v[64:67]\n"
  "buffer_load_dwordx4 v221, s[40:43], 0 offen offset:256 lds\n"
  "v_add_f32 v253, v253, v22\n"
  "v_mfma_f32_16x16x32_bf16 v[0:3], a[148:151], a[20:23], v[248:251]\n"
  "s_add_u32 s40, s86, s96\n"
  "v_add_f32 v253, v253, v23\n"
  "v_add_f32 v254, v254, v36\n"
  "v_mfma_f32_16x16x32_bf16 v[0:3], a[144:147], a[16:19], v[0:3]\n"
  "s_addc_u32 s41, s87, 0\n"
  "v_add_f32 v254, v254, v37\n"
  "v_add_f32 v254, v254, v38\n"
  "v_mfma_f32_16x16x32_bf16 v[0:3], a[120:123], a[12:15], v[0:3]\n"
  "s_mov_b32 s42, s30\n"
  "v_add_f32 v254, v254, v39\n"
  "v_add_f32 v255, v255, v52\n"
  "v_mfma_f32_16x16x32_bf16 v[0:3], a[124:127], a[8:11], v[0:3]\n"
  "s_add_u32 m0, s59, -128\n"
  "v_add_f32 v255, v255, v53\n"
  "v_add_f32 v255, v255, v54\n"
  "v_mfma_f32_16x16x32_bf16 v[0:3], a[128:131], a[4:7], v[0:3]\n"
  "v_add_f32 v255, v255, v55\n"
  "v_fma_f32 v64, v64, s73, v227\n"
  "v_mfma_f32_16x16x32_bf16 v[0:3], a[132:135], a[0:3], v[0:3]\n"
  "v_exp_f32 v64, v64\n"
  "v_mfma_f32_16x16x32_bf16 v[16:19], a[132:135], a[24:27], v[248:251]\n"
  "v_fma_f32 v65, v65, s73, v227\n"
  "v_fma_f32 v66, v66, s73, v227\n"
  "v_mfma_f32_16x16x32_bf16 v[16:19], a[128:131], a[28:31], v[16:19]\n"
  "buffer_load_dwordx4 v221, s[32:35], 0 offen offset:128 lds\n"
  "v_fma_f32 v67, v67, s73, v227\n"
  "v_mfma_f32_16x16x32_bf16 v[16:19], a[124:127], a[32:35], v[16:19]\n"
  "s_addk_i32 m0, 1024\n"
  "ds_read_b64_tr_b16 a[136:137], v217 offset:24704\n"
  "v_fma_f32 v0, v0, s73, v223\n"
  "v_mfma_f32_16x16x32_bf16 v[16:19], a[120:123], a[36:39], v[16:19]\n"
  "s_mov_b64 s[32:33], s[44:45]\n"
  "ds_read_b64_tr_b16 a[138:139], v218 offset:24704\n"
  "v_fma_f32 v1, v1, s73, v223\n"
  "v_mfma_f32_16x16x32_bf16 v[16:19], a[144:147], a[40:43], v[16:19]\n"
  "ds_read_b64_tr_b16 a[140:141], v217 offset:25728\n"
  "v_fma_f32 v2, v2, s73, v223\n"
  "v_mfma_f32_16x16x32_bf16 v[16:19], a[148:151], a[44:47], v[16:19]\n"
  "buffer_load_dwordx4 v221, s[44:47], 0 offen offset:128 lds\n"
  "v_fma_f32 v3, v3, s73, v223\n"
  "v_mfma_f32_16x16x32_bf16 v[32:35], a[148:151], a[68:71], v[248:251]\n"
  "s_addk_i32 m0, 896\n"
  "ds_read_b64_tr_b16 a[142:143], v218 offset:25728\n"
  "v_mfma_f32_16x16x32_bf16 v[32:35], a[144:147], a[64:67], v[32:35]\n"
  "v_exp_f32 v65, v65\n"
  "v_mfma_f32_16x16x32_bf16 v[32:35], a[120:123], a[60:63], v[32:35]\n"
  "v_exp_f32 v66, v66\n"
  "v_mfma_f32_16x16x32_bf16 v[32:35], a[124:127], a[56:59], v[32:35]\n"
  "buffer_load_dwordx4 v221, s[44:47], 0 offen offset:256 lds\n"
  "v_cvt_pk_bf16_f32 v64, v64, v65\n"
  "v_mfma_f32_16x16x32_bf16 v[32:35], a[128:131], a[52:55], v[32:35]\n"
  "s_add_u32 s44, s44, s31\n"
  "v_exp_f32 v67, v67\n"
  "v_mfma_f32_16x16x32_bf16 v[32:35], a[132:135], a[48:51], v[32:35]\n"
  "s_addc_u32 s45, s45, 0\n"
  "v_exp_f32 v0, v0\n"
  "v_mfma_f32_16x16x32_bf16 v[48:51], a[132:135], a[72:75], v[248:251]\n"
  "ds_read_b64_tr_b16 a[132:133], v217 offset:24720\n"
  "v_cvt_pk_bf16_f32 v65, v66, v67\n"
  "v_mfma_f32_16x16x32_bf16 v[48:51], a[128:131], a[76:79], v[48:51]\n"
  "ds_read_b64_tr_b16 a[134:135], v218 offset:24720\n"
  "v_add_f32 v252, v252, v0\n"
  "v_mfma_f32_16x16x32_bf16 v[48:51], a[124:127], a[80:83], v[48:51]\n"
  "ds_read_b64_tr_b16 a[128:129], v217 offset:25744\n"
  "v_fma_f32 v16, v16, s73, v224\n"
  "v_mfma_f32_16x16x32_bf16 v[48:51], a[120:123], a[84:87], v[48:51]\n"
  "ds_read_b64_tr_b16 a[130:131], v218 offset:25744\n"
  "v_fma_f32 v17, v17, s73, v224\n"
  "v_mfma_f32_16x16x32_bf16 v[48:51], a[144:147], a[88:91], v[48:51]\n"
  "v_exp_f32 v1, v1\n"
  "v_mfma_f32_16x16x32_bf16 v[48:51], a[148:151], a[92:95], v[48:51]\n"
  "s_waitcnt lgkmcnt(4)\n"
  "v_exp_f32 v2, v2\n"
  "v_mfma_f32_16x16x32_bf16 a[236:239], a[160:163], a[136:139], a[236:239]\n"
  "v_exp_f32 v3, v3\n"
  "v_mfma_f32_16x16x32_bf16 a[236:239], a[156:159], a[140:143], a[236:239]\n"
  "v_exp_f32 v16, v16\n"
  "v_mfma_f32_16x16x32_bf16 v[184:187], v[60:63], a[136:139], v[184:187]\n"
  "v_exp_f32 v17, v17\n"
  "v_mfma_f32_16x16x32_bf16 v[184:187], v[56:59], a[140:143], v[184:187]\n"
  "v_add_f32 v252, v252, v1\n"
  "v_add_f32 v252, v252, v2\n"
  "v_mfma_f32_16x16x32_bf16 v[152:155], v[44:47], a[136:139], v[152:155]\n"
  "ds_read_b64_tr_b16 a[124:125], v219 offset:24704\n"
  "v_add_f32 v252, v252, v3\n"
  "v_mfma_f32_16x16x32_bf16 v[152:155], v[40:43], a[140:143], v[152:155]\n"
  "ds_read_b64_tr_b16 a[126:127], v220 offset:24704\n"
  "v_fma_f32 v18, v18, s73, v224\n"
  "v_mfma_f32_16x16x32_bf16 v[120:123], v[28:31], a[136:139], v[120:123]\n"
  "ds_read_b64_tr_b16 a[120:121], v219 offset:25728\n"
  "v_fma_f32 v19, v19, s73, v224\n"
  "v_mfma_f32_16x16x32_bf16 v[120:123], v[24:27], a[140:143], v[120:123]\n"
  "ds_read_b64_tr_b16 a[122:123], v220 offset:25728\n"
  "v_fma_f32 v32, v32, s73, v225\n"
  "v_mfma_f32_16x16x32_bf16 v[88:91], v[12:15], a[136:139], v[88:91]\n"
  "v_exp_f32 v18, v18\n"
  "v_mfma_f32_16x16x32_bf16 v[88:91], v[8:11], a[140:143], v[88:91]\n"
  "s_waitcnt lgkmcnt(4)\n"
  "v_exp_f32 v19, v19\n"
  "v_mfma_f32_16x16x32_bf16 v[92:95], v[12:15], a[132:135], v[92:95]\n"
  "v_exp_f32 v32, v32\n"
  "v_mfma_f32_16x16x32_bf16 v[92:95], v[8:11], a[128:131], v[92:95]\n"
  "v_fma_f32 v33, v33, s73, v225\n"
  "v_fma_f32 v34, v34, s73, v225\n"
  "v_mfma_f32_16x16x32_bf16 v[124:127], v[28:31], a[132:135], v[124:127]\n"
  "ds_read_b128 a[136:139], v235 offset:24704\n"
  "v_fma_f32 v35, v35, s73, v225\n"
  "v_mfma_f32_16x16x32_bf16 v[124:127], v[24:27], a[128:131], v[124:127]\n"
  "ds_read_b128 a[140:143], v235 offset:24768\n"
  "v_fma_f32 v48, v48, s73, v226\n"
  "v_mfma_f32_16x16x32_bf16 v[156:159], v[44:47], a[132:135], v[156:159]\n"
  "ds_read_b128 a[144:147], v235 offset:25728\n"
  "v_fma_f32 v49, v49, s73, v226\n"
  "v_mfma_f32_16x16x32_bf16 v[156:159], v[40:43], a[128:131], v[156:159]\n"
  "ds_read_b128 a[148:151], v235 offset:25792\n"
  "v_fma_f32 v50, v50, s73, v226\n"
  "v_mfma_f32_16x16x32_bf16 v[188:191], v[60:63], a[132:135], v[188:191]\n"
  "v_exp_f32 v33, v33\n"
  "v_mfma_f32_16x16x32_bf16 v[188:191], v[56:59], a[128:131], v[188:191]\n"
  "v_exp_f32 v34, v34\n"
  "v_mfma_f32_16x16x32_bf16 a[240:243], a[160:163], a[132:135], a[240:243]\n"
  "ds_read_b128 a[132:135], v235 offset:26752\n"
  "v_fma_f32 v51, v51, s73, v226\n"
  "v_mfma_f32_16x16x32_bf16 a[240:243], a[156:159], a[128:131], a[240:243]\n"
  "s_waitcnt lgkmcnt(5)\n"
  "ds_read_b128 a[128:131], v235 offset:26816\n"
  "v_add_f32 v253, v253, v16\n"
  "v_mfma_f32_16x16x32_bf16 v[192:195], v[60:63], a[124:127], v[192:195]\n"
  "v_exp_f32 v35, v35\n"
  "v_mfma_f32_16x16x32_bf16 v[192:195], v[56:59], a[120:123], v[192:195]\n"
  "ds_write_b64 v231, v[64:65] offset:2048\n"
  "v_mfma_f32_16x16x32_bf16 v[160:163], v[44:47], a[124:127], v[160:163]\n"
  "v_exp_f32 v48, v48\n"
  "v_mfma_f32_16x16x32_bf16 v[160:163], v[40:43], a[120:123], v[160:163]\n"
  "v_exp_f32 v49, v49\n"
  "v_mfma_f32_16x16x32_bf16 v[128:131], v[28:31], a[124:127], v[128:131]\n"
  "v_exp_f32 v50, v50\n"
  "v_mfma_f32_16x16x32_bf16 v[128:131], v[24:27], a[120:123], v[128:131]\n"
  "v_exp_f32 v51, v51\n"
  "v_mfma_f32_16x16x32_bf16 v[96:99], v[12:15], a[124:127], v[96:99]\n"
  "v_add_f32 v253, v253, v17\n"
  "v_add_f32 v253, v253, v18\n"
  "v_mfma_f32_16x16x32_bf16 v[96:99], v[8:11], a[120:123], v[96:99]\n"
  "s_waitcnt lgkmcnt(5)\n"
  "v_add_f32 v253, v253, v19\n"
  "v_add_f32 v254, v254, v32\n"
  "v_mfma_f32_16x16x32_bf16 v[4:7], a[136:139], a[0:3], v[248:251]\n"
  "v_add_f32 v254, v254, v33\n"
  "v_add_f32 v254, v254, v34\n"
  "v_mfma_f32_16x16x32_bf16 v[4:7], a[140:143], a[4:7], v[4:7]\n"
  "s_waitcnt lgkmcnt(3)\n"
  "v_add_f32 v254, v254, v35\n"
  "v_add_f32 v255, v255, v48\n"
  "v_mfma_f32_16x16x32_bf16 v[4:7], a[144:147], a[8:11], v[4:7]\n"
  "v_add_f32 v255, v255, v49\n"
  "v_add_f32 v255, v255, v50\n"
  "v_mfma_f32_16x16x32_bf16 v[4:7], a[148:151], a[12:15], v[4:7]\n"
  "s_waitcnt lgkmcnt(1)\n"
  "v_add_f32 v255, v255, v51\n"
  "v_mfma_f32_16x16x32_bf16 v[4:7], a[132:135], a[16:19], v[4:7]\n"
  "v_mfma_f32_16x16x32_bf16 v[4:7], a[128:131], a[20:23], v[4:7]\n"
  "v_mfma_f32_16x16x32_bf16 v[20:23], a[128:131], a[44:47], v[248:251]\n"
  "v_mfma_f32_16x16x32_bf16 v[20:23], a[132:135], a[40:43], v[20:23]\n"
  "v_mfma_f32_16x16x32_bf16 v[20:23], a[148:151], a[36:39], v[20:23]\n"
  "ds_read_b64_tr_b16 a[124:125], v219 offset:24720\n"
  "v_fma_f32 v4, v4, s73, v223\n"
  "v_mfma_f32_16x16x32_bf16 v[20:23], a[144:147], a[32:35], v[20:23]\n"
  "ds_read_b64_tr_b16 a[126:127], v220 offset:24720\n"
  "v_fma_f32 v5, v5, s73, v223\n"
  "v_mfma_f32_16x16x32_bf16 v[20:23], a[140:143], a[28:31], v[20:23]\n"
  "ds_read_b64_tr_b16 a[120:121], v219 offset:25744\n"
  "v_fma_f32 v6, v6, s73, v223\n"
  "v_mfma_f32_16x16x32_bf16 v[20:23], a[136:139], a[24:27], v[20:23]\n"
  "ds_read_b64_tr_b16 a[122:123], v220 offset:25744\n"
  "v_fma_f32 v7, v7, s73, v223\n"
  "v_mfma_f32_16x16x32_bf16 v[36:39], a[136:139], a[48:51], v[248:251]\n"
  "v_exp_f32 v4, v4\n"
  "v_mfma_f32_16x16x32_bf16 v[36:39], a[140:143], a[52:55], v[36:39]\n"
  "v_exp_f32 v5, v5\n"
  "v_mfma_f32_16x16x32_bf16 v[36:39], a[144:147], a[56:59], v[36:39]\n"
  "v_exp_f32 v6, v6\n"
  "v_mfma_f32_16x16x32_bf16 v[36:39], a[148:151], a[60:63], v[36:39]\n"
  "v_exp_f32 v7, v7\n"
  "v_mfma_f32_16x16x32_bf16 v[36:39], a[132:135], a[64:67], v[36:39]\n"
  "v_fma_f32 v20, v20, s73, v224\n"
  "v_fma_f32 v21, v21, s73, v224\n"
  "v_mfma_f32_16x16x32_bf16 v[36:39], a[128:131], a[68:71], v[36:39]\n"
  "v_exp_f32 v20, v20\n"
  "v_mfma_f32_16x16x32_bf16 v[52:55], a[128:131], a[92:95], v[248:251]\n"
  "ds_read_b64_tr_b16 a[128:129], v217 offset:26752\n"
  "v_fma_f32 v22, v22, s73, v224\n"
  "v_mfma_f32_16x16x32_bf16 v[52:55], a[132:135], a[88:91], v[52:55]\n"
  "ds_read_b64_tr_b16 a[130:131], v218 offset:26752\n"
  "v_fma_f32 v23, v23, s73, v224\n"
  "v_mfma_f32_16x16x32_bf16 v[52:55], a[148:151], a[84:87], v[52:55]\n"
  "ds_read_b64_tr_b16 a[132:133], v217 offset:26768\n"
  "v_fma_f32 v36, v36, s73, v225\n"
  "v_mfma_f32_16x16x32_bf16 v[52:55], a[144:147], a[80:83], v[52:55]\n"
  "ds_read_b64_tr_b16 a[134:135], v218 offset:26768\n"
  "v_fma_f32 v37, v37, s73, v225\n"
  "v_mfma_f32_16x16x32_bf16 v[52:55], a[140:143], a[76:79], v[52:55]\n"
  "v_exp_f32 v21, v21\n"
  "v_mfma_f32_16x16x32_bf16 v[52:55], a[136:139], a[72:75], v[52:55]\n"
  "s_waitcnt lgkmcnt(4)\n"
  "v_exp_f32 v22, v22\n"
  "v_mfma_f32_16x16x32_bf16 v[100:103], v[12:15], a[124:127], v[100:103]\n"
  "v_exp_f32 v23, v23\n"
  "v_mfma_f32_16x16x32_bf16 v[100:103], v[8:11], a[120:123], v[100:103]\n"
  "v_exp_f32 v36, v36\n"
  "v_mfma_f32_16x16x32_bf16 v[132:135], v[28:31], a[124:127], v[132:135]\n"
  "ds_read_b64_tr_b16 a[148:149], v219 offset:26752\n"
  "v_cvt_pk_bf16_f32 v12, v0, v1\n"
  "v_mfma_f32_16x16x32_bf16 v[132:135], v[24:27], a[120:123], v[132:135]\n"
  "ds_read_b64_tr_b16 a[150:151], v220 offset:26752\n"
  "v_cvt_pk_bf16_f32 v13, v2, v3\n"
  "v_mfma_f32_16x16x32_bf16 v[164:167], v[44:47], a[124:127], v[164:167]\n"
  "ds_read_b64_tr_b16 a[144:145], v219 offset:26768\n"
  "v_fma_f32 v38, v38, s73, v225\n"
  "v_mfma_f32_16x16x32_bf16 v[164:167], v[40:43], a[120:123], v[164:167]\n"
  "ds_read_b64_tr_b16 a[146:147], v220 offset:26768\n"
  "v_fma_f32 v39, v39, s73, v225\n"
  "v_mfma_f32_16x16x32_bf16 v[196:199], v[60:63], a[124:127], v[196:199]\n"
  "v_exp_f32 v37, v37\n"
  "v_mfma_f32_16x16x32_bf16 v[196:199], v[56:59], a[120:123], v[196:199]\n"
  "s_waitcnt lgkmcnt(4)\n"
  "v_exp_f32 v38, v38\n"
  "v_mfma_f32_16x16x32_bf16 a[244:247], a[156:159], a[128:131], a[244:247]\n"
  "v_exp_f32 v39, v39\n"
  "v_mfma_f32_16x16x32_bf16 a[248:251], a[156:159], a[132:135], a[248:251]\n"
  "v_fma_f32 v52, v52, s73, v226\n"
  "v_fma_f32 v53, v53, s73, v226\n"
  "v_mfma_f32_16x16x32_bf16 v[200:203], v[56:59], a[128:131], v[200:203]\n"
  "v_exp_f32 v52, v52\n"
  "v_mfma_f32_16x16x32_bf16 v[204:207], v[56:59], a[132:135], v[204:207]\n"
  "v_exp_f32 v53, v53\n"
  "v_mfma_f32_16x16x32_bf16 v[168:171], v[40:43], a[128:131], v[168:171]\n"
  "v_fma_f32 v54, v54, s73, v226\n"
  "v_fma_f32 v55, v55, s73, v226\n"
  "v_mfma_f32_16x16x32_bf16 v[172:175], v[40:43], a[132:135], v[172:175]\n"
  "s_waitcnt vmcnt(11) lgkmcnt(0)\n"
  "s_barrier\n"
  "ds_read_b128 a[120:123], v216 offset:0\n"
  "v_mfma_f32_16x16x32_bf16 v[136:139], v[24:27], a[128:131], v[136:139]\n"
  "ds_read_b64 a[160:161], v231 offset:2048\n"
  "v_add_f32 v252, v252, v4\n"
  "v_mfma_f32_16x16x32_bf16 v[140:143], v[24:27], a[132:135], v[140:143]\n"
  "ds_read_b64 a[162:163], v232 offset:2048\n"
  "v_add_f32 v252, v252, v5\n"
  "v_mfma_f32_16x16x32_bf16 v[104:107], v[8:11], a[128:131], v[104:107]\n"
  "ds_read_b128 a[124:127], v216 offset:64\n"
  "v_add_f32 v252, v252, v6\n"
  "v_mfma_f32_16x16x32_bf16 v[108:111], v[8:11], a[132:135], v[108:111]\n"
  "ds_read_b128 a[128:131], v216 offset:1024\n"
  "v_add_f32 v252, v252, v7\n"
  "v_mfma_f32_16x16x32_bf16 v[112:115], v[8:11], a[148:151], v[112:115]\n"
  "ds_read_b128 a[132:135], v216 offset:1088\n"
  "v_add_f32 v253, v253, v20\n"
  "v_mfma_f32_16x16x32_bf16 v[116:119], v[8:11], a[144:147], v[116:119]\n"
  "s_mov_b32 m0, s68\n"
  "ds_read_b128 a[136:139], v216 offset:2048\n"
  "v_add_f32 v253, v253, v21\n"
  "v_mfma_f32_16x16x32_bf16 v[144:147], v[24:27], a[148:151], v[144:147]\n"
  "ds_read_b128 a[140:143], v216 offset:2112\n"
  "v_add_f32 v253, v253, v22\n"
  "v_mfma_f32_16x16x32_bf16 v[148:151], v[24:27], a[144:147], v[148:151]\n"
  "v_exp_f32 v54, v54\n"
  "v_mfma_f32_16x16x32_bf16 v[176:179], v[40:43], a[148:151], v[176:179]\n"
  "v_exp_f32 v55, v55\n"
  "v_mfma_f32_16x16x32_bf16 v[180:183], v[40:43], a[144:147], v[180:183]\n"
  "buffer_load_dwordx4 v221, s[40:43], 0 offen offset:0 lds\n"
  "v_mfma_f32_16x16x32_bf16 v[208:211], v[56:59], a[148:151], v[208:211]\n"
  "s_addk_i32 m0, 896\n"
  "v_add_f32 v253, v253, v23\n"
  "v_add_f32 v254, v254, v36\n"
  "v_mfma_f32_16x16x32_bf16 v[212:215], v[56:59], a[144:147], v[212:215]\n"
  "s_waitcnt lgkmcnt(4)\n"
  "v_add_f32 v254, v254, v37\n"
  "v_add_f32 v254, v254, v38\n"
  "v_mfma_f32_16x16x32_bf16 v[64:67], a[120:123], a[96:99], v[248:251]\n"
  "v_add_f32 v254, v254, v39\n"
  "v_add_f32 v255, v255, v52\n"
  "v_mfma_f32_16x16x32_bf16 v[64:67], a[124:127], a[100:103], v[64:67]\n"
  "s_waitcnt lgkmcnt(2)\n"
  "buffer_load_dwordx4 v221, s[40:43], 0 offen offset:128 lds\n"
  "v_add_f32 v255, v255, v53\n"
  "v_mfma_f32_16x16x32_bf16 v[64:67], a[128:131], a[104:107], v[64:67]\n"
  "s_addk_i32 m0, 896\n"
  "v_add_f32 v255, v255, v54\n"
  "v_add_f32 v255, v255, v55\n"
  "v_mfma_f32_16x16x32_bf16 v[64:67], a[132:135], a[108:111], v[64:67]\n"
  "s_waitcnt lgkmcnt(0)\n"
  "v_cvt_pk_bf16_f32 v14, v4, v5\n"
  "v_cvt_pk_bf16_f32 v15, v6, v7\n"
  "v_mfma_f32_16x16x32_bf16 v[64:67], a[136:139], a[112:115], v[64:67]\n"
  "v_cvt_pk_bf16_f32 v28, v16, v17\n"
  "v_cvt_pk_bf16_f32 v29, v18, v19\n"
  "v_mfma_f32_16x16x32_bf16 v[64:67], a[140:143], a[116:119], v[64:67]\n"
  "buffer_load_dwordx4 v221, s[40:43], 0 offen offset:256 lds\n"
  "v_cvt_pk_bf16_f32 v30, v20, v21\n"
  "v_mfma_f32_16x16x32_bf16 v[0:3], a[140:143], a[20:23], v[248:251]\n"
  "v_cvt_pk_bf16_f32 v31, v22, v23\n"
  "v_cvt_pk_bf16_f32 v44, v32, v33\n"
  "v_mfma_f32_16x16x32_bf16 v[0:3], a[136:139], a[16:19], v[0:3]\n"
  "v_cvt_pk_bf16_f32 v45, v34, v35\n"
  "v_cvt_pk_bf16_f32 v46, v36, v37\n"
  "v_mfma_f32_16x16x32_bf16 v[0:3], a[132:135], a[12:15], v[0:3]\n"
  "v_cvt_pk_bf16_f32 v47, v38, v39\n"
  "v_cvt_pk_bf16_f32 v60, v48, v49\n"
  "v_mfma_f32_16x16x32_bf16 v[0:3], a[128:131], a[8:11], v[0:3]\n"
  "v_cvt_pk_bf16_f32 v61, v50, v51\n"
  "v_cvt_pk_bf16_f32 v62, v52, v53\n"
  "v_mfma_f32_16x16x32_bf16 v[0:3], a[124:127], a[4:7], v[0:3]\n"
  "v_cvt_pk_bf16_f32 v63, v54, v55\n"
  "v_mfma_f32_16x16x32_bf16 v[0:3], a[120:123], a[0:3], v[0:3]\n"
  "s_add_u32 s40, s40, 24576\n"
  "s_addc_u32 s41, s41, 0\n"
  "s_branch L_BIG_%=\n"
  ".p2align 5\n"
  "L_TAIL_%=:\n"
  "ds_read_b64_tr_b16 a[120:121], v217 offset:26752\n"
  "ds_read_b64_tr_b16 a[122:123], v218 offset:26752\n"
  "v_exp_f32_e64 v55, v55\n"
  "v_cvt_pk_bf16_f32 v45, v34, v35\n"
  "ds_read_b64_tr_b16 a[124:125], v217 offset:26768\n"
  "ds_read_b64_tr_b16 a[126:127], v218 offset:26768\n"
  "v_cvt_pk_bf16_f32 v46, v36, v37\n"
  "v_cvt_pk_bf16_f32 v47, v38, v39\n"
  "v_cvt_pk_bf16_f32 v60, v48, v49\n"
  "s_waitcnt lgkmcnt(8)\n"
  "ds_read_b64_tr_b16 a[128:129], v219 offset:26752\n"
  "ds_read_b64_tr_b16 a[130:131], v220 offset:26752\n"
  "v_cvt_pk_bf16_f32 v61, v50, v51\n"
  "v_cvt_pk_bf16_f32 v62, v52, v53\n"
  "v_cvt_pk_bf16_f32 v63, v54, v55\n"
  "v_mfma_f32_16x16x32_bf16 v[84:87], a[156:159], a[152:155], v[84:87]\n"
  "s_nop 0\n"
  "ds_read_b64_tr_b16 a[132:133], v219 offset:26768\n"
  "ds_read_b64_tr_b16 a[134:135], v220 offset:26768\n"
  "v_add_u32 v233, s61, v229\n"
  "s_mov_b32 exec_lo, 0\n"
  "v_add_u32 v233, s69, v233\n"
  "s_mov_b32 exec_lo, -1\n"
  "v_mfma_f32_16x16x32_bf16 v[84:87], a[160:163], a[152:155], v[84:87]\n"
  "ds_read_b64_tr_b16 a[136:137], v217 offset:24704\n"
  "ds_read_b64_tr_b16 a[138:139], v218 offset:24704\n"
  "v_mfma_f32_16x16x32_bf16 v[68:71], v[12:15], a[152:155], v[68:71]\n"
  "ds_read_b64_tr_b16 a[140:141], v217 offset:25728\n"
  "ds_read_b64_tr_b16 a[142:143], v218 offset:25728\n"
  "v_mfma_f32_16x16x32_bf16 v[72:75], v[28:31], a[152:155], v[72:75]\n"
  "ds_read_b64_tr_b16 a[144:145], v217 offset:24720\n"
  "ds_read_b64_tr_b16 a[146:147], v218 offset:24720\n"
  "v_mfma_f32_16x16x32_bf16 v[76:79], v[44:47], a[152:155], v[76:79]\n"
  "ds_read_b64_tr_b16 a[148:149], v217 offset:25744\n"
  "ds_read_b64_tr_b16 a[150:151], v218 offset:25744\n"
  "v_mfma_f32_16x16x32_bf16 v[80:83], v[60:63], a[152:155], v[80:83]\n"
  "s_waitcnt lgkmcnt(12)\n"
  "v_maximum3_f32 v236, v68, v69, v70\n"
  "v_rcp_f32 v68, v68\n"
  "v_rcp_f32_e64 v69, v69\n"
  "v_mfma_f32_16x16x32_bf16 a[244:247], a[160:163], a[120:123], a[244:247]\n"
  "v_maximum3_f32 v236, v236, v71, v72\n"
  "v_rcp_f32 v70, v70\n"
  "v_rcp_f32 v71, v71\n"
  "v_mfma_f32_16x16x32_bf16 a[248:251], a[160:163], a[124:127], a[248:251]\n"
  "v_maximum3_f32 v236, v236, v73, v74\n"
  "v_rcp_f32 v72, v72\n"
  "v_rcp_f32 v73, v73\n"
  "v_mfma_f32_16x16x32_bf16 v[104:107], v[12:15], a[120:123], v[104:107]\n"
  "v_maximum3_f32 v236, v236, v75, v76\n"
  "v_rcp_f32 v74, v74\n"
  "v_rcp_f32 v75, v75\n"
  "v_mfma_f32_16x16x32_bf16 v[108:111], v[12:15], a[124:127], v[108:111]\n"
  "s_waitcnt lgkmcnt(8)\n"
  "v_maximum3_f32 v236, v236, v77, v78\n"
  "v_rcp_f32 v76, v76\n"
  "v_rcp_f32 v77, v77\n"
  "v_mfma_f32_16x16x32_bf16 v[112:115], v[12:15], a[128:131], v[112:115]\n"
  "v_maximum3_f32 v236, v236, v79, v80\n"
  "v_rcp_f32 v78, v78\n"
  "v_rcp_f32_e64 v79, v79\n"
  "v_mfma_f32_16x16x32_bf16 v[116:119], v[12:15], a[132:135], v[116:119]\n"
  "v_maximum3_f32 v236, v236, v81, v82\n"
  "v_rcp_f32 v80, v80\n"
  "v_rcp_f32 v81, v81\n"
  "v_pk_fma_f32 v[104:105], v[104:105], v[68:69], v[68:69] neg_lo:[0,0,1] neg_hi:[0,0,1]\n"
  "v_pk_fma_f32 v[106:107], v[106:107], v[70:71], v[70:71] neg_lo:[0,0,1] neg_hi:[0,0,1]\n"
  "v_mfma_f32_16x16x32_bf16 v[136:139], v[28:31], a[120:123], v[136:139]\n"
  "v_maximum3_f32 v236, v236, v83, v84\n"
  "v_rcp_f32 v82, v82\n"
  "v_rcp_f32 v83, v83\n"
  "v_pk_fma_f32 v[108:109], v[108:109], v[68:69], v[68:69] neg_lo:[0,0,1] neg_hi:[0,0,1]\n"
  "v_pk_fma_f32 v[110:111], v[110:111], v[70:71], v[70:71] neg_lo:[0,0,1] neg_hi:[0,0,1]\n"
  "v_mfma_f32_16x16x32_bf16 v[140:143], v[28:31], a[124:127], v[140:143]\n"
  "v_cvt_pk_bf16_f32 v104, v104, v108\n"
  "v_cvt_pk_bf16_f32 v108, v105, v109\n"
  "v_cvt_pk_bf16_f32 v106, v106, v110\n"
  "v_cvt_pk_bf16_f32 v110, v107, v111\n"
  "v_maximum3_f32 v236, v236, v85, v86\n"
  "v_pk_fma_f32 v[112:113], v[112:113], v[68:69], v[68:69] neg_lo:[0,0,1] neg_hi:[0,0,1]\n"
  "v_pk_fma_f32 v[114:115], v[114:115], v[70:71], v[70:71] neg_lo:[0,0,1] neg_hi:[0,0,1]\n"
  "v_mfma_f32_16x16x32_bf16 v[144:147], v[28:31], a[128:131], v[144:147]\n"
  "v_maximum3_f32 v236, v236, v87, v87\n"
  "v_rcp_f32_e64 v84, v84\n"
  "v_rcp_f32_e64 v85, v85\n"
  "v_pk_fma_f32 v[116:117], v[116:117], v[68:69], v[68:69] neg_lo:[0,0,1] neg_hi:[0,0,1]\n"
  "v_pk_fma_f32 v[118:119], v[118:119], v[70:71], v[70:71] neg_lo:[0,0,1] neg_hi:[0,0,1]\n"
  "v_mfma_f32_16x16x32_bf16 v[148:151], v[28:31], a[132:135], v[148:151]\n"
  "v_cvt_pk_bf16_f32 v105, v112, v116\n"
  "v_cvt_pk_bf16_f32 v109, v113, v117\n"
  "v_cvt_pk_bf16_f32 v107, v114, v118\n"
  "v_cvt_pk_bf16_f32 v111, v115, v119\n"
  "v_cmp_ngt_f32_e64 vcc, s75, v236\n"
  "v_pk_fma_f32 v[136:137], v[136:137], v[72:73], v[72:73] neg_lo:[0,0,1] neg_hi:[0,0,1]\n"
  "v_pk_fma_f32 v[138:139], v[138:139], v[74:75], v[74:75] neg_lo:[0,0,1] neg_hi:[0,0,1]\n"
  "v_mfma_f32_16x16x32_bf16 v[168:171], v[44:47], a[120:123], v[168:171]\n"
  "ds_write_b64 v229, v[104:105] offset:8960\n"
  "v_rcp_f32_e64 v86, v86\n"
  "v_pk_fma_f32 v[140:141], v[140:141], v[72:73], v[72:73] neg_lo:[0,0,1] neg_hi:[0,0,1]\n"
  "v_pk_fma_f32 v[142:143], v[142:143], v[74:75], v[74:75] neg_lo:[0,0,1] neg_hi:[0,0,1]\n"
  "v_mfma_f32_16x16x32_bf16 v[172:175], v[44:47], a[124:127], v[172:175]\n"
  "s_cmp_lg_u64 vcc, 0\n"
  "s_cselect_b32 s79, s77, 0\n"
  "s_or_b32 s78, s78, s79\n"
  "s_lshl_b32 s77, s77, 1\n"
  "ds_write_b64 v229, v[108:109] offset:9248\n"
  "v_cvt_pk_bf16_f32 v136, v136, v140\n"
  "v_cvt_pk_bf16_f32 v140, v137, v141\n"
  "v_pk_fma_f32 v[144:145], v[144:145], v[72:73], v[72:73] neg_lo:[0,0,1] neg_hi:[0,0,1]\n"
  "v_pk_fma_f32 v[146:147], v[146:147], v[74:75], v[74:75] neg_lo:[0,0,1] neg_hi:[0,0,1]\n"
  "v_mfma_f32_16x16x32_bf16 v[176:179], v[44:47], a[128:131], v[176:179]\n"
  "ds_write_b64 v229, v[106:107] offset:9536\n"
  "v_cvt_pk_bf16_f32 v138, v138, v142\n"
  "v_cvt_pk_bf16_f32 v142, v139, v143\n"
  "v_pk_fma_f32 v[148:149], v[148:149], v[72:73], v[72:73] neg_lo:[0,0,1] neg_hi:[0,0,1]\n"
  "v_pk_fma_f32 v[150:151], v[150:151], v[74:75], v[74:75] neg_lo:[0,0,1] neg_hi:[0,0,1]\n"
  "v_mfma_f32_16x16x32_bf16 v[180:183], v[44:47], a[132:135], v[180:183]\n"
  "ds_write_b64 v229, v[110:111] offset:9824\n"
  "v_cvt_pk_bf16_f32 v137, v144, v148\n"
  "v_cvt_pk_bf16_f32 v141, v145, v149\n"
  "v_pk_fma_f32 v[168:169], v[168:169], v[76:77], v[76:77] neg_lo:[0,0,1] neg_hi:[0,0,1]\n"
  "v_pk_fma_f32 v[170:171], v[170:171], v[78:79], v[78:79] neg_lo:[0,0,1] neg_hi:[0,0,1]\n"
  "v_mfma_f32_16x16x32_bf16 v[200:203], v[60:63], a[120:123], v[200:203]\n"
  "ds_read_b64_tr_b16 a[120:121], v219 offset:24704\n"
  "ds_read_b64_tr_b16 a[122:123], v220 offset:24704\n"
  "ds_write_b64 v229, v[136:137] offset:11200\n"
  "v_pk_fma_f32 v[172:173], v[172:173], v[76:77], v[76:77] neg_lo:[0,0,1] neg_hi:[0,0,1]\n"
  "v_pk_fma_f32 v[174:175], v[174:175], v[78:79], v[78:79] neg_lo:[0,0,1] neg_hi:[0,0,1]\n"
  "v_mfma_f32_16x16x32_bf16 v[204:207], v[60:63], a[124:127], v[204:207]\n"
  "ds_read_b64_tr_b16 a[124:125], v219 offset:25728\n"
  "ds_read_b64_tr_b16 a[126:127], v220 offset:25728\n"
  "ds_write_b64 v229, v[140:141] offset:11488\n"
  "v_pk_fma_f32 v[176:177], v[176:177], v[76:77], v[76:77] neg_lo:[0,0,1] neg_hi:[0,0,1]\n"
  "v_pk_fma_f32 v[178:179], v[178:179], v[78:79], v[78:79] neg_lo:[0,0,1] neg_hi:[0,0,1]\n"
  "v_mfma_f32_16x16x32_bf16 v[208:211], v[60:63], a[128:131], v[208:211]\n"
  "ds_read_b64_tr_b16 a[128:129], v219 offset:24720\n"
  "ds_read_b64_tr_b16 a[130:131], v220 offset:24720\n"
  "ds_read_b64_tr_b16 v[244:245], v230 offset:8960\n"
  "ds_read_b64_tr_b16 v[246:247], v230 offset:8992\n"
  "v_cvt_pk_bf16_f32 v139, v146, v150\n"
  "v_pk_fma_f32 v[180:181], v[180:181], v[76:77], v[76:77] neg_lo:[0,0,1] neg_hi:[0,0,1]\n"
  "v_pk_fma_f32 v[182:183], v[182:183], v[78:79], v[78:79] neg_lo:[0,0,1] neg_hi:[0,0,1]\n"
  "v_mfma_f32_16x16x32_bf16 v[212:215], v[60:63], a[132:135], v[212:215]\n"
  "s_nop 0\n"
  "s_waitcnt lgkmcnt(15)\n"
  "ds_read_b64_tr_b16 a[132:133], v219 offset:25744\n"
  "ds_read_b64_tr_b16 a[134:135], v220 offset:25744\n"
  "ds_write_b64 v229, v[138:139] offset:11776\n"
  "v_pk_fma_f32 v[200:201], v[200:201], v[80:81], v[80:81] neg_lo:[0,0,1] neg_hi:[0,0,1]\n"
  "v_pk_fma_f32 v[202:203], v[202:203], v[82:83], v[82:83] neg_lo:[0,0,1] neg_hi:[0,0,1]\n"
  "v_mfma_f32_16x16x32_bf16 a[228:231], a[156:159], a[136:139], a[228:231]\n"
  "ds_read_b64_tr_b16 a[252:253], v230 offset:10080\n"
  "ds_read_b64_tr_b16 a[254:255], v230 offset:10112\n"
  "v_cvt_pk_bf16_f32 v143, v147, v151\n"
  "v_rcp_f32 v87, v87\n"
  "v_pk_fma_f32 v[204:205], v[204:205], v[80:81], v[80:81] neg_lo:[0,0,1] neg_hi:[0,0,1]\n"
  "v_pk_fma_f32 v[206:207], v[206:207], v[82:83], v[82:83] neg_lo:[0,0,1] neg_hi:[0,0,1]\n"
  "v_mfma_f32_16x16x32_bf16 a[228:231], a[160:163], a[140:143], a[228:231]\n"
  "s_waitcnt lgkmcnt(15)\n"
  "ds_write_b64 v229, v[142:143] offset:12064\n"
  "v_cvt_pk_bf16_f32 v168, v168, v172\n"
  "v_cvt_pk_bf16_f32 v172, v169, v173\n"
  "v_pk_fma_f32 v[208:209], v[208:209], v[80:81], v[80:81] neg_lo:[0,0,1] neg_hi:[0,0,1]\n"
  "v_pk_fma_f32 v[210:211], v[210:211], v[82:83], v[82:83] neg_lo:[0,0,1] neg_hi:[0,0,1]\n"
  "v_mfma_f32_16x16x32_bf16 a[232:235], a[156:159], a[144:147], a[232:235]\n"
  "v_cvt_pk_bf16_f32 v169, v176, v180\n"
  "v_cvt_pk_bf16_f32 v173, v177, v181\n"
  "v_cvt_pk_bf16_f32 v170, v170, v174\n"
  "v_cvt_pk_bf16_f32 v174, v171, v175\n"
  "v_cvt_pk_bf16_f32 v171, v178, v182\n"
  "v_pk_fma_f32 v[212:213], v[212:213], v[80:81], v[80:81] neg_lo:[0,0,1] neg_hi:[0,0,1]\n"
  "v_pk_fma_f32 v[214:215], v[214:215], v[82:83], v[82:83] neg_lo:[0,0,1] neg_hi:[0,0,1]\n"
  "v_mfma_f32_16x16x32_bf16 a[232:235], a[160:163], a[148:151], a[232:235]\n"
  "ds_read_b64_tr_b16 v[0:1], v230 offset:11200\n"
  "ds_read_b64_tr_b16 v[2:3], v230 offset:11232\n"
  "ds_write_b64 v229, v[168:169] offset:13440\n"
  "v_mfma_f32_16x16x32_bf16 a[164:167], v[8:11], a[136:139], a[164:167]\n"
  "s_waitcnt lgkmcnt(9)\n"
  "buffer_store_dwordx4 v[244:247], v228, s[52:55], s80 offen offset:256 nt\n"
  "ds_write_b64 v229, v[172:173] offset:13728\n"
  "v_cvt_pk_bf16_f32 v175, v179, v183\n"
  "v_mfma_f32_16x16x32_bf16 a[164:167], v[12:15], a[140:143], a[164:167]\n"
  "ds_read_b64_tr_b16 v[4:5], v230 offset:12320\n"
  "ds_read_b64_tr_b16 v[6:7], v230 offset:12352\n"
  "ds_write_b64 v229, v[170:171] offset:14016\n"
  "v_mfma_f32_16x16x32_bf16 a[168:171], v[8:11], a[144:147], a[168:171]\n"
  "s_waitcnt lgkmcnt(8)\n"
  "buffer_store_dwordx4 a[252:255], v228, s[52:55], s80 offen offset:3328 nt\n"
  "ds_write_b64 v229, v[174:175] offset:14304\n"
  "v_cvt_pk_bf16_f32 v200, v200, v204\n"
  "v_mfma_f32_16x16x32_bf16 a[168:171], v[12:15], a[148:151], a[168:171]\n"
  "v_cvt_pk_bf16_f32 v204, v201, v205\n"
  "v_cvt_pk_bf16_f32 v201, v208, v212\n"
  "v_cvt_pk_bf16_f32 v205, v209, v213\n"
  "v_cvt_pk_bf16_f32 v202, v202, v206\n"
  "v_cvt_pk_bf16_f32 v206, v203, v207\n"
  "v_mfma_f32_16x16x32_bf16 a[180:183], v[24:27], a[136:139], a[180:183]\n"
  "ds_read_b64_tr_b16 v[16:17], v230 offset:13440\n"
  "ds_read_b64_tr_b16 v[18:19], v230 offset:13472\n"
  "ds_write_b64 v229, v[200:201] offset:15680\n"
  "v_mfma_f32_16x16x32_bf16 a[180:183], v[28:31], a[140:143], a[180:183]\n"
  "s_waitcnt lgkmcnt(9)\n"
  "buffer_store_dwordx4 v[0:3], v228, s[52:55], s81 offen offset:256 nt\n"
  "ds_write_b64 v229, v[204:205] offset:15968\n"
  "v_cvt_pk_bf16_f32 v203, v210, v214\n"
  "v_mfma_f32_16x16x32_bf16 a[184:187], v[24:27], a[144:147], a[184:187]\n"
  "ds_read_b64_tr_b16 v[20:21], v230 offset:14560\n"
  "ds_write_b64 v229, v[202:203] offset:16256\n"
  "v_cvt_pk_bf16_f32 v207, v211, v215\n"
  "v_mfma_f32_16x16x32_bf16 a[184:187], v[28:31], a[148:151], a[184:187]\n"
  "s_waitcnt lgkmcnt(8)\n"
  "buffer_store_dwordx4 v[4:7], v228, s[52:55], s81 offen offset:3328 nt\n"
  "ds_read_b64_tr_b16 v[22:23], v230 offset:14592\n"
  "ds_write_b64 v229, v[206:207] offset:16544\n"
  "v_mfma_f32_16x16x32_bf16 a[196:199], v[40:43], a[136:139], a[196:199]\n"
  "v_accvgpr_read_b32 v104, a164\n"
  "v_accvgpr_read_b32 v105, a165\n"
  "v_accvgpr_read_b32 v108, a168\n"
  "v_accvgpr_read_b32 v109, a169\n"
  "v_accvgpr_read_b32 v106, a166\n"
  "v_pk_fma_f32 v[104:105], v[104:105], v[68:69], v[68:69] neg_lo:[0,0,1] neg_hi:[0,0,1]\n"
  "v_pk_fma_f32 v[108:109], v[108:109], v[68:69], v[68:69] neg_lo:[0,0,1] neg_hi:[0,0,1]\n"
  "v_mfma_f32_16x16x32_bf16 a[196:199], v[44:47], a[140:143], a[196:199]\n"
  "ds_read_b64_tr_b16 v[244:245], v230 offset:15680\n"
  "ds_read_b64_tr_b16 v[246:247], v230 offset:15712\n"
  "v_accvgpr_read_b32 v107, a167\n"
  "v_accvgpr_read_b32 v110, a170\n"
  "v_accvgpr_read_b32 v111, a171\n"
  "v_pk_fma_f32 v[106:107], v[106:107], v[70:71], v[70:71] neg_lo:[0,0,1] neg_hi:[0,0,1]\n"
  "v_pk_fma_f32 v[110:111], v[110:111], v[70:71], v[70:71] neg_lo:[0,0,1] neg_hi:[0,0,1]\n"
  "v_mfma_f32_16x16x32_bf16 a[200:203], v[40:43], a[144:147], a[200:203]\n"
  "s_nop 0\n"
  "s_waitcnt lgkmcnt(8)\n"
  "buffer_store_dwordx4 v[16:19], v228, s[52:55], s82 offen offset:256 nt\n"
  "v_cvt_pk_bf16_f32 v104, v104, v108\n"
  "v_cvt_pk_bf16_f32 v108, v105, v109\n"
  "v_cvt_pk_bf16_f32 v106, v106, v110\n"
  "v_cvt_pk_bf16_f32 v110, v107, v111\n"
  "v_mfma_f32_16x16x32_bf16 a[200:203], v[44:47], a[148:151], a[200:203]\n"
  "ds_read_b64_tr_b16 a[252:253], v230 offset:16800\n"
  "ds_read_b64_tr_b16 a[254:255], v230 offset:16832\n"
  "v_accvgpr_read_b32 v136, a180\n"
  "v_accvgpr_read_b32 v137, a181\n"
  "v_accvgpr_read_b32 v140, a184\n"
  "v_pk_fma_f32 v[136:137], v[136:137], v[72:73], v[72:73] neg_lo:[0,0,1] neg_hi:[0,0,1]\n"
  "v_mfma_f32_16x16x32_bf16 a[212:215], v[56:59], a[136:139], a[212:215]\n"
  "v_accvgpr_read_b32 v141, a185\n"
  "v_accvgpr_read_b32 v138, a182\n"
  "v_accvgpr_read_b32 v139, a183\n"
  "v_accvgpr_read_b32 v142, a186\n"
  "v_accvgpr_read_b32 v143, a187\n"
  "v_pk_fma_f32 v[140:141], v[140:141], v[72:73], v[72:73] neg_lo:[0,0,1] neg_hi:[0,0,1]\n"
  "v_pk_fma_f32 v[138:139], v[138:139], v[74:75], v[74:75] neg_lo:[0,0,1] neg_hi:[0,0,1]\n"
  "v_pk_fma_f32 v[142:143], v[142:143], v[74:75], v[74:75] neg_lo:[0,0,1] neg_hi:[0,0,1]\n"
  "v_mfma_f32_16x16x32_bf16 a[212:215], v[60:63], a[140:143], a[212:215]\n"
  "s_nop 0\n"
  "s_waitcnt lgkmcnt(5)\n"
  "buffer_store_dwordx4 v[20:23], v228, s[52:55], s82 offen offset:3328 nt\n"
  "v_cvt_pk_bf16_f32 v136, v136, v140\n"
  "v_cvt_pk_bf16_f32 v140, v137, v141\n"
  "v_cvt_pk_bf16_f32 v138, v138, v142\n"
  "v_cvt_pk_bf16_f32 v142, v139, v143\n"
  "v_mfma_f32_16x16x32_bf16 a[216:219], v[56:59], a[144:147], a[216:219]\n"
  "v_accvgpr_read_b32 v168, a196\n"
  "v_accvgpr_read_b32 v169, a197\n"
  "v_accvgpr_read_b32 v172, a200\n"
  "v_accvgpr_read_b32 v173, a201\n"
  "v_accvgpr_read_b32 v170, a198\n"
  "v_pk_fma_f32 v[168:169], v[168:169], v[76:77], v[76:77] neg_lo:[0,0,1] neg_hi:[0,0,1]\n"
  "v_pk_fma_f32 v[172:173], v[172:173], v[76:77], v[76:77] neg_lo:[0,0,1] neg_hi:[0,0,1]\n"
  "v_mfma_f32_16x16x32_bf16 a[216:219], v[60:63], a[148:151], a[216:219]\n"
  "s_nop 0\n"
  "s_waitcnt lgkmcnt(2)\n"
  "buffer_store_dwordx4 v[244:247], v228, s[52:55], s83 offen offset:256 nt\n"
  "v_accvgpr_read_b32 v171, a199\n"
  "v_accvgpr_read_b32 v174, a202\n"
  "v_accvgpr_read_b32 v175, a203\n"
  "v_cvt_pk_bf16_f32 v168, v168, v172\n"
  "v_pk_fma_f32 v[170:171], v[170:171], v[78:79], v[78:79] neg_lo:[0,0,1] neg_hi:[0,0,1]\n"
  "v_pk_fma_f32 v[174:175], v[174:175], v[78:79], v[78:79] neg_lo:[0,0,1] neg_hi:[0,0,1]\n"
  "v_mfma_f32_16x16x32_bf16 a[172:175], v[8:11], a[120:123], a[172:175]\n"
  "s_waitcnt vmcnt(10)\n"
  "s_barrier\n"
  "ds_read_b64_tr_b16 a[136:137], v217 offset:0\n"
  "ds_read_b64_tr_b16 a[138:139], v218 offset:0\n"
  "v_cvt_pk_bf16_f32 v172, v169, v173\n"
  "v_mfma_f32_16x16x32_bf16 a[172:175], v[12:15], a[124:127], a[172:175]\n"
  "s_nop 0\n"
  "s_waitcnt lgkmcnt(2)\n"
  "buffer_store_dwordx4 a[252:255], v228, s[52:55], s83 offen offset:3328 nt\n"
  "ds_read_b64_tr_b16 a[140:141], v217 offset:16\n"
  "ds_read_b64_tr_b16 a[142:143], v218 offset:16\n"
  "v_cvt_pk_bf16_f32 v170, v170, v174\n"
  "v_cvt_pk_bf16_f32 v174, v171, v175\n"
  "v_mfma_f32_16x16x32_bf16 a[176:179], v[8:11], a[128:131], a[176:179]\n"
  "ds_read_b64_tr_b16 a[144:145], v219 offset:0\n"
  "ds_read_b64_tr_b16 a[146:147], v220 offset:0\n"
  "v_accvgpr_read_b32 v240, a244\n"
  "v_accvgpr_read_b32 v241, a245\n"
  "v_accvgpr_read_b32 v242, a248\n"
  "v_pk_fma_f32 v[240:241], v[240:241], v[84:85], v[84:85] neg_lo:[0,0,1] neg_hi:[0,0,1]\n"
  "v_mfma_f32_16x16x32_bf16 a[176:179], v[12:15], a[132:135], a[176:179]\n"
  "ds_read_b64_tr_b16 a[148:149], v219 offset:16\n"
  "ds_read_b64_tr_b16 a[150:151], v220 offset:16\n"
  "v_accvgpr_read_b32 v243, a249\n"
  "v_accvgpr_read_b32 v236, a246\n"
  "v_accvgpr_read_b32 v237, a247\n"
  "v_pk_fma_f32 v[242:243], v[242:243], v[84:85], v[84:85] neg_lo:[0,0,1] neg_hi:[0,0,1]\n"
  "v_pk_fma_f32 v[236:237], v[236:237], v[86:87], v[86:87] neg_lo:[0,0,1] neg_hi:[0,0,1]\n"
  "v_mfma_f32_16x16x32_bf16 a[188:191], v[24:27], a[120:123], a[188:191]\n"
  "v_accvgpr_read_b32 v112, a172\n"
  "v_accvgpr_read_b32 v113, a173\n"
  "v_accvgpr_read_b32 v114, a174\n"
  "v_accvgpr_read_b32 v115, a175\n"
  "v_accvgpr_read_b32 v238, a250\n"
  "v_pk_fma_f32 v[112:113], v[112:113], v[68:69], v[68:69] neg_lo:[0,0,1] neg_hi:[0,0,1]\n"
  "v_pk_fma_f32 v[114:115], v[114:115], v[70:71], v[70:71] neg_lo:[0,0,1] neg_hi:[0,0,1]\n"
  "v_mfma_f32_16x16x32_bf16 a[188:191], v[28:31], a[124:127], a[188:191]\n"
  "v_accvgpr_read_b32 v239, a251\n"
  "v_accvgpr_read_b32 v200, a212\n"
  "v_accvgpr_read_b32 v201, a213\n"
  "v_accvgpr_read_b32 v204, a216\n"
  "v_accvgpr_read_b32 v205, a217\n"
  "v_pk_fma_f32 v[238:239], v[238:239], v[86:87], v[86:87] neg_lo:[0,0,1] neg_hi:[0,0,1]\n"
  "v_pk_fma_f32 v[200:201], v[200:201], v[80:81], v[80:81] neg_lo:[0,0,1] neg_hi:[0,0,1]\n"
  "v_pk_fma_f32 v[204:205], v[204:205], v[80:81], v[80:81] neg_lo:[0,0,1] neg_hi:[0,0,1]\n"
  "v_mfma_f32_16x16x32_bf16 a[192:195], v[24:27], a[128:131], a[192:195]\n"
  "v_accvgpr_read_b32 v116, a176\n"
  "v_accvgpr_read_b32 v117, a177\n"
  "v_accvgpr_read_b32 v118, a178\n"
  "v_accvgpr_read_b32 v119, a179\n"
  "v_accvgpr_read_b32 v202, a214\n"
  "v_pk_fma_f32 v[116:117], v[116:117], v[68:69], v[68:69] neg_lo:[0,0,1] neg_hi:[0,0,1]\n"
  "v_pk_fma_f32 v[118:119], v[118:119], v[70:71], v[70:71] neg_lo:[0,0,1] neg_hi:[0,0,1]\n"
  "v_mfma_f32_16x16x32_bf16 a[192:195], v[28:31], a[132:135], a[192:195]\n"
  "v_cvt_pk_bf16_f32 v105, v112, v116\n"
  "v_cvt_pk_bf16_f32 v109, v113, v117\n"
  "v_cvt_pk_bf16_f32 v107, v114, v118\n"
  "v_cvt_pk_bf16_f32 v111, v115, v119\n"
  "v_accvgpr_read_b32 v203, a215\n"
  "v_pk_fma_f32 v[202:203], v[202:203], v[82:83], v[82:83] neg_lo:[0,0,1] neg_hi:[0,0,1]\n"
  "v_mfma_f32_16x16x32_bf16 a[204:207], v[40:43], a[120:123], a[204:207]\n"
  "ds_write_b64 v229, v[104:105] offset:0\n"
  "v_accvgpr_read_b32 v144, a188\n"
  "v_accvgpr_read_b32 v145, a189\n"
  "v_pk_fma_f32 v[144:145], v[144:145], v[72:73], v[72:73] neg_lo:[0,0,1] neg_hi:[0,0,1]\n"
  "v_mfma_f32_16x16x32_bf16 a[204:207], v[44:47], a[124:127], a[204:207]\n"
  "ds_write_b64 v229, v[108:109] offset:288\n"
  "v_accvgpr_read_b32 v146, a190\n"
  "v_accvgpr_read_b32 v147, a191\n"
  "v_pk_fma_f32 v[146:147], v[146:147], v[74:75], v[74:75] neg_lo:[0,0,1] neg_hi:[0,0,1]\n"
  "v_mfma_f32_16x16x32_bf16 a[208:211], v[40:43], a[128:131], a[208:211]\n"
  "ds_write_b64 v229, v[106:107] offset:576\n"
  "ds_write_b64 v229, v[110:111] offset:864\n"
  "v_mfma_f32_16x16x32_bf16 a[208:211], v[44:47], a[132:135], a[208:211]\n"
  "v_accvgpr_read_b32 v148, a192\n"
  "v_accvgpr_read_b32 v149, a193\n"
  "v_accvgpr_read_b32 v150, a194\n"
  "v_accvgpr_read_b32 v151, a195\n"
  "v_accvgpr_read_b32 v206, a218\n"
  "v_pk_fma_f32 v[148:149], v[148:149], v[72:73], v[72:73] neg_lo:[0,0,1] neg_hi:[0,0,1]\n"
  "v_pk_fma_f32 v[150:151], v[150:151], v[74:75], v[74:75] neg_lo:[0,0,1] neg_hi:[0,0,1]\n"
  "v_mfma_f32_16x16x32_bf16 a[220:223], v[56:59], a[120:123], a[220:223]\n"
  "ds_read_b64_tr_b16 v[0:1], v230 offset:0\n"
  "ds_read_b64_tr_b16 v[2:3], v230 offset:32\n"
  "v_cvt_pk_bf16_f32 v137, v144, v148\n"
  "v_cvt_pk_bf16_f32 v141, v145, v149\n"
  "v_cvt_pk_bf16_f32 v139, v146, v150\n"
  "v_mfma_f32_16x16x32_bf16 a[220:223], v[60:63], a[124:127], a[220:223]\n"
  "ds_read_b64_tr_b16 v[4:5], v230 offset:1120\n"
  "ds_write_b64 v229, v[136:137] offset:2240\n"
  "v_cvt_pk_bf16_f32 v143, v147, v151\n"
  "v_mfma_f32_16x16x32_bf16 a[224:227], v[56:59], a[128:131], a[224:227]\n"
  "ds_read_b64_tr_b16 v[6:7], v230 offset:1152\n"
  "ds_write_b64 v229, v[140:141] offset:2528\n"
  "v_accvgpr_read_b32 v176, a204\n"
  "v_mfma_f32_16x16x32_bf16 a[224:227], v[60:63], a[132:135], a[224:227]\n"
  "s_nop 0\n"
  "s_waitcnt lgkmcnt(14)\n"
  "ds_write_b64 v229, v[138:139] offset:2816\n"
  "ds_write_b64 v229, v[142:143] offset:3104\n"
  "v_mfma_f32_16x16x32_bf16 a[236:239], a[160:163], a[136:139], a[236:239]\n"
  "v_accvgpr_read_b32 v177, a205\n"
  "v_accvgpr_read_b32 v180, a208\n"
  "v_accvgpr_read_b32 v181, a209\n"
  "v_accvgpr_read_b32 v178, a206\n"
  "v_accvgpr_read_b32 v179, a207\n"
  "v_pk_fma_f32 v[176:177], v[176:177], v[76:77], v[76:77] neg_lo:[0,0,1] neg_hi:[0,0,1]\n"
  "v_pk_fma_f32 v[180:181], v[180:181], v[76:77], v[76:77] neg_lo:[0,0,1] neg_hi:[0,0,1]\n"
  "v_pk_fma_f32 v[178:179], v[178:179], v[78:79], v[78:79] neg_lo:[0,0,1] neg_hi:[0,0,1]\n"
  "v_mfma_f32_16x16x32_bf16 a[240:243], a[160:163], a[140:143], a[240:243]\n"
  "ds_read_b64_tr_b16 v[16:17], v230 offset:2240\n"
  "ds_read_b64_tr_b16 v[18:19], v230 offset:2272\n"
  "v_accvgpr_read_b32 v182, a210\n"
  "v_accvgpr_read_b32 v183, a211\n"
  "v_cvt_pk_bf16_f32 v169, v176, v180\n"
  "v_pk_fma_f32 v[182:183], v[182:183], v[78:79], v[78:79] neg_lo:[0,0,1] neg_hi:[0,0,1]\n"
  "v_mfma_f32_16x16x32_bf16 v[88:91], v[12:15], a[136:139], v[88:91]\n"
  "s_waitcnt lgkmcnt(8)\n"
  "buffer_store_dwordx4 v[0:3], v228, s[52:55], s80 offen offset:0 nt\n"
  "ds_write_b64 v229, v[168:169] offset:4480\n"
  "v_cvt_pk_bf16_f32 v173, v177, v181\n"
  "v_mfma_f32_16x16x32_bf16 v[92:95], v[12:15], a[140:143], v[92:95]\n"
  "ds_write_b64 v229, v[172:173] offset:4768\n"
  "v_cvt_pk_bf16_f32 v171, v178, v182\n"
  "v_cvt_pk_bf16_f32 v175, v179, v183\n"
  "v_mfma_f32_16x16x32_bf16 v[96:99], v[12:15], a[144:147], v[96:99]\n"
  "s_waitcnt lgkmcnt(7)\n"
  "buffer_store_dwordx4 v[4:7], v228, s[52:55], s80 offen offset:3072 nt\n"
  "ds_read_b64_tr_b16 v[20:21], v230 offset:3360\n"
  "ds_read_b64_tr_b16 v[22:23], v230 offset:3392\n"
  "ds_write_b64 v229, v[170:171] offset:5056\n"
  "ds_write_b64 v229, v[174:175] offset:5344\n"
  "v_mfma_f32_16x16x32_bf16 v[100:103], v[12:15], a[148:151], v[100:103]\n"
  "v_accvgpr_read_b32 v208, a220\n"
  "v_accvgpr_read_b32 v209, a221\n"
  "v_accvgpr_read_b32 v212, a224\n"
  "v_accvgpr_read_b32 v213, a225\n"
  "v_accvgpr_read_b32 v207, a219\n"
  "v_pk_fma_f32 v[208:209], v[208:209], v[80:81], v[80:81] neg_lo:[0,0,1] neg_hi:[0,0,1]\n"
  "v_pk_fma_f32 v[212:213], v[212:213], v[80:81], v[80:81] neg_lo:[0,0,1] neg_hi:[0,0,1]\n"
  "v_pk_fma_f32 v[206:207], v[206:207], v[82:83], v[82:83] neg_lo:[0,0,1] neg_hi:[0,0,1]\n"
  "v_pk_fma_f32 v[88:89], v[88:89], v[68:69], v[68:69] neg_lo:[0,0,1] neg_hi:[0,0,1]\n"
  "v_mfma_f32_16x16x32_bf16 v[120:123], v[28:31], a[136:139], v[120:123]\n"
  "ds_read_b64_tr_b16 v[244:245], v230 offset:4480\n"
  "ds_read_b64_tr_b16 v[246:247], v230 offset:4512\n"
  "v_accvgpr_read_b32 v210, a222\n"
  "v_accvgpr_read_b32 v211, a223\n"
  "v_accvgpr_read_b32 v214, a226\n"
  "v_pk_fma_f32 v[210:211], v[210:211], v[82:83], v[82:83] neg_lo:[0,0,1] neg_hi:[0,0,1]\n"
  "v_pk_fma_f32 v[92:93], v[92:93], v[68:69], v[68:69] neg_lo:[0,0,1] neg_hi:[0,0,1]\n"
  "v_pk_fma_f32 v[90:91], v[90:91], v[70:71], v[70:71] neg_lo:[0,0,1] neg_hi:[0,0,1]\n"
  "v_pk_fma_f32 v[94:95], v[94:95], v[70:71], v[70:71] neg_lo:[0,0,1] neg_hi:[0,0,1]\n"
  "v_mfma_f32_16x16x32_bf16 v[124:127], v[28:31], a[140:143], v[124:127]\n"
  "s_nop 0\n"
  "s_waitcnt lgkmcnt(8)\n"
  "buffer_store_dwordx4 v[16:19], v228, s[52:55], s81 offen offset:0 nt\n"
  "v_accvgpr_read_b32 v215, a227\n"
  "v_cvt_pk_bf16_f32 v240, v240, v242\n"
  "v_cvt_pk_bf16_f32 v241, v241, v243\n"
  "v_pk_fma_f32 v[214:215], v[214:215], v[82:83], v[82:83] neg_lo:[0,0,1] neg_hi:[0,0,1]\n"
  "v_pk_fma_f32 v[96:97], v[96:97], v[68:69], v[68:69] neg_lo:[0,0,1] neg_hi:[0,0,1]\n"
  "v_pk_fma_f32 v[98:99], v[98:99], v[70:71], v[70:71] neg_lo:[0,0,1] neg_hi:[0,0,1]\n"
  "v_mfma_f32_16x16x32_bf16 v[128:131], v[28:31], a[144:147], v[128:131]\n"
  "v_cvt_pk_bf16_f32 v236, v236, v238\n"
  "v_cvt_pk_bf16_f32 v237, v237, v239\n"
  "v_cvt_pk_bf16_f32 v200, v200, v204\n"
  "v_cvt_pk_bf16_f32 v204, v201, v205\n"
  "v_cvt_pk_bf16_f32 v201, v208, v212\n"
  "v_cvt_pk_bf16_f32 v205, v209, v213\n"
  "v_cvt_pk_bf16_f32 v202, v202, v206\n"
  "v_cvt_pk_bf16_f32 v206, v203, v207\n"
  "v_cvt_pk_bf16_f32 v203, v210, v214\n"
  "v_cvt_pk_bf16_f32 v207, v211, v215\n"
  "v_pk_fma_f32 v[100:101], v[100:101], v[68:69], v[68:69] neg_lo:[0,0,1] neg_hi:[0,0,1]\n"
  "v_pk_fma_f32 v[102:103], v[102:103], v[70:71], v[70:71] neg_lo:[0,0,1] neg_hi:[0,0,1]\n"
  "v_mfma_f32_16x16x32_bf16 v[132:135], v[28:31], a[148:151], v[132:135]\n"
  "ds_read_b64_tr_b16 a[252:253], v230 offset:5600\n"
  "ds_read_b64_tr_b16 a[254:255], v230 offset:5632\n"
  "ds_write_b32 v233, v240 offset:20160\n"
  "ds_write_b32 v233, v241 offset:20448\n"
  "ds_write_b32 v233, v236 offset:20736\n"
  "ds_write_b32 v233, v237 offset:21024\n"
  "ds_write_b64 v229, v[200:201] offset:6720\n"
  "ds_write_b64 v229, v[204:205] offset:7008\n"
  "ds_write_b64 v229, v[202:203] offset:7296\n"
  "ds_write_b64 v229, v[206:207] offset:7584\n"
  "v_pk_fma_f32 v[120:121], v[120:121], v[72:73], v[72:73] neg_lo:[0,0,1] neg_hi:[0,0,1]\n"
  "v_pk_fma_f32 v[122:123], v[122:123], v[74:75], v[74:75] neg_lo:[0,0,1] neg_hi:[0,0,1]\n"
  "v_mfma_f32_16x16x32_bf16 v[152:155], v[44:47], a[136:139], v[152:155]\n"
  "s_nop 0\n"
  "s_waitcnt lgkmcnt(14)\n"
  "buffer_store_dwordx4 v[20:23], v228, s[52:55], s81 offen offset:3072 nt\n"
  "v_accvgpr_read_b32 v240, a228\n"
  "v_accvgpr_read_b32 v241, a229\n"
  "v_accvgpr_read_b32 v242, a232\n"
  "v_accvgpr_read_b32 v243, a233\n"
  "v_accvgpr_read_b32 v236, a230\n"
  "v_accvgpr_read_b32 v237, a231\n"
  "v_accvgpr_read_b32 v238, a234\n"
  "v_accvgpr_read_b32 v239, a235\n"
  "v_pk_fma_f32 v[240:241], v[240:241], v[84:85], v[84:85] neg_lo:[0,0,1] neg_hi:[0,0,1]\n"
  "v_pk_fma_f32 v[242:243], v[242:243], v[84:85], v[84:85] neg_lo:[0,0,1] neg_hi:[0,0,1]\n"
  "v_pk_fma_f32 v[236:237], v[236:237], v[86:87], v[86:87] neg_lo:[0,0,1] neg_hi:[0,0,1]\n"
  "v_pk_fma_f32 v[238:239], v[238:239], v[86:87], v[86:87] neg_lo:[0,0,1] neg_hi:[0,0,1]\n"
  "v_mfma_f32_16x16x32_bf16 v[156:159], v[44:47], a[140:143], v[156:159]\n"
  "ds_read_b64_tr_b16 v[0:1], v230 offset:6720\n"
  "ds_read_b64_tr_b16 v[2:3], v230 offset:6752\n"
  "v_cvt_pk_bf16_f32 v240, v240, v242\n"
  "v_cvt_pk_bf16_f32 v241, v241, v243\n"
  "v_cvt_pk_bf16_f32 v236, v236, v238\n"
  "v_cvt_pk_bf16_f32 v237, v237, v239\n"
  "v_pk_fma_f32 v[124:125], v[124:125], v[72:73], v[72:73] neg_lo:[0,0,1] neg_hi:[0,0,1]\n"
  "v_pk_fma_f32 v[128:129], v[128:129], v[72:73], v[72:73] neg_lo:[0,0,1] neg_hi:[0,0,1]\n"
  "v_pk_fma_f32 v[126:127], v[126:127], v[74:75], v[74:75] neg_lo:[0,0,1] neg_hi:[0,0,1]\n"
  "v_pk_fma_f32 v[130:131], v[130:131], v[74:75], v[74:75] neg_lo:[0,0,1] neg_hi:[0,0,1]\n"
  "v_mfma_f32_16x16x32_bf16 v[160:163], v[44:47], a[144:147], v[160:163]\n"
  "s_nop 0\n"
  "s_waitcnt lgkmcnt(12)\n"
  "buffer_store_dwordx4 v[244:247], v228, s[52:55], s82 offen offset:0 nt\n"
  "ds_write_b32 v233, v240 offset:17920\n"
  "ds_write_b32 v233, v241 offset:18208\n"
  "ds_write_b32 v233, v236 offset:18496\n"
  "ds_write_b32 v233, v237 offset:18784\n"
  "v_pk_fma_f32 v[132:133], v[132:133], v[72:73], v[72:73] neg_lo:[0,0,1] neg_hi:[0,0,1]\n"
  "v_pk_fma_f32 v[134:135], v[134:135], v[74:75], v[74:75] neg_lo:[0,0,1] neg_hi:[0,0,1]\n"
  "v_mfma_f32_16x16x32_bf16 v[164:167], v[44:47], a[148:151], v[164:167]\n"
  "v_accvgpr_read_b32 v240, a236\n"
  "v_accvgpr_read_b32 v241, a237\n"
  "v_accvgpr_read_b32 v242, a240\n"
  "v_accvgpr_read_b32 v243, a241\n"
  "v_accvgpr_read_b32 v236, a238\n"
  "v_accvgpr_read_b32 v237, a239\n"
  "v_accvgpr_read_b32 v238, a242\n"
  "v_accvgpr_read_b32 v239, a243\n"
  "v_cvt_pk_bf16_f32 v88, v88, v92\n"
  "v_cvt_pk_bf16_f32 v92, v89, v93\n"
  "v_cvt_pk_bf16_f32 v89, v96, v100\n"
  "v_cvt_pk_bf16_f32 v93, v97, v101\n"
  "v_cvt_pk_bf16_f32 v90, v90, v94\n"
  "v_cvt_pk_bf16_f32 v94, v91, v95\n"
  "v_cvt_pk_bf16_f32 v91, v98, v102\n"
  "v_cvt_pk_bf16_f32 v95, v99, v103\n"
  "v_pk_fma_f32 v[240:241], v[240:241], v[84:85], v[84:85] neg_lo:[0,0,1] neg_hi:[0,0,1]\n"
  "v_pk_fma_f32 v[242:243], v[242:243], v[84:85], v[84:85] neg_lo:[0,0,1] neg_hi:[0,0,1]\n"
  "v_pk_fma_f32 v[236:237], v[236:237], v[86:87], v[86:87] neg_lo:[0,0,1] neg_hi:[0,0,1]\n"
  "v_pk_fma_f32 v[238:239], v[238:239], v[86:87], v[86:87] neg_lo:[0,0,1] neg_hi:[0,0,1]\n"
  "v_mfma_f32_16x16x32_bf16 v[184:187], v[60:63], a[136:139], v[184:187]\n"
  "ds_read_b64_tr_b16 v[4:5], v230 offset:7840\n"
  "ds_read_b64_tr_b16 v[6:7], v230 offset:7872\n"
  "ds_write_b64 v229, v[88:89] offset:8960\n"
  "ds_write_b64 v229, v[92:93] offset:9248\n"
  "ds_write_b64 v229, v[90:91] offset:9536\n"
  "ds_write_b64 v229, v[94:95] offset:9824\n"
  "v_cvt_pk_bf16_f32 v240, v240, v242\n"
  "v_cvt_pk_bf16_f32 v241, v241, v243\n"
  "v_cvt_pk_bf16_f32 v236, v236, v238\n"
  "v_cvt_pk_bf16_f32 v237, v237, v239\n"
  "v_pk_fma_f32 v[152:153], v[152:153], v[76:77], v[76:77] neg_lo:[0,0,1] neg_hi:[0,0,1]\n"
  "v_pk_fma_f32 v[156:157], v[156:157], v[76:77], v[76:77] neg_lo:[0,0,1] neg_hi:[0,0,1]\n"
  "v_pk_fma_f32 v[154:155], v[154:155], v[78:79], v[78:79] neg_lo:[0,0,1] neg_hi:[0,0,1]\n"
  "v_pk_fma_f32 v[158:159], v[158:159], v[78:79], v[78:79] neg_lo:[0,0,1] neg_hi:[0,0,1]\n"
  "v_mfma_f32_16x16x32_bf16 v[188:191], v[60:63], a[140:143], v[188:191]\n"
  "s_nop 0\n"
  "s_waitcnt lgkmcnt(15)\n"
  "buffer_store_dwordx4 a[252:255], v228, s[52:55], s82 offen offset:3072 nt\n"
  "ds_write_b32 v233, v240 offset:19040\n"
  "ds_write_b32 v233, v241 offset:19328\n"
  "ds_write_b32 v233, v236 offset:19616\n"
  "ds_write_b32 v233, v237 offset:19904\n"
  "v_pk_fma_f32 v[160:161], v[160:161], v[76:77], v[76:77] neg_lo:[0,0,1] neg_hi:[0,0,1]\n"
  "v_pk_fma_f32 v[162:163], v[162:163], v[78:79], v[78:79] neg_lo:[0,0,1] neg_hi:[0,0,1]\n"
  "v_mfma_f32_16x16x32_bf16 v[192:195], v[60:63], a[144:147], v[192:195]\n"
  "ds_read_b64_tr_b16 v[16:17], v230 offset:8960\n"
  "ds_read_b64_tr_b16 v[18:19], v230 offset:8992\n"
  "ds_read_b64_tr_b16 v[20:21], v230 offset:10080\n"
  "ds_read_b64_tr_b16 v[22:23], v230 offset:10112\n"
  "v_cvt_pk_bf16_f32 v120, v120, v124\n"
  "v_pk_fma_f32 v[164:165], v[164:165], v[76:77], v[76:77] neg_lo:[0,0,1] neg_hi:[0,0,1]\n"
  "v_pk_fma_f32 v[166:167], v[166:167], v[78:79], v[78:79] neg_lo:[0,0,1] neg_hi:[0,0,1]\n"
  "v_mfma_f32_16x16x32_bf16 v[196:199], v[60:63], a[148:151], v[196:199]\n"
  "s_waitcnt lgkmcnt(4)\n"
  "s_barrier\n"
  "buffer_store_dwordx4 v[0:3], v228, s[52:55], s83 offen offset:0 nt\n"
  "v_cvt_pk_bf16_f32 v124, v121, v125\n"
  "v_cvt_pk_bf16_f32 v121, v128, v132\n"
  "v_pk_fma_f32 v[184:185], v[184:185], v[80:81], v[80:81] neg_lo:[0,0,1] neg_hi:[0,0,1]\n"
  "v_pk_fma_f32 v[186:187], v[186:187], v[82:83], v[82:83] neg_lo:[0,0,1] neg_hi:[0,0,1]\n"
  "ds_read_b64_tr_b16 v[244:245], v230 offset:17920\n"
  "ds_read_b64_tr_b16 v[246:247], v230 offset:17952\n"
  "ds_write_b64 v229, v[120:121] offset:11200\n"
  "v_pk_fma_f32 v[188:189], v[188:189], v[80:81], v[80:81] neg_lo:[0,0,1] neg_hi:[0,0,1]\n"
  "v_pk_fma_f32 v[190:191], v[190:191], v[82:83], v[82:83] neg_lo:[0,0,1] neg_hi:[0,0,1]\n"
  "ds_read_b64_tr_b16 a[252:253], v230 offset:19040\n"
  "ds_read_b64_tr_b16 a[254:255], v230 offset:19072\n"
  "ds_read_b64_tr_b16 v[0:1], v230 offset:20160\n"
  "ds_read_b64_tr_b16 v[2:3], v230 offset:20192\n"
  "v_cvt_pk_bf16_f32 v125, v129, v133\n"
  "v_pk_fma_f32 v[192:193], v[192:193], v[80:81], v[80:81] neg_lo:[0,0,1] neg_hi:[0,0,1]\n"
  "v_pk_fma_f32 v[194:195], v[194:195], v[82:83], v[82:83] neg_lo:[0,0,1] neg_hi:[0,0,1]\n"
  "buffer_store_dwordx4 v[4:7], v228, s[52:55], s83 offen offset:3072 nt\n"
  "ds_write_b64 v229, v[124:125] offset:11488\n"
  "v_cvt_pk_bf16_f32 v122, v122, v126\n"
  "v_pk_fma_f32 v[196:197], v[196:197], v[80:81], v[80:81] neg_lo:[0,0,1] neg_hi:[0,0,1]\n"
  "v_pk_fma_f32 v[198:199], v[198:199], v[82:83], v[82:83] neg_lo:[0,0,1] neg_hi:[0,0,1]\n"
  "v_cvt_pk_bf16_f32 v126, v123, v127\n"
  "v_cvt_pk_bf16_f32 v123, v130, v134\n"
  "v_cvt_pk_bf16_f32 v127, v131, v135\n"
  "v_cvt_pk_bf16_f32 v152, v152, v156\n"
  "v_cvt_pk_bf16_f32 v156, v153, v157\n"
  "s_waitcnt lgkmcnt(10)\n"
  "buffer_store_dwordx4 v[16:19], v228, s[52:55], s80 offen offset:128 nt\n"
  "ds_write_b64 v229, v[122:123] offset:11776\n"
  "v_cvt_pk_bf16_f32 v153, v160, v164\n"
  "ds_write_b64 v229, v[126:127] offset:12064\n"
  "v_cvt_pk_bf16_f32 v157, v161, v165\n"
  "v_cvt_pk_bf16_f32 v154, v154, v158\n"
  "s_waitcnt lgkmcnt(10)\n"
  "buffer_store_dwordx4 v[20:23], v228, s[52:55], s80 offen offset:3200 nt\n"
  "ds_write_b64 v229, v[152:153] offset:13440\n"
  "v_cvt_pk_bf16_f32 v158, v155, v159\n"
  "ds_read_b64_tr_b16 v[4:5], v230 offset:11200\n"
  "ds_read_b64_tr_b16 v[6:7], v230 offset:11232\n"
  "ds_write_b64 v229, v[156:157] offset:13728\n"
  "s_waitcnt lgkmcnt(12)\n"
  "buffer_store_dwordx4 v[244:247], v228, s[52:55], s56 offen offset:0 nt\n"
  "ds_read_b64_tr_b16 v[16:17], v230 offset:12320\n"
  "ds_read_b64_tr_b16 v[18:19], v230 offset:12352\n"
  "v_cvt_pk_bf16_f32 v155, v162, v166\n"
  "v_cvt_pk_bf16_f32 v159, v163, v167\n"
  "ds_write_b64 v229, v[154:155] offset:14016\n"
  "v_cvt_pk_bf16_f32 v184, v184, v188\n"
  "v_cvt_pk_bf16_f32 v188, v185, v189\n"
  "s_waitcnt lgkmcnt(12)\n"
  "buffer_store_dwordx4 a[252:255], v228, s[52:55], s56 offen offset:128 nt\n"
  "ds_write_b64 v229, v[158:159] offset:14304\n"
  "v_cvt_pk_bf16_f32 v185, v192, v196\n"
  "ds_write_b64 v229, v[184:185] offset:15680\n"
  "v_cvt_pk_bf16_f32 v189, v193, v197\n"
  "v_cvt_pk_bf16_f32 v186, v186, v190\n"
  "s_waitcnt lgkmcnt(12)\n"
  "buffer_store_dwordx4 v[0:3], v228, s[52:55], s56 offen offset:256 nt\n"
  "ds_read_b64_tr_b16 v[20:21], v230 offset:13440\n"
  "ds_write_b64 v229, v[188:189] offset:15968\n"
  "ds_read_b64_tr_b16 v[22:23], v230 offset:13472\n"
  "ds_read_b64_tr_b16 v[244:245], v230 offset:14560\n"
  "ds_read_b64_tr_b16 v[246:247], v230 offset:14592\n"
  "v_cvt_pk_bf16_f32 v190, v187, v191\n"
  "v_cvt_pk_bf16_f32 v187, v194, v198\n"
  "s_waitcnt lgkmcnt(11)\n"
  "buffer_store_dwordx4 v[4:7], v228, s[52:55], s81 offen offset:128 nt\n"
  "ds_write_b64 v229, v[186:187] offset:16256\n"
  "v_cvt_pk_bf16_f32 v191, v195, v199\n"
  "ds_write_b64 v229, v[190:191] offset:16544\n"
  "s_waitcnt lgkmcnt(10)\n"
  "buffer_store_dwordx4 v[16:19], v228, s[52:55], s81 offen offset:3200 nt\n"
  "ds_read_b64_tr_b16 a[252:253], v230 offset:15680\n"
  "ds_read_b64_tr_b16 a[254:255], v230 offset:15712\n"
  "ds_read_b64_tr_b16 v[0:1], v230 offset:16800\n"
  "ds_read_b64_tr_b16 v[2:3], v230 offset:16832\n"
  "s_waitcnt lgkmcnt(8)\n"
  "buffer_store_dwordx4 v[20:23], v228, s[52:55], s82 offen offset:128 nt\n"
  "s_waitcnt lgkmcnt(6)\n"
  "buffer_store_dwordx4 v[244:247], v228, s[52:55], s82 offen offset:3200 nt\n"
  "s_waitcnt lgkmcnt(2)\n"
  "buffer_store_dwordx4 a[252:255], v228, s[52:55], s83 offen offset:128 nt\n"
  "s_waitcnt lgkmcnt(0)\n"
  "buffer_store_dwordx4 v[0:3], v228, s[52:55], s83 offen offset:3200 nt\n"
  "L_END_%=:\n"
  "s_mov_b32 %0, s78\n"
  : "=&s"(bad)
  : "s"((unsigned long long)__builtin_amdgcn_kernarg_segment_ptr()), "s"(wg), "s"(tid0), "s"((unsigned)(size_t)lds)
  : "v0", "v1", "v2", "v3", "v4", "v5", "v6", "v7", "v8", "v9", "v10", "v11", "v12", "v13", "v14", "v15", "v16", "v17", "v18", "v19", "v20", "v21", "v22", "v23", "v24", "v25", "v26", "v27", "v28", "v29", "v30", "v31", "v32", "v33", "v34", "v35", "v36", "v37", "v38", "v39", "v40", "v41", "v42", "v43", "v44", "v45", "v46", "v47", "v48", "v49", "v50", "v51", "v52", "v53", "v54", "v55", "v56", "v57", "v58", "v59", "v60", "v61", "v62", "v63", "v64", "v65", "v66", "v67", "v68", "v69", "v70", "v71", "v72", "v73", "v74", "v75", "v76", "v77", "v78", "v79", "v80", "v81", "v82", "v83", "v84", "v85", "v86", "v87", "v88", "v89", "v90", "v91", "v92", "v93", "v94", "v95", "v96", "v97", "v98", "v99", "v100", "v101", "v102", "v103", "v104", "v105", "v106", "v107", "v108", "v109", "v110", "v111", "v112", "v113", "v114", "v115", "v116", "v117", "v118", "v119", "v120", "v121", "v122", "v123", "v124", "v125", "v126", "v127", "v128", "v129", "v130", "v131", "v132", "v133", "v134", "v135", "v136", "v137", "v138", "v139", "v140", "v141", "v142", "v143", "v144", "v145", "v146", "v147", "v148", "v149", "v150", "v151", "v152", "v153", "v154", "v155", "v156", "v157", "v158", "v159", "v160", "v161", "v162", "v163", "v164", "v165", "v166", "v167", "v168", "v169", "v170", "v171", "v172", "v173", "v174", "v175", "v176", "v177", "v178", "v179", "v180", "v181", "v182", "v183", "v184", "v185", "v186", "v187", "v188", "v189", "v190", "v191", "v192", "v193", "v194", "v195", "v196", "v197", "v198", "v199", "v200", "v201", "v202", "v203", "v204", "v205", "v206", "v207", "v208", "v209", "v210", "v211", "v212", "v213", "v214", "v215", "v216", "v217", "v218", "v219", "v220", "v221", "v222", "v223", "v224", "v225", "v226", "v227", "v228", "v229", "v230", "v231", "v232", "v233", "v234", "v235", "v236", "v237", "v238", "v239", "v240", "v241", "v242", "v243", "v244", "v245", "v246", "v247", "v248", "v249", "v250", "v251", "v252", "v253", "v254", "v255", "a0", "a1", "a2", "a3", "a4", "a5", "a6", "a7", "a8", "a9", "a10", "a11", "a12", "a13", "a14", "a15", "a16", "a17", "a18", "a19", "a20", "a21", "a22", "a23", "a24", "a25", "a26", "a27", "a28", "a29", "a30", "a31", "a32", "a33", "a34", "a35", "a36", "a37", "a38", "a39", "a40", "a41", "a42", "a43", "a44", "a45", "a46", "a47", "a48", "a49", "a50", "a51", "a52", "a53", "a54", "a55", "a56", "a57", "a58", "a59", "a60", "a61", "a62", "a63", "a64", "a65", "a66", "a67", "a68", "a69", "a70", "a71", "a72", "a73", "a74", "a75", "a76", "a77", "a78", "a79", "a80", "a81", "a82", "a83", "a84", "a85", "a86", "a87", "a88", "a89", "a90", "a91", "a92", "a93", "a94", "a95", "a96", "a97", "a98", "a99", "a100", "a101", "a102", "a103", "a104", "a105", "a106", "a107", "a108", "a109", "a110", "a111", "a112", "a113", "a114", "a115", "a116", "a117", "a118", "a119", "a120", "a121", "a122", "a123", "a124", "a125", "a126", "a127", "a128", "a129", "a130", "a131", "a132", "a133", "a134", "a135", "a136", "a137", "a138", "a139", "a140", "a141", "a142", "a143", "a144", "a145", "a146", "a147", "a148", "a149", "a150", "a151", "a152", "a153", "a154", "a155", "a156", "a157", "a158", "a159", "a160", "a161", "a162", "a163", "a164", "a165", "a166", "a167", "a168", "a169", "a170", "a171", "a172", "a173", "a174", "a175", "a176", "a177", "a178", "a179", "a180", "a181", "a182", "a183", "a184", "a185", "a186", "a187", "a188", "a189", "a190", "a191", "a192", "a193", "a194", "a195", "a196", "a197", "a198", "a199", "a200", "a201", "a202", "a203", "a204", "a205", "a206", "a207", "a208", "a209", "a210", "a211", "a212", "a213", "a214", "a215", "a216", "a217", "a218", "a219", "a220", "a221", "a222", "a223", "a224", "a225", "a226", "a227", "a228", "a229", "a230", "a231", "a232", "a233", "a234", "a235", "a236", "a237", "a238", "a239", "a240", "a241", "a242", "a243", "a244", "a245", "a246", "a247", "a248", "a249", "a250", "a251", "a252", "a253", "a254", "a255", "s20", "s21", "s22", "s23", "s24", "s25", "s26", "s27", "s28", "s29", "s30", "s31", "s32", "s33", "s34", "s35", "s36", "s37", "s38", "s39", "s40", "s41", "s42", "s43", "s44", "s45", "s46", "s47", "s48", "s49", "s50", "s51", "s52", "s53", "s54", "s55", "s56", "s57", "s58", "s59", "s60", "s61", "s62", "s63", "s64", "s65", "s66", "s67", "s68", "s69", "s70", "s71", "s72", "s73", "s74", "s75", "s76", "s77", "s78", "s79", "s80", "s81", "s82", "s83", "s84", "s85", "s86", "s87", "s88", "s89", "s90", "s91", "s92", "s93", "s94", "s95", "s96", "s97", "s98", "s99", "s100", "s101", "vcc", "scc", "memory");
  if (bad) {
    unsigned long long ka = (unsigned long long)__builtin_amdgcn_kernarg_segment_ptr();
    asm volatile("s_waitcnt vmcnt(0)" : "+s"(ka) :: "memory");
    const unsigned short* const* arg = (const unsigned short* const*)ka;
    const int xcd = wg & 7, slot = wg >> 3;
    constexpr int HB = 0;
    auto head_of = [&](int lh) { return lh + (lh >> HB) * (7 << HB) + (xcd << HB); };
#pragma unroll 1
    for (int it = 0; it < 6; ++it) {
      if (!((bad >> it) & 1u)) continue;
      const int p = slot + 32 * it, head = head_of(p / 3), r0 = 288 * (p % 3);
#pragma unroll 1
      for (int Y = 0; Y < 5; ++Y) {
        const int row = r0 + (Y < 4 ? 64 * wid + 16 * Y : 256 + 16 * (wid >> 1)), lo = Y < 4 ? 0 : 8 * (wid & 1);
        if (row < SQ) exact_tile(arg[0], arg[1], arg[2], (unsigned short*)arg[3], head, row, lo, Y < 4 ? 16 : lo + 8);
      }
    }
  }
}

extern "C" void kernel_launch(void* const* d_in, const int* in_sizes, int n_in,
                              void* d_out, int out_size, void* d_ws, size_t ws_size,
                              hipStream_t stream) {
  (void)hipFuncSetAttribute((const void*)attn_fwd_g, hipFuncAttributeMaxDynamicSharedMemorySize, LDS_BYTES);
  hipLaunchKernelGGL(attn_fwd_g, dim3(256), dim3(256), LDS_BYTES, stream, (const unsigned short*)d_in[0], (const unsigned short*)d_in[1],
                     (const unsigned short*)d_in[2], (unsigned short*)d_out);
}
```

```cpp
#include <hip/hip_runtime.h>
#include <hip/hip_bf16.h>

typedef __bf16   bf16x8 __attribute__((ext_vector_type(8)));
typedef __bf16   bf16x2 __attribute__((ext_vector_type(2)));
typedef float    f32x4  __attribute__((ext_vector_type(4)));
typedef float    f32x2  __attribute__((ext_vector_type(2)));
typedef unsigned u32x4  __attribute__((ext_vector_type(4)));
typedef unsigned u32x2  __attribute__((ext_vector_type(2)));
typedef unsigned short u16x8 __attribute__((ext_vector_type(8)));

constexpr int SQ = 848, SK = 512, DH = 192, ROWB = 384;
constexpr int LDS_BYTES = 163840;
constexpr float CSC = 0.07216878364870322f * 1.4426950408889634f;

#define DI __device__ __forceinline__
DI unsigned cvtpk(float lo, float hi) { f32x2 v = {lo, hi}; bf16x2 b = __builtin_convertvector(v, bf16x2); return __builtin_bit_cast(unsigned, b); }
DI float xmax(float x) {
  auto r = __builtin_amdgcn_permlane32_swap(__float_as_uint(x), __float_as_uint(x), false, false);
  x = fmaxf(__uint_as_float(r[0]), __uint_as_float(r[1]));
  auto q = __builtin_amdgcn_permlane16_swap(__float_as_uint(x), __float_as_uint(x), false, false);
  return fmaxf(__uint_as_float(q[0]), __uint_as_float(q[1]));
}
DI float xsum(float x) {
  auto r = __builtin_amdgcn_permlane32_swap(__float_as_uint(x), __float_as_uint(x), false, false);
  x = __uint_as_float(r[0]) + __uint_as_float(r[1]);
  auto q = __builtin_amdgcn_permlane16_swap(__float_as_uint(x), __float_as_uint(x), false, false);
  return __uint_as_float(q[0]) + __uint_as_float(q[1]);
}
DI f32x4 score_tile(const unsigned short* krow, const bf16x8 (&qf)[6]) {
  f32x4 acc = {0.f, 0.f, 0.f, 0.f};
#pragma unroll
  for (int s = 0; s < 6; ++s) acc = __builtin_amdgcn_mfma_f32_16x16x32_bf16(*(const bf16x8*)(krow + 32 * s), qf[s], acc, 0, 0, 0);
  return acc;
}
DI void exact_tile(const unsigned short* Q, const unsigned short* K, const unsigned short* V, unsigned short* O, int head, int row0, int lo, int hi) {
  const unsigned lane = __builtin_amdgcn_mbcnt_hi(~0u, __builtin_amdgcn_mbcnt_lo(~0u, 0u));
  const int c = lane & 15, g4 = lane >> 4;
  const int qrow = min(row0 + c, SQ - 1);
  bf16x8 qf[6];
#pragma unroll
  for (int s = 0; s < 6; ++s) qf[s] = *(const bf16x8*)(Q + ((size_t)head * SQ + qrow) * DH + 32 * s + 8 * g4);
  const unsigned short* Kh = K + ((size_t)head * SK + c) * DH + 8 * g4;
  float m = -__builtin_huge_valf();
#pragma unroll 1
  for (int kb = 0; kb < SK / 16; ++kb) {
    const f32x4 a = score_tile(Kh + (size_t)kb * 16 * DH, qf);
    m = fmaxf(fmaxf(m, fmaxf(a[0], a[1])), fmaxf(a[2], a[3]));
  }
  const float mc = -xmax(m) * CSC;
  float l = 0.f;
  f32x4 o[12];
#pragma unroll
  for (int dt = 0; dt < 12; ++dt) o[dt] = f32x4{0.f, 0.f, 0.f, 0.f};
#pragma unroll 1
  for (int j = 0; j < SK / 32; ++j) {
    u32x4 p;
#pragma unroll
    for (int kt = 0; kt < 2; ++kt) {
      f32x4 a = score_tile(Kh + (size_t)(32 * j + 16 * kt) * DH, qf);
#pragma unroll
      for (int r = 0; r < 4; ++r) { a[r] = __builtin_amdgcn_exp2f(fmaf(a[r], CSC, mc)); l += a[r]; }
      p[2 * kt] = cvtpk(a[0], a[1]); p[2 * kt + 1] = cvtpk(a[2], a[3]);
    }
    const unsigned short* Vj = V + ((size_t)head * SK + 32 * j + 4 * g4) * DH + c;
#pragma unroll
    for (int dt = 0; dt < 12; ++dt) {
      u16x8 vf;
#pragma unroll
      for (int i = 0; i < 8; ++i) vf[i] = Vj[(16 * (i >> 2) + (i & 3)) * DH + 16 * dt];
      o[dt] = __builtin_amdgcn_mfma_f32_16x16x32_bf16(__builtin_bit_cast(bf16x8, vf), __builtin_bit_cast(bf16x8, p), o[dt], 0, 0, 0);
    }
  }
  const float inv = 1.0f / xsum(l);
  if (row0 + c < SQ && c >= lo && c < hi) {
#pragma unroll
    for (int dt = 0; dt < 12; ++dt)
      *(u32x2*)(O + ((size_t)head * SQ + qrow) * DH + 16 * dt + 4 * g4) = u32x2{cvtpk(o[dt][0] * inv, o[dt][1] * inv), cvtpk(o[dt][2] * inv, o[dt][3] * inv)};
  }
}

__global__ __launch_bounds__(256, 1)
void attn_fwd_g(const unsigned short* __restrict__ Qg, const unsigned short* __restrict__ Kg,
                const unsigned short* __restrict__ Vg, unsigned short* __restrict__ Og) {
  extern __shared__ __attribute__((aligned(512))) char lds[];
  const int tid0 = __builtin_amdgcn_readfirstlane(threadIdx.x), wg = blockIdx.x, wid = tid0 >> 6;
  unsigned bad;
  asm volatile(
  ".p2align 4\n"
  "s_load_dwordx8 s[84:91], %1, 0\n"
  "L_PC_%=:\n"
  "s_getpc_b64 s[52:53]\n"
  "s_lshr_b32 s79, %2, 3\n"
  "s_lshl_b32 s79, s79, 15\n"
  "s_add_u32 s52, s52, s79\n"
  "s_addc_u32 s53, s53, 0\n"
  "s_sub_i32 s54, L_END_%=-L_PC_%=, s79\n"
  "s_max_i32 s54, s54, 0\n"
  "s_mov_b32 s55, 0x20000\n"
  "s_add_u32 m0, %4, 2048\n"
  "v_mbcnt_lo_u32_b32 v0, -1, 0\n"
  "v_mbcnt_hi_u32_b32 v0, -1, v0\n"
  "v_add_lshl_u32 v1, v0, %3, 7\n"
  "buffer_load_dword v1, s[52:55], 0 offen lds\n"
  "s_bitcmp1_b32 %2, 0\n"
  "s_cbranch_scc1 2\n"
  "s_waitcnt vmcnt(0) lgkmcnt(0)\n"
  "s_sleep 110\n"
  "s_lshr_b32 s38, %3, 6\n"
  "s_lshr_b32 s79, %2, 3\n"
  "s_mul_i32 s100, s79, 43\n"
  "s_lshr_b32 s100, s100, 7\n"
  "s_mul_i32 s71, s100, 3\n"
  "s_sub_u32 s95, s79, s71\n"
  "s_and_b32 s101, %2, 7\n"
  "s_lshl_b32 s101, s101, 0\n"
  "s_lshr_b32 s79, s100, 0\n"
  "s_mul_i32 s79, s79, 7\n"
  "s_add_u32 s79, s79, s100\n"
  "s_add_u32 s79, s79, s101\n"
  "s_mul_i32 s37, s79, 196608\n"
  "s_mul_i32 s92, s79, 325632\n"
  "s_mul_i32 s79, s95, 110592\n"
  "s_add_u32 s92, s92, s79\n"
  "s_mul_i32 s79, s38, 3072\n"
  "s_add_u32 s96, s79, s37\n"
  "s_lshr_b32 s71, s38, 1\n"
  "s_lshl_b32 s71, s71, 6\n"
  "s_add_u32 s79, s79, s71\n"
  "s_add_u32 s79, s79, %4\n"
  "s_add_u32 s66, s79, 41152\n"
  "s_add_u32 s67, s79, 53504\n"
  "s_add_u32 s68, s79, 65856\n"
  "s_mul_i32 s79, s38, 3088\n"
  "s_add_u32 s79, s79, %4\n"
  "s_add_u32 s58, s79, 4096\n"
  "s_add_u32 s59, s79, 16448\n"
  "s_add_u32 s60, s79, 28800\n"
  "s_mul_i32 s72, s38, 21312\n"
  "s_add_u32 s72, s72, %4\n"
  "s_add_u32 s72, s72, 78208\n"
  "s_movk_i32 s42, 0xc00\n"
  "s_movk_i32 s31, 12288\n"
  "s_mov_b32 s43, 0x20000\n"
  "s_mov_b32 s51, 0x20000\n"
  "s_bitcmp1_b32 s95, 1\n"
  "s_movk_i32 s93, 288\n"
  "s_cmovk_i32 s93, 272\n"
  "s_mul_i32 s93, s93, 384\n"
  "s_mov_b32 s50, s93\n"
  "s_mul_i32 s80, s38, 24576\n"
  "s_add_u32 s81, s80, 6144\n"
  "v_lshrrev_b32 v1, 3, v0\n"
  "v_and_b32 v2, 2, v1\n"
  "v_and_b32 v3, 7, v0\n"
  "v_xor_b32 v2, v2, v3\n"
  "v_mul_u32_u24 v221, 0x180, v1\n"
  "v_lshl_add_u32 v221, v2, 4, v221\n"
  "s_waitcnt lgkmcnt(0)\n"
  "s_add_u32 s40, s86, s96\n"
  "s_addc_u32 s41, s87, 0\n"
  "s_add_u32 s48, s84, s92\n"
  "s_addc_u32 s49, s85, 0\n"
  "s_mov_b32 m0, s68\n"
  "s_nop 0\n"
  "buffer_load_dwordx4 v221, s[40:43], 0 offen offset:0 lds\n"
  "s_addk_i32 m0, 896\n"
  "s_nop 0\n"
  "buffer_load_dwordx4 v221, s[40:43], 0 offen offset:128 lds\n"
  "s_addk_i32 m0, 896\n"
  "s_nop 0\n"
  "buffer_load_dwordx4 v221, s[40:43], 0 offen offset:256 lds\n"
  "s_add_u32 s40, s40, s31\n"
  "s_addc_u32 s41, s41, 0\n"
  "s_add_u32 s44, s88, s96\n"
  "s_addc_u32 s45, s89, 0\n"
  "s_mov_b64 s[62:63], s[90:91]\n"
  "s_mov_b32 s90, 100663296\n"
  "s_mov_b32 s91, 0x20000\n"
  "s_add_u32 s29, %4, 163456\n"
  "v_mbcnt_lo_u32_b32 v4, -1, 0\n"
  "v_mbcnt_hi_u32_b32 v4, -1, v4\n"
  "v_mul_u32_u24 v4, 0x18c, v4\n"
  "s_mov_b32 m0, s29\n"
  "s_nop 0\n"
  "buffer_load_dword v4, s[88:91], s37 offen lds\n"
  "s_mul_i32 s56, s38, 3072\n"
  "s_add_u32 s56, s56, 98304\n"
  "s_add_u32 m0, s72, 17920\n"
  "s_nop 0\n"
  "buffer_load_dwordx4 v221, s[48:51], s56 offen offset:0  lds\n"
  "s_add_u32 s82, s80, 12288\n"
  "s_add_u32 s83, s80, 18432\n"
  "s_mov_b64 s[46:47], s[42:43]\n"
  "s_mov_b64 s[34:35], s[42:43]\n"
  "s_bitcmp1_b32 s38, 0\n"
  "s_movk_i32 s70, 512\n"
  "s_cmovk_i32 s70, -512\n"
  "s_movk_i32 s97, 0\n"
  "s_cmovk_i32 s97, -21312\n"
  "s_movk_i32 s61, 0\n"
  "s_cmovk_i32 s61, -21308\n"
  "s_movk_i32 s69, 20196\n"
  "s_cmovk_i32 s69, 20188\n"
  "s_add_u32 m0, s72, 18912\n"
  "s_nop 0\n"
  "buffer_load_dwordx4 v221, s[48:51], s56 offen offset:128  lds\n"
  "s_mov_b32 s98, 0xff00ff00\n"
  "s_mov_b32 s99, s98\n"
  "s_mov_b32 s73, 0x3dd53b94\n"
  "s_mov_b32 s74, 0xbdd53b94\n"
  "s_mov_b32 s75, 0x53800000\n"
  "s_mov_b32 s76, -1\n"
  "s_add_u32 m0, s72, 19904\n"
  "s_nop 0\n"
  "buffer_load_dwordx4 v221, s[48:51], s56 offen offset:256  lds\n"
  "s_mov_b32 s77, 1\n"
  "s_mov_b32 s78, 0\n"
  "s_mov_b32 s57, s92\n"
  "s_mul_i32 s39, s38, 3072\n"
  "s_mov_b32 s54, 0\n"
  "s_add_u32 m0, s58, 1792\n"
  "s_nop 0\n"
  "buffer_load_dwordx4 v221, s[48:51], s80 offen offset:256  lds\n"
  "s_mov_b32 s55, 0x20000\n"
  "L_PC2_%=:\n"
  "s_getpc_b64 s[20:21]\n"
  "s_lshl_b32 s24, s38, 13\n"
  "s_add_u32 s24, s24, L_BIG_%=-L_PC2_%=-4\n"
  "s_cmp_lt_u32 s38, 2\n"
  "s_cselect_b32 s24, s24, L_TAIL_%=-L_PC2_%=-4\n"
  "s_sub_u32 s25, L_END_%=-L_PC2_%=-4, s24\n"
  "s_add_u32 s20, s20, s24\n"
  "s_addc_u32 s21, s21, 0\n"
  "s_mov_b32 s23, 0x20000\n"
  "s_lshr_b32 s24, s38, 1\n"
  "s_mul_i32 s24, s24, s38\n"
  "s_mul_i32 s24, s24, 5\n"
  "s_lshr_b32 s24, s24, 1\n"
  "v_lshrrev_b32 v1, 4, v0\n"
  "v_and_b32 v2, 2, v0\n"
  "v_xor_b32_e64 v1, v1, v2\n"
  "s_add_u32 m0, s60, -1280\n"
  "s_nop 0\n"
  "buffer_load_dwordx4 v221, s[48:51], s80 offen offset:3328  lds\n"
  "v_lshlrev_b32 v1, 4, v1\n"
  "v_and_b32 v2, 7, v0\n"
  "v_lshl_add_u32 v72, v2, 7, v1\n"
  "v_bfe_u32 v3, v0, 3, 1\n"
  "v_mul_u32_u24 v2, 0x440, v3\n"
  "s_add_u32 m0, s66, -128\n"
  "s_nop 0\n"
  "buffer_load_dwordx4 v221, s[48:51], s80 offen offset:128  lds\n"
  "v_add3_u32 v222, v72, v2, s72\n"
  "v_mul_u32_u24 v2, 0x6080, v3\n"
  "v_add3_u32 v74, v72, v2, s58\n"
  "v_lshl_add_u32 v72, v3, 10, v72\n"
  "v_add_u32 v73, s66, v72\n"
  "s_add_u32 m0, s66, -2176\n"
  "s_nop 0\n"
  "buffer_load_dwordx4 v221, s[48:51], s80 offen offset:3200  lds\n"
  "v_add_u32 v72, s58, v72\n"
  "v_lshl_add_u32 v1, v3, 6, v1\n"
  "v_and_b32 v2, 3, v0\n"
  "v_lshl_add_u32 v1, v2, 7, v1\n"
  "v_bfe_u32 v2, v0, 2, 2\n"
  "s_add_u32 m0, s58, 0\n"
  "s_nop 0\n"
  "buffer_load_dwordx4 v221, s[48:51], s80 offen offset:0  lds\n"
  "v_mul_u32_u24 v2, 0xc00, v2\n"
  "s_and_b32 s79, s38, 1\n"
  "s_lshl_b32 s79, s79, 9\n"
  "s_add_u32 s79, s79, %4\n"
  "s_add_u32 s71, s79, 41152\n"
  "s_add_u32 m0, s58, -2048\n"
  "s_nop 0\n"
  "buffer_load_dwordx4 v221, s[48:51], s80 offen offset:3072  lds\n"
  "v_add3_u32 v216, v1, v2, s71\n"
  "v_add_u32 v235, s70, v216\n"
  "v_add_u32 v233, s97, v222\n"
  "s_mov_b64 exec, s[98:99]\n"
  "v_add_u32 v233, 0x4f00, v233\n"
  "s_mov_b64 exec, -1\n"
  "v_lshrrev_b32_e64 v1, 4, v0\n"
  "v_mul_u32_u24 v1, 0xc10, v1\n"
  "s_mov_b32 m0, s67\n"
  "s_nop 0\n"
  "buffer_load_dwordx4 v221, s[40:43], 0 offen offset:0 lds\n"
  "v_bfe_u32 v2, v0, 2, 2\n"
  "v_lshl_add_u32 v1, v2, 7, v1\n"
  "v_bfe_u32 v2, v0, 1, 1\n"
  "v_lshl_add_u32 v1, v2, 6, v1\n"
  "v_and_b32_e64 v2, 1, v0\n"
  "s_addk_i32 m0, 896\n"
  "s_nop 0\n"
  "buffer_load_dwordx4 v221, s[40:43], 0 offen offset:128 lds\n"
  "v_lshl_add_u32 v1, v2, 3, v1\n"
  "s_add_u32 s71, s79, 4096\n"
  "v_add_u32 v1, s71, v1\n"
  "v_lshrrev_b32 v2, 3, v0\n"
  "v_xor_b32_e64 v2, s38, v2\n"
  "s_addk_i32 m0, 896\n"
  "s_nop 0\n"
  "buffer_load_dwordx4 v221, s[40:43], 0 offen offset:256 lds\n"
  "s_add_u32 s40, s40, s31\n"
  "s_addc_u32 s41, s41, 0\n"
  "v_and_b32 v2, 1, v2\n"
  "v_lshl_add_u32 v217, v2, 5, v1\n"
  "v_xor_b32 v2, 1, v2\n"
  "v_lshl_add_u32 v219, v2, 5, v1\n"
  "v_add_u32 v218, s70, v217\n"
  "s_add_u32 m0, s59, 896\n"
  "s_nop 0\n"
  "buffer_load_dwordx4 v221, s[44:47], 0 offen offset:128 lds\n"
  "v_add_u32_e64 v220, s70, v219\n"
  "s_lshl_b32 s71, s38, 9\n"
  "s_add_u32 s71, s71, %4\n"
  "s_add_u32 s71, s71, 0\n"
  "v_lshl_add_u32 v231, v0, 3, s71\n"
  "s_addk_i32 m0, 896\n"
  "s_nop 0\n"
  "buffer_load_dwordx4 v221, s[44:47], 0 offen offset:256 lds\n"
  "s_mov_b64 s[32:33], s[44:45]\n"
  "s_add_u32 s44, s44, s31\n"
  "s_addc_u32 s45, s45, 0\n"
  "v_add_u32_e64 v232, s70, v231\n"
  "v_mov_b32 v248, 0x43400000\n"
  "v_mov_b32 v249, 0x43400000\n"
  "v_mov_b32 v250, 0x43400000\n"
  "v_mov_b32 v251, 0x43400000\n"
  "s_add_u32 m0, s72, 0\n"
  "s_nop 0\n"
  "buffer_load_dwordx4 v221, s[48:51], s81 offen offset:0  lds\n"
  "v_mov_b32 v1, 0x3f803f80\n"
  "v_accvgpr_write_b32 a152, v1\n"
  "v_accvgpr_write_b32 a153, v1\n"
  "v_accvgpr_write_b32 a154, v1\n"
  "v_accvgpr_write_b32 a155, v1\n"
  "s_add_u32 m0, s72, -1984\n"
  "s_nop 0\n"
  "buffer_load_dwordx4 v221, s[48:51], s81 offen offset:3072  lds\n"
  "v_bfe_u32 v1, v0, 3, 1\n"
  "v_lshrrev_b32 v230, 4, v0\n"
  "v_lshl_add_u32 v1, v1, 2, v230\n"
  "v_mul_u32_u24 v228, 0x180, v1\n"
  "s_and_b32 s71, s38, 1\n"
  "s_add_u32 m0, s72, 2112\n"
  "s_nop 0\n"
  "buffer_load_dwordx4 v221, s[48:51], s81 offen offset:128  lds\n"
  "s_lshl_b32 s71, s71, 1\n"
  "v_and_b32 v1, 7, v0\n"
  "v_xor_b32_e64 v1, s71, v1\n"
  "v_lshl_add_u32 v228, v1, 4, v228\n"
  "v_mul_u32_u24 v230, 0x120, v230\n"
  "s_add_u32 m0, s72, 128\n"
  "s_nop 0\n"
  "buffer_load_dwordx4 v221, s[48:51], s81 offen offset:3200  lds\n"
  "v_and_b32 v1, 3, v0\n"
  "v_lshl_add_u32 v230, v1, 6, v230\n"
  "v_bfe_u32 v1, v0, 2, 2\n"
  "v_lshl_add_u32 v230, v1, 3, v230\n"
  "v_add_u32 v230, s72, v230\n"
  "s_add_u32 m0, s60, -256\n"
  "s_nop 0\n"
  "buffer_load_dwordx4 v221, s[48:51], s81 offen offset:256  lds\n"
  "v_and_b32_e64 v1, 31, v0\n"
  "v_lshlrev_b32_e64 v229, 3, v1\n"
  "v_lshrrev_b32_e64 v1, 5, v0\n"
  "v_mul_u32_u24 v1, 0x460, v1\n"
  "v_add3_u32 v229, v1, v229, s72\n"
  "s_waitcnt vmcnt(16)\n"
  "s_barrier\n"
  "ds_read_b128 a[164:167], v216 offset:24704\n"
  "ds_read_b128 a[168:171], v216 offset:24768\n"
  "ds_read_b128 a[172:175], v216 offset:25728\n"
  "ds_read_b128 a[176:179], v216 offset:25792\n"
  "ds_read_b128 a[180:183], v216 offset:26752\n"
  "ds_read_b128 a[184:187], v216 offset:26816\n"
  "ds_read_b128 a[96:99], v233 offset:17920\n"
  "ds_read_b128 a[100:103], v233 offset:17984\n"
  "ds_read_b128 a[104:107], v233 offset:19040\n"
  "ds_read_b128 a[108:111], v233 offset:19104\n"
  "ds_read_b128 a[112:115], v233 offset:20160\n"
  "ds_read_b128 a[116:119], v233 offset:20224\n"
  "v_bfe_u32 v5, v231, 1, 8\n"
  "v_add_u32_e64 v5, s29, v5\n"
  "ds_read_b128 a[188:191], v235 offset:24704\n"
  "ds_read_b128 a[192:195], v235 offset:24768\n"
  "ds_read_b128 a[196:199], v235 offset:25728\n"
  "ds_read_b128 a[200:203], v235 offset:25792\n"
  "ds_read_b128 a[204:207], v235 offset:26752\n"
  "ds_read_b128 a[208:211], v235 offset:26816\n"
  "ds_read_b32 v4, v5\n"
  "v_bfe_u32 v234, v231, 1, 6\n"
  "s_waitcnt lgkmcnt(12)\n"
  "v_mfma_f32_16x16x32_bf16 v[64:67], a[164:167], a[96:99], v[248:251]\n"
  "s_waitcnt lgkmcnt(11)\n"
  "v_mfma_f32_16x16x32_bf16 v[64:67], a[168:171], a[100:103], v[64:67]\n"
  "s_add_u32 m0, s60, -2304\n"
  "s_nop 0\n"
  "buffer_load_dwordx4 v221, s[48:51], s81 offen offset:3328  lds\n"
  "s_waitcnt lgkmcnt(10)\n"
  "v_mfma_f32_16x16x32_bf16 v[64:67], a[172:175], a[104:107], v[64:67]\n"
  "s_waitcnt lgkmcnt(9)\n"
  "v_mfma_f32_16x16x32_bf16 v[64:67], a[176:179], a[108:111], v[64:67]\n"
  "s_waitcnt lgkmcnt(8)\n"
  "v_mfma_f32_16x16x32_bf16 v[64:67], a[180:183], a[112:115], v[64:67]\n"
  "s_waitcnt lgkmcnt(0)\n"
  "v_and_b32 v4, 0x7fff7fff, v4\n"
  "v_lshlrev_b32 v5, 16, v4\n"
  "v_mfma_f32_16x16x32_bf16 v[64:67], a[184:187], a[116:119], v[64:67]\n"
  "s_add_u32 m0, s72, 8704\n"
  "s_nop 0\n"
  "buffer_load_dwordx4 v221, s[48:51], s82 offen offset:256  lds\n"
  "v_mfma_f32_16x16x32_bf16 v[240:243], a[188:191], a[96:99], v[248:251]\n"
  "s_waitcnt vmcnt(12)\n"
  "ds_read_b128 a[20:23], v74 offset:2112\n"
  "ds_read_b128 a[16:19], v74 offset:2048\n"
  "ds_read_b128 a[12:15], v73 offset:64\n"
  "v_mfma_f32_16x16x32_bf16 v[240:243], a[192:195], a[100:103], v[240:243]\n"
  "ds_read_b128 a[8:11], v73 offset:0\n"
  "ds_read_b128 a[4:7], v72 offset:64\n"
  "ds_read_b128 a[0:3], v72 offset:0\n"
  "v_max_u32_e64 v4, v4, v5\n"
  "v_mfma_f32_16x16x32_bf16 v[240:243], a[196:199], a[104:107], v[240:243]\n"
  "s_nop 0\n"
  "v_max_u32_dpp v4, v4, v4 quad_perm:[1,0,3,2] row_mask:0xf bank_mask:0xf\n"
  "v_max3_f32 v227, v64, v65, v66\n"
  "v_mfma_f32_16x16x32_bf16 v[240:243], a[200:203], a[108:111], v[240:243]\n"
  "s_add_u32 m0, s72, 6720\n"
  "s_nop 0\n"
  "buffer_load_dwordx4 v221, s[48:51], s82 offen offset:3328  lds\n"
  "v_max_u32_dpp v4, v4, v4 quad_perm:[2,3,0,1] row_mask:0xf bank_mask:0xf\n"
  "v_add_u32 v4, -1, v4\n"
  "v_mfma_f32_16x16x32_bf16 v[240:243], a[204:207], a[112:115], v[240:243]\n"
  "s_nop 0\n"
  "v_min_u32_dpp v4, v4, v4 row_shr:4 row_mask:0xf bank_mask:0xf\n"
  "v_mfma_f32_16x16x32_bf16 v[240:243], a[208:211], a[116:119], v[240:243]\n"
  "s_waitcnt lgkmcnt(5)\n"
  "v_min_u32_dpp v4, v4, v4 row_shr:8 row_mask:0xf bank_mask:0xf\n"
  "v_mfma_f32_16x16x32_bf16 v[0:3], a[184:187], a[20:23], v[248:251]\n"
  "s_waitcnt lgkmcnt(4)\n"
  "v_min_u32_dpp v4, v4, v4 row_bcast:15 row_mask:0xa bank_mask:0xf\n"
  "v_mfma_f32_16x16x32_bf16 v[0:3], a[180:183], a[16:19], v[0:3]\n"
  "s_add_u32 m0, s72, 6592\n"
  "s_nop 0\n"
  "buffer_load_dwordx4 v221, s[48:51], s82 offen offset:128  lds\n"
  "s_waitcnt lgkmcnt(3)\n"
  "v_min_u32_dpp v4, v4, v4 row_bcast:31 row_mask:0xc bank_mask:0xf\n"
  "v_mfma_f32_16x16x32_bf16 v[0:3], a[176:179], a[12:15], v[0:3]\n"
  "s_waitcnt lgkmcnt(2)\n"
  "v_readlane_b32 s65, v4, 63\n"
  "v_mfma_f32_16x16x32_bf16 v[0:3], a[172:175], a[8:11], v[0:3]\n"
  "s_waitcnt lgkmcnt(1)\n"
  "v_max3_f32 v227, v227, v67, v240\n"
  "v_max3_f32 v227, v227, v241, v242\n"
  "v_mfma_f32_16x16x32_bf16 v[0:3], a[168:171], a[4:7], v[0:3]\n"
  "s_add_u32 s65, s65, 1\n"
  "s_lshr_b32 s65, s65, 23\n"
  "s_sub_i32 s65, 0x77, s65\n"
  "s_and_b32 s65, s65, -4\n"
  "s_max_i32 s65, s65, 0xffffffc4\n"
  "s_waitcnt lgkmcnt(0)\n"
  "v_cvt_f32_i32 v4, s65\n"
  "v_max_f32 v227, v227, v243\n"
  "v_mfma_f32_16x16x32_bf16 v[0:3], a[164:167], a[0:3], v[0:3]\n"
  "s_add_u32 m0, s72, 4608\n"
  "s_nop 0\n"
  "buffer_load_dwordx4 v221, s[48:51], s82 offen offset:3200  lds\n"
  "v_readfirstlane_b32 s64, v4\n"
  "v_mul_f32 v227, s74, v227\n"
  "s_nop 0\n"
  "v_add_f32 v227, s64, v227\n"
  "v_mfma_f32_16x16x32_bf16 v[4:7], a[208:211], a[20:23], v[248:251]\n"
  "s_add_u32 s65, s65, 0xab\n"
  "ds_bpermute_b32 v227, v234, v227\n"
  "v_mfma_f32_16x16x32_bf16 v[4:7], a[204:207], a[16:19], v[4:7]\n"
  "s_cmpk_gt_u32 s65, 0xd3\n"
  "s_cselect_b32 s65, 0, s65\n"
  "v_mfma_f32_16x16x32_bf16 v[4:7], a[200:203], a[12:15], v[4:7]\n"
  "s_lshl_b32 s65, s65, 23\n"
  "v_max3_f32 v223, v0, v1, v2\n"
  "v_max_f32 v223, v223, v3\n"
  "v_mfma_f32_16x16x32_bf16 v[4:7], a[196:199], a[8:11], v[4:7]\n"
  "s_add_u32 m0, s72, 4480\n"
  "s_nop 0\n"
  "buffer_load_dwordx4 v221, s[48:51], s82 offen offset:0  lds\n"
  "v_mfma_f32_16x16x32_bf16 v[4:7], a[192:195], a[4:7], v[4:7]\n"
  "s_mov_b32 s75, s65\n"
  "v_mul_f32 v223, s74, v223\n"
  "v_add_f32_e64 v223, s64, v223\n"
  "v_mfma_f32_16x16x32_bf16 v[4:7], a[188:191], a[0:3], v[4:7]\n"
  "ds_bpermute_b32 v223, v234, v223\n"
  "s_waitcnt lgkmcnt(1)\n"
  "v_fma_f32 v64, v64, s73, v227\n"
  "v_exp_f32 v64, v64\n"
  "v_fma_f32 v65, v65, s73, v227\n"
  "v_exp_f32 v65, v65\n"
  "v_fma_f32 v66, v66, s73, v227\n"
  "v_exp_f32 v66, v66\n"
  "v_fma_f32 v67, v67, s73, v227\n"
  "v_exp_f32_e64 v67, v67\n"
  "v_cvt_pk_bf16_f32 v64, v64, v65\n"
  "v_cvt_pk_bf16_f32 v65, v66, v67\n"
  "ds_write_b64 v231, v[64:65] offset:0\n"
  "s_waitcnt vmcnt(11) lgkmcnt(0)\n"
  "s_barrier\n"
  "ds_read_b128 a[212:215], v216 offset:12352\n"
  "ds_read_b128 a[216:219], v216 offset:12416\n"
  "ds_read_b128 a[220:223], v216 offset:13376\n"
  "ds_read_b128 a[224:227], v216 offset:13440\n"
  "ds_read_b128 a[228:231], v216 offset:14400\n"
  "ds_read_b128 a[232:235], v216 offset:14464\n"
  "ds_read_b64 a[156:157], v231 offset:0\n"
  "ds_read_b64 a[158:159], v232 offset:0\n"
  "s_waitcnt lgkmcnt(7)\n"
  "v_mfma_f32_16x16x32_bf16 v[64:67], a[212:215], a[96:99], v[248:251]\n"
  "s_add_u32 m0, s72, 2496\n"
  "s_nop 0\n"
  "buffer_load_dwordx4 v221, s[48:51], s82 offen offset:3072  lds\n"
  "ds_read_b64_tr_b16 a[144:145], v217 offset:14400\n"
  "ds_read_b64_tr_b16 a[146:147], v218 offset:14400\n"
  "s_waitcnt lgkmcnt(8)\n"
  "v_mfma_f32_16x16x32_bf16 v[64:67], a[216:219], a[100:103], v[64:67]\n"
  "ds_read_b64_tr_b16 a[148:149], v217 offset:14416\n"
  "ds_read_b64_tr_b16 a[150:151], v218 offset:14416\n"
  "s_waitcnt lgkmcnt(9)\n"
  "v_mfma_f32_16x16x32_bf16 v[64:67], a[220:223], a[104:107], v[64:67]\n"
  "ds_read_b64_tr_b16 v[76:77], v217 offset:13376\n"
  "ds_read_b64_tr_b16 v[78:79], v218 offset:13376\n"
  "s_waitcnt lgkmcnt(10)\n"
  "v_mfma_f32_16x16x32_bf16 v[64:67], a[224:227], a[108:111], v[64:67]\n"
  "ds_read_b64_tr_b16 v[80:81], v217 offset:13392\n"
  "ds_read_b64_tr_b16 v[82:83], v218 offset:13392\n"
  "s_waitcnt lgkmcnt(11)\n"
  "v_mfma_f32_16x16x32_bf16 v[64:67], a[228:231], a[112:115], v[64:67]\n"
  "s_add_u32 m0, s72, 11200\n"
  "s_nop 0\n"
  "buffer_load_dwordx4 v221, s[48:51], s83 offen offset:0  lds\n"
  "ds_read_b128 a[120:123], v235 offset:12352\n"
  "ds_read_b128 a[124:127], v235 offset:12416\n"
  "s_waitcnt lgkmcnt(12)\n"
  "v_mfma_f32_16x16x32_bf16 v[64:67], a[232:235], a[116:119], v[64:67]\n"
  "ds_read_b128 a[128:131], v235 offset:13376\n"
  "ds_read_b128 a[132:135], v235 offset:13440\n"
  "ds_read_b128 a[136:139], v235 offset:14400\n"
  "ds_read_b128 a[140:143], v235 offset:14464\n"
  "s_waitcnt lgkmcnt(12)\n"
  "v_mfma_f32_16x16x32_bf16 a[244:247], a[156:159], a[144:147], 1.0\n"
  "ds_read_b64_tr_b16 v[84:85], v219 offset:14400\n"
  "ds_read_b64_tr_b16 v[86:87], v220 offset:14400\n"
  "s_waitcnt lgkmcnt(12)\n"
  "v_mfma_f32_16x16x32_bf16 a[248:251], a[156:159], a[148:151], 1.0\n"
  "s_add_u32 m0, s72, 9216\n"
  "s_nop 0\n"
  "buffer_load_dwordx4 v221, s[48:51], s83 offen offset:3072  lds\n"
  "ds_read_b64_tr_b16 v[244:245], v219 offset:14416\n"
  "ds_read_b64_tr_b16 v[246:247], v220 offset:14416\n"
  "s_waitcnt lgkmcnt(12)\n"
  "v_mfma_f32_16x16x32_bf16 a[236:239], a[156:159], v[76:79], 1.0\n"
  "ds_read_b64_tr_b16 a[252:253], v219 offset:13376\n"
  "ds_read_b64_tr_b16 a[254:255], v220 offset:13376\n"
  "s_waitcnt lgkmcnt(12)\n"
  "v_fma_f32 v0, v0, s73, v223\n"
  "v_exp_f32 v0, v0\n"
  "v_fma_f32 v1, v1, s73, v223\n"
  "v_exp_f32 v1, v1\n"
  "v_fma_f32 v2, v2, s73, v223\n"
  "v_exp_f32 v2, v2\n"
  "v_fma_f32 v3, v3, s73, v223\n"
  "v_exp_f32_e64 v3, v3\n"
  "v_mfma_f32_16x16x32_bf16 a[240:243], a[156:159], v[80:83], 1.0\n"
  "ds_read_b64_tr_b16 v[236:237], v219 offset:13392\n"
  "ds_read_b64_tr_b16 v[238:239], v220 offset:13392\n"
  "v_cvt_pk_bf16_f32 v8, v0, v1\n"
  "v_cvt_pk_bf16_f32 v9, v2, v3\n"
  "v_mfma_f32_16x16x32_bf16 v[0:3], a[232:235], a[20:23], v[248:251]\n"
  "v_fma_f32 v4, v4, s73, v223\n"
  "v_fma_f32 v5, v5, s73, v223\n"
  "v_mfma_f32_16x16x32_bf16 v[0:3], a[228:231], a[16:19], v[0:3]\n"
  "s_add_u32 m0, s72, 13312\n"
  "s_nop 0\n"
  "buffer_load_dwordx4 v221, s[48:51], s83 offen offset:128  lds\n"
  "v_mfma_f32_16x16x32_bf16 v[0:3], a[224:227], a[12:15], v[0:3]\n"
  "v_exp_f32 v4, v4\n"
  "v_mfma_f32_16x16x32_bf16 v[0:3], a[220:223], a[8:11], v[0:3]\n"
  "v_exp_f32_e64 v5, v5\n"
  "v_mfma_f32_16x16x32_bf16 v[0:3], a[216:219], a[4:7], v[0:3]\n"
  "v_fma_f32 v6, v6, s73, v223\n"
  "v_exp_f32 v6, v6\n"
  "v_fma_f32 v7, v7, s73, v223\n"
  "v_exp_f32 v7, v7\n"
  "v_mfma_f32_16x16x32_bf16 v[0:3], a[212:215], a[0:3], v[0:3]\n"
  "s_add_u32 m0, s72, 11328\n"
  "s_nop 0\n"
  "buffer_load_dwordx4 v221, s[48:51], s83 offen offset:3200  lds\n"
  "s_waitcnt lgkmcnt(13)\n"
  "v_cvt_pk_bf16_f32 v10, v4, v5\n"
  "v_cvt_pk_bf16_f32 v11, v6, v7\n"
  "v_mfma_f32_16x16x32_bf16 v[4:7], a[120:123], a[0:3], v[248:251]\n"
  "s_waitcnt vmcnt(10)\n"
  "ds_read_b128 a[24:27], v222 offset:0\n"
  "ds_read_b128 a[28:31], v222 offset:64\n"
  "ds_read_b128 a[32:35], v222 offset:2240\n"
  "s_waitcnt lgkmcnt(15)\n"
  "v_mfma_f32_16x16x32_bf16 v[4:7], a[124:127], a[4:7], v[4:7]\n"
  "ds_read_b128 a[36:39], v222 offset:2304\n"
  "ds_read_b128 a[40:43], v72 offset:24704\n"
  "ds_read_b128 a[44:47], v72 offset:24768\n"
  "s_waitcnt lgkmcnt(15)\n"
  "v_mfma_f32_16x16x32_bf16 v[4:7], a[128:131], a[8:11], v[4:7]\n"
  "v_fma_f32 v64, v64, s73, v227\n"
  "v_fma_f32 v65, v65, s73, v227\n"
  "v_mfma_f32_16x16x32_bf16 v[4:7], a[132:135], a[12:15], v[4:7]\n"
  "s_add_u32 m0, s72, 15424\n"
  "s_nop 0\n"
  "buffer_load_dwordx4 v221, s[48:51], s83 offen offset:256  lds\n"
  "v_mfma_f32_16x16x32_bf16 v[4:7], a[136:139], a[16:19], v[4:7]\n"
  "s_waitcnt lgkmcnt(14)\n"
  "v_exp_f32 v64, v64\n"
  "v_mfma_f32_16x16x32_bf16 v[4:7], a[140:143], a[20:23], v[4:7]\n"
  "s_waitcnt lgkmcnt(5)\n"
  "v_exp_f32 v65, v65\n"
  "v_mfma_f32_16x16x32_bf16 v[16:19], a[164:167], a[24:27], v[248:251]\n"
  "s_waitcnt lgkmcnt(4)\n"
  "v_fma_f32 v66, v66, s73, v227\n"
  "v_fma_f32 v67, v67, s73, v227\n"
  "v_mfma_f32_16x16x32_bf16 v[16:19], a[168:171], a[28:31], v[16:19]\n"
  "s_add_u32 m0, s72, 13440\n"
  "s_nop 0\n"
  "buffer_load_dwordx4 v221, s[48:51], s83 offen offset:3328  lds\n"
  "s_waitcnt lgkmcnt(3)\n"
  "v_mfma_f32_16x16x32_bf16 v[16:19], a[172:175], a[32:35], v[16:19]\n"
  "s_waitcnt lgkmcnt(2)\n"
  "v_exp_f32 v66, v66\n"
  "v_mfma_f32_16x16x32_bf16 v[16:19], a[176:179], a[36:39], v[16:19]\n"
  "s_waitcnt lgkmcnt(1)\n"
  "v_exp_f32 v67, v67\n"
  "v_mfma_f32_16x16x32_bf16 v[16:19], a[180:183], a[40:43], v[16:19]\n"
  "s_waitcnt lgkmcnt(0)\n"
  "v_cvt_pk_bf16_f32 v64, v64, v65\n"
  "v_cvt_pk_bf16_f32 v65, v66, v67\n"
  "v_mfma_f32_16x16x32_bf16 v[16:19], a[184:187], a[44:47], v[16:19]\n"
  "s_mov_b32 m0, s66\n"
  "s_nop 0\n"
  "buffer_load_dwordx4 v221, s[40:43], 0 offen offset:0 lds\n"
  "v_mfma_f32_16x16x32_bf16 v[20:23], a[188:191], a[24:27], v[248:251]\n"
  "v_fma_f32 v0, v0, s73, v223\n"
  "v_fma_f32 v1, v1, s73, v223\n"
  "v_mfma_f32_16x16x32_bf16 v[20:23], a[192:195], a[28:31], v[20:23]\n"
  "v_exp_f32 v0, v0\n"
  "v_mfma_f32_16x16x32_bf16 v[20:23], a[196:199], a[32:35], v[20:23]\n"
  "v_max3_f32 v224, v16, v17, v18\n"
  "v_max_f32 v224, v224, v19\n"
  "v_mfma_f32_16x16x32_bf16 v[20:23], a[200:203], a[36:39], v[20:23]\n"
  "s_addk_i32 m0, 896\n"
  "s_nop 0\n"
  "buffer_load_dwordx4 v221, s[40:43], 0 offen offset:128 lds\n"
  "v_mfma_f32_16x16x32_bf16 v[20:23], a[204:207], a[40:43], v[20:23]\n"
  "v_mul_f32 v224, s74, v224\n"
  "v_add_f32 v224, s64, v224\n"
  "v_mfma_f32_16x16x32_bf16 v[20:23], a[208:211], a[44:47], v[20:23]\n"
  "ds_bpermute_b32 v224, v234, v224\n"
  "v_mfma_f32_16x16x32_bf16 v[104:107], v[8:11], a[144:147], 1.0\n"
  "v_exp_f32 v1, v1\n"
  "v_mfma_f32_16x16x32_bf16 v[108:111], v[8:11], a[148:151], 1.0\n"
  "s_addk_i32 m0, 896\n"
  "s_nop 0\n"
  "buffer_load_dwordx4 v221, s[40:43], 0 offen offset:256 lds\n"
  "s_add_u32 s40, s40, s31\n"
  "s_addc_u32 s41, s41, 0\n"
  "v_mfma_f32_16x16x32_bf16 v[112:115], v[8:11], v[84:87], 1.0\n"
  "v_fma_f32 v2, v2, s73, v223\n"
  "v_fma_f32 v3, v3, s73, v223\n"
  "v_mfma_f32_16x16x32_bf16 v[116:119], v[8:11], v[244:247], 1.0\n"
  "v_exp_f32 v2, v2\n"
  "v_mfma_f32_16x16x32_bf16 v[88:91], v[8:11], v[76:79], 1.0\n"
  "v_exp_f32_e64 v3, v3\n"
  "v_mfma_f32_16x16x32_bf16 v[92:95], v[8:11], v[80:83], 1.0\n"
  "s_mov_b32 m0, s58\n"
  "s_nop 0\n"
  "buffer_load_dwordx4 v221, s[32:35], 0 offen offset:0 lds\n"
  "v_mfma_f32_16x16x32_bf16 v[96:99], v[8:11], a[252:255], 1.0\n"
  "s_waitcnt lgkmcnt(0)\n"
  "v_fma_f32 v16, v16, s73, v224\n"
  "v_exp_f32 v16, v16\n"
  "v_fma_f32 v17, v17, s73, v224\n"
  "v_exp_f32 v17, v17\n"
  "v_fma_f32 v18, v18, s73, v224\n"
  "v_exp_f32 v18, v18\n"
  "v_fma_f32 v19, v19, s73, v224\n"
  "v_exp_f32_e64 v19, v19\n"
  "v_mfma_f32_16x16x32_bf16 v[100:103], v[8:11], v[236:239], 1.0\n"
  "v_cvt_pk_bf16_f32 v24, v16, v17\n"
  "v_cvt_pk_bf16_f32 v25, v18, v19\n"
  "v_mfma_f32_16x16x32_bf16 v[16:19], a[212:215], a[24:27], v[248:251]\n"
  "v_fma_f32 v20, v20, s73, v224\n"
  "v_fma_f32 v21, v21, s73, v224\n"
  "v_mfma_f32_16x16x32_bf16 v[16:19], a[216:219], a[28:31], v[16:19]\n"
  "s_addk_i32 m0, 1024\n"
  "s_nop 0\n"
  "buffer_load_dwordx4 v221, s[44:47], 0 offen offset:0 lds\n"
  "v_mfma_f32_16x16x32_bf16 v[16:19], a[220:223], a[32:35], v[16:19]\n"
  "v_exp_f32 v20, v20\n"
  "v_mfma_f32_16x16x32_bf16 v[16:19], a[224:227], a[36:39], v[16:19]\n"
  "v_exp_f32 v21, v21\n"
  "v_mfma_f32_16x16x32_bf16 v[16:19], a[228:231], a[40:43], v[16:19]\n"
  "v_fma_f32 v22, v22, s73, v224\n"
  "v_exp_f32 v22, v22\n"
  "v_fma_f32 v23, v23, s73, v224\n"
  "v_exp_f32 v23, v23\n"
  "v_mfma_f32_16x16x32_bf16 v[16:19], a[232:235], a[44:47], v[16:19]\n"
  "s_addk_i32 m0, 768\n"
  "s_nop 0\n"
  "buffer_load_dwordx4 v221, s[44:47], 0 offen offset:256 lds\n"
  "s_mov_b64 s[32:33], s[44:45]\n"
  "s_add_u32 s44, s44, s31\n"
  "s_addc_u32 s45, s45, 0\n"
  "v_cvt_pk_bf16_f32 v26, v20, v21\n"
  "v_cvt_pk_bf16_f32 v27, v22, v23\n"
  "v_mfma_f32_16x16x32_bf16 v[20:23], a[140:143], a[44:47], v[248:251]\n"
  "s_waitcnt vmcnt(12)\n"
  "ds_read_b128 a[68:71], v222 offset:9024\n"
  "ds_read_b128 a[64:67], v222 offset:8960\n"
  "ds_read_b128 a[60:63], v222 offset:6784\n"
  "v_mfma_f32_16x16x32_bf16 v[20:23], a[136:139], a[40:43], v[20:23]\n"
  "ds_read_b128 a[56:59], v222 offset:6720\n"
  "ds_read_b128 a[52:55], v222 offset:4544\n"
  "ds_read_b128 a[48:51], v222 offset:4480\n"
  "v_mfma_f32_16x16x32_bf16 v[20:23], a[132:135], a[36:39], v[20:23]\n"
  "v_fma_f32 v4, v4, s73, v223\n"
  "v_fma_f32 v5, v5, s73, v223\n"
  "v_mfma_f32_16x16x32_bf16 v[20:23], a[128:131], a[32:35], v[20:23]\n"
  "v_exp_f32 v4, v4\n"
  "v_mfma_f32_16x16x32_bf16 v[20:23], a[124:127], a[28:31], v[20:23]\n"
  "v_exp_f32 v5, v5\n"
  "v_mfma_f32_16x16x32_bf16 v[20:23], a[120:123], a[24:27], v[20:23]\n"
  "s_waitcnt lgkmcnt(5)\n"
  "v_fma_f32 v6, v6, s73, v223\n"
  "v_fma_f32 v7, v7, s73, v223\n"
  "v_mfma_f32_16x16x32_bf16 v[32:35], a[184:187], a[68:71], v[248:251]\n"
  "s_waitcnt lgkmcnt(4)\n"
  "v_exp_f32 v6, v6\n"
  "v_mfma_f32_16x16x32_bf16 v[32:35], a[180:183], a[64:67], v[32:35]\n"
  "s_waitcnt lgkmcnt(3)\n"
  "v_exp_f32 v7, v7\n"
  "v_mfma_f32_16x16x32_bf16 v[32:35], a[176:179], a[60:63], v[32:35]\n"
  "s_waitcnt lgkmcnt(2)\n"
  "v_fma_f32 v16, v16, s73, v224\n"
  "v_fma_f32 v17, v17, s73, v224\n"
  "v_mfma_f32_16x16x32_bf16 v[32:35], a[172:175], a[56:59], v[32:35]\n"
  "s_waitcnt lgkmcnt(1)\n"
  "v_exp_f32 v16, v16\n"
  "v_mfma_f32_16x16x32_bf16 v[32:35], a[168:171], a[52:55], v[32:35]\n"
  "s_waitcnt lgkmcnt(0)\n"
  "v_exp_f32 v17, v17\n"
  "v_mfma_f32_16x16x32_bf16 v[32:35], a[164:167], a[48:51], v[32:35]\n"
  "v_fma_f32 v18, v18, s73, v224\n"
  "v_fma_f32 v19, v19, s73, v224\n"
  "v_mfma_f32_16x16x32_bf16 v[36:39], a[208:211], a[68:71], v[248:251]\n"
  "v_exp_f32 v18, v18\n"
  "v_mfma_f32_16x16x32_bf16 v[36:39], a[204:207], a[64:67], v[36:39]\n"
  "v_exp_f32 v19, v19\n"
  "v_mfma_f32_16x16x32_bf16 v[36:39], a[200:203], a[60:63], v[36:39]\n"
  "v_max3_f32 v225, v32, v33, v34\n"
  "v_max_f32 v225, v225, v35\n"
  "v_mfma_f32_16x16x32_bf16 v[36:39], a[196:199], a[56:59], v[36:39]\n"
  "v_mul_f32 v225, s74, v225\n"
  "v_add_f32 v225, s64, v225\n"
  "v_mfma_f32_16x16x32_bf16 v[36:39], a[192:195], a[52:55], v[36:39]\n"
  "ds_bpermute_b32 v225, v234, v225\n"
  "v_mfma_f32_16x16x32_bf16 v[36:39], a[188:191], a[48:51], v[36:39]\n"
  "v_fma_f32 v20, v20, s73, v224\n"
  "v_fma_f32 v21, v21, s73, v224\n"
  "v_mfma_f32_16x16x32_bf16 v[136:139], v[24:27], a[144:147], 1.0\n"
  "v_exp_f32 v20, v20\n"
  "v_mfma_f32_16x16x32_bf16 v[140:143], v[24:27], a[148:151], 1.0\n"
  "v_exp_f32 v21, v21\n"
  "v_mfma_f32_16x16x32_bf16 v[144:147], v[24:27], v[84:87], 1.0\n"
  "v_fma_f32 v22, v22, s73, v224\n"
  "v_fma_f32 v23, v23, s73, v224\n"
  "v_mfma_f32_16x16x32_bf16 v[148:151], v[24:27], v[244:247], 1.0\n"
  "v_exp_f32 v22, v22\n"
  "v_mfma_f32_16x16x32_bf16 v[120:123], v[24:27], v[76:79], 1.0\n"
  "s_waitcnt lgkmcnt(0)\n"
  "v_fma_f32 v32, v32, s73, v225\n"
  "v_fma_f32 v33, v33, s73, v225\n"
  "v_mfma_f32_16x16x32_bf16 v[124:127], v[24:27], v[80:83], 1.0\n"
  "v_exp_f32 v32, v32\n"
  "v_mfma_f32_16x16x32_bf16 v[128:131], v[24:27], a[252:255], 1.0\n"
  "v_exp_f32 v33, v33\n"
  "v_fma_f32 v34, v34, s73, v225\n"
  "v_exp_f32 v34, v34\n"
  "v_fma_f32 v35, v35, s73, v225\n"
  "v_exp_f32 v35, v35\n"
  "v_mfma_f32_16x16x32_bf16 v[132:135], v[24:27], v[236:239], 1.0\n"
  "v_cvt_pk_bf16_f32 v40, v32, v33\n"
  "v_cvt_pk_bf16_f32 v41, v34, v35\n"
  "v_mfma_f32_16x16x32_bf16 v[32:35], a[232:235], a[68:71], v[248:251]\n"
  "v_fma_f32 v36, v36, s73, v225\n"
  "v_fma_f32 v37, v37, s73, v225\n"
  "v_mfma_f32_16x16x32_bf16 v[32:35], a[228:231], a[64:67], v[32:35]\n"
  "v_exp_f32 v36, v36\n"
  "v_mfma_f32_16x16x32_bf16 v[32:35], a[224:227], a[60:63], v[32:35]\n"
  "v_exp_f32 v37, v37\n"
  "v_mfma_f32_16x16x32_bf16 v[32:35], a[220:223], a[56:59], v[32:35]\n"
  "v_fma_f32 v38, v38, s73, v225\n"
  "v_fma_f32 v39, v39, s73, v225\n"
  "v_mfma_f32_16x16x32_bf16 v[32:35], a[216:219], a[52:55], v[32:35]\n"
  "v_exp_f32 v38, v38\n"
  "v_exp_f32 v39, v39\n"
  "v_mfma_f32_16x16x32_bf16 v[32:35], a[212:215], a[48:51], v[32:35]\n"
  "v_cvt_pk_bf16_f32 v42, v36, v37\n"
  "v_cvt_pk_bf16_f32 v43, v38, v39\n"
  "v_mfma_f32_16x16x32_bf16 v[36:39], a[120:123], a[48:51], v[248:251]\n"
  "s_waitcnt vmcnt(6)\n"
  "ds_read_b128 a[72:75], v222 offset:11200\n"
  "ds_read_b128 a[76:79], v222 offset:11264\n"
  "ds_read_b128 a[80:83], v222 offset:13440\n"
  "v_mfma_f32_16x16x32_bf16 v[36:39], a[124:127], a[52:55], v[36:39]\n"
  "ds_read_b128 a[84:87], v222 offset:13504\n"
  "ds_read_b128 a[88:91], v222 offset:15680\n"
  "ds_read_b128 a[92:95], v222 offset:15744\n"
  "v_mfma_f32_16x16x32_bf16 v[36:39], a[128:131], a[56:59], v[36:39]\n"
  "v_exp_f32 v23, v23\n"
  "v_mfma_f32_16x16x32_bf16 v[36:39], a[132:135], a[60:63], v[36:39]\n"
  "v_fma_f32 v32, v32, s73, v225\n"
  "v_fma_f32 v33, v33, s73, v225\n"
  "v_mfma_f32_16x16x32_bf16 v[36:39], a[136:139], a[64:67], v[36:39]\n"
  "v_exp_f32 v32, v32\n"
  "v_mfma_f32_16x16x32_bf16 v[36:39], a[140:143], a[68:71], v[36:39]\n"
  "s_waitcnt lgkmcnt(5)\n"
  "v_exp_f32 v33, v33\n"
  "v_mfma_f32_16x16x32_bf16 v[48:51], a[164:167], a[72:75], v[248:251]\n"
  "s_waitcnt lgkmcnt(4)\n"
  "v_fma_f32 v34, v34, s73, v225\n"
  "v_fma_f32 v35, v35, s73, v225\n"
  "v_mfma_f32_16x16x32_bf16 v[48:51], a[168:171], a[76:79], v[48:51]\n"
  "s_waitcnt lgkmcnt(3)\n"
  "v_exp_f32 v34, v34\n"
  "v_mfma_f32_16x16x32_bf16 v[48:51], a[172:175], a[80:83], v[48:51]\n"
  "s_waitcnt lgkmcnt(2)\n"
  "v_exp_f32 v35, v35\n"
  "v_mfma_f32_16x16x32_bf16 v[48:51], a[176:179], a[84:87], v[48:51]\n"
  "s_waitcnt lgkmcnt(1)\n"
  "v_fma_f32 v36, v36, s73, v225\n"
  "v_fma_f32 v37, v37, s73, v225\n"
  "v_mfma_f32_16x16x32_bf16 v[48:51], a[180:183], a[88:91], v[48:51]\n"
  "s_waitcnt lgkmcnt(0)\n"
  "v_exp_f32 v36, v36\n"
  "v_mfma_f32_16x16x32_bf16 v[48:51], a[184:187], a[92:95], v[48:51]\n"
  "v_exp_f32 v37, v37\n"
  "v_mfma_f32_16x16x32_bf16 v[52:55], a[188:191], a[72:75], v[248:251]\n"
  "v_fma_f32 v38, v38, s73, v225\n"
  "v_fma_f32 v39, v39, s73, v225\n"
  "v_mfma_f32_16x16x32_bf16 v[52:55], a[192:195], a[76:79], v[52:55]\n"
  "v_exp_f32 v38, v38\n"
  "v_mfma_f32_16x16x32_bf16 v[52:55], a[196:199], a[80:83], v[52:55]\n"
  "v_max3_f32 v226, v48, v49, v50\n"
  "v_max_f32 v226, v226, v51\n"
  "v_mfma_f32_16x16x32_bf16 v[52:55], a[200:203], a[84:87], v[52:55]\n"
  "v_mul_f32 v226, s74, v226\n"
  "v_add_f32 v226, s64, v226\n"
  "v_mfma_f32_16x16x32_bf16 v[52:55], a[204:207], a[88:91], v[52:55]\n"
  "ds_bpermute_b32 v226, v234, v226\n"
  "v_mfma_f32_16x16x32_bf16 v[52:55], a[208:211], a[92:95], v[52:55]\n"
  "v_exp_f32 v39, v39\n"
  "v_mfma_f32_16x16x32_bf16 v[168:171], v[40:43], a[144:147], 1.0\n"
  "ds_write_b64 v231, v[64:65] offset:2048\n"
  "v_mfma_f32_16x16x32_bf16 v[172:175], v[40:43], a[148:151], 1.0\n"
  "v_cvt_pk_bf16_f32 v12, v0, v1\n"
  "v_cvt_pk_bf16_f32 v13, v2, v3\n"
  "v_mfma_f32_16x16x32_bf16 v[176:179], v[40:43], v[84:87], 1.0\n"
  "v_cvt_pk_bf16_f32 v14, v4, v5\n"
  "v_cvt_pk_bf16_f32 v15, v6, v7\n"
  "v_mfma_f32_16x16x32_bf16 v[180:183], v[40:43], v[244:247], 1.0\n"
  "v_cvt_pk_bf16_f32 v28, v16, v17\n"
  "v_cvt_pk_bf16_f32 v29, v18, v19\n"
  "v_mfma_f32_16x16x32_bf16 v[152:155], v[40:43], v[76:79], 1.0\n"
  "s_waitcnt lgkmcnt(1)\n"
  "v_fma_f32 v48, v48, s73, v226\n"
  "v_fma_f32 v49, v49, s73, v226\n"
  "v_mfma_f32_16x16x32_bf16 v[156:159], v[40:43], v[80:83], 1.0\n"
  "v_exp_f32 v48, v48\n"
  "v_mfma_f32_16x16x32_bf16 v[160:163], v[40:43], a[252:255], 1.0\n"
  "v_exp_f32 v49, v49\n"
  "v_fma_f32 v50, v50, s73, v226\n"
  "v_exp_f32 v50, v50\n"
  "v_fma_f32 v51, v51, s73, v226\n"
  "v_exp_f32 v51, v51\n"
  "v_mfma_f32_16x16x32_bf16 v[164:167], v[40:43], v[236:239], 1.0\n"
  "v_cvt_pk_bf16_f32 v56, v48, v49\n"
  "v_cvt_pk_bf16_f32 v57, v50, v51\n"
  "v_mfma_f32_16x16x32_bf16 v[48:51], a[212:215], a[72:75], v[248:251]\n"
  "v_fma_f32 v52, v52, s73, v226\n"
  "v_fma_f32 v53, v53, s73, v226\n"
  "v_mfma_f32_16x16x32_bf16 v[48:51], a[216:219], a[76:79], v[48:51]\n"
  "v_exp_f32 v52, v52\n"
  "v_mfma_f32_16x16x32_bf16 v[48:51], a[220:223], a[80:83], v[48:51]\n"
  "v_exp_f32 v53, v53\n"
  "v_mfma_f32_16x16x32_bf16 v[48:51], a[224:227], a[84:87], v[48:51]\n"
  "v_fma_f32 v54, v54, s73, v226\n"
  "v_fma_f32 v55, v55, s73, v226\n"
  "v_mfma_f32_16x16x32_bf16 v[48:51], a[228:231], a[88:91], v[48:51]\n"
  "v_exp_f32 v54, v54\n"
  "v_exp_f32_e64 v55, v55\n"
  "v_mfma_f32_16x16x32_bf16 v[48:51], a[232:235], a[92:95], v[48:51]\n"
  "v_cvt_pk_bf16_f32 v58, v52, v53\n"
  "v_cvt_pk_bf16_f32 v59, v54, v55\n"
  "v_mfma_f32_16x16x32_bf16 v[52:55], a[140:143], a[92:95], v[248:251]\n"
  "v_cvt_pk_bf16_f32 v30, v20, v21\n"
  "v_cvt_pk_bf16_f32 v31, v22, v23\n"
  "v_mfma_f32_16x16x32_bf16 v[52:55], a[136:139], a[88:91], v[52:55]\n"
  "v_cvt_pk_bf16_f32 v44, v32, v33\n"
  "v_cvt_pk_bf16_f32 v45, v34, v35\n"
  "v_mfma_f32_16x16x32_bf16 v[52:55], a[132:135], a[84:87], v[52:55]\n"
  "v_fma_f32 v48, v48, s73, v226\n"
  "v_fma_f32 v49, v49, s73, v226\n"
  "v_mfma_f32_16x16x32_bf16 v[52:55], a[128:131], a[80:83], v[52:55]\n"
  "v_exp_f32 v48, v48\n"
  "v_mfma_f32_16x16x32_bf16 v[52:55], a[124:127], a[76:79], v[52:55]\n"
  "v_exp_f32 v49, v49\n"
  "v_mfma_f32_16x16x32_bf16 v[52:55], a[120:123], a[72:75], v[52:55]\n"
  "v_fma_f32 v50, v50, s73, v226\n"
  "v_fma_f32 v51, v51, s73, v226\n"
  "v_mfma_f32_16x16x32_bf16 v[200:203], v[56:59], a[144:147], 1.0\n"
  "v_exp_f32 v50, v50\n"
  "v_mfma_f32_16x16x32_bf16 v[204:207], v[56:59], a[148:151], 1.0\n"
  "v_exp_f32 v51, v51\n"
  "v_mfma_f32_16x16x32_bf16 v[208:211], v[56:59], v[84:87], 1.0\n"
  "v_fma_f32 v52, v52, s73, v226\n"
  "v_fma_f32 v53, v53, s73, v226\n"
  "v_mfma_f32_16x16x32_bf16 v[212:215], v[56:59], v[244:247], 1.0\n"
  "v_exp_f32 v52, v52\n"
  "v_mfma_f32_16x16x32_bf16 v[184:187], v[56:59], v[76:79], 1.0\n"
  "v_exp_f32 v53, v53\n"
  "v_mfma_f32_16x16x32_bf16 v[188:191], v[56:59], v[80:83], 1.0\n"
  "v_fma_f32 v54, v54, s73, v226\n"
  "v_fma_f32 v55, v55, s73, v226\n"
  "v_mfma_f32_16x16x32_bf16 v[192:195], v[56:59], a[252:255], 1.0\n"
  "v_exp_f32_e64 v54, v54\n"
  "v_mfma_f32_16x16x32_bf16 v[196:199], v[56:59], v[236:239], 1.0\n"
  "v_exp_f32_e64 v55, v55\n"
  "v_cvt_pk_bf16_f32 v46, v36, v37\n"
  "v_cvt_pk_bf16_f32 v47, v38, v39\n"
  "v_cvt_pk_bf16_f32 v60, v48, v49\n"
  "v_cvt_pk_bf16_f32 v61, v50, v51\n"
  "v_cvt_pk_bf16_f32 v62, v52, v53\n"
  "v_cvt_pk_bf16_f32 v63, v54, v55\n"
  "s_add_i32 s76, s76, 1\n"
  "s_mov_b32 m0, s67\n"
  "s_waitcnt vmcnt(0) lgkmcnt(0)\n"
  "s_barrier\n"
  "ds_read_b128 a[124:127], v216 offset:0\n"
  "ds_read_b64 a[160:161], v231 offset:2048\n"
  "ds_read_b64 a[162:163], v232 offset:2048\n"
  "buffer_load_dwordx4 v221, s[40:43], 0 offen offset:0 lds\n"
  "ds_read_b128 a[120:123], v216 offset:64\n"
  "s_addk_i32 m0, 896\n"
  "ds_read_b128 a[140:143], v216 offset:1024\n"
  "ds_read_b128 a[136:139], v216 offset:1088\n"
  "buffer_load_dwordx4 v221, s[40:43], 0 offen offset:128 lds\n"
  "s_addk_i32 m0, 896\n"
  "ds_read_b128 a[144:147], v216 offset:2048\n"
  "ds_read_b128 a[148:151], v216 offset:2112\n"
  "buffer_load_dwordx4 v221, s[40:43], 0 offen offset:256 lds\n"
  "s_waitcnt lgkmcnt(4)\n"
  "v_mfma_f32_16x16x32_bf16 v[64:67], a[124:127], a[96:99], v[248:251]\n"
  "s_add_u32 m0, s59, -128\n"
  "v_mfma_f32_16x16x32_bf16 v[64:67], a[120:123], a[100:103], v[64:67]\n"
  "s_waitcnt lgkmcnt(2)\n"
  "v_mfma_f32_16x16x32_bf16 v[64:67], a[140:143], a[104:107], v[64:67]\n"
  "buffer_load_dwordx4 v221, s[32:35], 0 offen offset:128 lds\n"
  "v_mfma_f32_16x16x32_bf16 v[64:67], a[136:139], a[108:111], v[64:67]\n"
  "s_waitcnt lgkmcnt(0)\n"
  "v_mfma_f32_16x16x32_bf16 v[64:67], a[144:147], a[112:115], v[64:67]\n"
  "s_addk_i32 m0, 1024\n"
  "v_mfma_f32_16x16x32_bf16 v[64:67], a[148:151], a[116:119], v[64:67]\n"
  "s_mov_b64 s[32:33], s[44:45]\n"
  "v_mfma_f32_16x16x32_bf16 v[0:3], a[148:151], a[20:23], v[248:251]\n"
  "buffer_load_dwordx4 v221, s[44:47], 0 offen offset:128 lds\n"
  "v_mfma_f32_16x16x32_bf16 v[0:3], a[144:147], a[16:19], v[0:3]\n"
  "s_addk_i32 m0, 896\n"
  "v_mfma_f32_16x16x32_bf16 v[0:3], a[136:139], a[12:15], v[0:3]\n"
  "s_add_u32 s40, s40, s31\n"
  "v_mfma_f32_16x16x32_bf16 v[0:3], a[140:143], a[8:11], v[0:3]\n"
  "s_addc_u32 s41, s41, 0\n"
  "v_mfma_f32_16x16x32_bf16 v[0:3], a[120:123], a[4:7], v[0:3]\n"
  "buffer_load_dwordx4 v221, s[44:47], 0 offen offset:256 lds\n"
  "v_mfma_f32_16x16x32_bf16 v[0:3], a[124:127], a[0:3], v[0:3]\n"
  "s_add_u32 s44, s44, s31\n"
  "v_mfma_f32_16x16x32_bf16 v[16:19], a[124:127], a[24:27], v[248:251]\n"
  "s_addc_u32 s45, s45, 0\n"
  "v_mfma_f32_16x16x32_bf16 v[16:19], a[120:123], a[28:31], v[16:19]\n"
  "v_mfma_f32_16x16x32_bf16 v[16:19], a[140:143], a[32:35], v[16:19]\n"
  "ds_read_b128 a[132:135], v235 offset:0\n"
  "v_mfma_f32_16x16x32_bf16 v[16:19], a[136:139], a[36:39], v[16:19]\n"
  "ds_read_b128 a[128:131], v235 offset:64\n"
  "v_mfma_f32_16x16x32_bf16 v[16:19], a[144:147], a[40:43], v[16:19]\n"
  "v_mfma_f32_16x16x32_bf16 v[16:19], a[148:151], a[44:47], v[16:19]\n"
  "v_mfma_f32_16x16x32_bf16 v[32:35], a[148:151], a[68:71], v[248:251]\n"
  "v_mfma_f32_16x16x32_bf16 v[32:35], a[144:147], a[64:67], v[32:35]\n"
  "v_mfma_f32_16x16x32_bf16 v[32:35], a[136:139], a[60:63], v[32:35]\n"
  "v_mfma_f32_16x16x32_bf16 v[32:35], a[140:143], a[56:59], v[32:35]\n"
  "v_mfma_f32_16x16x32_bf16 v[32:35], a[120:123], a[52:55], v[32:35]\n"
  "v_mfma_f32_16x16x32_bf16 v[32:35], a[124:127], a[48:51], v[32:35]\n"
  "v_mfma_f32_16x16x32_bf16 v[48:51], a[124:127], a[72:75], v[248:251]\n"
  "ds_read_b128 a[124:127], v235 offset:1024\n"
  "v_mfma_f32_16x16x32_bf16 v[48:51], a[120:123], a[76:79], v[48:51]\n"
  "ds_read_b128 a[120:123], v235 offset:1088\n"
  "v_mfma_f32_16x16x32_bf16 v[48:51], a[140:143], a[80:83], v[48:51]\n"
  "ds_read_b128 a[140:143], v235 offset:2048\n"
  "v_mfma_f32_16x16x32_bf16 v[48:51], a[136:139], a[84:87], v[48:51]\n"
  "ds_read_b128 a[136:139], v235 offset:2112\n"
  "v_mfma_f32_16x16x32_bf16 v[48:51], a[144:147], a[88:91], v[48:51]\n"
  "v_mfma_f32_16x16x32_bf16 v[48:51], a[148:151], a[92:95], v[48:51]\n"
  "s_waitcnt lgkmcnt(4)\n"
  "v_mfma_f32_16x16x32_bf16 v[4:7], a[132:135], a[0:3], v[248:251]\n"
  "v_mfma_f32_16x16x32_bf16 v[4:7], a[128:131], a[4:7], v[4:7]\n"
  "s_waitcnt lgkmcnt(2)\n"
  "v_mfma_f32_16x16x32_bf16 v[4:7], a[124:127], a[8:11], v[4:7]\n"
  "v_mfma_f32_16x16x32_bf16 v[4:7], a[120:123], a[12:15], v[4:7]\n"
  "s_waitcnt lgkmcnt(0)\n"
  "v_mfma_f32_16x16x32_bf16 v[4:7], a[140:143], a[16:19], v[4:7]\n"
  "v_mfma_f32_16x16x32_bf16 v[4:7], a[136:139], a[20:23], v[4:7]\n"
  "v_mfma_f32_16x16x32_bf16 v[20:23], a[136:139], a[44:47], v[248:251]\n"
  "v_mfma_f32_16x16x32_bf16 v[20:23], a[140:143], a[40:43], v[20:23]\n"
  "v_mfma_f32_16x16x32_bf16 v[20:23], a[120:123], a[36:39], v[20:23]\n"
  "ds_read_b64_tr_b16 a[144:145], v217 offset:2048\n"
  "v_mfma_f32_16x16x32_bf16 v[20:23], a[124:127], a[32:35], v[20:23]\n"
  "ds_read_b64_tr_b16 a[146:147], v218 offset:2048\n"
  "v_mfma_f32_16x16x32_bf16 v[20:23], a[128:131], a[28:31], v[20:23]\n"
  "ds_read_b64_tr_b16 a[148:149], v217 offset:2064\n"
  "v_mfma_f32_16x16x32_bf16 v[20:23], a[132:135], a[24:27], v[20:23]\n"
  "ds_read_b64_tr_b16 a[150:151], v218 offset:2064\n"
  "v_mfma_f32_16x16x32_bf16 v[36:39], a[132:135], a[48:51], v[248:251]\n"
  "v_mfma_f32_16x16x32_bf16 v[36:39], a[128:131], a[52:55], v[36:39]\n"
  "v_mfma_f32_16x16x32_bf16 v[36:39], a[124:127], a[56:59], v[36:39]\n"
  "v_mfma_f32_16x16x32_bf16 v[36:39], a[120:123], a[60:63], v[36:39]\n"
  "v_mfma_f32_16x16x32_bf16 v[36:39], a[140:143], a[64:67], v[36:39]\n"
  "v_mfma_f32_16x16x32_bf16 v[36:39], a[136:139], a[68:71], v[36:39]\n"
  "v_mfma_f32_16x16x32_bf16 v[52:55], a[136:139], a[92:95], v[248:251]\n"
  "ds_read_b64_tr_b16 a[136:137], v219 offset:2048\n"
  "v_mfma_f32_16x16x32_bf16 v[52:55], a[140:143], a[88:91], v[52:55]\n"
  "ds_read_b64_tr_b16 a[138:139], v220 offset:2048\n"
  "v_mfma_f32_16x16x32_bf16 v[52:55], a[120:123], a[84:87], v[52:55]\n"
  "ds_read_b64_tr_b16 a[140:141], v219 offset:2064\n"
  "v_mfma_f32_16x16x32_bf16 v[52:55], a[124:127], a[80:83], v[52:55]\n"
  "ds_read_b64_tr_b16 a[142:143], v220 offset:2064\n"
  "v_fma_f32 v64, v64, s73, v227\n"
  "v_mfma_f32_16x16x32_bf16 v[52:55], a[128:131], a[76:79], v[52:55]\n"
  "v_exp_f32 v64, v64\n"
  "v_mfma_f32_16x16x32_bf16 v[52:55], a[132:135], a[72:75], v[52:55]\n"
  "s_waitcnt lgkmcnt(4)\n"
  "v_fma_f32 v65, v65, s73, v227\n"
  "v_fma_f32 v66, v66, s73, v227\n"
  "v_mfma_f32_16x16x32_bf16 v[104:107], v[12:15], a[144:147], v[104:107]\n"
  "v_exp_f32 v65, v65\n"
  "v_mfma_f32_16x16x32_bf16 v[108:111], v[12:15], a[148:151], v[108:111]\n"
  "v_mfma_f32_16x16x32_bf16 v[136:139], v[28:31], a[144:147], v[136:139]\n"
  "ds_read_b64_tr_b16 a[120:121], v217 offset:0\n"
  "v_mfma_f32_16x16x32_bf16 v[140:143], v[28:31], a[148:151], v[140:143]\n"
  "ds_read_b64_tr_b16 a[122:123], v218 offset:0\n"
  "v_mfma_f32_16x16x32_bf16 v[168:171], v[44:47], a[144:147], v[168:171]\n"
  "ds_read_b64_tr_b16 a[124:125], v217 offset:1024\n"
  "v_mfma_f32_16x16x32_bf16 v[172:175], v[44:47], a[148:151], v[172:175]\n"
  "ds_read_b64_tr_b16 a[126:127], v218 offset:1024\n"
  "v_mfma_f32_16x16x32_bf16 v[200:203], v[60:63], a[144:147], v[200:203]\n"
  "v_mfma_f32_16x16x32_bf16 v[204:207], v[60:63], a[148:151], v[204:207]\n"
  "v_exp_f32_e64 v66, v66\n"
  "v_mfma_f32_16x16x32_bf16 a[244:247], a[160:163], a[144:147], a[244:247]\n"
  "v_mfma_f32_16x16x32_bf16 a[248:251], a[160:163], a[148:151], a[248:251]\n"
  "s_waitcnt lgkmcnt(4)\n"
  "v_mfma_f32_16x16x32_bf16 v[208:211], v[60:63], a[136:139], v[208:211]\n"
  "v_mfma_f32_16x16x32_bf16 v[212:215], v[60:63], a[140:143], v[212:215]\n"
  "v_mfma_f32_16x16x32_bf16 v[176:179], v[44:47], a[136:139], v[176:179]\n"
  "ds_read_b64_tr_b16 a[128:129], v217 offset:16\n"
  "v_mfma_f32_16x16x32_bf16 v[180:183], v[44:47], a[140:143], v[180:183]\n"
  "ds_read_b64_tr_b16 a[130:131], v218 offset:16\n"
  "v_mfma_f32_16x16x32_bf16 v[144:147], v[28:31], a[136:139], v[144:147]\n"
  "ds_read_b64_tr_b16 a[132:133], v217 offset:1040\n"
  "v_mfma_f32_16x16x32_bf16 v[148:151], v[28:31], a[140:143], v[148:151]\n"
  "ds_read_b64_tr_b16 a[134:135], v218 offset:1040\n"
  "v_mfma_f32_16x16x32_bf16 v[112:115], v[12:15], a[136:139], v[112:115]\n"
  "v_mfma_f32_16x16x32_bf16 v[116:119], v[12:15], a[140:143], v[116:119]\n"
  "s_waitcnt lgkmcnt(4)\n"
  "v_mfma_f32_16x16x32_bf16 a[164:167], v[8:11], a[120:123], 1.0\n"
  "v_fma_f32 v67, v67, s73, v227\n"
  "v_mfma_f32_16x16x32_bf16 a[164:167], v[12:15], a[124:127], a[164:167]\n"
  "v_exp_f32 v67, v67\n"
  "v_mfma_f32_16x16x32_bf16 a[180:183], v[24:27], a[120:123], 1.0\n"
  "v_mfma_f32_16x16x32_bf16 a[180:183], v[28:31], a[124:127], a[180:183]\n"
  "v_cvt_pk_bf16_f32 v64, v64, v65\n"
  "v_cvt_pk_bf16_f32 v65, v66, v67\n"
  "v_mfma_f32_16x16x32_bf16 a[196:199], v[40:43], a[120:123], 1.0\n"
  "s_mov_b32 m0, s68\n"
  "ds_read_b64_tr_b16 a[136:137], v219 offset:0\n"
  "v_fma_f32 v0, v0, s73, v223\n"
  "v_mfma_f32_16x16x32_bf16 a[196:199], v[44:47], a[124:127], a[196:199]\n"
  "ds_read_b64_tr_b16 a[138:139], v220 offset:0\n"
  "v_fma_f32 v1, v1, s73, v223\n"
  "v_mfma_f32_16x16x32_bf16 a[212:215], v[56:59], a[120:123], 1.0\n"
  "ds_read_b64_tr_b16 a[140:141], v219 offset:1024\n"
  "v_fma_f32 v2, v2, s73, v223\n"
  "v_mfma_f32_16x16x32_bf16 a[212:215], v[60:63], a[124:127], a[212:215]\n"
  "ds_read_b64_tr_b16 a[142:143], v220 offset:1024\n"
  "v_fma_f32 v3, v3, s73, v223\n"
  "v_mfma_f32_16x16x32_bf16 a[228:231], a[156:159], a[120:123], 1.0\n"
  "buffer_load_dwordx4 v221, s[40:43], 0 offen offset:0 lds\n"
  "v_mfma_f32_16x16x32_bf16 a[228:231], a[160:163], a[124:127], a[228:231]\n"
  "s_waitcnt lgkmcnt(4)\n"
  "v_mfma_f32_16x16x32_bf16 a[232:235], a[156:159], a[128:131], 1.0\n"
  "s_addk_i32 m0, 896\n"
  "v_mfma_f32_16x16x32_bf16 a[232:235], a[160:163], a[132:135], a[232:235]\n"
  "ds_write_b64 v231, v[64:65] offset:0\n"
  "v_mfma_f32_16x16x32_bf16 a[216:219], v[56:59], a[128:131], 1.0\n"
  "buffer_load_dwordx4 v221, s[40:43], 0 offen offset:128 lds\n"
  "v_fma_f32 v16, v16, s73, v224\n"
  "v_mfma_f32_16x16x32_bf16 a[216:219], v[60:63], a[132:135], a[216:219]\n"
  "s_addk_i32 m0, 896\n"
  "ds_read_b64_tr_b16 a[120:121], v219 offset:16\n"
  "v_fma_f32 v17, v17, s73, v224\n"
  "v_mfma_f32_16x16x32_bf16 a[200:203], v[40:43], a[128:131], 1.0\n"
  "ds_read_b64_tr_b16 a[122:123], v220 offset:16\n"
  "v_fma_f32 v18, v18, s73, v224\n"
  "v_mfma_f32_16x16x32_bf16 a[200:203], v[44:47], a[132:135], a[200:203]\n"
  "s_nop 0\n"
  "ds_read_b64_tr_b16 a[124:125], v219 offset:1040\n"
  "v_fma_f32 v19, v19, s73, v224\n"
  "v_mfma_f32_16x16x32_bf16 a[184:187], v[24:27], a[128:131], 1.0\n"
  "buffer_load_dwordx4 v221, s[40:43], 0 offen offset:256 lds\n"
  "v_fma_f32 v32, v32, s73, v225\n"
  "v_mfma_f32_16x16x32_bf16 a[184:187], v[28:31], a[132:135], a[184:187]\n"
  "s_mov_b32 m0, s60\n"
  "ds_read_b64_tr_b16 a[126:127], v220 offset:1040\n"
  "v_fma_f32 v33, v33, s73, v225\n"
  "v_mfma_f32_16x16x32_bf16 a[168:171], v[8:11], a[128:131], 1.0\n"
  "s_add_u32 s28, s96, 184448\n"
  "v_exp_f32 v0, v0\n"
  "v_mfma_f32_16x16x32_bf16 a[168:171], v[12:15], a[132:135], a[168:171]\n"
  "s_waitcnt lgkmcnt(5)\n"
  "v_exp_f32 v1, v1\n"
  "v_mfma_f32_16x16x32_bf16 a[172:175], v[8:11], a[136:139], 1.0\n"
  "buffer_load_dwordx4 v221, s[32:35], 0 offen offset:0 lds\n"
  "v_add_f32 v252, v0, v1\n"
  "v_mfma_f32_16x16x32_bf16 a[172:175], v[12:15], a[140:143], a[172:175]\n"
  "s_addk_i32 m0, 1024\n"
  "v_exp_f32 v2, v2\n"
  "v_mfma_f32_16x16x32_bf16 a[188:191], v[24:27], a[136:139], 1.0\n"
  "s_add_u32 s40, s40, s31\n"
  "v_exp_f32 v3, v3\n"
  "v_mfma_f32_16x16x32_bf16 a[188:191], v[28:31], a[140:143], a[188:191]\n"
  "s_addc_u32 s41, s41, 0\n"
  "v_exp_f32 v16, v16\n"
  "v_mfma_f32_16x16x32_bf16 a[204:207], v[40:43], a[136:139], 1.0\n"
  "buffer_load_dwordx4 v221, s[44:47], 0 offen offset:0 lds\n"
  "v_add_f32 v252, v252, v2\n"
  "v_mfma_f32_16x16x32_bf16 a[204:207], v[44:47], a[140:143], a[204:207]\n"
  "s_addk_i32 m0, 768\n"
  "v_exp_f32 v17, v17\n"
  "v_mfma_f32_16x16x32_bf16 a[220:223], v[56:59], a[136:139], 1.0\n"
  "s_mov_b64 s[32:33], s[44:45]\n"
  "v_exp_f32 v18, v18\n"
  "v_mfma_f32_16x16x32_bf16 a[220:223], v[60:63], a[140:143], a[220:223]\n"
  "s_waitcnt lgkmcnt(0)\n"
  "v_exp_f32 v19, v19\n"
  "v_mfma_f32_16x16x32_bf16 a[224:227], v[56:59], a[120:123], 1.0\n"
  "buffer_load_dwordx4 v221, s[44:47], 0 offen offset:256 lds\n"
  "v_add_f32_e64 v252, v252, v3\n"
  "v_mfma_f32_16x16x32_bf16 a[224:227], v[60:63], a[124:127], a[224:227]\n"
  "s_add_u32 s44, s44, s31\n"
  "v_exp_f32 v32, v32\n"
  "v_mfma_f32_16x16x32_bf16 a[208:211], v[40:43], a[120:123], 1.0\n"
  "s_addc_u32 s45, s45, 0\n"
  "v_exp_f32 v33, v33\n"
  "v_mfma_f32_16x16x32_bf16 a[208:211], v[44:47], a[124:127], a[208:211]\n"
  "s_cmp_eq_u32 s95, 0\n"
  "s_cselect_b32 s95, 3, s95\n"
  "s_subb_u32 s100, s100, -11\n"
  "v_add_f32 v253, v16, v17\n"
  "v_add_f32 v253, v253, v18\n"
  "v_mfma_f32_16x16x32_bf16 a[192:195], v[24:27], a[120:123], 1.0\n"
  "s_sub_u32 s95, s95, 1\n"
  "v_add_f32 v253, v253, v19\n"
  "v_fma_f32 v34, v34, s73, v225\n"
  "v_mfma_f32_16x16x32_bf16 a[192:195], v[28:31], a[124:127], a[192:195]\n"
  "s_waitcnt vmcnt(6)\n"
  "s_barrier\n"
  "ds_read_b128 a[128:131], v216 offset:12352\n"
  "v_mfma_f32_16x16x32_bf16 a[176:179], v[8:11], a[120:123], 1.0\n"
  "s_lshr_b32 s79, s100, 0\n"
  "ds_read_b128 a[132:135], v216 offset:12416\n"
  "v_fma_f32 v35, v35, s73, v225\n"
  "v_mfma_f32_16x16x32_bf16 a[176:179], v[12:15], a[124:127], a[176:179]\n"
  "s_mul_i32 s79, s79, 7\n"
  "ds_read_b128 a[120:123], v216 offset:13376\n"
  "v_add_f32 v254, v32, v33\n"
  "v_mfma_f32_16x16x32_bf16 v[68:71], v[8:11], a[152:155], 0\n"
  "s_add_u32 s79, s79, s100\n"
  "ds_read_b128 a[124:127], v216 offset:13440\n"
  "v_cvt_pk_bf16_f32 v8, v0, v1\n"
  "v_mfma_f32_16x16x32_bf16 v[68:71], v[12:15], a[152:155], v[68:71]\n"
  "s_add_u32 s79, s79, s101\n"
  "ds_read_b128 a[136:139], v216 offset:14400\n"
  "v_cvt_pk_bf16_f32 v9, v2, v3\n"
  "v_mfma_f32_16x16x32_bf16 v[72:75], v[24:27], a[152:155], 0\n"
  "ds_read_b128 a[140:143], v216 offset:14464\n"
  "v_cvt_pk_bf16_f32 v24, v16, v17\n"
  "v_mfma_f32_16x16x32_bf16 v[72:75], v[28:31], a[152:155], v[72:75]\n"
  "s_mov_b32 m0, s66\n"
  "v_exp_f32 v34, v34\n"
  "v_mfma_f32_16x16x32_bf16 v[76:79], v[40:43], a[152:155], 0\n"
  "s_min_u32 s79, s79, 0x1ff\n"
  "v_exp_f32 v35, v35\n"
  "v_mfma_f32_16x16x32_bf16 v[76:79], v[44:47], a[152:155], v[76:79]\n"
  "s_mul_i32 s96, s79, 196608\n"
  "v_cvt_pk_bf16_f32 v25, v18, v19\n"
  "v_add_f32 v254, v254, v34\n"
  "v_mfma_f32_16x16x32_bf16 v[80:83], v[56:59], a[152:155], 0\n"
  "s_mul_i32 s92, s79, 325632\n"
  "v_add_f32 v254, v254, v35\n"
  "v_cvt_pk_bf16_f32 v40, v32, v33\n"
  "v_mfma_f32_16x16x32_bf16 v[80:83], v[60:63], a[152:155], v[80:83]\n"
  "buffer_load_dwordx4 v221, s[40:43], 0 offen offset:0 lds\n"
  "v_mfma_f32_16x16x32_bf16 v[84:87], a[156:159], a[152:155], 0\n"
  "s_add_u32 s96, s96, s39\n"
  "ds_read_b64 a[156:157], v231 offset:0\n"
  "v_cvt_pk_bf16_f32 v41, v34, v35\n"
  "v_mfma_f32_16x16x32_bf16 v[84:87], a[160:163], a[152:155], v[84:87]\n"
  "s_branch L_CUT_%=\n"
  ".p2align 5\n"
  "L_BIG_%=:\n"
  "v_mfma_f32_16x16x32_bf16 v[16:19], a[120:123], a[24:27], v[248:251]\n"
  "s_mov_b32 m0, s60\n"
  "v_fma_f32 v64, v64, s73, v227\n"
  "v_fma_f32 v65, v65, s73, v227\n"
  "v_mfma_f32_16x16x32_bf16 v[16:19], a[124:127], a[28:31], v[16:19]\n"
  "buffer_load_dwordx4 v221, s[32:35], 0 offen offset:0 lds\n"
  "v_fma_f32 v66, v66, s73, v227\n"
  "v_mfma_f32_16x16x32_bf16 v[16:19], a[128:131], a[32:35], v[16:19]\n"
  "s_addk_i32 m0, 1024\n"
  "ds_read_b64_tr_b16 a[144:145], v217 offset:0\n"
  "v_fma_f32 v67, v67, s73, v227\n"
  "v_mfma_f32_16x16x32_bf16 v[16:19], a[132:135], a[36:39], v[16:19]\n"
  "s_mov_b64 s[36:37], s[44:45]\n"
  "ds_read_b64_tr_b16 a[146:147], v218 offset:0\n"
  "v_fma_f32 v0, v0, s73, v223\n"
  "v_mfma_f32_16x16x32_bf16 v[16:19], a[136:139], a[40:43], v[16:19]\n"
  "ds_read_b64_tr_b16 a[148:149], v217 offset:1024\n"
  "v_fma_f32 v1, v1, s73, v223\n"
  "v_mfma_f32_16x16x32_bf16 v[16:19], a[140:143], a[44:47], v[16:19]\n"
  "buffer_load_dwordx4 v221, s[44:47], 0 offen offset:0 lds\n"
  "v_fma_f32 v2, v2, s73, v223\n"
  "v_mfma_f32_16x16x32_bf16 v[32:35], a[140:143], a[68:71], v[248:251]\n"
  "s_addk_i32 m0, 768\n"
  "ds_read_b64_tr_b16 a[150:151], v218 offset:1024\n"
  "v_fma_f32 v3, v3, s73, v223\n"
  "v_mfma_f32_16x16x32_bf16 v[32:35], a[136:139], a[64:67], v[32:35]\n"
  "s_add_u32 s32, s88, s28\n"
  "v_exp_f32 v64, v64\n"
  "v_mfma_f32_16x16x32_bf16 v[32:35], a[132:135], a[60:63], v[32:35]\n"
  "s_addc_u32 s33, s89, 0\n"
  "v_exp_f32 v65, v65\n"
  "v_mfma_f32_16x16x32_bf16 v[32:35], a[128:131], a[56:59], v[32:35]\n"
  "buffer_load_dwordx4 v221, s[44:47], 0 offen offset:256 lds\n"
  "v_cvt_pk_bf16_f32 v64, v64, v65\n"
  "v_mfma_f32_16x16x32_bf16 v[32:35], a[124:127], a[52:55], v[32:35]\n"
  "s_add_u32 s44, s32, s31\n"
  "v_exp_f32 v66, v66\n"
  "v_mfma_f32_16x16x32_bf16 v[32:35], a[120:123], a[48:51], v[32:35]\n"
  "s_addc_u32 s45, s33, 0\n"
  "v_exp_f32 v67, v67\n"
  "v_mfma_f32_16x16x32_bf16 v[48:51], a[120:123], a[72:75], v[248:251]\n"
  "s_mov_b32 s46, s30\n"
  "v_exp_f32 v0, v0\n"
  "v_mfma_f32_16x16x32_bf16 v[48:51], a[124:127], a[76:79], v[48:51]\n"
  "v_exp_f32 v1, v1\n"
  "v_mfma_f32_16x16x32_bf16 v[48:51], a[128:131], a[80:83], v[48:51]\n"
  "ds_read_b64_tr_b16 a[120:121], v217 offset:16\n"
  "v_cvt_pk_bf16_f32 v65, v66, v67\n"
  "v_mfma_f32_16x16x32_bf16 v[48:51], a[132:135], a[84:87], v[48:51]\n"
  "ds_read_b64_tr_b16 a[122:123], v218 offset:16\n"
  "v_add_f32 v252, v252, v0\n"
  "v_mfma_f32_16x16x32_bf16 v[48:51], a[136:139], a[88:91], v[48:51]\n"
  "ds_read_b64_tr_b16 a[124:125], v217 offset:1040\n"
  "v_add_f32 v252, v252, v1\n"
  "v_mfma_f32_16x16x32_bf16 v[48:51], a[140:143], a[92:95], v[48:51]\n"
  "s_waitcnt lgkmcnt(3)\n"
  "ds_read_b64_tr_b16 a[126:127], v218 offset:1040\n"
  "v_fma_f32 v16, v16, s73, v224\n"
  "v_mfma_f32_16x16x32_bf16 a[228:231], a[156:159], a[144:147], a[228:231]\n"
  "v_exp_f32 v2, v2\n"
  "v_mfma_f32_16x16x32_bf16 a[228:231], a[160:163], a[148:151], a[228:231]\n"
  "v_exp_f32 v3, v3\n"
  "v_mfma_f32_16x16x32_bf16 a[212:215], v[56:59], a[144:147], a[212:215]\n"
  "v_exp_f32 v16, v16\n"
  "v_mfma_f32_16x16x32_bf16 a[212:215], v[60:63], a[148:151], a[212:215]\n"
  "v_add_f32 v252, v252, v2\n"
  "v_add_f32 v252, v252, v3\n"
  "v_mfma_f32_16x16x32_bf16 a[196:199], v[40:43], a[144:147], a[196:199]\n"
  "v_fma_f32 v17, v17, s73, v224\n"
  "v_fma_f32 v18, v18, s73, v224\n"
  "v_mfma_f32_16x16x32_bf16 a[196:199], v[44:47], a[148:151], a[196:199]\n"
  "v_exp_f32 v17, v17\n"
  "v_mfma_f32_16x16x32_bf16 a[180:183], v[24:27], a[144:147], a[180:183]\n"
  "ds_read_b64_tr_b16 a[128:129], v219 offset:0\n"
  "v_fma_f32 v19, v19, s73, v224\n"
  "v_mfma_f32_16x16x32_bf16 a[180:183], v[28:31], a[148:151], a[180:183]\n"
  "s_waitcnt vmcnt(14)\n"
  "ds_read_b64_tr_b16 a[130:131], v220 offset:0\n"
  "v_add_f32 v253, v253, v16\n"
  "v_mfma_f32_16x16x32_bf16 a[164:167], v[8:11], a[144:147], a[164:167]\n"
  "s_waitcnt vmcnt(12)\n"
  "ds_read_b64_tr_b16 a[132:133], v219 offset:1024\n"
  "v_add_f32 v253, v253, v17\n"
  "v_mfma_f32_16x16x32_bf16 a[164:167], v[12:15], a[148:151], a[164:167]\n"
  "ds_read_b64_tr_b16 a[134:135], v220 offset:1024\n"
  "v_fma_f32 v32, v32, s73, v225\n"
  "v_mfma_f32_16x16x32_bf16 v[84:87], a[156:159], a[152:155], v[84:87]\n"
  "v_exp_f32 v18, v18\n"
  "v_mfma_f32_16x16x32_bf16 v[84:87], a[160:163], a[152:155], v[84:87]\n"
  "s_waitcnt lgkmcnt(4)\n"
  "v_exp_f32 v19, v19\n"
  "v_mfma_f32_16x16x32_bf16 a[168:171], v[8:11], a[120:123], a[168:171]\n"
  "v_exp_f32 v32, v32\n"
  "v_mfma_f32_16x16x32_bf16 a[168:171], v[12:15], a[124:127], a[168:171]\n"
  "v_add_f32 v253, v253, v18\n"
  "v_add_f32 v253, v253, v19\n"
  "v_mfma_f32_16x16x32_bf16 a[184:187], v[24:27], a[120:123], a[184:187]\n"
  "ds_read_b128 a[136:139], v235 offset:0\n"
  "v_fma_f32 v33, v33, s73, v225\n"
  "v_mfma_f32_16x16x32_bf16 a[184:187], v[28:31], a[124:127], a[184:187]\n"
  "ds_read_b128 a[140:143], v235 offset:64\n"
  "v_fma_f32 v34, v34, s73, v225\n"
  "v_mfma_f32_16x16x32_bf16 a[200:203], v[40:43], a[120:123], a[200:203]\n"
  "ds_read_b128 a[144:147], v235 offset:1024\n"
  "v_fma_f32 v35, v35, s73, v225\n"
  "v_mfma_f32_16x16x32_bf16 a[200:203], v[44:47], a[124:127], a[200:203]\n"
  "ds_read_b128 a[148:151], v235 offset:1088\n"
  "v_add_f32 v254, v254, v32\n"
  "v_mfma_f32_16x16x32_bf16 a[216:219], v[56:59], a[120:123], a[216:219]\n"
  "v_exp_f32 v33, v33\n"
  "v_mfma_f32_16x16x32_bf16 a[216:219], v[60:63], a[124:127], a[216:219]\n"
  "v_exp_f32 v34, v34\n"
  "v_mfma_f32_16x16x32_bf16 a[232:235], a[156:159], a[120:123], a[232:235]\n"
  "ds_read_b128 a[120:123], v235 offset:2048\n"
  "v_add_f32 v254, v254, v33\n"
  "v_mfma_f32_16x16x32_bf16 a[232:235], a[160:163], a[124:127], a[232:235]\n"
  "s_waitcnt lgkmcnt(5)\n"
  "ds_read_b128 a[124:127], v235 offset:2112\n"
  "v_add_f32 v254, v254, v34\n"
  "v_mfma_f32_16x16x32_bf16 a[220:223], v[56:59], a[128:131], a[220:223]\n"
  "v_exp_f32 v35, v35\n"
  "v_mfma_f32_16x16x32_bf16 a[220:223], v[60:63], a[132:135], a[220:223]\n"
  "ds_write_b64 v231, v[64:65] offset:0\n"
  "v_mfma_f32_16x16x32_bf16 a[204:207], v[40:43], a[128:131], a[204:207]\n"
  "v_add_f32 v254, v254, v35\n"
  "v_fma_f32 v48, v48, s73, v226\n"
  "v_mfma_f32_16x16x32_bf16 a[204:207], v[44:47], a[132:135], a[204:207]\n"
  "v_exp_f32 v48, v48\n"
  "v_mfma_f32_16x16x32_bf16 a[188:191], v[24:27], a[128:131], a[188:191]\n"
  "v_fma_f32 v49, v49, s73, v226\n"
  "v_fma_f32 v50, v50, s73, v226\n"
  "v_mfma_f32_16x16x32_bf16 a[188:191], v[28:31], a[132:135], a[188:191]\n"
  "v_exp_f32 v49, v49\n"
  "v_mfma_f32_16x16x32_bf16 a[172:175], v[8:11], a[128:131], a[172:175]\n"
  "v_exp_f32 v50, v50\n"
  "v_mfma_f32_16x16x32_bf16 a[172:175], v[12:15], a[132:135], a[172:175]\n"
  "s_waitcnt lgkmcnt(5)\n"
  "v_fma_f32 v51, v51, s73, v226\n"
  "v_add_f32 v255, v255, v48\n"
  "v_mfma_f32_16x16x32_bf16 v[4:7], a[136:139], a[0:3], v[248:251]\n"
  "v_exp_f32 v51, v51\n"
  "v_mfma_f32_16x16x32_bf16 v[4:7], a[140:143], a[4:7], v[4:7]\n"
  "s_waitcnt lgkmcnt(3)\n"
  "v_add_f32 v255, v255, v49\n"
  "v_add_f32 v255, v255, v50\n"
  "v_mfma_f32_16x16x32_bf16 v[4:7], a[144:147], a[8:11], v[4:7]\n"
  "v_add_f32 v255, v255, v51\n"
  "v_mov_b32 v237, 0\n"
  "v_mfma_f32_16x16x32_bf16 v[4:7], a[148:151], a[12:15], v[4:7]\n"
  "s_waitcnt lgkmcnt(1)\n"
  "v_mov_b32 v238, 0\n"
  "v_mov_b32 v239, 0\n"
  "v_mfma_f32_16x16x32_bf16 v[4:7], a[120:123], a[16:19], v[4:7]\n"
  "v_mov_b32 v241, 0\n"
  "v_mov_b32 v242, 0\n"
  "v_mfma_f32_16x16x32_bf16 v[4:7], a[124:127], a[20:23], v[4:7]\n"
  "v_mov_b32 v243, 0\n"
  "v_bfe_u32 v234, v231, 1, 6\n"
  "v_mfma_f32_16x16x32_bf16 v[20:23], a[124:127], a[44:47], v[248:251]\n"
  "v_mfma_f32_16x16x32_bf16 v[20:23], a[120:123], a[40:43], v[20:23]\n"
  "v_mfma_f32_16x16x32_bf16 v[20:23], a[148:151], a[36:39], v[20:23]\n"
  "ds_read_b64_tr_b16 a[128:129], v219 offset:16\n"
  "v_fma_f32 v4, v4, s73, v223\n"
  "v_mfma_f32_16x16x32_bf16 v[20:23], a[144:147], a[32:35], v[20:23]\n"
  "ds_read_b64_tr_b16 a[130:131], v220 offset:16\n"
  "v_fma_f32 v5, v5, s73, v223\n"
  "v_mfma_f32_16x16x32_bf16 v[20:23], a[140:143], a[28:31], v[20:23]\n"
  "ds_read_b64_tr_b16 a[132:133], v219 offset:1040\n"
  "v_fma_f32 v6, v6, s73, v223\n"
  "v_mfma_f32_16x16x32_bf16 v[20:23], a[136:139], a[24:27], v[20:23]\n"
  "ds_read_b64_tr_b16 a[134:135], v220 offset:1040\n"
  "v_fma_f32 v7, v7, s73, v223\n"
  "v_mfma_f32_16x16x32_bf16 v[36:39], a[136:139], a[48:51], v[248:251]\n"
  "v_exp_f32 v4, v4\n"
  "v_mfma_f32_16x16x32_bf16 v[36:39], a[140:143], a[52:55], v[36:39]\n"
  "v_exp_f32 v5, v5\n"
  "v_mfma_f32_16x16x32_bf16 v[36:39], a[144:147], a[56:59], v[36:39]\n"
  "v_exp_f32 v6, v6\n"
  "v_mfma_f32_16x16x32_bf16 v[36:39], a[148:151], a[60:63], v[36:39]\n"
  "v_exp_f32 v7, v7\n"
  "v_mfma_f32_16x16x32_bf16 v[36:39], a[120:123], a[64:67], v[36:39]\n"
  "v_add_f32 v252, v252, v4\n"
  "v_add_f32 v252, v252, v5\n"
  "v_mfma_f32_16x16x32_bf16 v[36:39], a[124:127], a[68:71], v[36:39]\n"
  "v_add_f32 v252, v252, v6\n"
  "v_add_f32 v252, v252, v7\n"
  "v_mfma_f32_16x16x32_bf16 v[52:55], a[124:127], a[92:95], v[248:251]\n"
  "ds_read_b64_tr_b16 a[124:125], v217 offset:2048\n"
  "v_fma_f32 v20, v20, s73, v224\n"
  "v_mfma_f32_16x16x32_bf16 v[52:55], a[120:123], a[88:91], v[52:55]\n"
  "ds_read_b64_tr_b16 a[126:127], v218 offset:2048\n"
  "v_fma_f32 v21, v21, s73, v224\n"
  "v_mfma_f32_16x16x32_bf16 v[52:55], a[148:151], a[84:87], v[52:55]\n"
  "ds_read_b64_tr_b16 a[120:121], v217 offset:2064\n"
  "v_fma_f32 v22, v22, s73, v224\n"
  "v_mfma_f32_16x16x32_bf16 v[52:55], a[144:147], a[80:83], v[52:55]\n"
  "ds_read_b64_tr_b16 a[122:123], v218 offset:2064\n"
  "v_fma_f32 v23, v23, s73, v224\n"
  "v_mfma_f32_16x16x32_bf16 v[52:55], a[140:143], a[76:79], v[52:55]\n"
  "v_exp_f32 v20, v20\n"
  "v_mfma_f32_16x16x32_bf16 v[52:55], a[136:139], a[72:75], v[52:55]\n"
  "s_waitcnt lgkmcnt(4)\n"
  "v_exp_f32 v21, v21\n"
  "v_mfma_f32_16x16x32_bf16 a[176:179], v[8:11], a[128:131], a[176:179]\n"
  "v_cvt_pk_bf16_f32 v8, v0, v1\n"
  "v_cvt_pk_bf16_f32 v9, v2, v3\n"
  "v_mfma_f32_16x16x32_bf16 a[176:179], v[12:15], a[132:135], a[176:179]\n"
  "v_exp_f32 v22, v22\n"
  "v_mfma_f32_16x16x32_bf16 a[192:195], v[24:27], a[128:131], a[192:195]\n"
  "ds_read_b64_tr_b16 a[140:141], v219 offset:2048\n"
  "v_cvt_pk_bf16_f32 v24, v16, v17\n"
  "v_mfma_f32_16x16x32_bf16 a[192:195], v[28:31], a[132:135], a[192:195]\n"
  "ds_read_b64_tr_b16 a[142:143], v220 offset:2048\n"
  "v_cvt_pk_bf16_f32 v25, v18, v19\n"
  "v_mfma_f32_16x16x32_bf16 a[208:211], v[40:43], a[128:131], a[208:211]\n"
  "ds_read_b64_tr_b16 a[136:137], v219 offset:2064\n"
  "v_cvt_pk_bf16_f32 v40, v32, v33\n"
  "v_mfma_f32_16x16x32_bf16 a[208:211], v[44:47], a[132:135], a[208:211]\n"
  "ds_read_b64_tr_b16 a[138:139], v220 offset:2064\n"
  "v_cvt_pk_bf16_f32 v41, v34, v35\n"
  "v_mfma_f32_16x16x32_bf16 a[224:227], v[56:59], a[128:131], a[224:227]\n"
  "v_exp_f32 v23, v23\n"
  "v_mfma_f32_16x16x32_bf16 a[224:227], v[60:63], a[132:135], a[224:227]\n"
  "s_waitcnt lgkmcnt(4)\n"
  "v_cvt_pk_bf16_f32 v56, v48, v49\n"
  "v_cvt_pk_bf16_f32 v57, v50, v51\n"
  "v_mfma_f32_16x16x32_bf16 a[244:247], a[160:163], a[124:127], a[244:247]\n"
  "v_cvt_pk_bf16_f32 v10, v4, v5\n"
  "v_cvt_pk_bf16_f32 v11, v6, v7\n"
  "v_mfma_f32_16x16x32_bf16 a[248:251], a[160:163], a[120:123], a[248:251]\n"
  "v_add_f32 v253, v253, v20\n"
  "v_add_f32 v253, v253, v21\n"
  "v_mfma_f32_16x16x32_bf16 v[200:203], v[60:63], a[124:127], v[200:203]\n"
  "v_add_f32 v253, v253, v22\n"
  "v_add_f32 v253, v253, v23\n"
  "v_mfma_f32_16x16x32_bf16 v[204:207], v[60:63], a[120:123], v[204:207]\n"
  "v_cvt_pk_bf16_f32 v26, v20, v21\n"
  "v_cvt_pk_bf16_f32 v27, v22, v23\n"
  "v_mfma_f32_16x16x32_bf16 v[168:171], v[44:47], a[124:127], v[168:171]\n"
  "v_fma_f32 v36, v36, s73, v225\n"
  "v_fma_f32 v37, v37, s73, v225\n"
  "v_mfma_f32_16x16x32_bf16 v[172:175], v[44:47], a[120:123], v[172:175]\n"
  "s_waitcnt vmcnt(6) lgkmcnt(0)\n"
  "s_barrier\n"
  "ds_read_b128 a[128:131], v216 offset:12352\n"
  "v_mfma_f32_16x16x32_bf16 v[136:139], v[28:31], a[124:127], v[136:139]\n"
  "ds_read_b64 a[156:157], v231 offset:0\n"
  "v_fma_f32 v38, v38, s73, v225\n"
  "v_mfma_f32_16x16x32_bf16 v[140:143], v[28:31], a[120:123], v[140:143]\n"
  "ds_read_b64 a[158:159], v232 offset:0\n"
  "v_fma_f32 v39, v39, s73, v225\n"
  "v_mfma_f32_16x16x32_bf16 v[104:107], v[12:15], a[124:127], v[104:107]\n"
  "ds_read_b128 a[132:135], v216 offset:12416\n"
  "v_fma_f32 v52, v52, s73, v226\n"
  "v_mfma_f32_16x16x32_bf16 v[108:111], v[12:15], a[120:123], v[108:111]\n"
  "ds_read_b128 a[124:127], v216 offset:13376\n"
  "v_fma_f32 v53, v53, s73, v226\n"
  "v_mfma_f32_16x16x32_bf16 v[112:115], v[12:15], a[140:143], v[112:115]\n"
  "ds_read_b128 a[120:123], v216 offset:13440\n"
  "v_fma_f32 v54, v54, s73, v226\n"
  "v_mfma_f32_16x16x32_bf16 v[116:119], v[12:15], a[136:139], v[116:119]\n"
  "s_mov_b32 m0, s66\n"
  "ds_read_b128 a[148:151], v216 offset:14400\n"
  "v_fma_f32 v55, v55, s73, v226\n"
  "v_mfma_f32_16x16x32_bf16 v[144:147], v[28:31], a[140:143], v[144:147]\n"
  "ds_read_b128 a[144:147], v216 offset:14464\n"
  "v_and_b32 v236, 0xffff0000, v252\n"
  "v_mfma_f32_16x16x32_bf16 v[148:151], v[28:31], a[136:139], v[148:151]\n"
  "v_exp_f32 v36, v36\n"
  "v_mfma_f32_16x16x32_bf16 v[176:179], v[44:47], a[140:143], v[176:179]\n"
  "v_exp_f32 v37, v37\n"
  "v_mfma_f32_16x16x32_bf16 v[180:183], v[44:47], a[136:139], v[180:183]\n"
  "buffer_load_dwordx4 v221, s[40:43], 0 offen offset:0 lds\n"
  "v_mfma_f32_16x16x32_bf16 v[208:211], v[60:63], a[140:143], v[208:211]\n"
  "s_addk_i32 m0, 896\n"
  "v_exp_f32 v38, v38\n"
  "v_mfma_f32_16x16x32_bf16 v[212:215], v[60:63], a[136:139], v[212:215]\n"
  "s_waitcnt lgkmcnt(4)\n"
  "v_exp_f32 v39, v39\n"
  "v_mfma_f32_16x16x32_bf16 v[64:67], a[128:131], a[96:99], v[248:251]\n"
  "v_exp_f32 v52, v52\n"
  "v_mfma_f32_16x16x32_bf16 v[64:67], a[132:135], a[100:103], v[64:67]\n"
  "s_waitcnt lgkmcnt(2)\n"
  "buffer_load_dwordx4 v221, s[40:43], 0 offen offset:128 lds\n"
  "v_add_f32 v254, v254, v36\n"
  "v_mfma_f32_16x16x32_bf16 v[64:67], a[124:127], a[104:107], v[64:67]\n"
  "s_addk_i32 m0, 896\n"
  "v_exp_f32 v53, v53\n"
  "v_mfma_f32_16x16x32_bf16 v[64:67], a[120:123], a[108:111], v[64:67]\n"
  "s_waitcnt lgkmcnt(0)\n"
  "v_exp_f32 v54, v54\n"
  "v_mfma_f32_16x16x32_bf16 v[64:67], a[148:151], a[112:115], v[64:67]\n"
  "v_exp_f32 v55, v55\n"
  "v_mfma_f32_16x16x32_bf16 v[64:67], a[144:147], a[116:119], v[64:67]\n"
  "buffer_load_dwordx4 v221, s[40:43], 0 offen offset:256 lds\n"
  "v_add_f32 v254, v254, v37\n"
  "v_mfma_f32_16x16x32_bf16 v[0:3], a[144:147], a[20:23], v[248:251]\n"
  "s_add_u32 s40, s40, -12288\n"
  "v_add_f32 v254, v254, v38\n"
  "v_add_f32 v254, v254, v39\n"
  "v_mfma_f32_16x16x32_bf16 v[0:3], a[148:151], a[16:19], v[0:3]\n"
  "s_addc_u32 s41, s41, -1\n"
  "v_cvt_pk_bf16_f32 v42, v36, v37\n"
  "v_cvt_pk_bf16_f32 v43, v38, v39\n"
  "v_mfma_f32_16x16x32_bf16 v[0:3], a[120:123], a[12:15], v[0:3]\n"
  "v_add_f32 v255, v255, v52\n"
  "v_add_f32 v255, v255, v53\n"
  "v_mfma_f32_16x16x32_bf16 v[0:3], a[124:127], a[8:11], v[0:3]\n"
  "s_mov_b32 m0, s58\n"
  "v_add_f32 v255, v255, v54\n"
  "v_add_f32 v255, v255, v55\n"
  "v_mfma_f32_16x16x32_bf16 v[0:3], a[132:135], a[4:7], v[0:3]\n"
  "v_cvt_pk_bf16_f32 v58, v52, v53\n"
  "v_cvt_pk_bf16_f32 v59, v54, v55\n"
  "v_mfma_f32_16x16x32_bf16 v[0:3], a[128:131], a[0:3], v[0:3]\n"
  "v_sub_f32 v252, v252, v236\n"
  "v_or_b32_sdwa v236, v236, v252 dst_sel:DWORD dst_unused:UNUSED_PAD src0_sel:DWORD src1_sel:WORD_1\n"
  "v_mfma_f32_16x16x32_bf16 v[16:19], a[128:131], a[24:27], v[248:251]\n"
  "s_add_u32 s71, s92, s83\n"
  "v_and_b32 v240, 0xffff0000, v253\n"
  "v_sub_f32 v253, v253, v240\n"
  "v_mfma_f32_16x16x32_bf16 v[16:19], a[132:135], a[28:31], v[16:19]\n"
  "buffer_load_dwordx4 v221, s[32:35], 0 offen offset:0 lds\n"
  "v_or_b32_sdwa v240, v240, v253 dst_sel:DWORD dst_unused:UNUSED_PAD src0_sel:DWORD src1_sel:WORD_1\n"
  "v_mfma_f32_16x16x32_bf16 v[16:19], a[124:127], a[32:35], v[16:19]\n"
  "s_addk_i32 m0, 1024\n"
  "ds_read_b128 a[140:143], v235 offset:12352\n"
  "v_fma_f32 v64, v64, s73, v227\n"
  "v_mfma_f32_16x16x32_bf16 v[16:19], a[120:123], a[36:39], v[16:19]\n"
  "s_mov_b64 s[32:33], s[36:37]\n"
  "ds_read_b128 a[136:139], v235 offset:12416\n"
  "v_fma_f32 v65, v65, s73, v227\n"
  "v_mfma_f32_16x16x32_bf16 v[16:19], a[148:151], a[40:43], v[16:19]\n"
  "v_exp_f32 v64, v64\n"
  "v_mfma_f32_16x16x32_bf16 v[16:19], a[144:147], a[44:47], v[16:19]\n"
  "buffer_load_dwordx4 v221, s[44:47], 0 offen offset:0 lds\n"
  "v_fma_f32 v66, v66, s73, v227\n"
  "v_mfma_f32_16x16x32_bf16 v[32:35], a[144:147], a[68:71], v[248:251]\n"
  "s_addk_i32 m0, 768\n"
  "v_exp_f32 v65, v65\n"
  "v_mfma_f32_16x16x32_bf16 v[32:35], a[148:151], a[64:67], v[32:35]\n"
  "v_exp_f32 v66, v66\n"
  "v_mfma_f32_16x16x32_bf16 v[32:35], a[120:123], a[60:63], v[32:35]\n"
  "v_fma_f32 v67, v67, s73, v227\n"
  "v_cvt_pk_bf16_f32 v64, v64, v65\n"
  "v_mfma_f32_16x16x32_bf16 v[32:35], a[124:127], a[56:59], v[32:35]\n"
  "buffer_load_dwordx4 v221, s[44:47], 0 offen offset:256 lds\n"
  "v_fma_f32 v0, v0, s73, v223\n"
  "v_mfma_f32_16x16x32_bf16 v[32:35], a[132:135], a[52:55], v[32:35]\n"
  "s_add_u32 s44, s44, -12288\n"
  "v_exp_f32 v67, v67\n"
  "v_mfma_f32_16x16x32_bf16 v[32:35], a[128:131], a[48:51], v[32:35]\n"
  "s_addc_u32 s45, s45, -1\n"
  "v_exp_f32 v0, v0\n"
  "v_mfma_f32_16x16x32_bf16 v[48:51], a[128:131], a[72:75], v[248:251]\n"
  "ds_read_b128 a[128:131], v235 offset:13376\n"
  "v_cvt_pk_bf16_f32 v65, v66, v67\n"
  "v_mfma_f32_16x16x32_bf16 v[48:51], a[132:135], a[76:79], v[48:51]\n"
  "ds_read_b128 a[132:135], v235 offset:13440\n"
  "v_fma_f32 v1, v1, s73, v223\n"
  "v_mfma_f32_16x16x32_bf16 v[48:51], a[124:127], a[80:83], v[48:51]\n"
  "ds_read_b128 a[124:127], v235 offset:14400\n"
  "v_fma_f32 v2, v2, s73, v223\n"
  "v_mfma_f32_16x16x32_bf16 v[48:51], a[120:123], a[84:87], v[48:51]\n"
  "ds_read_b128 a[120:123], v235 offset:14464\n"
  "v_fma_f32 v3, v3, s73, v223\n"
  "v_mfma_f32_16x16x32_bf16 v[48:51], a[148:151], a[88:91], v[48:51]\n"
  "v_exp_f32 v1, v1\n"
  "v_mfma_f32_16x16x32_bf16 v[48:51], a[144:147], a[92:95], v[48:51]\n"
  "s_waitcnt lgkmcnt(4)\n"
  "v_exp_f32 v2, v2\n"
  "v_mfma_f32_16x16x32_bf16 v[4:7], a[140:143], a[0:3], v[248:251]\n"
  "ds_read_b128 a[0:3], v222 offset:0\n"
  "v_fma_f32 v16, v16, s73, v224\n"
  "v_mfma_f32_16x16x32_bf16 v[4:7], a[136:139], a[4:7], v[4:7]\n"
  "s_waitcnt lgkmcnt(3)\n"
  "ds_read_b128 a[4:7], v222 offset:64\n"
  "v_fma_f32 v17, v17, s73, v224\n"
  "v_mfma_f32_16x16x32_bf16 v[4:7], a[128:131], a[8:11], v[4:7]\n"
  "ds_read_b128 a[8:11], v222 offset:2240\n"
  "v_fma_f32 v18, v18, s73, v224\n"
  "v_mfma_f32_16x16x32_bf16 v[4:7], a[132:135], a[12:15], v[4:7]\n"
  "s_waitcnt lgkmcnt(3)\n"
  "ds_read_b128 a[12:15], v222 offset:2304\n"
  "v_fma_f32 v19, v19, s73, v224\n"
  "v_mfma_f32_16x16x32_bf16 v[4:7], a[124:127], a[16:19], v[4:7]\n"
  "ds_read_b128 a[16:19], v222 offset:4480\n"
  "v_fma_f32 v32, v32, s73, v225\n"
  "v_mfma_f32_16x16x32_bf16 v[4:7], a[120:123], a[20:23], v[4:7]\n"
  "ds_read_b128 a[20:23], v222 offset:4544\n"
  "v_fma_f32 v33, v33, s73, v225\n"
  "v_mfma_f32_16x16x32_bf16 v[20:23], a[120:123], a[44:47], v[248:251]\n"
  "ds_read_b128 a[44:47], v222 offset:6784\n"
  "v_fma_f32 v34, v34, s73, v225\n"
  "v_mfma_f32_16x16x32_bf16 v[20:23], a[124:127], a[40:43], v[20:23]\n"
  "ds_read_b128 a[40:43], v222 offset:6720\n"
  "v_fma_f32 v35, v35, s73, v225\n"
  "v_mfma_f32_16x16x32_bf16 v[20:23], a[132:135], a[36:39], v[20:23]\n"
  "ds_read_b64_tr_b16 a[148:149], v217 offset:12352\n"
  "v_fma_f32 v4, v4, s73, v223\n"
  "v_mfma_f32_16x16x32_bf16 v[20:23], a[128:131], a[32:35], v[20:23]\n"
  "ds_read_b64_tr_b16 a[150:151], v218 offset:12352\n"
  "v_fma_f32 v5, v5, s73, v223\n"
  "v_mfma_f32_16x16x32_bf16 v[20:23], a[136:139], a[28:31], v[20:23]\n"
  "s_add_u32 m0, s72, -256\n"
  "ds_read_b64_tr_b16 a[144:145], v217 offset:13376\n"
  "v_mfma_f32_16x16x32_bf16 v[20:23], a[140:143], a[24:27], v[20:23]\n"
  "s_add_u32 s52, s62, s57\n"
  "ds_read_b64_tr_b16 a[146:147], v218 offset:13376\n"
  "v_fma_f32 v6, v6, s73, v223\n"
  "v_mfma_f32_16x16x32_bf16 v[36:39], a[140:143], a[48:51], v[248:251]\n"
  "s_addc_u32 s53, s63, 0\n"
  "v_exp_f32 v3, v3\n"
  "v_mfma_f32_16x16x32_bf16 v[36:39], a[136:139], a[52:55], v[36:39]\n"
  "s_mov_b32 s54, s93\n"
  "v_exp_f32 v4, v4\n"
  "v_mfma_f32_16x16x32_bf16 v[36:39], a[128:131], a[56:59], v[36:39]\n"
  "s_waitcnt lgkmcnt(10)\n"
  "buffer_load_dwordx4 v221, s[48:51], 0 offen offset:256  lds\n"
  "v_mfma_f32_16x16x32_bf16 v[36:39], a[132:135], a[60:63], v[36:39]\n"
  "s_add_u32 m0, s72, -2240\n"
  "v_exp_f32 v5, v5\n"
  "v_mfma_f32_16x16x32_bf16 v[36:39], a[124:127], a[64:67], v[36:39]\n"
  "s_mov_b32 s57, s92\n"
  "v_exp_f32 v6, v6\n"
  "v_mfma_f32_16x16x32_bf16 v[36:39], a[120:123], a[68:71], v[36:39]\n"
  "s_mov_b32 s93, s94\n"
  "v_exp_f32 v16, v16\n"
  "v_mfma_f32_16x16x32_bf16 v[52:55], a[120:123], a[92:95], v[248:251]\n"
  "buffer_load_dwordx4 v221, s[48:51], 0 offen offset:3328  lds\n"
  "v_fma_f32 v7, v7, s73, v223\n"
  "v_mfma_f32_16x16x32_bf16 v[52:55], a[124:127], a[88:91], v[52:55]\n"
  "s_add_u32 s48, s84, s71\n"
  "ds_read_b64_tr_b16 a[120:121], v217 offset:12368\n"
  "v_fma_f32 v20, v20, s73, v224\n"
  "v_mfma_f32_16x16x32_bf16 v[52:55], a[132:135], a[84:87], v[52:55]\n"
  "s_addc_u32 s49, s85, 0\n"
  "ds_read_b64_tr_b16 a[122:123], v218 offset:12368\n"
  "v_fma_f32 v21, v21, s73, v224\n"
  "v_mfma_f32_16x16x32_bf16 v[52:55], a[128:131], a[80:83], v[52:55]\n"
  "s_add_u32 m0, s72, 2240\n"
  "ds_read_b64_tr_b16 a[124:125], v217 offset:13392\n"
  "v_mfma_f32_16x16x32_bf16 v[52:55], a[136:139], a[76:79], v[52:55]\n"
  "s_sub_i32 s71, s38, s83\n"
  "s_max_i32 s50, s71, 0\n"
  "s_waitcnt lgkmcnt(11)\n"
  "buffer_load_dwordx4 v221, s[48:51], 0 offen offset:0 nt lds\n"
  "v_fma_f32 v22, v22, s73, v224\n"
  "v_mfma_f32_16x16x32_bf16 v[52:55], a[140:143], a[72:75], v[52:55]\n"
  "s_waitcnt lgkmcnt(3)\n"
  "ds_read_b64_tr_b16 a[126:127], v218 offset:13392\n"
  "v_fma_f32 v23, v23, s73, v224\n"
  "v_mfma_f32_16x16x32_bf16 a[236:239], a[160:163], a[148:151], a[236:239]\n"
  "v_exp_f32 v7, v7\n"
  "v_mfma_f32_16x16x32_bf16 a[236:239], a[156:159], a[144:147], a[236:239]\n"
  "s_add_u32 m0, s72, 256\n"
  "ds_write_b64 v231, v[64:65] offset:2048\n"
  "v_mfma_f32_16x16x32_bf16 v[184:187], v[60:63], a[148:151], v[184:187]\n"
  "buffer_load_dwordx4 v221, s[48:51], 0 offen offset:3072 nt lds\n"
  "v_fma_f32 v36, v36, s73, v225\n"
  "v_mfma_f32_16x16x32_bf16 v[184:187], v[56:59], a[144:147], v[184:187]\n"
  "s_add_u32 m0, s72, 4352\n"
  "v_exp_f32 v17, v17\n"
  "v_mfma_f32_16x16x32_bf16 v[152:155], v[44:47], a[148:151], v[152:155]\n"
  "v_exp_f32 v18, v18\n"
  "v_mfma_f32_16x16x32_bf16 v[152:155], v[40:43], a[144:147], v[152:155]\n"
  "ds_read_b64_tr_b16 a[132:133], v219 offset:12352\n"
  "v_fma_f32 v37, v37, s73, v225\n"
  "v_mfma_f32_16x16x32_bf16 v[120:123], v[28:31], a[148:151], v[120:123]\n"
  "buffer_load_dwordx4 v221, s[48:51], 0 offen offset:128 nt lds\n"
  "v_fma_f32 v38, v38, s73, v225\n"
  "v_mfma_f32_16x16x32_bf16 v[120:123], v[24:27], a[144:147], v[120:123]\n"
  "s_add_u32 m0, s72, 2368\n"
  "ds_read_b64_tr_b16 a[134:135], v220 offset:12352\n"
  "v_mfma_f32_16x16x32_bf16 v[88:91], v[12:15], a[148:151], v[88:91]\n"
  "ds_read_b64_tr_b16 a[128:129], v219 offset:13376\n"
  "v_fma_f32 v39, v39, s73, v225\n"
  "v_mfma_f32_16x16x32_bf16 v[88:91], v[8:11], a[144:147], v[88:91]\n"
  "s_waitcnt lgkmcnt(4)\n"
  "ds_read_b64_tr_b16 a[130:131], v220 offset:13376\n"
  "v_fma_f32 v48, v48, s73, v226\n"
  "v_mfma_f32_16x16x32_bf16 v[92:95], v[12:15], a[120:123], v[92:95]\n"
  "buffer_load_dwordx4 v221, s[48:51], 0 offen offset:3200 nt lds\n"
  "v_fma_f32 v49, v49, s73, v226\n"
  "v_mfma_f32_16x16x32_bf16 v[92:95], v[8:11], a[124:127], v[92:95]\n"
  "s_add_u32 m0, s72, 6464\n"
  "v_exp_f32 v19, v19\n"
  "v_mfma_f32_16x16x32_bf16 v[124:127], v[28:31], a[120:123], v[124:127]\n"
  "v_exp_f32 v20, v20\n"
  "v_mfma_f32_16x16x32_bf16 v[124:127], v[24:27], a[124:127], v[124:127]\n"
  "v_exp_f32 v21, v21\n"
  "v_mfma_f32_16x16x32_bf16 v[156:159], v[44:47], a[120:123], v[156:159]\n"
  "buffer_load_dwordx4 v221, s[48:51], 0 offen offset:256 nt lds\n"
  "v_fma_f32 v50, v50, s73, v226\n"
  "v_mfma_f32_16x16x32_bf16 v[156:159], v[40:43], a[124:127], v[156:159]\n"
  "s_add_u32 m0, s72, 4480\n"
  "ds_read_b64_tr_b16 a[136:137], v219 offset:12368\n"
  "v_mfma_f32_16x16x32_bf16 v[188:191], v[60:63], a[120:123], v[188:191]\n"
  "ds_read_b64_tr_b16 a[138:139], v220 offset:12368\n"
  "v_fma_f32 v51, v51, s73, v226\n"
  "v_mfma_f32_16x16x32_bf16 v[188:191], v[56:59], a[124:127], v[188:191]\n"
  "ds_read_b64_tr_b16 a[140:141], v219 offset:13392\n"
  "v_fma_f32 v52, v52, s73, v226\n"
  "v_mfma_f32_16x16x32_bf16 a[240:243], a[160:163], a[120:123], a[240:243]\n"
  "buffer_load_dwordx4 v221, s[48:51], 0 offen offset:3328 nt lds\n"
  "v_fma_f32 v53, v53, s73, v226\n"
  "v_mfma_f32_16x16x32_bf16 a[240:243], a[156:159], a[124:127], a[240:243]\n"
  "ds_read_b64_tr_b16 a[142:143], v220 offset:13392\n"
  "v_fma_f32 v54, v54, s73, v226\n"
  "v_mfma_f32_16x16x32_bf16 v[68:71], v[236:239], a[152:155], v[68:71]\n"
  "s_waitcnt lgkmcnt(4)\n"
  "v_exp_f32 v22, v22\n"
  "v_mfma_f32_16x16x32_bf16 v[192:195], v[60:63], a[132:135], v[192:195]\n"
  "v_exp_f32 v23, v23\n"
  "v_mfma_f32_16x16x32_bf16 v[192:195], v[56:59], a[128:131], v[192:195]\n"
  "v_exp_f32 v32, v32\n"
  "v_mfma_f32_16x16x32_bf16 v[160:163], v[44:47], a[132:135], v[160:163]\n"
  "ds_read_b64_tr_b16 a[120:121], v217 offset:14400\n"
  "v_and_b32 v236, 0xffff0000, v254\n"
  "v_mfma_f32_16x16x32_bf16 v[160:163], v[40:43], a[128:131], v[160:163]\n"
  "ds_read_b64_tr_b16 a[122:123], v218 offset:14400\n"
  "v_sub_f32 v254, v254, v236\n"
  "v_mfma_f32_16x16x32_bf16 v[128:131], v[28:31], a[132:135], v[128:131]\n"
  "ds_read_b64_tr_b16 a[124:125], v217 offset:14416\n"
  "v_or_b32_sdwa v236, v236, v254 dst_sel:DWORD dst_unused:UNUSED_PAD src0_sel:DWORD src1_sel:WORD_1\n"
  "v_mfma_f32_16x16x32_bf16 v[128:131], v[24:27], a[128:131], v[128:131]\n"
  "ds_read_b64_tr_b16 a[126:127], v218 offset:14416\n"
  "v_fma_f32 v55, v55, s73, v226\n"
  "v_mfma_f32_16x16x32_bf16 v[96:99], v[12:15], a[132:135], v[96:99]\n"
  "v_exp_f32 v33, v33\n"
  "v_mfma_f32_16x16x32_bf16 v[96:99], v[8:11], a[128:131], v[96:99]\n"
  "v_exp_f32 v34, v34\n"
  "v_mfma_f32_16x16x32_bf16 v[72:75], v[240:243], a[152:155], v[72:75]\n"
  "s_waitcnt lgkmcnt(4)\n"
  "v_exp_f32 v35, v35\n"
  "v_mfma_f32_16x16x32_bf16 v[100:103], v[12:15], a[136:139], v[100:103]\n"
  "v_exp_f32 v36, v36\n"
  "v_mfma_f32_16x16x32_bf16 v[100:103], v[8:11], a[140:143], v[100:103]\n"
  "v_exp_f32 v37, v37\n"
  "v_mfma_f32_16x16x32_bf16 v[132:135], v[28:31], a[136:139], v[132:135]\n"
  "v_exp_f32 v38, v38\n"
  "v_mfma_f32_16x16x32_bf16 v[132:135], v[24:27], a[140:143], v[132:135]\n"
  "v_exp_f32 v39, v39\n"
  "v_mfma_f32_16x16x32_bf16 v[164:167], v[44:47], a[136:139], v[164:167]\n"
  "ds_read_b64_tr_b16 a[132:133], v219 offset:14400\n"
  "v_and_b32 v240, 0xffff0000, v255\n"
  "v_mfma_f32_16x16x32_bf16 v[164:167], v[40:43], a[140:143], v[164:167]\n"
  "ds_read_b64_tr_b16 a[134:135], v220 offset:14400\n"
  "v_sub_f32 v255, v255, v240\n"
  "v_mfma_f32_16x16x32_bf16 v[196:199], v[60:63], a[136:139], v[196:199]\n"
  "ds_read_b64_tr_b16 a[128:129], v219 offset:14416\n"
  "v_or_b32_sdwa v240, v240, v255 dst_sel:DWORD dst_unused:UNUSED_PAD src0_sel:DWORD src1_sel:WORD_1\n"
  "v_mfma_f32_16x16x32_bf16 v[196:199], v[56:59], a[140:143], v[196:199]\n"
  "ds_read_b64_tr_b16 a[130:131], v220 offset:14416\n"
  "v_cvt_pk_bf16_f32 v12, v0, v1\n"
  "v_mfma_f32_16x16x32_bf16 v[76:79], v[236:239], a[152:155], v[76:79]\n"
  "s_waitcnt lgkmcnt(4)\n"
  "v_exp_f32 v48, v48\n"
  "v_mfma_f32_16x16x32_bf16 a[244:247], a[156:159], a[120:123], a[244:247]\n"
  "v_exp_f32 v49, v49\n"
  "v_mfma_f32_16x16x32_bf16 a[248:251], a[156:159], a[124:127], a[248:251]\n"
  "v_exp_f32_e64 v50, v50\n"
  "v_mfma_f32_16x16x32_bf16 v[200:203], v[56:59], a[120:123], v[200:203]\n"
  "v_add_u32 v233, s97, v222\n"
  "s_mov_b64 exec, s[98:99]\n"
  "v_add_u32 v233, 0x4f00, v233\n"
  "s_mov_b64 exec, -1\n"
  "v_mfma_f32_16x16x32_bf16 v[204:207], v[56:59], a[124:127], v[204:207]\n"
  "ds_read_b128 a[96:99], v233 offset:17920\n"
  "ds_read_b128 a[100:103], v233 offset:17984\n"
  "ds_read_b128 a[104:107], v233 offset:19040\n"
  "ds_read_b128 a[108:111], v233 offset:19104\n"
  "ds_read_b128 a[112:115], v233 offset:20160\n"
  "ds_read_b128 a[116:119], v233 offset:20224\n"
  "v_mfma_f32_16x16x32_bf16 v[168:171], v[40:43], a[120:123], v[168:171]\n"
  "v_exp_f32 v51, v51\n"
  "v_mfma_f32_16x16x32_bf16 v[172:175], v[40:43], a[124:127], v[172:175]\n"
  "s_mov_b32 m0, s67\n"
  "v_exp_f32 v52, v52\n"
  "v_mfma_f32_16x16x32_bf16 v[136:139], v[24:27], a[120:123], v[136:139]\n"
  "s_waitcnt vmcnt(14) lgkmcnt(0)\n"
  "s_barrier\n"
  "v_mfma_f32_16x16x32_bf16 v[140:143], v[24:27], a[124:127], v[140:143]\n"
  "ds_read_b64 a[160:161], v231 offset:2048\n"
  "v_cvt_pk_bf16_f32 v13, v2, v3\n"
  "v_mfma_f32_16x16x32_bf16 v[104:107], v[8:11], a[120:123], v[104:107]\n"
  "ds_read_b64 a[162:163], v232 offset:2048\n"
  "v_cvt_pk_bf16_f32 v14, v4, v5\n"
  "v_mfma_f32_16x16x32_bf16 v[108:111], v[8:11], a[124:127], v[108:111]\n"
  "buffer_load_dwordx4 v221, s[40:43], 0 offen offset:0 lds\n"
  "v_mfma_f32_16x16x32_bf16 v[80:83], v[240:243], a[152:155], v[80:83]\n"
  "ds_read_b64_tr_b16 a[120:121], v217 offset:26752\n"
  "v_cvt_pk_bf16_f32 v15, v6, v7\n"
  "v_mfma_f32_16x16x32_bf16 v[112:115], v[8:11], a[132:135], v[112:115]\n"
  "s_addk_i32 m0, 896\n"
  "ds_read_b64_tr_b16 a[122:123], v218 offset:26752\n"
  "v_cvt_pk_bf16_f32 v28, v16, v17\n"
  "v_mfma_f32_16x16x32_bf16 v[116:119], v[8:11], a[128:131], v[116:119]\n"
  "ds_read_b64_tr_b16 a[124:125], v217 offset:26768\n"
  "v_cvt_pk_bf16_f32 v29, v18, v19\n"
  "v_mfma_f32_16x16x32_bf16 v[144:147], v[24:27], a[132:135], v[144:147]\n"
  "buffer_load_dwordx4 v221, s[40:43], 0 offen offset:128 lds\n"
  "v_cvt_pk_bf16_f32 v30, v20, v21\n"
  "v_mfma_f32_16x16x32_bf16 v[148:151], v[24:27], a[128:131], v[148:151]\n"
  "s_addk_i32 m0, 896\n"
  "ds_read_b64_tr_b16 a[126:127], v218 offset:26768\n"
  "v_cvt_pk_bf16_f32 v31, v22, v23\n"
  "v_mfma_f32_16x16x32_bf16 v[176:179], v[40:43], a[132:135], v[176:179]\n"
  "v_exp_f32 v53, v53\n"
  "v_mfma_f32_16x16x32_bf16 v[180:183], v[40:43], a[128:131], v[180:183]\n"
  "v_exp_f32 v54, v54\n"
  "v_mfma_f32_16x16x32_bf16 v[208:211], v[56:59], a[132:135], v[208:211]\n"
  "buffer_load_dwordx4 v221, s[40:43], 0 offen offset:256 lds\n"
  "v_cvt_pk_bf16_f32 v44, v32, v33\n"
  "v_mfma_f32_16x16x32_bf16 v[212:215], v[56:59], a[128:131], v[212:215]\n"
  "s_cmp_eq_i32 s76, 5\n"
  "s_cbranch_scc1 L_TAIL_%=\n"
  "v_exp_f32 v55, v55\n"
  "v_mfma_f32_16x16x32_bf16 v[68:71], v[12:15], a[152:155], v[68:71]\n"
  "ds_read_b64_tr_b16 a[132:133], v219 offset:26752\n"
  "v_cvt_pk_bf16_f32 v45, v34, v35\n"
  "v_cvt_pk_bf16_f32 v46, v36, v37\n"
  "v_cvt_pk_bf16_f32 v47, v38, v39\n"
  "v_mfma_f32_16x16x32_bf16 v[72:75], v[28:31], a[152:155], v[72:75]\n"
  "s_add_u32 m0, s59, -128\n"
  "ds_read_b64_tr_b16 a[134:135], v220 offset:26752\n"
  "v_cvt_pk_bf16_f32 v60, v48, v49\n"
  "v_cvt_pk_bf16_f32 v61, v50, v51\n"
  "v_cvt_pk_bf16_f32 v62, v52, v53\n"
  "v_cvt_pk_bf16_f32 v63, v54, v55\n"
  "v_mfma_f32_16x16x32_bf16 v[76:79], v[44:47], a[152:155], v[76:79]\n"
  "buffer_load_dwordx4 v221, s[32:35], 0 offen offset:128 lds\n"
  "ds_read_b128 a[24:27], v222 offset:11200\n"
  "v_mfma_f32_16x16x32_bf16 v[80:83], v[60:63], a[152:155], v[80:83]\n"
  "s_addk_i32 m0, 1024\n"
  "ds_read_b64_tr_b16 a[128:129], v219 offset:26768\n"
  "ds_read_b128 a[28:31], v222 offset:11264\n"
  "v_mfma_f32_16x16x32_bf16 v[84:87], a[156:159], a[152:155], v[84:87]\n"
  "s_waitcnt lgkmcnt(9)\n"
  "ds_read_b64_tr_b16 a[130:131], v220 offset:26768\n"
  "ds_read_b128 a[32:35], v222 offset:8960\n"
  "v_mfma_f32_16x16x32_bf16 v[84:87], a[160:163], a[152:155], v[84:87]\n"
  "s_waitcnt lgkmcnt(7)\n"
  "ds_read_b128 a[36:39], v222 offset:9024\n"
  "ds_read_b128 a[48:51], v222 offset:13440\n"
  "v_mfma_f32_16x16x32_bf16 a[244:247], a[160:163], a[120:123], a[244:247]\n"
  "buffer_load_dwordx4 v221, s[44:47], 0 offen offset:128 lds\n"
  "ds_read_b128 a[52:55], v222 offset:13504\n"
  "v_mfma_f32_16x16x32_bf16 a[248:251], a[160:163], a[124:127], a[248:251]\n"
  "s_addk_i32 m0, 896\n"
  "ds_read_b128 a[56:59], v222 offset:15680\n"
  "ds_read_b128 a[60:63], v222 offset:15744\n"
  "v_mfma_f32_16x16x32_bf16 v[200:203], v[60:63], a[120:123], v[200:203]\n"
  "ds_read_b64_tr_b16 a[136:137], v217 offset:24704\n"
  "v_maximum3_f32 v236, v68, v69, v70\n"
  "v_mfma_f32_16x16x32_bf16 v[204:207], v[60:63], a[124:127], v[204:207]\n"
  "ds_read_b64_tr_b16 a[138:139], v218 offset:24704\n"
  "v_maximum3_f32 v236, v236, v71, v72\n"
  "v_mfma_f32_16x16x32_bf16 v[168:171], v[44:47], a[120:123], v[168:171]\n"
  "buffer_load_dwordx4 v221, s[44:47], 0 offen offset:256 lds\n"
  "v_maximum3_f32 v236, v236, v73, v74\n"
  "v_mfma_f32_16x16x32_bf16 v[172:175], v[44:47], a[124:127], v[172:175]\n"
  "ds_read_b64_tr_b16 a[140:141], v217 offset:25728\n"
  "v_maximum3_f32 v236, v236, v75, v76\n"
  "v_mfma_f32_16x16x32_bf16 v[136:139], v[28:31], a[120:123], v[136:139]\n"
  "ds_read_b64_tr_b16 a[142:143], v218 offset:25728\n"
  "v_maximum3_f32 v236, v236, v77, v78\n"
  "v_mfma_f32_16x16x32_bf16 v[140:143], v[28:31], a[124:127], v[140:143]\n"
  "v_maximum3_f32 v236, v236, v79, v80\n"
  "v_maximum3_f32 v236, v236, v81, v82\n"
  "v_mfma_f32_16x16x32_bf16 v[104:107], v[12:15], a[120:123], v[104:107]\n"
  "v_maximum3_f32 v236, v236, v83, v84\n"
  "v_maximum3_f32 v236, v236, v85, v86\n"
  "v_mfma_f32_16x16x32_bf16 v[108:111], v[12:15], a[124:127], v[108:111]\n"
  "s_waitcnt lgkmcnt(10)\n"
  "v_rcp_f32 v84, v84\n"
  "v_mfma_f32_16x16x32_bf16 v[112:115], v[12:15], a[132:135], v[112:115]\n"
  "s_add_u32 s40, s40, 24576\n"
  "v_rcp_f32 v85, v85\n"
  "v_mfma_f32_16x16x32_bf16 v[116:119], v[12:15], a[128:131], v[116:119]\n"
  "s_addc_u32 s41, s41, 0\n"
  "v_accvgpr_read_b32 v240, a244\n"
  "v_accvgpr_read_b32 v241, a245\n"
  "v_mfma_f32_16x16x32_bf16 v[144:147], v[28:31], a[132:135], v[144:147]\n"
  "ds_read_b64_tr_b16 a[120:121], v217 offset:24720\n"
  "v_accvgpr_read_b32 v242, a248\n"
  "v_mfma_f32_16x16x32_bf16 v[148:151], v[28:31], a[128:131], v[148:151]\n"
  "ds_read_b64_tr_b16 a[122:123], v218 offset:24720\n"
  "v_accvgpr_read_b32 v243, a249\n"
  "v_mfma_f32_16x16x32_bf16 v[176:179], v[44:47], a[132:135], v[176:179]\n"
  "ds_read_b64_tr_b16 a[124:125], v217 offset:25744\n"
  "v_maximum3_f32 v236, v236, v87, v87\n"
  "v_mfma_f32_16x16x32_bf16 v[180:183], v[44:47], a[128:131], v[180:183]\n"
  "ds_read_b64_tr_b16 a[126:127], v218 offset:25744\n"
  "v_cmp_ngt_f32 vcc, s75, v236\n"
  "v_mfma_f32_16x16x32_bf16 v[208:211], v[60:63], a[132:135], v[208:211]\n"
  "s_mov_b32 s75, s65\n"
  "v_rcp_f32 v68, v68\n"
  "v_mfma_f32_16x16x32_bf16 v[212:215], v[60:63], a[128:131], v[212:215]\n"
  "s_waitcnt lgkmcnt(4)\n"
  "v_rcp_f32 v69, v69\n"
  "v_mfma_f32_16x16x32_bf16 a[228:231], a[156:159], a[136:139], a[228:231]\n"
  "s_add_u32 s32, s44, s31\n"
  "v_rcp_f32 v70, v70\n"
  "v_mfma_f32_16x16x32_bf16 a[228:231], a[160:163], a[140:143], a[228:231]\n"
  "s_addc_u32 s33, s45, 0\n"
  "v_rcp_f32 v71, v71\n"
  "v_mfma_f32_16x16x32_bf16 a[212:215], v[56:59], a[136:139], a[212:215]\n"
  "s_add_u32 s44, s44, 24576\n"
  "v_rcp_f32 v72, v72\n"
  "v_mfma_f32_16x16x32_bf16 a[212:215], v[60:63], a[140:143], a[212:215]\n"
  "s_addc_u32 s45, s45, 0\n"
  "v_rcp_f32 v73, v73\n"
  "v_mfma_f32_16x16x32_bf16 a[196:199], v[40:43], a[136:139], a[196:199]\n"
  "ds_read_b128 a[132:135], v216 offset:24704\n"
  "v_accvgpr_read_b32 v236, a246\n"
  "v_mfma_f32_16x16x32_bf16 a[196:199], v[44:47], a[140:143], a[196:199]\n"
  "ds_read_b128 a[128:131], v216 offset:24768\n"
  "v_accvgpr_read_b32 v237, a247\n"
  "v_mfma_f32_16x16x32_bf16 a[180:183], v[24:27], a[136:139], a[180:183]\n"
  "ds_read_b128 a[148:151], v216 offset:25728\n"
  "v_accvgpr_read_b32 v238, a250\n"
  "v_mfma_f32_16x16x32_bf16 a[180:183], v[28:31], a[140:143], a[180:183]\n"
  "ds_read_b128 a[144:147], v216 offset:25792\n"
  "v_add_u32 v233, s61, v229\n"
  "s_mov_b32 exec_lo, 0\n"
  "v_add_u32 v233, s69, v233\n"
  "s_mov_b32 exec_lo, -1\n"
  "v_rcp_f32_e64 v74, v74\n"
  "v_pk_fma_f32 v[240:241], v[240:241], v[84:85], v[84:85] neg_lo:[0,0,1] neg_hi:[0,0,1]\n"
  "v_pk_fma_f32 v[242:243], v[242:243], v[84:85], v[84:85] neg_lo:[0,0,1] neg_hi:[0,0,1]\n"
  "v_pk_fma_f32 v[104:105], v[104:105], v[68:69], v[68:69] neg_lo:[0,0,1] neg_hi:[0,0,1]\n"
  "v_pk_fma_f32 v[108:109], v[108:109], v[68:69], v[68:69] neg_lo:[0,0,1] neg_hi:[0,0,1]\n"
  "v_mfma_f32_16x16x32_bf16 a[164:167], v[8:11], a[136:139], a[164:167]\n"
  "s_nop 0\n"
  "ds_read_b128 a[136:139], v216 offset:26752\n"
  "v_cvt_pk_bf16_f32 v240, v240, v242\n"
  "v_mfma_f32_16x16x32_bf16 a[164:167], v[12:15], a[140:143], a[164:167]\n"
  "s_waitcnt lgkmcnt(5)\n"
  "ds_read_b128 a[140:143], v216 offset:26816\n"
  "v_cvt_pk_bf16_f32 v241, v241, v243\n"
  "v_cvt_pk_bf16_f32 v104, v104, v108\n"
  "v_rcp_f32_e64 v75, v75\n"
  "v_pk_fma_f32 v[112:113], v[112:113], v[68:69], v[68:69] neg_lo:[0,0,1] neg_hi:[0,0,1]\n"
  "v_pk_fma_f32 v[116:117], v[116:117], v[68:69], v[68:69] neg_lo:[0,0,1] neg_hi:[0,0,1]\n"
  "v_pk_fma_f32 v[106:107], v[106:107], v[70:71], v[70:71] neg_lo:[0,0,1] neg_hi:[0,0,1]\n"
  "v_pk_fma_f32 v[110:111], v[110:111], v[70:71], v[70:71] neg_lo:[0,0,1] neg_hi:[0,0,1]\n"
  "v_mfma_f32_16x16x32_bf16 a[168:171], v[8:11], a[120:123], a[168:171]\n"
  "s_cmp_lg_u64 vcc, 0\n"
  "s_cselect_b32 s79, s77, 0\n"
  "s_or_b32 s78, s78, s79\n"
  "s_lshl_b32 s77, s77, 1\n"
  "v_rcp_f32 v76, v76\n"
  "v_mfma_f32_16x16x32_bf16 a[168:171], v[12:15], a[124:127], a[168:171]\n"
  "v_cvt_pk_bf16_f32 v108, v105, v109\n"
  "v_rcp_f32 v77, v77\n"
  "v_rcp_f32_e64 v78, v78\n"
  "v_pk_fma_f32 v[114:115], v[114:115], v[70:71], v[70:71] neg_lo:[0,0,1] neg_hi:[0,0,1]\n"
  "v_pk_fma_f32 v[118:119], v[118:119], v[70:71], v[70:71] neg_lo:[0,0,1] neg_hi:[0,0,1]\n"
  "v_pk_fma_f32 v[136:137], v[136:137], v[72:73], v[72:73] neg_lo:[0,0,1] neg_hi:[0,0,1]\n"
  "v_pk_fma_f32 v[140:141], v[140:141], v[72:73], v[72:73] neg_lo:[0,0,1] neg_hi:[0,0,1]\n"
  "v_mfma_f32_16x16x32_bf16 a[184:187], v[24:27], a[120:123], a[184:187]\n"
  "v_rcp_f32_e64 v79, v79\n"
  "v_mfma_f32_16x16x32_bf16 a[184:187], v[28:31], a[124:127], a[184:187]\n"
  "v_cvt_pk_bf16_f32 v105, v112, v116\n"
  "v_rcp_f32_e64 v80, v80\n"
  "v_rcp_f32_e64 v81, v81\n"
  "v_pk_fma_f32 v[144:145], v[144:145], v[72:73], v[72:73] neg_lo:[0,0,1] neg_hi:[0,0,1]\n"
  "v_pk_fma_f32 v[148:149], v[148:149], v[72:73], v[72:73] neg_lo:[0,0,1] neg_hi:[0,0,1]\n"
  "v_pk_fma_f32 v[138:139], v[138:139], v[74:75], v[74:75] neg_lo:[0,0,1] neg_hi:[0,0,1]\n"
  "v_pk_fma_f32 v[142:143], v[142:143], v[74:75], v[74:75] neg_lo:[0,0,1] neg_hi:[0,0,1]\n"
  "v_mfma_f32_16x16x32_bf16 a[200:203], v[40:43], a[120:123], a[200:203]\n"
  "v_rcp_f32_e64 v82, v82\n"
  "v_mfma_f32_16x16x32_bf16 a[200:203], v[44:47], a[124:127], a[200:203]\n"
  "ds_write_b32 v233, v240 offset:20160\n"
  "v_cvt_pk_bf16_f32 v109, v113, v117\n"
  "v_rcp_f32_e64 v83, v83\n"
  "v_pk_fma_f32 v[146:147], v[146:147], v[74:75], v[74:75] neg_lo:[0,0,1] neg_hi:[0,0,1]\n"
  "v_pk_fma_f32 v[150:151], v[150:151], v[74:75], v[74:75] neg_lo:[0,0,1] neg_hi:[0,0,1]\n"
  "v_pk_fma_f32 v[168:169], v[168:169], v[76:77], v[76:77] neg_lo:[0,0,1] neg_hi:[0,0,1]\n"
  "v_pk_fma_f32 v[172:173], v[172:173], v[76:77], v[76:77] neg_lo:[0,0,1] neg_hi:[0,0,1]\n"
  "v_mfma_f32_16x16x32_bf16 a[216:219], v[56:59], a[120:123], a[216:219]\n"
  "ds_write_b32 v233, v241 offset:20448\n"
  "v_mfma_f32_16x16x32_bf16 a[216:219], v[60:63], a[124:127], a[216:219]\n"
  "ds_write_b64 v229, v[104:105] offset:8960\n"
  "v_cvt_pk_bf16_f32 v106, v106, v110\n"
  "v_cvt_pk_bf16_f32 v110, v107, v111\n"
  "v_pk_fma_f32 v[176:177], v[176:177], v[76:77], v[76:77] neg_lo:[0,0,1] neg_hi:[0,0,1]\n"
  "v_pk_fma_f32 v[180:181], v[180:181], v[76:77], v[76:77] neg_lo:[0,0,1] neg_hi:[0,0,1]\n"
  "v_pk_fma_f32 v[170:171], v[170:171], v[78:79], v[78:79] neg_lo:[0,0,1] neg_hi:[0,0,1]\n"
  "v_pk_fma_f32 v[174:175], v[174:175], v[78:79], v[78:79] neg_lo:[0,0,1] neg_hi:[0,0,1]\n"
  "v_mfma_f32_16x16x32_bf16 a[232:235], a[156:159], a[120:123], a[232:235]\n"
  "s_nop 0\n"
  "v_cvt_pk_bf16_f32 v107, v114, v118\n"
  "v_cvt_pk_bf16_f32 v111, v115, v119\n"
  "v_mfma_f32_16x16x32_bf16 a[232:235], a[160:163], a[124:127], a[232:235]\n"
  "s_waitcnt lgkmcnt(7)\n"
  "ds_write_b64 v229, v[108:109] offset:9248\n"
  "v_cvt_pk_bf16_f32 v136, v136, v140\n"
  "v_cvt_pk_bf16_f32 v140, v137, v141\n"
  "v_pk_fma_f32 v[178:179], v[178:179], v[78:79], v[78:79] neg_lo:[0,0,1] neg_hi:[0,0,1]\n"
  "v_pk_fma_f32 v[182:183], v[182:183], v[78:79], v[78:79] neg_lo:[0,0,1] neg_hi:[0,0,1]\n"
  "v_pk_fma_f32 v[200:201], v[200:201], v[80:81], v[80:81] neg_lo:[0,0,1] neg_hi:[0,0,1]\n"
  "v_pk_fma_f32 v[204:205], v[204:205], v[80:81], v[80:81] neg_lo:[0,0,1] neg_hi:[0,0,1]\n"
  "v_mfma_f32_16x16x32_bf16 v[64:67], a[132:135], a[96:99], v[248:251]\n"
  "v_cvt_pk_bf16_f32 v137, v144, v148\n"
  "v_cvt_pk_bf16_f32 v141, v145, v149\n"
  "v_mfma_f32_16x16x32_bf16 v[64:67], a[128:131], a[100:103], v[64:67]\n"
  "s_waitcnt lgkmcnt(6)\n"
  "v_cvt_pk_bf16_f32 v138, v138, v142\n"
  "v_cvt_pk_bf16_f32 v142, v139, v143\n"
  "v_mfma_f32_16x16x32_bf16 v[64:67], a[148:151], a[104:107], v[64:67]\n"
  "s_waitcnt vmcnt(12)\n"
  "ds_read_b128 a[64:67], v222 offset:0\n"
  "ds_read_b128 a[68:71], v222 offset:64\n"
  "v_mfma_f32_16x16x32_bf16 v[64:67], a[144:147], a[108:111], v[64:67]\n"
  "s_waitcnt lgkmcnt(6)\n"
  "v_cvt_pk_bf16_f32 v139, v146, v150\n"
  "v_cvt_pk_bf16_f32 v143, v147, v151\n"
  "v_mfma_f32_16x16x32_bf16 v[64:67], a[136:139], a[112:115], v[64:67]\n"
  "s_nop 0\n"
  "v_cvt_pk_bf16_f32 v168, v168, v172\n"
  "v_cvt_pk_bf16_f32 v172, v169, v173\n"
  "v_mfma_f32_16x16x32_bf16 v[64:67], a[140:143], a[116:119], v[64:67]\n"
  "ds_write_b64 v229, v[106:107] offset:9536\n"
  "v_cvt_pk_bf16_f32 v169, v176, v180\n"
  "v_cvt_pk_bf16_f32 v173, v177, v181\n"
  "v_pk_fma_f32 v[208:209], v[208:209], v[80:81], v[80:81] neg_lo:[0,0,1] neg_hi:[0,0,1]\n"
  "v_pk_fma_f32 v[212:213], v[212:213], v[80:81], v[80:81] neg_lo:[0,0,1] neg_hi:[0,0,1]\n"
  "v_pk_fma_f32 v[202:203], v[202:203], v[82:83], v[82:83] neg_lo:[0,0,1] neg_hi:[0,0,1]\n"
  "v_pk_fma_f32 v[206:207], v[206:207], v[82:83], v[82:83] neg_lo:[0,0,1] neg_hi:[0,0,1]\n"
  "v_mfma_f32_16x16x32_bf16 v[0:3], a[140:143], a[20:23], v[248:251]\n"
  "v_cvt_pk_bf16_f32 v170, v170, v174\n"
  "v_cvt_pk_bf16_f32 v174, v171, v175\n"
  "v_mfma_f32_16x16x32_bf16 v[0:3], a[136:139], a[16:19], v[0:3]\n"
  "v_cvt_pk_bf16_f32 v171, v178, v182\n"
  "v_cvt_pk_bf16_f32 v175, v179, v183\n"
  "v_mfma_f32_16x16x32_bf16 v[0:3], a[144:147], a[12:15], v[0:3]\n"
  "s_waitcnt vmcnt(10)\n"
  "ds_read_b128 a[72:75], v222 offset:2240\n"
  "ds_read_b128 a[76:79], v222 offset:2304\n"
  "v_mfma_f32_16x16x32_bf16 v[0:3], a[148:151], a[8:11], v[0:3]\n"
  "s_waitcnt vmcnt(8)\n"
  "ds_read_b128 a[80:83], v222 offset:4480\n"
  "ds_read_b128 a[84:87], v222 offset:4544\n"
  "v_mfma_f32_16x16x32_bf16 v[0:3], a[128:131], a[4:7], v[0:3]\n"
  "s_waitcnt vmcnt(6)\n"
  "ds_read_b128 a[88:91], v222 offset:6720\n"
  "ds_read_b128 a[92:95], v222 offset:6784\n"
  "v_mfma_f32_16x16x32_bf16 v[0:3], a[132:135], a[0:3], v[0:3]\n"
  "s_nop 0\n"
  "ds_write_b64 v229, v[110:111] offset:9824\n"
  "v_max3_f32 v227, v64, v65, v66\n"
  "v_cvt_pk_bf16_f32 v200, v200, v204\n"
  "v_pk_fma_f32 v[210:211], v[210:211], v[82:83], v[82:83] neg_lo:[0,0,1] neg_hi:[0,0,1]\n"
  "v_pk_fma_f32 v[214:215], v[214:215], v[82:83], v[82:83] neg_lo:[0,0,1] neg_hi:[0,0,1]\n"
  "v_mfma_f32_16x16x32_bf16 v[16:19], a[132:135], a[24:27], v[248:251]\n"
  "v_rcp_f32 v86, v86\n"
  "v_mfma_f32_16x16x32_bf16 v[16:19], a[128:131], a[28:31], v[16:19]\n"
  "ds_read_b64_tr_b16 a[252:253], v230 offset:8960\n"
  "ds_read_b64_tr_b16 a[254:255], v230 offset:8992\n"
  "v_mfma_f32_16x16x32_bf16 v[16:19], a[148:151], a[32:35], v[16:19]\n"
  "ds_read_b128 a[120:123], v235 offset:24704\n"
  "v_max3_f32 v223, v0, v1, v2\n"
  "v_mfma_f32_16x16x32_bf16 v[16:19], a[144:147], a[36:39], v[16:19]\n"
  "ds_read_b128 a[124:127], v235 offset:24768\n"
  "v_max_f32 v223, v223, v3\n"
  "v_mfma_f32_16x16x32_bf16 v[16:19], a[136:139], a[40:43], v[16:19]\n"
  "v_mul_f32 v223, s74, v223\n"
  "v_add_f32 v223, s64, v223\n"
  "v_mfma_f32_16x16x32_bf16 v[16:19], a[140:143], a[44:47], v[16:19]\n"
  "s_waitcnt lgkmcnt(12)\n"
  "ds_bpermute_b32 v223, v234, v223\n"
  "ds_write_b64 v229, v[136:137] offset:11200\n"
  "v_mfma_f32_16x16x32_bf16 v[32:35], a[140:143], a[68:71], v[248:251]\n"
  "ds_read_b64_tr_b16 v[244:245], v230 offset:10080\n"
  "ds_read_b64_tr_b16 v[246:247], v230 offset:10112\n"
  "v_mfma_f32_16x16x32_bf16 v[32:35], a[136:139], a[64:67], v[32:35]\n"
  "v_rcp_f32 v87, v87\n"
  "v_mfma_f32_16x16x32_bf16 v[32:35], a[144:147], a[60:63], v[32:35]\n"
  "v_max3_f32 v224, v16, v17, v18\n"
  "v_max_f32 v224, v224, v19\n"
  "v_mfma_f32_16x16x32_bf16 v[32:35], a[148:151], a[56:59], v[32:35]\n"
  "v_mul_f32 v224, s74, v224\n"
  "v_add_f32_e64 v224, s64, v224\n"
  "v_mfma_f32_16x16x32_bf16 v[32:35], a[128:131], a[52:55], v[32:35]\n"
  "ds_bpermute_b32 v224, v234, v224\n"
  "v_mfma_f32_16x16x32_bf16 v[32:35], a[132:135], a[48:51], v[32:35]\n"
  "s_waitcnt lgkmcnt(10)\n"
  "s_waitcnt lgkmcnt(7)\n"
  "buffer_store_dwordx4 a[252:255], v228, s[52:55], s80 offen offset:256 nt\n"
  "ds_write_b64 v229, v[140:141] offset:11488\n"
  "v_cvt_pk_bf16_f32 v204, v201, v205\n"
  "v_cvt_pk_bf16_f32 v201, v208, v212\n"
  "v_cvt_pk_bf16_f32 v205, v209, v213\n"
  "v_pk_fma_f32 v[236:237], v[236:237], v[86:87], v[86:87] neg_lo:[0,0,1] neg_hi:[0,0,1]\n"
  "v_mfma_f32_16x16x32_bf16 v[48:51], a[132:135], a[72:75], v[248:251]\n"
  "s_waitcnt lgkmcnt(5)\n"
  "ds_read_b128 a[132:135], v235 offset:25728\n"
  "v_fma_f32 v0, v0, s73, v223\n"
  "v_mfma_f32_16x16x32_bf16 v[48:51], a[128:131], a[76:79], v[48:51]\n"
  "ds_read_b128 a[128:131], v235 offset:25792\n"
  "v_fma_f32 v1, v1, s73, v223\n"
  "v_mfma_f32_16x16x32_bf16 v[48:51], a[148:151], a[80:83], v[48:51]\n"
  "ds_read_b128 a[148:151], v235 offset:26752\n"
  "v_fma_f32 v2, v2, s73, v223\n"
  "v_mfma_f32_16x16x32_bf16 v[48:51], a[144:147], a[84:87], v[48:51]\n"
  "ds_read_b128 a[144:147], v235 offset:26816\n"
  "v_fma_f32 v3, v3, s73, v223\n"
  "v_mfma_f32_16x16x32_bf16 v[48:51], a[136:139], a[88:91], v[48:51]\n"
  "s_waitcnt lgkmcnt(5)\n"
  "v_exp_f32 v0, v0\n"
  "v_mfma_f32_16x16x32_bf16 v[48:51], a[140:143], a[92:95], v[48:51]\n"
  "buffer_store_dwordx4 v[244:247], v228, s[52:55], s80 offen offset:3328 nt\n"
  "ds_write_b64 v229, v[138:139] offset:11776\n"
  "v_exp_f32 v1, v1\n"
  "v_max3_f32 v225, v32, v33, v34\n"
  "v_mfma_f32_16x16x32_bf16 v[240:243], a[120:123], a[96:99], v[248:251]\n"
  "v_exp_f32 v2, v2\n"
  "v_mfma_f32_16x16x32_bf16 v[240:243], a[124:127], a[100:103], v[240:243]\n"
  "s_waitcnt lgkmcnt(3)\n"
  "v_exp_f32 v3, v3\n"
  "v_mfma_f32_16x16x32_bf16 v[240:243], a[132:135], a[104:107], v[240:243]\n"
  "v_max_f32 v225, v225, v35\n"
  "v_mul_f32 v225, s74, v225\n"
  "v_mfma_f32_16x16x32_bf16 v[240:243], a[128:131], a[108:111], v[240:243]\n"
  "s_waitcnt lgkmcnt(1)\n"
  "v_add_f32 v225, s64, v225\n"
  "v_fma_f32 v16, v16, s73, v224\n"
  "v_mfma_f32_16x16x32_bf16 v[240:243], a[148:151], a[112:115], v[240:243]\n"
  "v_exp_f32 v16, v16\n"
  "v_mfma_f32_16x16x32_bf16 v[240:243], a[144:147], a[116:119], v[240:243]\n"
  "ds_bpermute_b32 v225, v234, v225\n"
  "ds_write_b64 v229, v[142:143] offset:12064\n"
  "v_mfma_f32_16x16x32_bf16 v[4:7], a[144:147], a[20:23], v[248:251]\n"
  "v_fma_f32 v17, v17, s73, v224\n"
  "v_fma_f32 v18, v18, s73, v224\n"
  "v_mfma_f32_16x16x32_bf16 v[4:7], a[148:151], a[16:19], v[4:7]\n"
  "v_exp_f32 v17, v17\n"
  "v_mfma_f32_16x16x32_bf16 v[4:7], a[128:131], a[12:15], v[4:7]\n"
  "v_max3_f32 v227, v227, v67, v240\n"
  "v_max3_f32 v227, v227, v241, v242\n"
  "v_mfma_f32_16x16x32_bf16 v[4:7], a[132:135], a[8:11], v[4:7]\n"
  "v_max_f32 v227, v227, v243\n"
  "v_mul_f32 v227, s74, v227\n"
  "v_mfma_f32_16x16x32_bf16 v[4:7], a[124:127], a[4:7], v[4:7]\n"
  "v_add_f32_e64 v227, s64, v227\n"
  "v_fma_f32 v19, v19, s73, v224\n"
  "v_mfma_f32_16x16x32_bf16 v[4:7], a[120:123], a[0:3], v[4:7]\n"
  "s_waitcnt lgkmcnt(1)\n"
  "ds_bpermute_b32 v227, v234, v227\n"
  "ds_write_b64 v229, v[168:169] offset:13440\n"
  "v_mfma_f32_16x16x32_bf16 v[20:23], a[120:123], a[24:27], v[248:251]\n"
  "v_exp_f32 v18, v18\n"
  "v_mfma_f32_16x16x32_bf16 v[20:23], a[124:127], a[28:31], v[20:23]\n"
  "v_exp_f32 v19, v19\n"
  "v_mfma_f32_16x16x32_bf16 v[20:23], a[132:135], a[32:35], v[20:23]\n"
  "ds_read_b64_tr_b16 a[136:137], v219 offset:24704\n"
  "v_max3_f32 v226, v48, v49, v50\n"
  "v_mfma_f32_16x16x32_bf16 v[20:23], a[128:131], a[36:39], v[20:23]\n"
  "ds_read_b64_tr_b16 a[138:139], v220 offset:24704\n"
  "v_max_f32 v226, v226, v51\n"
  "v_mfma_f32_16x16x32_bf16 v[20:23], a[148:151], a[40:43], v[20:23]\n"
  "ds_read_b64_tr_b16 a[140:141], v219 offset:25728\n"
  "v_mul_f32_e64 v226, s74, v226\n"
  "v_mfma_f32_16x16x32_bf16 v[20:23], a[144:147], a[44:47], v[20:23]\n"
  "s_waitcnt lgkmcnt(4)\n"
  "ds_read_b64_tr_b16 a[142:143], v220 offset:25728\n"
  "ds_write_b64 v229, v[172:173] offset:13728\n"
  "v_fma_f32 v64, v64, s73, v227\n"
  "v_mfma_f32_16x16x32_bf16 v[36:39], a[144:147], a[68:71], v[248:251]\n"
  "v_exp_f32 v64, v64\n"
  "v_mfma_f32_16x16x32_bf16 v[36:39], a[148:151], a[64:67], v[36:39]\n"
  "v_fma_f32 v65, v65, s73, v227\n"
  "v_fma_f32 v66, v66, s73, v227\n"
  "v_mfma_f32_16x16x32_bf16 v[36:39], a[128:131], a[60:63], v[36:39]\n"
  "v_exp_f32 v65, v65\n"
  "v_mfma_f32_16x16x32_bf16 v[36:39], a[132:135], a[56:59], v[36:39]\n"
  "v_exp_f32_e64 v66, v66\n"
  "v_mfma_f32_16x16x32_bf16 v[36:39], a[124:127], a[52:55], v[36:39]\n"
  "v_fma_f32 v67, v67, s73, v227\n"
  "v_cvt_pk_bf16_f32 v64, v64, v65\n"
  "v_mfma_f32_16x16x32_bf16 v[36:39], a[120:123], a[48:51], v[36:39]\n"
  "ds_write_b64 v229, v[170:171] offset:14016\n"
  "v_exp_f32_e64 v67, v67\n"
  "v_mfma_f32_16x16x32_bf16 v[52:55], a[120:123], a[72:75], v[248:251]\n"
  "ds_read_b64_tr_b16 a[120:121], v219 offset:24720\n"
  "v_cvt_pk_bf16_f32 v65, v66, v67\n"
  "v_mfma_f32_16x16x32_bf16 v[52:55], a[124:127], a[76:79], v[52:55]\n"
  "ds_read_b64_tr_b16 a[122:123], v220 offset:24720\n"
  "v_add_f32 v226, s64, v226\n"
  "v_mfma_f32_16x16x32_bf16 v[52:55], a[132:135], a[80:83], v[52:55]\n"
  "ds_read_b64_tr_b16 a[124:125], v219 offset:25744\n"
  "v_fma_f32 v32, v32, s73, v225\n"
  "v_mfma_f32_16x16x32_bf16 v[52:55], a[128:131], a[84:87], v[52:55]\n"
  "ds_read_b64_tr_b16 a[126:127], v220 offset:25744\n"
  "v_fma_f32 v33, v33, s73, v225\n"
  "v_mfma_f32_16x16x32_bf16 v[52:55], a[148:151], a[88:91], v[52:55]\n"
  "v_exp_f32_e64 v32, v32\n"
  "v_mfma_f32_16x16x32_bf16 v[52:55], a[144:147], a[92:95], v[52:55]\n"
  "s_waitcnt lgkmcnt(6)\n"
  "ds_read_b64_tr_b16 a[252:253], v230 offset:11200\n"
  "ds_read_b64_tr_b16 a[254:255], v230 offset:11232\n"
  "ds_write_b64 v231, v[64:65] offset:0\n"
  "v_mfma_f32_16x16x32_bf16 a[220:223], v[56:59], a[136:139], a[220:223]\n"
  "v_exp_f32_e64 v33, v33\n"
  "v_mfma_f32_16x16x32_bf16 a[220:223], v[60:63], a[140:143], a[220:223]\n"
  "ds_bpermute_b32 v226, v234, v226\n"
  "ds_write_b64 v229, v[174:175] offset:14304\n"
  "v_mfma_f32_16x16x32_bf16 a[204:207], v[40:43], a[136:139], a[204:207]\n"
  "v_fma_f32 v34, v34, s73, v225\n"
  "v_fma_f32 v35, v35, s73, v225\n"
  "v_mfma_f32_16x16x32_bf16 a[204:207], v[44:47], a[140:143], a[204:207]\n"
  "s_waitcnt vmcnt(2) lgkmcnt(2)\n"
  "s_barrier\n"
  "ds_read_b64_tr_b16 a[132:133], v217 offset:12352\n"
  "ds_read_b64_tr_b16 v[244:245], v230 offset:12320\n"
  "ds_read_b64_tr_b16 v[246:247], v230 offset:12352\n"
  "v_mfma_f32_16x16x32_bf16 a[188:191], v[24:27], a[136:139], a[188:191]\n"
  "ds_read_b64 a[156:157], v231 offset:0\n"
  "v_fma_f32 v4, v4, s73, v223\n"
  "v_mfma_f32_16x16x32_bf16 a[188:191], v[28:31], a[140:143], a[188:191]\n"
  "buffer_store_dwordx4 a[252:255], v228, s[52:55], s81 offen offset:256 nt\n"
  "ds_read_b64 a[158:159], v232 offset:0\n"
  "ds_write_b64 v229, v[200:201] offset:15680\n"
  "v_exp_f32_e64 v34, v34\n"
  "v_mfma_f32_16x16x32_bf16 a[172:175], v[8:11], a[136:139], a[172:175]\n"
  "ds_read_b64_tr_b16 a[134:135], v218 offset:12352\n"
  "v_fma_f32 v5, v5, s73, v223\n"
  "v_mfma_f32_16x16x32_bf16 a[172:175], v[12:15], a[140:143], a[172:175]\n"
  "ds_read_b64_tr_b16 a[128:129], v217 offset:12368\n"
  "ds_write_b64 v229, v[204:205] offset:15968\n"
  "v_fma_f32 v6, v6, s73, v223\n"
  "v_mfma_f32_16x16x32_bf16 a[176:179], v[8:11], a[120:123], a[176:179]\n"
  "s_waitcnt lgkmcnt(10)\n"
  "ds_read_b64_tr_b16 a[130:131], v218 offset:12368\n"
  "v_cvt_pk_bf16_f32 v8, v0, v1\n"
  "v_mfma_f32_16x16x32_bf16 a[176:179], v[12:15], a[124:127], a[176:179]\n"
  "s_nop 0\n"
  "ds_read_b64_tr_b16 a[252:253], v230 offset:13440\n"
  "ds_read_b64_tr_b16 a[254:255], v230 offset:13472\n"
  "v_cvt_pk_bf16_f32 v9, v2, v3\n"
  "v_fma_f32 v48, v48, s73, v226\n"
  "v_cvt_pk_bf16_f32 v202, v202, v206\n"
  "v_mfma_f32_16x16x32_bf16 a[192:195], v[24:27], a[120:123], a[192:195]\n"
  "ds_read_b128 a[136:139], v216 offset:12352\n"
  "v_cvt_pk_bf16_f32 v24, v16, v17\n"
  "v_mfma_f32_16x16x32_bf16 a[192:195], v[28:31], a[124:127], a[192:195]\n"
  "s_waitcnt lgkmcnt(10)\n"
  "buffer_store_dwordx4 v[244:247], v228, s[52:55], s81 offen offset:3328 nt\n"
  "ds_read_b128 a[140:143], v216 offset:12416\n"
  "v_cvt_pk_bf16_f32 v25, v18, v19\n"
  "v_exp_f32 v35, v35\n"
  "v_cvt_pk_bf16_f32 v206, v203, v207\n"
  "v_cvt_pk_bf16_f32 v203, v210, v214\n"
  "v_mfma_f32_16x16x32_bf16 a[208:211], v[40:43], a[120:123], a[208:211]\n"
  "ds_read_b128 a[148:151], v216 offset:13376\n"
  "v_cvt_pk_bf16_f32 v40, v32, v33\n"
  "v_mfma_f32_16x16x32_bf16 a[208:211], v[44:47], a[124:127], a[208:211]\n"
  "ds_read_b128 a[144:147], v216 offset:13440\n"
  "ds_write_b64 v229, v[202:203] offset:16256\n"
  "v_cvt_pk_bf16_f32 v41, v34, v35\n"
  "v_mfma_f32_16x16x32_bf16 a[224:227], v[56:59], a[120:123], a[224:227]\n"
  "s_nop 0\n"
  "ds_read_b128 a[120:123], v216 offset:14400\n"
  "v_fma_f32 v49, v49, s73, v226\n"
  "v_mfma_f32_16x16x32_bf16 a[224:227], v[60:63], a[124:127], a[224:227]\n"
  "L_JOIN_%=:\n"
  "s_waitcnt lgkmcnt(8)\n"
  "ds_read_b128 a[124:127], v216 offset:14464\n"
  "ds_read_b64_tr_b16 v[244:245], v230 offset:14560\n"
  "ds_read_b64_tr_b16 v[246:247], v230 offset:14592\n"
  "v_fma_f32 v50, v50, s73, v226\n"
  "v_cvt_pk_bf16_f32 v207, v211, v215\n"
  "v_mfma_f32_16x16x32_bf16 a[236:239], a[160:163], a[132:135], a[236:239]\n"
  "s_add_i32 s76, s76, 1\n"
  "v_exp_f32_e64 v48, v48\n"
  "v_mfma_f32_16x16x32_bf16 a[240:243], a[160:163], a[128:131], a[240:243]\n"
  "s_waitcnt lgkmcnt(9)\n"
  "buffer_store_dwordx4 a[252:255], v228, s[52:55], s82 offen offset:256 nt\n"
  "ds_write_b64 v229, v[206:207] offset:16544\n"
  "v_exp_f32 v49, v49\n"
  "v_fma_f32 v51, v51, s73, v226\n"
  "v_mfma_f32_16x16x32_bf16 v[184:187], v[60:63], a[132:135], v[184:187]\n"
  "v_exp_f32 v50, v50\n"
  "v_accvgpr_read_b32 v239, a251\n"
  "v_accvgpr_read_b32 v136, a180\n"
  "v_accvgpr_read_b32 v137, a181\n"
  "v_pk_fma_f32 v[136:137], v[136:137], v[72:73], v[72:73] neg_lo:[0,0,1] neg_hi:[0,0,1]\n"
  "v_pk_fma_f32 v[238:239], v[238:239], v[86:87], v[86:87] neg_lo:[0,0,1] neg_hi:[0,0,1]\n"
  "v_mfma_f32_16x16x32_bf16 v[188:191], v[60:63], a[128:131], v[188:191]\n"
  "v_exp_f32_e64 v51, v51\n"
  "v_mfma_f32_16x16x32_bf16 v[152:155], v[44:47], a[132:135], v[152:155]\n"
  "ds_read_b64_tr_b16 a[252:253], v230 offset:15680\n"
  "ds_read_b64_tr_b16 a[254:255], v230 offset:15712\n"
  "v_cvt_pk_bf16_f32 v56, v48, v49\n"
  "v_cvt_pk_bf16_f32 v57, v50, v51\n"
  "v_accvgpr_read_b32 v140, a184\n"
  "v_mfma_f32_16x16x32_bf16 v[156:159], v[44:47], a[128:131], v[156:159]\n"
  "v_exp_f32_e64 v4, v4\n"
  "v_accvgpr_read_b32 v141, a185\n"
  "v_accvgpr_read_b32 v144, a188\n"
  "v_accvgpr_read_b32 v145, a189\n"
  "v_pk_fma_f32 v[140:141], v[140:141], v[72:73], v[72:73] neg_lo:[0,0,1] neg_hi:[0,0,1]\n"
  "v_pk_fma_f32 v[144:145], v[144:145], v[72:73], v[72:73] neg_lo:[0,0,1] neg_hi:[0,0,1]\n"
  "v_pk_fma_f32 v[184:185], v[184:185], v[80:81], v[80:81] neg_lo:[0,0,1] neg_hi:[0,0,1]\n"
  "v_pk_fma_f32 v[186:187], v[186:187], v[82:83], v[82:83] neg_lo:[0,0,1] neg_hi:[0,0,1]\n"
  "v_mfma_f32_16x16x32_bf16 v[120:123], v[28:31], a[132:135], v[120:123]\n"
  "v_exp_f32_e64 v5, v5\n"
  "v_mfma_f32_16x16x32_bf16 v[124:127], v[28:31], a[128:131], v[124:127]\n"
  "s_waitcnt lgkmcnt(3)\n"
  "buffer_store_dwordx4 v[244:247], v228, s[52:55], s82 offen offset:3328 nt\n"
  "v_exp_f32 v6, v6\n"
  "v_fma_f32 v7, v7, s73, v223\n"
  "v_cvt_pk_bf16_f32 v10, v4, v5\n"
  "v_accvgpr_read_b32 v148, a192\n"
  "v_accvgpr_read_b32 v149, a193\n"
  "v_pk_fma_f32 v[148:149], v[148:149], v[72:73], v[72:73] neg_lo:[0,0,1] neg_hi:[0,0,1]\n"
  "v_pk_fma_f32 v[188:189], v[188:189], v[80:81], v[80:81] neg_lo:[0,0,1] neg_hi:[0,0,1]\n"
  "v_pk_fma_f32 v[190:191], v[190:191], v[82:83], v[82:83] neg_lo:[0,0,1] neg_hi:[0,0,1]\n"
  "v_pk_fma_f32 v[152:153], v[152:153], v[76:77], v[76:77] neg_lo:[0,0,1] neg_hi:[0,0,1]\n"
  "v_mfma_f32_16x16x32_bf16 v[88:91], v[12:15], a[132:135], v[88:91]\n"
  "ds_read_b64_tr_b16 v[244:245], v230 offset:16800\n"
  "ds_read_b64_tr_b16 v[246:247], v230 offset:16832\n"
  "v_exp_f32_e64 v7, v7\n"
  "v_accvgpr_read_b32 v138, a182\n"
  "v_pk_fma_f32 v[156:157], v[156:157], v[76:77], v[76:77] neg_lo:[0,0,1] neg_hi:[0,0,1]\n"
  "v_pk_fma_f32 v[154:155], v[154:155], v[78:79], v[78:79] neg_lo:[0,0,1] neg_hi:[0,0,1]\n"
  "v_pk_fma_f32 v[158:159], v[158:159], v[78:79], v[78:79] neg_lo:[0,0,1] neg_hi:[0,0,1]\n"
  "v_mfma_f32_16x16x32_bf16 v[92:95], v[12:15], a[128:131], v[92:95]\n"
  "v_cvt_pk_bf16_f32 v11, v6, v7\n"
  "v_fma_f32 v20, v20, s73, v224\n"
  "v_accvgpr_read_b32 v139, a183\n"
  "v_accvgpr_read_b32 v142, a186\n"
  "v_accvgpr_read_b32 v143, a187\n"
  "v_pk_fma_f32 v[138:139], v[138:139], v[74:75], v[74:75] neg_lo:[0,0,1] neg_hi:[0,0,1]\n"
  "v_pk_fma_f32 v[142:143], v[142:143], v[74:75], v[74:75] neg_lo:[0,0,1] neg_hi:[0,0,1]\n"
  "v_pk_fma_f32 v[120:121], v[120:121], v[72:73], v[72:73] neg_lo:[0,0,1] neg_hi:[0,0,1]\n"
  "v_pk_fma_f32 v[122:123], v[122:123], v[74:75], v[74:75] neg_lo:[0,0,1] neg_hi:[0,0,1]\n"
  "v_mfma_f32_16x16x32_bf16 v[64:67], a[136:139], a[96:99], v[248:251]\n"
  "v_exp_f32 v20, v20\n"
  "v_mfma_f32_16x16x32_bf16 v[64:67], a[140:143], a[100:103], v[64:67]\n"
  "v_fma_f32 v21, v21, s73, v224\n"
  "v_fma_f32 v22, v22, s73, v224\n"
  "v_mfma_f32_16x16x32_bf16 v[64:67], a[148:151], a[104:107], v[64:67]\n"
  "v_exp_f32 v21, v21\n"
  "v_mfma_f32_16x16x32_bf16 v[64:67], a[144:147], a[108:111], v[64:67]\n"
  "v_exp_f32_e64 v22, v22\n"
  "v_mfma_f32_16x16x32_bf16 v[64:67], a[120:123], a[112:115], v[64:67]\n"
  "v_fma_f32 v23, v23, s73, v224\n"
  "v_cvt_pk_bf16_f32 v26, v20, v21\n"
  "v_mfma_f32_16x16x32_bf16 v[64:67], a[124:127], a[116:119], v[64:67]\n"
  "s_waitcnt lgkmcnt(2)\n"
  "buffer_store_dwordx4 a[252:255], v228, s[52:55], s83 offen offset:256 nt\n"
  "v_exp_f32 v23, v23\n"
  "v_fma_f32 v36, v36, s73, v225\n"
  "v_mfma_f32_16x16x32_bf16 v[0:3], a[124:127], a[20:23], v[248:251]\n"
  "v_exp_f32 v36, v36\n"
  "v_mfma_f32_16x16x32_bf16 v[0:3], a[120:123], a[16:19], v[0:3]\n"
  "v_cvt_pk_bf16_f32 v27, v22, v23\n"
  "v_fma_f32 v37, v37, s73, v225\n"
  "v_mfma_f32_16x16x32_bf16 v[0:3], a[144:147], a[12:15], v[0:3]\n"
  "v_exp_f32 v37, v37\n"
  "v_mfma_f32_16x16x32_bf16 v[0:3], a[148:151], a[8:11], v[0:3]\n"
  "v_fma_f32 v38, v38, s73, v225\n"
  "v_fma_f32 v39, v39, s73, v225\n"
  "v_mfma_f32_16x16x32_bf16 v[0:3], a[140:143], a[4:7], v[0:3]\n"
  "v_exp_f32 v38, v38\n"
  "v_mfma_f32_16x16x32_bf16 v[0:3], a[136:139], a[0:3], v[0:3]\n"
  "s_waitcnt lgkmcnt(0)\n"
  "buffer_store_dwordx4 v[244:247], v228, s[52:55], s83 offen offset:3328 nt\n"
  "v_exp_f32_e64 v39, v39\n"
  "v_cvt_pk_bf16_f32 v42, v36, v37\n"
  "v_fma_f32 v52, v52, s73, v226\n"
  "v_accvgpr_read_b32 v146, a190\n"
  "v_accvgpr_read_b32 v147, a191\n"
  "v_pk_fma_f32 v[146:147], v[146:147], v[74:75], v[74:75] neg_lo:[0,0,1] neg_hi:[0,0,1]\n"
  "v_pk_fma_f32 v[124:125], v[124:125], v[72:73], v[72:73] neg_lo:[0,0,1] neg_hi:[0,0,1]\n"
  "v_pk_fma_f32 v[126:127], v[126:127], v[74:75], v[74:75] neg_lo:[0,0,1] neg_hi:[0,0,1]\n"
  "v_pk_fma_f32 v[88:89], v[88:89], v[68:69], v[68:69] neg_lo:[0,0,1] neg_hi:[0,0,1]\n"
  "v_mfma_f32_16x16x32_bf16 v[16:19], a[136:139], a[24:27], v[248:251]\n"
  "v_exp_f32_e64 v52, v52\n"
  "v_mfma_f32_16x16x32_bf16 v[16:19], a[140:143], a[28:31], v[16:19]\n"
  "v_cvt_pk_bf16_f32 v43, v38, v39\n"
  "v_fma_f32 v53, v53, s73, v226\n"
  "v_mfma_f32_16x16x32_bf16 v[16:19], a[148:151], a[32:35], v[16:19]\n"
  "ds_read_b64_tr_b16 a[132:133], v219 offset:12352\n"
  "v_fma_f32 v54, v54, s73, v226\n"
  "v_mfma_f32_16x16x32_bf16 v[16:19], a[144:147], a[36:39], v[16:19]\n"
  "ds_read_b64_tr_b16 a[134:135], v220 offset:12352\n"
  "v_fma_f32 v55, v55, s73, v226\n"
  "v_mfma_f32_16x16x32_bf16 v[16:19], a[120:123], a[40:43], v[16:19]\n"
  "ds_read_b64_tr_b16 a[128:129], v219 offset:12368\n"
  "v_accvgpr_read_b32 v150, a194\n"
  "v_mfma_f32_16x16x32_bf16 v[16:19], a[124:127], a[44:47], v[16:19]\n"
  "ds_read_b64_tr_b16 a[130:131], v220 offset:12368\n"
  "v_accvgpr_read_b32 v151, a195\n"
  "v_cvt_pk_bf16_f32 v136, v136, v140\n"
  "v_cvt_pk_bf16_f32 v140, v137, v141\n"
  "v_cvt_pk_bf16_f32 v137, v144, v148\n"
  "v_pk_fma_f32 v[150:151], v[150:151], v[74:75], v[74:75] neg_lo:[0,0,1] neg_hi:[0,0,1]\n"
  "v_pk_fma_f32 v[92:93], v[92:93], v[68:69], v[68:69] neg_lo:[0,0,1] neg_hi:[0,0,1]\n"
  "v_pk_fma_f32 v[90:91], v[90:91], v[70:71], v[70:71] neg_lo:[0,0,1] neg_hi:[0,0,1]\n"
  "v_pk_fma_f32 v[94:95], v[94:95], v[70:71], v[70:71] neg_lo:[0,0,1] neg_hi:[0,0,1]\n"
  "v_mfma_f32_16x16x32_bf16 v[32:35], a[124:127], a[68:71], v[248:251]\n"
  "v_exp_f32 v53, v53\n"
  "v_mfma_f32_16x16x32_bf16 v[32:35], a[120:123], a[64:67], v[32:35]\n"
  "v_exp_f32 v54, v54\n"
  "v_mfma_f32_16x16x32_bf16 v[32:35], a[144:147], a[60:63], v[32:35]\n"
  "v_exp_f32 v55, v55\n"
  "v_mfma_f32_16x16x32_bf16 v[32:35], a[148:151], a[56:59], v[32:35]\n"
  "v_cvt_pk_bf16_f32 v58, v52, v53\n"
  "v_cvt_pk_bf16_f32 v59, v54, v55\n"
  "v_mfma_f32_16x16x32_bf16 v[32:35], a[140:143], a[52:55], v[32:35]\n"
  "v_cvt_pk_bf16_f32 v138, v138, v142\n"
  "v_cvt_pk_bf16_f32 v142, v139, v143\n"
  "v_mfma_f32_16x16x32_bf16 v[32:35], a[136:139], a[48:51], v[32:35]\n"
  "v_cvt_pk_bf16_f32 v139, v146, v150\n"
  "v_accvgpr_read_b32 v104, a164\n"
  "v_mfma_f32_16x16x32_bf16 v[48:51], a[136:139], a[72:75], v[248:251]\n"
  "ds_read_b128 a[136:139], v235 offset:12352\n"
  "v_accvgpr_read_b32 v105, a165\n"
  "v_mfma_f32_16x16x32_bf16 v[48:51], a[140:143], a[76:79], v[48:51]\n"
  "ds_read_b128 a[140:143], v235 offset:12416\n"
  "v_accvgpr_read_b32 v108, a168\n"
  "v_mfma_f32_16x16x32_bf16 v[48:51], a[148:151], a[80:83], v[48:51]\n"
  "ds_read_b128 a[148:151], v235 offset:13376\n"
  "v_accvgpr_read_b32 v109, a169\n"
  "v_mfma_f32_16x16x32_bf16 v[48:51], a[144:147], a[84:87], v[48:51]\n"
  "ds_read_b128 a[144:147], v235 offset:13440\n"
  "v_accvgpr_read_b32 v112, a172\n"
  "v_mfma_f32_16x16x32_bf16 v[48:51], a[120:123], a[88:91], v[48:51]\n"
  "ds_read_b128 a[120:123], v235 offset:14400\n"
  "v_accvgpr_read_b32 v113, a173\n"
  "v_mfma_f32_16x16x32_bf16 v[48:51], a[124:127], a[92:95], v[48:51]\n"
  "s_waitcnt lgkmcnt(5)\n"
  "ds_read_b128 a[124:127], v235 offset:14464\n"
  "ds_write_b64 v229, v[136:137] offset:2240\n"
  "v_accvgpr_read_b32 v116, a176\n"
  "v_pk_fma_f32 v[104:105], v[104:105], v[68:69], v[68:69] neg_lo:[0,0,1] neg_hi:[0,0,1]\n"
  "v_pk_fma_f32 v[108:109], v[108:109], v[68:69], v[68:69] neg_lo:[0,0,1] neg_hi:[0,0,1]\n"
  "v_pk_fma_f32 v[112:113], v[112:113], v[68:69], v[68:69] neg_lo:[0,0,1] neg_hi:[0,0,1]\n"
  "v_mfma_f32_16x16x32_bf16 v[96:99], v[12:15], a[132:135], v[96:99]\n"
  "ds_write_b64 v229, v[138:139] offset:2816\n"
  "v_accvgpr_read_b32 v117, a177\n"
  "v_accvgpr_read_b32 v106, a166\n"
  "v_pk_fma_f32 v[116:117], v[116:117], v[68:69], v[68:69] neg_lo:[0,0,1] neg_hi:[0,0,1]\n"
  "v_mfma_f32_16x16x32_bf16 v[100:103], v[12:15], a[128:131], v[100:103]\n"
  "v_accvgpr_read_b32 v107, a167\n"
  "v_accvgpr_read_b32 v110, a170\n"
  "v_mfma_f32_16x16x32_bf16 v[128:131], v[28:31], a[132:135], v[128:131]\n"
  "v_accvgpr_read_b32 v111, a171\n"
  "v_accvgpr_read_b32 v114, a174\n"
  "v_accvgpr_read_b32 v115, a175\n"
  "v_accvgpr_read_b32 v118, a178\n"
  "v_accvgpr_read_b32 v119, a179\n"
  "v_pk_fma_f32 v[106:107], v[106:107], v[70:71], v[70:71] neg_lo:[0,0,1] neg_hi:[0,0,1]\n"
  "v_pk_fma_f32 v[110:111], v[110:111], v[70:71], v[70:71] neg_lo:[0,0,1] neg_hi:[0,0,1]\n"
  "v_pk_fma_f32 v[114:115], v[114:115], v[70:71], v[70:71] neg_lo:[0,0,1] neg_hi:[0,0,1]\n"
  "v_pk_fma_f32 v[118:119], v[118:119], v[70:71], v[70:71] neg_lo:[0,0,1] neg_hi:[0,0,1]\n"
  "v_mfma_f32_16x16x32_bf16 v[132:135], v[28:31], a[128:131], v[132:135]\n"
  "v_cvt_pk_bf16_f32 v141, v145, v149\n"
  "v_cvt_pk_bf16_f32 v143, v147, v151\n"
  "v_cvt_pk_bf16_f32 v104, v104, v108\n"
  "v_cvt_pk_bf16_f32 v108, v105, v109\n"
  "v_cvt_pk_bf16_f32 v105, v112, v116\n"
  "v_pk_fma_f32 v[96:97], v[96:97], v[68:69], v[68:69] neg_lo:[0,0,1] neg_hi:[0,0,1]\n"
  "v_pk_fma_f32 v[98:99], v[98:99], v[70:71], v[70:71] neg_lo:[0,0,1] neg_hi:[0,0,1]\n"
  "v_mfma_f32_16x16x32_bf16 v[160:163], v[44:47], a[132:135], v[160:163]\n"
  "ds_write_b64 v229, v[140:141] offset:2528\n"
  "v_cvt_pk_bf16_f32 v109, v113, v117\n"
  "v_cvt_pk_bf16_f32 v106, v106, v110\n"
  "v_pk_fma_f32 v[100:101], v[100:101], v[68:69], v[68:69] neg_lo:[0,0,1] neg_hi:[0,0,1]\n"
  "v_pk_fma_f32 v[102:103], v[102:103], v[70:71], v[70:71] neg_lo:[0,0,1] neg_hi:[0,0,1]\n"
  "v_mfma_f32_16x16x32_bf16 v[164:167], v[44:47], a[128:131], v[164:167]\n"
  "v_cvt_pk_bf16_f32 v110, v107, v111\n"
  "v_cvt_pk_bf16_f32 v107, v114, v118\n"
  "v_mfma_f32_16x16x32_bf16 v[192:195], v[60:63], a[132:135], v[192:195]\n"
  "ds_write_b64 v229, v[142:143] offset:3104\n"
  "v_cvt_pk_bf16_f32 v111, v115, v119\n"
  "v_cvt_pk_bf16_f32 v236, v236, v238\n"
  "v_pk_fma_f32 v[128:129], v[128:129], v[72:73], v[72:73] neg_lo:[0,0,1] neg_hi:[0,0,1]\n"
  "v_pk_fma_f32 v[132:133], v[132:133], v[72:73], v[72:73] neg_lo:[0,0,1] neg_hi:[0,0,1]\n"
  "v_pk_fma_f32 v[130:131], v[130:131], v[74:75], v[74:75] neg_lo:[0,0,1] neg_hi:[0,0,1]\n"
  "v_pk_fma_f32 v[134:135], v[134:135], v[74:75], v[74:75] neg_lo:[0,0,1] neg_hi:[0,0,1]\n"
  "v_mfma_f32_16x16x32_bf16 v[196:199], v[60:63], a[128:131], v[196:199]\n"
  "s_nop 0\n"
  "s_waitcnt lgkmcnt(8)\n"
  "ds_write_b64 v229, v[104:105] offset:0\n"
  "v_cvt_pk_bf16_f32 v237, v237, v239\n"
  "v_cvt_pk_bf16_f32 v184, v184, v188\n"
  "v_pk_fma_f32 v[160:161], v[160:161], v[76:77], v[76:77] neg_lo:[0,0,1] neg_hi:[0,0,1]\n"
  "v_pk_fma_f32 v[162:163], v[162:163], v[78:79], v[78:79] neg_lo:[0,0,1] neg_hi:[0,0,1]\n"
  "v_mfma_f32_16x16x32_bf16 v[4:7], a[136:139], a[0:3], v[248:251]\n"
  "ds_write_b32 v233, v236 offset:20736\n"
  "v_mfma_f32_16x16x32_bf16 v[4:7], a[140:143], a[4:7], v[4:7]\n"
  "s_waitcnt lgkmcnt(8)\n"
  "ds_write_b32 v233, v237 offset:21024\n"
  "v_mfma_f32_16x16x32_bf16 v[4:7], a[148:151], a[8:11], v[4:7]\n"
  "v_cvt_pk_bf16_f32 v188, v185, v189\n"
  "v_cvt_pk_bf16_f32 v186, v186, v190\n"
  "v_mfma_f32_16x16x32_bf16 v[4:7], a[144:147], a[12:15], v[4:7]\n"
  "s_waitcnt lgkmcnt(7)\n"
  "v_cvt_pk_bf16_f32 v190, v187, v191\n"
  "v_accvgpr_read_b32 v240, a236\n"
  "v_mfma_f32_16x16x32_bf16 v[4:7], a[120:123], a[16:19], v[4:7]\n"
  "v_accvgpr_read_b32 v241, a237\n"
  "v_accvgpr_read_b32 v236, a238\n"
  "v_mfma_f32_16x16x32_bf16 v[4:7], a[124:127], a[20:23], v[4:7]\n"
  "ds_write_b64 v229, v[108:109] offset:288\n"
  "v_mfma_f32_16x16x32_bf16 v[20:23], a[124:127], a[44:47], v[248:251]\n"
  "v_accvgpr_read_b32 v237, a239\n"
  "v_accvgpr_read_b32 v242, a240\n"
  "v_mfma_f32_16x16x32_bf16 v[20:23], a[120:123], a[40:43], v[20:23]\n"
  "v_accvgpr_read_b32 v243, a241\n"
  "v_accvgpr_read_b32 v238, a242\n"
  "v_mfma_f32_16x16x32_bf16 v[20:23], a[144:147], a[36:39], v[20:23]\n"
  "ds_read_b64_tr_b16 a[132:133], v217 offset:14400\n"
  "v_accvgpr_read_b32 v239, a243\n"
  "v_mfma_f32_16x16x32_bf16 v[20:23], a[148:151], a[32:35], v[20:23]\n"
  "ds_read_b64_tr_b16 a[134:135], v218 offset:14400\n"
  "v_fma_f32 v64, v64, s73, v227\n"
  "v_mfma_f32_16x16x32_bf16 v[20:23], a[140:143], a[28:31], v[20:23]\n"
  "ds_read_b64_tr_b16 a[128:129], v217 offset:14416\n"
  "v_fma_f32 v65, v65, s73, v227\n"
  "v_mfma_f32_16x16x32_bf16 v[20:23], a[136:139], a[24:27], v[20:23]\n"
  "ds_read_b64_tr_b16 a[130:131], v218 offset:14416\n"
  "ds_write_b64 v229, v[106:107] offset:576\n"
  "v_fma_f32 v66, v66, s73, v227\n"
  "v_pk_fma_f32 v[192:193], v[192:193], v[80:81], v[80:81] neg_lo:[0,0,1] neg_hi:[0,0,1]\n"
  "v_pk_fma_f32 v[196:197], v[196:197], v[80:81], v[80:81] neg_lo:[0,0,1] neg_hi:[0,0,1]\n"
  "v_pk_fma_f32 v[194:195], v[194:195], v[82:83], v[82:83] neg_lo:[0,0,1] neg_hi:[0,0,1]\n"
  "v_pk_fma_f32 v[198:199], v[198:199], v[82:83], v[82:83] neg_lo:[0,0,1] neg_hi:[0,0,1]\n"
  "v_mfma_f32_16x16x32_bf16 v[36:39], a[136:139], a[48:51], v[248:251]\n"
  "v_exp_f32 v64, v64\n"
  "v_mfma_f32_16x16x32_bf16 v[36:39], a[140:143], a[52:55], v[36:39]\n"
  "v_exp_f32 v65, v65\n"
  "v_mfma_f32_16x16x32_bf16 v[36:39], a[148:151], a[56:59], v[36:39]\n"
  "v_exp_f32_e64 v66, v66\n"
  "v_mfma_f32_16x16x32_bf16 v[36:39], a[144:147], a[60:63], v[36:39]\n"
  "v_cvt_pk_bf16_f32 v185, v192, v196\n"
  "v_cvt_pk_bf16_f32 v187, v194, v198\n"
  "v_mfma_f32_16x16x32_bf16 v[36:39], a[120:123], a[64:67], v[36:39]\n"
  "v_cvt_pk_bf16_f32 v189, v193, v197\n"
  "v_cvt_pk_bf16_f32 v191, v195, v199\n"
  "v_mfma_f32_16x16x32_bf16 v[36:39], a[124:127], a[68:71], v[36:39]\n"
  "ds_write_b64 v229, v[110:111] offset:864\n"
  "v_fma_f32 v67, v67, s73, v227\n"
  "v_cvt_pk_bf16_f32 v152, v152, v156\n"
  "v_pk_fma_f32 v[164:165], v[164:165], v[76:77], v[76:77] neg_lo:[0,0,1] neg_hi:[0,0,1]\n"
  "v_pk_fma_f32 v[166:167], v[166:167], v[78:79], v[78:79] neg_lo:[0,0,1] neg_hi:[0,0,1]\n"
  "v_pk_fma_f32 v[240:241], v[240:241], v[84:85], v[84:85] neg_lo:[0,0,1] neg_hi:[0,0,1]\n"
  "v_pk_fma_f32 v[242:243], v[242:243], v[84:85], v[84:85] neg_lo:[0,0,1] neg_hi:[0,0,1]\n"
  "v_mfma_f32_16x16x32_bf16 v[52:55], a[124:127], a[92:95], v[248:251]\n"
  "ds_read_b64_tr_b16 a[124:125], v219 offset:14400\n"
  "v_cvt_pk_bf16_f32 v156, v153, v157\n"
  "v_mfma_f32_16x16x32_bf16 v[52:55], a[120:123], a[88:91], v[52:55]\n"
  "ds_read_b64_tr_b16 a[126:127], v220 offset:14400\n"
  "ds_read_b64_tr_b16 a[252:253], v230 offset:0\n"
  "v_mfma_f32_16x16x32_bf16 v[52:55], a[144:147], a[84:87], v[52:55]\n"
  "ds_read_b64_tr_b16 a[120:121], v219 offset:14416\n"
  "ds_read_b64_tr_b16 a[254:255], v230 offset:32\n"
  "v_mfma_f32_16x16x32_bf16 v[52:55], a[148:151], a[80:83], v[52:55]\n"
  "ds_read_b64_tr_b16 a[122:123], v220 offset:14416\n"
  "ds_read_b64_tr_b16 v[244:245], v230 offset:1120\n"
  "v_mfma_f32_16x16x32_bf16 v[52:55], a[140:143], a[76:79], v[52:55]\n"
  "v_exp_f32 v67, v67\n"
  "v_mfma_f32_16x16x32_bf16 v[52:55], a[136:139], a[72:75], v[52:55]\n"
  "s_waitcnt lgkmcnt(9)\n"
  "ds_read_b64_tr_b16 v[246:247], v230 offset:1152\n"
  "v_cvt_pk_bf16_f32 v153, v160, v164\n"
  "v_mfma_f32_16x16x32_bf16 a[244:247], a[156:159], a[132:135], 1.0\n"
  "ds_write_b64 v229, v[184:185] offset:15680\n"
  "v_cvt_pk_bf16_f32 v154, v154, v158\n"
  "v_cvt_pk_bf16_f32 v158, v155, v159\n"
  "v_pk_fma_f32 v[236:237], v[236:237], v[86:87], v[86:87] neg_lo:[0,0,1] neg_hi:[0,0,1]\n"
  "v_pk_fma_f32 v[238:239], v[238:239], v[86:87], v[86:87] neg_lo:[0,0,1] neg_hi:[0,0,1]\n"
  "v_mfma_f32_16x16x32_bf16 a[248:251], a[156:159], a[128:131], 1.0\n"
  "ds_write_b64 v229, v[186:187] offset:16256\n"
  "v_cvt_pk_bf16_f32 v155, v162, v166\n"
  "v_cvt_pk_bf16_f32 v64, v64, v65\n"
  "v_mfma_f32_16x16x32_bf16 v[200:203], v[56:59], a[132:135], 1.0\n"
  "s_nop 0\n"
  "s_waitcnt lgkmcnt(5)\n"
  "buffer_store_dwordx4 a[252:255], v228, s[52:55], s80 offen offset:0 nt\n"
  "ds_read_b64_tr_b16 a[140:141], v217 offset:13376\n"
  "ds_write_b64 v229, v[188:189] offset:15968\n"
  "v_cvt_pk_bf16_f32 v65, v66, v67\n"
  "v_cvt_pk_bf16_f32 v157, v161, v165\n"
  "v_mfma_f32_16x16x32_bf16 v[204:207], v[56:59], a[128:131], 1.0\n"
  "ds_read_b64_tr_b16 a[142:143], v218 offset:13376\n"
  "ds_read_b64_tr_b16 a[252:253], v230 offset:2240\n"
  "v_mfma_f32_16x16x32_bf16 v[168:171], v[40:43], a[132:135], 1.0\n"
  "ds_read_b64_tr_b16 a[136:137], v217 offset:13392\n"
  "ds_read_b64_tr_b16 a[254:255], v230 offset:2272\n"
  "ds_write_b64 v229, v[190:191] offset:16544\n"
  "v_mfma_f32_16x16x32_bf16 v[172:175], v[40:43], a[128:131], 1.0\n"
  "ds_read_b64_tr_b16 a[138:139], v218 offset:13392\n"
  "ds_write_b64 v229, v[152:153] offset:13440\n"
  "v_cvt_pk_bf16_f32 v159, v163, v167\n"
  "v_mfma_f32_16x16x32_bf16 v[136:139], v[24:27], a[132:135], 1.0\n"
  "ds_write_b64 v229, v[154:155] offset:14016\n"
  "v_mfma_f32_16x16x32_bf16 v[140:143], v[24:27], a[128:131], 1.0\n"
  "ds_write_b64 v231, v[64:65] offset:2048\n"
  "v_cvt_pk_bf16_f32 v120, v120, v124\n"
  "v_cvt_pk_bf16_f32 v124, v121, v125\n"
  "v_mfma_f32_16x16x32_bf16 v[104:107], v[8:11], a[132:135], 1.0\n"
  "s_nop 0\n"
  "s_waitcnt lgkmcnt(13)\n"
  "buffer_store_dwordx4 v[244:247], v228, s[52:55], s80 offen offset:3072 nt\n"
  "ds_write_b64 v229, v[156:157] offset:13728\n"
  "v_cvt_pk_bf16_f32 v121, v128, v132\n"
  "v_cvt_pk_bf16_f32 v122, v122, v126\n"
  "v_cvt_pk_bf16_f32 v126, v123, v127\n"
  "v_mfma_f32_16x16x32_bf16 v[108:111], v[8:11], a[128:131], 1.0\n"
  "ds_read_b64_tr_b16 v[244:245], v230 offset:3360\n"
  "ds_read_b64_tr_b16 v[246:247], v230 offset:3392\n"
  "v_mfma_f32_16x16x32_bf16 v[112:115], v[8:11], a[124:127], 1.0\n"
  "ds_write_b64 v229, v[158:159] offset:14304\n"
  "v_cvt_pk_bf16_f32 v123, v130, v134\n"
  "v_cvt_pk_bf16_f32 v125, v129, v133\n"
  "v_mfma_f32_16x16x32_bf16 v[116:119], v[8:11], a[120:123], 1.0\n"
  "ds_write_b64 v229, v[120:121] offset:11200\n"
  "v_cvt_pk_bf16_f32 v127, v131, v135\n"
  "v_cvt_pk_bf16_f32 v88, v88, v92\n"
  "v_mfma_f32_16x16x32_bf16 v[144:147], v[24:27], a[124:127], 1.0\n"
  "ds_read_b64_tr_b16 a[132:133], v219 offset:13376\n"
  "ds_write_b64 v229, v[122:123] offset:11776\n"
  "v_cvt_pk_bf16_f32 v92, v89, v93\n"
  "v_mfma_f32_16x16x32_bf16 v[148:151], v[24:27], a[120:123], 1.0\n"
  "s_nop 0\n"
  "ds_read_b64_tr_b16 a[134:135], v220 offset:13376\n"
  "v_cvt_pk_bf16_f32 v89, v96, v100\n"
  "v_mfma_f32_16x16x32_bf16 v[176:179], v[40:43], a[124:127], 1.0\n"
  "s_waitcnt lgkmcnt(13)\n"
  "buffer_store_dwordx4 a[252:255], v228, s[52:55], s81 offen offset:0 nt\n"
  "ds_read_b64_tr_b16 a[128:129], v219 offset:13392\n"
  "ds_write_b64 v229, v[124:125] offset:11488\n"
  "v_cvt_pk_bf16_f32 v90, v90, v94\n"
  "v_cvt_pk_bf16_f32 v94, v91, v95\n"
  "v_mfma_f32_16x16x32_bf16 v[180:183], v[40:43], a[120:123], 1.0\n"
  "ds_read_b64_tr_b16 a[130:131], v220 offset:13392\n"
  "ds_write_b64 v229, v[126:127] offset:12064\n"
  "v_cvt_pk_bf16_f32 v91, v98, v102\n"
  "v_mfma_f32_16x16x32_bf16 v[208:211], v[56:59], a[124:127], 1.0\n"
  "s_nop 0\n"
  "v_cvt_pk_bf16_f32 v93, v97, v101\n"
  "v_cvt_pk_bf16_f32 v95, v99, v103\n"
  "v_mfma_f32_16x16x32_bf16 v[212:215], v[56:59], a[120:123], 1.0\n"
  "s_waitcnt lgkmcnt(15)\n"
  "ds_write_b64 v229, v[88:89] offset:8960\n"
  "v_fma_f32 v0, v0, s73, v223\n"
  "v_fma_f32 v1, v1, s73, v223\n"
  "v_mfma_f32_16x16x32_bf16 a[236:239], a[156:159], a[140:143], 1.0\n"
  "ds_write_b64 v229, v[90:91] offset:9536\n"
  "v_exp_f32_e64 v0, v0\n"
  "v_mfma_f32_16x16x32_bf16 a[240:243], a[156:159], a[136:139], 1.0\n"
  "s_nop 0\n"
  "ds_write_b64 v229, v[92:93] offset:9248\n"
  "v_mfma_f32_16x16x32_bf16 v[184:187], v[56:59], a[140:143], 1.0\n"
  "s_waitcnt lgkmcnt(12)\n"
  "buffer_store_dwordx4 v[244:247], v228, s[52:55], s81 offen offset:3072 nt\n"
  "ds_write_b64 v229, v[94:95] offset:9824\n"
  "v_exp_f32_e64 v1, v1\n"
  "v_fma_f32 v2, v2, s73, v223\n"
  "v_mfma_f32_16x16x32_bf16 v[188:191], v[56:59], a[136:139], 1.0\n"
  "v_exp_f32 v2, v2\n"
  "v_cvt_pk_bf16_f32 v240, v240, v242\n"
  "v_cvt_pk_bf16_f32 v241, v241, v243\n"
  "v_cvt_pk_bf16_f32 v236, v236, v238\n"
  "v_mfma_f32_16x16x32_bf16 v[152:155], v[40:43], a[140:143], 1.0\n"
  "s_mov_b32 m0, s67\n"
  "ds_write_b32 v233, v240 offset:19040\n"
  "v_fma_f32 v3, v3, s73, v223\n"
  "v_cvt_pk_bf16_f32 v12, v0, v1\n"
  "v_cvt_pk_bf16_f32 v237, v237, v239\n"
  "v_mfma_f32_16x16x32_bf16 v[156:159], v[40:43], a[136:139], 1.0\n"
  "s_waitcnt lgkmcnt(6)\n"
  "s_barrier\n"
  "ds_read_b128 a[124:127], v216 offset:0\n"
  "v_mfma_f32_16x16x32_bf16 v[120:123], v[24:27], a[140:143], 1.0\n"
  "ds_read_b64 a[160:161], v231 offset:2048\n"
  "ds_write_b32 v233, v241 offset:19328\n"
  "v_fma_f32 v16, v16, s73, v224\n"
  "v_accvgpr_read_b32 v240, a196\n"
  "v_mfma_f32_16x16x32_bf16 v[124:127], v[24:27], a[136:139], 1.0\n"
  "ds_read_b64 a[162:163], v232 offset:2048\n"
  "ds_write_b32 v233, v236 offset:19616\n"
  "v_fma_f32 v17, v17, s73, v224\n"
  "v_accvgpr_read_b32 v241, a197\n"
  "v_pk_fma_f32 v[240:241], v[240:241], v[76:77], v[76:77] neg_lo:[0,0,1] neg_hi:[0,0,1]\n"
  "v_mfma_f32_16x16x32_bf16 v[88:91], v[8:11], a[140:143], 1.0\n"
  "buffer_load_dwordx4 v221, s[40:43], 0 offen offset:0 lds\n"
  "v_mfma_f32_16x16x32_bf16 v[92:95], v[8:11], a[136:139], 1.0\n"
  "s_nop 0\n"
  "ds_read_b128 a[120:123], v216 offset:64\n"
  "v_fma_f32 v18, v18, s73, v224\n"
  "v_mfma_f32_16x16x32_bf16 v[96:99], v[8:11], a[132:135], 1.0\n"
  "s_addk_i32 m0, 896\n"
  "ds_read_b128 a[140:143], v216 offset:1024\n"
  "ds_write_b32 v233, v237 offset:19904\n"
  "v_fma_f32 v19, v19, s73, v224\n"
  "v_accvgpr_read_b32 v242, a200\n"
  "v_mfma_f32_16x16x32_bf16 v[100:103], v[8:11], a[128:131], 1.0\n"
  "ds_read_b128 a[136:139], v216 offset:1088\n"
  "v_fma_f32 v32, v32, s73, v225\n"
  "v_accvgpr_read_b32 v243, a201\n"
  "v_accvgpr_read_b32 v236, a204\n"
  "v_accvgpr_read_b32 v237, a205\n"
  "v_pk_fma_f32 v[242:243], v[242:243], v[76:77], v[76:77] neg_lo:[0,0,1] neg_hi:[0,0,1]\n"
  "v_pk_fma_f32 v[236:237], v[236:237], v[76:77], v[76:77] neg_lo:[0,0,1] neg_hi:[0,0,1]\n"
  "v_mfma_f32_16x16x32_bf16 v[128:131], v[24:27], a[132:135], 1.0\n"
  "buffer_load_dwordx4 v221, s[40:43], 0 offen offset:128 lds\n"
  "v_fma_f32 v33, v33, s73, v225\n"
  "v_mfma_f32_16x16x32_bf16 v[132:135], v[24:27], a[128:131], 1.0\n"
  "s_addk_i32 m0, 896\n"
  "ds_read_b128 a[144:147], v216 offset:2048\n"
  "v_fma_f32 v34, v34, s73, v225\n"
  "v_mfma_f32_16x16x32_bf16 v[160:163], v[40:43], a[132:135], 1.0\n"
  "s_nop 0\n"
  "ds_read_b128 a[148:151], v216 offset:2112\n"
  "v_fma_f32 v35, v35, s73, v225\n"
  "v_accvgpr_read_b32 v238, a208\n"
  "v_accvgpr_read_b32 v239, a209\n"
  "v_cvt_pk_bf16_f32 v240, v240, v242\n"
  "v_pk_fma_f32 v[238:239], v[238:239], v[76:77], v[76:77] neg_lo:[0,0,1] neg_hi:[0,0,1]\n"
  "v_mfma_f32_16x16x32_bf16 v[164:167], v[40:43], a[128:131], 1.0\n"
  "v_exp_f32_e64 v3, v3\n"
  "v_cvt_pk_bf16_f32 v242, v241, v243\n"
  "v_cvt_pk_bf16_f32 v241, v236, v238\n"
  "v_cvt_pk_bf16_f32 v243, v237, v239\n"
  "v_mfma_f32_16x16x32_bf16 v[192:195], v[56:59], a[132:135], 1.0\n"
  "buffer_load_dwordx4 v221, s[40:43], 0 offen offset:256 lds\n"
  "v_cvt_pk_bf16_f32 v13, v2, v3\n"
  "v_mfma_f32_16x16x32_bf16 v[196:199], v[56:59], a[128:131], 1.0\n"
  "s_waitcnt lgkmcnt(5)\n"
  "v_exp_f32 v16, v16\n"
  "v_accvgpr_read_b32 v236, a198\n"
  "v_accvgpr_read_b32 v237, a199\n"
  "v_accvgpr_read_b32 v238, a202\n"
  "v_pk_fma_f32 v[236:237], v[236:237], v[78:79], v[78:79] neg_lo:[0,0,1] neg_hi:[0,0,1]\n"
  "v_mfma_f32_16x16x32_bf16 v[64:67], a[124:127], a[96:99], v[248:251]\n"
  "s_add_u32 m0, s59, -128\n"
  "v_exp_f32 v17, v17\n"
  "v_mfma_f32_16x16x32_bf16 v[64:67], a[120:123], a[100:103], v[64:67]\n"
  "s_waitcnt lgkmcnt(2)\n"
  "v_exp_f32 v18, v18\n"
  "v_mfma_f32_16x16x32_bf16 v[64:67], a[140:143], a[104:107], v[64:67]\n"
  "buffer_load_dwordx4 v221, s[32:35], 0 offen offset:128 lds\n"
  "v_cvt_pk_bf16_f32 v28, v16, v17\n"
  "v_mfma_f32_16x16x32_bf16 v[64:67], a[136:139], a[108:111], v[64:67]\n"
  "s_waitcnt lgkmcnt(0)\n"
  "v_exp_f32 v19, v19\n"
  "v_mfma_f32_16x16x32_bf16 v[64:67], a[144:147], a[112:115], v[64:67]\n"
  "s_addk_i32 m0, 1024\n"
  "v_exp_f32 v32, v32\n"
  "v_mfma_f32_16x16x32_bf16 v[64:67], a[148:151], a[116:119], v[64:67]\n"
  "s_mov_b64 s[32:33], s[44:45]\n"
  "v_exp_f32 v33, v33\n"
  "v_mfma_f32_16x16x32_bf16 v[0:3], a[148:151], a[20:23], v[248:251]\n"
  "buffer_load_dwordx4 v221, s[44:47], 0 offen offset:128 lds\n"
  "v_cvt_pk_bf16_f32 v29, v18, v19\n"
  "v_mfma_f32_16x16x32_bf16 v[0:3], a[144:147], a[16:19], v[0:3]\n"
  "s_addk_i32 m0, 896\n"
  "v_exp_f32 v34, v34\n"
  "v_mfma_f32_16x16x32_bf16 v[0:3], a[136:139], a[12:15], v[0:3]\n"
  "s_add_u32 s40, s40, s31\n"
  "v_exp_f32 v35, v35\n"
  "v_mfma_f32_16x16x32_bf16 v[0:3], a[140:143], a[8:11], v[0:3]\n"
  "s_addc_u32 s41, s41, 0\n"
  "v_cvt_pk_bf16_f32 v44, v32, v33\n"
  "v_cvt_pk_bf16_f32 v45, v34, v35\n"
  "v_mfma_f32_16x16x32_bf16 v[0:3], a[120:123], a[4:7], v[0:3]\n"
  "buffer_load_dwordx4 v221, s[44:47], 0 offen offset:256 lds\n"
  "v_fma_f32 v48, v48, s73, v226\n"
  "v_mfma_f32_16x16x32_bf16 v[0:3], a[124:127], a[0:3], v[0:3]\n"
  "s_add_u32 s44, s44, s31\n"
  "v_exp_f32 v48, v48\n"
  "v_accvgpr_read_b32 v239, a203\n"
  "v_mfma_f32_16x16x32_bf16 v[16:19], a[124:127], a[24:27], v[248:251]\n"
  "s_addc_u32 s45, s45, 0\n"
  "v_fma_f32 v49, v49, s73, v226\n"
  "v_fma_f32 v50, v50, s73, v226\n"
  "v_mfma_f32_16x16x32_bf16 v[16:19], a[120:123], a[28:31], v[16:19]\n"
  "v_exp_f32 v49, v49\n"
  "v_mfma_f32_16x16x32_bf16 v[16:19], a[140:143], a[32:35], v[16:19]\n"
  "ds_read_b128 a[132:135], v235 offset:0\n"
  "v_fma_f32 v51, v51, s73, v226\n"
  "v_mfma_f32_16x16x32_bf16 v[16:19], a[136:139], a[36:39], v[16:19]\n"
  "ds_read_b128 a[128:131], v235 offset:64\n"
  "v_cvt_pk_bf16_f32 v60, v48, v49\n"
  "v_mfma_f32_16x16x32_bf16 v[16:19], a[144:147], a[40:43], v[16:19]\n"
  "v_exp_f32 v50, v50\n"
  "v_mfma_f32_16x16x32_bf16 v[16:19], a[148:151], a[44:47], v[16:19]\n"
  "v_exp_f32 v51, v51\n"
  "v_mfma_f32_16x16x32_bf16 v[32:35], a[148:151], a[68:71], v[248:251]\n"
  "v_cvt_pk_bf16_f32 v61, v50, v51\n"
  "v_fma_f32 v4, v4, s73, v223\n"
  "v_mfma_f32_16x16x32_bf16 v[32:35], a[144:147], a[64:67], v[32:35]\n"
  "v_exp_f32 v4, v4\n"
  "v_mfma_f32_16x16x32_bf16 v[32:35], a[136:139], a[60:63], v[32:35]\n"
  "v_fma_f32 v5, v5, s73, v223\n"
  "v_fma_f32 v6, v6, s73, v223\n"
  "v_mfma_f32_16x16x32_bf16 v[32:35], a[140:143], a[56:59], v[32:35]\n"
  "v_exp_f32 v5, v5\n"
  "v_mfma_f32_16x16x32_bf16 v[32:35], a[120:123], a[52:55], v[32:35]\n"
  "v_exp_f32 v6, v6\n"
  "v_mfma_f32_16x16x32_bf16 v[32:35], a[124:127], a[48:51], v[32:35]\n"
  "v_fma_f32 v7, v7, s73, v223\n"
  "v_cvt_pk_bf16_f32 v14, v4, v5\n"
  "v_mfma_f32_16x16x32_bf16 v[48:51], a[124:127], a[72:75], v[248:251]\n"
  "ds_read_b128 a[124:127], v235 offset:1024\n"
  "v_fma_f32 v20, v20, s73, v224\n"
  "v_mfma_f32_16x16x32_bf16 v[48:51], a[120:123], a[76:79], v[48:51]\n"
  "ds_read_b128 a[120:123], v235 offset:1088\n"
  "v_fma_f32 v21, v21, s73, v224\n"
  "v_mfma_f32_16x16x32_bf16 v[48:51], a[140:143], a[80:83], v[48:51]\n"
  "ds_read_b128 a[140:143], v235 offset:2048\n"
  "v_fma_f32 v22, v22, s73, v224\n"
  "v_mfma_f32_16x16x32_bf16 v[48:51], a[136:139], a[84:87], v[48:51]\n"
  "ds_read_b128 a[136:139], v235 offset:2112\n"
  "v_fma_f32 v23, v23, s73, v224\n"
  "v_mfma_f32_16x16x32_bf16 v[48:51], a[144:147], a[88:91], v[48:51]\n"
  "v_exp_f32 v7, v7\n"
  "v_mfma_f32_16x16x32_bf16 v[48:51], a[148:151], a[92:95], v[48:51]\n"
  "s_waitcnt lgkmcnt(4)\n"
  "v_cvt_pk_bf16_f32 v15, v6, v7\n"
  "v_fma_f32 v36, v36, s73, v225\n"
  "v_mfma_f32_16x16x32_bf16 v[4:7], a[132:135], a[0:3], v[248:251]\n"
  "v_exp_f32 v20, v20\n"
  "v_mfma_f32_16x16x32_bf16 v[4:7], a[128:131], a[4:7], v[4:7]\n"
  "s_waitcnt lgkmcnt(2)\n"
  "v_exp_f32 v21, v21\n"
  "v_mfma_f32_16x16x32_bf16 v[4:7], a[124:127], a[8:11], v[4:7]\n"
  "v_exp_f32 v22, v22\n"
  "v_mfma_f32_16x16x32_bf16 v[4:7], a[120:123], a[12:15], v[4:7]\n"
  "s_waitcnt lgkmcnt(0)\n"
  "v_exp_f32_e64 v23, v23\n"
  "v_mfma_f32_16x16x32_bf16 v[4:7], a[140:143], a[16:19], v[4:7]\n"
  "v_cvt_pk_bf16_f32 v30, v20, v21\n"
  "v_cvt_pk_bf16_f32 v31, v22, v23\n"
  "v_mfma_f32_16x16x32_bf16 v[4:7], a[136:139], a[20:23], v[4:7]\n"
  "v_exp_f32 v36, v36\n"
  "v_fma_f32 v37, v37, s73, v225\n"
  "v_exp_f32 v37, v37\n"
  "v_mfma_f32_16x16x32_bf16 v[20:23], a[136:139], a[44:47], v[248:251]\n"
  "v_fma_f32 v38, v38, s73, v225\n"
  "v_fma_f32 v39, v39, s73, v225\n"
  "v_mfma_f32_16x16x32_bf16 v[20:23], a[140:143], a[40:43], v[20:23]\n"
  "v_exp_f32 v38, v38\n"
  "v_mfma_f32_16x16x32_bf16 v[20:23], a[120:123], a[36:39], v[20:23]\n"
  "ds_read_b64_tr_b16 a[144:145], v217 offset:2048\n"
  "v_cvt_pk_bf16_f32 v46, v36, v37\n"
  "v_mfma_f32_16x16x32_bf16 v[20:23], a[124:127], a[32:35], v[20:23]\n"
  "ds_read_b64_tr_b16 a[146:147], v218 offset:2048\n"
  "v_fma_f32 v52, v52, s73, v226\n"
  "v_mfma_f32_16x16x32_bf16 v[20:23], a[128:131], a[28:31], v[20:23]\n"
  "ds_read_b64_tr_b16 a[148:149], v217 offset:2064\n"
  "v_exp_f32 v39, v39\n"
  "v_mfma_f32_16x16x32_bf16 v[20:23], a[132:135], a[24:27], v[20:23]\n"
  "ds_read_b64_tr_b16 a[150:151], v218 offset:2064\n"
  "ds_write_b64 v229, v[240:241] offset:4480\n"
  "v_cvt_pk_bf16_f32 v47, v38, v39\n"
  "v_exp_f32 v52, v52\n"
  "v_pk_fma_f32 v[238:239], v[238:239], v[78:79], v[78:79] neg_lo:[0,0,1] neg_hi:[0,0,1]\n"
  "v_mfma_f32_16x16x32_bf16 v[36:39], a[132:135], a[48:51], v[248:251]\n"
  "v_fma_f32 v53, v53, s73, v226\n"
  "v_fma_f32 v54, v54, s73, v226\n"
  "v_mfma_f32_16x16x32_bf16 v[36:39], a[128:131], a[52:55], v[36:39]\n"
  "v_exp_f32 v53, v53\n"
  "v_mfma_f32_16x16x32_bf16 v[36:39], a[124:127], a[56:59], v[36:39]\n"
  "v_exp_f32 v54, v54\n"
  "v_mfma_f32_16x16x32_bf16 v[36:39], a[120:123], a[60:63], v[36:39]\n"
  "v_fma_f32 v55, v55, s73, v226\n"
  "v_cvt_pk_bf16_f32 v62, v52, v53\n"
  "v_mfma_f32_16x16x32_bf16 v[36:39], a[140:143], a[64:67], v[36:39]\n"
  "v_exp_f32 v55, v55\n"
  "v_mfma_f32_16x16x32_bf16 v[36:39], a[136:139], a[68:71], v[36:39]\n"
  "v_cvt_pk_bf16_f32 v63, v54, v55\n"
  "v_accvgpr_read_b32 v240, a206\n"
  "v_mfma_f32_16x16x32_bf16 v[52:55], a[136:139], a[92:95], v[248:251]\n"
  "ds_read_b64_tr_b16 a[136:137], v219 offset:2048\n"
  "v_accvgpr_read_b32 v241, a207\n"
  "v_mfma_f32_16x16x32_bf16 v[52:55], a[140:143], a[88:91], v[52:55]\n"
  "ds_read_b64_tr_b16 a[138:139], v220 offset:2048\n"
  "v_cvt_pk_bf16_f32 v236, v236, v238\n"
  "v_mfma_f32_16x16x32_bf16 v[52:55], a[120:123], a[84:87], v[52:55]\n"
  "ds_read_b64_tr_b16 a[140:141], v219 offset:2064\n"
  "v_cvt_pk_bf16_f32 v238, v237, v239\n"
  "v_mfma_f32_16x16x32_bf16 v[52:55], a[124:127], a[80:83], v[52:55]\n"
  "ds_read_b64_tr_b16 a[142:143], v220 offset:2064\n"
  "v_fma_f32 v64, v64, s73, v227\n"
  "v_mfma_f32_16x16x32_bf16 v[52:55], a[128:131], a[76:79], v[52:55]\n"
  "v_exp_f32 v64, v64\n"
  "v_mfma_f32_16x16x32_bf16 v[52:55], a[132:135], a[72:75], v[52:55]\n"
  "s_waitcnt lgkmcnt(5)\n"
  "ds_write_b64 v229, v[242:243] offset:4768\n"
  "v_fma_f32 v65, v65, s73, v227\n"
  "v_fma_f32 v66, v66, s73, v227\n"
  "v_pk_fma_f32 v[240:241], v[240:241], v[78:79], v[78:79] neg_lo:[0,0,1] neg_hi:[0,0,1]\n"
  "v_mfma_f32_16x16x32_bf16 v[104:107], v[12:15], a[144:147], v[104:107]\n"
  "v_accvgpr_read_b32 v242, a210\n"
  "v_accvgpr_read_b32 v243, a211\n"
  "v_exp_f32_e64 v65, v65\n"
  "v_pk_fma_f32 v[242:243], v[242:243], v[78:79], v[78:79] neg_lo:[0,0,1] neg_hi:[0,0,1]\n"
  "v_mfma_f32_16x16x32_bf16 v[108:111], v[12:15], a[148:151], v[108:111]\n"
  "v_cvt_pk_bf16_f32 v237, v240, v242\n"
  "v_cvt_pk_bf16_f32 v239, v241, v243\n"
  "v_mfma_f32_16x16x32_bf16 v[136:139], v[28:31], a[144:147], v[136:139]\n"
  "ds_read_b64_tr_b16 a[120:121], v217 offset:0\n"
  "ds_write_b64 v229, v[236:237] offset:5056\n"
  "v_accvgpr_read_b32 v240, a212\n"
  "v_mfma_f32_16x16x32_bf16 v[140:143], v[28:31], a[148:151], v[140:143]\n"
  "ds_read_b64_tr_b16 a[122:123], v218 offset:0\n"
  "v_accvgpr_read_b32 v241, a213\n"
  "v_mfma_f32_16x16x32_bf16 v[168:171], v[44:47], a[144:147], v[168:171]\n"
  "ds_read_b64_tr_b16 a[124:125], v217 offset:1024\n"
  "ds_write_b64 v229, v[238:239] offset:5344\n"
  "v_accvgpr_read_b32 v242, a216\n"
  "v_pk_fma_f32 v[240:241], v[240:241], v[80:81], v[80:81] neg_lo:[0,0,1] neg_hi:[0,0,1]\n"
  "v_mfma_f32_16x16x32_bf16 v[172:175], v[44:47], a[148:151], v[172:175]\n"
  "ds_read_b64_tr_b16 a[126:127], v218 offset:1024\n"
  "v_accvgpr_read_b32 v243, a217\n"
  "v_accvgpr_read_b32 v236, a220\n"
  "v_accvgpr_read_b32 v237, a221\n"
  "v_accvgpr_read_b32 v238, a224\n"
  "v_pk_fma_f32 v[242:243], v[242:243], v[80:81], v[80:81] neg_lo:[0,0,1] neg_hi:[0,0,1]\n"
  "v_pk_fma_f32 v[236:237], v[236:237], v[80:81], v[80:81] neg_lo:[0,0,1] neg_hi:[0,0,1]\n"
  "v_mfma_f32_16x16x32_bf16 v[200:203], v[60:63], a[144:147], v[200:203]\n"
  "v_accvgpr_read_b32 v239, a225\n"
  "v_cvt_pk_bf16_f32 v240, v240, v242\n"
  "v_mfma_f32_16x16x32_bf16 v[204:207], v[60:63], a[148:151], v[204:207]\n"
  "ds_read_b64_tr_b16 a[252:253], v230 offset:4480\n"
  "ds_read_b64_tr_b16 a[254:255], v230 offset:4512\n"
  "v_cvt_pk_bf16_f32 v242, v241, v243\n"
  "v_exp_f32 v66, v66\n"
  "v_pk_fma_f32 v[238:239], v[238:239], v[80:81], v[80:81] neg_lo:[0,0,1] neg_hi:[0,0,1]\n"
  "v_mfma_f32_16x16x32_bf16 a[244:247], a[160:163], a[144:147], a[244:247]\n"
  "v_cvt_pk_bf16_f32 v241, v236, v238\n"
  "v_cvt_pk_bf16_f32 v243, v237, v239\n"
  "v_mfma_f32_16x16x32_bf16 a[248:251], a[160:163], a[148:151], a[248:251]\n"
  "s_waitcnt lgkmcnt(9)\n"
  "ds_write_b64 v229, v[240:241] offset:6720\n"
  "v_accvgpr_read_b32 v236, a214\n"
  "v_accvgpr_read_b32 v237, a215\n"
  "v_pk_fma_f32 v[236:237], v[236:237], v[82:83], v[82:83] neg_lo:[0,0,1] neg_hi:[0,0,1]\n"
  "v_mfma_f32_16x16x32_bf16 v[208:211], v[60:63], a[136:139], v[208:211]\n"
  "ds_write_b64 v229, v[242:243] offset:7008\n"
  "v_accvgpr_read_b32 v238, a218\n"
  "v_accvgpr_read_b32 v239, a219\n"
  "v_pk_fma_f32 v[238:239], v[238:239], v[82:83], v[82:83] neg_lo:[0,0,1] neg_hi:[0,0,1]\n"
  "v_mfma_f32_16x16x32_bf16 v[212:215], v[60:63], a[140:143], v[212:215]\n"
  "v_accvgpr_read_b32 v240, a222\n"
  "v_accvgpr_read_b32 v241, a223\n"
  "v_mfma_f32_16x16x32_bf16 v[176:179], v[44:47], a[136:139], v[176:179]\n"
  "ds_read_b64_tr_b16 a[128:129], v217 offset:16\n"
  "v_accvgpr_read_b32 v242, a226\n"
  "v_accvgpr_read_b32 v243, a227\n"
  "v_cvt_pk_bf16_f32 v236, v236, v238\n"
  "v_cvt_pk_bf16_f32 v238, v237, v239\n"
  "v_pk_fma_f32 v[240:241], v[240:241], v[82:83], v[82:83] neg_lo:[0,0,1] neg_hi:[0,0,1]\n"
  "v_pk_fma_f32 v[242:243], v[242:243], v[82:83], v[82:83] neg_lo:[0,0,1] neg_hi:[0,0,1]\n"
  "v_mfma_f32_16x16x32_bf16 v[180:183], v[44:47], a[140:143], v[180:183]\n"
  "ds_read_b64_tr_b16 a[130:131], v218 offset:16\n"
  "v_cvt_pk_bf16_f32 v237, v240, v242\n"
  "v_mfma_f32_16x16x32_bf16 v[144:147], v[28:31], a[136:139], v[144:147]\n"
  "ds_read_b64_tr_b16 a[132:133], v217 offset:1040\n"
  "ds_write_b64 v229, v[236:237] offset:7296\n"
  "v_cvt_pk_bf16_f32 v239, v241, v243\n"
  "v_mfma_f32_16x16x32_bf16 v[148:151], v[28:31], a[140:143], v[148:151]\n"
  "ds_read_b64_tr_b16 a[134:135], v218 offset:1040\n"
  "ds_write_b64 v229, v[238:239] offset:7584\n"
  "v_accvgpr_read_b32 v240, a228\n"
  "v_mfma_f32_16x16x32_bf16 v[112:115], v[12:15], a[136:139], v[112:115]\n"
  "s_nop 0\n"
  "v_accvgpr_read_b32 v241, a229\n"
  "v_accvgpr_read_b32 v236, a230\n"
  "v_mfma_f32_16x16x32_bf16 v[116:119], v[12:15], a[140:143], v[116:119]\n"
  "s_waitcnt lgkmcnt(10)\n"
  "v_accvgpr_read_b32 v237, a231\n"
  "v_accvgpr_read_b32 v242, a232\n"
  "v_accvgpr_read_b32 v243, a233\n"
  "v_accvgpr_read_b32 v238, a234\n"
  "v_accvgpr_read_b32 v239, a235\n"
  "v_pk_fma_f32 v[240:241], v[240:241], v[84:85], v[84:85] neg_lo:[0,0,1] neg_hi:[0,0,1]\n"
  "v_pk_fma_f32 v[242:243], v[242:243], v[84:85], v[84:85] neg_lo:[0,0,1] neg_hi:[0,0,1]\n"
  "v_pk_fma_f32 v[236:237], v[236:237], v[86:87], v[86:87] neg_lo:[0,0,1] neg_hi:[0,0,1]\n"
  "v_pk_fma_f32 v[238:239], v[238:239], v[86:87], v[86:87] neg_lo:[0,0,1] neg_hi:[0,0,1]\n"
  "v_mfma_f32_16x16x32_bf16 a[164:167], v[8:11], a[120:123], 1.0\n"
  "v_fma_f32 v67, v67, s73, v227\n"
  "v_cvt_pk_bf16_f32 v240, v240, v242\n"
  "v_mfma_f32_16x16x32_bf16 a[164:167], v[12:15], a[124:127], a[164:167]\n"
  "v_exp_f32_e64 v67, v67\n"
  "v_mfma_f32_16x16x32_bf16 a[180:183], v[24:27], a[120:123], 1.0\n"
  "ds_write_b32 v233, v240 offset:17920\n"
  "v_mfma_f32_16x16x32_bf16 a[180:183], v[28:31], a[124:127], a[180:183]\n"
  "v_cvt_pk_bf16_f32 v241, v241, v243\n"
  "v_cvt_pk_bf16_f32 v236, v236, v238\n"
  "v_cvt_pk_bf16_f32 v237, v237, v239\n"
  "v_cvt_pk_bf16_f32 v64, v64, v65\n"
  "v_cvt_pk_bf16_f32 v65, v66, v67\n"
  "v_mfma_f32_16x16x32_bf16 a[196:199], v[40:43], a[120:123], 1.0\n"
  "s_mov_b32 m0, s68\n"
  "ds_read_b64_tr_b16 a[136:137], v219 offset:0\n"
  "v_fma_f32 v0, v0, s73, v223\n"
  "v_mfma_f32_16x16x32_bf16 a[196:199], v[44:47], a[124:127], a[196:199]\n"
  "s_nop 0\n"
  "ds_read_b64_tr_b16 a[138:139], v220 offset:0\n"
  "ds_read_b64_tr_b16 v[244:245], v230 offset:5600\n"
  "ds_write_b32 v233, v241 offset:18208\n"
  "v_fma_f32 v1, v1, s73, v223\n"
  "v_mfma_f32_16x16x32_bf16 a[212:215], v[56:59], a[120:123], 1.0\n"
  "ds_read_b64_tr_b16 a[140:141], v219 offset:1024\n"
  "v_fma_f32 v2, v2, s73, v223\n"
  "v_mfma_f32_16x16x32_bf16 a[212:215], v[60:63], a[124:127], a[212:215]\n"
  "ds_read_b64_tr_b16 a[142:143], v220 offset:1024\n"
  "v_fma_f32 v3, v3, s73, v223\n"
  "v_mfma_f32_16x16x32_bf16 a[228:231], a[156:159], a[120:123], 1.0\n"
  "buffer_load_dwordx4 v221, s[40:43], 0 offen offset:0 lds\n"
  "v_mfma_f32_16x16x32_bf16 a[228:231], a[160:163], a[124:127], a[228:231]\n"
  "s_waitcnt lgkmcnt(8)\n"
  "ds_write_b32 v233, v236 offset:18496\n"
  "v_mfma_f32_16x16x32_bf16 a[232:235], a[156:159], a[128:131], 1.0\n"
  "s_addk_i32 m0, 896\n"
  "ds_write_b32 v233, v237 offset:18784\n"
  "v_mfma_f32_16x16x32_bf16 a[232:235], a[160:163], a[132:135], a[232:235]\n"
  "ds_write_b64 v231, v[64:65] offset:0\n"
  "v_mfma_f32_16x16x32_bf16 a[216:219], v[56:59], a[128:131], 1.0\n"
  "buffer_load_dwordx4 v221, s[40:43], 0 offen offset:128 lds\n"
  "v_fma_f32 v16, v16, s73, v224\n"
  "v_mfma_f32_16x16x32_bf16 a[216:219], v[60:63], a[132:135], a[216:219]\n"
  "s_addk_i32 m0, 896\n"
  "ds_read_b64_tr_b16 a[120:121], v219 offset:16\n"
  "v_fma_f32 v17, v17, s73, v224\n"
  "v_mfma_f32_16x16x32_bf16 a[200:203], v[40:43], a[128:131], 1.0\n"
  "ds_read_b64_tr_b16 a[122:123], v220 offset:16\n"
  "v_fma_f32 v18, v18, s73, v224\n"
  "v_mfma_f32_16x16x32_bf16 a[200:203], v[44:47], a[132:135], a[200:203]\n"
  "ds_read_b64_tr_b16 a[124:125], v219 offset:1040\n"
  "v_fma_f32 v19, v19, s73, v224\n"
  "v_mfma_f32_16x16x32_bf16 a[184:187], v[24:27], a[128:131], 1.0\n"
  "buffer_load_dwordx4 v221, s[40:43], 0 offen offset:256 lds\n"
  "v_fma_f32 v32, v32, s73, v225\n"
  "v_mfma_f32_16x16x32_bf16 a[184:187], v[28:31], a[132:135], a[184:187]\n"
  "s_mov_b32 m0, s60\n"
  "ds_read_b64_tr_b16 a[126:127], v220 offset:1040\n"
  "v_fma_f32 v33, v33, s73, v225\n"
  "v_mfma_f32_16x16x32_bf16 a[168:171], v[8:11], a[128:131], 1.0\n"
  "s_add_u32 s28, s96, 184448\n"
  "v_exp_f32 v0, v0\n"
  "v_mfma_f32_16x16x32_bf16 a[168:171], v[12:15], a[132:135], a[168:171]\n"
  "s_waitcnt lgkmcnt(7)\n"
  "v_exp_f32 v1, v1\n"
  "v_mfma_f32_16x16x32_bf16 a[172:175], v[8:11], a[136:139], 1.0\n"
  "buffer_load_dwordx4 v221, s[32:35], 0 offen offset:0 lds\n"
  "v_add_f32 v252, v0, v1\n"
  "v_mfma_f32_16x16x32_bf16 a[172:175], v[12:15], a[140:143], a[172:175]\n"
  "s_addk_i32 m0, 1024\n"
  "v_exp_f32 v2, v2\n"
  "v_mfma_f32_16x16x32_bf16 a[188:191], v[24:27], a[136:139], 1.0\n"
  "s_add_u32 s40, s40, s31\n"
  "v_exp_f32 v3, v3\n"
  "v_mfma_f32_16x16x32_bf16 a[188:191], v[28:31], a[140:143], a[188:191]\n"
  "s_addc_u32 s41, s41, 0\n"
  "v_exp_f32 v16, v16\n"
  "v_mfma_f32_16x16x32_bf16 a[204:207], v[40:43], a[136:139], 1.0\n"
  "buffer_load_dwordx4 v221, s[44:47], 0 offen offset:0 lds\n"
  "v_add_f32 v252, v252, v2\n"
  "v_mfma_f32_16x16x32_bf16 a[204:207], v[44:47], a[140:143], a[204:207]\n"
  "s_addk_i32 m0, 768\n"
  "v_exp_f32 v17, v17\n"
  "v_mfma_f32_16x16x32_bf16 a[220:223], v[56:59], a[136:139], 1.0\n"
  "s_mov_b64 s[32:33], s[44:45]\n"
  "v_exp_f32 v18, v18\n"
  "v_mfma_f32_16x16x32_bf16 a[220:223], v[60:63], a[140:143], a[220:223]\n"
  "s_waitcnt lgkmcnt(0)\n"
  "v_exp_f32 v19, v19\n"
  "v_mfma_f32_16x16x32_bf16 a[224:227], v[56:59], a[120:123], 1.0\n"
  "buffer_load_dwordx4 v221, s[44:47], 0 offen offset:256 lds\n"
  "v_add_f32 v252, v252, v3\n"
  "v_mfma_f32_16x16x32_bf16 a[224:227], v[60:63], a[124:127], a[224:227]\n"
  "s_add_u32 s44, s44, s31\n"
  "v_exp_f32 v32, v32\n"
  "v_mfma_f32_16x16x32_bf16 a[208:211], v[40:43], a[120:123], 1.0\n"
  "s_addc_u32 s45, s45, 0\n"
  "v_exp_f32 v33, v33\n"
  "v_mfma_f32_16x16x32_bf16 a[208:211], v[44:47], a[124:127], a[208:211]\n"
  "s_cmp_eq_u32 s95, 0\n"
  "s_cselect_b32 s95, 3, s95\n"
  "s_subb_u32 s100, s100, -11\n"
  "v_add_f32 v253, v16, v17\n"
  "v_add_f32 v253, v253, v18\n"
  "v_mfma_f32_16x16x32_bf16 a[192:195], v[24:27], a[120:123], 1.0\n"
  "s_sub_u32 s95, s95, 1\n"
  "v_add_f32 v253, v253, v19\n"
  "v_fma_f32 v34, v34, s73, v225\n"
  "v_mfma_f32_16x16x32_bf16 a[192:195], v[28:31], a[124:127], a[192:195]\n"
  "s_waitcnt vmcnt(6)\n"
  "s_barrier\n"
  "ds_read_b128 a[128:131], v216 offset:12352\n"
  "v_mfma_f32_16x16x32_bf16 a[176:179], v[8:11], a[120:123], 1.0\n"
  "s_lshr_b32 s79, s100, 0\n"
  "ds_read_b128 a[132:135], v216 offset:12416\n"
  "v_fma_f32 v35, v35, s73, v225\n"
  "v_mfma_f32_16x16x32_bf16 a[176:179], v[12:15], a[124:127], a[176:179]\n"
  "s_mul_i32 s79, s79, 7\n"
  "ds_read_b128 a[120:123], v216 offset:13376\n"
  "v_add_f32 v254, v32, v33\n"
  "v_mfma_f32_16x16x32_bf16 v[68:71], v[8:11], a[152:155], 0\n"
  "s_add_u32 s79, s79, s100\n"
  "ds_read_b128 a[124:127], v216 offset:13440\n"
  "v_cvt_pk_bf16_f32 v8, v0, v1\n"
  "v_mfma_f32_16x16x32_bf16 v[68:71], v[12:15], a[152:155], v[68:71]\n"
  "s_add_u32 s79, s79, s101\n"
  "ds_read_b128 a[136:139], v216 offset:14400\n"
  "v_cvt_pk_bf16_f32 v9, v2, v3\n"
  "v_mfma_f32_16x16x32_bf16 v[72:75], v[24:27], a[152:155], 0\n"
  "ds_read_b128 a[140:143], v216 offset:14464\n"
  "v_cvt_pk_bf16_f32 v24, v16, v17\n"
  "v_mfma_f32_16x16x32_bf16 v[72:75], v[28:31], a[152:155], v[72:75]\n"
  "s_mov_b32 m0, s66\n"
  "v_exp_f32 v34, v34\n"
  "v_mfma_f32_16x16x32_bf16 v[76:79], v[40:43], a[152:155], 0\n"
  "s_min_u32 s79, s79, 0x1ff\n"
  "v_exp_f32 v35, v35\n"
  "v_mfma_f32_16x16x32_bf16 v[76:79], v[44:47], a[152:155], v[76:79]\n"
  "s_mul_i32 s96, s79, 196608\n"
  "v_cvt_pk_bf16_f32 v25, v18, v19\n"
  "v_add_f32 v254, v254, v34\n"
  "v_mfma_f32_16x16x32_bf16 v[80:83], v[56:59], a[152:155], 0\n"
  "s_mul_i32 s92, s79, 325632\n"
  "v_add_f32 v254, v254, v35\n"
  "v_cvt_pk_bf16_f32 v40, v32, v33\n"
  "v_mfma_f32_16x16x32_bf16 v[80:83], v[60:63], a[152:155], v[80:83]\n"
  "buffer_load_dwordx4 v221, s[40:43], 0 offen offset:0 lds\n"
  "v_mfma_f32_16x16x32_bf16 v[84:87], a[156:159], a[152:155], 0\n"
  "s_add_u32 s96, s96, s39\n"
  "ds_read_b64 a[156:157], v231 offset:0\n"
  "v_cvt_pk_bf16_f32 v41, v34, v35\n"
  "v_mfma_f32_16x16x32_bf16 v[84:87], a[160:163], a[152:155], v[84:87]\n"
  "L_CUT_%=:\n"
  "s_waitcnt lgkmcnt(5)\n"
  "ds_read_b64 a[158:159], v232 offset:0\n"
  "v_fma_f32 v48, v48, s73, v226\n"
  "v_mfma_f32_16x16x32_bf16 v[64:67], a[128:131], a[96:99], v[248:251]\n"
  "s_addk_i32 m0, 896\n"
  "v_exp_f32 v48, v48\n"
  "v_mfma_f32_16x16x32_bf16 v[64:67], a[132:135], a[100:103], v[64:67]\n"
  "s_waitcnt lgkmcnt(4)\n"
  "buffer_load_dwordx4 v221, s[40:43], 0 offen offset:128 lds\n"
  "v_fma_f32 v49, v49, s73, v226\n"
  "v_mfma_f32_16x16x32_bf16 v[64:67], a[120:123], a[104:107], v[64:67]\n"
  "s_addk_i32 m0, 896\n"
  "v_exp_f32 v49, v49\n"
  "v_mfma_f32_16x16x32_bf16 v[64:67], a[124:127], a[108:111], v[64:67]\n"
  "s_waitcnt lgkmcnt(2)\n"
  "v_fma_f32 v50, v50, s73, v226\n"
  "v_fma_f32 v51, v51, s73, v226\n"
  "v_mfma_f32_16x16x32_bf16 v[64:67], a[136:139], a[112:115], v[64:67]\n"
  "s_mul_i32 s79, s95, 110592\n"
  "v_exp_f32 v50, v50\n"
  "v_mfma_f32_16x16x32_bf16 v[64:67], a[140:143], a[116:119], v[64:67]\n"
  "buffer_load_dwordx4 v221, s[40:43], 0 offen offset:256 lds\n"
  "v_add_f32 v255, v48, v49\n"
  "v_mfma_f32_16x16x32_bf16 v[0:3], a[140:143], a[20:23], v[248:251]\n"
  "s_add_u32 s92, s92, s79\n"
  "v_exp_f32 v51, v51\n"
  "v_mfma_f32_16x16x32_bf16 v[0:3], a[136:139], a[16:19], v[0:3]\n"
  "s_add_u32 s71, s92, s80\n"
  "v_add_f32 v255, v255, v50\n"
  "v_add_f32 v255, v255, v51\n"
  "v_mfma_f32_16x16x32_bf16 v[0:3], a[124:127], a[12:15], v[0:3]\n"
  "v_cvt_pk_bf16_f32 v56, v48, v49\n"
  "v_cvt_pk_bf16_f32 v57, v50, v51\n"
  "v_mfma_f32_16x16x32_bf16 v[0:3], a[120:123], a[8:11], v[0:3]\n"
  "s_add_u32 m0, s58, -128\n"
  "v_fma_f32 v52, v52, s73, v226\n"
  "v_mfma_f32_16x16x32_bf16 v[0:3], a[132:135], a[4:7], v[0:3]\n"
  "s_add_u32 s48, s84, s71\n"
  "v_exp_f32 v52, v52\n"
  "v_mfma_f32_16x16x32_bf16 v[0:3], a[128:131], a[0:3], v[0:3]\n"
  "s_addc_u32 s49, s85, 0\n"
  "v_fma_f32 v53, v53, s73, v226\n"
  "v_fma_f32 v54, v54, s73, v226\n"
  "v_mfma_f32_16x16x32_bf16 v[16:19], a[128:131], a[24:27], v[248:251]\n"
  "s_cmp_eq_u32 s95, 2\n"
  "s_cselect_b32 s94, 17, 18\n"
  "v_exp_f32 v53, v53\n"
  "v_mfma_f32_16x16x32_bf16 v[16:19], a[132:135], a[28:31], v[16:19]\n"
  "buffer_load_dwordx4 v221, s[32:35], 0 offen offset:128 lds\n"
  "v_fma_f32 v55, v55, s73, v226\n"
  "v_mfma_f32_16x16x32_bf16 v[16:19], a[120:123], a[32:35], v[16:19]\n"
  "s_addk_i32 m0, 1024\n"
  "ds_read_b64_tr_b16 a[144:145], v217 offset:12352\n"
  "v_cvt_pk_bf16_f32 v58, v52, v53\n"
  "v_mfma_f32_16x16x32_bf16 v[16:19], a[124:127], a[36:39], v[16:19]\n"
  "s_mul_i32 s94, s94, 6144\n"
  "ds_read_b64_tr_b16 a[146:147], v218 offset:12352\n"
  "v_add_f32 v255, v255, v52\n"
  "v_mfma_f32_16x16x32_bf16 v[16:19], a[136:139], a[40:43], v[16:19]\n"
  "s_cmp_lt_i32 s76, 5\n"
  "s_cselect_b32 s38, s94, 0\n"
  "s_cselect_b32 s28, s96, s28\n"
  "ds_read_b64_tr_b16 a[148:149], v217 offset:13376\n"
  "v_mfma_f32_16x16x32_bf16 v[16:19], a[140:143], a[44:47], v[16:19]\n"
  "buffer_load_dwordx4 v221, s[44:47], 0 offen offset:128 lds\n"
  "v_fma_f32 v36, v36, s73, v225\n"
  "v_mfma_f32_16x16x32_bf16 v[32:35], a[140:143], a[68:71], v[248:251]\n"
  "s_addk_i32 m0, 896\n"
  "ds_read_b64_tr_b16 a[150:151], v218 offset:13376\n"
  "v_fma_f32 v37, v37, s73, v225\n"
  "v_mfma_f32_16x16x32_bf16 v[32:35], a[136:139], a[64:67], v[32:35]\n"
  "s_sub_i32 s71, s38, s80\n"
  "v_exp_f32 v54, v54\n"
  "v_mfma_f32_16x16x32_bf16 v[32:35], a[124:127], a[60:63], v[32:35]\n"
  "s_max_i32 s50, s71, 0\n"
  "v_exp_f32 v55, v55\n"
  "v_mfma_f32_16x16x32_bf16 v[32:35], a[120:123], a[56:59], v[32:35]\n"
  "buffer_load_dwordx4 v221, s[44:47], 0 offen offset:256 lds\n"
  "v_cvt_pk_bf16_f32 v59, v54, v55\n"
  "v_mfma_f32_16x16x32_bf16 v[32:35], a[132:135], a[52:55], v[32:35]\n"
  "s_add_u32 s40, s40, s31\n"
  "v_exp_f32 v36, v36\n"
  "v_mfma_f32_16x16x32_bf16 v[32:35], a[128:131], a[48:51], v[32:35]\n"
  "s_addc_u32 s41, s41, 0\n"
  "v_exp_f32 v37, v37\n"
  "v_mfma_f32_16x16x32_bf16 v[48:51], a[128:131], a[72:75], v[248:251]\n"
  "s_mov_b64 s[32:33], s[44:45]\n"
  "ds_read_b64_tr_b16 a[128:129], v217 offset:12368\n"
  "v_fma_f32 v38, v38, s73, v225\n"
  "v_mfma_f32_16x16x32_bf16 v[48:51], a[132:135], a[76:79], v[48:51]\n"
  "ds_read_b64_tr_b16 a[130:131], v218 offset:12368\n"
  "v_fma_f32 v39, v39, s73, v225\n"
  "v_mfma_f32_16x16x32_bf16 v[48:51], a[120:123], a[80:83], v[48:51]\n"
  "ds_read_b64_tr_b16 a[132:133], v217 offset:13392\n"
  "v_cvt_pk_bf16_f32 v42, v36, v37\n"
  "v_mfma_f32_16x16x32_bf16 v[48:51], a[124:127], a[84:87], v[48:51]\n"
  "s_add_u32 m0, s72, 0\n"
  "ds_read_b64_tr_b16 a[134:135], v218 offset:13392\n"
  "v_fma_f32 v20, v20, s73, v224\n"
  "v_mfma_f32_16x16x32_bf16 v[48:51], a[136:139], a[88:91], v[48:51]\n"
  "s_min_u32 s30, s38, 0xc00\n"
  "v_exp_f32 v38, v38\n"
  "v_mfma_f32_16x16x32_bf16 v[48:51], a[140:143], a[92:95], v[48:51]\n"
  "s_waitcnt lgkmcnt(4)\n"
  "v_exp_f32 v39, v39\n"
  "v_mfma_f32_16x16x32_bf16 a[236:239], a[160:163], a[144:147], a[236:239]\n"
  "v_exp_f32 v20, v20\n"
  "v_mfma_f32_16x16x32_bf16 a[236:239], a[156:159], a[148:151], a[236:239]\n"
  "buffer_load_dwordx4 v221, s[48:51], 0 offen offset:0 nt lds\n"
  "v_cvt_pk_bf16_f32 v43, v38, v39\n"
  "v_mfma_f32_16x16x32_bf16 v[184:187], v[60:63], a[144:147], v[184:187]\n"
  "v_fma_f32 v21, v21, s73, v224\n"
  "v_fma_f32 v22, v22, s73, v224\n"
  "v_mfma_f32_16x16x32_bf16 v[184:187], v[56:59], a[148:151], v[184:187]\n"
  "s_add_u32 s44, s44, s31\n"
  "v_exp_f32 v21, v21\n"
  "v_exp_f32_e64 v22, v22\n"
  "v_fma_f32 v23, v23, s73, v224\n"
  "v_exp_f32 v23, v23\n"
  "v_mfma_f32_16x16x32_bf16 v[152:155], v[44:47], a[144:147], v[152:155]\n"
  "s_addc_u32 s45, s45, 0\n"
  "ds_read_b64_tr_b16 a[120:121], v219 offset:12352\n"
  "v_cvt_pk_bf16_f32 v26, v20, v21\n"
  "v_mfma_f32_16x16x32_bf16 v[152:155], v[40:43], a[148:151], v[152:155]\n"
  "ds_read_b64_tr_b16 a[122:123], v220 offset:12352\n"
  "v_cvt_pk_bf16_f32 v27, v22, v23\n"
  "v_fma_f32 v4, v4, s73, v223\n"
  "v_exp_f32 v4, v4\n"
  "v_fma_f32 v5, v5, s73, v223\n"
  "v_exp_f32 v5, v5\n"
  "v_fma_f32 v6, v6, s73, v223\n"
  "v_exp_f32 v6, v6\n"
  "v_fma_f32 v7, v7, s73, v223\n"
  "v_exp_f32 v7, v7\n"
  "v_mfma_f32_16x16x32_bf16 v[120:123], v[28:31], a[144:147], v[120:123]\n"
  "ds_read_b64_tr_b16 a[124:125], v219 offset:13376\n"
  "v_cvt_pk_bf16_f32 v10, v4, v5\n"
  "v_mfma_f32_16x16x32_bf16 v[120:123], v[24:27], a[148:151], v[120:123]\n"
  "ds_read_b64_tr_b16 a[126:127], v220 offset:13376\n"
  "v_cvt_pk_bf16_f32 v11, v6, v7\n"
  "v_mfma_f32_16x16x32_bf16 v[88:91], v[12:15], a[144:147], v[88:91]\n"
  "s_add_u32 m0, s72, -1984\n"
  "v_add_f32 v252, v252, v4\n"
  "v_add_f32 v252, v252, v5\n"
  "v_mfma_f32_16x16x32_bf16 v[88:91], v[8:11], a[148:151], v[88:91]\n"
  "s_waitcnt lgkmcnt(4)\n"
  "buffer_load_dwordx4 v221, s[48:51], 0 offen offset:3072 nt lds\n"
  "v_add_f32 v252, v252, v6\n"
  "v_mfma_f32_16x16x32_bf16 v[92:95], v[12:15], a[128:131], v[92:95]\n"
  "v_add_f32 v252, v252, v7\n"
  "v_add_f32 v253, v253, v20\n"
  "v_mfma_f32_16x16x32_bf16 v[92:95], v[8:11], a[132:135], v[92:95]\n"
  "v_add_f32 v253, v253, v21\n"
  "v_add_f32 v253, v253, v22\n"
  "v_mfma_f32_16x16x32_bf16 v[124:127], v[28:31], a[128:131], v[124:127]\n"
  "ds_read_b128 a[136:139], v235 offset:12352\n"
  "v_add_f32 v253, v253, v23\n"
  "v_mfma_f32_16x16x32_bf16 v[124:127], v[24:27], a[132:135], v[124:127]\n"
  "s_add_u32 m0, s72, 2112\n"
  "ds_read_b128 a[140:143], v235 offset:12416\n"
  "v_add_f32 v254, v254, v36\n"
  "v_mfma_f32_16x16x32_bf16 v[156:159], v[44:47], a[128:131], v[156:159]\n"
  "ds_read_b128 a[144:147], v235 offset:13376\n"
  "v_add_f32 v254, v254, v37\n"
  "v_mfma_f32_16x16x32_bf16 v[156:159], v[40:43], a[132:135], v[156:159]\n"
  "ds_read_b128 a[148:151], v235 offset:13440\n"
  "v_add_f32 v254, v254, v38\n"
  "v_mfma_f32_16x16x32_bf16 v[188:191], v[60:63], a[128:131], v[188:191]\n"
  "v_add_f32 v254, v254, v39\n"
  "v_add_f32 v255, v255, v53\n"
  "v_mfma_f32_16x16x32_bf16 v[188:191], v[56:59], a[132:135], v[188:191]\n"
  "buffer_load_dwordx4 v221, s[48:51], 0 offen offset:128 nt lds\n"
  "v_add_f32 v255, v255, v54\n"
  "v_mfma_f32_16x16x32_bf16 a[240:243], a[160:163], a[128:131], a[240:243]\n"
  "ds_read_b128 a[128:131], v235 offset:14400\n"
  "v_add_f32 v255, v255, v55\n"
  "v_mfma_f32_16x16x32_bf16 a[240:243], a[156:159], a[132:135], a[240:243]\n"
  "s_waitcnt lgkmcnt(5)\n"
  "ds_read_b128 a[132:135], v235 offset:14464\n"
  "v_fma_f32 v64, v64, s73, v227\n"
  "v_mfma_f32_16x16x32_bf16 v[192:195], v[60:63], a[120:123], v[192:195]\n"
  "v_exp_f32 v64, v64\n"
  "v_mfma_f32_16x16x32_bf16 v[192:195], v[56:59], a[124:127], v[192:195]\n"
  "s_add_u32 m0, s72, 128\n"
  "v_fma_f32 v65, v65, s73, v227\n"
  "v_mfma_f32_16x16x32_bf16 v[160:163], v[44:47], a[120:123], v[160:163]\n"
  "v_exp_f32 v65, v65\n"
  "v_mfma_f32_16x16x32_bf16 v[160:163], v[40:43], a[124:127], v[160:163]\n"
  "v_fma_f32 v66, v66, s73, v227\n"
  "v_fma_f32 v67, v67, s73, v227\n"
  "v_mfma_f32_16x16x32_bf16 v[128:131], v[28:31], a[120:123], v[128:131]\n"
  "v_exp_f32 v66, v66\n"
  "v_mfma_f32_16x16x32_bf16 v[128:131], v[24:27], a[124:127], v[128:131]\n"
  "buffer_load_dwordx4 v221, s[48:51], 0 offen offset:3200 nt lds\n"
  "v_cvt_pk_bf16_f32 v64, v64, v65\n"
  "v_mfma_f32_16x16x32_bf16 v[96:99], v[12:15], a[120:123], v[96:99]\n"
  "v_exp_f32 v67, v67\n"
  "v_mfma_f32_16x16x32_bf16 v[96:99], v[8:11], a[124:127], v[96:99]\n"
  "s_waitcnt lgkmcnt(4)\n"
  "v_cvt_pk_bf16_f32 v65, v66, v67\n"
  "v_fma_f32 v0, v0, s73, v223\n"
  "v_mfma_f32_16x16x32_bf16 v[4:7], a[136:139], a[0:3], v[248:251]\n"
  "v_exp_f32 v0, v0\n"
  "v_mfma_f32_16x16x32_bf16 v[4:7], a[140:143], a[4:7], v[4:7]\n"
  "s_waitcnt lgkmcnt(2)\n"
  "v_fma_f32 v1, v1, s73, v223\n"
  "v_fma_f32 v2, v2, s73, v223\n"
  "v_mfma_f32_16x16x32_bf16 v[4:7], a[144:147], a[8:11], v[4:7]\n"
  "v_exp_f32 v1, v1\n"
  "v_mfma_f32_16x16x32_bf16 v[4:7], a[148:151], a[12:15], v[4:7]\n"
  "s_waitcnt lgkmcnt(0)\n"
  "v_exp_f32 v2, v2\n"
  "v_mfma_f32_16x16x32_bf16 v[4:7], a[128:131], a[16:19], v[4:7]\n"
  "v_fma_f32 v3, v3, s73, v223\n"
  "v_add_f32 v252, v252, v0\n"
  "v_mfma_f32_16x16x32_bf16 v[4:7], a[132:135], a[20:23], v[4:7]\n"
  "ds_write_b64 v231, v[64:65] offset:2048\n"
  "v_mfma_f32_16x16x32_bf16 v[20:23], a[132:135], a[44:47], v[248:251]\n"
  "v_exp_f32 v3, v3\n"
  "v_mfma_f32_16x16x32_bf16 v[20:23], a[128:131], a[40:43], v[20:23]\n"
  "v_add_f32 v252, v252, v1\n"
  "v_add_f32 v252, v252, v2\n"
  "v_mfma_f32_16x16x32_bf16 v[20:23], a[148:151], a[36:39], v[20:23]\n"
  "ds_read_b64_tr_b16 a[120:121], v219 offset:12368\n"
  "v_add_f32 v252, v252, v3\n"
  "v_mfma_f32_16x16x32_bf16 v[20:23], a[144:147], a[32:35], v[20:23]\n"
  "ds_read_b64_tr_b16 a[122:123], v220 offset:12368\n"
  "v_fma_f32 v16, v16, s73, v224\n"
  "v_mfma_f32_16x16x32_bf16 v[20:23], a[140:143], a[28:31], v[20:23]\n"
  "ds_read_b64_tr_b16 a[124:125], v219 offset:13392\n"
  "v_fma_f32 v17, v17, s73, v224\n"
  "v_mfma_f32_16x16x32_bf16 v[20:23], a[136:139], a[24:27], v[20:23]\n"
  "ds_read_b64_tr_b16 a[126:127], v220 offset:13392\n"
  "v_fma_f32 v18, v18, s73, v224\n"
  "v_mfma_f32_16x16x32_bf16 v[36:39], a[136:139], a[48:51], v[248:251]\n"
  "v_exp_f32 v16, v16\n"
  "v_mfma_f32_16x16x32_bf16 v[36:39], a[140:143], a[52:55], v[36:39]\n"
  "v_exp_f32 v17, v17\n"
  "v_mfma_f32_16x16x32_bf16 v[36:39], a[144:147], a[56:59], v[36:39]\n"
  "v_exp_f32 v18, v18\n"
  "v_mfma_f32_16x16x32_bf16 v[36:39], a[148:151], a[60:63], v[36:39]\n"
  "v_fma_f32 v19, v19, s73, v224\n"
  "v_add_f32 v253, v253, v16\n"
  "v_mfma_f32_16x16x32_bf16 v[36:39], a[128:131], a[64:67], v[36:39]\n"
  "v_exp_f32 v19, v19\n"
  "v_mfma_f32_16x16x32_bf16 v[36:39], a[132:135], a[68:71], v[36:39]\n"
  "v_add_f32 v253, v253, v17\n"
  "v_add_f32 v253, v253, v18\n"
  "v_mfma_f32_16x16x32_bf16 v[52:55], a[132:135], a[92:95], v[248:251]\n"
  "ds_read_b64_tr_b16 a[132:133], v217 offset:14400\n"
  "v_add_f32 v253, v253, v19\n"
  "v_mfma_f32_16x16x32_bf16 v[52:55], a[128:131], a[88:91], v[52:55]\n"
  "ds_read_b64_tr_b16 a[134:135], v218 offset:14400\n"
  "v_fma_f32 v32, v32, s73, v225\n"
  "v_mfma_f32_16x16x32_bf16 v[52:55], a[148:151], a[84:87], v[52:55]\n"
  "ds_read_b64_tr_b16 a[128:129], v217 offset:14416\n"
  "v_fma_f32 v33, v33, s73, v225\n"
  "v_mfma_f32_16x16x32_bf16 v[52:55], a[144:147], a[80:83], v[52:55]\n"
  "ds_read_b64_tr_b16 a[130:131], v218 offset:14416\n"
  "v_fma_f32 v34, v34, s73, v225\n"
  "v_mfma_f32_16x16x32_bf16 v[52:55], a[140:143], a[76:79], v[52:55]\n"
  "v_exp_f32 v32, v32\n"
  "v_mfma_f32_16x16x32_bf16 v[52:55], a[136:139], a[72:75], v[52:55]\n"
  "s_waitcnt lgkmcnt(4)\n"
  "v_exp_f32 v33, v33\n"
  "v_mfma_f32_16x16x32_bf16 v[100:103], v[12:15], a[120:123], v[100:103]\n"
  "v_exp_f32 v34, v34\n"
  "v_mfma_f32_16x16x32_bf16 v[100:103], v[8:11], a[124:127], v[100:103]\n"
  "v_cvt_pk_bf16_f32 v12, v0, v1\n"
  "v_cvt_pk_bf16_f32 v13, v2, v3\n"
  "v_mfma_f32_16x16x32_bf16 v[132:135], v[28:31], a[120:123], v[132:135]\n"
  "ds_read_b64_tr_b16 a[140:141], v219 offset:14400\n"
  "v_cvt_pk_bf16_f32 v28, v16, v17\n"
  "v_mfma_f32_16x16x32_bf16 v[132:135], v[24:27], a[124:127], v[132:135]\n"
  "ds_read_b64_tr_b16 a[142:143], v220 offset:14400\n"
  "v_cvt_pk_bf16_f32 v29, v18, v19\n"
  "v_mfma_f32_16x16x32_bf16 v[164:167], v[44:47], a[120:123], v[164:167]\n"
  "ds_read_b64_tr_b16 a[136:137], v219 offset:14416\n"
  "v_fma_f32 v35, v35, s73, v225\n"
  "v_mfma_f32_16x16x32_bf16 v[164:167], v[40:43], a[124:127], v[164:167]\n"
  "ds_read_b64_tr_b16 a[138:139], v220 offset:14416\n"
  "v_add_f32 v254, v254, v32\n"
  "v_mfma_f32_16x16x32_bf16 v[196:199], v[60:63], a[120:123], v[196:199]\n"
  "v_exp_f32 v35, v35\n"
  "v_mfma_f32_16x16x32_bf16 v[196:199], v[56:59], a[124:127], v[196:199]\n"
  "s_waitcnt lgkmcnt(4)\n"
  "v_add_f32 v254, v254, v33\n"
  "v_add_f32 v254, v254, v34\n"
  "v_mfma_f32_16x16x32_bf16 a[244:247], a[156:159], a[132:135], a[244:247]\n"
  "v_add_f32 v254, v254, v35\n"
  "v_cvt_pk_bf16_f32 v44, v32, v33\n"
  "v_mfma_f32_16x16x32_bf16 a[248:251], a[156:159], a[128:131], a[248:251]\n"
  "v_cvt_pk_bf16_f32 v45, v34, v35\n"
  "v_fma_f32 v48, v48, s73, v226\n"
  "v_mfma_f32_16x16x32_bf16 v[200:203], v[56:59], a[132:135], v[200:203]\n"
  "v_exp_f32 v48, v48\n"
  "v_mfma_f32_16x16x32_bf16 v[204:207], v[56:59], a[128:131], v[204:207]\n"
  "v_fma_f32 v49, v49, s73, v226\n"
  "v_fma_f32 v50, v50, s73, v226\n"
  "v_mfma_f32_16x16x32_bf16 v[168:171], v[40:43], a[132:135], v[168:171]\n"
  "v_exp_f32 v49, v49\n"
  "v_mfma_f32_16x16x32_bf16 v[172:175], v[40:43], a[128:131], v[172:175]\n"
  "s_waitcnt vmcnt(10) lgkmcnt(0)\n"
  "s_barrier\n"
  "ds_read_b128 a[120:123], v216 offset:24704\n"
  "v_mfma_f32_16x16x32_bf16 v[136:139], v[24:27], a[132:135], v[136:139]\n"
  "ds_read_b64 a[160:161], v231 offset:2048\n"
  "v_fma_f32 v51, v51, s73, v226\n"
  "v_mfma_f32_16x16x32_bf16 v[140:143], v[24:27], a[128:131], v[140:143]\n"
  "ds_read_b64 a[162:163], v232 offset:2048\n"
  "v_add_f32 v255, v255, v48\n"
  "v_mfma_f32_16x16x32_bf16 v[104:107], v[8:11], a[132:135], v[104:107]\n"
  "ds_read_b128 a[124:127], v216 offset:24768\n"
  "v_add_f32 v255, v255, v49\n"
  "v_mfma_f32_16x16x32_bf16 v[108:111], v[8:11], a[128:131], v[108:111]\n"
  "ds_read_b128 a[132:135], v216 offset:25728\n"
  "v_cvt_pk_bf16_f32 v60, v48, v49\n"
  "v_mfma_f32_16x16x32_bf16 v[112:115], v[8:11], a[140:143], v[112:115]\n"
  "ds_read_b128 a[128:131], v216 offset:25792\n"
  "v_fma_f32 v52, v52, s73, v226\n"
  "v_mfma_f32_16x16x32_bf16 v[116:119], v[8:11], a[136:139], v[116:119]\n"
  "s_mov_b32 m0, s67\n"
  "ds_read_b128 a[148:151], v216 offset:26752\n"
  "v_fma_f32 v53, v53, s73, v226\n"
  "v_mfma_f32_16x16x32_bf16 v[144:147], v[24:27], a[140:143], v[144:147]\n"
  "ds_read_b128 a[144:147], v216 offset:26816\n"
  "v_fma_f32 v54, v54, s73, v226\n"
  "v_mfma_f32_16x16x32_bf16 v[148:151], v[24:27], a[136:139], v[148:151]\n"
  "v_exp_f32 v50, v50\n"
  "v_mfma_f32_16x16x32_bf16 v[176:179], v[40:43], a[140:143], v[176:179]\n"
  "v_exp_f32 v51, v51\n"
  "v_mfma_f32_16x16x32_bf16 v[180:183], v[40:43], a[136:139], v[180:183]\n"
  "buffer_load_dwordx4 v221, s[40:43], 0 offen offset:0 lds\n"
  "v_mfma_f32_16x16x32_bf16 v[208:211], v[56:59], a[140:143], v[208:211]\n"
  "s_addk_i32 m0, 896\n"
  "v_exp_f32 v52, v52\n"
  "v_mfma_f32_16x16x32_bf16 v[212:215], v[56:59], a[136:139], v[212:215]\n"
  "s_waitcnt lgkmcnt(4)\n"
  "v_exp_f32 v53, v53\n"
  "v_mfma_f32_16x16x32_bf16 v[64:67], a[120:123], a[96:99], v[248:251]\n"
  "v_exp_f32 v54, v54\n"
  "v_mfma_f32_16x16x32_bf16 v[64:67], a[124:127], a[100:103], v[64:67]\n"
  "s_waitcnt lgkmcnt(2)\n"
  "buffer_load_dwordx4 v221, s[40:43], 0 offen offset:128 lds\n"
  "v_add_f32 v255, v255, v50\n"
  "v_mfma_f32_16x16x32_bf16 v[64:67], a[132:135], a[104:107], v[64:67]\n"
  "s_addk_i32 m0, 896\n"
  "v_add_f32 v255, v255, v51\n"
  "v_cvt_pk_bf16_f32 v61, v50, v51\n"
  "v_mfma_f32_16x16x32_bf16 v[64:67], a[128:131], a[108:111], v[64:67]\n"
  "s_waitcnt lgkmcnt(0)\n"
  "v_fma_f32 v55, v55, s73, v226\n"
  "v_cvt_pk_bf16_f32 v62, v52, v53\n"
  "v_mfma_f32_16x16x32_bf16 v[64:67], a[148:151], a[112:115], v[64:67]\n"
  "v_exp_f32 v55, v55\n"
  "v_mfma_f32_16x16x32_bf16 v[64:67], a[144:147], a[116:119], v[64:67]\n"
  "buffer_load_dwordx4 v221, s[40:43], 0 offen offset:256 lds\n"
  "v_cvt_pk_bf16_f32 v63, v54, v55\n"
  "v_mfma_f32_16x16x32_bf16 v[0:3], a[144:147], a[20:23], v[248:251]\n"
  "v_fma_f32 v36, v36, s73, v225\n"
  "v_fma_f32 v37, v37, s73, v225\n"
  "v_mfma_f32_16x16x32_bf16 v[0:3], a[148:151], a[16:19], v[0:3]\n"
  "s_add_u32 s40, s40, s31\n"
  "v_exp_f32 v36, v36\n"
  "v_mfma_f32_16x16x32_bf16 v[0:3], a[128:131], a[12:15], v[0:3]\n"
  "s_addc_u32 s41, s41, 0\n"
  "v_exp_f32 v37, v37\n"
  "v_mfma_f32_16x16x32_bf16 v[0:3], a[132:135], a[8:11], v[0:3]\n"
  "s_mov_b32 m0, s59\n"
  "v_fma_f32 v38, v38, s73, v225\n"
  "v_fma_f32 v39, v39, s73, v225\n"
  "v_mfma_f32_16x16x32_bf16 v[0:3], a[124:127], a[4:7], v[0:3]\n"
  "v_exp_f32 v38, v38\n"
  "v_mfma_f32_16x16x32_bf16 v[0:3], a[120:123], a[0:3], v[0:3]\n"
  "v_exp_f32 v39, v39\n"
  "v_mfma_f32_16x16x32_bf16 v[16:19], a[120:123], a[24:27], v[248:251]\n"
  "v_cvt_pk_bf16_f32 v46, v36, v37\n"
  "v_cvt_pk_bf16_f32 v47, v38, v39\n"
  "v_mfma_f32_16x16x32_bf16 v[16:19], a[124:127], a[28:31], v[16:19]\n"
  "buffer_load_dwordx4 v221, s[32:35], 0 offen offset:0 lds\n"
  "v_fma_f32 v20, v20, s73, v224\n"
  "v_mfma_f32_16x16x32_bf16 v[16:19], a[132:135], a[32:35], v[16:19]\n"
  "s_addk_i32 m0, 1024\n"
  "ds_read_b64_tr_b16 a[140:141], v217 offset:24704\n"
  "v_fma_f32 v21, v21, s73, v224\n"
  "v_mfma_f32_16x16x32_bf16 v[16:19], a[128:131], a[36:39], v[16:19]\n"
  "s_mov_b64 s[32:33], s[44:45]\n"
  "ds_read_b64_tr_b16 a[142:143], v218 offset:24704\n"
  "v_fma_f32 v22, v22, s73, v224\n"
  "v_mfma_f32_16x16x32_bf16 v[16:19], a[148:151], a[40:43], v[16:19]\n"
  "ds_read_b64_tr_b16 a[136:137], v217 offset:25728\n"
  "v_fma_f32 v23, v23, s73, v224\n"
  "v_mfma_f32_16x16x32_bf16 v[16:19], a[144:147], a[44:47], v[16:19]\n"
  "buffer_load_dwordx4 v221, s[44:47], 0 offen offset:0 lds\n"
  "v_fma_f32 v4, v4, s73, v223\n"
  "v_mfma_f32_16x16x32_bf16 v[32:35], a[144:147], a[68:71], v[248:251]\n"
  "s_addk_i32 m0, 768\n"
  "ds_read_b64_tr_b16 a[138:139], v218 offset:25728\n"
  "v_fma_f32 v5, v5, s73, v223\n"
  "v_mfma_f32_16x16x32_bf16 v[32:35], a[148:151], a[64:67], v[32:35]\n"
  "v_exp_f32 v20, v20\n"
  "v_mfma_f32_16x16x32_bf16 v[32:35], a[128:131], a[60:63], v[32:35]\n"
  "v_exp_f32 v21, v21\n"
  "v_mfma_f32_16x16x32_bf16 v[32:35], a[132:135], a[56:59], v[32:35]\n"
  "buffer_load_dwordx4 v221, s[44:47], 0 offen offset:256 lds\n"
  "v_cvt_pk_bf16_f32 v30, v20, v21\n"
  "v_mfma_f32_16x16x32_bf16 v[32:35], a[124:127], a[52:55], v[32:35]\n"
  "s_add_u32 s44, s44, s31\n"
  "v_exp_f32 v22, v22\n"
  "v_mfma_f32_16x16x32_bf16 v[32:35], a[120:123], a[48:51], v[32:35]\n"
  "s_addc_u32 s45, s45, 0\n"
  "v_exp_f32 v23, v23\n"
  "v_mfma_f32_16x16x32_bf16 v[48:51], a[120:123], a[72:75], v[248:251]\n"
  "v_exp_f32 v4, v4\n"
  "v_mfma_f32_16x16x32_bf16 v[48:51], a[124:127], a[76:79], v[48:51]\n"
  "v_exp_f32 v5, v5\n"
  "v_mfma_f32_16x16x32_bf16 v[48:51], a[132:135], a[80:83], v[48:51]\n"
  "ds_read_b64_tr_b16 a[120:121], v217 offset:24720\n"
  "v_cvt_pk_bf16_f32 v31, v22, v23\n"
  "v_mfma_f32_16x16x32_bf16 v[48:51], a[128:131], a[84:87], v[48:51]\n"
  "ds_read_b64_tr_b16 a[122:123], v218 offset:24720\n"
  "v_fma_f32 v6, v6, s73, v223\n"
  "v_mfma_f32_16x16x32_bf16 v[48:51], a[148:151], a[88:91], v[48:51]\n"
  "ds_read_b64_tr_b16 a[124:125], v217 offset:25744\n"
  "v_fma_f32 v7, v7, s73, v223\n"
  "v_mfma_f32_16x16x32_bf16 v[48:51], a[144:147], a[92:95], v[48:51]\n"
  "s_waitcnt lgkmcnt(3)\n"
  "ds_read_b64_tr_b16 a[126:127], v218 offset:25744\n"
  "v_cvt_pk_bf16_f32 v14, v4, v5\n"
  "v_mfma_f32_16x16x32_bf16 a[228:231], a[156:159], a[140:143], a[228:231]\n"
  "v_exp_f32 v6, v6\n"
  "v_mfma_f32_16x16x32_bf16 a[228:231], a[160:163], a[136:139], a[228:231]\n"
  "v_exp_f32 v7, v7\n"
  "v_mfma_f32_16x16x32_bf16 a[212:215], v[56:59], a[140:143], a[212:215]\n"
  "v_cvt_pk_bf16_f32 v15, v6, v7\n"
  "v_add_f32 v252, v252, v4\n"
  "v_mfma_f32_16x16x32_bf16 a[212:215], v[60:63], a[136:139], a[212:215]\n"
  "v_add_f32 v252, v252, v5\n"
  "v_add_f32 v252, v252, v6\n"
  "v_mfma_f32_16x16x32_bf16 a[196:199], v[40:43], a[140:143], a[196:199]\n"
  "v_add_f32 v252, v252, v7\n"
  "v_add_f32 v253, v253, v20\n"
  "v_mfma_f32_16x16x32_bf16 a[196:199], v[44:47], a[136:139], a[196:199]\n"
  "v_add_f32 v253, v253, v21\n"
  "v_add_f32 v253, v253, v22\n"
  "v_mfma_f32_16x16x32_bf16 a[180:183], v[24:27], a[140:143], a[180:183]\n"
  "ds_read_b64_tr_b16 a[132:133], v219 offset:24704\n"
  "v_add_f32 v253, v253, v23\n"
  "v_mfma_f32_16x16x32_bf16 a[180:183], v[28:31], a[136:139], a[180:183]\n"
  "ds_read_b64_tr_b16 a[134:135], v220 offset:24704\n"
  "v_add_f32 v254, v254, v36\n"
  "v_mfma_f32_16x16x32_bf16 a[164:167], v[8:11], a[140:143], a[164:167]\n"
  "ds_read_b64_tr_b16 a[128:129], v219 offset:25728\n"
  "v_add_f32 v254, v254, v37\n"
  "v_mfma_f32_16x16x32_bf16 a[164:167], v[12:15], a[136:139], a[164:167]\n"
  "ds_read_b64_tr_b16 a[130:131], v220 offset:25728\n"
  "v_add_f32 v254, v254, v38\n"
  "v_mfma_f32_16x16x32_bf16 v[84:87], a[156:159], a[152:155], v[84:87]\n"
  "v_add_f32 v254, v254, v39\n"
  "v_add_f32 v255, v255, v52\n"
  "v_mfma_f32_16x16x32_bf16 v[84:87], a[160:163], a[152:155], v[84:87]\n"
  "s_waitcnt lgkmcnt(4)\n"
  "v_add_f32 v255, v255, v53\n"
  "v_add_f32 v255, v255, v54\n"
  "v_mfma_f32_16x16x32_bf16 a[168:171], v[8:11], a[120:123], a[168:171]\n"
  "ds_read_b64_tr_b16 v[246:247], v230 offset:5632\n"
  "v_add_f32 v255, v255, v55\n"
  "v_mfma_f32_16x16x32_bf16 a[168:171], v[12:15], a[124:127], a[168:171]\n"
  "v_fma_f32 v64, v64, s73, v227\n"
  "v_fma_f32 v65, v65, s73, v227\n"
  "v_mfma_f32_16x16x32_bf16 a[184:187], v[24:27], a[120:123], a[184:187]\n"
  "ds_read_b128 a[140:143], v235 offset:24704\n"
  "v_fma_f32 v66, v66, s73, v227\n"
  "v_mfma_f32_16x16x32_bf16 a[184:187], v[28:31], a[124:127], a[184:187]\n"
  "ds_read_b128 a[136:139], v235 offset:24768\n"
  "v_fma_f32 v67, v67, s73, v227\n"
  "v_mfma_f32_16x16x32_bf16 a[200:203], v[40:43], a[120:123], a[200:203]\n"
  "ds_read_b128 a[148:151], v235 offset:25728\n"
  "v_fma_f32 v0, v0, s73, v223\n"
  "v_mfma_f32_16x16x32_bf16 a[200:203], v[44:47], a[124:127], a[200:203]\n"
  "ds_read_b128 a[144:147], v235 offset:25792\n"
  "v_fma_f32 v1, v1, s73, v223\n"
  "v_mfma_f32_16x16x32_bf16 a[216:219], v[56:59], a[120:123], a[216:219]\n"
  "v_exp_f32 v64, v64\n"
  "v_mfma_f32_16x16x32_bf16 a[216:219], v[60:63], a[124:127], a[216:219]\n"
  "v_exp_f32 v65, v65\n"
  "v_mfma_f32_16x16x32_bf16 a[232:235], a[156:159], a[120:123], a[232:235]\n"
  "ds_read_b128 a[120:123], v235 offset:26752\n"
  "v_cvt_pk_bf16_f32 v64, v64, v65\n"
  "v_mfma_f32_16x16x32_bf16 a[232:235], a[160:163], a[124:127], a[232:235]\n"
  "s_waitcnt lgkmcnt(6)\n"
  "ds_read_b128 a[124:127], v235 offset:26816\n"
  "v_fma_f32 v2, v2, s73, v223\n"
  "v_mfma_f32_16x16x32_bf16 a[220:223], v[56:59], a[132:135], a[220:223]\n"
  "v_exp_f32 v66, v66\n"
  "v_mfma_f32_16x16x32_bf16 a[220:223], v[60:63], a[128:131], a[220:223]\n"
  "v_exp_f32 v67, v67\n"
  "v_mfma_f32_16x16x32_bf16 a[204:207], v[40:43], a[132:135], a[204:207]\n"
  "v_exp_f32 v0, v0\n"
  "v_mfma_f32_16x16x32_bf16 a[204:207], v[44:47], a[128:131], a[204:207]\n"
  "buffer_store_dwordx4 a[252:255], v228, s[52:55], s82 offen offset:0 nt\n"
  "v_cvt_pk_bf16_f32 v65, v66, v67\n"
  "v_exp_f32 v1, v1\n"
  "v_mfma_f32_16x16x32_bf16 a[188:191], v[24:27], a[132:135], a[188:191]\n"
  "ds_read_b64_tr_b16 a[252:253], v230 offset:6720\n"
  "ds_read_b64_tr_b16 a[254:255], v230 offset:6752\n"
  "v_mfma_f32_16x16x32_bf16 a[188:191], v[28:31], a[128:131], a[188:191]\n"
  "ds_write_b64 v231, v[64:65] offset:0\n"
  "v_mfma_f32_16x16x32_bf16 a[172:175], v[8:11], a[132:135], a[172:175]\n"
  "v_exp_f32 v2, v2\n"
  "v_mfma_f32_16x16x32_bf16 a[172:175], v[12:15], a[128:131], a[172:175]\n"
  "s_waitcnt lgkmcnt(7)\n"
  "v_fma_f32 v3, v3, s73, v223\n"
  "v_add_f32 v252, v252, v0\n"
  "v_mfma_f32_16x16x32_bf16 v[4:7], a[140:143], a[0:3], v[248:251]\n"
  "v_exp_f32 v3, v3\n"
  "v_mfma_f32_16x16x32_bf16 v[4:7], a[136:139], a[4:7], v[4:7]\n"
  "s_waitcnt lgkmcnt(5)\n"
  "v_add_f32 v252, v252, v1\n"
  "v_add_f32 v252, v252, v2\n"
  "v_mfma_f32_16x16x32_bf16 v[4:7], a[148:151], a[8:11], v[4:7]\n"
  "v_add_f32 v252, v252, v3\n"
  "v_fma_f32 v16, v16, s73, v224\n"
  "v_mfma_f32_16x16x32_bf16 v[4:7], a[144:147], a[12:15], v[4:7]\n"
  "s_waitcnt lgkmcnt(3)\n"
  "v_exp_f32_e64 v16, v16\n"
  "v_mfma_f32_16x16x32_bf16 v[4:7], a[120:123], a[16:19], v[4:7]\n"
  "v_fma_f32 v17, v17, s73, v224\n"
  "v_fma_f32 v18, v18, s73, v224\n"
  "v_mfma_f32_16x16x32_bf16 v[4:7], a[124:127], a[20:23], v[4:7]\n"
  "buffer_store_dwordx4 v[244:247], v228, s[52:55], s82 offen offset:3072 nt\n"
  "v_exp_f32 v17, v17\n"
  "v_fma_f32 v19, v19, s73, v224\n"
  "v_mfma_f32_16x16x32_bf16 v[20:23], a[124:127], a[44:47], v[248:251]\n"
  "v_exp_f32 v18, v18\n"
  "v_mfma_f32_16x16x32_bf16 v[20:23], a[120:123], a[40:43], v[20:23]\n"
  "v_exp_f32 v19, v19\n"
  "v_mfma_f32_16x16x32_bf16 v[20:23], a[144:147], a[36:39], v[20:23]\n"
  "ds_read_b64_tr_b16 a[132:133], v219 offset:24720\n"
  "v_add_f32 v253, v253, v16\n"
  "v_mfma_f32_16x16x32_bf16 v[20:23], a[148:151], a[32:35], v[20:23]\n"
  "ds_read_b64_tr_b16 a[134:135], v220 offset:24720\n"
  "v_add_f32 v253, v253, v17\n"
  "v_mfma_f32_16x16x32_bf16 v[20:23], a[136:139], a[28:31], v[20:23]\n"
  "ds_read_b64_tr_b16 a[128:129], v219 offset:25744\n"
  "v_add_f32_e64 v253, v253, v18\n"
  "v_mfma_f32_16x16x32_bf16 v[20:23], a[140:143], a[24:27], v[20:23]\n"
  "s_waitcnt lgkmcnt(4)\n"
  "buffer_store_dwordx4 a[252:255], v228, s[52:55], s83 offen offset:0 nt\n"
  "ds_read_b64_tr_b16 a[130:131], v220 offset:25744\n"
  "v_add_f32 v253, v253, v19\n"
  "v_fma_f32 v32, v32, s73, v225\n"
  "v_mfma_f32_16x16x32_bf16 v[36:39], a[140:143], a[48:51], v[248:251]\n"
  "v_exp_f32 v32, v32\n"
  "v_mfma_f32_16x16x32_bf16 v[36:39], a[136:139], a[52:55], v[36:39]\n"
  "v_fma_f32 v33, v33, s73, v225\n"
  "v_fma_f32 v34, v34, s73, v225\n"
  "v_mfma_f32_16x16x32_bf16 v[36:39], a[148:151], a[56:59], v[36:39]\n"
  "v_exp_f32 v33, v33\n"
  "v_mfma_f32_16x16x32_bf16 v[36:39], a[144:147], a[60:63], v[36:39]\n"
  "v_exp_f32 v34, v34\n"
  "v_mfma_f32_16x16x32_bf16 v[36:39], a[120:123], a[64:67], v[36:39]\n"
  "v_fma_f32 v35, v35, s73, v225\n"
  "v_add_f32 v254, v254, v32\n"
  "v_mfma_f32_16x16x32_bf16 v[36:39], a[124:127], a[68:71], v[36:39]\n"
  "v_exp_f32 v35, v35\n"
  "v_mfma_f32_16x16x32_bf16 v[52:55], a[124:127], a[92:95], v[248:251]\n"
  "ds_read_b64_tr_b16 a[124:125], v217 offset:26752\n"
  "v_add_f32 v254, v254, v33\n"
  "v_mfma_f32_16x16x32_bf16 v[52:55], a[120:123], a[88:91], v[52:55]\n"
  "ds_read_b64_tr_b16 a[126:127], v218 offset:26752\n"
  "v_add_f32 v254, v254, v34\n"
  "v_mfma_f32_16x16x32_bf16 v[52:55], a[144:147], a[84:87], v[52:55]\n"
  "ds_read_b64_tr_b16 a[120:121], v217 offset:26768\n"
  "v_add_f32 v254, v254, v35\n"
  "v_mfma_f32_16x16x32_bf16 v[52:55], a[148:151], a[80:83], v[52:55]\n"
  "ds_read_b64_tr_b16 a[122:123], v218 offset:26768\n"
  "v_fma_f32 v48, v48, s73, v226\n"
  "v_mfma_f32_16x16x32_bf16 v[52:55], a[136:139], a[76:79], v[52:55]\n"
  "v_exp_f32 v48, v48\n"
  "v_mfma_f32_16x16x32_bf16 v[52:55], a[140:143], a[72:75], v[52:55]\n"
  "s_waitcnt lgkmcnt(4)\n"
  "v_fma_f32 v49, v49, s73, v226\n"
  "v_fma_f32 v50, v50, s73, v226\n"
  "v_mfma_f32_16x16x32_bf16 a[176:179], v[8:11], a[132:135], a[176:179]\n"
  "v_exp_f32 v49, v49\n"
  "v_mfma_f32_16x16x32_bf16 a[176:179], v[12:15], a[128:131], a[176:179]\n"
  "v_exp_f32 v50, v50\n"
  "v_mfma_f32_16x16x32_bf16 a[192:195], v[24:27], a[132:135], a[192:195]\n"
  "ds_read_b64_tr_b16 a[136:137], v219 offset:26752\n"
  "v_cvt_pk_bf16_f32 v8, v0, v1\n"
  "v_mfma_f32_16x16x32_bf16 a[192:195], v[28:31], a[128:131], a[192:195]\n"
  "ds_read_b64_tr_b16 a[138:139], v220 offset:26752\n"
  "v_cvt_pk_bf16_f32 v9, v2, v3\n"
  "v_mfma_f32_16x16x32_bf16 a[208:211], v[40:43], a[132:135], a[208:211]\n"
  "ds_read_b64_tr_b16 a[140:141], v219 offset:26768\n"
  "v_cvt_pk_bf16_f32 v24, v16, v17\n"
  "v_mfma_f32_16x16x32_bf16 a[208:211], v[44:47], a[128:131], a[208:211]\n"
  "ds_read_b64_tr_b16 a[142:143], v220 offset:26768\n"
  "v_cvt_pk_bf16_f32 v25, v18, v19\n"
  "v_mfma_f32_16x16x32_bf16 a[224:227], v[56:59], a[132:135], a[224:227]\n"
  "v_cvt_pk_bf16_f32 v40, v32, v33\n"
  "v_cvt_pk_bf16_f32 v41, v34, v35\n"
  "v_mfma_f32_16x16x32_bf16 a[224:227], v[60:63], a[128:131], a[224:227]\n"
  "s_waitcnt lgkmcnt(4)\n"
  "v_fma_f32 v51, v51, s73, v226\n"
  "v_add_f32 v255, v255, v48\n"
  "v_mfma_f32_16x16x32_bf16 a[244:247], a[160:163], a[124:127], a[244:247]\n"
  "v_exp_f32 v51, v51\n"
  "v_mfma_f32_16x16x32_bf16 a[248:251], a[160:163], a[120:123], a[248:251]\n"
  "v_add_f32 v255, v255, v49\n"
  "v_add_f32 v255, v255, v50\n"
  "v_mfma_f32_16x16x32_bf16 v[200:203], v[60:63], a[124:127], v[200:203]\n"
  "v_add_f32 v255, v255, v51\n"
  "v_cvt_pk_bf16_f32 v56, v48, v49\n"
  "v_mfma_f32_16x16x32_bf16 v[204:207], v[60:63], a[120:123], v[204:207]\n"
  "v_cvt_pk_bf16_f32 v57, v50, v51\n"
  "v_fma_f32 v52, v52, s73, v226\n"
  "v_mfma_f32_16x16x32_bf16 v[168:171], v[44:47], a[124:127], v[168:171]\n"
  "v_exp_f32 v52, v52\n"
  "v_mfma_f32_16x16x32_bf16 v[172:175], v[44:47], a[120:123], v[172:175]\n"
  "s_waitcnt vmcnt(13) lgkmcnt(0)\n"
  "s_barrier\n"
  "ds_read_b128 a[132:135], v216 offset:0\n"
  "v_mfma_f32_16x16x32_bf16 v[136:139], v[28:31], a[124:127], v[136:139]\n"
  "ds_read_b64 a[156:157], v231 offset:0\n"
  "v_fma_f32 v53, v53, s73, v226\n"
  "v_mfma_f32_16x16x32_bf16 v[140:143], v[28:31], a[120:123], v[140:143]\n"
  "ds_read_b64 a[158:159], v232 offset:0\n"
  "v_fma_f32 v54, v54, s73, v226\n"
  "v_mfma_f32_16x16x32_bf16 v[104:107], v[12:15], a[124:127], v[104:107]\n"
  "ds_read_b128 a[128:131], v216 offset:64\n"
  "v_fma_f32 v55, v55, s73, v226\n"
  "v_mfma_f32_16x16x32_bf16 v[108:111], v[12:15], a[120:123], v[108:111]\n"
  "ds_read_b128 a[124:127], v216 offset:1024\n"
  "v_fma_f32 v36, v36, s73, v225\n"
  "v_mfma_f32_16x16x32_bf16 v[112:115], v[12:15], a[136:139], v[112:115]\n"
  "ds_read_b128 a[120:123], v216 offset:1088\n"
  "v_fma_f32 v37, v37, s73, v225\n"
  "v_mfma_f32_16x16x32_bf16 v[116:119], v[12:15], a[140:143], v[116:119]\n"
  "s_mov_b32 m0, s68\n"
  "ds_read_b128 a[144:147], v216 offset:2048\n"
  "v_fma_f32 v38, v38, s73, v225\n"
  "v_mfma_f32_16x16x32_bf16 v[144:147], v[28:31], a[136:139], v[144:147]\n"
  "ds_read_b128 a[148:151], v216 offset:2112\n"
  "v_fma_f32 v39, v39, s73, v225\n"
  "v_mfma_f32_16x16x32_bf16 v[148:151], v[28:31], a[140:143], v[148:151]\n"
  "v_exp_f32 v53, v53\n"
  "v_mfma_f32_16x16x32_bf16 v[176:179], v[44:47], a[136:139], v[176:179]\n"
  "v_exp_f32 v54, v54\n"
  "v_mfma_f32_16x16x32_bf16 v[180:183], v[44:47], a[140:143], v[180:183]\n"
  "buffer_load_dwordx4 v221, s[40:43], 0 offen offset:0 lds\n"
  "v_mfma_f32_16x16x32_bf16 v[208:211], v[60:63], a[136:139], v[208:211]\n"
  "s_addk_i32 m0, 896\n"
  "v_exp_f32 v55, v55\n"
  "v_mfma_f32_16x16x32_bf16 v[212:215], v[60:63], a[140:143], v[212:215]\n"
  "s_waitcnt lgkmcnt(4)\n"
  "v_exp_f32 v36, v36\n"
  "v_mfma_f32_16x16x32_bf16 v[64:67], a[132:135], a[96:99], v[248:251]\n"
  "v_exp_f32 v37, v37\n"
  "v_mfma_f32_16x16x32_bf16 v[64:67], a[128:131], a[100:103], v[64:67]\n"
  "s_waitcnt lgkmcnt(2)\n"
  "buffer_load_dwordx4 v221, s[40:43], 0 offen offset:128 lds\n"
  "v_cvt_pk_bf16_f32 v58, v52, v53\n"
  "v_mfma_f32_16x16x32_bf16 v[64:67], a[124:127], a[104:107], v[64:67]\n"
  "s_addk_i32 m0, 896\n"
  "v_exp_f32 v38, v38\n"
  "v_mfma_f32_16x16x32_bf16 v[64:67], a[120:123], a[108:111], v[64:67]\n"
  "s_waitcnt lgkmcnt(0)\n"
  "v_exp_f32 v39, v39\n"
  "v_mfma_f32_16x16x32_bf16 v[64:67], a[144:147], a[112:115], v[64:67]\n"
  "v_cvt_pk_bf16_f32 v59, v54, v55\n"
  "v_cvt_pk_bf16_f32 v42, v36, v37\n"
  "v_mfma_f32_16x16x32_bf16 v[64:67], a[148:151], a[116:119], v[64:67]\n"
  "buffer_load_dwordx4 v221, s[40:43], 0 offen offset:256 lds\n"
  "v_cvt_pk_bf16_f32 v43, v38, v39\n"
  "v_mfma_f32_16x16x32_bf16 v[0:3], a[148:151], a[20:23], v[248:251]\n"
  "v_fma_f32 v20, v20, s73, v224\n"
  "v_fma_f32 v21, v21, s73, v224\n"
  "v_mfma_f32_16x16x32_bf16 v[0:3], a[144:147], a[16:19], v[0:3]\n"
  "s_add_u32 s40, s40, s31\n"
  "v_exp_f32 v20, v20\n"
  "v_mfma_f32_16x16x32_bf16 v[0:3], a[120:123], a[12:15], v[0:3]\n"
  "s_addc_u32 s41, s41, 0\n"
  "v_exp_f32 v21, v21\n"
  "v_mfma_f32_16x16x32_bf16 v[0:3], a[124:127], a[8:11], v[0:3]\n"
  "s_add_u32 m0, s60, -128\n"
  "v_fma_f32 v22, v22, s73, v224\n"
  "v_add_f32 v253, v253, v20\n"
  "v_mfma_f32_16x16x32_bf16 v[0:3], a[128:131], a[4:7], v[0:3]\n"
  "v_exp_f32 v22, v22\n"
  "v_mfma_f32_16x16x32_bf16 v[0:3], a[132:135], a[0:3], v[0:3]\n"
  "v_fma_f32 v23, v23, s73, v224\n"
  "v_cvt_pk_bf16_f32 v26, v20, v21\n"
  "v_mfma_f32_16x16x32_bf16 v[16:19], a[132:135], a[24:27], v[248:251]\n"
  "v_exp_f32 v23, v23\n"
  "v_mfma_f32_16x16x32_bf16 v[16:19], a[128:131], a[28:31], v[16:19]\n"
  "buffer_load_dwordx4 v221, s[32:35], 0 offen offset:128 lds\n"
  "v_cvt_pk_bf16_f32 v27, v22, v23\n"
  "v_mfma_f32_16x16x32_bf16 v[16:19], a[124:127], a[32:35], v[16:19]\n"
  "s_addk_i32 m0, 1024\n"
  "ds_read_b64_tr_b16 a[136:137], v217 offset:0\n"
  "v_fma_f32 v4, v4, s73, v223\n"
  "v_mfma_f32_16x16x32_bf16 v[16:19], a[120:123], a[36:39], v[16:19]\n"
  "s_mov_b64 s[32:33], s[44:45]\n"
  "ds_read_b64_tr_b16 a[138:139], v218 offset:0\n"
  "v_fma_f32 v5, v5, s73, v223\n"
  "v_mfma_f32_16x16x32_bf16 v[16:19], a[144:147], a[40:43], v[16:19]\n"
  "ds_read_b64_tr_b16 a[140:141], v217 offset:1024\n"
  "v_fma_f32 v6, v6, s73, v223\n"
  "v_mfma_f32_16x16x32_bf16 v[16:19], a[148:151], a[44:47], v[16:19]\n"
  "buffer_load_dwordx4 v221, s[44:47], 0 offen offset:128 lds\n"
  "v_fma_f32 v7, v7, s73, v223\n"
  "v_mfma_f32_16x16x32_bf16 v[32:35], a[148:151], a[68:71], v[248:251]\n"
  "s_addk_i32 m0, 896\n"
  "ds_read_b64_tr_b16 a[142:143], v218 offset:1024\n"
  "v_add_f32 v253, v253, v21\n"
  "v_mfma_f32_16x16x32_bf16 v[32:35], a[144:147], a[64:67], v[32:35]\n"
  "v_exp_f32 v4, v4\n"
  "v_mfma_f32_16x16x32_bf16 v[32:35], a[120:123], a[60:63], v[32:35]\n"
  "v_exp_f32 v5, v5\n"
  "v_mfma_f32_16x16x32_bf16 v[32:35], a[124:127], a[56:59], v[32:35]\n"
  "buffer_load_dwordx4 v221, s[44:47], 0 offen offset:256 lds\n"
  "v_cvt_pk_bf16_f32 v10, v4, v5\n"
  "v_mfma_f32_16x16x32_bf16 v[32:35], a[128:131], a[52:55], v[32:35]\n"
  "s_add_u32 s44, s44, s31\n"
  "v_exp_f32 v6, v6\n"
  "v_mfma_f32_16x16x32_bf16 v[32:35], a[132:135], a[48:51], v[32:35]\n"
  "s_addc_u32 s45, s45, 0\n"
  "v_exp_f32 v7, v7\n"
  "v_mfma_f32_16x16x32_bf16 v[48:51], a[132:135], a[72:75], v[248:251]\n"
  "ds_read_b64_tr_b16 a[132:133], v217 offset:16\n"
  "v_cvt_pk_bf16_f32 v11, v6, v7\n"
  "v_mfma_f32_16x16x32_bf16 v[48:51], a[128:131], a[76:79], v[48:51]\n"
  "ds_read_b64_tr_b16 a[134:135], v218 offset:16\n"
  "v_add_f32 v252, v252, v4\n"
  "v_mfma_f32_16x16x32_bf16 v[48:51], a[124:127], a[80:83], v[48:51]\n"
  "ds_read_b64_tr_b16 a[128:129], v217 offset:1040\n"
  "v_add_f32 v252, v252, v5\n"
  "v_mfma_f32_16x16x32_bf16 v[48:51], a[120:123], a[84:87], v[48:51]\n"
  "s_add_u32 m0, s72, 4224\n"
  "ds_read_b64_tr_b16 a[130:131], v218 offset:1040\n"
  "v_add_f32 v252, v252, v6\n"
  "v_mfma_f32_16x16x32_bf16 v[48:51], a[144:147], a[88:91], v[48:51]\n"
  "v_add_f32 v252, v252, v7\n"
  "v_add_f32 v253, v253, v22\n"
  "v_mfma_f32_16x16x32_bf16 v[48:51], a[148:151], a[92:95], v[48:51]\n"
  "s_waitcnt lgkmcnt(4)\n"
  "v_add_f32 v253, v253, v23\n"
  "v_add_f32 v254, v254, v36\n"
  "v_mfma_f32_16x16x32_bf16 a[236:239], a[160:163], a[136:139], a[236:239]\n"
  "v_add_f32 v254, v254, v37\n"
  "v_add_f32 v254, v254, v38\n"
  "v_mfma_f32_16x16x32_bf16 a[236:239], a[156:159], a[140:143], a[236:239]\n"
  "buffer_load_dwordx4 v221, s[48:51], 0 offen offset:256 nt lds\n"
  "v_add_f32 v254, v254, v39\n"
  "v_mfma_f32_16x16x32_bf16 v[184:187], v[60:63], a[136:139], v[184:187]\n"
  "v_add_f32 v255, v255, v52\n"
  "v_add_f32 v255, v255, v53\n"
  "v_mfma_f32_16x16x32_bf16 v[184:187], v[56:59], a[140:143], v[184:187]\n"
  "v_add_f32 v255, v255, v54\n"
  "v_add_f32 v255, v255, v55\n"
  "v_mfma_f32_16x16x32_bf16 v[152:155], v[44:47], a[136:139], v[152:155]\n"
  "ds_read_b64_tr_b16 a[124:125], v219 offset:0\n"
  "v_fma_f32 v64, v64, s73, v227\n"
  "v_mfma_f32_16x16x32_bf16 v[152:155], v[40:43], a[140:143], v[152:155]\n"
  "s_add_u32 m0, s72, 2240\n"
  "ds_read_b64_tr_b16 a[126:127], v220 offset:0\n"
  "v_mfma_f32_16x16x32_bf16 v[120:123], v[28:31], a[136:139], v[120:123]\n"
  "ds_read_b64_tr_b16 a[120:121], v219 offset:1024\n"
  "v_fma_f32 v65, v65, s73, v227\n"
  "v_mfma_f32_16x16x32_bf16 v[120:123], v[24:27], a[140:143], v[120:123]\n"
  "ds_read_b64_tr_b16 a[122:123], v220 offset:1024\n"
  "v_fma_f32 v66, v66, s73, v227\n"
  "v_mfma_f32_16x16x32_bf16 v[88:91], v[12:15], a[136:139], v[88:91]\n"
  "v_exp_f32 v64, v64\n"
  "v_mfma_f32_16x16x32_bf16 v[88:91], v[8:11], a[140:143], v[88:91]\n"
  "s_waitcnt lgkmcnt(4)\n"
  "buffer_load_dwordx4 v221, s[48:51], 0 offen offset:3328 nt lds\n"
  "v_fma_f32 v67, v67, s73, v227\n"
  "v_mfma_f32_16x16x32_bf16 v[92:95], v[12:15], a[132:135], v[92:95]\n"
  "v_exp_f32 v65, v65\n"
  "v_mfma_f32_16x16x32_bf16 v[92:95], v[8:11], a[128:131], v[92:95]\n"
  "v_exp_f32 v66, v66\n"
  "v_mfma_f32_16x16x32_bf16 v[124:127], v[28:31], a[132:135], v[124:127]\n"
  "ds_read_b128 a[136:139], v235 offset:0\n"
  "v_cvt_pk_bf16_f32 v64, v64, v65\n"
  "v_mfma_f32_16x16x32_bf16 v[124:127], v[24:27], a[128:131], v[124:127]\n"
  "ds_read_b128 a[140:143], v235 offset:64\n"
  "v_fma_f32 v0, v0, s73, v223\n"
  "v_mfma_f32_16x16x32_bf16 v[156:159], v[44:47], a[132:135], v[156:159]\n"
  "ds_read_b128 a[144:147], v235 offset:1024\n"
  "v_fma_f32 v1, v1, s73, v223\n"
  "v_mfma_f32_16x16x32_bf16 v[156:159], v[40:43], a[128:131], v[156:159]\n"
  "ds_read_b128 a[148:151], v235 offset:1088\n"
  "v_fma_f32 v2, v2, s73, v223\n"
  "v_mfma_f32_16x16x32_bf16 v[188:191], v[60:63], a[132:135], v[188:191]\n"
  "v_exp_f32 v67, v67\n"
  "v_mfma_f32_16x16x32_bf16 v[188:191], v[56:59], a[128:131], v[188:191]\n"
  "v_exp_f32 v0, v0\n"
  "v_mfma_f32_16x16x32_bf16 a[240:243], a[160:163], a[132:135], a[240:243]\n"
  "ds_read_b128 a[132:135], v235 offset:2048\n"
  "v_cvt_pk_bf16_f32 v65, v66, v67\n"
  "v_mfma_f32_16x16x32_bf16 a[240:243], a[156:159], a[128:131], a[240:243]\n"
  "s_waitcnt lgkmcnt(5)\n"
  "ds_read_b128 a[128:131], v235 offset:2112\n"
  "v_fma_f32 v3, v3, s73, v223\n"
  "v_mfma_f32_16x16x32_bf16 v[192:195], v[60:63], a[124:127], v[192:195]\n"
  "v_exp_f32_e64 v1, v1\n"
  "v_mfma_f32_16x16x32_bf16 v[192:195], v[56:59], a[120:123], v[192:195]\n"
  "ds_write_b64 v231, v[64:65] offset:2048\n"
  "v_mfma_f32_16x16x32_bf16 v[160:163], v[44:47], a[124:127], v[160:163]\n"
  "v_exp_f32 v2, v2\n"
  "v_mfma_f32_16x16x32_bf16 v[160:163], v[40:43], a[120:123], v[160:163]\n"
  "v_exp_f32 v3, v3\n"
  "v_mfma_f32_16x16x32_bf16 v[128:131], v[28:31], a[124:127], v[128:131]\n"
  "v_add_f32 v252, v252, v0\n"
  "v_add_f32 v252, v252, v1\n"
  "v_mfma_f32_16x16x32_bf16 v[128:131], v[24:27], a[120:123], v[128:131]\n"
  "v_add_f32 v252, v252, v2\n"
  "v_add_f32 v252, v252, v3\n"
  "v_mfma_f32_16x16x32_bf16 v[96:99], v[12:15], a[124:127], v[96:99]\n"
  "v_fma_f32 v16, v16, s73, v224\n"
  "v_fma_f32 v17, v17, s73, v224\n"
  "v_mfma_f32_16x16x32_bf16 v[96:99], v[8:11], a[120:123], v[96:99]\n"
  "s_waitcnt lgkmcnt(5)\n"
  "v_exp_f32 v16, v16\n"
  "v_mfma_f32_16x16x32_bf16 v[4:7], a[136:139], a[0:3], v[248:251]\n"
  "v_exp_f32 v17, v17\n"
  "v_mfma_f32_16x16x32_bf16 v[4:7], a[140:143], a[4:7], v[4:7]\n"
  "s_waitcnt lgkmcnt(3)\n"
  "v_fma_f32 v18, v18, s73, v224\n"
  "v_fma_f32 v19, v19, s73, v224\n"
  "v_mfma_f32_16x16x32_bf16 v[4:7], a[144:147], a[8:11], v[4:7]\n"
  "v_exp_f32 v18, v18\n"
  "v_mfma_f32_16x16x32_bf16 v[4:7], a[148:151], a[12:15], v[4:7]\n"
  "s_waitcnt lgkmcnt(1)\n"
  "v_exp_f32 v19, v19\n"
  "v_mfma_f32_16x16x32_bf16 v[4:7], a[132:135], a[16:19], v[4:7]\n"
  "v_add_f32 v253, v253, v16\n"
  "v_add_f32 v253, v253, v17\n"
  "v_mfma_f32_16x16x32_bf16 v[4:7], a[128:131], a[20:23], v[4:7]\n"
  "v_add_f32 v253, v253, v18\n"
  "v_add_f32 v253, v253, v19\n"
  "v_mfma_f32_16x16x32_bf16 v[20:23], a[128:131], a[44:47], v[248:251]\n"
  "v_fma_f32 v32, v32, s73, v225\n"
  "v_fma_f32 v33, v33, s73, v225\n"
  "v_mfma_f32_16x16x32_bf16 v[20:23], a[132:135], a[40:43], v[20:23]\n"
  "v_exp_f32 v32, v32\n"
  "v_mfma_f32_16x16x32_bf16 v[20:23], a[148:151], a[36:39], v[20:23]\n"
  "ds_read_b64_tr_b16 a[124:125], v219 offset:16\n"
  "v_fma_f32 v34, v34, s73, v225\n"
  "v_mfma_f32_16x16x32_bf16 v[20:23], a[144:147], a[32:35], v[20:23]\n"
  "ds_read_b64_tr_b16 a[126:127], v220 offset:16\n"
  "v_fma_f32 v35, v35, s73, v225\n"
  "v_mfma_f32_16x16x32_bf16 v[20:23], a[140:143], a[28:31], v[20:23]\n"
  "ds_read_b64_tr_b16 a[120:121], v219 offset:1040\n"
  "v_add_f32 v254, v254, v32\n"
  "v_mfma_f32_16x16x32_bf16 v[20:23], a[136:139], a[24:27], v[20:23]\n"
  "ds_read_b64_tr_b16 a[122:123], v220 offset:1040\n"
  "v_fma_f32 v48, v48, s73, v226\n"
  "v_mfma_f32_16x16x32_bf16 v[36:39], a[136:139], a[48:51], v[248:251]\n"
  "v_exp_f32 v33, v33\n"
  "v_mfma_f32_16x16x32_bf16 v[36:39], a[140:143], a[52:55], v[36:39]\n"
  "v_exp_f32 v34, v34\n"
  "v_mfma_f32_16x16x32_bf16 v[36:39], a[144:147], a[56:59], v[36:39]\n"
  "v_exp_f32 v35, v35\n"
  "v_mfma_f32_16x16x32_bf16 v[36:39], a[148:151], a[60:63], v[36:39]\n"
  "v_exp_f32 v48, v48\n"
  "v_mfma_f32_16x16x32_bf16 v[36:39], a[132:135], a[64:67], v[36:39]\n"
  "v_add_f32 v254, v254, v33\n"
  "v_add_f32 v254, v254, v34\n"
  "v_mfma_f32_16x16x32_bf16 v[36:39], a[128:131], a[68:71], v[36:39]\n"
  "v_add_f32 v254, v254, v35\n"
  "v_fma_f32 v49, v49, s73, v226\n"
  "v_mfma_f32_16x16x32_bf16 v[52:55], a[128:131], a[92:95], v[248:251]\n"
  "ds_read_b64_tr_b16 a[128:129], v217 offset:2048\n"
  "v_fma_f32 v50, v50, s73, v226\n"
  "v_mfma_f32_16x16x32_bf16 v[52:55], a[132:135], a[88:91], v[52:55]\n"
  "ds_read_b64_tr_b16 a[130:131], v218 offset:2048\n"
  "v_fma_f32 v51, v51, s73, v226\n"
  "v_mfma_f32_16x16x32_bf16 v[52:55], a[148:151], a[84:87], v[52:55]\n"
  "ds_read_b64_tr_b16 a[132:133], v217 offset:2064\n"
  "v_add_f32 v255, v255, v48\n"
  "v_mfma_f32_16x16x32_bf16 v[52:55], a[144:147], a[80:83], v[52:55]\n"
  "ds_read_b64_tr_b16 a[134:135], v218 offset:2064\n"
  "v_fma_f32 v36, v36, s73, v225\n"
  "v_mfma_f32_16x16x32_bf16 v[52:55], a[140:143], a[76:79], v[52:55]\n"
  "v_exp_f32 v49, v49\n"
  "v_mfma_f32_16x16x32_bf16 v[52:55], a[136:139], a[72:75], v[52:55]\n"
  "s_waitcnt lgkmcnt(4)\n"
  "v_exp_f32 v50, v50\n"
  "v_mfma_f32_16x16x32_bf16 v[100:103], v[12:15], a[124:127], v[100:103]\n"
  "v_exp_f32 v51, v51\n"
  "v_mfma_f32_16x16x32_bf16 v[100:103], v[8:11], a[120:123], v[100:103]\n"
  "v_cvt_pk_bf16_f32 v12, v0, v1\n"
  "v_cvt_pk_bf16_f32 v13, v2, v3\n"
  "v_mfma_f32_16x16x32_bf16 v[132:135], v[28:31], a[124:127], v[132:135]\n"
  "ds_read_b64_tr_b16 a[140:141], v219 offset:2048\n"
  "v_cvt_pk_bf16_f32 v28, v16, v17\n"
  "v_mfma_f32_16x16x32_bf16 v[132:135], v[24:27], a[120:123], v[132:135]\n"
  "ds_read_b64_tr_b16 a[142:143], v220 offset:2048\n"
  "v_cvt_pk_bf16_f32 v29, v18, v19\n"
  "v_mfma_f32_16x16x32_bf16 v[164:167], v[44:47], a[124:127], v[164:167]\n"
  "ds_read_b64_tr_b16 a[136:137], v219 offset:2064\n"
  "v_cvt_pk_bf16_f32 v44, v32, v33\n"
  "v_mfma_f32_16x16x32_bf16 v[164:167], v[40:43], a[120:123], v[164:167]\n"
  "ds_read_b64_tr_b16 a[138:139], v220 offset:2064\n"
  "v_cvt_pk_bf16_f32 v45, v34, v35\n"
  "v_mfma_f32_16x16x32_bf16 v[196:199], v[60:63], a[124:127], v[196:199]\n"
  "v_exp_f32 v36, v36\n"
  "v_mfma_f32_16x16x32_bf16 v[196:199], v[56:59], a[120:123], v[196:199]\n"
  "s_waitcnt lgkmcnt(4)\n"
  "v_add_f32 v255, v255, v49\n"
  "v_add_f32 v255, v255, v50\n"
  "v_mfma_f32_16x16x32_bf16 a[244:247], a[156:159], a[128:131], a[244:247]\n"
  "v_add_f32 v255, v255, v51\n"
  "v_cvt_pk_bf16_f32 v60, v48, v49\n"
  "v_mfma_f32_16x16x32_bf16 a[248:251], a[156:159], a[132:135], a[248:251]\n"
  "v_cvt_pk_bf16_f32 v61, v50, v51\n"
  "v_fma_f32 v52, v52, s73, v226\n"
  "v_mfma_f32_16x16x32_bf16 v[200:203], v[56:59], a[128:131], v[200:203]\n"
  "v_exp_f32 v52, v52\n"
  "v_mfma_f32_16x16x32_bf16 v[204:207], v[56:59], a[132:135], v[204:207]\n"
  "v_fma_f32 v53, v53, s73, v226\n"
  "v_fma_f32 v54, v54, s73, v226\n"
  "v_mfma_f32_16x16x32_bf16 v[168:171], v[40:43], a[128:131], v[168:171]\n"
  "v_exp_f32 v53, v53\n"
  "v_mfma_f32_16x16x32_bf16 v[172:175], v[40:43], a[132:135], v[172:175]\n"
  "s_waitcnt vmcnt(11) lgkmcnt(0)\n"
  "s_barrier\n"
  "ds_read_b128 a[124:127], v216 offset:12352\n"
  "v_mfma_f32_16x16x32_bf16 v[136:139], v[24:27], a[128:131], v[136:139]\n"
  "ds_read_b64 a[160:161], v231 offset:2048\n"
  "v_fma_f32 v55, v55, s73, v226\n"
  "v_mfma_f32_16x16x32_bf16 v[140:143], v[24:27], a[132:135], v[140:143]\n"
  "ds_read_b64 a[162:163], v232 offset:2048\n"
  "v_cvt_pk_bf16_f32 v62, v52, v53\n"
  "v_mfma_f32_16x16x32_bf16 v[104:107], v[8:11], a[128:131], v[104:107]\n"
  "ds_read_b128 a[120:123], v216 offset:12416\n"
  "v_fma_f32 v37, v37, s73, v225\n"
  "v_mfma_f32_16x16x32_bf16 v[108:111], v[8:11], a[132:135], v[108:111]\n"
  "ds_read_b128 a[128:131], v216 offset:13376\n"
  "v_fma_f32 v38, v38, s73, v225\n"
  "v_mfma_f32_16x16x32_bf16 v[112:115], v[8:11], a[140:143], v[112:115]\n"
  "ds_read_b128 a[132:135], v216 offset:13440\n"
  "v_fma_f32 v39, v39, s73, v225\n"
  "v_mfma_f32_16x16x32_bf16 v[116:119], v[8:11], a[136:139], v[116:119]\n"
  "s_mov_b32 m0, s66\n"
  "ds_read_b128 a[148:151], v216 offset:14400\n"
  "v_fma_f32 v20, v20, s73, v224\n"
  "v_mfma_f32_16x16x32_bf16 v[144:147], v[24:27], a[140:143], v[144:147]\n"
  "ds_read_b128 a[144:147], v216 offset:14464\n"
  "v_fma_f32 v21, v21, s73, v224\n"
  "v_mfma_f32_16x16x32_bf16 v[148:151], v[24:27], a[136:139], v[148:151]\n"
  "v_exp_f32 v54, v54\n"
  "v_mfma_f32_16x16x32_bf16 v[176:179], v[40:43], a[140:143], v[176:179]\n"
  "v_exp_f32 v55, v55\n"
  "v_mfma_f32_16x16x32_bf16 v[180:183], v[40:43], a[136:139], v[180:183]\n"
  "buffer_load_dwordx4 v221, s[40:43], 0 offen offset:0 lds\n"
  "v_mfma_f32_16x16x32_bf16 v[208:211], v[56:59], a[140:143], v[208:211]\n"
  "s_addk_i32 m0, 896\n"
  "v_exp_f32 v37, v37\n"
  "v_mfma_f32_16x16x32_bf16 v[212:215], v[56:59], a[136:139], v[212:215]\n"
  "s_waitcnt lgkmcnt(4)\n"
  "v_exp_f32 v38, v38\n"
  "v_mfma_f32_16x16x32_bf16 v[64:67], a[124:127], a[96:99], v[248:251]\n"
  "v_exp_f32 v39, v39\n"
  "v_mfma_f32_16x16x32_bf16 v[64:67], a[120:123], a[100:103], v[64:67]\n"
  "s_waitcnt lgkmcnt(2)\n"
  "buffer_load_dwordx4 v221, s[40:43], 0 offen offset:128 lds\n"
  "v_cvt_pk_bf16_f32 v63, v54, v55\n"
  "v_mfma_f32_16x16x32_bf16 v[64:67], a[128:131], a[104:107], v[64:67]\n"
  "s_addk_i32 m0, 896\n"
  "v_exp_f32 v20, v20\n"
  "v_mfma_f32_16x16x32_bf16 v[64:67], a[132:135], a[108:111], v[64:67]\n"
  "s_waitcnt lgkmcnt(0)\n"
  "v_exp_f32 v21, v21\n"
  "v_mfma_f32_16x16x32_bf16 v[64:67], a[148:151], a[112:115], v[64:67]\n"
  "v_cvt_pk_bf16_f32 v46, v36, v37\n"
  "v_cvt_pk_bf16_f32 v47, v38, v39\n"
  "v_mfma_f32_16x16x32_bf16 v[64:67], a[144:147], a[116:119], v[64:67]\n"
  "buffer_load_dwordx4 v221, s[40:43], 0 offen offset:256 lds\n"
  "v_fma_f32 v22, v22, s73, v224\n"
  "v_mfma_f32_16x16x32_bf16 v[0:3], a[144:147], a[20:23], v[248:251]\n"
  "s_add_u32 s40, s40, s31\n"
  "v_exp_f32 v22, v22\n"
  "v_mfma_f32_16x16x32_bf16 v[0:3], a[148:151], a[16:19], v[0:3]\n"
  "s_addc_u32 s41, s41, 0\n"
  "v_mbcnt_lo_u32_b32 v236, -1, 0\n"
  "v_mbcnt_hi_u32_b32 v236, -1, v236\n"
  "v_mfma_f32_16x16x32_bf16 v[0:3], a[132:135], a[12:15], v[0:3]\n"
  "s_sub_u32 s79, s96, s39\n"
  "v_mul_u32_u24 v236, 0x18c, v236\n"
  "v_fma_f32 v23, v23, s73, v224\n"
  "v_mfma_f32_16x16x32_bf16 v[0:3], a[128:131], a[8:11], v[0:3]\n"
  "s_mov_b32 m0, s58\n"
  "v_exp_f32 v23, v23\n"
  "v_mfma_f32_16x16x32_bf16 v[0:3], a[120:123], a[4:7], v[0:3]\n"
  "v_cvt_pk_bf16_f32 v30, v20, v21\n"
  "v_cvt_pk_bf16_f32 v31, v22, v23\n"
  "v_mfma_f32_16x16x32_bf16 v[0:3], a[124:127], a[0:3], v[0:3]\n"
  "v_fma_f32 v4, v4, s73, v223\n"
  "v_fma_f32 v5, v5, s73, v223\n"
  "v_mfma_f32_16x16x32_bf16 v[16:19], a[124:127], a[24:27], v[248:251]\n"
  "v_exp_f32 v4, v4\n"
  "v_mfma_f32_16x16x32_bf16 v[16:19], a[120:123], a[28:31], v[16:19]\n"
  "buffer_load_dwordx4 v221, s[32:35], 0 offen offset:0 lds\n"
  "v_fma_f32 v6, v6, s73, v223\n"
  "v_mfma_f32_16x16x32_bf16 v[16:19], a[128:131], a[32:35], v[16:19]\n"
  "s_addk_i32 m0, 1024\n"
  "ds_read_b64_tr_b16 a[140:141], v217 offset:12352\n"
  "v_fma_f32 v7, v7, s73, v223\n"
  "v_mfma_f32_16x16x32_bf16 v[16:19], a[132:135], a[36:39], v[16:19]\n"
  "s_mov_b64 s[32:33], s[44:45]\n"
  "ds_read_b64_tr_b16 a[142:143], v218 offset:12352\n"
  "v_add_f32 v252, v252, v4\n"
  "v_mfma_f32_16x16x32_bf16 v[16:19], a[148:151], a[40:43], v[16:19]\n"
  "ds_read_b64_tr_b16 a[136:137], v217 offset:13376\n"
  "v_add_f32 v253, v253, v20\n"
  "v_mfma_f32_16x16x32_bf16 v[16:19], a[144:147], a[44:47], v[16:19]\n"
  "buffer_load_dwordx4 v221, s[44:47], 0 offen offset:0 lds\n"
  "v_add_f32 v253, v253, v21\n"
  "v_mfma_f32_16x16x32_bf16 v[32:35], a[144:147], a[68:71], v[248:251]\n"
  "s_addk_i32 m0, 768\n"
  "ds_read_b64_tr_b16 a[138:139], v218 offset:13376\n"
  "v_add_f32 v253, v253, v22\n"
  "v_mfma_f32_16x16x32_bf16 v[32:35], a[148:151], a[64:67], v[32:35]\n"
  "v_exp_f32 v5, v5\n"
  "v_mfma_f32_16x16x32_bf16 v[32:35], a[132:135], a[60:63], v[32:35]\n"
  "v_exp_f32 v6, v6\n"
  "v_mfma_f32_16x16x32_bf16 v[32:35], a[128:131], a[56:59], v[32:35]\n"
  "buffer_load_dwordx4 v221, s[44:47], 0 offen offset:256 lds\n"
  "v_cvt_pk_bf16_f32 v14, v4, v5\n"
  "v_mfma_f32_16x16x32_bf16 v[32:35], a[120:123], a[52:55], v[32:35]\n"
  "s_add_u32 s44, s44, s31\n"
  "v_exp_f32 v7, v7\n"
  "v_mfma_f32_16x16x32_bf16 v[32:35], a[124:127], a[48:51], v[32:35]\n"
  "s_addc_u32 s45, s45, 0\n"
  "v_cvt_pk_bf16_f32 v15, v6, v7\n"
  "v_add_f32 v252, v252, v5\n"
  "v_mfma_f32_16x16x32_bf16 v[48:51], a[124:127], a[72:75], v[248:251]\n"
  "s_mov_b32 m0, s29\n"
  "v_add_f32 v252, v252, v6\n"
  "v_add_f32 v252, v252, v7\n"
  "v_mfma_f32_16x16x32_bf16 v[48:51], a[120:123], a[76:79], v[48:51]\n"
  "v_add_f32 v253, v253, v23\n"
  "v_add_f32 v254, v254, v36\n"
  "v_mfma_f32_16x16x32_bf16 v[48:51], a[128:131], a[80:83], v[48:51]\n"
  "ds_read_b64_tr_b16 a[124:125], v217 offset:12368\n"
  "v_add_f32 v254, v254, v37\n"
  "v_mfma_f32_16x16x32_bf16 v[48:51], a[132:135], a[84:87], v[48:51]\n"
  "buffer_load_dword v236, s[88:91], s79 offen lds\n"
  "v_add_f32 v254, v254, v38\n"
  "v_mfma_f32_16x16x32_bf16 v[48:51], a[148:151], a[88:91], v[48:51]\n"
  "ds_read_b64_tr_b16 a[126:127], v218 offset:12368\n"
  "v_add_f32 v254, v254, v39\n"
  "v_mfma_f32_16x16x32_bf16 v[48:51], a[144:147], a[92:95], v[48:51]\n"
  "s_waitcnt lgkmcnt(2)\n"
  "ds_read_b64_tr_b16 a[120:121], v217 offset:13392\n"
  "v_add_f32 v255, v255, v52\n"
  "v_mfma_f32_16x16x32_bf16 a[228:231], a[156:159], a[140:143], a[228:231]\n"
  "ds_read_b64_tr_b16 a[122:123], v218 offset:13392\n"
  "v_add_f32 v255, v255, v53\n"
  "v_mfma_f32_16x16x32_bf16 a[228:231], a[160:163], a[136:139], a[228:231]\n"
  "v_add_f32 v255, v255, v54\n"
  "v_add_f32 v255, v255, v55\n"
  "v_mfma_f32_16x16x32_bf16 a[212:215], v[56:59], a[140:143], a[212:215]\n"
  "ds_read_b64_tr_b16 v[244:245], v230 offset:7840\n"
  "ds_read_b64_tr_b16 v[246:247], v230 offset:7872\n"
  "v_mfma_f32_16x16x32_bf16 a[212:215], v[60:63], a[136:139], a[212:215]\n"
  "ds_read_b64_tr_b16 a[252:253], v230 offset:17920\n"
  "ds_read_b64_tr_b16 a[254:255], v230 offset:17952\n"
  "v_mfma_f32_16x16x32_bf16 a[196:199], v[40:43], a[140:143], a[196:199]\n"
  "v_fma_f32 v64, v64, s73, v227\n"
  "v_fma_f32 v65, v65, s73, v227\n"
  "v_mfma_f32_16x16x32_bf16 a[196:199], v[44:47], a[136:139], a[196:199]\n"
  "v_exp_f32 v64, v64\n"
  "v_mfma_f32_16x16x32_bf16 a[180:183], v[24:27], a[140:143], a[180:183]\n"
  "ds_read_b64_tr_b16 a[128:129], v219 offset:12352\n"
  "v_fma_f32 v66, v66, s73, v227\n"
  "v_mfma_f32_16x16x32_bf16 a[180:183], v[28:31], a[136:139], a[180:183]\n"
  "ds_read_b64_tr_b16 a[130:131], v220 offset:12352\n"
  "v_fma_f32 v67, v67, s73, v227\n"
  "v_mfma_f32_16x16x32_bf16 a[164:167], v[8:11], a[140:143], a[164:167]\n"
  "ds_read_b64_tr_b16 a[132:133], v219 offset:13376\n"
  "v_fma_f32 v0, v0, s73, v223\n"
  "v_mfma_f32_16x16x32_bf16 a[164:167], v[12:15], a[136:139], a[164:167]\n"
  "ds_read_b64_tr_b16 a[134:135], v220 offset:13376\n"
  "v_fma_f32 v1, v1, s73, v223\n"
  "v_mfma_f32_16x16x32_bf16 v[84:87], a[156:159], a[152:155], v[84:87]\n"
  "v_exp_f32 v65, v65\n"
  "v_mfma_f32_16x16x32_bf16 v[84:87], a[160:163], a[152:155], v[84:87]\n"
  "s_waitcnt lgkmcnt(8)\n"
  "v_exp_f32 v66, v66\n"
  "v_mfma_f32_16x16x32_bf16 a[168:171], v[8:11], a[124:127], a[168:171]\n"
  "v_exp_f32 v67, v67\n"
  "v_mfma_f32_16x16x32_bf16 a[168:171], v[12:15], a[120:123], a[168:171]\n"
  "v_exp_f32 v0, v0\n"
  "v_mfma_f32_16x16x32_bf16 a[184:187], v[24:27], a[124:127], a[184:187]\n"
  "ds_read_b128 a[140:143], v235 offset:12352\n"
  "v_cvt_pk_bf16_f32 v64, v64, v65\n"
  "v_mfma_f32_16x16x32_bf16 a[184:187], v[28:31], a[120:123], a[184:187]\n"
  "ds_read_b128 a[136:139], v235 offset:12416\n"
  "v_cvt_pk_bf16_f32 v65, v66, v67\n"
  "v_mfma_f32_16x16x32_bf16 a[200:203], v[40:43], a[124:127], a[200:203]\n"
  "ds_read_b128 a[148:151], v235 offset:13376\n"
  "v_fma_f32 v2, v2, s73, v223\n"
  "v_mfma_f32_16x16x32_bf16 a[200:203], v[44:47], a[120:123], a[200:203]\n"
  "ds_read_b128 a[144:147], v235 offset:13440\n"
  "v_fma_f32 v3, v3, s73, v223\n"
  "v_mfma_f32_16x16x32_bf16 a[216:219], v[56:59], a[124:127], a[216:219]\n"
  "v_exp_f32 v1, v1\n"
  "v_mfma_f32_16x16x32_bf16 a[216:219], v[60:63], a[120:123], a[216:219]\n"
  "v_exp_f32 v2, v2\n"
  "v_mfma_f32_16x16x32_bf16 a[232:235], a[156:159], a[124:127], a[232:235]\n"
  "ds_read_b128 a[124:127], v235 offset:14400\n"
  "v_add_f32 v252, v252, v0\n"
  "v_mfma_f32_16x16x32_bf16 a[232:235], a[160:163], a[120:123], a[232:235]\n"
  "s_waitcnt lgkmcnt(5)\n"
  "ds_read_b128 a[120:123], v235 offset:14464\n"
  "v_add_f32 v252, v252, v1\n"
  "v_mfma_f32_16x16x32_bf16 a[220:223], v[56:59], a[128:131], a[220:223]\n"
  "v_exp_f32 v3, v3\n"
  "v_mfma_f32_16x16x32_bf16 a[220:223], v[60:63], a[132:135], a[220:223]\n"
  "ds_write_b64 v231, v[64:65] offset:0\n"
  "v_mfma_f32_16x16x32_bf16 a[204:207], v[40:43], a[128:131], a[204:207]\n"
  "v_add_f32 v252, v252, v2\n"
  "v_add_f32 v252, v252, v3\n"
  "v_mfma_f32_16x16x32_bf16 a[204:207], v[44:47], a[132:135], a[204:207]\n"
  "buffer_store_dwordx4 v[244:247], v228, s[52:55], s83 offen offset:3072 nt\n"
  "v_fma_f32 v16, v16, s73, v224\n"
  "v_exp_f32 v16, v16\n"
  "v_mfma_f32_16x16x32_bf16 a[188:191], v[24:27], a[128:131], a[188:191]\n"
  "ds_read_b64_tr_b16 v[244:245], v230 offset:19040\n"
  "ds_read_b64_tr_b16 v[246:247], v230 offset:19072\n"
  "v_mfma_f32_16x16x32_bf16 a[188:191], v[28:31], a[132:135], a[188:191]\n"
  "v_fma_f32 v17, v17, s73, v224\n"
  "v_fma_f32 v18, v18, s73, v224\n"
  "v_mfma_f32_16x16x32_bf16 a[172:175], v[8:11], a[128:131], a[172:175]\n"
  "v_exp_f32 v17, v17\n"
  "v_mfma_f32_16x16x32_bf16 a[172:175], v[12:15], a[132:135], a[172:175]\n"
  "s_waitcnt lgkmcnt(7)\n"
  "v_exp_f32 v18, v18\n"
  "v_mfma_f32_16x16x32_bf16 v[4:7], a[140:143], a[0:3], v[248:251]\n"
  "v_fma_f32 v19, v19, s73, v224\n"
  "v_add_f32 v253, v253, v16\n"
  "v_mfma_f32_16x16x32_bf16 v[4:7], a[136:139], a[4:7], v[4:7]\n"
  "s_waitcnt lgkmcnt(5)\n"
  "v_exp_f32 v19, v19\n"
  "v_mfma_f32_16x16x32_bf16 v[4:7], a[148:151], a[8:11], v[4:7]\n"
  "v_add_f32 v253, v253, v17\n"
  "v_add_f32 v253, v253, v18\n"
  "v_mfma_f32_16x16x32_bf16 v[4:7], a[144:147], a[12:15], v[4:7]\n"
  "s_waitcnt lgkmcnt(3)\n"
  "v_add_f32 v253, v253, v19\n"
  "v_fma_f32 v32, v32, s73, v225\n"
  "v_mfma_f32_16x16x32_bf16 v[4:7], a[124:127], a[16:19], v[4:7]\n"
  "v_exp_f32 v32, v32\n"
  "v_mfma_f32_16x16x32_bf16 v[4:7], a[120:123], a[20:23], v[4:7]\n"
  "buffer_store_dwordx4 a[252:255], v228, s[52:55], s56 offen offset:0 nt\n"
  "v_fma_f32 v33, v33, s73, v225\n"
  "v_exp_f32 v33, v33\n"
  "v_mfma_f32_16x16x32_bf16 v[20:23], a[120:123], a[44:47], v[248:251]\n"
  "v_fma_f32 v34, v34, s73, v225\n"
  "v_fma_f32 v35, v35, s73, v225\n"
  "v_mfma_f32_16x16x32_bf16 v[20:23], a[124:127], a[40:43], v[20:23]\n"
  "v_exp_f32 v34, v34\n"
  "v_mfma_f32_16x16x32_bf16 v[20:23], a[144:147], a[36:39], v[20:23]\n"
  "ds_read_b64_tr_b16 a[128:129], v219 offset:12368\n"
  "v_add_f32 v254, v254, v32\n"
  "v_mfma_f32_16x16x32_bf16 v[20:23], a[148:151], a[32:35], v[20:23]\n"
  "ds_read_b64_tr_b16 a[130:131], v220 offset:12368\n"
  "v_add_f32 v254, v254, v33\n"
  "v_mfma_f32_16x16x32_bf16 v[20:23], a[136:139], a[28:31], v[20:23]\n"
  "ds_read_b64_tr_b16 a[132:133], v219 offset:13392\n"
  "v_add_f32 v254, v254, v34\n"
  "v_mfma_f32_16x16x32_bf16 v[20:23], a[140:143], a[24:27], v[20:23]\n"
  "s_waitcnt lgkmcnt(3)\n"
  "buffer_store_dwordx4 v[244:247], v228, s[52:55], s56 offen offset:128 nt\n"
  "ds_read_b64_tr_b16 a[134:135], v220 offset:13392\n"
  "v_exp_f32 v35, v35\n"
  "v_mfma_f32_16x16x32_bf16 v[36:39], a[140:143], a[48:51], v[248:251]\n"
  "v_add_f32 v254, v254, v35\n"
  "v_fma_f32 v48, v48, s73, v226\n"
  "v_mfma_f32_16x16x32_bf16 v[36:39], a[136:139], a[52:55], v[36:39]\n"
  "v_exp_f32 v48, v48\n"
  "v_mfma_f32_16x16x32_bf16 v[36:39], a[148:151], a[56:59], v[36:39]\n"
  "v_fma_f32 v49, v49, s73, v226\n"
  "v_fma_f32 v50, v50, s73, v226\n"
  "v_mfma_f32_16x16x32_bf16 v[36:39], a[144:147], a[60:63], v[36:39]\n"
  "v_exp_f32 v49, v49\n"
  "v_mfma_f32_16x16x32_bf16 v[36:39], a[124:127], a[64:67], v[36:39]\n"
  "v_exp_f32 v50, v50\n"
  "v_mfma_f32_16x16x32_bf16 v[36:39], a[120:123], a[68:71], v[36:39]\n"
  "v_fma_f32 v51, v51, s73, v226\n"
  "v_add_f32 v255, v255, v48\n"
  "v_mfma_f32_16x16x32_bf16 v[52:55], a[120:123], a[92:95], v[248:251]\n"
  "ds_read_b64_tr_b16 a[120:121], v217 offset:14400\n"
  "v_add_f32 v255, v255, v49\n"
  "v_mfma_f32_16x16x32_bf16 v[52:55], a[124:127], a[88:91], v[52:55]\n"
  "ds_read_b64_tr_b16 a[122:123], v218 offset:14400\n"
  "v_add_f32 v255, v255, v50\n"
  "v_mfma_f32_16x16x32_bf16 v[52:55], a[144:147], a[84:87], v[52:55]\n"
  "ds_read_b64_tr_b16 a[124:125], v217 offset:14416\n"
  "v_fma_f32 v36, v36, s73, v225\n"
  "v_mfma_f32_16x16x32_bf16 v[52:55], a[148:151], a[80:83], v[52:55]\n"
  "ds_read_b64_tr_b16 a[126:127], v218 offset:14416\n"
  "v_fma_f32 v37, v37, s73, v225\n"
  "v_mfma_f32_16x16x32_bf16 v[52:55], a[136:139], a[76:79], v[52:55]\n"
  "v_exp_f32 v51, v51\n"
  "v_mfma_f32_16x16x32_bf16 v[52:55], a[140:143], a[72:75], v[52:55]\n"
  "s_waitcnt lgkmcnt(4)\n"
  "v_exp_f32 v36, v36\n"
  "v_mfma_f32_16x16x32_bf16 a[176:179], v[8:11], a[128:131], a[176:179]\n"
  "v_cvt_pk_bf16_f32 v8, v0, v1\n"
  "v_cvt_pk_bf16_f32 v9, v2, v3\n"
  "v_mfma_f32_16x16x32_bf16 a[176:179], v[12:15], a[132:135], a[176:179]\n"
  "v_exp_f32 v37, v37\n"
  "v_mfma_f32_16x16x32_bf16 a[192:195], v[24:27], a[128:131], a[192:195]\n"
  "ds_read_b64_tr_b16 a[136:137], v219 offset:14400\n"
  "v_cvt_pk_bf16_f32 v24, v16, v17\n"
  "v_mfma_f32_16x16x32_bf16 a[192:195], v[28:31], a[132:135], a[192:195]\n"
  "ds_read_b64_tr_b16 a[138:139], v220 offset:14400\n"
  "v_cvt_pk_bf16_f32 v25, v18, v19\n"
  "v_mfma_f32_16x16x32_bf16 a[208:211], v[40:43], a[128:131], a[208:211]\n"
  "ds_read_b64_tr_b16 a[140:141], v219 offset:14416\n"
  "v_cvt_pk_bf16_f32 v40, v32, v33\n"
  "v_mfma_f32_16x16x32_bf16 a[208:211], v[44:47], a[132:135], a[208:211]\n"
  "ds_read_b64_tr_b16 a[142:143], v220 offset:14416\n"
  "v_cvt_pk_bf16_f32 v41, v34, v35\n"
  "v_mfma_f32_16x16x32_bf16 a[224:227], v[56:59], a[128:131], a[224:227]\n"
  "v_add_f32 v255, v255, v51\n"
  "v_cvt_pk_bf16_f32 v56, v48, v49\n"
  "v_mfma_f32_16x16x32_bf16 a[224:227], v[60:63], a[132:135], a[224:227]\n"
  "s_waitcnt lgkmcnt(4)\n"
  "v_cvt_pk_bf16_f32 v57, v50, v51\n"
  "v_fma_f32 v52, v52, s73, v226\n"
  "v_mfma_f32_16x16x32_bf16 a[244:247], a[160:163], a[120:123], a[244:247]\n"
  "v_exp_f32 v52, v52\n"
  "v_mfma_f32_16x16x32_bf16 a[248:251], a[160:163], a[124:127], a[248:251]\n"
  "v_fma_f32 v53, v53, s73, v226\n"
  "v_fma_f32 v54, v54, s73, v226\n"
  "v_mfma_f32_16x16x32_bf16 v[200:203], v[60:63], a[120:123], v[200:203]\n"
  "v_exp_f32 v53, v53\n"
  "v_mfma_f32_16x16x32_bf16 v[204:207], v[60:63], a[124:127], v[204:207]\n"
  "v_exp_f32_e64 v54, v54\n"
  "v_mfma_f32_16x16x32_bf16 v[168:171], v[44:47], a[120:123], v[168:171]\n"
  "v_fma_f32 v55, v55, s73, v226\n"
  "v_cvt_pk_bf16_f32 v58, v52, v53\n"
  "v_mfma_f32_16x16x32_bf16 v[172:175], v[44:47], a[124:127], v[172:175]\n"
  "s_waitcnt vmcnt(12) lgkmcnt(0)\n"
  "s_barrier\n"
  "ds_read_b128 a[128:131], v216 offset:24704\n"
  "v_mfma_f32_16x16x32_bf16 v[136:139], v[28:31], a[120:123], v[136:139]\n"
  "ds_read_b64 a[156:157], v231 offset:0\n"
  "v_fma_f32 v38, v38, s73, v225\n"
  "v_mfma_f32_16x16x32_bf16 v[140:143], v[28:31], a[124:127], v[140:143]\n"
  "ds_read_b64 a[158:159], v232 offset:0\n"
  "v_fma_f32 v39, v39, s73, v225\n"
  "v_mfma_f32_16x16x32_bf16 v[104:107], v[12:15], a[120:123], v[104:107]\n"
  "ds_read_b128 a[132:135], v216 offset:24768\n"
  "v_cvt_pk_bf16_f32 v42, v36, v37\n"
  "v_mfma_f32_16x16x32_bf16 v[108:111], v[12:15], a[124:127], v[108:111]\n"
  "ds_read_b128 a[120:123], v216 offset:25728\n"
  "v_fma_f32 v20, v20, s73, v224\n"
  "v_mfma_f32_16x16x32_bf16 v[112:115], v[12:15], a[136:139], v[112:115]\n"
  "s_add_u32 s71, s92, s81\n"
  "ds_read_b128 a[124:127], v216 offset:25792\n"
  "v_fma_f32 v21, v21, s73, v224\n"
  "v_mfma_f32_16x16x32_bf16 v[116:119], v[12:15], a[140:143], v[116:119]\n"
  "s_mov_b32 m0, s67\n"
  "ds_read_b128 a[144:147], v216 offset:26752\n"
  "v_fma_f32 v22, v22, s73, v224\n"
  "v_mfma_f32_16x16x32_bf16 v[144:147], v[28:31], a[136:139], v[144:147]\n"
  "s_add_u32 s48, s84, s71\n"
  "ds_read_b128 a[148:151], v216 offset:26816\n"
  "v_fma_f32 v23, v23, s73, v224\n"
  "v_mfma_f32_16x16x32_bf16 v[148:151], v[28:31], a[140:143], v[148:151]\n"
  "s_addc_u32 s49, s85, 0\n"
  "v_exp_f32 v55, v55\n"
  "v_mfma_f32_16x16x32_bf16 v[176:179], v[44:47], a[136:139], v[176:179]\n"
  "s_sub_i32 s71, s38, s81\n"
  "v_exp_f32 v38, v38\n"
  "v_mfma_f32_16x16x32_bf16 v[180:183], v[44:47], a[140:143], v[180:183]\n"
  "buffer_load_dwordx4 v221, s[40:43], 0 offen offset:0 lds\n"
  "v_mfma_f32_16x16x32_bf16 v[208:211], v[60:63], a[136:139], v[208:211]\n"
  "s_addk_i32 m0, 896\n"
  "v_exp_f32 v39, v39\n"
  "v_mfma_f32_16x16x32_bf16 v[212:215], v[60:63], a[140:143], v[212:215]\n"
  "s_waitcnt lgkmcnt(4)\n"
  "v_exp_f32 v20, v20\n"
  "v_mfma_f32_16x16x32_bf16 v[64:67], a[128:131], a[96:99], v[248:251]\n"
  "s_max_i32 s50, s71, 0\n"
  "v_exp_f32 v21, v21\n"
  "v_mfma_f32_16x16x32_bf16 v[64:67], a[132:135], a[100:103], v[64:67]\n"
  "s_waitcnt lgkmcnt(2)\n"
  "buffer_load_dwordx4 v221, s[40:43], 0 offen offset:128 lds\n"
  "v_cvt_pk_bf16_f32 v59, v54, v55\n"
  "v_mfma_f32_16x16x32_bf16 v[64:67], a[120:123], a[104:107], v[64:67]\n"
  "s_addk_i32 m0, 896\n"
  "v_exp_f32 v22, v22\n"
  "v_mfma_f32_16x16x32_bf16 v[64:67], a[124:127], a[108:111], v[64:67]\n"
  "s_waitcnt lgkmcnt(0)\n"
  "v_exp_f32 v23, v23\n"
  "v_mfma_f32_16x16x32_bf16 v[64:67], a[144:147], a[112:115], v[64:67]\n"
  "v_cvt_pk_bf16_f32 v43, v38, v39\n"
  "v_cvt_pk_bf16_f32 v26, v20, v21\n"
  "v_mfma_f32_16x16x32_bf16 v[64:67], a[148:151], a[116:119], v[64:67]\n"
  "buffer_load_dwordx4 v221, s[40:43], 0 offen offset:256 lds\n"
  "v_cvt_pk_bf16_f32 v27, v22, v23\n"
  "v_mfma_f32_16x16x32_bf16 v[0:3], a[148:151], a[20:23], v[248:251]\n"
  "v_fma_f32 v4, v4, s73, v223\n"
  "v_fma_f32 v5, v5, s73, v223\n"
  "v_mfma_f32_16x16x32_bf16 v[0:3], a[144:147], a[16:19], v[0:3]\n"
  "s_add_u32 s71, s92, s56\n"
  "v_exp_f32 v4, v4\n"
  "v_mfma_f32_16x16x32_bf16 v[0:3], a[124:127], a[12:15], v[0:3]\n"
  "s_add_u32 s40, s40, s31\n"
  "v_exp_f32 v5, v5\n"
  "v_mfma_f32_16x16x32_bf16 v[0:3], a[120:123], a[8:11], v[0:3]\n"
  "s_addc_u32 s41, s41, 0\n"
  "v_fma_f32 v6, v6, s73, v223\n"
  "v_fma_f32 v7, v7, s73, v223\n"
  "v_mfma_f32_16x16x32_bf16 v[0:3], a[132:135], a[4:7], v[0:3]\n"
  "s_add_u32 m0, s59, -128\n"
  "v_exp_f32 v6, v6\n"
  "v_mfma_f32_16x16x32_bf16 v[0:3], a[128:131], a[0:3], v[0:3]\n"
  "v_exp_f32 v7, v7\n"
  "v_mfma_f32_16x16x32_bf16 v[16:19], a[128:131], a[24:27], v[248:251]\n"
  "v_cvt_pk_bf16_f32 v10, v4, v5\n"
  "v_cvt_pk_bf16_f32 v11, v6, v7\n"
  "v_mfma_f32_16x16x32_bf16 v[16:19], a[132:135], a[28:31], v[16:19]\n"
  "buffer_load_dwordx4 v221, s[32:35], 0 offen offset:128 lds\n"
  "v_add_f32 v252, v252, v4\n"
  "v_mfma_f32_16x16x32_bf16 v[16:19], a[120:123], a[32:35], v[16:19]\n"
  "s_addk_i32 m0, 1024\n"
  "ds_read_b64_tr_b16 a[136:137], v217 offset:24704\n"
  "v_add_f32 v252, v252, v5\n"
  "v_mfma_f32_16x16x32_bf16 v[16:19], a[124:127], a[36:39], v[16:19]\n"
  "s_mov_b64 s[32:33], s[44:45]\n"
  "ds_read_b64_tr_b16 a[138:139], v218 offset:24704\n"
  "v_add_f32 v252, v252, v6\n"
  "v_mfma_f32_16x16x32_bf16 v[16:19], a[144:147], a[40:43], v[16:19]\n"
  "ds_read_b64_tr_b16 a[140:141], v217 offset:25728\n"
  "v_add_f32 v252, v252, v7\n"
  "v_mfma_f32_16x16x32_bf16 v[16:19], a[148:151], a[44:47], v[16:19]\n"
  "buffer_load_dwordx4 v221, s[44:47], 0 offen offset:128 lds\n"
  "v_add_f32 v253, v253, v20\n"
  "v_mfma_f32_16x16x32_bf16 v[32:35], a[148:151], a[68:71], v[248:251]\n"
  "s_addk_i32 m0, 896\n"
  "ds_read_b64_tr_b16 a[142:143], v218 offset:25728\n"
  "v_add_f32 v253, v253, v21\n"
  "v_mfma_f32_16x16x32_bf16 v[32:35], a[144:147], a[64:67], v[32:35]\n"
  "v_add_f32 v253, v253, v22\n"
  "v_add_f32 v253, v253, v23\n"
  "v_mfma_f32_16x16x32_bf16 v[32:35], a[124:127], a[60:63], v[32:35]\n"
  "v_add_f32 v254, v254, v36\n"
  "v_add_f32 v254, v254, v37\n"
  "v_mfma_f32_16x16x32_bf16 v[32:35], a[120:123], a[56:59], v[32:35]\n"
  "buffer_load_dwordx4 v221, s[44:47], 0 offen offset:256 lds\n"
  "v_add_f32 v254, v254, v38\n"
  "v_mfma_f32_16x16x32_bf16 v[32:35], a[132:135], a[52:55], v[32:35]\n"
  "s_add_u32 s44, s44, s31\n"
  "v_add_f32 v254, v254, v39\n"
  "v_add_f32 v255, v255, v52\n"
  "v_mfma_f32_16x16x32_bf16 v[32:35], a[128:131], a[48:51], v[32:35]\n"
  "s_addc_u32 s45, s45, 0\n"
  "v_add_f32 v255, v255, v53\n"
  "v_add_f32 v255, v255, v54\n"
  "v_mfma_f32_16x16x32_bf16 v[48:51], a[128:131], a[72:75], v[248:251]\n"
  "ds_read_b64_tr_b16 a[128:129], v217 offset:24720\n"
  "v_add_f32 v255, v255, v55\n"
  "v_mfma_f32_16x16x32_bf16 v[48:51], a[132:135], a[76:79], v[48:51]\n"
  "ds_read_b64_tr_b16 a[130:131], v218 offset:24720\n"
  "v_fma_f32 v64, v64, s73, v227\n"
  "v_mfma_f32_16x16x32_bf16 v[48:51], a[120:123], a[80:83], v[48:51]\n"
  "ds_read_b64_tr_b16 a[132:133], v217 offset:25744\n"
  "v_fma_f32 v65, v65, s73, v227\n"
  "v_mfma_f32_16x16x32_bf16 v[48:51], a[124:127], a[84:87], v[48:51]\n"
  "s_add_u32 m0, s72, 6464\n"
  "ds_read_b64_tr_b16 a[134:135], v218 offset:25744\n"
  "v_mfma_f32_16x16x32_bf16 v[48:51], a[144:147], a[88:91], v[48:51]\n"
  "v_exp_f32 v64, v64\n"
  "v_mfma_f32_16x16x32_bf16 v[48:51], a[148:151], a[92:95], v[48:51]\n"
  "s_waitcnt lgkmcnt(4)\n"
  "v_exp_f32 v65, v65\n"
  "v_mfma_f32_16x16x32_bf16 a[236:239], a[160:163], a[136:139], a[236:239]\n"
  "v_fma_f32 v66, v66, s73, v227\n"
  "v_fma_f32 v67, v67, s73, v227\n"
  "v_mfma_f32_16x16x32_bf16 a[236:239], a[156:159], a[140:143], a[236:239]\n"
  "buffer_load_dwordx4 v221, s[48:51], 0 offen offset:256 nt lds\n"
  "v_cvt_pk_bf16_f32 v64, v64, v65\n"
  "v_mfma_f32_16x16x32_bf16 v[184:187], v[60:63], a[136:139], v[184:187]\n"
  "v_exp_f32 v66, v66\n"
  "v_mfma_f32_16x16x32_bf16 v[184:187], v[56:59], a[140:143], v[184:187]\n"
  "v_exp_f32 v67, v67\n"
  "v_mfma_f32_16x16x32_bf16 v[152:155], v[44:47], a[136:139], v[152:155]\n"
  "ds_read_b64_tr_b16 a[120:121], v219 offset:24704\n"
  "v_cvt_pk_bf16_f32 v65, v66, v67\n"
  "v_mfma_f32_16x16x32_bf16 v[152:155], v[40:43], a[140:143], v[152:155]\n"
  "s_add_u32 m0, s72, 4480\n"
  "ds_read_b64_tr_b16 a[122:123], v220 offset:24704\n"
  "v_mfma_f32_16x16x32_bf16 v[120:123], v[28:31], a[136:139], v[120:123]\n"
  "ds_read_b64_tr_b16 a[124:125], v219 offset:25728\n"
  "v_fma_f32 v0, v0, s73, v223\n"
  "v_mfma_f32_16x16x32_bf16 v[120:123], v[24:27], a[140:143], v[120:123]\n"
  "ds_read_b64_tr_b16 a[126:127], v220 offset:25728\n"
  "v_fma_f32 v1, v1, s73, v223\n"
  "v_mfma_f32_16x16x32_bf16 v[88:91], v[12:15], a[136:139], v[88:91]\n"
  "v_exp_f32 v0, v0\n"
  "v_mfma_f32_16x16x32_bf16 v[88:91], v[8:11], a[140:143], v[88:91]\n"
  "s_waitcnt lgkmcnt(4)\n"
  "buffer_load_dwordx4 v221, s[48:51], 0 offen offset:3328 nt lds\n"
  "v_fma_f32 v2, v2, s73, v223\n"
  "v_mfma_f32_16x16x32_bf16 v[92:95], v[12:15], a[128:131], v[92:95]\n"
  "s_add_u32 s48, s84, s71\n"
  "v_exp_f32 v1, v1\n"
  "v_mfma_f32_16x16x32_bf16 v[92:95], v[8:11], a[132:135], v[92:95]\n"
  "s_addc_u32 s49, s85, 0\n"
  "v_exp_f32 v2, v2\n"
  "v_mfma_f32_16x16x32_bf16 v[124:127], v[28:31], a[128:131], v[124:127]\n"
  "s_sub_i32 s71, s38, s56\n"
  "ds_read_b128 a[136:139], v235 offset:24704\n"
  "v_fma_f32 v3, v3, s73, v223\n"
  "v_mfma_f32_16x16x32_bf16 v[124:127], v[24:27], a[132:135], v[124:127]\n"
  "s_add_u32 m0, s72, 17920\n"
  "ds_read_b128 a[140:143], v235 offset:24768\n"
  "v_add_f32 v252, v252, v0\n"
  "v_mfma_f32_16x16x32_bf16 v[156:159], v[44:47], a[128:131], v[156:159]\n"
  "s_max_i32 s50, s71, 0\n"
  "ds_read_b128 a[144:147], v235 offset:25728\n"
  "v_add_f32 v252, v252, v1\n"
  "v_mfma_f32_16x16x32_bf16 v[156:159], v[40:43], a[132:135], v[156:159]\n"
  "ds_read_b128 a[148:151], v235 offset:25792\n"
  "v_add_f32 v252, v252, v2\n"
  "v_mfma_f32_16x16x32_bf16 v[188:191], v[60:63], a[128:131], v[188:191]\n"
  "v_exp_f32 v3, v3\n"
  "v_mfma_f32_16x16x32_bf16 v[188:191], v[56:59], a[132:135], v[188:191]\n"
  "buffer_load_dwordx4 v221, s[48:51], 0 offen offset:0 nt lds\n"
  "v_add_f32 v252, v252, v3\n"
  "v_mfma_f32_16x16x32_bf16 a[240:243], a[160:163], a[128:131], a[240:243]\n"
  "ds_read_b128 a[128:131], v235 offset:26752\n"
  "v_fma_f32 v16, v16, s73, v224\n"
  "v_mfma_f32_16x16x32_bf16 a[240:243], a[156:159], a[132:135], a[240:243]\n"
  "s_waitcnt lgkmcnt(5)\n"
  "ds_read_b128 a[132:135], v235 offset:26816\n"
  "v_fma_f32 v17, v17, s73, v224\n"
  "v_mfma_f32_16x16x32_bf16 v[192:195], v[60:63], a[120:123], v[192:195]\n"
  "v_exp_f32 v16, v16\n"
  "v_mfma_f32_16x16x32_bf16 v[192:195], v[56:59], a[124:127], v[192:195]\n"
  "s_add_u32 m0, s72, 18912\n"
  "ds_write_b64 v231, v[64:65] offset:2048\n"
  "v_mfma_f32_16x16x32_bf16 v[160:163], v[44:47], a[120:123], v[160:163]\n"
  "v_exp_f32 v17, v17\n"
  "v_mfma_f32_16x16x32_bf16 v[160:163], v[40:43], a[124:127], v[160:163]\n"
  "v_fma_f32 v18, v18, s73, v224\n"
  "v_fma_f32 v19, v19, s73, v224\n"
  "v_mfma_f32_16x16x32_bf16 v[128:131], v[28:31], a[120:123], v[128:131]\n"
  "v_exp_f32 v18, v18\n"
  "v_mfma_f32_16x16x32_bf16 v[128:131], v[24:27], a[124:127], v[128:131]\n"
  "buffer_load_dwordx4 v221, s[48:51], 0 offen offset:128 nt lds\n"
  "v_add_f32 v253, v253, v16\n"
  "v_mfma_f32_16x16x32_bf16 v[96:99], v[12:15], a[120:123], v[96:99]\n"
  "v_exp_f32 v19, v19\n"
  "v_mfma_f32_16x16x32_bf16 v[96:99], v[8:11], a[124:127], v[96:99]\n"
  "s_waitcnt lgkmcnt(5)\n"
  "v_add_f32 v253, v253, v17\n"
  "v_add_f32 v253, v253, v18\n"
  "v_mfma_f32_16x16x32_bf16 v[4:7], a[136:139], a[0:3], v[248:251]\n"
  "v_add_f32 v253, v253, v19\n"
  "v_fma_f32 v32, v32, s73, v225\n"
  "v_mfma_f32_16x16x32_bf16 v[4:7], a[140:143], a[4:7], v[4:7]\n"
  "s_waitcnt lgkmcnt(3)\n"
  "v_exp_f32 v32, v32\n"
  "v_mfma_f32_16x16x32_bf16 v[4:7], a[144:147], a[8:11], v[4:7]\n"
  "v_fma_f32 v33, v33, s73, v225\n"
  "v_fma_f32 v34, v34, s73, v225\n"
  "v_mfma_f32_16x16x32_bf16 v[4:7], a[148:151], a[12:15], v[4:7]\n"
  "s_waitcnt lgkmcnt(1)\n"
  "v_exp_f32 v33, v33\n"
  "v_mfma_f32_16x16x32_bf16 v[4:7], a[128:131], a[16:19], v[4:7]\n"
  "v_exp_f32 v34, v34\n"
  "v_mfma_f32_16x16x32_bf16 v[4:7], a[132:135], a[20:23], v[4:7]\n"
  "v_fma_f32 v35, v35, s73, v225\n"
  "v_add_f32 v254, v254, v32\n"
  "v_mfma_f32_16x16x32_bf16 v[20:23], a[132:135], a[44:47], v[248:251]\n"
  "v_exp_f32 v35, v35\n"
  "v_mfma_f32_16x16x32_bf16 v[20:23], a[128:131], a[40:43], v[20:23]\n"
  "v_add_f32 v254, v254, v33\n"
  "v_add_f32 v254, v254, v34\n"
  "v_mfma_f32_16x16x32_bf16 v[20:23], a[148:151], a[36:39], v[20:23]\n"
  "ds_read_b64_tr_b16 a[120:121], v219 offset:24720\n"
  "v_add_f32 v254, v254, v35\n"
  "v_mfma_f32_16x16x32_bf16 v[20:23], a[144:147], a[32:35], v[20:23]\n"
  "ds_read_b64_tr_b16 a[122:123], v220 offset:24720\n"
  "v_fma_f32 v48, v48, s73, v226\n"
  "v_mfma_f32_16x16x32_bf16 v[20:23], a[140:143], a[28:31], v[20:23]\n"
  "ds_read_b64_tr_b16 a[124:125], v219 offset:25744\n"
  "v_fma_f32 v49, v49, s73, v226\n"
  "v_mfma_f32_16x16x32_bf16 v[20:23], a[136:139], a[24:27], v[20:23]\n"
  "ds_read_b64_tr_b16 a[126:127], v220 offset:25744\n"
  "v_fma_f32 v50, v50, s73, v226\n"
  "v_mfma_f32_16x16x32_bf16 v[36:39], a[136:139], a[48:51], v[248:251]\n"
  "v_exp_f32 v48, v48\n"
  "v_mfma_f32_16x16x32_bf16 v[36:39], a[140:143], a[52:55], v[36:39]\n"
  "v_exp_f32 v49, v49\n"
  "v_mfma_f32_16x16x32_bf16 v[36:39], a[144:147], a[56:59], v[36:39]\n"
  "v_exp_f32 v50, v50\n"
  "v_mfma_f32_16x16x32_bf16 v[36:39], a[148:151], a[60:63], v[36:39]\n"
  "v_fma_f32 v51, v51, s73, v226\n"
  "v_add_f32 v255, v255, v48\n"
  "v_mfma_f32_16x16x32_bf16 v[36:39], a[128:131], a[64:67], v[36:39]\n"
  "v_exp_f32 v51, v51\n"
  "v_mfma_f32_16x16x32_bf16 v[36:39], a[132:135], a[68:71], v[36:39]\n"
  "v_add_f32 v255, v255, v49\n"
  "v_add_f32 v255, v255, v50\n"
  "v_mfma_f32_16x16x32_bf16 v[52:55], a[132:135], a[92:95], v[248:251]\n"
  "ds_read_b64_tr_b16 a[132:133], v217 offset:26752\n"
  "v_add_f32 v255, v255, v51\n"
  "v_mfma_f32_16x16x32_bf16 v[52:55], a[128:131], a[88:91], v[52:55]\n"
  "ds_read_b64_tr_b16 a[134:135], v218 offset:26752\n"
  "v_fma_f32 v20, v20, s73, v224\n"
  "v_mfma_f32_16x16x32_bf16 v[52:55], a[148:151], a[84:87], v[52:55]\n"
  "ds_read_b64_tr_b16 a[128:129], v217 offset:26768\n"
  "v_fma_f32 v36, v36, s73, v225\n"
  "v_mfma_f32_16x16x32_bf16 v[52:55], a[144:147], a[80:83], v[52:55]\n"
  "ds_read_b64_tr_b16 a[130:131], v218 offset:26768\n"
  "v_fma_f32 v37, v37, s73, v225\n"
  "v_mfma_f32_16x16x32_bf16 v[52:55], a[140:143], a[76:79], v[52:55]\n"
  "v_exp_f32 v36, v36\n"
  "v_mfma_f32_16x16x32_bf16 v[52:55], a[136:139], a[72:75], v[52:55]\n"
  "s_waitcnt lgkmcnt(4)\n"
  "v_exp_f32 v37, v37\n"
  "v_mfma_f32_16x16x32_bf16 v[100:103], v[12:15], a[120:123], v[100:103]\n"
  "v_cvt_pk_bf16_f32 v12, v0, v1\n"
  "v_cvt_pk_bf16_f32 v13, v2, v3\n"
  "v_mfma_f32_16x16x32_bf16 v[100:103], v[8:11], a[124:127], v[100:103]\n"
  "v_exp_f32 v20, v20\n"
  "v_mfma_f32_16x16x32_bf16 v[132:135], v[28:31], a[120:123], v[132:135]\n"
  "ds_read_b64_tr_b16 a[140:141], v219 offset:26752\n"
  "v_cvt_pk_bf16_f32 v28, v16, v17\n"
  "v_mfma_f32_16x16x32_bf16 v[132:135], v[24:27], a[124:127], v[132:135]\n"
  "ds_read_b64_tr_b16 a[142:143], v220 offset:26752\n"
  "v_cvt_pk_bf16_f32 v29, v18, v19\n"
  "v_mfma_f32_16x16x32_bf16 v[164:167], v[44:47], a[120:123], v[164:167]\n"
  "ds_read_b64_tr_b16 a[136:137], v219 offset:26768\n"
  "v_cvt_pk_bf16_f32 v44, v32, v33\n"
  "v_mfma_f32_16x16x32_bf16 v[164:167], v[40:43], a[124:127], v[164:167]\n"
  "ds_read_b64_tr_b16 a[138:139], v220 offset:26768\n"
  "v_cvt_pk_bf16_f32 v45, v34, v35\n"
  "v_mfma_f32_16x16x32_bf16 v[196:199], v[60:63], a[120:123], v[196:199]\n"
  "v_cvt_pk_bf16_f32 v60, v48, v49\n"
  "v_cvt_pk_bf16_f32 v61, v50, v51\n"
  "v_mfma_f32_16x16x32_bf16 v[196:199], v[56:59], a[124:127], v[196:199]\n"
  "s_waitcnt lgkmcnt(4)\n"
  "v_fma_f32 v52, v52, s73, v226\n"
  "v_fma_f32 v53, v53, s73, v226\n"
  "v_mfma_f32_16x16x32_bf16 a[244:247], a[156:159], a[132:135], a[244:247]\n"
  "v_exp_f32 v52, v52\n"
  "v_mfma_f32_16x16x32_bf16 a[248:251], a[156:159], a[128:131], a[248:251]\n"
  "v_exp_f32 v53, v53\n"
  "v_mfma_f32_16x16x32_bf16 v[200:203], v[56:59], a[132:135], v[200:203]\n"
  "v_fma_f32 v54, v54, s73, v226\n"
  "v_fma_f32 v55, v55, s73, v226\n"
  "v_mfma_f32_16x16x32_bf16 v[204:207], v[56:59], a[128:131], v[204:207]\n"
  "v_exp_f32 v54, v54\n"
  "v_mfma_f32_16x16x32_bf16 v[168:171], v[40:43], a[132:135], v[168:171]\n"
  "v_exp_f32_e64 v55, v55\n"
  "v_mfma_f32_16x16x32_bf16 v[172:175], v[40:43], a[128:131], v[172:175]\n"
  "s_waitcnt vmcnt(14) lgkmcnt(0)\n"
  "s_barrier\n"
  "ds_read_b128 a[120:123], v216 offset:0\n"
  "v_mfma_f32_16x16x32_bf16 v[136:139], v[24:27], a[132:135], v[136:139]\n"
  "ds_read_b64 a[160:161], v231 offset:2048\n"
  "v_cvt_pk_bf16_f32 v62, v52, v53\n"
  "v_mfma_f32_16x16x32_bf16 v[140:143], v[24:27], a[128:131], v[140:143]\n"
  "ds_read_b64 a[162:163], v232 offset:2048\n"
  "v_cvt_pk_bf16_f32 v63, v54, v55\n"
  "v_mfma_f32_16x16x32_bf16 v[104:107], v[8:11], a[132:135], v[104:107]\n"
  "ds_read_b128 a[124:127], v216 offset:64\n"
  "v_fma_f32 v38, v38, s73, v225\n"
  "v_mfma_f32_16x16x32_bf16 v[108:111], v[8:11], a[128:131], v[108:111]\n"
  "ds_read_b128 a[132:135], v216 offset:1024\n"
  "v_fma_f32 v39, v39, s73, v225\n"
  "v_mfma_f32_16x16x32_bf16 v[112:115], v[8:11], a[140:143], v[112:115]\n"
  "ds_read_b128 a[128:131], v216 offset:1088\n"
  "v_cvt_pk_bf16_f32 v46, v36, v37\n"
  "v_mfma_f32_16x16x32_bf16 v[116:119], v[8:11], a[136:139], v[116:119]\n"
  "s_mov_b32 m0, s68\n"
  "ds_read_b128 a[148:151], v216 offset:2048\n"
  "v_fma_f32 v21, v21, s73, v224\n"
  "v_mfma_f32_16x16x32_bf16 v[144:147], v[24:27], a[140:143], v[144:147]\n"
  "ds_read_b128 a[144:147], v216 offset:2112\n"
  "v_fma_f32 v22, v22, s73, v224\n"
  "v_mfma_f32_16x16x32_bf16 v[148:151], v[24:27], a[136:139], v[148:151]\n"
  "v_exp_f32 v38, v38\n"
  "v_mfma_f32_16x16x32_bf16 v[176:179], v[40:43], a[140:143], v[176:179]\n"
  "v_exp_f32 v39, v39\n"
  "v_mfma_f32_16x16x32_bf16 v[180:183], v[40:43], a[136:139], v[180:183]\n"
  "buffer_load_dwordx4 v221, s[40:43], 0 offen offset:0 lds\n"
  "v_mfma_f32_16x16x32_bf16 v[208:211], v[56:59], a[140:143], v[208:211]\n"
  "s_addk_i32 m0, 896\n"
  "v_exp_f32 v21, v21\n"
  "v_mfma_f32_16x16x32_bf16 v[212:215], v[56:59], a[136:139], v[212:215]\n"
  "s_waitcnt lgkmcnt(4)\n"
  "v_exp_f32 v22, v22\n"
  "v_mfma_f32_16x16x32_bf16 v[64:67], a[120:123], a[96:99], v[248:251]\n"
  "v_cvt_pk_bf16_f32 v47, v38, v39\n"
  "v_fma_f32 v23, v23, s73, v224\n"
  "v_mfma_f32_16x16x32_bf16 v[64:67], a[124:127], a[100:103], v[64:67]\n"
  "s_waitcnt lgkmcnt(2)\n"
  "buffer_load_dwordx4 v221, s[40:43], 0 offen offset:128 lds\n"
  "v_cvt_pk_bf16_f32 v30, v20, v21\n"
  "v_mfma_f32_16x16x32_bf16 v[64:67], a[132:135], a[104:107], v[64:67]\n"
  "s_addk_i32 m0, 896\n"
  "v_exp_f32 v23, v23\n"
  "v_mfma_f32_16x16x32_bf16 v[64:67], a[128:131], a[108:111], v[64:67]\n"
  "s_waitcnt lgkmcnt(0)\n"
  "v_cvt_pk_bf16_f32 v31, v22, v23\n"
  "v_fma_f32 v4, v4, s73, v223\n"
  "v_mfma_f32_16x16x32_bf16 v[64:67], a[148:151], a[112:115], v[64:67]\n"
  "v_exp_f32 v4, v4\n"
  "v_mfma_f32_16x16x32_bf16 v[64:67], a[144:147], a[116:119], v[64:67]\n"
  "buffer_load_dwordx4 v221, s[40:43], 0 offen offset:256 lds\n"
  "v_fma_f32 v5, v5, s73, v223\n"
  "v_mfma_f32_16x16x32_bf16 v[0:3], a[144:147], a[20:23], v[248:251]\n"
  "s_add_u32 s40, s40, s31\n"
  "v_exp_f32 v5, v5\n"
  "v_mfma_f32_16x16x32_bf16 v[0:3], a[148:151], a[16:19], v[0:3]\n"
  "s_addc_u32 s41, s41, 0\n"
  "v_fma_f32 v6, v6, s73, v223\n"
  "v_fma_f32 v7, v7, s73, v223\n"
  "v_mfma_f32_16x16x32_bf16 v[0:3], a[128:131], a[12:15], v[0:3]\n"
  "v_exp_f32 v6, v6\n"
  "v_mfma_f32_16x16x32_bf16 v[0:3], a[132:135], a[8:11], v[0:3]\n"
  "s_mov_b32 m0, s60\n"
  "v_exp_f32 v7, v7\n"
  "v_mfma_f32_16x16x32_bf16 v[0:3], a[124:127], a[4:7], v[0:3]\n"
  "v_cvt_pk_bf16_f32 v14, v4, v5\n"
  "v_cvt_pk_bf16_f32 v15, v6, v7\n"
  "v_mfma_f32_16x16x32_bf16 v[0:3], a[120:123], a[0:3], v[0:3]\n"
  "v_add_f32 v252, v252, v4\n"
  "v_add_f32 v252, v252, v5\n"
  "v_mfma_f32_16x16x32_bf16 v[16:19], a[120:123], a[24:27], v[248:251]\n"
  "v_add_f32 v252, v252, v6\n"
  "v_add_f32 v252, v252, v7\n"
  "v_mfma_f32_16x16x32_bf16 v[16:19], a[124:127], a[28:31], v[16:19]\n"
  "buffer_load_dwordx4 v221, s[32:35], 0 offen offset:0 lds\n"
  "v_add_f32 v253, v253, v20\n"
  "v_mfma_f32_16x16x32_bf16 v[16:19], a[132:135], a[32:35], v[16:19]\n"
  "s_addk_i32 m0, 1024\n"
  "ds_read_b64_tr_b16 a[140:141], v217 offset:0\n"
  "v_add_f32 v253, v253, v21\n"
  "v_mfma_f32_16x16x32_bf16 v[16:19], a[128:131], a[36:39], v[16:19]\n"
  "s_mov_b64 s[32:33], s[44:45]\n"
  "ds_read_b64_tr_b16 a[142:143], v218 offset:0\n"
  "v_add_f32 v253, v253, v22\n"
  "v_mfma_f32_16x16x32_bf16 v[16:19], a[148:151], a[40:43], v[16:19]\n"
  "ds_read_b64_tr_b16 a[136:137], v217 offset:1024\n"
  "v_add_f32 v253, v253, v23\n"
  "v_mfma_f32_16x16x32_bf16 v[16:19], a[144:147], a[44:47], v[16:19]\n"
  "buffer_load_dwordx4 v221, s[44:47], 0 offen offset:0 lds\n"
  "v_add_f32 v254, v254, v36\n"
  "v_mfma_f32_16x16x32_bf16 v[32:35], a[144:147], a[68:71], v[248:251]\n"
  "s_addk_i32 m0, 768\n"
  "ds_read_b64_tr_b16 a[138:139], v218 offset:1024\n"
  "v_add_f32 v254, v254, v37\n"
  "v_mfma_f32_16x16x32_bf16 v[32:35], a[148:151], a[64:67], v[32:35]\n"
  "v_add_f32 v254, v254, v38\n"
  "v_add_f32 v254, v254, v39\n"
  "v_mfma_f32_16x16x32_bf16 v[32:35], a[128:131], a[60:63], v[32:35]\n"
  "v_add_f32 v255, v255, v52\n"
  "v_add_f32 v255, v255, v53\n"
  "v_mfma_f32_16x16x32_bf16 v[32:35], a[132:135], a[56:59], v[32:35]\n"
  "buffer_load_dwordx4 v221, s[44:47], 0 offen offset:256 lds\n"
  "v_add_f32 v255, v255, v54\n"
  "v_mfma_f32_16x16x32_bf16 v[32:35], a[124:127], a[52:55], v[32:35]\n"
  "s_add_u32 s44, s44, s31\n"
  "ds_read_b64_tr_b16 a[252:253], v230 offset:20160\n"
  "v_add_f32 v255, v255, v55\n"
  "v_mfma_f32_16x16x32_bf16 v[32:35], a[120:123], a[48:51], v[32:35]\n"
  "s_addc_u32 s45, s45, 0\n"
  "ds_read_b64_tr_b16 a[254:255], v230 offset:20192\n"
  "ds_read_b64_tr_b16 v[244:245], v230 offset:8960\n"
  "v_mfma_f32_16x16x32_bf16 v[48:51], a[120:123], a[72:75], v[248:251]\n"
  "ds_read_b64_tr_b16 v[246:247], v230 offset:8992\n"
  "v_fma_f32 v64, v64, s73, v227\n"
  "v_mfma_f32_16x16x32_bf16 v[48:51], a[124:127], a[76:79], v[48:51]\n"
  "v_exp_f32 v64, v64\n"
  "v_mfma_f32_16x16x32_bf16 v[48:51], a[132:135], a[80:83], v[48:51]\n"
  "ds_read_b64_tr_b16 a[120:121], v217 offset:16\n"
  "v_fma_f32 v65, v65, s73, v227\n"
  "v_mfma_f32_16x16x32_bf16 v[48:51], a[128:131], a[84:87], v[48:51]\n"
  "ds_read_b64_tr_b16 a[122:123], v218 offset:16\n"
  "v_fma_f32 v66, v66, s73, v227\n"
  "v_mfma_f32_16x16x32_bf16 v[48:51], a[148:151], a[88:91], v[48:51]\n"
  "ds_read_b64_tr_b16 a[124:125], v217 offset:1040\n"
  "v_fma_f32 v67, v67, s73, v227\n"
  "v_mfma_f32_16x16x32_bf16 v[48:51], a[144:147], a[92:95], v[48:51]\n"
  "s_waitcnt lgkmcnt(7)\n"
  "ds_read_b64_tr_b16 a[126:127], v218 offset:1040\n"
  "v_fma_f32 v0, v0, s73, v223\n"
  "v_mfma_f32_16x16x32_bf16 a[228:231], a[156:159], a[140:143], a[228:231]\n"
  "v_exp_f32 v65, v65\n"
  "v_mfma_f32_16x16x32_bf16 a[228:231], a[160:163], a[136:139], a[228:231]\n"
  "v_exp_f32 v66, v66\n"
  "v_mfma_f32_16x16x32_bf16 a[212:215], v[56:59], a[140:143], a[212:215]\n"
  "v_exp_f32 v67, v67\n"
  "v_mfma_f32_16x16x32_bf16 a[212:215], v[60:63], a[136:139], a[212:215]\n"
  "v_exp_f32 v0, v0\n"
  "v_mfma_f32_16x16x32_bf16 a[196:199], v[40:43], a[140:143], a[196:199]\n"
  "v_cvt_pk_bf16_f32 v64, v64, v65\n"
  "v_cvt_pk_bf16_f32 v65, v66, v67\n"
  "v_mfma_f32_16x16x32_bf16 a[196:199], v[44:47], a[136:139], a[196:199]\n"
  "v_fma_f32 v1, v1, s73, v223\n"
  "v_fma_f32 v2, v2, s73, v223\n"
  "v_mfma_f32_16x16x32_bf16 a[180:183], v[24:27], a[140:143], a[180:183]\n"
  "ds_read_b64_tr_b16 a[132:133], v219 offset:0\n"
  "v_fma_f32 v3, v3, s73, v223\n"
  "v_mfma_f32_16x16x32_bf16 a[180:183], v[28:31], a[136:139], a[180:183]\n"
  "ds_read_b64_tr_b16 a[134:135], v220 offset:0\n"
  "v_add_f32 v252, v252, v0\n"
  "v_mfma_f32_16x16x32_bf16 a[164:167], v[8:11], a[140:143], a[164:167]\n"
  "ds_read_b64_tr_b16 a[128:129], v219 offset:1024\n"
  "v_fma_f32 v16, v16, s73, v224\n"
  "v_mfma_f32_16x16x32_bf16 a[164:167], v[12:15], a[136:139], a[164:167]\n"
  "ds_read_b64_tr_b16 a[130:131], v220 offset:1024\n"
  "v_fma_f32 v17, v17, s73, v224\n"
  "v_mfma_f32_16x16x32_bf16 v[84:87], a[156:159], a[152:155], v[84:87]\n"
  "v_exp_f32 v1, v1\n"
  "v_mfma_f32_16x16x32_bf16 v[84:87], a[160:163], a[152:155], v[84:87]\n"
  "s_waitcnt lgkmcnt(4)\n"
  "v_exp_f32 v2, v2\n"
  "v_mfma_f32_16x16x32_bf16 a[168:171], v[8:11], a[120:123], a[168:171]\n"
  "v_exp_f32 v3, v3\n"
  "v_mfma_f32_16x16x32_bf16 a[168:171], v[12:15], a[124:127], a[168:171]\n"
  "v_exp_f32 v16, v16\n"
  "v_mfma_f32_16x16x32_bf16 a[184:187], v[24:27], a[120:123], a[184:187]\n"
  "ds_read_b128 a[140:143], v235 offset:0\n"
  "v_add_f32 v252, v252, v1\n"
  "v_mfma_f32_16x16x32_bf16 a[184:187], v[28:31], a[124:127], a[184:187]\n"
  "ds_read_b128 a[136:139], v235 offset:64\n"
  "v_add_f32 v252, v252, v2\n"
  "v_mfma_f32_16x16x32_bf16 a[200:203], v[40:43], a[120:123], a[200:203]\n"
  "ds_read_b128 a[148:151], v235 offset:1024\n"
  "v_add_f32 v252, v252, v3\n"
  "v_mfma_f32_16x16x32_bf16 a[200:203], v[44:47], a[124:127], a[200:203]\n"
  "ds_read_b128 a[144:147], v235 offset:1088\n"
  "v_fma_f32 v18, v18, s73, v224\n"
  "v_mfma_f32_16x16x32_bf16 a[216:219], v[56:59], a[120:123], a[216:219]\n"
  "v_exp_f32 v17, v17\n"
  "v_mfma_f32_16x16x32_bf16 a[216:219], v[60:63], a[124:127], a[216:219]\n"
  "v_exp_f32 v18, v18\n"
  "v_mfma_f32_16x16x32_bf16 a[232:235], a[156:159], a[120:123], a[232:235]\n"
  "ds_read_b128 a[120:123], v235 offset:2048\n"
  "v_fma_f32 v19, v19, s73, v224\n"
  "v_mfma_f32_16x16x32_bf16 a[232:235], a[160:163], a[124:127], a[232:235]\n"
  "s_waitcnt lgkmcnt(5)\n"
  "ds_read_b128 a[124:127], v235 offset:2112\n"
  "v_add_f32 v253, v253, v16\n"
  "v_mfma_f32_16x16x32_bf16 a[220:223], v[56:59], a[132:135], a[220:223]\n"
  "v_exp_f32 v19, v19\n"
  "v_mfma_f32_16x16x32_bf16 a[220:223], v[60:63], a[128:131], a[220:223]\n"
  "ds_write_b64 v231, v[64:65] offset:0\n"
  "v_mfma_f32_16x16x32_bf16 a[204:207], v[40:43], a[132:135], a[204:207]\n"
  "v_add_f32 v253, v253, v17\n"
  "v_add_f32 v253, v253, v18\n"
  "v_mfma_f32_16x16x32_bf16 a[204:207], v[44:47], a[128:131], a[204:207]\n"
  "buffer_store_dwordx4 a[252:255], v228, s[52:55], s56 offen offset:256 nt\n"
  "v_add_f32 v253, v253, v19\n"
  "v_fma_f32 v32, v32, s73, v225\n"
  "v_fma_f32 v33, v33, s73, v225\n"
  "v_mfma_f32_16x16x32_bf16 a[188:191], v[24:27], a[132:135], a[188:191]\n"
  "ds_read_b64_tr_b16 a[252:253], v230 offset:10080\n"
  "ds_read_b64_tr_b16 a[254:255], v230 offset:10112\n"
  "v_mfma_f32_16x16x32_bf16 a[188:191], v[28:31], a[128:131], a[188:191]\n"
  "v_exp_f32 v32, v32\n"
  "v_mfma_f32_16x16x32_bf16 a[172:175], v[8:11], a[132:135], a[172:175]\n"
  "v_exp_f32 v33, v33\n"
  "v_mfma_f32_16x16x32_bf16 a[172:175], v[12:15], a[128:131], a[172:175]\n"
  "s_waitcnt lgkmcnt(7)\n"
  "v_fma_f32 v34, v34, s73, v225\n"
  "v_fma_f32 v35, v35, s73, v225\n"
  "v_mfma_f32_16x16x32_bf16 v[4:7], a[140:143], a[0:3], v[248:251]\n"
  "v_exp_f32 v34, v34\n"
  "v_mfma_f32_16x16x32_bf16 v[4:7], a[136:139], a[4:7], v[4:7]\n"
  "s_waitcnt lgkmcnt(5)\n"
  "v_exp_f32 v35, v35\n"
  "v_mfma_f32_16x16x32_bf16 v[4:7], a[148:151], a[8:11], v[4:7]\n"
  "v_add_f32 v254, v254, v32\n"
  "v_add_f32 v254, v254, v33\n"
  "v_mfma_f32_16x16x32_bf16 v[4:7], a[144:147], a[12:15], v[4:7]\n"
  "s_waitcnt lgkmcnt(3)\n"
  "v_add_f32 v254, v254, v34\n"
  "v_add_f32 v254, v254, v35\n"
  "v_mfma_f32_16x16x32_bf16 v[4:7], a[120:123], a[16:19], v[4:7]\n"
  "v_fma_f32 v48, v48, s73, v226\n"
  "v_fma_f32 v49, v49, s73, v226\n"
  "v_mfma_f32_16x16x32_bf16 v[4:7], a[124:127], a[20:23], v[4:7]\n"
  "buffer_store_dwordx4 v[244:247], v228, s[52:55], s80 offen offset:128 nt\n"
  "v_exp_f32 v48, v48\n"
  "v_fma_f32 v50, v50, s73, v226\n"
  "v_mfma_f32_16x16x32_bf16 v[20:23], a[124:127], a[44:47], v[248:251]\n"
  "v_exp_f32 v49, v49\n"
  "v_mfma_f32_16x16x32_bf16 v[20:23], a[120:123], a[40:43], v[20:23]\n"
  "v_exp_f32 v50, v50\n"
  "v_mfma_f32_16x16x32_bf16 v[20:23], a[144:147], a[36:39], v[20:23]\n"
  "ds_read_b64_tr_b16 a[132:133], v219 offset:16\n"
  "v_fma_f32 v51, v51, s73, v226\n"
  "v_mfma_f32_16x16x32_bf16 v[20:23], a[148:151], a[32:35], v[20:23]\n"
  "ds_read_b64_tr_b16 a[134:135], v220 offset:16\n"
  "v_add_f32 v255, v255, v48\n"
  "v_mfma_f32_16x16x32_bf16 v[20:23], a[136:139], a[28:31], v[20:23]\n"
  "ds_read_b64_tr_b16 a[128:129], v219 offset:1040\n"
  "v_add_f32 v255, v255, v49\n"
  "v_mfma_f32_16x16x32_bf16 v[20:23], a[140:143], a[24:27], v[20:23]\n"
  "s_waitcnt lgkmcnt(3)\n"
  "buffer_store_dwordx4 a[252:255], v228, s[52:55], s80 offen offset:3200 nt\n"
  "ds_read_b64_tr_b16 a[130:131], v220 offset:1040\n"
  "v_exp_f32 v51, v51\n"
  "v_mfma_f32_16x16x32_bf16 v[36:39], a[140:143], a[48:51], v[248:251]\n"
  "v_add_f32 v255, v255, v50\n"
  "v_add_f32 v255, v255, v51\n"
  "v_mfma_f32_16x16x32_bf16 v[36:39], a[136:139], a[52:55], v[36:39]\n"
  "v_fma_f32 v4, v4, s73, v223\n"
  "v_fma_f32 v5, v5, s73, v223\n"
  "v_mfma_f32_16x16x32_bf16 v[36:39], a[148:151], a[56:59], v[36:39]\n"
  "v_exp_f32 v4, v4\n"
  "v_mfma_f32_16x16x32_bf16 v[36:39], a[144:147], a[60:63], v[36:39]\n"
  "v_exp_f32 v5, v5\n"
  "v_mfma_f32_16x16x32_bf16 v[36:39], a[120:123], a[64:67], v[36:39]\n"
  "v_fma_f32 v20, v20, s73, v224\n"
  "v_fma_f32 v21, v21, s73, v224\n"
  "v_mfma_f32_16x16x32_bf16 v[36:39], a[124:127], a[68:71], v[36:39]\n"
  "v_exp_f32 v20, v20\n"
  "v_mfma_f32_16x16x32_bf16 v[52:55], a[124:127], a[92:95], v[248:251]\n"
  "ds_read_b64_tr_b16 a[124:125], v217 offset:2048\n"
  "v_fma_f32 v22, v22, s73, v224\n"
  "v_mfma_f32_16x16x32_bf16 v[52:55], a[120:123], a[88:91], v[52:55]\n"
  "ds_read_b64_tr_b16 a[126:127], v218 offset:2048\n"
  "v_fma_f32 v23, v23, s73, v224\n"
  "v_mfma_f32_16x16x32_bf16 v[52:55], a[144:147], a[84:87], v[52:55]\n"
  "ds_read_b64_tr_b16 a[120:121], v217 offset:2064\n"
  "v_fma_f32 v36, v36, s73, v225\n"
  "v_mfma_f32_16x16x32_bf16 v[52:55], a[148:151], a[80:83], v[52:55]\n"
  "ds_read_b64_tr_b16 a[122:123], v218 offset:2064\n"
  "v_fma_f32 v37, v37, s73, v225\n"
  "v_mfma_f32_16x16x32_bf16 v[52:55], a[136:139], a[76:79], v[52:55]\n"
  "v_exp_f32 v36, v36\n"
  "v_mfma_f32_16x16x32_bf16 v[52:55], a[140:143], a[72:75], v[52:55]\n"
  "s_waitcnt lgkmcnt(4)\n"
  "v_exp_f32 v37, v37\n"
  "v_mfma_f32_16x16x32_bf16 a[176:179], v[8:11], a[132:135], a[176:179]\n"
  "v_cvt_pk_bf16_f32 v8, v0, v1\n"
  "v_cvt_pk_bf16_f32 v9, v2, v3\n"
  "v_mfma_f32_16x16x32_bf16 a[176:179], v[12:15], a[128:131], a[176:179]\n"
  "v_exp_f32 v21, v21\n"
  "v_mfma_f32_16x16x32_bf16 a[192:195], v[24:27], a[132:135], a[192:195]\n"
  "ds_read_b64_tr_b16 a[136:137], v219 offset:2048\n"
  "v_cvt_pk_bf16_f32 v24, v16, v17\n"
  "v_mfma_f32_16x16x32_bf16 a[192:195], v[28:31], a[128:131], a[192:195]\n"
  "ds_read_b64_tr_b16 a[138:139], v220 offset:2048\n"
  "v_cvt_pk_bf16_f32 v25, v18, v19\n"
  "v_mfma_f32_16x16x32_bf16 a[208:211], v[40:43], a[132:135], a[208:211]\n"
  "ds_read_b64_tr_b16 a[140:141], v219 offset:2064\n"
  "v_cvt_pk_bf16_f32 v40, v32, v33\n"
  "v_mfma_f32_16x16x32_bf16 a[208:211], v[44:47], a[128:131], a[208:211]\n"
  "ds_read_b64_tr_b16 a[142:143], v220 offset:2064\n"
  "v_cvt_pk_bf16_f32 v41, v34, v35\n"
  "v_mfma_f32_16x16x32_bf16 a[224:227], v[56:59], a[132:135], a[224:227]\n"
  "v_exp_f32 v22, v22\n"
  "v_mfma_f32_16x16x32_bf16 a[224:227], v[60:63], a[128:131], a[224:227]\n"
  "s_waitcnt lgkmcnt(4)\n"
  "v_exp_f32 v23, v23\n"
  "v_mfma_f32_16x16x32_bf16 a[244:247], a[160:163], a[124:127], a[244:247]\n"
  "v_cvt_pk_bf16_f32 v56, v48, v49\n"
  "v_cvt_pk_bf16_f32 v57, v50, v51\n"
  "v_mfma_f32_16x16x32_bf16 a[248:251], a[160:163], a[120:123], a[248:251]\n"
  "v_fma_f32 v52, v52, s73, v226\n"
  "v_fma_f32 v53, v53, s73, v226\n"
  "v_mfma_f32_16x16x32_bf16 v[200:203], v[60:63], a[124:127], v[200:203]\n"
  "v_exp_f32 v52, v52\n"
  "v_mfma_f32_16x16x32_bf16 v[204:207], v[60:63], a[120:123], v[204:207]\n"
  "v_exp_f32 v53, v53\n"
  "v_mfma_f32_16x16x32_bf16 v[168:171], v[44:47], a[124:127], v[168:171]\n"
  "v_fma_f32 v54, v54, s73, v226\n"
  "v_fma_f32 v55, v55, s73, v226\n"
  "v_mfma_f32_16x16x32_bf16 v[172:175], v[44:47], a[120:123], v[172:175]\n"
  "s_waitcnt vmcnt(13) lgkmcnt(0)\n"
  "s_barrier\n"
  "ds_read_b128 a[132:135], v216 offset:12352\n"
  "v_mfma_f32_16x16x32_bf16 v[136:139], v[28:31], a[124:127], v[136:139]\n"
  "ds_read_b64 a[156:157], v231 offset:0\n"
  "v_cvt_pk_bf16_f32 v58, v52, v53\n"
  "v_mfma_f32_16x16x32_bf16 v[140:143], v[28:31], a[120:123], v[140:143]\n"
  "ds_read_b64 a[158:159], v232 offset:0\n"
  "v_fma_f32 v38, v38, s73, v225\n"
  "v_mfma_f32_16x16x32_bf16 v[104:107], v[12:15], a[124:127], v[104:107]\n"
  "ds_read_b128 a[128:131], v216 offset:12416\n"
  "v_fma_f32 v39, v39, s73, v225\n"
  "v_mfma_f32_16x16x32_bf16 v[108:111], v[12:15], a[120:123], v[108:111]\n"
  "ds_read_b128 a[124:127], v216 offset:13376\n"
  "v_cvt_pk_bf16_f32 v42, v36, v37\n"
  "v_mfma_f32_16x16x32_bf16 v[112:115], v[12:15], a[136:139], v[112:115]\n"
  "ds_read_b128 a[120:123], v216 offset:13440\n"
  "v_cvt_pk_bf16_f32 v26, v20, v21\n"
  "v_mfma_f32_16x16x32_bf16 v[116:119], v[12:15], a[140:143], v[116:119]\n"
  "s_mov_b32 m0, s66\n"
  "ds_read_b128 a[144:147], v216 offset:14400\n"
  "v_cvt_pk_bf16_f32 v27, v22, v23\n"
  "v_mfma_f32_16x16x32_bf16 v[144:147], v[28:31], a[136:139], v[144:147]\n"
  "ds_read_b128 a[148:151], v216 offset:14464\n"
  "v_fma_f32 v6, v6, s73, v223\n"
  "v_mfma_f32_16x16x32_bf16 v[148:151], v[28:31], a[140:143], v[148:151]\n"
  "v_exp_f32 v54, v54\n"
  "v_mfma_f32_16x16x32_bf16 v[176:179], v[44:47], a[136:139], v[176:179]\n"
  "v_exp_f32 v55, v55\n"
  "v_mfma_f32_16x16x32_bf16 v[180:183], v[44:47], a[140:143], v[180:183]\n"
  "buffer_load_dwordx4 v221, s[40:43], 0 offen offset:0 lds\n"
  "v_mfma_f32_16x16x32_bf16 v[208:211], v[60:63], a[136:139], v[208:211]\n"
  "s_addk_i32 m0, 896\n"
  "v_exp_f32 v38, v38\n"
  "v_mfma_f32_16x16x32_bf16 v[212:215], v[60:63], a[140:143], v[212:215]\n"
  "s_waitcnt lgkmcnt(4)\n"
  "v_exp_f32 v39, v39\n"
  "v_mfma_f32_16x16x32_bf16 v[64:67], a[132:135], a[96:99], v[248:251]\n"
  "s_add_u32 s71, s92, s81\n"
  "v_exp_f32 v6, v6\n"
  "v_mfma_f32_16x16x32_bf16 v[64:67], a[128:131], a[100:103], v[64:67]\n"
  "s_waitcnt lgkmcnt(2)\n"
  "buffer_load_dwordx4 v221, s[40:43], 0 offen offset:128 lds\n"
  "v_cvt_pk_bf16_f32 v59, v54, v55\n"
  "v_mfma_f32_16x16x32_bf16 v[64:67], a[124:127], a[104:107], v[64:67]\n"
  "s_addk_i32 m0, 896\n"
  "v_cvt_pk_bf16_f32 v43, v38, v39\n"
  "v_fma_f32 v7, v7, s73, v223\n"
  "v_mfma_f32_16x16x32_bf16 v[64:67], a[120:123], a[108:111], v[64:67]\n"
  "s_waitcnt lgkmcnt(0)\n"
  "v_exp_f32 v7, v7\n"
  "v_mfma_f32_16x16x32_bf16 v[64:67], a[144:147], a[112:115], v[64:67]\n"
  "v_cvt_pk_bf16_f32 v10, v4, v5\n"
  "v_cvt_pk_bf16_f32 v11, v6, v7\n"
  "v_mfma_f32_16x16x32_bf16 v[64:67], a[148:151], a[116:119], v[64:67]\n"
  "buffer_load_dwordx4 v221, s[40:43], 0 offen offset:256 lds\n"
  "v_add_f32 v252, v252, v4\n"
  "v_mfma_f32_16x16x32_bf16 v[0:3], a[148:151], a[20:23], v[248:251]\n"
  "s_add_u32 s40, s40, s31\n"
  "v_add_f32 v252, v252, v5\n"
  "v_add_f32 v252, v252, v6\n"
  "v_mfma_f32_16x16x32_bf16 v[0:3], a[144:147], a[16:19], v[0:3]\n"
  "s_addc_u32 s41, s41, 0\n"
  "v_add_f32 v252, v252, v7\n"
  "v_add_f32 v253, v253, v20\n"
  "v_mfma_f32_16x16x32_bf16 v[0:3], a[120:123], a[12:15], v[0:3]\n"
  "v_add_f32 v253, v253, v21\n"
  "v_add_f32 v253, v253, v22\n"
  "v_mfma_f32_16x16x32_bf16 v[0:3], a[124:127], a[8:11], v[0:3]\n"
  "s_add_u32 m0, s58, -128\n"
  "v_add_f32 v253, v253, v23\n"
  "v_add_f32 v254, v254, v36\n"
  "v_mfma_f32_16x16x32_bf16 v[0:3], a[128:131], a[4:7], v[0:3]\n"
  "v_add_f32 v254, v254, v37\n"
  "v_add_f32 v254, v254, v38\n"
  "v_mfma_f32_16x16x32_bf16 v[0:3], a[132:135], a[0:3], v[0:3]\n"
  "v_add_f32 v254, v254, v39\n"
  "v_bfe_u32 v237, v231, 1, 8\n"
  "v_mfma_f32_16x16x32_bf16 v[16:19], a[132:135], a[24:27], v[248:251]\n"
  "v_add_u32 v237, s29, v237\n"
  "v_add_f32 v255, v255, v52\n"
  "v_mfma_f32_16x16x32_bf16 v[16:19], a[128:131], a[28:31], v[16:19]\n"
  "buffer_load_dwordx4 v221, s[32:35], 0 offen offset:128 lds\n"
  "ds_read_b32 v236, v237\n"
  "v_mfma_f32_16x16x32_bf16 v[16:19], a[124:127], a[32:35], v[16:19]\n"
  "s_addk_i32 m0, 1024\n"
  "ds_read_b64_tr_b16 a[136:137], v217 offset:12352\n"
  "v_add_f32 v255, v255, v53\n"
  "v_mfma_f32_16x16x32_bf16 v[16:19], a[120:123], a[36:39], v[16:19]\n"
  "s_mov_b64 s[32:33], s[44:45]\n"
  "ds_read_b64_tr_b16 a[138:139], v218 offset:12352\n"
  "v_add_f32 v255, v255, v54\n"
  "v_mfma_f32_16x16x32_bf16 v[16:19], a[144:147], a[40:43], v[16:19]\n"
  "ds_read_b64_tr_b16 a[140:141], v217 offset:13376\n"
  "v_add_f32 v255, v255, v55\n"
  "v_mfma_f32_16x16x32_bf16 v[16:19], a[148:151], a[44:47], v[16:19]\n"
  "buffer_load_dwordx4 v221, s[44:47], 0 offen offset:128 lds\n"
  "v_fma_f32 v64, v64, s73, v227\n"
  "v_mfma_f32_16x16x32_bf16 v[32:35], a[148:151], a[68:71], v[248:251]\n"
  "s_addk_i32 m0, 896\n"
  "ds_read_b64_tr_b16 a[142:143], v218 offset:13376\n"
  "v_fma_f32 v65, v65, s73, v227\n"
  "v_mfma_f32_16x16x32_bf16 v[32:35], a[144:147], a[64:67], v[32:35]\n"
  "s_waitcnt lgkmcnt(4)\n"
  "v_exp_f32 v64, v64\n"
  "v_mfma_f32_16x16x32_bf16 v[32:35], a[120:123], a[60:63], v[32:35]\n"
  "v_exp_f32 v65, v65\n"
  "v_mfma_f32_16x16x32_bf16 v[32:35], a[124:127], a[56:59], v[32:35]\n"
  "buffer_load_dwordx4 v221, s[44:47], 0 offen offset:256 lds\n"
  "v_fma_f32 v66, v66, s73, v227\n"
  "v_mfma_f32_16x16x32_bf16 v[32:35], a[128:131], a[52:55], v[32:35]\n"
  "s_add_u32 s44, s44, s31\n"
  "v_exp_f32 v66, v66\n"
  "v_mfma_f32_16x16x32_bf16 v[32:35], a[132:135], a[48:51], v[32:35]\n"
  "s_addc_u32 s45, s45, 0\n"
  "v_fma_f32 v67, v67, s73, v227\n"
  "v_cvt_pk_bf16_f32 v64, v64, v65\n"
  "v_mfma_f32_16x16x32_bf16 v[48:51], a[132:135], a[72:75], v[248:251]\n"
  "ds_read_b64_tr_b16 a[132:133], v217 offset:12368\n"
  "v_fma_f32 v0, v0, s73, v223\n"
  "v_mfma_f32_16x16x32_bf16 v[48:51], a[128:131], a[76:79], v[48:51]\n"
  "ds_read_b64_tr_b16 a[134:135], v218 offset:12368\n"
  "v_fma_f32 v1, v1, s73, v223\n"
  "v_mfma_f32_16x16x32_bf16 v[48:51], a[124:127], a[80:83], v[48:51]\n"
  "ds_read_b64_tr_b16 a[128:129], v217 offset:13392\n"
  "v_fma_f32 v2, v2, s73, v223\n"
  "v_mfma_f32_16x16x32_bf16 v[48:51], a[120:123], a[84:87], v[48:51]\n"
  "s_add_u32 m0, s72, 19904\n"
  "ds_read_b64_tr_b16 a[130:131], v218 offset:13392\n"
  "v_mfma_f32_16x16x32_bf16 v[48:51], a[144:147], a[88:91], v[48:51]\n"
  "v_exp_f32 v67, v67\n"
  "v_mfma_f32_16x16x32_bf16 v[48:51], a[148:151], a[92:95], v[48:51]\n"
  "s_waitcnt lgkmcnt(4)\n"
  "v_exp_f32 v0, v0\n"
  "v_mfma_f32_16x16x32_bf16 a[236:239], a[160:163], a[136:139], a[236:239]\n"
  "v_exp_f32 v1, v1\n"
  "v_mfma_f32_16x16x32_bf16 a[236:239], a[156:159], a[140:143], a[236:239]\n"
  "buffer_load_dwordx4 v221, s[48:51], 0 offen offset:256 nt lds\n"
  "v_cvt_pk_bf16_f32 v65, v66, v67\n"
  "v_mfma_f32_16x16x32_bf16 v[184:187], v[60:63], a[136:139], v[184:187]\n"
  "s_add_u32 s48, s84, s71\n"
  "v_exp_f32 v2, v2\n"
  "v_mfma_f32_16x16x32_bf16 v[184:187], v[56:59], a[140:143], v[184:187]\n"
  "s_addc_u32 s49, s85, 0\n"
  "v_fma_f32 v3, v3, s73, v223\n"
  "v_add_f32 v252, v252, v0\n"
  "v_mfma_f32_16x16x32_bf16 v[152:155], v[44:47], a[136:139], v[152:155]\n"
  "s_sub_i32 s71, s38, s81\n"
  "ds_read_b64_tr_b16 a[124:125], v219 offset:12352\n"
  "v_add_f32 v252, v252, v1\n"
  "v_mfma_f32_16x16x32_bf16 v[152:155], v[40:43], a[140:143], v[152:155]\n"
  "s_add_u32 m0, s72, 8832\n"
  "ds_read_b64_tr_b16 a[126:127], v220 offset:12352\n"
  "v_add_f32 v252, v252, v2\n"
  "v_mfma_f32_16x16x32_bf16 v[120:123], v[28:31], a[136:139], v[120:123]\n"
  "s_max_i32 s50, s71, 0\n"
  "ds_read_b64_tr_b16 a[120:121], v219 offset:13376\n"
  "v_fma_f32 v16, v16, s73, v224\n"
  "v_mfma_f32_16x16x32_bf16 v[120:123], v[24:27], a[140:143], v[120:123]\n"
  "ds_read_b64_tr_b16 a[122:123], v220 offset:13376\n"
  "v_fma_f32 v17, v17, s73, v224\n"
  "v_mfma_f32_16x16x32_bf16 v[88:91], v[12:15], a[136:139], v[88:91]\n"
  "v_exp_f32 v3, v3\n"
  "v_mfma_f32_16x16x32_bf16 v[88:91], v[8:11], a[140:143], v[88:91]\n"
  "s_waitcnt lgkmcnt(4)\n"
  "buffer_load_dwordx4 v221, s[48:51], 0 offen offset:128 nt lds\n"
  "v_add_f32 v252, v252, v3\n"
  "v_mfma_f32_16x16x32_bf16 v[92:95], v[12:15], a[132:135], v[92:95]\n"
  "v_exp_f32 v16, v16\n"
  "v_mfma_f32_16x16x32_bf16 v[92:95], v[8:11], a[128:131], v[92:95]\n"
  "v_exp_f32 v17, v17\n"
  "v_mfma_f32_16x16x32_bf16 v[124:127], v[28:31], a[132:135], v[124:127]\n"
  "ds_read_b128 a[136:139], v235 offset:12352\n"
  "v_fma_f32 v18, v18, s73, v224\n"
  "v_mfma_f32_16x16x32_bf16 v[124:127], v[24:27], a[128:131], v[124:127]\n"
  "s_add_u32 m0, s72, 6848\n"
  "ds_read_b128 a[140:143], v235 offset:12416\n"
  "v_add_f32 v253, v253, v16\n"
  "v_mfma_f32_16x16x32_bf16 v[156:159], v[44:47], a[132:135], v[156:159]\n"
  "ds_read_b128 a[144:147], v235 offset:13376\n"
  "v_fma_f32 v19, v19, s73, v224\n"
  "v_mfma_f32_16x16x32_bf16 v[156:159], v[40:43], a[128:131], v[156:159]\n"
  "ds_read_b128 a[148:151], v235 offset:13440\n"
  "v_add_f32 v253, v253, v17\n"
  "v_mfma_f32_16x16x32_bf16 v[188:191], v[60:63], a[132:135], v[188:191]\n"
  "v_exp_f32 v18, v18\n"
  "v_mfma_f32_16x16x32_bf16 v[188:191], v[56:59], a[128:131], v[188:191]\n"
  "buffer_load_dwordx4 v221, s[48:51], 0 offen offset:3200 nt lds\n"
  "v_add_f32 v253, v253, v18\n"
  "v_mfma_f32_16x16x32_bf16 a[240:243], a[160:163], a[132:135], a[240:243]\n"
  "ds_read_b128 a[132:135], v235 offset:14400\n"
  "v_fma_f32 v32, v32, s73, v225\n"
  "v_mfma_f32_16x16x32_bf16 a[240:243], a[156:159], a[128:131], a[240:243]\n"
  "s_waitcnt lgkmcnt(5)\n"
  "ds_read_b128 a[128:131], v235 offset:14464\n"
  "v_fma_f32 v33, v33, s73, v225\n"
  "v_mfma_f32_16x16x32_bf16 v[192:195], v[60:63], a[124:127], v[192:195]\n"
  "v_exp_f32 v19, v19\n"
  "v_mfma_f32_16x16x32_bf16 v[192:195], v[56:59], a[120:123], v[192:195]\n"
  "ds_write_b64 v231, v[64:65] offset:2048\n"
  "v_mfma_f32_16x16x32_bf16 v[160:163], v[44:47], a[124:127], v[160:163]\n"
  "v_exp_f32 v32, v32\n"
  "v_mfma_f32_16x16x32_bf16 v[160:163], v[40:43], a[120:123], v[160:163]\n"
  "v_exp_f32 v33, v33\n"
  "v_mfma_f32_16x16x32_bf16 v[128:131], v[28:31], a[124:127], v[128:131]\n"
  "v_add_f32 v253, v253, v19\n"
  "v_fma_f32 v34, v34, s73, v225\n"
  "v_mfma_f32_16x16x32_bf16 v[128:131], v[24:27], a[120:123], v[128:131]\n"
  "v_exp_f32 v34, v34\n"
  "v_mfma_f32_16x16x32_bf16 v[96:99], v[12:15], a[124:127], v[96:99]\n"
  "v_fma_f32 v35, v35, s73, v225\n"
  "v_add_f32 v254, v254, v32\n"
  "v_mfma_f32_16x16x32_bf16 v[96:99], v[8:11], a[120:123], v[96:99]\n"
  "s_waitcnt lgkmcnt(5)\n"
  "v_exp_f32 v35, v35\n"
  "v_mfma_f32_16x16x32_bf16 v[4:7], a[136:139], a[0:3], v[248:251]\n"
  "v_add_f32 v254, v254, v33\n"
  "v_add_f32 v254, v254, v34\n"
  "v_mfma_f32_16x16x32_bf16 v[4:7], a[140:143], a[4:7], v[4:7]\n"
  "s_waitcnt lgkmcnt(3)\n"
  "v_add_f32 v254, v254, v35\n"
  "v_fma_f32 v48, v48, s73, v226\n"
  "v_mfma_f32_16x16x32_bf16 v[4:7], a[144:147], a[8:11], v[4:7]\n"
  "v_exp_f32 v48, v48\n"
  "v_mfma_f32_16x16x32_bf16 v[4:7], a[148:151], a[12:15], v[4:7]\n"
  "s_waitcnt lgkmcnt(1)\n"
  "v_fma_f32 v49, v49, s73, v226\n"
  "v_fma_f32 v50, v50, s73, v226\n"
  "v_mfma_f32_16x16x32_bf16 v[4:7], a[132:135], a[16:19], v[4:7]\n"
  "v_exp_f32 v49, v49\n"
  "v_mfma_f32_16x16x32_bf16 v[4:7], a[128:131], a[20:23], v[4:7]\n"
  "v_exp_f32 v50, v50\n"
  "v_mfma_f32_16x16x32_bf16 v[20:23], a[128:131], a[44:47], v[248:251]\n"
  "v_fma_f32 v51, v51, s73, v226\n"
  "v_add_f32 v255, v255, v48\n"
  "v_mfma_f32_16x16x32_bf16 v[20:23], a[132:135], a[40:43], v[20:23]\n"
  "v_exp_f32 v51, v51\n"
  "v_mfma_f32_16x16x32_bf16 v[20:23], a[148:151], a[36:39], v[20:23]\n"
  "ds_read_b64_tr_b16 a[124:125], v219 offset:12368\n"
  "v_add_f32 v255, v255, v49\n"
  "v_mfma_f32_16x16x32_bf16 v[20:23], a[144:147], a[32:35], v[20:23]\n"
  "ds_read_b64_tr_b16 a[126:127], v220 offset:12368\n"
  "v_add_f32 v255, v255, v50\n"
  "v_mfma_f32_16x16x32_bf16 v[20:23], a[140:143], a[28:31], v[20:23]\n"
  "ds_read_b64_tr_b16 a[120:121], v219 offset:13392\n"
  "v_add_f32 v255, v255, v51\n"
  "v_mfma_f32_16x16x32_bf16 v[20:23], a[136:139], a[24:27], v[20:23]\n"
  "ds_read_b64_tr_b16 a[122:123], v220 offset:13392\n"
  "v_fma_f32 v4, v4, s73, v223\n"
  "v_mfma_f32_16x16x32_bf16 v[36:39], a[136:139], a[48:51], v[248:251]\n"
  "v_exp_f32 v4, v4\n"
  "v_mfma_f32_16x16x32_bf16 v[36:39], a[140:143], a[52:55], v[36:39]\n"
  "v_fma_f32 v5, v5, s73, v223\n"
  "v_fma_f32 v6, v6, s73, v223\n"
  "v_mfma_f32_16x16x32_bf16 v[36:39], a[144:147], a[56:59], v[36:39]\n"
  "v_exp_f32 v5, v5\n"
  "v_mfma_f32_16x16x32_bf16 v[36:39], a[148:151], a[60:63], v[36:39]\n"
  "v_exp_f32 v6, v6\n"
  "v_mfma_f32_16x16x32_bf16 v[36:39], a[132:135], a[64:67], v[36:39]\n"
  "v_fma_f32 v20, v20, s73, v224\n"
  "v_fma_f32 v21, v21, s73, v224\n"
  "v_mfma_f32_16x16x32_bf16 v[36:39], a[128:131], a[68:71], v[36:39]\n"
  "v_exp_f32 v20, v20\n"
  "v_mfma_f32_16x16x32_bf16 v[52:55], a[128:131], a[92:95], v[248:251]\n"
  "ds_read_b64_tr_b16 a[128:129], v217 offset:14400\n"
  "v_fma_f32 v22, v22, s73, v224\n"
  "v_mfma_f32_16x16x32_bf16 v[52:55], a[132:135], a[88:91], v[52:55]\n"
  "ds_read_b64_tr_b16 a[130:131], v218 offset:14400\n"
  "v_fma_f32 v23, v23, s73, v224\n"
  "v_mfma_f32_16x16x32_bf16 v[52:55], a[148:151], a[84:87], v[52:55]\n"
  "ds_read_b64_tr_b16 a[132:133], v217 offset:14416\n"
  "v_fma_f32 v36, v36, s73, v225\n"
  "v_mfma_f32_16x16x32_bf16 v[52:55], a[144:147], a[80:83], v[52:55]\n"
  "ds_read_b64_tr_b16 a[134:135], v218 offset:14416\n"
  "v_fma_f32 v37, v37, s73, v225\n"
  "v_mfma_f32_16x16x32_bf16 v[52:55], a[140:143], a[76:79], v[52:55]\n"
  "v_exp_f32 v36, v36\n"
  "v_mfma_f32_16x16x32_bf16 v[52:55], a[136:139], a[72:75], v[52:55]\n"
  "s_waitcnt lgkmcnt(4)\n"
  "v_exp_f32 v37, v37\n"
  "v_mfma_f32_16x16x32_bf16 v[100:103], v[12:15], a[124:127], v[100:103]\n"
  "v_cvt_pk_bf16_f32 v12, v0, v1\n"
  "v_cvt_pk_bf16_f32 v13, v2, v3\n"
  "v_mfma_f32_16x16x32_bf16 v[100:103], v[8:11], a[120:123], v[100:103]\n"
  "v_exp_f32 v21, v21\n"
  "v_mfma_f32_16x16x32_bf16 v[132:135], v[28:31], a[124:127], v[132:135]\n"
  "ds_read_b64_tr_b16 a[140:141], v219 offset:14400\n"
  "v_cvt_pk_bf16_f32 v28, v16, v17\n"
  "v_mfma_f32_16x16x32_bf16 v[132:135], v[24:27], a[120:123], v[132:135]\n"
  "ds_read_b64_tr_b16 a[142:143], v220 offset:14400\n"
  "v_cvt_pk_bf16_f32 v29, v18, v19\n"
  "v_mfma_f32_16x16x32_bf16 v[164:167], v[44:47], a[124:127], v[164:167]\n"
  "ds_read_b64_tr_b16 a[136:137], v219 offset:14416\n"
  "v_cvt_pk_bf16_f32 v44, v32, v33\n"
  "v_mfma_f32_16x16x32_bf16 v[164:167], v[40:43], a[120:123], v[164:167]\n"
  "ds_read_b64_tr_b16 a[138:139], v220 offset:14416\n"
  "v_cvt_pk_bf16_f32 v45, v34, v35\n"
  "v_mfma_f32_16x16x32_bf16 v[196:199], v[60:63], a[124:127], v[196:199]\n"
  "v_exp_f32 v22, v22\n"
  "v_mfma_f32_16x16x32_bf16 v[196:199], v[56:59], a[120:123], v[196:199]\n"
  "s_waitcnt lgkmcnt(4)\n"
  "v_exp_f32 v23, v23\n"
  "v_mfma_f32_16x16x32_bf16 a[244:247], a[156:159], a[128:131], a[244:247]\n"
  "v_cvt_pk_bf16_f32 v60, v48, v49\n"
  "v_cvt_pk_bf16_f32 v61, v50, v51\n"
  "v_mfma_f32_16x16x32_bf16 a[248:251], a[156:159], a[132:135], a[248:251]\n"
  "v_fma_f32 v52, v52, s73, v226\n"
  "v_fma_f32 v53, v53, s73, v226\n"
  "v_mfma_f32_16x16x32_bf16 v[200:203], v[56:59], a[128:131], v[200:203]\n"
  "v_exp_f32 v52, v52\n"
  "v_mfma_f32_16x16x32_bf16 v[204:207], v[56:59], a[132:135], v[204:207]\n"
  "v_exp_f32 v53, v53\n"
  "v_mfma_f32_16x16x32_bf16 v[168:171], v[40:43], a[128:131], v[168:171]\n"
  "v_fma_f32 v54, v54, s73, v226\n"
  "v_fma_f32 v55, v55, s73, v226\n"
  "v_mfma_f32_16x16x32_bf16 v[172:175], v[40:43], a[132:135], v[172:175]\n"
  "s_waitcnt vmcnt(12) lgkmcnt(0)\n"
  "s_barrier\n"
  "ds_read_b128 a[124:127], v216 offset:24704\n"
  "v_mfma_f32_16x16x32_bf16 v[136:139], v[24:27], a[128:131], v[136:139]\n"
  "ds_read_b64 a[160:161], v231 offset:2048\n"
  "v_cvt_pk_bf16_f32 v62, v52, v53\n"
  "v_mfma_f32_16x16x32_bf16 v[140:143], v[24:27], a[132:135], v[140:143]\n"
  "ds_read_b64 a[162:163], v232 offset:2048\n"
  "v_fma_f32 v38, v38, s73, v225\n"
  "v_mfma_f32_16x16x32_bf16 v[104:107], v[8:11], a[128:131], v[104:107]\n"
  "ds_read_b128 a[120:123], v216 offset:24768\n"
  "v_fma_f32 v39, v39, s73, v225\n"
  "v_mfma_f32_16x16x32_bf16 v[108:111], v[8:11], a[132:135], v[108:111]\n"
  "ds_read_b128 a[128:131], v216 offset:25728\n"
  "v_cvt_pk_bf16_f32 v46, v36, v37\n"
  "v_mfma_f32_16x16x32_bf16 v[112:115], v[8:11], a[140:143], v[112:115]\n"
  "ds_read_b128 a[132:135], v216 offset:25792\n"
  "v_cvt_pk_bf16_f32 v30, v20, v21\n"
  "v_mfma_f32_16x16x32_bf16 v[116:119], v[8:11], a[136:139], v[116:119]\n"
  "s_mov_b32 m0, s67\n"
  "ds_read_b128 a[148:151], v216 offset:26752\n"
  "v_cvt_pk_bf16_f32 v31, v22, v23\n"
  "v_mfma_f32_16x16x32_bf16 v[144:147], v[24:27], a[140:143], v[144:147]\n"
  "ds_read_b128 a[144:147], v216 offset:26816\n"
  "v_fma_f32 v7, v7, s73, v223\n"
  "v_mfma_f32_16x16x32_bf16 v[148:151], v[24:27], a[136:139], v[148:151]\n"
  "v_exp_f32 v54, v54\n"
  "v_mfma_f32_16x16x32_bf16 v[176:179], v[40:43], a[140:143], v[176:179]\n"
  "v_exp_f32 v55, v55\n"
  "v_mfma_f32_16x16x32_bf16 v[180:183], v[40:43], a[136:139], v[180:183]\n"
  "buffer_load_dwordx4 v221, s[40:43], 0 offen offset:0 lds\n"
  "v_mfma_f32_16x16x32_bf16 v[208:211], v[56:59], a[140:143], v[208:211]\n"
  "s_addk_i32 m0, 896\n"
  "v_exp_f32 v38, v38\n"
  "v_mfma_f32_16x16x32_bf16 v[212:215], v[56:59], a[136:139], v[212:215]\n"
  "s_waitcnt lgkmcnt(4)\n"
  "v_exp_f32 v39, v39\n"
  "v_mfma_f32_16x16x32_bf16 v[64:67], a[124:127], a[96:99], v[248:251]\n"
  "v_exp_f32 v7, v7\n"
  "v_mfma_f32_16x16x32_bf16 v[64:67], a[120:123], a[100:103], v[64:67]\n"
  "s_waitcnt lgkmcnt(2)\n"
  "buffer_load_dwordx4 v221, s[40:43], 0 offen offset:128 lds\n"
  "v_cvt_pk_bf16_f32 v63, v54, v55\n"
  "v_mfma_f32_16x16x32_bf16 v[64:67], a[128:131], a[104:107], v[64:67]\n"
  "s_addk_i32 m0, 896\n"
  "v_cvt_pk_bf16_f32 v47, v38, v39\n"
  "v_cvt_pk_bf16_f32 v14, v4, v5\n"
  "v_mfma_f32_16x16x32_bf16 v[64:67], a[132:135], a[108:111], v[64:67]\n"
  "s_waitcnt lgkmcnt(0)\n"
  "v_cvt_pk_bf16_f32 v15, v6, v7\n"
  "v_add_f32 v252, v252, v4\n"
  "v_mfma_f32_16x16x32_bf16 v[64:67], a[148:151], a[112:115], v[64:67]\n"
  "v_add_f32 v252, v252, v5\n"
  "v_add_f32 v252, v252, v6\n"
  "v_mfma_f32_16x16x32_bf16 v[64:67], a[144:147], a[116:119], v[64:67]\n"
  "buffer_load_dwordx4 v221, s[40:43], 0 offen offset:256 lds\n"
  "v_add_f32 v252, v252, v7\n"
  "v_mfma_f32_16x16x32_bf16 v[0:3], a[144:147], a[20:23], v[248:251]\n"
  "s_add_u32 s40, s40, s31\n"
  "v_add_f32 v253, v253, v20\n"
  "v_add_f32 v253, v253, v21\n"
  "v_mfma_f32_16x16x32_bf16 v[0:3], a[148:151], a[16:19], v[0:3]\n"
  "s_addc_u32 s41, s41, 0\n"
  "v_add_f32 v253, v253, v22\n"
  "v_add_f32 v253, v253, v23\n"
  "v_mfma_f32_16x16x32_bf16 v[0:3], a[132:135], a[12:15], v[0:3]\n"
  "v_add_f32 v254, v254, v36\n"
  "v_add_f32 v254, v254, v37\n"
  "v_mfma_f32_16x16x32_bf16 v[0:3], a[128:131], a[8:11], v[0:3]\n"
  "s_mov_b32 m0, s59\n"
  "v_add_f32 v254, v254, v38\n"
  "v_add_f32 v254, v254, v39\n"
  "v_mfma_f32_16x16x32_bf16 v[0:3], a[120:123], a[4:7], v[0:3]\n"
  "v_add_f32 v255, v255, v52\n"
  "v_add_f32 v255, v255, v53\n"
  "v_mfma_f32_16x16x32_bf16 v[0:3], a[124:127], a[0:3], v[0:3]\n"
  "v_add_f32 v255, v255, v54\n"
  "v_add_f32 v255, v255, v55\n"
  "v_mfma_f32_16x16x32_bf16 v[16:19], a[124:127], a[24:27], v[248:251]\n"
  "ds_read_b64_tr_b16 v[244:245], v230 offset:11200\n"
  "ds_read_b64_tr_b16 v[246:247], v230 offset:11232\n"
  "v_mfma_f32_16x16x32_bf16 v[16:19], a[120:123], a[28:31], v[16:19]\n"
  "buffer_load_dwordx4 v221, s[32:35], 0 offen offset:0 lds\n"
  "ds_read_b64_tr_b16 a[252:253], v230 offset:12320\n"
  "v_mfma_f32_16x16x32_bf16 v[16:19], a[128:131], a[32:35], v[16:19]\n"
  "s_addk_i32 m0, 1024\n"
  "ds_read_b64_tr_b16 a[140:141], v217 offset:24704\n"
  "ds_read_b64_tr_b16 a[254:255], v230 offset:12352\n"
  "v_mfma_f32_16x16x32_bf16 v[16:19], a[132:135], a[36:39], v[16:19]\n"
  "s_mov_b64 s[32:33], s[44:45]\n"
  "ds_read_b64_tr_b16 a[142:143], v218 offset:24704\n"
  "v_fma_f32 v64, v64, s73, v227\n"
  "v_mfma_f32_16x16x32_bf16 v[16:19], a[148:151], a[40:43], v[16:19]\n"
  "ds_read_b64_tr_b16 a[136:137], v217 offset:25728\n"
  "v_fma_f32 v65, v65, s73, v227\n"
  "v_mfma_f32_16x16x32_bf16 v[16:19], a[144:147], a[44:47], v[16:19]\n"
  "buffer_load_dwordx4 v221, s[44:47], 0 offen offset:0 lds\n"
  "v_fma_f32 v66, v66, s73, v227\n"
  "v_mfma_f32_16x16x32_bf16 v[32:35], a[144:147], a[68:71], v[248:251]\n"
  "s_addk_i32 m0, 768\n"
  "ds_read_b64_tr_b16 a[138:139], v218 offset:25728\n"
  "v_fma_f32 v67, v67, s73, v227\n"
  "v_mfma_f32_16x16x32_bf16 v[32:35], a[148:151], a[64:67], v[32:35]\n"
  "v_exp_f32 v64, v64\n"
  "v_mfma_f32_16x16x32_bf16 v[32:35], a[132:135], a[60:63], v[32:35]\n"
  "v_exp_f32 v65, v65\n"
  "v_mfma_f32_16x16x32_bf16 v[32:35], a[128:131], a[56:59], v[32:35]\n"
  "buffer_load_dwordx4 v221, s[44:47], 0 offen offset:256 lds\n"
  "v_cvt_pk_bf16_f32 v64, v64, v65\n"
  "v_mfma_f32_16x16x32_bf16 v[32:35], a[120:123], a[52:55], v[32:35]\n"
  "s_add_u32 s44, s44, s31\n"
  "v_exp_f32 v66, v66\n"
  "v_mfma_f32_16x16x32_bf16 v[32:35], a[124:127], a[48:51], v[32:35]\n"
  "s_addc_u32 s45, s45, 0\n"
  "v_exp_f32 v67, v67\n"
  "v_mfma_f32_16x16x32_bf16 v[48:51], a[124:127], a[72:75], v[248:251]\n"
  "v_cvt_pk_bf16_f32 v65, v66, v67\n"
  "v_fma_f32 v0, v0, s73, v223\n"
  "v_mfma_f32_16x16x32_bf16 v[48:51], a[120:123], a[76:79], v[48:51]\n"
  "v_exp_f32 v0, v0\n"
  "v_mfma_f32_16x16x32_bf16 v[48:51], a[128:131], a[80:83], v[48:51]\n"
  "ds_read_b64_tr_b16 a[124:125], v217 offset:24720\n"
  "v_fma_f32 v1, v1, s73, v223\n"
  "v_mfma_f32_16x16x32_bf16 v[48:51], a[132:135], a[84:87], v[48:51]\n"
  "ds_read_b64_tr_b16 a[126:127], v218 offset:24720\n"
  "v_fma_f32 v2, v2, s73, v223\n"
  "v_mfma_f32_16x16x32_bf16 v[48:51], a[148:151], a[88:91], v[48:51]\n"
  "ds_read_b64_tr_b16 a[120:121], v217 offset:25744\n"
  "v_fma_f32 v3, v3, s73, v223\n"
  "v_mfma_f32_16x16x32_bf16 v[48:51], a[144:147], a[92:95], v[48:51]\n"
  "s_waitcnt lgkmcnt(3)\n"
  "ds_read_b64_tr_b16 a[122:123], v218 offset:25744\n"
  "v_add_f32 v252, v252, v0\n"
  "v_mfma_f32_16x16x32_bf16 a[228:231], a[156:159], a[140:143], a[228:231]\n"
  "v_exp_f32 v1, v1\n"
  "v_mfma_f32_16x16x32_bf16 a[228:231], a[160:163], a[136:139], a[228:231]\n"
  "v_exp_f32 v2, v2\n"
  "v_mfma_f32_16x16x32_bf16 a[212:215], v[56:59], a[140:143], a[212:215]\n"
  "v_exp_f32 v3, v3\n"
  "v_mfma_f32_16x16x32_bf16 a[212:215], v[60:63], a[136:139], a[212:215]\n"
  "v_add_f32 v252, v252, v1\n"
  "v_add_f32 v252, v252, v2\n"
  "v_mfma_f32_16x16x32_bf16 a[196:199], v[40:43], a[140:143], a[196:199]\n"
  "v_add_f32 v252, v252, v3\n"
  "v_fma_f32 v16, v16, s73, v224\n"
  "v_mfma_f32_16x16x32_bf16 a[196:199], v[44:47], a[136:139], a[196:199]\n"
  "v_exp_f32 v16, v16\n"
  "v_mfma_f32_16x16x32_bf16 a[180:183], v[24:27], a[140:143], a[180:183]\n"
  "ds_read_b64_tr_b16 a[128:129], v219 offset:24704\n"
  "v_fma_f32 v17, v17, s73, v224\n"
  "v_mfma_f32_16x16x32_bf16 a[180:183], v[28:31], a[136:139], a[180:183]\n"
  "ds_read_b64_tr_b16 a[130:131], v220 offset:24704\n"
  "v_fma_f32 v18, v18, s73, v224\n"
  "v_mfma_f32_16x16x32_bf16 a[164:167], v[8:11], a[140:143], a[164:167]\n"
  "ds_read_b64_tr_b16 a[132:133], v219 offset:25728\n"
  "v_fma_f32 v19, v19, s73, v224\n"
  "v_mfma_f32_16x16x32_bf16 a[164:167], v[12:15], a[136:139], a[164:167]\n"
  "ds_read_b64_tr_b16 a[134:135], v220 offset:25728\n"
  "v_add_f32 v253, v253, v16\n"
  "v_mfma_f32_16x16x32_bf16 v[84:87], a[156:159], a[152:155], v[84:87]\n"
  "v_exp_f32 v17, v17\n"
  "v_mfma_f32_16x16x32_bf16 v[84:87], a[160:163], a[152:155], v[84:87]\n"
  "s_waitcnt lgkmcnt(4)\n"
  "v_exp_f32 v18, v18\n"
  "v_mfma_f32_16x16x32_bf16 a[168:171], v[8:11], a[124:127], a[168:171]\n"
  "v_exp_f32 v19, v19\n"
  "v_mfma_f32_16x16x32_bf16 a[168:171], v[12:15], a[120:123], a[168:171]\n"
  "v_add_f32 v253, v253, v17\n"
  "v_add_f32 v253, v253, v18\n"
  "v_mfma_f32_16x16x32_bf16 a[184:187], v[24:27], a[124:127], a[184:187]\n"
  "ds_read_b128 a[140:143], v235 offset:24704\n"
  "v_add_f32 v253, v253, v19\n"
  "v_mfma_f32_16x16x32_bf16 a[184:187], v[28:31], a[120:123], a[184:187]\n"
  "ds_read_b128 a[136:139], v235 offset:24768\n"
  "v_fma_f32 v32, v32, s73, v225\n"
  "v_mfma_f32_16x16x32_bf16 a[200:203], v[40:43], a[124:127], a[200:203]\n"
  "ds_read_b128 a[148:151], v235 offset:25728\n"
  "v_fma_f32 v33, v33, s73, v225\n"
  "v_mfma_f32_16x16x32_bf16 a[200:203], v[44:47], a[120:123], a[200:203]\n"
  "ds_read_b128 a[144:147], v235 offset:25792\n"
  "v_fma_f32 v34, v34, s73, v225\n"
  "v_mfma_f32_16x16x32_bf16 a[216:219], v[56:59], a[124:127], a[216:219]\n"
  "v_exp_f32 v32, v32\n"
  "v_mfma_f32_16x16x32_bf16 a[216:219], v[60:63], a[120:123], a[216:219]\n"
  "v_exp_f32 v33, v33\n"
  "v_mfma_f32_16x16x32_bf16 a[232:235], a[156:159], a[124:127], a[232:235]\n"
  "ds_read_b128 a[124:127], v235 offset:26752\n"
  "v_fma_f32 v35, v35, s73, v225\n"
  "v_mfma_f32_16x16x32_bf16 a[232:235], a[160:163], a[120:123], a[232:235]\n"
  "s_waitcnt lgkmcnt(5)\n"
  "ds_read_b128 a[120:123], v235 offset:26816\n"
  "v_add_f32 v254, v254, v32\n"
  "v_mfma_f32_16x16x32_bf16 a[220:223], v[56:59], a[128:131], a[220:223]\n"
  "v_exp_f32_e64 v34, v34\n"
  "v_mfma_f32_16x16x32_bf16 a[220:223], v[60:63], a[132:135], a[220:223]\n"
  "ds_write_b64 v231, v[64:65] offset:0\n"
  "v_mfma_f32_16x16x32_bf16 a[204:207], v[40:43], a[128:131], a[204:207]\n"
  "v_exp_f32 v35, v35\n"
  "v_mfma_f32_16x16x32_bf16 a[204:207], v[44:47], a[132:135], a[204:207]\n"
  "buffer_store_dwordx4 v[244:247], v228, s[52:55], s81 offen offset:128 nt\n"
  "v_add_f32 v254, v254, v33\n"
  "v_add_f32 v254, v254, v34\n"
  "v_add_f32 v254, v254, v35\n"
  "v_mfma_f32_16x16x32_bf16 a[188:191], v[24:27], a[128:131], a[188:191]\n"
  "ds_read_b64_tr_b16 v[244:245], v230 offset:13440\n"
  "ds_read_b64_tr_b16 v[246:247], v230 offset:13472\n"
  "v_mfma_f32_16x16x32_bf16 a[188:191], v[28:31], a[132:135], a[188:191]\n"
  "v_fma_f32 v48, v48, s73, v226\n"
  "v_fma_f32 v49, v49, s73, v226\n"
  "v_mfma_f32_16x16x32_bf16 a[172:175], v[8:11], a[128:131], a[172:175]\n"
  "v_exp_f32 v48, v48\n"
  "v_mfma_f32_16x16x32_bf16 a[172:175], v[12:15], a[132:135], a[172:175]\n"
  "s_waitcnt lgkmcnt(7)\n"
  "v_exp_f32 v49, v49\n"
  "v_mfma_f32_16x16x32_bf16 v[4:7], a[140:143], a[0:3], v[248:251]\n"
  "v_fma_f32 v50, v50, s73, v226\n"
  "v_fma_f32 v51, v51, s73, v226\n"
  "v_mfma_f32_16x16x32_bf16 v[4:7], a[136:139], a[4:7], v[4:7]\n"
  "s_waitcnt lgkmcnt(5)\n"
  "v_exp_f32 v50, v50\n"
  "v_mfma_f32_16x16x32_bf16 v[4:7], a[148:151], a[8:11], v[4:7]\n"
  "v_exp_f32 v51, v51\n"
  "v_mfma_f32_16x16x32_bf16 v[4:7], a[144:147], a[12:15], v[4:7]\n"
  "s_waitcnt lgkmcnt(3)\n"
  "v_add_f32 v255, v255, v48\n"
  "v_add_f32 v255, v255, v49\n"
  "v_mfma_f32_16x16x32_bf16 v[4:7], a[124:127], a[16:19], v[4:7]\n"
  "v_add_f32 v255, v255, v50\n"
  "v_add_f32_e64 v255, v255, v51\n"
  "v_mfma_f32_16x16x32_bf16 v[4:7], a[120:123], a[20:23], v[4:7]\n"
  "buffer_store_dwordx4 a[252:255], v228, s[52:55], s81 offen offset:3200 nt\n"
  "v_and_b32 v236, 0x7fff7fff, v236\n"
  "v_lshlrev_b32 v237, 16, v236\n"
  "v_max_u32 v236, v236, v237\n"
  "v_mfma_f32_16x16x32_bf16 v[20:23], a[120:123], a[44:47], v[248:251]\n"
  "ds_read_b64_tr_b16 a[252:253], v230 offset:14560\n"
  "ds_read_b64_tr_b16 a[254:255], v230 offset:14592\n"
  "v_mfma_f32_16x16x32_bf16 v[20:23], a[124:127], a[40:43], v[20:23]\n"
  "v_max_u32_dpp v236, v236, v236 quad_perm:[1,0,3,2] row_mask:0xf bank_mask:0xf\n"
  "v_mfma_f32_16x16x32_bf16 v[20:23], a[144:147], a[36:39], v[20:23]\n"
  "ds_read_b64_tr_b16 a[128:129], v219 offset:24720\n"
  "v_fma_f32 v4, v4, s73, v223\n"
  "v_mfma_f32_16x16x32_bf16 v[20:23], a[148:151], a[32:35], v[20:23]\n"
  "ds_read_b64_tr_b16 a[130:131], v220 offset:24720\n"
  "v_fma_f32 v5, v5, s73, v223\n"
  "v_mfma_f32_16x16x32_bf16 v[20:23], a[136:139], a[28:31], v[20:23]\n"
  "ds_read_b64_tr_b16 a[132:133], v219 offset:25744\n"
  "v_fma_f32 v6, v6, s73, v223\n"
  "v_mfma_f32_16x16x32_bf16 v[20:23], a[140:143], a[24:27], v[20:23]\n"
  "s_waitcnt lgkmcnt(5)\n"
  "buffer_store_dwordx4 v[244:247], v228, s[52:55], s82 offen offset:128 nt\n"
  "ds_read_b64_tr_b16 a[134:135], v220 offset:25744\n"
  "v_exp_f32 v4, v4\n"
  "v_mfma_f32_16x16x32_bf16 v[36:39], a[140:143], a[48:51], v[248:251]\n"
  "v_exp_f32 v5, v5\n"
  "v_mfma_f32_16x16x32_bf16 v[36:39], a[136:139], a[52:55], v[36:39]\n"
  "v_exp_f32 v6, v6\n"
  "v_mfma_f32_16x16x32_bf16 v[36:39], a[148:151], a[56:59], v[36:39]\n"
  "v_fma_f32 v20, v20, s73, v224\n"
  "v_fma_f32 v21, v21, s73, v224\n"
  "v_mfma_f32_16x16x32_bf16 v[36:39], a[144:147], a[60:63], v[36:39]\n"
  "v_exp_f32 v20, v20\n"
  "v_mfma_f32_16x16x32_bf16 v[36:39], a[124:127], a[64:67], v[36:39]\n"
  "v_exp_f32 v21, v21\n"
  "v_mfma_f32_16x16x32_bf16 v[36:39], a[120:123], a[68:71], v[36:39]\n"
  "v_fma_f32 v22, v22, s73, v224\n"
  "v_fma_f32 v23, v23, s73, v224\n"
  "v_mfma_f32_16x16x32_bf16 v[52:55], a[120:123], a[92:95], v[248:251]\n"
  "ds_read_b64_tr_b16 a[120:121], v217 offset:26752\n"
  "v_fma_f32 v7, v7, s73, v223\n"
  "v_mfma_f32_16x16x32_bf16 v[52:55], a[124:127], a[88:91], v[52:55]\n"
  "ds_read_b64_tr_b16 a[122:123], v218 offset:26752\n"
  "v_add_f32 v252, v252, v4\n"
  "v_mfma_f32_16x16x32_bf16 v[52:55], a[144:147], a[84:87], v[52:55]\n"
  "ds_read_b64_tr_b16 a[124:125], v217 offset:26768\n"
  "v_fma_f32 v36, v36, s73, v225\n"
  "v_mfma_f32_16x16x32_bf16 v[52:55], a[148:151], a[80:83], v[52:55]\n"
  "ds_read_b64_tr_b16 a[126:127], v218 offset:26768\n"
  "v_fma_f32 v37, v37, s73, v225\n"
  "v_mfma_f32_16x16x32_bf16 v[52:55], a[136:139], a[76:79], v[52:55]\n"
  "v_exp_f32 v36, v36\n"
  "v_mfma_f32_16x16x32_bf16 v[52:55], a[140:143], a[72:75], v[52:55]\n"
  "s_waitcnt lgkmcnt(4)\n"
  "v_exp_f32 v37, v37\n"
  "v_mfma_f32_16x16x32_bf16 a[176:179], v[8:11], a[128:131], a[176:179]\n"
  "v_cvt_pk_bf16_f32 v8, v0, v1\n"
  "v_cvt_pk_bf16_f32 v9, v2, v3\n"
  "v_mfma_f32_16x16x32_bf16 a[176:179], v[12:15], a[132:135], a[176:179]\n"
  "v_exp_f32 v22, v22\n"
  "v_mfma_f32_16x16x32_bf16 a[192:195], v[24:27], a[128:131], a[192:195]\n"
  "ds_read_b64_tr_b16 a[136:137], v219 offset:26752\n"
  "v_cvt_pk_bf16_f32 v24, v16, v17\n"
  "v_mfma_f32_16x16x32_bf16 a[192:195], v[28:31], a[132:135], a[192:195]\n"
  "ds_read_b64_tr_b16 a[138:139], v220 offset:26752\n"
  "v_cvt_pk_bf16_f32 v25, v18, v19\n"
  "v_mfma_f32_16x16x32_bf16 a[208:211], v[40:43], a[128:131], a[208:211]\n"
  "ds_read_b64_tr_b16 a[140:141], v219 offset:26768\n"
  "v_cvt_pk_bf16_f32 v40, v32, v33\n"
  "v_mfma_f32_16x16x32_bf16 a[208:211], v[44:47], a[132:135], a[208:211]\n"
  "ds_read_b64_tr_b16 a[142:143], v220 offset:26768\n"
  "v_cvt_pk_bf16_f32 v41, v34, v35\n"
  "v_mfma_f32_16x16x32_bf16 a[224:227], v[56:59], a[128:131], a[224:227]\n"
  "v_exp_f32 v23, v23\n"
  "v_mfma_f32_16x16x32_bf16 a[224:227], v[60:63], a[132:135], a[224:227]\n"
  "s_waitcnt lgkmcnt(4)\n"
  "v_exp_f32 v7, v7\n"
  "v_mfma_f32_16x16x32_bf16 a[244:247], a[160:163], a[120:123], a[244:247]\n"
  "v_cvt_pk_bf16_f32 v56, v48, v49\n"
  "v_cvt_pk_bf16_f32 v57, v50, v51\n"
  "v_mfma_f32_16x16x32_bf16 a[248:251], a[160:163], a[124:127], a[248:251]\n"
  "v_fma_f32 v52, v52, s73, v226\n"
  "v_fma_f32 v53, v53, s73, v226\n"
  "v_mfma_f32_16x16x32_bf16 v[200:203], v[60:63], a[120:123], v[200:203]\n"
  "v_exp_f32 v52, v52\n"
  "v_mfma_f32_16x16x32_bf16 v[204:207], v[60:63], a[124:127], v[204:207]\n"
  "v_exp_f32 v53, v53\n"
  "v_mfma_f32_16x16x32_bf16 v[168:171], v[44:47], a[120:123], v[168:171]\n"
  "v_fma_f32 v54, v54, s73, v226\n"
  "v_fma_f32 v55, v55, s73, v226\n"
  "v_mfma_f32_16x16x32_bf16 v[172:175], v[44:47], a[124:127], v[172:175]\n"
  "s_waitcnt vmcnt(12) lgkmcnt(0)\n"
  "s_barrier\n"
  "ds_read_b128 a[128:131], v216 offset:0\n"
  "v_mfma_f32_16x16x32_bf16 v[136:139], v[28:31], a[120:123], v[136:139]\n"
  "ds_read_b64 a[156:157], v231 offset:0\n"
  "v_cvt_pk_bf16_f32 v58, v52, v53\n"
  "v_mfma_f32_16x16x32_bf16 v[140:143], v[28:31], a[124:127], v[140:143]\n"
  "ds_read_b64 a[158:159], v232 offset:0\n"
  "v_fma_f32 v38, v38, s73, v225\n"
  "v_mfma_f32_16x16x32_bf16 v[104:107], v[12:15], a[120:123], v[104:107]\n"
  "ds_read_b128 a[132:135], v216 offset:64\n"
  "v_fma_f32 v39, v39, s73, v225\n"
  "v_mfma_f32_16x16x32_bf16 v[108:111], v[12:15], a[124:127], v[108:111]\n"
  "ds_read_b128 a[120:123], v216 offset:1024\n"
  "v_cvt_pk_bf16_f32 v42, v36, v37\n"
  "v_mfma_f32_16x16x32_bf16 v[112:115], v[12:15], a[136:139], v[112:115]\n"
  "ds_read_b128 a[124:127], v216 offset:1088\n"
  "v_cvt_pk_bf16_f32 v26, v20, v21\n"
  "v_mfma_f32_16x16x32_bf16 v[116:119], v[12:15], a[140:143], v[116:119]\n"
  "s_mov_b32 m0, s68\n"
  "ds_read_b128 a[144:147], v216 offset:2048\n"
  "v_cvt_pk_bf16_f32 v27, v22, v23\n"
  "v_mfma_f32_16x16x32_bf16 v[144:147], v[28:31], a[136:139], v[144:147]\n"
  "ds_read_b128 a[148:151], v216 offset:2112\n"
  "v_cvt_pk_bf16_f32 v10, v4, v5\n"
  "v_mfma_f32_16x16x32_bf16 v[148:151], v[28:31], a[140:143], v[148:151]\n"
  "v_exp_f32 v54, v54\n"
  "v_mfma_f32_16x16x32_bf16 v[176:179], v[44:47], a[136:139], v[176:179]\n"
  "v_exp_f32 v55, v55\n"
  "v_mfma_f32_16x16x32_bf16 v[180:183], v[44:47], a[140:143], v[180:183]\n"
  "buffer_load_dwordx4 v221, s[40:43], 0 offen offset:0 lds\n"
  "v_mfma_f32_16x16x32_bf16 v[208:211], v[60:63], a[136:139], v[208:211]\n"
  "s_addk_i32 m0, 896\n"
  "v_exp_f32 v38, v38\n"
  "v_mfma_f32_16x16x32_bf16 v[212:215], v[60:63], a[140:143], v[212:215]\n"
  "s_waitcnt lgkmcnt(4)\n"
  "v_exp_f32 v39, v39\n"
  "v_mfma_f32_16x16x32_bf16 v[64:67], a[128:131], a[96:99], v[248:251]\n"
  "v_cvt_pk_bf16_f32 v59, v54, v55\n"
  "v_cvt_pk_bf16_f32 v43, v38, v39\n"
  "v_mfma_f32_16x16x32_bf16 v[64:67], a[132:135], a[100:103], v[64:67]\n"
  "s_waitcnt lgkmcnt(2)\n"
  "buffer_load_dwordx4 v221, s[40:43], 0 offen offset:128 lds\n"
  "v_cvt_pk_bf16_f32 v11, v6, v7\n"
  "v_mfma_f32_16x16x32_bf16 v[64:67], a[120:123], a[104:107], v[64:67]\n"
  "s_addk_i32 m0, 896\n"
  "v_add_f32 v252, v252, v5\n"
  "v_add_f32 v252, v252, v6\n"
  "v_mfma_f32_16x16x32_bf16 v[64:67], a[124:127], a[108:111], v[64:67]\n"
  "s_waitcnt lgkmcnt(0)\n"
  "v_add_f32 v252, v252, v7\n"
  "v_add_f32 v253, v253, v20\n"
  "v_mfma_f32_16x16x32_bf16 v[64:67], a[144:147], a[112:115], v[64:67]\n"
  "v_add_f32 v253, v253, v21\n"
  "v_add_f32 v253, v253, v22\n"
  "v_mfma_f32_16x16x32_bf16 v[64:67], a[148:151], a[116:119], v[64:67]\n"
  "buffer_load_dwordx4 v221, s[40:43], 0 offen offset:256 lds\n"
  "v_add_f32 v253, v253, v23\n"
  "v_mfma_f32_16x16x32_bf16 v[0:3], a[148:151], a[20:23], v[248:251]\n"
  "s_add_u32 s40, s40, s31\n"
  "v_add_f32 v254, v254, v36\n"
  "v_add_f32 v254, v254, v37\n"
  "v_mfma_f32_16x16x32_bf16 v[0:3], a[144:147], a[16:19], v[0:3]\n"
  "s_addc_u32 s41, s41, 0\n"
  "v_add_f32 v254, v254, v38\n"
  "v_add_f32 v254, v254, v39\n"
  "v_mfma_f32_16x16x32_bf16 v[0:3], a[124:127], a[12:15], v[0:3]\n"
  "v_add_f32 v255, v255, v52\n"
  "v_add_f32 v255, v255, v53\n"
  "v_mfma_f32_16x16x32_bf16 v[0:3], a[120:123], a[8:11], v[0:3]\n"
  "s_add_u32 m0, s60, -128\n"
  "v_add_f32 v255, v255, v54\n"
  "v_add_f32 v255, v255, v55\n"
  "v_mfma_f32_16x16x32_bf16 v[0:3], a[132:135], a[4:7], v[0:3]\n"
  "ds_read_b64_tr_b16 v[244:245], v230 offset:15680\n"
  "ds_read_b64_tr_b16 v[246:247], v230 offset:15712\n"
  "v_mfma_f32_16x16x32_bf16 v[0:3], a[128:131], a[0:3], v[0:3]\n"
  "v_fma_f32 v64, v64, s73, v227\n"
  "v_fma_f32 v65, v65, s73, v227\n"
  "v_mfma_f32_16x16x32_bf16 v[16:19], a[128:131], a[24:27], v[248:251]\n"
  "v_exp_f32 v64, v64\n"
  "v_mfma_f32_16x16x32_bf16 v[16:19], a[132:135], a[28:31], v[16:19]\n"
  "buffer_load_dwordx4 v221, s[32:35], 0 offen offset:128 lds\n"
  "v_fma_f32 v66, v66, s73, v227\n"
  "v_mfma_f32_16x16x32_bf16 v[16:19], a[120:123], a[32:35], v[16:19]\n"
  "s_addk_i32 m0, 1024\n"
  "ds_read_b64_tr_b16 a[136:137], v217 offset:0\n"
  "v_fma_f32 v67, v67, s73, v227\n"
  "v_mfma_f32_16x16x32_bf16 v[16:19], a[124:127], a[36:39], v[16:19]\n"
  "s_mov_b64 s[32:33], s[44:45]\n"
  "ds_read_b64_tr_b16 a[138:139], v218 offset:0\n"
  "v_fma_f32 v0, v0, s73, v223\n"
  "v_mfma_f32_16x16x32_bf16 v[16:19], a[144:147], a[40:43], v[16:19]\n"
  "ds_read_b64_tr_b16 a[140:141], v217 offset:1024\n"
  "v_fma_f32 v1, v1, s73, v223\n"
  "v_mfma_f32_16x16x32_bf16 v[16:19], a[148:151], a[44:47], v[16:19]\n"
  "buffer_load_dwordx4 v221, s[44:47], 0 offen offset:128 lds\n"
  "v_fma_f32 v2, v2, s73, v223\n"
  "v_mfma_f32_16x16x32_bf16 v[32:35], a[148:151], a[68:71], v[248:251]\n"
  "s_addk_i32 m0, 896\n"
  "ds_read_b64_tr_b16 a[142:143], v218 offset:1024\n"
  "v_fma_f32 v3, v3, s73, v223\n"
  "v_mfma_f32_16x16x32_bf16 v[32:35], a[144:147], a[64:67], v[32:35]\n"
  "v_exp_f32 v65, v65\n"
  "v_mfma_f32_16x16x32_bf16 v[32:35], a[124:127], a[60:63], v[32:35]\n"
  "v_exp_f32 v66, v66\n"
  "v_mfma_f32_16x16x32_bf16 v[32:35], a[120:123], a[56:59], v[32:35]\n"
  "buffer_load_dwordx4 v221, s[44:47], 0 offen offset:256 lds\n"
  "v_cvt_pk_bf16_f32 v64, v64, v65\n"
  "v_mfma_f32_16x16x32_bf16 v[32:35], a[132:135], a[52:55], v[32:35]\n"
  "s_add_u32 s44, s44, s31\n"
  "v_exp_f32 v67, v67\n"
  "v_mfma_f32_16x16x32_bf16 v[32:35], a[128:131], a[48:51], v[32:35]\n"
  "s_addc_u32 s45, s45, 0\n"
  "v_exp_f32 v0, v0\n"
  "v_mfma_f32_16x16x32_bf16 v[48:51], a[128:131], a[72:75], v[248:251]\n"
  "ds_read_b64_tr_b16 a[128:129], v217 offset:16\n"
  "v_cvt_pk_bf16_f32 v65, v66, v67\n"
  "v_mfma_f32_16x16x32_bf16 v[48:51], a[132:135], a[76:79], v[48:51]\n"
  "ds_read_b64_tr_b16 a[130:131], v218 offset:16\n"
  "v_add_f32 v252, v252, v0\n"
  "v_mfma_f32_16x16x32_bf16 v[48:51], a[120:123], a[80:83], v[48:51]\n"
  "ds_read_b64_tr_b16 a[132:133], v217 offset:1040\n"
  "v_fma_f32 v16, v16, s73, v224\n"
  "v_mfma_f32_16x16x32_bf16 v[48:51], a[124:127], a[84:87], v[48:51]\n"
  "s_add_u32 m0, s72, 11200\n"
  "ds_read_b64_tr_b16 a[134:135], v218 offset:1040\n"
  "v_mfma_f32_16x16x32_bf16 v[48:51], a[144:147], a[88:91], v[48:51]\n"
  "v_exp_f32 v1, v1\n"
  "v_mfma_f32_16x16x32_bf16 v[48:51], a[148:151], a[92:95], v[48:51]\n"
  "s_waitcnt lgkmcnt(4)\n"
  "v_exp_f32 v2, v2\n"
  "v_mfma_f32_16x16x32_bf16 a[236:239], a[160:163], a[136:139], a[236:239]\n"
  "v_exp_f32 v3, v3\n"
  "v_mfma_f32_16x16x32_bf16 a[236:239], a[156:159], a[140:143], a[236:239]\n"
  "buffer_load_dwordx4 v221, s[48:51], 0 offen offset:0 nt lds\n"
  "v_add_f32 v252, v252, v1\n"
  "v_mfma_f32_16x16x32_bf16 v[184:187], v[60:63], a[136:139], v[184:187]\n"
  "v_exp_f32 v16, v16\n"
  "v_mfma_f32_16x16x32_bf16 v[184:187], v[56:59], a[140:143], v[184:187]\n"
  "v_add_f32 v252, v252, v2\n"
  "v_add_f32 v252, v252, v3\n"
  "v_mfma_f32_16x16x32_bf16 v[152:155], v[44:47], a[136:139], v[152:155]\n"
  "ds_read_b64_tr_b16 a[120:121], v219 offset:0\n"
  "v_fma_f32 v17, v17, s73, v224\n"
  "v_mfma_f32_16x16x32_bf16 v[152:155], v[40:43], a[140:143], v[152:155]\n"
  "s_add_u32 m0, s72, 9216\n"
  "ds_read_b64_tr_b16 a[122:123], v220 offset:0\n"
  "v_add_f32 v253, v253, v16\n"
  "v_mfma_f32_16x16x32_bf16 v[120:123], v[28:31], a[136:139], v[120:123]\n"
  "ds_read_b64_tr_b16 a[124:125], v219 offset:1024\n"
  "v_fma_f32 v18, v18, s73, v224\n"
  "v_mfma_f32_16x16x32_bf16 v[120:123], v[24:27], a[140:143], v[120:123]\n"
  "ds_read_b64_tr_b16 a[126:127], v220 offset:1024\n"
  "v_fma_f32 v19, v19, s73, v224\n"
  "v_mfma_f32_16x16x32_bf16 v[88:91], v[12:15], a[136:139], v[88:91]\n"
  "v_exp_f32 v17, v17\n"
  "v_mfma_f32_16x16x32_bf16 v[88:91], v[8:11], a[140:143], v[88:91]\n"
  "s_waitcnt lgkmcnt(4)\n"
  "buffer_load_dwordx4 v221, s[48:51], 0 offen offset:3072 nt lds\n"
  "v_add_f32 v253, v253, v17\n"
  "v_mfma_f32_16x16x32_bf16 v[92:95], v[12:15], a[128:131], v[92:95]\n"
  "v_exp_f32 v18, v18\n"
  "v_mfma_f32_16x16x32_bf16 v[92:95], v[8:11], a[132:135], v[92:95]\n"
  "v_exp_f32 v19, v19\n"
  "v_mfma_f32_16x16x32_bf16 v[124:127], v[28:31], a[128:131], v[124:127]\n"
  "ds_read_b128 a[136:139], v235 offset:0\n"
  "v_add_f32 v253, v253, v18\n"
  "v_mfma_f32_16x16x32_bf16 v[124:127], v[24:27], a[132:135], v[124:127]\n"
  "ds_read_b128 a[140:143], v235 offset:64\n"
  "v_add_f32 v253, v253, v19\n"
  "v_mfma_f32_16x16x32_bf16 v[156:159], v[44:47], a[128:131], v[156:159]\n"
  "ds_read_b128 a[144:147], v235 offset:1024\n"
  "v_fma_f32 v32, v32, s73, v225\n"
  "v_mfma_f32_16x16x32_bf16 v[156:159], v[40:43], a[132:135], v[156:159]\n"
  "ds_read_b128 a[148:151], v235 offset:1088\n"
  "v_fma_f32 v33, v33, s73, v225\n"
  "v_mfma_f32_16x16x32_bf16 v[188:191], v[60:63], a[128:131], v[188:191]\n"
  "v_exp_f32 v32, v32\n"
  "v_mfma_f32_16x16x32_bf16 v[188:191], v[56:59], a[132:135], v[188:191]\n"
  "v_exp_f32 v33, v33\n"
  "v_mfma_f32_16x16x32_bf16 a[240:243], a[160:163], a[128:131], a[240:243]\n"
  "ds_read_b128 a[128:131], v235 offset:2048\n"
  "v_fma_f32 v34, v34, s73, v225\n"
  "v_mfma_f32_16x16x32_bf16 a[240:243], a[156:159], a[132:135], a[240:243]\n"
  "s_waitcnt lgkmcnt(5)\n"
  "ds_read_b128 a[132:135], v235 offset:2112\n"
  "v_fma_f32 v35, v35, s73, v225\n"
  "v_mfma_f32_16x16x32_bf16 v[192:195], v[60:63], a[120:123], v[192:195]\n"
  "v_exp_f32 v34, v34\n"
  "v_mfma_f32_16x16x32_bf16 v[192:195], v[56:59], a[124:127], v[192:195]\n"
  "ds_write_b64 v231, v[64:65] offset:2048\n"
  "v_mfma_f32_16x16x32_bf16 v[160:163], v[44:47], a[120:123], v[160:163]\n"
  "v_exp_f32 v35, v35\n"
  "v_mfma_f32_16x16x32_bf16 v[160:163], v[40:43], a[124:127], v[160:163]\n"
  "v_add_f32 v254, v254, v32\n"
  "v_add_f32 v254, v254, v33\n"
  "v_mfma_f32_16x16x32_bf16 v[128:131], v[28:31], a[120:123], v[128:131]\n"
  "v_add_f32 v254, v254, v34\n"
  "v_add_f32_e64 v254, v254, v35\n"
  "v_mfma_f32_16x16x32_bf16 v[128:131], v[24:27], a[124:127], v[128:131]\n"
  "buffer_store_dwordx4 a[252:255], v228, s[52:55], s82 offen offset:3200 nt\n"
  "v_max_u32_dpp v236, v236, v236 quad_perm:[2,3,0,1] row_mask:0xf bank_mask:0xf\n"
  "v_add_u32_e64 v236, -1, v236\n"
  "v_fma_f32 v48, v48, s73, v226\n"
  "v_mfma_f32_16x16x32_bf16 v[96:99], v[12:15], a[120:123], v[96:99]\n"
  "ds_read_b64_tr_b16 a[252:253], v230 offset:16800\n"
  "ds_read_b64_tr_b16 a[254:255], v230 offset:16832\n"
  "v_mfma_f32_16x16x32_bf16 v[96:99], v[8:11], a[124:127], v[96:99]\n"
  "s_waitcnt lgkmcnt(7)\n"
  "v_exp_f32 v48, v48\n"
  "v_mfma_f32_16x16x32_bf16 v[4:7], a[136:139], a[0:3], v[248:251]\n"
  "v_min_u32_dpp v236, v236, v236 row_shr:4 row_mask:0xf bank_mask:0xf\n"
  "v_fma_f32 v49, v49, s73, v226\n"
  "v_mfma_f32_16x16x32_bf16 v[4:7], a[140:143], a[4:7], v[4:7]\n"
  "s_waitcnt lgkmcnt(5)\n"
  "v_exp_f32 v49, v49\n"
  "v_mfma_f32_16x16x32_bf16 v[4:7], a[144:147], a[8:11], v[4:7]\n"
  "v_min_u32_dpp v236, v236, v236 row_shr:8 row_mask:0xf bank_mask:0xf\n"
  "v_fma_f32 v50, v50, s73, v226\n"
  "v_mfma_f32_16x16x32_bf16 v[4:7], a[148:151], a[12:15], v[4:7]\n"
  "s_waitcnt lgkmcnt(3)\n"
  "v_exp_f32 v50, v50\n"
  "v_mfma_f32_16x16x32_bf16 v[4:7], a[128:131], a[16:19], v[4:7]\n"
  "v_min_u32_dpp v236, v236, v236 row_bcast:15 row_mask:0xa bank_mask:0xf\n"
  "v_fma_f32 v51, v51, s73, v226\n"
  "v_mfma_f32_16x16x32_bf16 v[4:7], a[132:135], a[20:23], v[4:7]\n"
  "buffer_store_dwordx4 v[244:247], v228, s[52:55], s83 offen offset:128 nt\n"
  "v_min_u32_dpp v236, v236, v236 row_bcast:31 row_mask:0xc bank_mask:0xf\n"
  "v_exp_f32 v51, v51\n"
  "v_mfma_f32_16x16x32_bf16 v[20:23], a[132:135], a[44:47], v[248:251]\n"
  "v_readlane_b32 s65, v236, 63\n"
  "v_mfma_f32_16x16x32_bf16 v[20:23], a[128:131], a[40:43], v[20:23]\n"
  "v_add_f32 v255, v255, v48\n"
  "v_add_f32 v255, v255, v49\n"
  "v_mfma_f32_16x16x32_bf16 v[20:23], a[148:151], a[36:39], v[20:23]\n"
  "s_add_u32 s65, s65, 1\n"
  "ds_read_b64_tr_b16 a[120:121], v219 offset:16\n"
  "v_add_f32 v255, v255, v50\n"
  "v_mfma_f32_16x16x32_bf16 v[20:23], a[144:147], a[32:35], v[20:23]\n"
  "s_lshr_b32 s65, s65, 23\n"
  "ds_read_b64_tr_b16 a[122:123], v220 offset:16\n"
  "v_add_f32_e64 v255, v255, v51\n"
  "v_mfma_f32_16x16x32_bf16 v[20:23], a[140:143], a[28:31], v[20:23]\n"
  "s_sub_i32 s65, 0x77, s65\n"
  "ds_read_b64_tr_b16 a[124:125], v219 offset:1040\n"
  "v_fma_f32 v4, v4, s73, v223\n"
  "v_mfma_f32_16x16x32_bf16 v[20:23], a[136:139], a[24:27], v[20:23]\n"
  "s_and_b32 s65, s65, -4\n"
  "s_waitcnt lgkmcnt(3)\n"
  "buffer_store_dwordx4 a[252:255], v228, s[52:55], s83 offen offset:3200 nt\n"
  "ds_read_b64_tr_b16 a[126:127], v220 offset:1040\n"
  "v_exp_f32 v4, v4\n"
  "v_mfma_f32_16x16x32_bf16 v[36:39], a[136:139], a[48:51], v[248:251]\n"
  "s_max_i32 s65, s65, 0xffffffc4\n"
  "v_cvt_f32_i32 v236, s65\n"
  "v_add_f32 v252, v252, v4\n"
  "v_mfma_f32_16x16x32_bf16 v[36:39], a[140:143], a[52:55], v[36:39]\n"
  "s_add_u32 s65, s65, 0xab\n"
  "v_readfirstlane_b32 s64, v236\n"
  "v_mfma_f32_16x16x32_bf16 v[36:39], a[144:147], a[56:59], v[36:39]\n"
  "v_fma_f32 v20, v20, s73, v224\n"
  "v_fma_f32 v21, v21, s73, v224\n"
  "v_mfma_f32_16x16x32_bf16 v[36:39], a[148:151], a[60:63], v[36:39]\n"
  "s_cmpk_gt_u32 s65, 0xd3\n"
  "s_cselect_b32 s65, 0, s65\n"
  "v_exp_f32 v20, v20\n"
  "v_mfma_f32_16x16x32_bf16 v[36:39], a[128:131], a[64:67], v[36:39]\n"
  "s_lshl_b32 s65, s65, 23\n"
  "v_exp_f32 v21, v21\n"
  "v_mfma_f32_16x16x32_bf16 v[36:39], a[132:135], a[68:71], v[36:39]\n"
  "v_fma_f32 v22, v22, s73, v224\n"
  "v_fma_f32 v23, v23, s73, v224\n"
  "v_mfma_f32_16x16x32_bf16 v[52:55], a[132:135], a[92:95], v[248:251]\n"
  "ds_read_b64_tr_b16 a[132:133], v217 offset:2048\n"
  "v_fma_f32 v5, v5, s73, v223\n"
  "v_mfma_f32_16x16x32_bf16 v[52:55], a[128:131], a[88:91], v[52:55]\n"
  "ds_read_b64_tr_b16 a[134:135], v218 offset:2048\n"
  "v_fma_f32 v6, v6, s73, v223\n"
  "v_mfma_f32_16x16x32_bf16 v[52:55], a[148:151], a[84:87], v[52:55]\n"
  "ds_read_b64_tr_b16 a[128:129], v217 offset:2064\n"
  "v_fma_f32 v36, v36, s73, v225\n"
  "v_mfma_f32_16x16x32_bf16 v[52:55], a[144:147], a[80:83], v[52:55]\n"
  "ds_read_b64_tr_b16 a[130:131], v218 offset:2064\n"
  "v_fma_f32 v37, v37, s73, v225\n"
  "v_mfma_f32_16x16x32_bf16 v[52:55], a[140:143], a[76:79], v[52:55]\n"
  "v_exp_f32 v36, v36\n"
  "v_mfma_f32_16x16x32_bf16 v[52:55], a[136:139], a[72:75], v[52:55]\n"
  "s_waitcnt lgkmcnt(4)\n"
  "v_exp_f32 v37, v37\n"
  "v_mfma_f32_16x16x32_bf16 v[100:103], v[12:15], a[120:123], v[100:103]\n"
  "v_cvt_pk_bf16_f32 v12, v0, v1\n"
  "v_cvt_pk_bf16_f32 v13, v2, v3\n"
  "v_mfma_f32_16x16x32_bf16 v[100:103], v[8:11], a[124:127], v[100:103]\n"
  "v_exp_f32 v22, v22\n"
  "v_mfma_f32_16x16x32_bf16 v[132:135], v[28:31], a[120:123], v[132:135]\n"
  "ds_read_b64_tr_b16 a[140:141], v219 offset:2048\n"
  "v_cvt_pk_bf16_f32 v28, v16, v17\n"
  "v_mfma_f32_16x16x32_bf16 v[132:135], v[24:27], a[124:127], v[132:135]\n"
  "ds_read_b64_tr_b16 a[142:143], v220 offset:2048\n"
  "v_cvt_pk_bf16_f32 v29, v18, v19\n"
  "v_mfma_f32_16x16x32_bf16 v[164:167], v[44:47], a[120:123], v[164:167]\n"
  "ds_read_b64_tr_b16 a[136:137], v219 offset:2064\n"
  "v_cvt_pk_bf16_f32 v44, v32, v33\n"
  "v_mfma_f32_16x16x32_bf16 v[164:167], v[40:43], a[124:127], v[164:167]\n"
  "ds_read_b64_tr_b16 a[138:139], v220 offset:2064\n"
  "v_cvt_pk_bf16_f32 v45, v34, v35\n"
  "v_mfma_f32_16x16x32_bf16 v[196:199], v[60:63], a[120:123], v[196:199]\n"
  "v_exp_f32 v23, v23\n"
  "v_mfma_f32_16x16x32_bf16 v[196:199], v[56:59], a[124:127], v[196:199]\n"
  "s_waitcnt lgkmcnt(4)\n"
  "v_exp_f32 v5, v5\n"
  "v_mfma_f32_16x16x32_bf16 a[244:247], a[156:159], a[132:135], a[244:247]\n"
  "v_exp_f32 v6, v6\n"
  "v_mfma_f32_16x16x32_bf16 a[248:251], a[156:159], a[128:131], a[248:251]\n"
  "v_cvt_pk_bf16_f32 v60, v48, v49\n"
  "v_cvt_pk_bf16_f32 v61, v50, v51\n"
  "v_mfma_f32_16x16x32_bf16 v[200:203], v[56:59], a[132:135], v[200:203]\n"
  "v_fma_f32 v52, v52, s73, v226\n"
  "v_fma_f32 v53, v53, s73, v226\n"
  "v_mfma_f32_16x16x32_bf16 v[204:207], v[56:59], a[128:131], v[204:207]\n"
  "v_exp_f32 v52, v52\n"
  "v_mfma_f32_16x16x32_bf16 v[168:171], v[40:43], a[132:135], v[168:171]\n"
  "v_exp_f32_e64 v53, v53\n"
  "v_mfma_f32_16x16x32_bf16 v[172:175], v[40:43], a[128:131], v[172:175]\n"
  "s_waitcnt vmcnt(14) lgkmcnt(0)\n"
  "s_barrier\n"
  "ds_read_b128 a[120:123], v216 offset:12352\n"
  "v_mfma_f32_16x16x32_bf16 v[136:139], v[24:27], a[132:135], v[136:139]\n"
  "ds_read_b64 a[160:161], v231 offset:2048\n"
  "v_fma_f32 v54, v54, s73, v226\n"
  "v_mfma_f32_16x16x32_bf16 v[140:143], v[24:27], a[128:131], v[140:143]\n"
  "ds_read_b64 a[162:163], v232 offset:2048\n"
  "v_fma_f32 v55, v55, s73, v226\n"
  "v_mfma_f32_16x16x32_bf16 v[104:107], v[8:11], a[132:135], v[104:107]\n"
  "ds_read_b128 a[124:127], v216 offset:12416\n"
  "v_cvt_pk_bf16_f32 v62, v52, v53\n"
  "v_mfma_f32_16x16x32_bf16 v[108:111], v[8:11], a[128:131], v[108:111]\n"
  "ds_read_b128 a[132:135], v216 offset:13376\n"
  "v_fma_f32 v38, v38, s73, v225\n"
  "v_mfma_f32_16x16x32_bf16 v[112:115], v[8:11], a[140:143], v[112:115]\n"
  "s_add_u32 s71, s92, s82\n"
  "ds_read_b128 a[128:131], v216 offset:13440\n"
  "v_fma_f32 v39, v39, s73, v225\n"
  "v_mfma_f32_16x16x32_bf16 v[116:119], v[8:11], a[136:139], v[116:119]\n"
  "s_mov_b32 m0, s66\n"
  "ds_read_b128 a[148:151], v216 offset:14400\n"
  "v_cvt_pk_bf16_f32 v46, v36, v37\n"
  "v_mfma_f32_16x16x32_bf16 v[144:147], v[24:27], a[140:143], v[144:147]\n"
  "s_add_u32 s48, s84, s71\n"
  "ds_read_b128 a[144:147], v216 offset:14464\n"
  "v_cvt_pk_bf16_f32 v30, v20, v21\n"
  "v_mfma_f32_16x16x32_bf16 v[148:151], v[24:27], a[136:139], v[148:151]\n"
  "s_addc_u32 s49, s85, 0\n"
  "v_exp_f32 v54, v54\n"
  "v_mfma_f32_16x16x32_bf16 v[176:179], v[40:43], a[140:143], v[176:179]\n"
  "s_sub_i32 s71, s38, s82\n"
  "v_exp_f32 v55, v55\n"
  "v_mfma_f32_16x16x32_bf16 v[180:183], v[40:43], a[136:139], v[180:183]\n"
  "buffer_load_dwordx4 v221, s[40:43], 0 offen offset:0 lds\n"
  "v_mfma_f32_16x16x32_bf16 v[208:211], v[56:59], a[140:143], v[208:211]\n"
  "s_addk_i32 m0, 896\n"
  "v_exp_f32 v38, v38\n"
  "v_mfma_f32_16x16x32_bf16 v[212:215], v[56:59], a[136:139], v[212:215]\n"
  "s_waitcnt lgkmcnt(4)\n"
  "v_exp_f32 v39, v39\n"
  "v_mfma_f32_16x16x32_bf16 v[64:67], a[120:123], a[96:99], v[248:251]\n"
  "s_max_i32 s50, s71, 0\n"
  "v_cvt_pk_bf16_f32 v63, v54, v55\n"
  "v_cvt_pk_bf16_f32 v47, v38, v39\n"
  "v_mfma_f32_16x16x32_bf16 v[64:67], a[124:127], a[100:103], v[64:67]\n"
  "s_waitcnt lgkmcnt(2)\n"
  "buffer_load_dwordx4 v221, s[40:43], 0 offen offset:128 lds\n"
  "v_cvt_pk_bf16_f32 v31, v22, v23\n"
  "v_mfma_f32_16x16x32_bf16 v[64:67], a[132:135], a[104:107], v[64:67]\n"
  "s_addk_i32 m0, 896\n"
  "v_fma_f32 v7, v7, s73, v223\n"
  "v_cvt_pk_bf16_f32 v14, v4, v5\n"
  "v_mfma_f32_16x16x32_bf16 v[64:67], a[128:131], a[108:111], v[64:67]\n"
  "s_waitcnt lgkmcnt(0)\n"
  "v_exp_f32 v7, v7\n"
  "v_mfma_f32_16x16x32_bf16 v[64:67], a[148:151], a[112:115], v[64:67]\n"
  "v_cvt_pk_bf16_f32 v15, v6, v7\n"
  "v_add_f32 v252, v252, v5\n"
  "v_mfma_f32_16x16x32_bf16 v[64:67], a[144:147], a[116:119], v[64:67]\n"
  "buffer_load_dwordx4 v221, s[40:43], 0 offen offset:256 lds\n"
  "v_add_f32 v252, v252, v6\n"
  "v_mfma_f32_16x16x32_bf16 v[0:3], a[144:147], a[20:23], v[248:251]\n"
  "s_add_u32 s40, s40, s31\n"
  "v_add_f32 v252, v252, v7\n"
  "v_add_f32 v253, v253, v20\n"
  "v_mfma_f32_16x16x32_bf16 v[0:3], a[148:151], a[16:19], v[0:3]\n"
  "s_addc_u32 s41, s41, 0\n"
  "v_bfe_u32 v236, v231, 3, 6\n"
  "v_lshlrev_b32 v236, 7, v236\n"
  "v_mfma_f32_16x16x32_bf16 v[0:3], a[128:131], a[12:15], v[0:3]\n"
  "s_cmp_eq_u32 s76, s24\n"
  "s_cselect_b32 s22, s25, 0\n"
  "v_add_f32 v253, v253, v21\n"
  "v_add_f32 v253, v253, v22\n"
  "v_mfma_f32_16x16x32_bf16 v[0:3], a[132:135], a[8:11], v[0:3]\n"
  "s_mov_b32 m0, s58\n"
  "v_add_f32 v253, v253, v23\n"
  "v_add_f32 v254, v254, v36\n"
  "v_mfma_f32_16x16x32_bf16 v[0:3], a[124:127], a[4:7], v[0:3]\n"
  "v_add_f32 v254, v254, v37\n"
  "v_add_f32 v254, v254, v38\n"
  "v_mfma_f32_16x16x32_bf16 v[0:3], a[120:123], a[0:3], v[0:3]\n"
  "v_add_f32 v254, v254, v39\n"
  "v_add_f32 v255, v255, v52\n"
  "v_mfma_f32_16x16x32_bf16 v[16:19], a[120:123], a[24:27], v[248:251]\n"
  "v_add_f32 v255, v255, v53\n"
  "v_add_f32 v255, v255, v54\n"
  "v_mfma_f32_16x16x32_bf16 v[16:19], a[124:127], a[28:31], v[16:19]\n"
  "buffer_load_dwordx4 v221, s[32:35], 0 offen offset:0 lds\n"
  "v_add_f32 v255, v255, v55\n"
  "v_mfma_f32_16x16x32_bf16 v[16:19], a[132:135], a[32:35], v[16:19]\n"
  "s_addk_i32 m0, 1024\n"
  "ds_read_b64_tr_b16 a[140:141], v217 offset:12352\n"
  "v_fma_f32 v64, v64, s73, v227\n"
  "v_mfma_f32_16x16x32_bf16 v[16:19], a[128:131], a[36:39], v[16:19]\n"
  "s_mov_b64 s[32:33], s[44:45]\n"
  "ds_read_b64_tr_b16 a[142:143], v218 offset:12352\n"
  "v_fma_f32 v65, v65, s73, v227\n"
  "v_mfma_f32_16x16x32_bf16 v[16:19], a[148:151], a[40:43], v[16:19]\n"
  "ds_read_b64_tr_b16 a[136:137], v217 offset:13376\n"
  "v_fma_f32 v66, v66, s73, v227\n"
  "v_mfma_f32_16x16x32_bf16 v[16:19], a[144:147], a[44:47], v[16:19]\n"
  "buffer_load_dwordx4 v221, s[44:47], 0 offen offset:0 lds\n"
  "v_fma_f32 v67, v67, s73, v227\n"
  "v_mfma_f32_16x16x32_bf16 v[32:35], a[144:147], a[68:71], v[248:251]\n"
  "s_addk_i32 m0, 768\n"
  "ds_read_b64_tr_b16 a[138:139], v218 offset:13376\n"
  "v_fma_f32 v0, v0, s73, v223\n"
  "v_mfma_f32_16x16x32_bf16 v[32:35], a[148:151], a[64:67], v[32:35]\n"
  "v_exp_f32 v64, v64\n"
  "v_mfma_f32_16x16x32_bf16 v[32:35], a[128:131], a[60:63], v[32:35]\n"
  "v_exp_f32 v65, v65\n"
  "v_mfma_f32_16x16x32_bf16 v[32:35], a[132:135], a[56:59], v[32:35]\n"
  "buffer_load_dwordx4 v221, s[44:47], 0 offen offset:256 lds\n"
  "v_cvt_pk_bf16_f32 v64, v64, v65\n"
  "v_mfma_f32_16x16x32_bf16 v[32:35], a[124:127], a[52:55], v[32:35]\n"
  "s_add_u32 s44, s44, s31\n"
  "v_exp_f32 v66, v66\n"
  "v_mfma_f32_16x16x32_bf16 v[32:35], a[120:123], a[48:51], v[32:35]\n"
  "s_addc_u32 s45, s45, 0\n"
  "v_exp_f32 v67, v67\n"
  "v_mfma_f32_16x16x32_bf16 v[48:51], a[120:123], a[72:75], v[248:251]\n"
  "s_mov_b32 m0, s29\n"
  "v_exp_f32 v0, v0\n"
  "v_mfma_f32_16x16x32_bf16 v[48:51], a[124:127], a[76:79], v[48:51]\n"
  "v_cvt_pk_bf16_f32 v65, v66, v67\n"
  "v_fma_f32 v1, v1, s73, v223\n"
  "v_mfma_f32_16x16x32_bf16 v[48:51], a[132:135], a[80:83], v[48:51]\n"
  "ds_read_b64_tr_b16 a[120:121], v217 offset:12368\n"
  "v_fma_f32 v2, v2, s73, v223\n"
  "v_mfma_f32_16x16x32_bf16 v[48:51], a[128:131], a[84:87], v[48:51]\n"
  "buffer_load_dword v236, s[20:23], 0 offen lds\n"
  "v_fma_f32 v3, v3, s73, v223\n"
  "v_mfma_f32_16x16x32_bf16 v[48:51], a[148:151], a[88:91], v[48:51]\n"
  "s_add_u32 m0, s72, 13440\n"
  "ds_read_b64_tr_b16 a[122:123], v218 offset:12368\n"
  "v_add_f32 v252, v252, v0\n"
  "v_mfma_f32_16x16x32_bf16 v[48:51], a[144:147], a[92:95], v[48:51]\n"
  "s_waitcnt lgkmcnt(2)\n"
  "ds_read_b64_tr_b16 a[124:125], v217 offset:13392\n"
  "v_fma_f32 v16, v16, s73, v224\n"
  "v_mfma_f32_16x16x32_bf16 a[228:231], a[156:159], a[140:143], a[228:231]\n"
  "ds_read_b64_tr_b16 a[126:127], v218 offset:13392\n"
  "v_fma_f32 v17, v17, s73, v224\n"
  "v_mfma_f32_16x16x32_bf16 a[228:231], a[160:163], a[136:139], a[228:231]\n"
  "buffer_load_dwordx4 v221, s[48:51], 0 offen offset:0 nt lds\n"
  "v_fma_f32 v18, v18, s73, v224\n"
  "v_mfma_f32_16x16x32_bf16 a[212:215], v[56:59], a[140:143], a[212:215]\n"
  "v_exp_f32 v1, v1\n"
  "v_mfma_f32_16x16x32_bf16 a[212:215], v[60:63], a[136:139], a[212:215]\n"
  "v_exp_f32 v2, v2\n"
  "v_mfma_f32_16x16x32_bf16 a[196:199], v[40:43], a[140:143], a[196:199]\n"
  "v_exp_f32 v3, v3\n"
  "v_mfma_f32_16x16x32_bf16 a[196:199], v[44:47], a[136:139], a[196:199]\n"
  "s_add_u32 m0, s72, 11456\n"
  "v_exp_f32 v16, v16\n"
  "v_mfma_f32_16x16x32_bf16 a[180:183], v[24:27], a[140:143], a[180:183]\n"
  "ds_read_b64_tr_b16 a[132:133], v219 offset:12352\n"
  "v_add_f32 v252, v252, v1\n"
  "v_mfma_f32_16x16x32_bf16 a[180:183], v[28:31], a[136:139], a[180:183]\n"
  "ds_read_b64_tr_b16 a[134:135], v220 offset:12352\n"
  "v_add_f32 v252, v252, v2\n"
  "v_mfma_f32_16x16x32_bf16 a[164:167], v[8:11], a[140:143], a[164:167]\n"
  "ds_read_b64_tr_b16 a[128:129], v219 offset:13376\n"
  "v_add_f32 v252, v252, v3\n"
  "v_mfma_f32_16x16x32_bf16 a[164:167], v[12:15], a[136:139], a[164:167]\n"
  "buffer_load_dwordx4 v221, s[48:51], 0 offen offset:3072 nt lds\n"
  "v_fma_f32 v19, v19, s73, v224\n"
  "v_mfma_f32_16x16x32_bf16 v[84:87], a[156:159], a[152:155], v[84:87]\n"
  "ds_read_b64_tr_b16 a[130:131], v220 offset:13376\n"
  "v_add_f32 v253, v253, v16\n"
  "v_mfma_f32_16x16x32_bf16 v[84:87], a[160:163], a[152:155], v[84:87]\n"
  "s_waitcnt lgkmcnt(4)\n"
  "v_exp_f32 v17, v17\n"
  "v_mfma_f32_16x16x32_bf16 a[168:171], v[8:11], a[120:123], a[168:171]\n"
  "v_exp_f32 v18, v18\n"
  "v_mfma_f32_16x16x32_bf16 a[168:171], v[12:15], a[124:127], a[168:171]\n"
  "s_add_u32 m0, s72, 15552\n"
  "v_exp_f32 v19, v19\n"
  "v_mfma_f32_16x16x32_bf16 a[184:187], v[24:27], a[120:123], a[184:187]\n"
  "ds_read_b128 a[140:143], v235 offset:12352\n"
  "v_add_f32 v253, v253, v17\n"
  "v_mfma_f32_16x16x32_bf16 a[184:187], v[28:31], a[124:127], a[184:187]\n"
  "ds_read_b128 a[136:139], v235 offset:12416\n"
  "v_add_f32 v253, v253, v18\n"
  "v_mfma_f32_16x16x32_bf16 a[200:203], v[40:43], a[120:123], a[200:203]\n"
  "ds_read_b128 a[148:151], v235 offset:13376\n"
  "v_add_f32 v253, v253, v19\n"
  "v_mfma_f32_16x16x32_bf16 a[200:203], v[44:47], a[124:127], a[200:203]\n"
  "buffer_load_dwordx4 v221, s[48:51], 0 offen offset:128 nt lds\n"
  "v_fma_f32 v32, v32, s73, v225\n"
  "v_mfma_f32_16x16x32_bf16 a[216:219], v[56:59], a[120:123], a[216:219]\n"
  "ds_read_b128 a[144:147], v235 offset:13440\n"
  "v_fma_f32 v33, v33, s73, v225\n"
  "v_mfma_f32_16x16x32_bf16 a[216:219], v[60:63], a[124:127], a[216:219]\n"
  "v_exp_f32 v32, v32\n"
  "v_mfma_f32_16x16x32_bf16 a[232:235], a[156:159], a[120:123], a[232:235]\n"
  "ds_read_b128 a[120:123], v235 offset:14400\n"
  "v_fma_f32 v34, v34, s73, v225\n"
  "v_mfma_f32_16x16x32_bf16 a[232:235], a[160:163], a[124:127], a[232:235]\n"
  "s_waitcnt lgkmcnt(5)\n"
  "ds_read_b128 a[124:127], v235 offset:14464\n"
  "v_fma_f32 v35, v35, s73, v225\n"
  "v_mfma_f32_16x16x32_bf16 a[220:223], v[56:59], a[132:135], a[220:223]\n"
  "s_add_u32 m0, s72, 13568\n"
  "v_exp_f32 v33, v33\n"
  "v_mfma_f32_16x16x32_bf16 a[220:223], v[60:63], a[128:131], a[220:223]\n"
  "ds_write_b64 v231, v[64:65] offset:0\n"
  "v_mfma_f32_16x16x32_bf16 a[204:207], v[40:43], a[132:135], a[204:207]\n"
  "v_exp_f32 v34, v34\n"
  "v_mfma_f32_16x16x32_bf16 a[204:207], v[44:47], a[128:131], a[204:207]\n"
  "buffer_load_dwordx4 v221, s[48:51], 0 offen offset:3200 nt lds\n"
  "v_add_f32 v254, v254, v32\n"
  "v_mfma_f32_16x16x32_bf16 a[188:191], v[24:27], a[132:135], a[188:191]\n"
  "v_exp_f32 v35, v35\n"
  "v_mfma_f32_16x16x32_bf16 a[188:191], v[28:31], a[128:131], a[188:191]\n"
  "v_add_f32 v254, v254, v33\n"
  "v_add_f32 v254, v254, v34\n"
  "v_mfma_f32_16x16x32_bf16 a[172:175], v[8:11], a[132:135], a[172:175]\n"
  "v_add_f32 v254, v254, v35\n"
  "v_fma_f32 v48, v48, s73, v226\n"
  "v_mfma_f32_16x16x32_bf16 a[172:175], v[12:15], a[128:131], a[172:175]\n"
  "s_waitcnt lgkmcnt(5)\n"
  "v_exp_f32 v48, v48\n"
  "v_mfma_f32_16x16x32_bf16 v[4:7], a[140:143], a[0:3], v[248:251]\n"
  "v_fma_f32 v49, v49, s73, v226\n"
  "v_fma_f32 v50, v50, s73, v226\n"
  "v_mfma_f32_16x16x32_bf16 v[4:7], a[136:139], a[4:7], v[4:7]\n"
  "s_waitcnt lgkmcnt(3)\n"
  "v_exp_f32 v49, v49\n"
  "v_mfma_f32_16x16x32_bf16 v[4:7], a[148:151], a[8:11], v[4:7]\n"
  "v_exp_f32 v50, v50\n"
  "v_mfma_f32_16x16x32_bf16 v[4:7], a[144:147], a[12:15], v[4:7]\n"
  "s_waitcnt lgkmcnt(1)\n"
  "v_fma_f32 v51, v51, s73, v226\n"
  "v_add_f32 v255, v255, v48\n"
  "v_mfma_f32_16x16x32_bf16 v[4:7], a[120:123], a[16:19], v[4:7]\n"
  "v_exp_f32 v51, v51\n"
  "v_mfma_f32_16x16x32_bf16 v[4:7], a[124:127], a[20:23], v[4:7]\n"
  "v_add_f32 v255, v255, v49\n"
  "v_add_f32 v255, v255, v50\n"
  "v_mfma_f32_16x16x32_bf16 v[20:23], a[124:127], a[44:47], v[248:251]\n"
  "v_add_f32 v255, v255, v51\n"
  "v_mfma_f32_16x16x32_bf16 v[20:23], a[120:123], a[40:43], v[20:23]\n"
  "v_mfma_f32_16x16x32_bf16 v[20:23], a[144:147], a[36:39], v[20:23]\n"
  "ds_read_b64_tr_b16 a[132:133], v219 offset:12368\n"
  "v_fma_f32 v4, v4, s73, v223\n"
  "v_mfma_f32_16x16x32_bf16 v[20:23], a[148:151], a[32:35], v[20:23]\n"
  "ds_read_b64_tr_b16 a[134:135], v220 offset:12368\n"
  "v_fma_f32 v5, v5, s73, v223\n"
  "v_mfma_f32_16x16x32_bf16 v[20:23], a[136:139], a[28:31], v[20:23]\n"
  "ds_read_b64_tr_b16 a[128:129], v219 offset:13392\n"
  "v_fma_f32 v6, v6, s73, v223\n"
  "v_mfma_f32_16x16x32_bf16 v[20:23], a[140:143], a[24:27], v[20:23]\n"
  "ds_read_b64_tr_b16 a[130:131], v220 offset:13392\n"
  "v_fma_f32 v7, v7, s73, v223\n"
  "v_mfma_f32_16x16x32_bf16 v[36:39], a[140:143], a[48:51], v[248:251]\n"
  "v_exp_f32 v4, v4\n"
  "v_mfma_f32_16x16x32_bf16 v[36:39], a[136:139], a[52:55], v[36:39]\n"
  "v_exp_f32 v5, v5\n"
  "v_mfma_f32_16x16x32_bf16 v[36:39], a[148:151], a[56:59], v[36:39]\n"
  "v_exp_f32 v6, v6\n"
  "v_mfma_f32_16x16x32_bf16 v[36:39], a[144:147], a[60:63], v[36:39]\n"
  "v_exp_f32 v7, v7\n"
  "v_mfma_f32_16x16x32_bf16 v[36:39], a[120:123], a[64:67], v[36:39]\n"
  "v_fma_f32 v20, v20, s73, v224\n"
  "v_fma_f32 v21, v21, s73, v224\n"
  "v_mfma_f32_16x16x32_bf16 v[36:39], a[124:127], a[68:71], v[36:39]\n"
  "v_exp_f32 v20, v20\n"
  "v_mfma_f32_16x16x32_bf16 v[52:55], a[124:127], a[92:95], v[248:251]\n"
  "ds_read_b64_tr_b16 a[124:125], v217 offset:14400\n"
  "v_fma_f32 v22, v22, s73, v224\n"
  "v_mfma_f32_16x16x32_bf16 v[52:55], a[120:123], a[88:91], v[52:55]\n"
  "ds_read_b64_tr_b16 a[126:127], v218 offset:14400\n"
  "v_fma_f32 v23, v23, s73, v224\n"
  "v_mfma_f32_16x16x32_bf16 v[52:55], a[144:147], a[84:87], v[52:55]\n"
  "ds_read_b64_tr_b16 a[120:121], v217 offset:14416\n"
  "v_fma_f32 v36, v36, s73, v225\n"
  "v_mfma_f32_16x16x32_bf16 v[52:55], a[148:151], a[80:83], v[52:55]\n"
  "ds_read_b64_tr_b16 a[122:123], v218 offset:14416\n"
  "v_fma_f32 v37, v37, s73, v225\n"
  "v_mfma_f32_16x16x32_bf16 v[52:55], a[136:139], a[76:79], v[52:55]\n"
  "v_exp_f32 v36, v36\n"
  "v_mfma_f32_16x16x32_bf16 v[52:55], a[140:143], a[72:75], v[52:55]\n"
  "s_waitcnt lgkmcnt(4)\n"
  "v_exp_f32 v37, v37\n"
  "v_mfma_f32_16x16x32_bf16 a[176:179], v[8:11], a[132:135], a[176:179]\n"
  "v_cvt_pk_bf16_f32 v8, v0, v1\n"
  "v_cvt_pk_bf16_f32 v9, v2, v3\n"
  "v_mfma_f32_16x16x32_bf16 a[176:179], v[12:15], a[128:131], a[176:179]\n"
  "v_exp_f32 v21, v21\n"
  "v_mfma_f32_16x16x32_bf16 a[192:195], v[24:27], a[132:135], a[192:195]\n"
  "ds_read_b64_tr_b16 a[136:137], v219 offset:14400\n"
  "v_cvt_pk_bf16_f32 v24, v16, v17\n"
  "v_mfma_f32_16x16x32_bf16 a[192:195], v[28:31], a[128:131], a[192:195]\n"
  "ds_read_b64_tr_b16 a[138:139], v220 offset:14400\n"
  "v_cvt_pk_bf16_f32 v25, v18, v19\n"
  "v_mfma_f32_16x16x32_bf16 a[208:211], v[40:43], a[132:135], a[208:211]\n"
  "ds_read_b64_tr_b16 a[140:141], v219 offset:14416\n"
  "v_cvt_pk_bf16_f32 v40, v32, v33\n"
  "v_mfma_f32_16x16x32_bf16 a[208:211], v[44:47], a[128:131], a[208:211]\n"
  "ds_read_b64_tr_b16 a[142:143], v220 offset:14416\n"
  "v_cvt_pk_bf16_f32 v41, v34, v35\n"
  "v_mfma_f32_16x16x32_bf16 a[224:227], v[56:59], a[132:135], a[224:227]\n"
  "v_exp_f32 v22, v22\n"
  "v_mfma_f32_16x16x32_bf16 a[224:227], v[60:63], a[128:131], a[224:227]\n"
  "s_waitcnt lgkmcnt(4)\n"
  "v_exp_f32 v23, v23\n"
  "v_mfma_f32_16x16x32_bf16 a[244:247], a[160:163], a[124:127], a[244:247]\n"
  "v_cvt_pk_bf16_f32 v56, v48, v49\n"
  "v_cvt_pk_bf16_f32 v57, v50, v51\n"
  "v_mfma_f32_16x16x32_bf16 a[248:251], a[160:163], a[120:123], a[248:251]\n"
  "v_fma_f32 v52, v52, s73, v226\n"
  "v_fma_f32 v53, v53, s73, v226\n"
  "v_mfma_f32_16x16x32_bf16 v[200:203], v[60:63], a[124:127], v[200:203]\n"
  "v_exp_f32 v52, v52\n"
  "v_mfma_f32_16x16x32_bf16 v[204:207], v[60:63], a[120:123], v[204:207]\n"
  "v_exp_f32_e64 v53, v53\n"
  "v_mfma_f32_16x16x32_bf16 v[168:171], v[44:47], a[124:127], v[168:171]\n"
  "v_fma_f32 v54, v54, s73, v226\n"
  "v_fma_f32 v55, v55, s73, v226\n"
  "v_mfma_f32_16x16x32_bf16 v[172:175], v[44:47], a[120:123], v[172:175]\n"
  "s_waitcnt vmcnt(16) lgkmcnt(0)\n"
  "s_barrier\n"
  "ds_read_b128 a[132:135], v216 offset:24704\n"
  "v_mfma_f32_16x16x32_bf16 v[136:139], v[28:31], a[124:127], v[136:139]\n"
  "ds_read_b64 a[156:157], v231 offset:0\n"
  "v_cvt_pk_bf16_f32 v58, v52, v53\n"
  "v_mfma_f32_16x16x32_bf16 v[140:143], v[28:31], a[120:123], v[140:143]\n"
  "ds_read_b64 a[158:159], v232 offset:0\n"
  "v_fma_f32 v38, v38, s73, v225\n"
  "v_mfma_f32_16x16x32_bf16 v[104:107], v[12:15], a[124:127], v[104:107]\n"
  "ds_read_b128 a[128:131], v216 offset:24768\n"
  "v_fma_f32 v39, v39, s73, v225\n"
  "v_mfma_f32_16x16x32_bf16 v[108:111], v[12:15], a[120:123], v[108:111]\n"
  "ds_read_b128 a[124:127], v216 offset:25728\n"
  "v_cvt_pk_bf16_f32 v42, v36, v37\n"
  "v_mfma_f32_16x16x32_bf16 v[112:115], v[12:15], a[136:139], v[112:115]\n"
  "ds_read_b128 a[120:123], v216 offset:25792\n"
  "v_cvt_pk_bf16_f32 v26, v20, v21\n"
  "v_mfma_f32_16x16x32_bf16 v[116:119], v[12:15], a[140:143], v[116:119]\n"
  "s_mov_b32 m0, s67\n"
  "ds_read_b128 a[144:147], v216 offset:26752\n"
  "v_cvt_pk_bf16_f32 v27, v22, v23\n"
  "v_mfma_f32_16x16x32_bf16 v[144:147], v[28:31], a[136:139], v[144:147]\n"
  "ds_read_b128 a[148:151], v216 offset:26816\n"
  "v_cvt_pk_bf16_f32 v10, v4, v5\n"
  "v_mfma_f32_16x16x32_bf16 v[148:151], v[28:31], a[140:143], v[148:151]\n"
  "v_exp_f32 v54, v54\n"
  "v_mfma_f32_16x16x32_bf16 v[176:179], v[44:47], a[136:139], v[176:179]\n"
  "v_exp_f32 v55, v55\n"
  "v_mfma_f32_16x16x32_bf16 v[180:183], v[44:47], a[140:143], v[180:183]\n"
  "buffer_load_dwordx4 v221, s[40:43], 0 offen offset:0 lds\n"
  "v_mfma_f32_16x16x32_bf16 v[208:211], v[60:63], a[136:139], v[208:211]\n"
  "s_addk_i32 m0, 896\n"
  "v_exp_f32 v38, v38\n"
  "v_mfma_f32_16x16x32_bf16 v[212:215], v[60:63], a[140:143], v[212:215]\n"
  "s_waitcnt lgkmcnt(4)\n"
  "v_exp_f32 v39, v39\n"
  "v_mfma_f32_16x16x32_bf16 v[64:67], a[132:135], a[96:99], v[248:251]\n"
  "v_cvt_pk_bf16_f32 v59, v54, v55\n"
  "v_cvt_pk_bf16_f32 v43, v38, v39\n"
  "v_mfma_f32_16x16x32_bf16 v[64:67], a[128:131], a[100:103], v[64:67]\n"
  "s_waitcnt lgkmcnt(2)\n"
  "buffer_load_dwordx4 v221, s[40:43], 0 offen offset:128 lds\n"
  "v_cvt_pk_bf16_f32 v11, v6, v7\n"
  "v_mfma_f32_16x16x32_bf16 v[64:67], a[124:127], a[104:107], v[64:67]\n"
  "s_addk_i32 m0, 896\n"
  "v_add_f32 v252, v252, v4\n"
  "v_add_f32 v252, v252, v5\n"
  "v_mfma_f32_16x16x32_bf16 v[64:67], a[120:123], a[108:111], v[64:67]\n"
  "s_waitcnt lgkmcnt(0)\n"
  "v_add_f32 v252, v252, v6\n"
  "v_add_f32 v252, v252, v7\n"
  "v_mfma_f32_16x16x32_bf16 v[64:67], a[144:147], a[112:115], v[64:67]\n"
  "v_add_f32 v253, v253, v20\n"
  "v_add_f32 v253, v253, v21\n"
  "v_mfma_f32_16x16x32_bf16 v[64:67], a[148:151], a[116:119], v[64:67]\n"
  "buffer_load_dwordx4 v221, s[40:43], 0 offen offset:256 lds\n"
  "v_add_f32 v253, v253, v22\n"
  "v_mfma_f32_16x16x32_bf16 v[0:3], a[148:151], a[20:23], v[248:251]\n"
  "s_add_u32 s40, s86, s96\n"
  "v_add_f32 v253, v253, v23\n"
  "v_add_f32 v254, v254, v36\n"
  "v_mfma_f32_16x16x32_bf16 v[0:3], a[144:147], a[16:19], v[0:3]\n"
  "s_addc_u32 s41, s87, 0\n"
  "v_add_f32 v254, v254, v37\n"
  "v_add_f32 v254, v254, v38\n"
  "v_mfma_f32_16x16x32_bf16 v[0:3], a[120:123], a[12:15], v[0:3]\n"
  "s_mov_b32 s42, s30\n"
  "v_add_f32 v254, v254, v39\n"
  "v_add_f32 v255, v255, v52\n"
  "v_mfma_f32_16x16x32_bf16 v[0:3], a[124:127], a[8:11], v[0:3]\n"
  "s_add_u32 m0, s59, -128\n"
  "v_add_f32 v255, v255, v53\n"
  "v_add_f32 v255, v255, v54\n"
  "v_mfma_f32_16x16x32_bf16 v[0:3], a[128:131], a[4:7], v[0:3]\n"
  "v_add_f32 v255, v255, v55\n"
  "v_fma_f32 v64, v64, s73, v227\n"
  "v_mfma_f32_16x16x32_bf16 v[0:3], a[132:135], a[0:3], v[0:3]\n"
  "v_exp_f32 v64, v64\n"
  "v_mfma_f32_16x16x32_bf16 v[16:19], a[132:135], a[24:27], v[248:251]\n"
  "v_fma_f32 v65, v65, s73, v227\n"
  "v_fma_f32 v66, v66, s73, v227\n"
  "v_mfma_f32_16x16x32_bf16 v[16:19], a[128:131], a[28:31], v[16:19]\n"
  "buffer_load_dwordx4 v221, s[32:35], 0 offen offset:128 lds\n"
  "v_fma_f32 v67, v67, s73, v227\n"
  "v_mfma_f32_16x16x32_bf16 v[16:19], a[124:127], a[32:35], v[16:19]\n"
  "s_addk_i32 m0, 1024\n"
  "ds_read_b64_tr_b16 a[136:137], v217 offset:24704\n"
  "v_fma_f32 v0, v0, s73, v223\n"
  "v_mfma_f32_16x16x32_bf16 v[16:19], a[120:123], a[36:39], v[16:19]\n"
  "s_mov_b64 s[32:33], s[44:45]\n"
  "ds_read_b64_tr_b16 a[138:139], v218 offset:24704\n"
  "v_fma_f32 v1, v1, s73, v223\n"
  "v_mfma_f32_16x16x32_bf16 v[16:19], a[144:147], a[40:43], v[16:19]\n"
  "ds_read_b64_tr_b16 a[140:141], v217 offset:25728\n"
  "v_fma_f32 v2, v2, s73, v223\n"
  "v_mfma_f32_16x16x32_bf16 v[16:19], a[148:151], a[44:47], v[16:19]\n"
  "buffer_load_dwordx4 v221, s[44:47], 0 offen offset:128 lds\n"
  "v_fma_f32 v3, v3, s73, v223\n"
  "v_mfma_f32_16x16x32_bf16 v[32:35], a[148:151], a[68:71], v[248:251]\n"
  "s_addk_i32 m0, 896\n"
  "ds_read_b64_tr_b16 a[142:143], v218 offset:25728\n"
  "v_mfma_f32_16x16x32_bf16 v[32:35], a[144:147], a[64:67], v[32:35]\n"
  "v_exp_f32 v65, v65\n"
  "v_mfma_f32_16x16x32_bf16 v[32:35], a[120:123], a[60:63], v[32:35]\n"
  "v_exp_f32 v66, v66\n"
  "v_mfma_f32_16x16x32_bf16 v[32:35], a[124:127], a[56:59], v[32:35]\n"
  "buffer_load_dwordx4 v221, s[44:47], 0 offen offset:256 lds\n"
  "v_cvt_pk_bf16_f32 v64, v64, v65\n"
  "v_mfma_f32_16x16x32_bf16 v[32:35], a[128:131], a[52:55], v[32:35]\n"
  "s_add_u32 s44, s44, s31\n"
  "v_exp_f32 v67, v67\n"
  "v_mfma_f32_16x16x32_bf16 v[32:35], a[132:135], a[48:51], v[32:35]\n"
  "s_addc_u32 s45, s45, 0\n"
  "v_exp_f32 v0, v0\n"
  "v_mfma_f32_16x16x32_bf16 v[48:51], a[132:135], a[72:75], v[248:251]\n"
  "ds_read_b64_tr_b16 a[132:133], v217 offset:24720\n"
  "v_cvt_pk_bf16_f32 v65, v66, v67\n"
  "v_mfma_f32_16x16x32_bf16 v[48:51], a[128:131], a[76:79], v[48:51]\n"
  "ds_read_b64_tr_b16 a[134:135], v218 offset:24720\n"
  "v_add_f32 v252, v252, v0\n"
  "v_mfma_f32_16x16x32_bf16 v[48:51], a[124:127], a[80:83], v[48:51]\n"
  "ds_read_b64_tr_b16 a[128:129], v217 offset:25744\n"
  "v_fma_f32 v16, v16, s73, v224\n"
  "v_mfma_f32_16x16x32_bf16 v[48:51], a[120:123], a[84:87], v[48:51]\n"
  "ds_read_b64_tr_b16 a[130:131], v218 offset:25744\n"
  "v_fma_f32 v17, v17, s73, v224\n"
  "v_mfma_f32_16x16x32_bf16 v[48:51], a[144:147], a[88:91], v[48:51]\n"
  "v_exp_f32 v1, v1\n"
  "v_mfma_f32_16x16x32_bf16 v[48:51], a[148:151], a[92:95], v[48:51]\n"
  "s_waitcnt lgkmcnt(4)\n"
  "v_exp_f32 v2, v2\n"
  "v_mfma_f32_16x16x32_bf16 a[236:239], a[160:163], a[136:139], a[236:239]\n"
  "v_exp_f32 v3, v3\n"
  "v_mfma_f32_16x16x32_bf16 a[236:239], a[156:159], a[140:143], a[236:239]\n"
  "v_exp_f32 v16, v16\n"
  "v_mfma_f32_16x16x32_bf16 v[184:187], v[60:63], a[136:139], v[184:187]\n"
  "v_exp_f32 v17, v17\n"
  "v_mfma_f32_16x16x32_bf16 v[184:187], v[56:59], a[140:143], v[184:187]\n"
  "v_add_f32 v252, v252, v1\n"
  "v_add_f32 v252, v252, v2\n"
  "v_mfma_f32_16x16x32_bf16 v[152:155], v[44:47], a[136:139], v[152:155]\n"
  "ds_read_b64_tr_b16 a[124:125], v219 offset:24704\n"
  "v_add_f32 v252, v252, v3\n"
  "v_mfma_f32_16x16x32_bf16 v[152:155], v[40:43], a[140:143], v[152:155]\n"
  "ds_read_b64_tr_b16 a[126:127], v220 offset:24704\n"
  "v_fma_f32 v18, v18, s73, v224\n"
  "v_mfma_f32_16x16x32_bf16 v[120:123], v[28:31], a[136:139], v[120:123]\n"
  "ds_read_b64_tr_b16 a[120:121], v219 offset:25728\n"
  "v_fma_f32 v19, v19, s73, v224\n"
  "v_mfma_f32_16x16x32_bf16 v[120:123], v[24:27], a[140:143], v[120:123]\n"
  "ds_read_b64_tr_b16 a[122:123], v220 offset:25728\n"
  "v_fma_f32 v32, v32, s73, v225\n"
  "v_mfma_f32_16x16x32_bf16 v[88:91], v[12:15], a[136:139], v[88:91]\n"
  "v_exp_f32 v18, v18\n"
  "v_mfma_f32_16x16x32_bf16 v[88:91], v[8:11], a[140:143], v[88:91]\n"
  "s_waitcnt lgkmcnt(4)\n"
  "v_exp_f32 v19, v19\n"
  "v_mfma_f32_16x16x32_bf16 v[92:95], v[12:15], a[132:135], v[92:95]\n"
  "v_exp_f32 v32, v32\n"
  "v_mfma_f32_16x16x32_bf16 v[92:95], v[8:11], a[128:131], v[92:95]\n"
  "v_fma_f32 v33, v33, s73, v225\n"
  "v_fma_f32 v34, v34, s73, v225\n"
  "v_mfma_f32_16x16x32_bf16 v[124:127], v[28:31], a[132:135], v[124:127]\n"
  "ds_read_b128 a[136:139], v235 offset:24704\n"
  "v_fma_f32 v35, v35, s73, v225\n"
  "v_mfma_f32_16x16x32_bf16 v[124:127], v[24:27], a[128:131], v[124:127]\n"
  "ds_read_b128 a[140:143], v235 offset:24768\n"
  "v_fma_f32 v48, v48, s73, v226\n"
  "v_mfma_f32_16x16x32_bf16 v[156:159], v[44:47], a[132:135], v[156:159]\n"
  "ds_read_b128 a[144:147], v235 offset:25728\n"
  "v_fma_f32 v49, v49, s73, v226\n"
  "v_mfma_f32_16x16x32_bf16 v[156:159], v[40:43], a[128:131], v[156:159]\n"
  "ds_read_b128 a[148:151], v235 offset:25792\n"
  "v_fma_f32 v50, v50, s73, v226\n"
  "v_mfma_f32_16x16x32_bf16 v[188:191], v[60:63], a[132:135], v[188:191]\n"
  "v_exp_f32 v33, v33\n"
  "v_mfma_f32_16x16x32_bf16 v[188:191], v[56:59], a[128:131], v[188:191]\n"
  "v_exp_f32 v34, v34\n"
  "v_mfma_f32_16x16x32_bf16 a[240:243], a[160:163], a[132:135], a[240:243]\n"
  "ds_read_b128 a[132:135], v235 offset:26752\n"
  "v_fma_f32 v51, v51, s73, v226\n"
  "v_mfma_f32_16x16x32_bf16 a[240:243], a[156:159], a[128:131], a[240:243]\n"
  "s_waitcnt lgkmcnt(5)\n"
  "ds_read_b128 a[128:131], v235 offset:26816\n"
  "v_add_f32 v253, v253, v16\n"
  "v_mfma_f32_16x16x32_bf16 v[192:195], v[60:63], a[124:127], v[192:195]\n"
  "v_exp_f32 v35, v35\n"
  "v_mfma_f32_16x16x32_bf16 v[192:195], v[56:59], a[120:123], v[192:195]\n"
  "ds_write_b64 v231, v[64:65] offset:2048\n"
  "v_mfma_f32_16x16x32_bf16 v[160:163], v[44:47], a[124:127], v[160:163]\n"
  "v_exp_f32 v48, v48\n"
  "v_mfma_f32_16x16x32_bf16 v[160:163], v[40:43], a[120:123], v[160:163]\n"
  "v_exp_f32 v49, v49\n"
  "v_mfma_f32_16x16x32_bf16 v[128:131], v[28:31], a[124:127], v[128:131]\n"
  "v_exp_f32 v50, v50\n"
  "v_mfma_f32_16x16x32_bf16 v[128:131], v[24:27], a[120:123], v[128:131]\n"
  "v_exp_f32 v51, v51\n"
  "v_mfma_f32_16x16x32_bf16 v[96:99], v[12:15], a[124:127], v[96:99]\n"
  "v_add_f32 v253, v253, v17\n"
  "v_add_f32 v253, v253, v18\n"
  "v_mfma_f32_16x16x32_bf16 v[96:99], v[8:11], a[120:123], v[96:99]\n"
  "s_waitcnt lgkmcnt(5)\n"
  "v_add_f32 v253, v253, v19\n"
  "v_add_f32 v254, v254, v32\n"
  "v_mfma_f32_16x16x32_bf16 v[4:7], a[136:139], a[0:3], v[248:251]\n"
  "v_add_f32 v254, v254, v33\n"
  "v_add_f32 v254, v254, v34\n"
  "v_mfma_f32_16x16x32_bf16 v[4:7], a[140:143], a[4:7], v[4:7]\n"
  "s_waitcnt lgkmcnt(3)\n"
  "v_add_f32 v254, v254, v35\n"
  "v_add_f32 v255, v255, v48\n"
  "v_mfma_f32_16x16x32_bf16 v[4:7], a[144:147], a[8:11], v[4:7]\n"
  "v_add_f32 v255, v255, v49\n"
  "v_add_f32 v255, v255, v50\n"
  "v_mfma_f32_16x16x32_bf16 v[4:7], a[148:151], a[12:15], v[4:7]\n"
  "s_waitcnt lgkmcnt(1)\n"
  "v_add_f32 v255, v255, v51\n"
  "v_mfma_f32_16x16x32_bf16 v[4:7], a[132:135], a[16:19], v[4:7]\n"
  "v_mfma_f32_16x16x32_bf16 v[4:7], a[128:131], a[20:23], v[4:7]\n"
  "v_mfma_f32_16x16x32_bf16 v[20:23], a[128:131], a[44:47], v[248:251]\n"
  "v_mfma_f32_16x16x32_bf16 v[20:23], a[132:135], a[40:43], v[20:23]\n"
  "v_mfma_f32_16x16x32_bf16 v[20:23], a[148:151], a[36:39], v[20:23]\n"
  "ds_read_b64_tr_b16 a[124:125], v219 offset:24720\n"
  "v_fma_f32 v4, v4, s73, v223\n"
  "v_mfma_f32_16x16x32_bf16 v[20:23], a[144:147], a[32:35], v[20:23]\n"
  "ds_read_b64_tr_b16 a[126:127], v220 offset:24720\n"
  "v_fma_f32 v5, v5, s73, v223\n"
  "v_mfma_f32_16x16x32_bf16 v[20:23], a[140:143], a[28:31], v[20:23]\n"
  "ds_read_b64_tr_b16 a[120:121], v219 offset:25744\n"
  "v_fma_f32 v6, v6, s73, v223\n"
  "v_mfma_f32_16x16x32_bf16 v[20:23], a[136:139], a[24:27], v[20:23]\n"
  "ds_read_b64_tr_b16 a[122:123], v220 offset:25744\n"
  "v_fma_f32 v7, v7, s73, v223\n"
  "v_mfma_f32_16x16x32_bf16 v[36:39], a[136:139], a[48:51], v[248:251]\n"
  "v_exp_f32 v4, v4\n"
  "v_mfma_f32_16x16x32_bf16 v[36:39], a[140:143], a[52:55], v[36:39]\n"
  "v_exp_f32 v5, v5\n"
  "v_mfma_f32_16x16x32_bf16 v[36:39], a[144:147], a[56:59], v[36:39]\n"
  "v_exp_f32 v6, v6\n"
  "v_mfma_f32_16x16x32_bf16 v[36:39], a[148:151], a[60:63], v[36:39]\n"
  "v_exp_f32 v7, v7\n"
  "v_mfma_f32_16x16x32_bf16 v[36:39], a[132:135], a[64:67], v[36:39]\n"
  "v_fma_f32 v20, v20, s73, v224\n"
  "v_fma_f32 v21, v21, s73, v224\n"
  "v_mfma_f32_16x16x32_bf16 v[36:39], a[128:131], a[68:71], v[36:39]\n"
  "v_exp_f32 v20, v20\n"
  "v_mfma_f32_16x16x32_bf16 v[52:55], a[128:131], a[92:95], v[248:251]\n"
  "ds_read_b64_tr_b16 a[128:129], v217 offset:26752\n"
  "v_fma_f32 v22, v22, s73, v224\n"
  "v_mfma_f32_16x16x32_bf16 v[52:55], a[132:135], a[88:91], v[52:55]\n"
  "ds_read_b64_tr_b16 a[130:131], v218 offset:26752\n"
  "v_fma_f32 v23, v23, s73, v224\n"
  "v_mfma_f32_16x16x32_bf16 v[52:55], a[148:151], a[84:87], v[52:55]\n"
  "ds_read_b64_tr_b16 a[132:133], v217 offset:26768\n"
  "v_fma_f32 v36, v36, s73, v225\n"
  "v_mfma_f32_16x16x32_bf16 v[52:55], a[144:147], a[80:83], v[52:55]\n"
  "ds_read_b64_tr_b16 a[134:135], v218 offset:26768\n"
  "v_fma_f32 v37, v37, s73, v225\n"
  "v_mfma_f32_16x16x32_bf16 v[52:55], a[140:143], a[76:79], v[52:55]\n"
  "v_exp_f32 v21, v21\n"
  "v_mfma_f32_16x16x32_bf16 v[52:55], a[136:139], a[72:75], v[52:55]\n"
  "s_waitcnt lgkmcnt(4)\n"
  "v_exp_f32 v22, v22\n"
  "v_mfma_f32_16x16x32_bf16 v[100:103], v[12:15], a[124:127], v[100:103]\n"
  "v_exp_f32 v23, v23\n"
  "v_mfma_f32_16x16x32_bf16 v[100:103], v[8:11], a[120:123], v[100:103]\n"
  "v_exp_f32 v36, v36\n"
  "v_mfma_f32_16x16x32_bf16 v[132:135], v[28:31], a[124:127], v[132:135]\n"
  "ds_read_b64_tr_b16 a[148:149], v219 offset:26752\n"
  "v_cvt_pk_bf16_f32 v12, v0, v1\n"
  "v_mfma_f32_16x16x32_bf16 v[132:135], v[24:27], a[120:123], v[132:135]\n"
  "ds_read_b64_tr_b16 a[150:151], v220 offset:26752\n"
  "v_cvt_pk_bf16_f32 v13, v2, v3\n"
  "v_mfma_f32_16x16x32_bf16 v[164:167], v[44:47], a[124:127], v[164:167]\n"
  "ds_read_b64_tr_b16 a[144:145], v219 offset:26768\n"
  "v_fma_f32 v38, v38, s73, v225\n"
  "v_mfma_f32_16x16x32_bf16 v[164:167], v[40:43], a[120:123], v[164:167]\n"
  "ds_read_b64_tr_b16 a[146:147], v220 offset:26768\n"
  "v_fma_f32 v39, v39, s73, v225\n"
  "v_mfma_f32_16x16x32_bf16 v[196:199], v[60:63], a[124:127], v[196:199]\n"
  "v_exp_f32 v37, v37\n"
  "v_mfma_f32_16x16x32_bf16 v[196:199], v[56:59], a[120:123], v[196:199]\n"
  "s_waitcnt lgkmcnt(4)\n"
  "v_exp_f32 v38, v38\n"
  "v_mfma_f32_16x16x32_bf16 a[244:247], a[156:159], a[128:131], a[244:247]\n"
  "v_exp_f32 v39, v39\n"
  "v_mfma_f32_16x16x32_bf16 a[248:251], a[156:159], a[132:135], a[248:251]\n"
  "v_fma_f32 v52, v52, s73, v226\n"
  "v_fma_f32 v53, v53, s73, v226\n"
  "v_mfma_f32_16x16x32_bf16 v[200:203], v[56:59], a[128:131], v[200:203]\n"
  "v_exp_f32 v52, v52\n"
  "v_mfma_f32_16x16x32_bf16 v[204:207], v[56:59], a[132:135], v[204:207]\n"
  "v_exp_f32 v53, v53\n"
  "v_mfma_f32_16x16x32_bf16 v[168:171], v[40:43], a[128:131], v[168:171]\n"
  "v_fma_f32 v54, v54, s73, v226\n"
  "v_fma_f32 v55, v55, s73, v226\n"
  "v_mfma_f32_16x16x32_bf16 v[172:175], v[40:43], a[132:135], v[172:175]\n"
  "s_waitcnt vmcnt(11) lgkmcnt(0)\n"
  "s_barrier\n"
  "ds_read_b128 a[120:123], v216 offset:0\n"
  "v_mfma_f32_16x16x32_bf16 v[136:139], v[24:27], a[128:131], v[136:139]\n"
  "ds_read_b64 a[160:161], v231 offset:2048\n"
  "v_add_f32 v252, v252, v4\n"
  "v_mfma_f32_16x16x32_bf16 v[140:143], v[24:27], a[132:135], v[140:143]\n"
  "ds_read_b64 a[162:163], v232 offset:2048\n"
  "v_add_f32 v252, v252, v5\n"
  "v_mfma_f32_16x16x32_bf16 v[104:107], v[8:11], a[128:131], v[104:107]\n"
  "ds_read_b128 a[124:127], v216 offset:64\n"
  "v_add_f32 v252, v252, v6\n"
  "v_mfma_f32_16x16x32_bf16 v[108:111], v[8:11], a[132:135], v[108:111]\n"
  "ds_read_b128 a[128:131], v216 offset:1024\n"
  "v_add_f32 v252, v252, v7\n"
  "v_mfma_f32_16x16x32_bf16 v[112:115], v[8:11], a[148:151], v[112:115]\n"
  "ds_read_b128 a[132:135], v216 offset:1088\n"
  "v_add_f32 v253, v253, v20\n"
  "v_mfma_f32_16x16x32_bf16 v[116:119], v[8:11], a[144:147], v[116:119]\n"
  "s_mov_b32 m0, s68\n"
  "ds_read_b128 a[136:139], v216 offset:2048\n"
  "v_add_f32 v253, v253, v21\n"
  "v_mfma_f32_16x16x32_bf16 v[144:147], v[24:27], a[148:151], v[144:147]\n"
  "ds_read_b128 a[140:143], v216 offset:2112\n"
  "v_add_f32 v253, v253, v22\n"
  "v_mfma_f32_16x16x32_bf16 v[148:151], v[24:27], a[144:147], v[148:151]\n"
  "v_exp_f32 v54, v54\n"
  "v_mfma_f32_16x16x32_bf16 v[176:179], v[40:43], a[148:151], v[176:179]\n"
  "v_exp_f32 v55, v55\n"
  "v_mfma_f32_16x16x32_bf16 v[180:183], v[40:43], a[144:147], v[180:183]\n"
  "buffer_load_dwordx4 v221, s[40:43], 0 offen offset:0 lds\n"
  "v_mfma_f32_16x16x32_bf16 v[208:211], v[56:59], a[148:151], v[208:211]\n"
  "s_addk_i32 m0, 896\n"
  "v_add_f32 v253, v253, v23\n"
  "v_add_f32 v254, v254, v36\n"
  "v_mfma_f32_16x16x32_bf16 v[212:215], v[56:59], a[144:147], v[212:215]\n"
  "s_waitcnt lgkmcnt(4)\n"
  "v_add_f32 v254, v254, v37\n"
  "v_add_f32 v254, v254, v38\n"
  "v_mfma_f32_16x16x32_bf16 v[64:67], a[120:123], a[96:99], v[248:251]\n"
  "v_add_f32 v254, v254, v39\n"
  "v_add_f32 v255, v255, v52\n"
  "v_mfma_f32_16x16x32_bf16 v[64:67], a[124:127], a[100:103], v[64:67]\n"
  "s_waitcnt lgkmcnt(2)\n"
  "buffer_load_dwordx4 v221, s[40:43], 0 offen offset:128 lds\n"
  "v_add_f32 v255, v255, v53\n"
  "v_mfma_f32_16x16x32_bf16 v[64:67], a[128:131], a[104:107], v[64:67]\n"
  "s_addk_i32 m0, 896\n"
  "v_add_f32 v255, v255, v54\n"
  "v_add_f32 v255, v255, v55\n"
  "v_mfma_f32_16x16x32_bf16 v[64:67], a[132:135], a[108:111], v[64:67]\n"
  "s_waitcnt lgkmcnt(0)\n"
  "v_cvt_pk_bf16_f32 v14, v4, v5\n"
  "v_cvt_pk_bf16_f32 v15, v6, v7\n"
  "v_mfma_f32_16x16x32_bf16 v[64:67], a[136:139], a[112:115], v[64:67]\n"
  "v_cvt_pk_bf16_f32 v28, v16, v17\n"
  "v_cvt_pk_bf16_f32 v29, v18, v19\n"
  "v_mfma_f32_16x16x32_bf16 v[64:67], a[140:143], a[116:119], v[64:67]\n"
  "buffer_load_dwordx4 v221, s[40:43], 0 offen offset:256 lds\n"
  "v_cvt_pk_bf16_f32 v30, v20, v21\n"
  "v_mfma_f32_16x16x32_bf16 v[0:3], a[140:143], a[20:23], v[248:251]\n"
  "v_cvt_pk_bf16_f32 v31, v22, v23\n"
  "v_cvt_pk_bf16_f32 v44, v32, v33\n"
  "v_mfma_f32_16x16x32_bf16 v[0:3], a[136:139], a[16:19], v[0:3]\n"
  "v_cvt_pk_bf16_f32 v45, v34, v35\n"
  "v_cvt_pk_bf16_f32 v46, v36, v37\n"
  "v_mfma_f32_16x16x32_bf16 v[0:3], a[132:135], a[12:15], v[0:3]\n"
  "v_cvt_pk_bf16_f32 v47, v38, v39\n"
  "v_cvt_pk_bf16_f32 v60, v48, v49\n"
  "v_mfma_f32_16x16x32_bf16 v[0:3], a[128:131], a[8:11], v[0:3]\n"
  "v_cvt_pk_bf16_f32 v61, v50, v51\n"
  "v_cvt_pk_bf16_f32 v62, v52, v53\n"
  "v_mfma_f32_16x16x32_bf16 v[0:3], a[124:127], a[4:7], v[0:3]\n"
  "v_cvt_pk_bf16_f32 v63, v54, v55\n"
  "v_mfma_f32_16x16x32_bf16 v[0:3], a[120:123], a[0:3], v[0:3]\n"
  "s_add_u32 s40, s40, 24576\n"
  "s_addc_u32 s41, s41, 0\n"
  "s_branch L_BIG_%=\n"
  ".p2align 5\n"
  "L_TAIL_%=:\n"
  "ds_read_b64_tr_b16 a[120:121], v217 offset:26752\n"
  "ds_read_b64_tr_b16 a[122:123], v218 offset:26752\n"
  "v_exp_f32_e64 v55, v55\n"
  "v_cvt_pk_bf16_f32 v45, v34, v35\n"
  "ds_read_b64_tr_b16 a[124:125], v217 offset:26768\n"
  "ds_read_b64_tr_b16 a[126:127], v218 offset:26768\n"
  "v_cvt_pk_bf16_f32 v46, v36, v37\n"
  "v_cvt_pk_bf16_f32 v47, v38, v39\n"
  "v_cvt_pk_bf16_f32 v60, v48, v49\n"
  "s_waitcnt lgkmcnt(8)\n"
  "ds_read_b64_tr_b16 a[128:129], v219 offset:26752\n"
  "ds_read_b64_tr_b16 a[130:131], v220 offset:26752\n"
  "v_cvt_pk_bf16_f32 v61, v50, v51\n"
  "v_cvt_pk_bf16_f32 v62, v52, v53\n"
  "v_cvt_pk_bf16_f32 v63, v54, v55\n"
  "v_mfma_f32_16x16x32_bf16 v[84:87], a[156:159], a[152:155], v[84:87]\n"
  "s_nop 0\n"
  "ds_read_b64_tr_b16 a[132:133], v219 offset:26768\n"
  "ds_read_b64_tr_b16 a[134:135], v220 offset:26768\n"
  "v_add_u32 v233, s61, v229\n"
  "s_mov_b32 exec_lo, 0\n"
  "v_add_u32 v233, s69, v233\n"
  "s_mov_b32 exec_lo, -1\n"
  "v_mfma_f32_16x16x32_bf16 v[84:87], a[160:163], a[152:155], v[84:87]\n"
  "ds_read_b64_tr_b16 a[136:137], v217 offset:24704\n"
  "ds_read_b64_tr_b16 a[138:139], v218 offset:24704\n"
  "v_mfma_f32_16x16x32_bf16 v[68:71], v[12:15], a[152:155], v[68:71]\n"
  "ds_read_b64_tr_b16 a[140:141], v217 offset:25728\n"
  "ds_read_b64_tr_b16 a[142:143], v218 offset:25728\n"
  "v_mfma_f32_16x16x32_bf16 v[72:75], v[28:31], a[152:155], v[72:75]\n"
  "ds_read_b64_tr_b16 a[144:145], v217 offset:24720\n"
  "ds_read_b64_tr_b16 a[146:147], v218 offset:24720\n"
  "v_mfma_f32_16x16x32_bf16 v[76:79], v[44:47], a[152:155], v[76:79]\n"
  "ds_read_b64_tr_b16 a[148:149], v217 offset:25744\n"
  "ds_read_b64_tr_b16 a[150:151], v218 offset:25744\n"
  "v_mfma_f32_16x16x32_bf16 v[80:83], v[60:63], a[152:155], v[80:83]\n"
  "s_waitcnt lgkmcnt(12)\n"
  "v_maximum3_f32 v236, v68, v69, v70\n"
  "v_rcp_f32 v68, v68\n"
  "v_rcp_f32_e64 v69, v69\n"
  "v_mfma_f32_16x16x32_bf16 a[244:247], a[160:163], a[120:123], a[244:247]\n"
  "v_maximum3_f32 v236, v236, v71, v72\n"
  "v_rcp_f32 v70, v70\n"
  "v_rcp_f32 v71, v71\n"
  "v_mfma_f32_16x16x32_bf16 a[248:251], a[160:163], a[124:127], a[248:251]\n"
  "v_maximum3_f32 v236, v236, v73, v74\n"
  "v_rcp_f32 v72, v72\n"
  "v_rcp_f32 v73, v73\n"
  "v_mfma_f32_16x16x32_bf16 v[104:107], v[12:15], a[120:123], v[104:107]\n"
  "v_maximum3_f32 v236, v236, v75, v76\n"
  "v_rcp_f32 v74, v74\n"
  "v_rcp_f32 v75, v75\n"
  "v_mfma_f32_16x16x32_bf16 v[108:111], v[12:15], a[124:127], v[108:111]\n"
  "s_waitcnt lgkmcnt(8)\n"
  "v_maximum3_f32 v236, v236, v77, v78\n"
  "v_rcp_f32 v76, v76\n"
  "v_rcp_f32 v77, v77\n"
  "v_mfma_f32_16x16x32_bf16 v[112:115], v[12:15], a[128:131], v[112:115]\n"
  "v_maximum3_f32 v236, v236, v79, v80\n"
  "v_rcp_f32 v78, v78\n"
  "v_rcp_f32_e64 v79, v79\n"
  "v_mfma_f32_16x16x32_bf16 v[116:119], v[12:15], a[132:135], v[116:119]\n"
  "v_maximum3_f32 v236, v236, v81, v82\n"
  "v_rcp_f32 v80, v80\n"
  "v_rcp_f32 v81, v81\n"
  "v_pk_fma_f32 v[104:105], v[104:105], v[68:69], v[68:69] neg_lo:[0,0,1] neg_hi:[0,0,1]\n"
  "v_pk_fma_f32 v[106:107], v[106:107], v[70:71], v[70:71] neg_lo:[0,0,1] neg_hi:[0,0,1]\n"
  "v_mfma_f32_16x16x32_bf16 v[136:139], v[28:31], a[120:123], v[136:139]\n"
  "v_maximum3_f32 v236, v236, v83, v84\n"
  "v_rcp_f32 v82, v82\n"
  "v_rcp_f32 v83, v83\n"
  "v_pk_fma_f32 v[108:109], v[108:109], v[68:69], v[68:69] neg_lo:[0,0,1] neg_hi:[0,0,1]\n"
  "v_pk_fma_f32 v[110:111], v[110:111], v[70:71], v[70:71] neg_lo:[0,0,1] neg_hi:[0,0,1]\n"
  "v_mfma_f32_16x16x32_bf16 v[140:143], v[28:31], a[124:127], v[140:143]\n"
  "v_cvt_pk_bf16_f32 v104, v104, v108\n"
  "v_cvt_pk_bf16_f32 v108, v105, v109\n"
  "v_cvt_pk_bf16_f32 v106, v106, v110\n"
  "v_cvt_pk_bf16_f32 v110, v107, v111\n"
  "v_maximum3_f32 v236, v236, v85, v86\n"
  "v_pk_fma_f32 v[112:113], v[112:113], v[68:69], v[68:69] neg_lo:[0,0,1] neg_hi:[0,0,1]\n"
  "v_pk_fma_f32 v[114:115], v[114:115], v[70:71], v[70:71] neg_lo:[0,0,1] neg_hi:[0,0,1]\n"
  "v_mfma_f32_16x16x32_bf16 v[144:147], v[28:31], a[128:131], v[144:147]\n"
  "v_maximum3_f32 v236, v236, v87, v87\n"
  "v_rcp_f32_e64 v84, v84\n"
  "v_rcp_f32_e64 v85, v85\n"
  "v_pk_fma_f32 v[116:117], v[116:117], v[68:69], v[68:69] neg_lo:[0,0,1] neg_hi:[0,0,1]\n"
  "v_pk_fma_f32 v[118:119], v[118:119], v[70:71], v[70:71] neg_lo:[0,0,1] neg_hi:[0,0,1]\n"
  "v_mfma_f32_16x16x32_bf16 v[148:151], v[28:31], a[132:135], v[148:151]\n"
  "v_cvt_pk_bf16_f32 v105, v112, v116\n"
  "v_cvt_pk_bf16_f32 v109, v113, v117\n"
  "v_cvt_pk_bf16_f32 v107, v114, v118\n"
  "v_cvt_pk_bf16_f32 v111, v115, v119\n"
  "v_cmp_ngt_f32_e64 vcc, s75, v236\n"
  "v_pk_fma_f32 v[136:137], v[136:137], v[72:73], v[72:73] neg_lo:[0,0,1] neg_hi:[0,0,1]\n"
  "v_pk_fma_f32 v[138:139], v[138:139], v[74:75], v[74:75] neg_lo:[0,0,1] neg_hi:[0,0,1]\n"
  "v_mfma_f32_16x16x32_bf16 v[168:171], v[44:47], a[120:123], v[168:171]\n"
  "ds_write_b64 v229, v[104:105] offset:8960\n"
  "v_rcp_f32_e64 v86, v86\n"
  "v_pk_fma_f32 v[140:141], v[140:141], v[72:73], v[72:73] neg_lo:[0,0,1] neg_hi:[0,0,1]\n"
  "v_pk_fma_f32 v[142:143], v[142:143], v[74:75], v[74:75] neg_lo:[0,0,1] neg_hi:[0,0,1]\n"
  "v_mfma_f32_16x16x32_bf16 v[172:175], v[44:47], a[124:127], v[172:175]\n"
  "s_cmp_lg_u64 vcc, 0\n"
  "s_cselect_b32 s79, s77, 0\n"
  "s_or_b32 s78, s78, s79\n"
  "s_lshl_b32 s77, s77, 1\n"
  "ds_write_b64 v229, v[108:109] offset:9248\n"
  "v_cvt_pk_bf16_f32 v136, v136, v140\n"
  "v_cvt_pk_bf16_f32 v140, v137, v141\n"
  "v_pk_fma_f32 v[144:145], v[144:145], v[72:73], v[72:73] neg_lo:[0,0,1] neg_hi:[0,0,1]\n"
  "v_pk_fma_f32 v[146:147], v[146:147], v[74:75], v[74:75] neg_lo:[0,0,1] neg_hi:[0,0,1]\n"
  "v_mfma_f32_16x16x32_bf16 v[176:179], v[44:47], a[128:131], v[176:179]\n"
  "ds_write_b64 v229, v[106:107] offset:9536\n"
  "v_cvt_pk_bf16_f32 v138, v138, v142\n"
  "v_cvt_pk_bf16_f32 v142, v139, v143\n"
  "v_pk_fma_f32 v[148:149], v[148:149], v[72:73], v[72:73] neg_lo:[0,0,1] neg_hi:[0,0,1]\n"
  "v_pk_fma_f32 v[150:151], v[150:151], v[74:75], v[74:75] neg_lo:[0,0,1] neg_hi:[0,0,1]\n"
  "v_mfma_f32_16x16x32_bf16 v[180:183], v[44:47], a[132:135], v[180:183]\n"
  "ds_write_b64 v229, v[110:111] offset:9824\n"
  "v_cvt_pk_bf16_f32 v137, v144, v148\n"
  "v_cvt_pk_bf16_f32 v141, v145, v149\n"
  "v_pk_fma_f32 v[168:169], v[168:169], v[76:77], v[76:77] neg_lo:[0,0,1] neg_hi:[0,0,1]\n"
  "v_pk_fma_f32 v[170:171], v[170:171], v[78:79], v[78:79] neg_lo:[0,0,1] neg_hi:[0,0,1]\n"
  "v_mfma_f32_16x16x32_bf16 v[200:203], v[60:63], a[120:123], v[200:203]\n"
  "ds_read_b64_tr_b16 a[120:121], v219 offset:24704\n"
  "ds_read_b64_tr_b16 a[122:123], v220 offset:24704\n"
  "ds_write_b64 v229, v[136:137] offset:11200\n"
  "v_pk_fma_f32 v[172:173], v[172:173], v[76:77], v[76:77] neg_lo:[0,0,1] neg_hi:[0,0,1]\n"
  "v_pk_fma_f32 v[174:175], v[174:175], v[78:79], v[78:79] neg_lo:[0,0,1] neg_hi:[0,0,1]\n"
  "v_mfma_f32_16x16x32_bf16 v[204:207], v[60:63], a[124:127], v[204:207]\n"
  "ds_read_b64_tr_b16 a[124:125], v219 offset:25728\n"
  "ds_read_b64_tr_b16 a[126:127], v220 offset:25728\n"
  "ds_write_b64 v229, v[140:141] offset:11488\n"
  "v_pk_fma_f32 v[176:177], v[176:177], v[76:77], v[76:77] neg_lo:[0,0,1] neg_hi:[0,0,1]\n"
  "v_pk_fma_f32 v[178:179], v[178:179], v[78:79], v[78:79] neg_lo:[0,0,1] neg_hi:[0,0,1]\n"
  "v_mfma_f32_16x16x32_bf16 v[208:211], v[60:63], a[128:131], v[208:211]\n"
  "ds_read_b64_tr_b16 a[128:129], v219 offset:24720\n"
  "ds_read_b64_tr_b16 a[130:131], v220 offset:24720\n"
  "ds_read_b64_tr_b16 v[244:245], v230 offset:8960\n"
  "ds_read_b64_tr_b16 v[246:247], v230 offset:8992\n"
  "v_cvt_pk_bf16_f32 v139, v146, v150\n"
  "v_pk_fma_f32 v[180:181], v[180:181], v[76:77], v[76:77] neg_lo:[0,0,1] neg_hi:[0,0,1]\n"
  "v_pk_fma_f32 v[182:183], v[182:183], v[78:79], v[78:79] neg_lo:[0,0,1] neg_hi:[0,0,1]\n"
  "v_mfma_f32_16x16x32_bf16 v[212:215], v[60:63], a[132:135], v[212:215]\n"
  "s_nop 0\n"
  "s_waitcnt lgkmcnt(15)\n"
  "ds_read_b64_tr_b16 a[132:133], v219 offset:25744\n"
  "ds_read_b64_tr_b16 a[134:135], v220 offset:25744\n"
  "ds_write_b64 v229, v[138:139] offset:11776\n"
  "v_pk_fma_f32 v[200:201], v[200:201], v[80:81], v[80:81] neg_lo:[0,0,1] neg_hi:[0,0,1]\n"
  "v_pk_fma_f32 v[202:203], v[202:203], v[82:83], v[82:83] neg_lo:[0,0,1] neg_hi:[0,0,1]\n"
  "v_mfma_f32_16x16x32_bf16 a[228:231], a[156:159], a[136:139], a[228:231]\n"
  "ds_read_b64_tr_b16 a[252:253], v230 offset:10080\n"
  "ds_read_b64_tr_b16 a[254:255], v230 offset:10112\n"
  "v_cvt_pk_bf16_f32 v143, v147, v151\n"
  "v_rcp_f32 v87, v87\n"
  "v_pk_fma_f32 v[204:205], v[204:205], v[80:81], v[80:81] neg_lo:[0,0,1] neg_hi:[0,0,1]\n"
  "v_pk_fma_f32 v[206:207], v[206:207], v[82:83], v[82:83] neg_lo:[0,0,1] neg_hi:[0,0,1]\n"
  "v_mfma_f32_16x16x32_bf16 a[228:231], a[160:163], a[140:143], a[228:231]\n"
  "s_waitcnt lgkmcnt(15)\n"
  "ds_write_b64 v229, v[142:143] offset:12064\n"
  "v_cvt_pk_bf16_f32 v168, v168, v172\n"
  "v_cvt_pk_bf16_f32 v172, v169, v173\n"
  "v_pk_fma_f32 v[208:209], v[208:209], v[80:81], v[80:81] neg_lo:[0,0,1] neg_hi:[0,0,1]\n"
  "v_pk_fma_f32 v[210:211], v[210:211], v[82:83], v[82:83] neg_lo:[0,0,1] neg_hi:[0,0,1]\n"
  "v_mfma_f32_16x16x32_bf16 a[232:235], a[156:159], a[144:147], a[232:235]\n"
  "v_cvt_pk_bf16_f32 v169, v176, v180\n"
  "v_cvt_pk_bf16_f32 v173, v177, v181\n"
  "v_cvt_pk_bf16_f32 v170, v170, v174\n"
  "v_cvt_pk_bf16_f32 v174, v171, v175\n"
  "v_cvt_pk_bf16_f32 v171, v178, v182\n"
  "v_pk_fma_f32 v[212:213], v[212:213], v[80:81], v[80:81] neg_lo:[0,0,1] neg_hi:[0,0,1]\n"
  "v_pk_fma_f32 v[214:215], v[214:215], v[82:83], v[82:83] neg_lo:[0,0,1] neg_hi:[0,0,1]\n"
  "v_mfma_f32_16x16x32_bf16 a[232:235], a[160:163], a[148:151], a[232:235]\n"
  "ds_read_b64_tr_b16 v[0:1], v230 offset:11200\n"
  "ds_read_b64_tr_b16 v[2:3], v230 offset:11232\n"
  "ds_write_b64 v229, v[168:169] offset:13440\n"
  "v_mfma_f32_16x16x32_bf16 a[164:167], v[8:11], a[136:139], a[164:167]\n"
  "s_waitcnt lgkmcnt(9)\n"
  "buffer_store_dwordx4 v[244:247], v228, s[52:55], s80 offen offset:256 nt\n"
  "ds_write_b64 v229, v[172:173] offset:13728\n"
  "v_cvt_pk_bf16_f32 v175, v179, v183\n"
  "v_mfma_f32_16x16x32_bf16 a[164:167], v[12:15], a[140:143], a[164:167]\n"
  "ds_read_b64_tr_b16 v[4:5], v230 offset:12320\n"
  "ds_read_b64_tr_b16 v[6:7], v230 offset:12352\n"
  "ds_write_b64 v229, v[170:171] offset:14016\n"
  "v_mfma_f32_16x16x32_bf16 a[168:171], v[8:11], a[144:147], a[168:171]\n"
  "s_waitcnt lgkmcnt(8)\n"
  "buffer_store_dwordx4 a[252:255], v228, s[52:55], s80 offen offset:3328 nt\n"
  "ds_write_b64 v229, v[174:175] offset:14304\n"
  "v_cvt_pk_bf16_f32 v200, v200, v204\n"
  "v_mfma_f32_16x16x32_bf16 a[168:171], v[12:15], a[148:151], a[168:171]\n"
  "v_cvt_pk_bf16_f32 v204, v201, v205\n"
  "v_cvt_pk_bf16_f32 v201, v208, v212\n"
  "v_cvt_pk_bf16_f32 v205, v209, v213\n"
  "v_cvt_pk_bf16_f32 v202, v202, v206\n"
  "v_cvt_pk_bf16_f32 v206, v203, v207\n"
  "v_mfma_f32_16x16x32_bf16 a[180:183], v[24:27], a[136:139], a[180:183]\n"
  "ds_read_b64_tr_b16 v[16:17], v230 offset:13440\n"
  "ds_read_b64_tr_b16 v[18:19], v230 offset:13472\n"
  "ds_write_b64 v229, v[200:201] offset:15680\n"
  "v_mfma_f32_16x16x32_bf16 a[180:183], v[28:31], a[140:143], a[180:183]\n"
  "s_waitcnt lgkmcnt(9)\n"
  "buffer_store_dwordx4 v[0:3], v228, s[52:55], s81 offen offset:256 nt\n"
  "ds_write_b64 v229, v[204:205] offset:15968\n"
  "v_cvt_pk_bf16_f32 v203, v210, v214\n"
  "v_mfma_f32_16x16x32_bf16 a[184:187], v[24:27], a[144:147], a[184:187]\n"
  "ds_read_b64_tr_b16 v[20:21], v230 offset:14560\n"
  "ds_write_b64 v229, v[202:203] offset:16256\n"
  "v_cvt_pk_bf16_f32 v207, v211, v215\n"
  "v_mfma_f32_16x16x32_bf16 a[184:187], v[28:31], a[148:151], a[184:187]\n"
  "s_waitcnt lgkmcnt(8)\n"
  "buffer_store_dwordx4 v[4:7], v228, s[52:55], s81 offen offset:3328 nt\n"
  "ds_read_b64_tr_b16 v[22:23], v230 offset:14592\n"
  "ds_write_b64 v229, v[206:207] offset:16544\n"
  "v_mfma_f32_16x16x32_bf16 a[196:199], v[40:43], a[136:139], a[196:199]\n"
  "v_accvgpr_read_b32 v104, a164\n"
  "v_accvgpr_read_b32 v105, a165\n"
  "v_accvgpr_read_b32 v108, a168\n"
  "v_accvgpr_read_b32 v109, a169\n"
  "v_accvgpr_read_b32 v106, a166\n"
  "v_pk_fma_f32 v[104:105], v[104:105], v[68:69], v[68:69] neg_lo:[0,0,1] neg_hi:[0,0,1]\n"
  "v_pk_fma_f32 v[108:109], v[108:109], v[68:69], v[68:69] neg_lo:[0,0,1] neg_hi:[0,0,1]\n"
  "v_mfma_f32_16x16x32_bf16 a[196:199], v[44:47], a[140:143], a[196:199]\n"
  "ds_read_b64_tr_b16 v[244:245], v230 offset:15680\n"
  "ds_read_b64_tr_b16 v[246:247], v230 offset:15712\n"
  "v_accvgpr_read_b32 v107, a167\n"
  "v_accvgpr_read_b32 v110, a170\n"
  "v_accvgpr_read_b32 v111, a171\n"
  "v_pk_fma_f32 v[106:107], v[106:107], v[70:71], v[70:71] neg_lo:[0,0,1] neg_hi:[0,0,1]\n"
  "v_pk_fma_f32 v[110:111], v[110:111], v[70:71], v[70:71] neg_lo:[0,0,1] neg_hi:[0,0,1]\n"
  "v_mfma_f32_16x16x32_bf16 a[200:203], v[40:43], a[144:147], a[200:203]\n"
  "s_nop 0\n"
  "s_waitcnt lgkmcnt(8)\n"
  "buffer_store_dwordx4 v[16:19], v228, s[52:55], s82 offen offset:256 nt\n"
  "v_cvt_pk_bf16_f32 v104, v104, v108\n"
  "v_cvt_pk_bf16_f32 v108, v105, v109\n"
  "v_cvt_pk_bf16_f32 v106, v106, v110\n"
  "v_cvt_pk_bf16_f32 v110, v107, v111\n"
  "v_mfma_f32_16x16x32_bf16 a[200:203], v[44:47], a[148:151], a[200:203]\n"
  "ds_read_b64_tr_b16 a[252:253], v230 offset:16800\n"
  "ds_read_b64_tr_b16 a[254:255], v230 offset:16832\n"
  "v_accvgpr_read_b32 v136, a180\n"
  "v_accvgpr_read_b32 v137, a181\n"
  "v_accvgpr_read_b32 v140, a184\n"
  "v_pk_fma_f32 v[136:137], v[136:137], v[72:73], v[72:73] neg_lo:[0,0,1] neg_hi:[0,0,1]\n"
  "v_mfma_f32_16x16x32_bf16 a[212:215], v[56:59], a[136:139], a[212:215]\n"
  "v_accvgpr_read_b32 v141, a185\n"
  "v_accvgpr_read_b32 v138, a182\n"
  "v_accvgpr_read_b32 v139, a183\n"
  "v_accvgpr_read_b32 v142, a186\n"
  "v_accvgpr_read_b32 v143, a187\n"
  "v_pk_fma_f32 v[140:141], v[140:141], v[72:73], v[72:73] neg_lo:[0,0,1] neg_hi:[0,0,1]\n"
  "v_pk_fma_f32 v[138:139], v[138:139], v[74:75], v[74:75] neg_lo:[0,0,1] neg_hi:[0,0,1]\n"
  "v_pk_fma_f32 v[142:143], v[142:143], v[74:75], v[74:75] neg_lo:[0,0,1] neg_hi:[0,0,1]\n"
  "v_mfma_f32_16x16x32_bf16 a[212:215], v[60:63], a[140:143], a[212:215]\n"
  "s_nop 0\n"
  "s_waitcnt lgkmcnt(5)\n"
  "buffer_store_dwordx4 v[20:23], v228, s[52:55], s82 offen offset:3328 nt\n"
  "v_cvt_pk_bf16_f32 v136, v136, v140\n"
  "v_cvt_pk_bf16_f32 v140, v137, v141\n"
  "v_cvt_pk_bf16_f32 v138, v138, v142\n"
  "v_cvt_pk_bf16_f32 v142, v139, v143\n"
  "v_mfma_f32_16x16x32_bf16 a[216:219], v[56:59], a[144:147], a[216:219]\n"
  "v_accvgpr_read_b32 v168, a196\n"
  "v_accvgpr_read_b32 v169, a197\n"
  "v_accvgpr_read_b32 v172, a200\n"
  "v_accvgpr_read_b32 v173, a201\n"
  "v_accvgpr_read_b32 v170, a198\n"
  "v_pk_fma_f32 v[168:169], v[168:169], v[76:77], v[76:77] neg_lo:[0,0,1] neg_hi:[0,0,1]\n"
  "v_pk_fma_f32 v[172:173], v[172:173], v[76:77], v[76:77] neg_lo:[0,0,1] neg_hi:[0,0,1]\n"
  "v_mfma_f32_16x16x32_bf16 a[216:219], v[60:63], a[148:151], a[216:219]\n"
  "s_nop 0\n"
  "s_waitcnt lgkmcnt(2)\n"
  "buffer_store_dwordx4 v[244:247], v228, s[52:55], s83 offen offset:256 nt\n"
  "v_accvgpr_read_b32 v171, a199\n"
  "v_accvgpr_read_b32 v174, a202\n"
  "v_accvgpr_read_b32 v175, a203\n"
  "v_cvt_pk_bf16_f32 v168, v168, v172\n"
  "v_pk_fma_f32 v[170:171], v[170:171], v[78:79], v[78:79] neg_lo:[0,0,1] neg_hi:[0,0,1]\n"
  "v_pk_fma_f32 v[174:175], v[174:175], v[78:79], v[78:79] neg_lo:[0,0,1] neg_hi:[0,0,1]\n"
  "v_mfma_f32_16x16x32_bf16 a[172:175], v[8:11], a[120:123], a[172:175]\n"
  "s_waitcnt vmcnt(10)\n"
  "s_barrier\n"
  "ds_read_b64_tr_b16 a[136:137], v217 offset:0\n"
  "ds_read_b64_tr_b16 a[138:139], v218 offset:0\n"
  "v_cvt_pk_bf16_f32 v172, v169, v173\n"
  "v_mfma_f32_16x16x32_bf16 a[172:175], v[12:15], a[124:127], a[172:175]\n"
  "s_nop 0\n"
  "s_waitcnt lgkmcnt(2)\n"
  "buffer_store_dwordx4 a[252:255], v228, s[52:55], s83 offen offset:3328 nt\n"
  "ds_read_b64_tr_b16 a[140:141], v217 offset:16\n"
  "ds_read_b64_tr_b16 a[142:143], v218 offset:16\n"
  "v_cvt_pk_bf16_f32 v170, v170, v174\n"
  "v_cvt_pk_bf16_f32 v174, v171, v175\n"
  "v_mfma_f32_16x16x32_bf16 a[176:179], v[8:11], a[128:131], a[176:179]\n"
  "ds_read_b64_tr_b16 a[144:145], v219 offset:0\n"
  "ds_read_b64_tr_b16 a[146:147], v220 offset:0\n"
  "v_accvgpr_read_b32 v240, a244\n"
  "v_accvgpr_read_b32 v241, a245\n"
  "v_accvgpr_read_b32 v242, a248\n"
  "v_pk_fma_f32 v[240:241], v[240:241], v[84:85], v[84:85] neg_lo:[0,0,1] neg_hi:[0,0,1]\n"
  "v_mfma_f32_16x16x32_bf16 a[176:179], v[12:15], a[132:135], a[176:179]\n"
  "ds_read_b64_tr_b16 a[148:149], v219 offset:16\n"
  "ds_read_b64_tr_b16 a[150:151], v220 offset:16\n"
  "v_accvgpr_read_b32 v243, a249\n"
  "v_accvgpr_read_b32 v236, a246\n"
  "v_accvgpr_read_b32 v237, a247\n"
  "v_pk_fma_f32 v[242:243], v[242:243], v[84:85], v[84:85] neg_lo:[0,0,1] neg_hi:[0,0,1]\n"
  "v_pk_fma_f32 v[236:237], v[236:237], v[86:87], v[86:87] neg_lo:[0,0,1] neg_hi:[0,0,1]\n"
  "v_mfma_f32_16x16x32_bf16 a[188:191], v[24:27], a[120:123], a[188:191]\n"
  "v_accvgpr_read_b32 v112, a172\n"
  "v_accvgpr_read_b32 v113, a173\n"
  "v_accvgpr_read_b32 v114, a174\n"
  "v_accvgpr_read_b32 v115, a175\n"
  "v_accvgpr_read_b32 v238, a250\n"
  "v_pk_fma_f32 v[112:113], v[112:113], v[68:69], v[68:69] neg_lo:[0,0,1] neg_hi:[0,0,1]\n"
  "v_pk_fma_f32 v[114:115], v[114:115], v[70:71], v[70:71] neg_lo:[0,0,1] neg_hi:[0,0,1]\n"
  "v_mfma_f32_16x16x32_bf16 a[188:191], v[28:31], a[124:127], a[188:191]\n"
  "v_accvgpr_read_b32 v239, a251\n"
  "v_accvgpr_read_b32 v200, a212\n"
  "v_accvgpr_read_b32 v201, a213\n"
  "v_accvgpr_read_b32 v204, a216\n"
  "v_accvgpr_read_b32 v205, a217\n"
  "v_pk_fma_f32 v[238:239], v[238:239], v[86:87], v[86:87] neg_lo:[0,0,1] neg_hi:[0,0,1]\n"
  "v_pk_fma_f32 v[200:201], v[200:201], v[80:81], v[80:81] neg_lo:[0,0,1] neg_hi:[0,0,1]\n"
  "v_pk_fma_f32 v[204:205], v[204:205], v[80:81], v[80:81] neg_lo:[0,0,1] neg_hi:[0,0,1]\n"
  "v_mfma_f32_16x16x32_bf16 a[192:195], v[24:27], a[128:131], a[192:195]\n"
  "v_accvgpr_read_b32 v116, a176\n"
  "v_accvgpr_read_b32 v117, a177\n"
  "v_accvgpr_read_b32 v118, a178\n"
  "v_accvgpr_read_b32 v119, a179\n"
  "v_accvgpr_read_b32 v202, a214\n"
  "v_pk_fma_f32 v[116:117], v[116:117], v[68:69], v[68:69] neg_lo:[0,0,1] neg_hi:[0,0,1]\n"
  "v_pk_fma_f32 v[118:119], v[118:119], v[70:71], v[70:71] neg_lo:[0,0,1] neg_hi:[0,0,1]\n"
  "v_mfma_f32_16x16x32_bf16 a[192:195], v[28:31], a[132:135], a[192:195]\n"
  "v_cvt_pk_bf16_f32 v105, v112, v116\n"
  "v_cvt_pk_bf16_f32 v109, v113, v117\n"
  "v_cvt_pk_bf16_f32 v107, v114, v118\n"
  "v_cvt_pk_bf16_f32 v111, v115, v119\n"
  "v_accvgpr_read_b32 v203, a215\n"
  "v_pk_fma_f32 v[202:203], v[202:203], v[82:83], v[82:83] neg_lo:[0,0,1] neg_hi:[0,0,1]\n"
  "v_mfma_f32_16x16x32_bf16 a[204:207], v[40:43], a[120:123], a[204:207]\n"
  "ds_write_b64 v229, v[104:105] offset:0\n"
  "v_accvgpr_read_b32 v144, a188\n"
  "v_accvgpr_read_b32 v145, a189\n"
  "v_pk_fma_f32 v[144:145], v[144:145], v[72:73], v[72:73] neg_lo:[0,0,1] neg_hi:[0,0,1]\n"
  "v_mfma_f32_16x16x32_bf16 a[204:207], v[44:47], a[124:127], a[204:207]\n"
  "ds_write_b64 v229, v[108:109] offset:288\n"
  "v_accvgpr_read_b32 v146, a190\n"
  "v_accvgpr_read_b32 v147, a191\n"
  "v_pk_fma_f32 v[146:147], v[146:147], v[74:75], v[74:75] neg_lo:[0,0,1] neg_hi:[0,0,1]\n"
  "v_mfma_f32_16x16x32_bf16 a[208:211], v[40:43], a[128:131], a[208:211]\n"
  "ds_write_b64 v229, v[106:107] offset:576\n"
  "ds_write_b64 v229, v[110:111] offset:864\n"
  "v_mfma_f32_16x16x32_bf16 a[208:211], v[44:47], a[132:135], a[208:211]\n"
  "v_accvgpr_read_b32 v148, a192\n"
  "v_accvgpr_read_b32 v149, a193\n"
  "v_accvgpr_read_b32 v150, a194\n"
  "v_accvgpr_read_b32 v151, a195\n"
  "v_accvgpr_read_b32 v206, a218\n"
  "v_pk_fma_f32 v[148:149], v[148:149], v[72:73], v[72:73] neg_lo:[0,0,1] neg_hi:[0,0,1]\n"
  "v_pk_fma_f32 v[150:151], v[150:151], v[74:75], v[74:75] neg_lo:[0,0,1] neg_hi:[0,0,1]\n"
  "v_mfma_f32_16x16x32_bf16 a[220:223], v[56:59], a[120:123], a[220:223]\n"
  "ds_read_b64_tr_b16 v[0:1], v230 offset:0\n"
  "ds_read_b64_tr_b16 v[2:3], v230 offset:32\n"
  "v_cvt_pk_bf16_f32 v137, v144, v148\n"
  "v_cvt_pk_bf16_f32 v141, v145, v149\n"
  "v_cvt_pk_bf16_f32 v139, v146, v150\n"
  "v_mfma_f32_16x16x32_bf16 a[220:223], v[60:63], a[124:127], a[220:223]\n"
  "ds_read_b64_tr_b16 v[4:5], v230 offset:1120\n"
  "ds_write_b64 v229, v[136:137] offset:2240\n"
  "v_cvt_pk_bf16_f32 v143, v147, v151\n"
  "v_mfma_f32_16x16x32_bf16 a[224:227], v[56:59], a[128:131], a[224:227]\n"
  "ds_read_b64_tr_b16 v[6:7], v230 offset:1152\n"
  "ds_write_b64 v229, v[140:141] offset:2528\n"
  "v_accvgpr_read_b32 v176, a204\n"
  "v_mfma_f32_16x16x32_bf16 a[224:227], v[60:63], a[132:135], a[224:227]\n"
  "s_nop 0\n"
  "s_waitcnt lgkmcnt(14)\n"
  "ds_write_b64 v229, v[138:139] offset:2816\n"
  "ds_write_b64 v229, v[142:143] offset:3104\n"
  "v_mfma_f32_16x16x32_bf16 a[236:239], a[160:163], a[136:139], a[236:239]\n"
  "v_accvgpr_read_b32 v177, a205\n"
  "v_accvgpr_read_b32 v180, a208\n"
  "v_accvgpr_read_b32 v181, a209\n"
  "v_accvgpr_read_b32 v178, a206\n"
  "v_accvgpr_read_b32 v179, a207\n"
  "v_pk_fma_f32 v[176:177], v[176:177], v[76:77], v[76:77] neg_lo:[0,0,1] neg_hi:[0,0,1]\n"
  "v_pk_fma_f32 v[180:181], v[180:181], v[76:77], v[76:77] neg_lo:[0,0,1] neg_hi:[0,0,1]\n"
  "v_pk_fma_f32 v[178:179], v[178:179], v[78:79], v[78:79] neg_lo:[0,0,1] neg_hi:[0,0,1]\n"
  "v_mfma_f32_16x16x32_bf16 a[240:243], a[160:163], a[140:143], a[240:243]\n"
  "ds_read_b64_tr_b16 v[16:17], v230 offset:2240\n"
  "ds_read_b64_tr_b16 v[18:19], v230 offset:2272\n"
  "v_accvgpr_read_b32 v182, a210\n"
  "v_accvgpr_read_b32 v183, a211\n"
  "v_cvt_pk_bf16_f32 v169, v176, v180\n"
  "v_pk_fma_f32 v[182:183], v[182:183], v[78:79], v[78:79] neg_lo:[0,0,1] neg_hi:[0,0,1]\n"
  "v_mfma_f32_16x16x32_bf16 v[88:91], v[12:15], a[136:139], v[88:91]\n"
  "s_waitcnt lgkmcnt(8)\n"
  "buffer_store_dwordx4 v[0:3], v228, s[52:55], s80 offen offset:0 nt\n"
  "ds_write_b64 v229, v[168:169] offset:4480\n"
  "v_cvt_pk_bf16_f32 v173, v177, v181\n"
  "v_mfma_f32_16x16x32_bf16 v[92:95], v[12:15], a[140:143], v[92:95]\n"
  "ds_write_b64 v229, v[172:173] offset:4768\n"
  "v_cvt_pk_bf16_f32 v171, v178, v182\n"
  "v_cvt_pk_bf16_f32 v175, v179, v183\n"
  "v_mfma_f32_16x16x32_bf16 v[96:99], v[12:15], a[144:147], v[96:99]\n"
  "s_waitcnt lgkmcnt(7)\n"
  "buffer_store_dwordx4 v[4:7], v228, s[52:55], s80 offen offset:3072 nt\n"
  "ds_read_b64_tr_b16 v[20:21], v230 offset:3360\n"
  "ds_read_b64_tr_b16 v[22:23], v230 offset:3392\n"
  "ds_write_b64 v229, v[170:171] offset:5056\n"
  "ds_write_b64 v229, v[174:175] offset:5344\n"
  "v_mfma_f32_16x16x32_bf16 v[100:103], v[12:15], a[148:151], v[100:103]\n"
  "v_accvgpr_read_b32 v208, a220\n"
  "v_accvgpr_read_b32 v209, a221\n"
  "v_accvgpr_read_b32 v212, a224\n"
  "v_accvgpr_read_b32 v213, a225\n"
  "v_accvgpr_read_b32 v207, a219\n"
  "v_pk_fma_f32 v[208:209], v[208:209], v[80:81], v[80:81] neg_lo:[0,0,1] neg_hi:[0,0,1]\n"
  "v_pk_fma_f32 v[212:213], v[212:213], v[80:81], v[80:81] neg_lo:[0,0,1] neg_hi:[0,0,1]\n"
  "v_pk_fma_f32 v[206:207], v[206:207], v[82:83], v[82:83] neg_lo:[0,0,1] neg_hi:[0,0,1]\n"
  "v_pk_fma_f32 v[88:89], v[88:89], v[68:69], v[68:69] neg_lo:[0,0,1] neg_hi:[0,0,1]\n"
  "v_mfma_f32_16x16x32_bf16 v[120:123], v[28:31], a[136:139], v[120:123]\n"
  "ds_read_b64_tr_b16 v[244:245], v230 offset:4480\n"
  "ds_read_b64_tr_b16 v[246:247], v230 offset:4512\n"
  "v_accvgpr_read_b32 v210, a222\n"
  "v_accvgpr_read_b32 v211, a223\n"
  "v_accvgpr_read_b32 v214, a226\n"
  "v_pk_fma_f32 v[210:211], v[210:211], v[82:83], v[82:83] neg_lo:[0,0,1] neg_hi:[0,0,1]\n"
  "v_pk_fma_f32 v[92:93], v[92:93], v[68:69], v[68:69] neg_lo:[0,0,1] neg_hi:[0,0,1]\n"
  "v_pk_fma_f32 v[90:91], v[90:91], v[70:71], v[70:71] neg_lo:[0,0,1] neg_hi:[0,0,1]\n"
  "v_pk_fma_f32 v[94:95], v[94:95], v[70:71], v[70:71] neg_lo:[0,0,1] neg_hi:[0,0,1]\n"
  "v_mfma_f32_16x16x32_bf16 v[124:127], v[28:31], a[140:143], v[124:127]\n"
  "s_nop 0\n"
  "s_waitcnt lgkmcnt(8)\n"
  "buffer_store_dwordx4 v[16:19], v228, s[52:55], s81 offen offset:0 nt\n"
  "v_accvgpr_read_b32 v215, a227\n"
  "v_cvt_pk_bf16_f32 v240, v240, v242\n"
  "v_cvt_pk_bf16_f32 v241, v241, v243\n"
  "v_pk_fma_f32 v[214:215], v[214:215], v[82:83], v[82:83] neg_lo:[0,0,1] neg_hi:[0,0,1]\n"
  "v_pk_fma_f32 v[96:97], v[96:97], v[68:69], v[68:69] neg_lo:[0,0,1] neg_hi:[0,0,1]\n"
  "v_pk_fma_f32 v[98:99], v[98:99], v[70:71], v[70:71] neg_lo:[0,0,1] neg_hi:[0,0,1]\n"
  "v_mfma_f32_16x16x32_bf16 v[128:131], v[28:31], a[144:147], v[128:131]\n"
  "v_cvt_pk_bf16_f32 v236, v236, v238\n"
  "v_cvt_pk_bf16_f32 v237, v237, v239\n"
  "v_cvt_pk_bf16_f32 v200, v200, v204\n"
  "v_cvt_pk_bf16_f32 v204, v201, v205\n"
  "v_cvt_pk_bf16_f32 v201, v208, v212\n"
  "v_cvt_pk_bf16_f32 v205, v209, v213\n"
  "v_cvt_pk_bf16_f32 v202, v202, v206\n"
  "v_cvt_pk_bf16_f32 v206, v203, v207\n"
  "v_cvt_pk_bf16_f32 v203, v210, v214\n"
  "v_cvt_pk_bf16_f32 v207, v211, v215\n"
  "v_pk_fma_f32 v[100:101], v[100:101], v[68:69], v[68:69] neg_lo:[0,0,1] neg_hi:[0,0,1]\n"
  "v_pk_fma_f32 v[102:103], v[102:103], v[70:71], v[70:71] neg_lo:[0,0,1] neg_hi:[0,0,1]\n"
  "v_mfma_f32_16x16x32_bf16 v[132:135], v[28:31], a[148:151], v[132:135]\n"
  "ds_read_b64_tr_b16 a[252:253], v230 offset:5600\n"
  "ds_read_b64_tr_b16 a[254:255], v230 offset:5632\n"
  "ds_write_b32 v233, v240 offset:20160\n"
  "ds_write_b32 v233, v241 offset:20448\n"
  "ds_write_b32 v233, v236 offset:20736\n"
  "ds_write_b32 v233, v237 offset:21024\n"
  "ds_write_b64 v229, v[200:201] offset:6720\n"
  "ds_write_b64 v229, v[204:205] offset:7008\n"
  "ds_write_b64 v229, v[202:203] offset:7296\n"
  "ds_write_b64 v229, v[206:207] offset:7584\n"
  "v_pk_fma_f32 v[120:121], v[120:121], v[72:73], v[72:73] neg_lo:[0,0,1] neg_hi:[0,0,1]\n"
  "v_pk_fma_f32 v[122:123], v[122:123], v[74:75], v[74:75] neg_lo:[0,0,1] neg_hi:[0,0,1]\n"
  "v_mfma_f32_16x16x32_bf16 v[152:155], v[44:47], a[136:139], v[152:155]\n"
  "s_nop 0\n"
  "s_waitcnt lgkmcnt(14)\n"
  "buffer_store_dwordx4 v[20:23], v228, s[52:55], s81 offen offset:3072 nt\n"
  "v_accvgpr_read_b32 v240, a228\n"
  "v_accvgpr_read_b32 v241, a229\n"
  "v_accvgpr_read_b32 v242, a232\n"
  "v_accvgpr_read_b32 v243, a233\n"
  "v_accvgpr_read_b32 v236, a230\n"
  "v_accvgpr_read_b32 v237, a231\n"
  "v_accvgpr_read_b32 v238, a234\n"
  "v_accvgpr_read_b32 v239, a235\n"
  "v_pk_fma_f32 v[240:241], v[240:241], v[84:85], v[84:85] neg_lo:[0,0,1] neg_hi:[0,0,1]\n"
  "v_pk_fma_f32 v[242:243], v[242:243], v[84:85], v[84:85] neg_lo:[0,0,1] neg_hi:[0,0,1]\n"
  "v_pk_fma_f32 v[236:237], v[236:237], v[86:87], v[86:87] neg_lo:[0,0,1] neg_hi:[0,0,1]\n"
  "v_pk_fma_f32 v[238:239], v[238:239], v[86:87], v[86:87] neg_lo:[0,0,1] neg_hi:[0,0,1]\n"
  "v_mfma_f32_16x16x32_bf16 v[156:159], v[44:47], a[140:143], v[156:159]\n"
  "ds_read_b64_tr_b16 v[0:1], v230 offset:6720\n"
  "ds_read_b64_tr_b16 v[2:3], v230 offset:6752\n"
  "v_cvt_pk_bf16_f32 v240, v240, v242\n"
  "v_cvt_pk_bf16_f32 v241, v241, v243\n"
  "v_cvt_pk_bf16_f32 v236, v236, v238\n"
  "v_cvt_pk_bf16_f32 v237, v237, v239\n"
  "v_pk_fma_f32 v[124:125], v[124:125], v[72:73], v[72:73] neg_lo:[0,0,1] neg_hi:[0,0,1]\n"
  "v_pk_fma_f32 v[128:129], v[128:129], v[72:73], v[72:73] neg_lo:[0,0,1] neg_hi:[0,0,1]\n"
  "v_pk_fma_f32 v[126:127], v[126:127], v[74:75], v[74:75] neg_lo:[0,0,1] neg_hi:[0,0,1]\n"
  "v_pk_fma_f32 v[130:131], v[130:131], v[74:75], v[74:75] neg_lo:[0,0,1] neg_hi:[0,0,1]\n"
  "v_mfma_f32_16x16x32_bf16 v[160:163], v[44:47], a[144:147], v[160:163]\n"
  "s_nop 0\n"
  "s_waitcnt lgkmcnt(12)\n"
  "buffer_store_dwordx4 v[244:247], v228, s[52:55], s82 offen offset:0 nt\n"
  "ds_write_b32 v233, v240 offset:17920\n"
  "ds_write_b32 v233, v241 offset:18208\n"
  "ds_write_b32 v233, v236 offset:18496\n"
  "ds_write_b32 v233, v237 offset:18784\n"
  "v_pk_fma_f32 v[132:133], v[132:133], v[72:73], v[72:73] neg_lo:[0,0,1] neg_hi:[0,0,1]\n"
  "v_pk_fma_f32 v[134:135], v[134:135], v[74:75], v[74:75] neg_lo:[0,0,1] neg_hi:[0,0,1]\n"
  "v_mfma_f32_16x16x32_bf16 v[164:167], v[44:47], a[148:151], v[164:167]\n"
  "v_accvgpr_read_b32 v240, a236\n"
  "v_accvgpr_read_b32 v241, a237\n"
  "v_accvgpr_read_b32 v242, a240\n"
  "v_accvgpr_read_b32 v243, a241\n"
  "v_accvgpr_read_b32 v236, a238\n"
  "v_accvgpr_read_b32 v237, a239\n"
  "v_accvgpr_read_b32 v238, a242\n"
  "v_accvgpr_read_b32 v239, a243\n"
  "v_cvt_pk_bf16_f32 v88, v88, v92\n"
  "v_cvt_pk_bf16_f32 v92, v89, v93\n"
  "v_cvt_pk_bf16_f32 v89, v96, v100\n"
  "v_cvt_pk_bf16_f32 v93, v97, v101\n"
  "v_cvt_pk_bf16_f32 v90, v90, v94\n"
  "v_cvt_pk_bf16_f32 v94, v91, v95\n"
  "v_cvt_pk_bf16_f32 v91, v98, v102\n"
  "v_cvt_pk_bf16_f32 v95, v99, v103\n"
  "v_pk_fma_f32 v[240:241], v[240:241], v[84:85], v[84:85] neg_lo:[0,0,1] neg_hi:[0,0,1]\n"
  "v_pk_fma_f32 v[242:243], v[242:243], v[84:85], v[84:85] neg_lo:[0,0,1] neg_hi:[0,0,1]\n"
  "v_pk_fma_f32 v[236:237], v[236:237], v[86:87], v[86:87] neg_lo:[0,0,1] neg_hi:[0,0,1]\n"
  "v_pk_fma_f32 v[238:239], v[238:239], v[86:87], v[86:87] neg_lo:[0,0,1] neg_hi:[0,0,1]\n"
  "v_mfma_f32_16x16x32_bf16 v[184:187], v[60:63], a[136:139], v[184:187]\n"
  "ds_read_b64_tr_b16 v[4:5], v230 offset:7840\n"
  "ds_read_b64_tr_b16 v[6:7], v230 offset:7872\n"
  "ds_write_b64 v229, v[88:89] offset:8960\n"
  "ds_write_b64 v229, v[92:93] offset:9248\n"
  "ds_write_b64 v229, v[90:91] offset:9536\n"
  "ds_write_b64 v229, v[94:95] offset:9824\n"
  "v_cvt_pk_bf16_f32 v240, v240, v242\n"
  "v_cvt_pk_bf16_f32 v241, v241, v243\n"
  "v_cvt_pk_bf16_f32 v236, v236, v238\n"
  "v_cvt_pk_bf16_f32 v237, v237, v239\n"
  "v_pk_fma_f32 v[152:153], v[152:153], v[76:77], v[76:77] neg_lo:[0,0,1] neg_hi:[0,0,1]\n"
  "v_pk_fma_f32 v[156:157], v[156:157], v[76:77], v[76:77] neg_lo:[0,0,1] neg_hi:[0,0,1]\n"
  "v_pk_fma_f32 v[154:155], v[154:155], v[78:79], v[78:79] neg_lo:[0,0,1] neg_hi:[0,0,1]\n"
  "v_pk_fma_f32 v[158:159], v[158:159], v[78:79], v[78:79] neg_lo:[0,0,1] neg_hi:[0,0,1]\n"
  "v_mfma_f32_16x16x32_bf16 v[188:191], v[60:63], a[140:143], v[188:191]\n"
  "s_nop 0\n"
  "s_waitcnt lgkmcnt(15)\n"
  "buffer_store_dwordx4 a[252:255], v228, s[52:55], s82 offen offset:3072 nt\n"
  "ds_write_b32 v233, v240 offset:19040\n"
  "ds_write_b32 v233, v241 offset:19328\n"
  "ds_write_b32 v233, v236 offset:19616\n"
  "ds_write_b32 v233, v237 offset:19904\n"
  "v_pk_fma_f32 v[160:161], v[160:161], v[76:77], v[76:77] neg_lo:[0,0,1] neg_hi:[0,0,1]\n"
  "v_pk_fma_f32 v[162:163], v[162:163], v[78:79], v[78:79] neg_lo:[0,0,1] neg_hi:[0,0,1]\n"
  "v_mfma_f32_16x16x32_bf16 v[192:195], v[60:63], a[144:147], v[192:195]\n"
  "ds_read_b64_tr_b16 v[16:17], v230 offset:8960\n"
  "ds_read_b64_tr_b16 v[18:19], v230 offset:8992\n"
  "ds_read_b64_tr_b16 v[20:21], v230 offset:10080\n"
  "ds_read_b64_tr_b16 v[22:23], v230 offset:10112\n"
  "v_cvt_pk_bf16_f32 v120, v120, v124\n"
  "v_pk_fma_f32 v[164:165], v[164:165], v[76:77], v[76:77] neg_lo:[0,0,1] neg_hi:[0,0,1]\n"
  "v_pk_fma_f32 v[166:167], v[166:167], v[78:79], v[78:79] neg_lo:[0,0,1] neg_hi:[0,0,1]\n"
  "v_mfma_f32_16x16x32_bf16 v[196:199], v[60:63], a[148:151], v[196:199]\n"
  "s_waitcnt lgkmcnt(4)\n"
  "s_barrier\n"
  "buffer_store_dwordx4 v[0:3], v228, s[52:55], s83 offen offset:0 nt\n"
  "v_cvt_pk_bf16_f32 v124, v121, v125\n"
  "v_cvt_pk_bf16_f32 v121, v128, v132\n"
  "v_pk_fma_f32 v[184:185], v[184:185], v[80:81], v[80:81] neg_lo:[0,0,1] neg_hi:[0,0,1]\n"
  "v_pk_fma_f32 v[186:187], v[186:187], v[82:83], v[82:83] neg_lo:[0,0,1] neg_hi:[0,0,1]\n"
  "ds_read_b64_tr_b16 v[244:245], v230 offset:17920\n"
  "ds_read_b64_tr_b16 v[246:247], v230 offset:17952\n"
  "ds_write_b64 v229, v[120:121] offset:11200\n"
  "v_pk_fma_f32 v[188:189], v[188:189], v[80:81], v[80:81] neg_lo:[0,0,1] neg_hi:[0,0,1]\n"
  "v_pk_fma_f32 v[190:191], v[190:191], v[82:83], v[82:83] neg_lo:[0,0,1] neg_hi:[0,0,1]\n"
  "ds_read_b64_tr_b16 a[252:253], v230 offset:19040\n"
  "ds_read_b64_tr_b16 a[254:255], v230 offset:19072\n"
  "ds_read_b64_tr_b16 v[0:1], v230 offset:20160\n"
  "ds_read_b64_tr_b16 v[2:3], v230 offset:20192\n"
  "v_cvt_pk_bf16_f32 v125, v129, v133\n"
  "v_pk_fma_f32 v[192:193], v[192:193], v[80:81], v[80:81] neg_lo:[0,0,1] neg_hi:[0,0,1]\n"
  "v_pk_fma_f32 v[194:195], v[194:195], v[82:83], v[82:83] neg_lo:[0,0,1] neg_hi:[0,0,1]\n"
  "buffer_store_dwordx4 v[4:7], v228, s[52:55], s83 offen offset:3072 nt\n"
  "ds_write_b64 v229, v[124:125] offset:11488\n"
  "v_cvt_pk_bf16_f32 v122, v122, v126\n"
  "v_pk_fma_f32 v[196:197], v[196:197], v[80:81], v[80:81] neg_lo:[0,0,1] neg_hi:[0,0,1]\n"
  "v_pk_fma_f32 v[198:199], v[198:199], v[82:83], v[82:83] neg_lo:[0,0,1] neg_hi:[0,0,1]\n"
  "v_cvt_pk_bf16_f32 v126, v123, v127\n"
  "v_cvt_pk_bf16_f32 v123, v130, v134\n"
  "v_cvt_pk_bf16_f32 v127, v131, v135\n"
  "v_cvt_pk_bf16_f32 v152, v152, v156\n"
  "v_cvt_pk_bf16_f32 v156, v153, v157\n"
  "s_waitcnt lgkmcnt(10)\n"
  "buffer_store_dwordx4 v[16:19], v228, s[52:55], s80 offen offset:128 nt\n"
  "ds_write_b64 v229, v[122:123] offset:11776\n"
  "v_cvt_pk_bf16_f32 v153, v160, v164\n"
  "ds_write_b64 v229, v[126:127] offset:12064\n"
  "v_cvt_pk_bf16_f32 v157, v161, v165\n"
  "v_cvt_pk_bf16_f32 v154, v154, v158\n"
  "s_waitcnt lgkmcnt(10)\n"
  "buffer_store_dwordx4 v[20:23], v228, s[52:55], s80 offen offset:3200 nt\n"
  "ds_write_b64 v229, v[152:153] offset:13440\n"
  "v_cvt_pk_bf16_f32 v158, v155, v159\n"
  "ds_read_b64_tr_b16 v[4:5], v230 offset:11200\n"
  "ds_read_b64_tr_b16 v[6:7], v230 offset:11232\n"
  "ds_write_b64 v229, v[156:157] offset:13728\n"
  "s_waitcnt lgkmcnt(12)\n"
  "buffer_store_dwordx4 v[244:247], v228, s[52:55], s56 offen offset:0 nt\n"
  "ds_read_b64_tr_b16 v[16:17], v230 offset:12320\n"
  "ds_read_b64_tr_b16 v[18:19], v230 offset:12352\n"
  "v_cvt_pk_bf16_f32 v155, v162, v166\n"
  "v_cvt_pk_bf16_f32 v159, v163, v167\n"
  "ds_write_b64 v229, v[154:155] offset:14016\n"
  "v_cvt_pk_bf16_f32 v184, v184, v188\n"
  "v_cvt_pk_bf16_f32 v188, v185, v189\n"
  "s_waitcnt lgkmcnt(12)\n"
  "buffer_store_dwordx4 a[252:255], v228, s[52:55], s56 offen offset:128 nt\n"
  "ds_write_b64 v229, v[158:159] offset:14304\n"
  "v_cvt_pk_bf16_f32 v185, v192, v196\n"
  "ds_write_b64 v229, v[184:185] offset:15680\n"
  "v_cvt_pk_bf16_f32 v189, v193, v197\n"
  "v_cvt_pk_bf16_f32 v186, v186, v190\n"
  "s_waitcnt lgkmcnt(12)\n"
  "buffer_store_dwordx4 v[0:3], v228, s[52:55], s56 offen offset:256 nt\n"
  "ds_read_b64_tr_b16 v[20:21], v230 offset:13440\n"
  "ds_write_b64 v229, v[188:189] offset:15968\n"
  "ds_read_b64_tr_b16 v[22:23], v230 offset:13472\n"
  "ds_read_b64_tr_b16 v[244:245], v230 offset:14560\n"
  "ds_read_b64_tr_b16 v[246:247], v230 offset:14592\n"
  "v_cvt_pk_bf16_f32 v190, v187, v191\n"
  "v_cvt_pk_bf16_f32 v187, v194, v198\n"
  "s_waitcnt lgkmcnt(11)\n"
  "buffer_store_dwordx4 v[4:7], v228, s[52:55], s81 offen offset:128 nt\n"
  "ds_write_b64 v229, v[186:187] offset:16256\n"
  "v_cvt_pk_bf16_f32 v191, v195, v199\n"
  "ds_write_b64 v229, v[190:191] offset:16544\n"
  "s_waitcnt lgkmcnt(10)\n"
  "buffer_store_dwordx4 v[16:19], v228, s[52:55], s81 offen offset:3200 nt\n"
  "ds_read_b64_tr_b16 a[252:253], v230 offset:15680\n"
  "ds_read_b64_tr_b16 a[254:255], v230 offset:15712\n"
  "ds_read_b64_tr_b16 v[0:1], v230 offset:16800\n"
  "ds_read_b64_tr_b16 v[2:3], v230 offset:16832\n"
  "s_waitcnt lgkmcnt(8)\n"
  "buffer_store_dwordx4 v[20:23], v228, s[52:55], s82 offen offset:128 nt\n"
  "s_waitcnt lgkmcnt(6)\n"
  "buffer_store_dwordx4 v[244:247], v228, s[52:55], s82 offen offset:3200 nt\n"
  "s_waitcnt lgkmcnt(2)\n"
  "buffer_store_dwordx4 a[252:255], v228, s[52:55], s83 offen offset:128 nt\n"
  "s_waitcnt lgkmcnt(0)\n"
  "buffer_store_dwordx4 v[0:3], v228, s[52:55], s83 offen offset:3200 nt\n"
  "L_END_%=:\n"
  "s_mov_b32 %0, s78\n"
  : "=&s"(bad)
  : "s"((unsigned long long)__builtin_amdgcn_kernarg_segment_ptr()), "s"(wg), "s"(tid0), "s"((unsigned)(size_t)lds)
  : "v0", "v1", "v2", "v3", "v4", "v5", "v6", "v7", "v8", "v9", "v10", "v11", "v12", "v13", "v14", "v15", "v16", "v17", "v18", "v19", "v20", "v21", "v22", "v23", "v24", "v25", "v26", "v27", "v28", "v29", "v30", "v31", "v32", "v33", "v34", "v35", "v36", "v37", "v38", "v39", "v40", "v41", "v42", "v43", "v44", "v45", "v46", "v47", "v48", "v49", "v50", "v51", "v52", "v53", "v54", "v55", "v56", "v57", "v58", "v59", "v60", "v61", "v62", "v63", "v64", "v65", "v66", "v67", "v68", "v69", "v70", "v71", "v72", "v73", "v74", "v75", "v76", "v77", "v78", "v79", "v80", "v81", "v82", "v83", "v84", "v85", "v86", "v87", "v88", "v89", "v90", "v91", "v92", "v93", "v94", "v95", "v96", "v97", "v98", "v99", "v100", "v101", "v102", "v103", "v104", "v105", "v106", "v107", "v108", "v109", "v110", "v111", "v112", "v113", "v114", "v115", "v116", "v117", "v118", "v119", "v120", "v121", "v122", "v123", "v124", "v125", "v126", "v127", "v128", "v129", "v130", "v131", "v132", "v133", "v134", "v135", "v136", "v137", "v138", "v139", "v140", "v141", "v142", "v143", "v144", "v145", "v146", "v147", "v148", "v149", "v150", "v151", "v152", "v153", "v154", "v155", "v156", "v157", "v158", "v159", "v160", "v161", "v162", "v163", "v164", "v165", "v166", "v167", "v168", "v169", "v170", "v171", "v172", "v173", "v174", "v175", "v176", "v177", "v178", "v179", "v180", "v181", "v182", "v183", "v184", "v185", "v186", "v187", "v188", "v189", "v190", "v191", "v192", "v193", "v194", "v195", "v196", "v197", "v198", "v199", "v200", "v201", "v202", "v203", "v204", "v205", "v206", "v207", "v208", "v209", "v210", "v211", "v212", "v213", "v214", "v215", "v216", "v217", "v218", "v219", "v220", "v221", "v222", "v223", "v224", "v225", "v226", "v227", "v228", "v229", "v230", "v231", "v232", "v233", "v234", "v235", "v236", "v237", "v238", "v239", "v240", "v241", "v242", "v243", "v244", "v245", "v246", "v247", "v248", "v249", "v250", "v251", "v252", "v253", "v254", "v255", "a0", "a1", "a2", "a3", "a4", "a5", "a6", "a7", "a8", "a9", "a10", "a11", "a12", "a13", "a14", "a15", "a16", "a17", "a18", "a19", "a20", "a21", "a22", "a23", "a24", "a25", "a26", "a27", "a28", "a29", "a30", "a31", "a32", "a33", "a34", "a35", "a36", "a37", "a38", "a39", "a40", "a41", "a42", "a43", "a44", "a45", "a46", "a47", "a48", "a49", "a50", "a51", "a52", "a53", "a54", "a55", "a56", "a57", "a58", "a59", "a60", "a61", "a62", "a63", "a64", "a65", "a66", "a67", "a68", "a69", "a70", "a71", "a72", "a73", "a74", "a75", "a76", "a77", "a78", "a79", "a80", "a81", "a82", "a83", "a84", "a85", "a86", "a87", "a88", "a89", "a90", "a91", "a92", "a93", "a94", "a95", "a96", "a97", "a98", "a99", "a100", "a101", "a102", "a103", "a104", "a105", "a106", "a107", "a108", "a109", "a110", "a111", "a112", "a113", "a114", "a115", "a116", "a117", "a118", "a119", "a120", "a121", "a122", "a123", "a124", "a125", "a126", "a127", "a128", "a129", "a130", "a131", "a132", "a133", "a134", "a135", "a136", "a137", "a138", "a139", "a140", "a141", "a142", "a143", "a144", "a145", "a146", "a147", "a148", "a149", "a150", "a151", "a152", "a153", "a154", "a155", "a156", "a157", "a158", "a159", "a160", "a161", "a162", "a163", "a164", "a165", "a166", "a167", "a168", "a169", "a170", "a171", "a172", "a173", "a174", "a175", "a176", "a177", "a178", "a179", "a180", "a181", "a182", "a183", "a184", "a185", "a186", "a187", "a188", "a189", "a190", "a191", "a192", "a193", "a194", "a195", "a196", "a197", "a198", "a199", "a200", "a201", "a202", "a203", "a204", "a205", "a206", "a207", "a208", "a209", "a210", "a211", "a212", "a213", "a214", "a215", "a216", "a217", "a218", "a219", "a220", "a221", "a222", "a223", "a224", "a225", "a226", "a227", "a228", "a229", "a230", "a231", "a232", "a233", "a234", "a235", "a236", "a237", "a238", "a239", "a240", "a241", "a242", "a243", "a244", "a245", "a246", "a247", "a248", "a249", "a250", "a251", "a252", "a253", "a254", "a255", "s20", "s21", "s22", "s23", "s24", "s25", "s26", "s27", "s28", "s29", "s30", "s31", "s32", "s33", "s34", "s35", "s36", "s37", "s38", "s39", "s40", "s41", "s42", "s43", "s44", "s45", "s46", "s47", "s48", "s49", "s50", "s51", "s52", "s53", "s54", "s55", "s56", "s57", "s58", "s59", "s60", "s61", "s62", "s63", "s64", "s65", "s66", "s67", "s68", "s69", "s70", "s71", "s72", "s73", "s74", "s75", "s76", "s77", "s78", "s79", "s80", "s81", "s82", "s83", "s84", "s85", "s86", "s87", "s88", "s89", "s90", "s91", "s92", "s93", "s94", "s95", "s96", "s97", "s98", "s99", "s100", "s101", "vcc", "scc", "memory");
  if (bad) {
    unsigned long long ka = (unsigned long long)__builtin_amdgcn_kernarg_segment_ptr();
    asm volatile("s_waitcnt vmcnt(0)" : "+s"(ka) :: "memory");
    const unsigned short* const* arg = (const unsigned short* const*)ka;
    const int xcd = wg & 7, slot = wg >> 3;
    constexpr int HB = 0;
    auto head_of = [&](int lh) { return lh + (lh >> HB) * (7 << HB) + (xcd << HB); };
#pragma unroll 1
    for (int it = 0; it < 6; ++it) {
      if (!((bad >> it) & 1u)) continue;
      const int p = slot + 32 * it, head = head_of(p / 3), r0 = 288 * (p % 3);
#pragma unroll 1
      for (int Y = 0; Y < 5; ++Y) {
        const int row = r0 + (Y < 4 ? 64 * wid + 16 * Y : 256 + 16 * (wid >> 1)), lo = Y < 4 ? 0 : 8 * (wid & 1);
        if (row < SQ) exact_tile(arg[0], arg[1], arg[2], (unsigned short*)arg[3], head, row, lo, Y < 4 ? 16 : lo + 8);
      }
    }
  }
}

extern "C" void kernel_launch(void* const* d_in, const int* in_sizes, int n_in,
                              void* d_out, int out_size, void* d_ws, size_t ws_size,
                              hipStream_t stream) {
  (void)hipFuncSetAttribute((const void*)attn_fwd_g, hipFuncAttributeMaxDynamicSharedMemorySize, LDS_BYTES);
  hipLaunchKernelGGL(attn_fwd_g, dim3(256), dim3(256), LDS_BYTES, stream, (const unsigned short*)d_in[0], (const unsigned short*)d_in[1],
                     (const unsigned short*)d_in[2], (unsigned short*)d_out);
}
```

```cpp
#include <hip/hip_runtime.h>
#include <hip/hip_bf16.h>

typedef __bf16   bf16x8 __attribute__((ext_vector_type(8)));
typedef __bf16   bf16x2 __attribute__((ext_vector_type(2)));
typedef float    f32x4  __attribute__((ext_vector_type(4)));
typedef float    f32x2  __attribute__((ext_vector_type(2)));
typedef unsigned u32x4  __attribute__((ext_vector_type(4)));
typedef unsigned u32x2  __attribute__((ext_vector_type(2)));
typedef unsigned short u16x8 __attribute__((ext_vector_type(8)));

constexpr int SQ = 848, SK = 512, DH = 192, ROWB = 384;
constexpr int LDS_BYTES = 163840;
constexpr float CSC = 0.07216878364870322f * 1.4426950408889634f;

#define DI __device__ __forceinline__
DI unsigned cvtpk(float lo, float hi) { f32x2 v = {lo, hi}; bf16x2 b = __builtin_convertvector(v, bf16x2); return __builtin_bit_cast(unsigned, b); }
DI float xmax(float x) {
  auto r = __builtin_amdgcn_permlane32_swap(__float_as_uint(x), __float_as_uint(x), false, false);
  x = fmaxf(__uint_as_float(r[0]), __uint_as_float(r[1]));
  auto q = __builtin_amdgcn_permlane16_swap(__float_as_uint(x), __float_as_uint(x), false, false);
  return fmaxf(__uint_as_float(q[0]), __uint_as_float(q[1]));
}
DI float xsum(float x) {
  auto r = __builtin_amdgcn_permlane32_swap(__float_as_uint(x), __float_as_uint(x), false, false);
  x = __uint_as_float(r[0]) + __uint_as_float(r[1]);
  auto q = __builtin_amdgcn_permlane16_swap(__float_as_uint(x), __float_as_uint(x), false, false);
  return __uint_as_float(q[0]) + __uint_as_float(q[1]);
}
DI f32x4 score_tile(const unsigned short* krow, const bf16x8 (&qf)[6]) {
  f32x4 acc = {0.f, 0.f, 0.f, 0.f};
#pragma unroll
  for (int s = 0; s < 6; ++s) acc = __builtin_amdgcn_mfma_f32_16x16x32_bf16(*(const bf16x8*)(krow + 32 * s), qf[s], acc, 0, 0, 0);
  return acc;
}
DI void exact_tile(const unsigned short* Q, const unsigned short* K, const unsigned short* V, unsigned short* O, int head, int row0, int lo, int hi) {
  const unsigned lane = __builtin_amdgcn_mbcnt_hi(~0u, __builtin_amdgcn_mbcnt_lo(~0u, 0u));
  const int c = lane & 15, g4 = lane >> 4;
  const int qrow = min(row0 + c, SQ - 1);
  bf16x8 qf[6];
#pragma unroll
  for (int s = 0; s < 6; ++s) qf[s] = *(const bf16x8*)(Q + ((size_t)head * SQ + qrow) * DH + 32 * s + 8 * g4);
  const unsigned short* Kh = K + ((size_t)head * SK + c) * DH + 8 * g4;
  float m = -__builtin_huge_valf();
#pragma unroll 1
  for (int kb = 0; kb < SK / 16; ++kb) {
    const f32x4 a = score_tile(Kh + (size_t)kb * 16 * DH, qf);
    m = fmaxf(fmaxf(m, fmaxf(a[0], a[1])), fmaxf(a[2], a[3]));
  }
  const float mc = -xmax(m) * CSC;
  float l = 0.f;
  f32x4 o[12];
#pragma unroll
  for (int dt = 0; dt < 12; ++dt) o[dt] = f32x4{0.f, 0.f, 0.f, 0.f};
#pragma unroll 1
  for (int j = 0; j < SK / 32; ++j) {
    u32x4 p;
#pragma unroll
    for (int kt = 0; kt < 2; ++kt) {
      f32x4 a = score_tile(Kh + (size_t)(32 * j + 16 * kt) * DH, qf);
#pragma unroll
      for (int r = 0; r < 4; ++r) { a[r] = __builtin_amdgcn_exp2f(fmaf(a[r], CSC, mc)); l += a[r]; }
      p[2 * kt] = cvtpk(a[0], a[1]); p[2 * kt + 1] = cvtpk(a[2], a[3]);
    }
    const unsigned short* Vj = V + ((size_t)head * SK + 32 * j + 4 * g4) * DH + c;
#pragma unroll
    for (int dt = 0; dt < 12; ++dt) {
      u16x8 vf;
#pragma unroll
      for (int i = 0; i < 8; ++i) vf[i] = Vj[(16 * (i >> 2) + (i & 3)) * DH + 16 * dt];
      o[dt] = __builtin_amdgcn_mfma_f32_16x16x32_bf16(__builtin_bit_cast(bf16x8, vf), __builtin_bit_cast(bf16x8, p), o[dt], 0, 0, 0);
    }
  }
  const float inv = 1.0f / xsum(l);
  if (row0 + c < SQ && c >= lo && c < hi) {
#pragma unroll
    for (int dt = 0; dt < 12; ++dt)
      *(u32x2*)(O + ((size_t)head * SQ + qrow) * DH + 16 * dt + 4 * g4) = u32x2{cvtpk(o[dt][0] * inv, o[dt][1] * inv), cvtpk(o[dt][2] * inv, o[dt][3] * inv)};
  }
}

__global__ __launch_bounds__(256, 1)
void attn_fwd_g(const unsigned short* __restrict__ Qg, const unsigned short* __restrict__ Kg,
                const unsigned short* __restrict__ Vg, unsigned short* __restrict__ Og) {
  extern __shared__ __attribute__((aligned(512))) char lds[];
  const int tid0 = __builtin_amdgcn_readfirstlane(threadIdx.x), wg = blockIdx.x, wid = tid0 >> 6;
  unsigned bad;
  asm volatile(
  ".p2align 4\n"
  "s_load_dwordx8 s[84:91], %1, 0\n"
  "L_PC_%=:\n"
  "s_getpc_b64 s[52:53]\n"
  "s_lshr_b32 s79, %2, 3\n"
  "s_lshl_b32 s79, s79, 15\n"
  "s_add_u32 s52, s52, s79\n"
  "s_addc_u32 s53, s53, 0\n"
  "s_sub_i32 s54, L_END_%=-L_PC_%=, s79\n"
  "s_max_i32 s54, s54, 0\n"
  "s_mov_b32 s55, 0x20000\n"
  "s_add_u32 m0, %4, 2048\n"
  "v_mbcnt_lo_u32_b32 v0, -1, 0\n"
  "v_mbcnt_hi_u32_b32 v0, -1, v0\n"
  "v_add_lshl_u32 v1, v0, %3, 7\n"
  "buffer_load_dword v1, s[52:55], 0 offen lds\n"
  "s_bitcmp1_b32 %2, 0\n"
  "s_cbranch_scc1 2\n"
  "s_waitcnt vmcnt(0) lgkmcnt(0)\n"
  "s_sleep 110\n"
  "s_lshr_b32 s38, %3, 6\n"
  "s_lshr_b32 s79, %2, 3\n"
  "s_mul_i32 s100, s79, 43\n"
  "s_lshr_b32 s100, s100, 7\n"
  "s_mul_i32 s71, s100, 3\n"
  "s_sub_u32 s95, s79, s71\n"
  "s_and_b32 s101, %2, 7\n"
  "s_lshl_b32 s101, s101, 0\n"
  "s_lshr_b32 s79, s100, 0\n"
  "s_mul_i32 s79, s79, 7\n"
  "s_add_u32 s79, s79, s100\n"
  "s_add_u32 s79, s79, s101\n"
  "s_mul_i32 s37, s79, 196608\n"
  "s_mul_i32 s92, s79, 325632\n"
  "s_mul_i32 s79, s95, 110592\n"
  "s_add_u32 s92, s92, s79\n"
  "s_mul_i32 s79, s38, 3072\n"
  "s_add_u32 s96, s79, s37\n"
  "s_lshr_b32 s71, s38, 1\n"
  "s_lshl_b32 s71, s71, 6\n"
  "s_add_u32 s79, s79, s71\n"
  "s_add_u32 s79, s79, %4\n"
  "s_add_u32 s66, s79, 41152\n"
  "s_add_u32 s67, s79, 53504\n"
  "s_add_u32 s68, s79, 65856\n"
  "s_mul_i32 s79, s38, 3088\n"
  "s_add_u32 s79, s79, %4\n"
  "s_add_u32 s58, s79, 4096\n"
  "s_add_u32 s59, s79, 16448\n"
  "s_add_u32 s60, s79, 28800\n"
  "s_mul_i32 s72, s38, 21312\n"
  "s_add_u32 s72, s72, %4\n"
  "s_add_u32 s72, s72, 78208\n"
  "s_movk_i32 s42, 0xc00\n"
  "s_movk_i32 s31, 12288\n"
  "s_mov_b32 s43, 0x20000\n"
  "s_mov_b32 s51, 0x20000\n"
  "s_bitcmp1_b32 s95, 1\n"
  "s_movk_i32 s93, 288\n"
  "s_cmovk_i32 s93, 272\n"
  "s_mul_i32 s93, s93, 384\n"
  "s_mov_b32 s50, s93\n"
  "s_mul_i32 s80, s38, 24576\n"
  "s_add_u32 s81, s80, 6144\n"
  "v_lshrrev_b32 v1, 3, v0\n"
  "v_and_b32 v2, 2, v1\n"
  "v_and_b32 v3, 7, v0\n"
  "v_xor_b32 v2, v2, v3\n"
  "v_mul_u32_u24 v221, 0x180, v1\n"
  "v_lshl_add_u32 v221, v2, 4, v221\n"
  "s_waitcnt lgkmcnt(0)\n"
  "s_add_u32 s40, s86, s96\n"
  "s_addc_u32 s41, s87, 0\n"
  "s_add_u32 s48, s84, s92\n"
  "s_addc_u32 s49, s85, 0\n"
  "s_mov_b32 m0, s68\n"
  "s_nop 0\n"
  "buffer_load_dwordx4 v221, s[40:43], 0 offen offset:0 lds\n"
  "s_addk_i32 m0, 896\n"
  "s_nop 0\n"
  "buffer_load_dwordx4 v221, s[40:43], 0 offen offset:128 lds\n"
  "s_addk_i32 m0, 896\n"
  "s_nop 0\n"
  "buffer_load_dwordx4 v221, s[40:43], 0 offen offset:256 lds\n"
  "s_add_u32 s40, s40, s31\n"
  "s_addc_u32 s41, s41, 0\n"
  "s_add_u32 s44, s88, s96\n"
  "s_addc_u32 s45, s89, 0\n"
  "s_mov_b64 s[62:63], s[90:91]\n"
  "s_mov_b32 s90, 100663296\n"
  "s_mov_b32 s91, 0x20000\n"
  "s_add_u32 s29, %4, 163456\n"
  "v_mbcnt_lo_u32_b32 v4, -1, 0\n"
  "v_mbcnt_hi_u32_b32 v4, -1, v4\n"
  "v_mul_u32_u24 v4, 0x18c, v4\n"
  "s_mov_b32 m0, s29\n"
  "s_nop 0\n"
  "buffer_load_dword v4, s[88:91], s37 offen lds\n"
  "s_mul_i32 s56, s38, 3072\n"
  "s_add_u32 s56, s56, 98304\n"
  "s_add_u32 m0, s72, 17920\n"
  "s_nop 0\n"
  "buffer_load_dwordx4 v221, s[48:51], s56 offen offset:0  lds\n"
  "s_add_u32 s82, s80, 12288\n"
  "s_add_u32 s83, s80, 18432\n"
  "s_mov_b64 s[46:47], s[42:43]\n"
  "s_mov_b64 s[34:35], s[42:43]\n"
  "s_bitcmp1_b32 s38, 0\n"
  "s_movk_i32 s70, 512\n"
  "s_cmovk_i32 s70, -512\n"
  "s_movk_i32 s97, 0\n"
  "s_cmovk_i32 s97, -21312\n"
  "s_movk_i32 s61, 0\n"
  "s_cmovk_i32 s61, -21308\n"
  "s_movk_i32 s69, 20196\n"
  "s_cmovk_i32 s69, 20188\n"
  "s_add_u32 m0, s72, 18912\n"
  "s_nop 0\n"
  "buffer_load_dwordx4 v221, s[48:51], s56 offen offset:128  lds\n"
  "s_mov_b32 s98, 0xff00ff00\n"
  "s_mov_b32 s99, s98\n"
  "s_mov_b32 s73, 0x3dd53b94\n"
  "s_mov_b32 s74, 0xbdd53b94\n"
  "s_mov_b32 s75, 0x53800000\n"
  "s_mov_b32 s76, -1\n"
  "s_add_u32 m0, s72, 19904\n"
  "s_nop 0\n"
  "buffer_load_dwordx4 v221, s[48:51], s56 offen offset:256  lds\n"
  "s_mov_b32 s77, 1\n"
  "s_mov_b32 s78, 0\n"
  "s_mov_b32 s57, s92\n"
  "s_mul_i32 s39, s38, 3072\n"
  "s_mov_b32 s54, 0\n"
  "s_add_u32 m0, s58, 1792\n"
  "s_nop 0\n"
  "buffer_load_dwordx4 v221, s[48:51], s80 offen offset:256  lds\n"
  "s_mov_b32 s55, 0x20000\n"
  "L_PC2_%=:\n"
  "s_getpc_b64 s[20:21]\n"
  "s_lshl_b32 s24, s38, 13\n"
  "s_add_u32 s24, s24, L_BIG_%=-L_PC2_%=-4\n"
  "s_cmp_lt_u32 s38, 2\n"
  "s_cselect_b32 s24, s24, L_TAIL_%=-L_PC2_%=-4\n"
  "s_sub_u32 s25, L_END_%=-L_PC2_%=-4, s24\n"
  "s_add_u32 s20, s20, s24\n"
  "s_addc_u32 s21, s21, 0\n"
  "s_mov_b32 s23, 0x20000\n"
  "s_lshr_b32 s24, s38, 1\n"
  "s_mul_i32 s24, s24, s38\n"
  "s_mul_i32 s24, s24, 5\n"
  "s_lshr_b32 s24, s24, 1\n"
  "v_lshrrev_b32 v1, 4, v0\n"
  "v_and_b32 v2, 2, v0\n"
  "v_xor_b32_e64 v1, v1, v2\n"
  "s_add_u32 m0, s60, -1280\n"
  "s_nop 0\n"
  "buffer_load_dwordx4 v221, s[48:51], s80 offen offset:3328  lds\n"
  "v_lshlrev_b32 v1, 4, v1\n"
  "v_and_b32 v2, 7, v0\n"
  "v_lshl_add_u32 v72, v2, 7, v1\n"
  "v_bfe_u32 v3, v0, 3, 1\n"
  "v_mul_u32_u24 v2, 0x440, v3\n"
  "s_add_u32 m0, s66, -128\n"
  "s_nop 0\n"
  "buffer_load_dwordx4 v221, s[48:51], s80 offen offset:128  lds\n"
  "v_add3_u32 v222, v72, v2, s72\n"
  "v_mul_u32_u24 v2, 0x6080, v3\n"
  "v_add3_u32 v74, v72, v2, s58\n"
  "v_lshl_add_u32 v72, v3, 10, v72\n"
  "v_add_u32 v73, s66, v72\n"
  "s_add_u32 m0, s66, -2176\n"
  "s_nop 0\n"
  "buffer_load_dwordx4 v221, s[48:51], s80 offen offset:3200  lds\n"
  "v_add_u32 v72, s58, v72\n"
  "v_lshl_add_u32 v1, v3, 6, v1\n"
  "v_and_b32 v2, 3, v0\n"
  "v_lshl_add_u32 v1, v2, 7, v1\n"
  "v_bfe_u32 v2, v0, 2, 2\n"
  "s_add_u32 m0, s58, 0\n"
  "s_nop 0\n"
  "buffer_load_dwordx4 v221, s[48:51], s80 offen offset:0  lds\n"
  "v_mul_u32_u24 v2, 0xc00, v2\n"
  "s_and_b32 s79, s38, 1\n"
  "s_lshl_b32 s79, s79, 9\n"
  "s_add_u32 s79, s79, %4\n"
  "s_add_u32 s71, s79, 41152\n"
  "s_add_u32 m0, s58, -2048\n"
  "s_nop 0\n"
  "buffer_load_dwordx4 v221, s[48:51], s80 offen offset:3072  lds\n"
  "v_add3_u32 v216, v1, v2, s71\n"
  "v_add_u32 v235, s70, v216\n"
  "v_add_u32 v233, s97, v222\n"
  "s_mov_b64 exec, s[98:99]\n"
  "v_add_u32 v233, 0x4f00, v233\n"
  "s_mov_b64 exec, -1\n"
  "v_lshrrev_b32_e64 v1, 4, v0\n"
  "v_mul_u32_u24 v1, 0xc10, v1\n"
  "s_mov_b32 m0, s67\n"
  "s_nop 0\n"
  "buffer_load_dwordx4 v221, s[40:43], 0 offen offset:0 lds\n"
  "v_bfe_u32 v2, v0, 2, 2\n"
  "v_lshl_add_u32 v1, v2, 7, v1\n"
  "v_bfe_u32 v2, v0, 1, 1\n"
  "v_lshl_add_u32 v1, v2, 6, v1\n"
  "v_and_b32_e64 v2, 1, v0\n"
  "s_addk_i32 m0, 896\n"
  "s_nop 0\n"
  "buffer_load_dwordx4 v221, s[40:43], 0 offen offset:128 lds\n"
  "v_lshl_add_u32 v1, v2, 3, v1\n"
  "s_add_u32 s71, s79, 4096\n"
  "v_add_u32 v1, s71, v1\n"
  "v_lshrrev_b32 v2, 3, v0\n"
  "v_xor_b32_e64 v2, s38, v2\n"
  "s_addk_i32 m0, 896\n"
  "s_nop 0\n"
  "buffer_load_dwordx4 v221, s[40:43], 0 offen offset:256 lds\n"
  "s_add_u32 s40, s40, s31\n"
  "s_addc_u32 s41, s41, 0\n"
  "v_and_b32 v2, 1, v2\n"
  "v_lshl_add_u32 v217, v2, 5, v1\n"
  "v_xor_b32 v2, 1, v2\n"
  "v_lshl_add_u32 v219, v2, 5, v1\n"
  "v_add_u32 v218, s70, v217\n"
  "s_add_u32 m0, s59, 896\n"
  "s_nop 0\n"
  "buffer_load_dwordx4 v221, s[44:47], 0 offen offset:128 lds\n"
  "v_add_u32_e64 v220, s70, v219\n"
  "s_lshl_b32 s71, s38, 9\n"
  "s_add_u32 s71, s71, %4\n"
  "s_add_u32 s71, s71, 0\n"
  "v_lshl_add_u32 v231, v0, 3, s71\n"
  "s_addk_i32 m0, 896\n"
  "s_nop 0\n"
  "buffer_load_dwordx4 v221, s[44:47], 0 offen offset:256 lds\n"
  "s_mov_b64 s[32:33], s[44:45]\n"
  "s_add_u32 s44, s44, s31\n"
  "s_addc_u32 s45, s45, 0\n"
  "v_add_u32_e64 v232, s70, v231\n"
  "v_mov_b32 v248, 0x43400000\n"
  "v_mov_b32 v249, 0x43400000\n"
  "v_mov_b32 v250, 0x43400000\n"
  "v_mov_b32 v251, 0x43400000\n"
  "s_add_u32 m0, s72, 0\n"
  "s_nop 0\n"
  "buffer_load_dwordx4 v221, s[48:51], s81 offen offset:0  lds\n"
  "v_mov_b32 v1, 0x3f803f80\n"
  "v_accvgpr_write_b32 a152, v1\n"
  "v_accvgpr_write_b32 a153, v1\n"
  "v_accvgpr_write_b32 a154, v1\n"
  "v_accvgpr_write_b32 a155, v1\n"
  "s_add_u32 m0, s72, -1984\n"
  "s_nop 0\n"
  "buffer_load_dwordx4 v221, s[48:51], s81 offen offset:3072  lds\n"
  "v_bfe_u32 v1, v0, 3, 1\n"
  "v_lshrrev_b32 v230, 4, v0\n"
  "v_lshl_add_u32 v1, v1, 2, v230\n"
  "v_mul_u32_u24 v228, 0x180, v1\n"
  "s_and_b32 s71, s38, 1\n"
  "s_add_u32 m0, s72, 2112\n"
  "s_nop 0\n"
  "buffer_load_dwordx4 v221, s[48:51], s81 offen offset:128  lds\n"
  "s_lshl_b32 s71, s71, 1\n"
  "v_and_b32 v1, 7, v0\n"
  "v_xor_b32_e64 v1, s71, v1\n"
  "v_lshl_add_u32 v228, v1, 4, v228\n"
  "v_mul_u32_u24 v230, 0x120, v230\n"
  "s_add_u32 m0, s72, 128\n"
  "s_nop 0\n"
  "buffer_load_dwordx4 v221, s[48:51], s81 offen offset:3200  lds\n"
  "v_and_b32 v1, 3, v0\n"
  "v_lshl_add_u32 v230, v1, 6, v230\n"
  "v_bfe_u32 v1, v0, 2, 2\n"
  "v_lshl_add_u32 v230, v1, 3, v230\n"
  "v_add_u32 v230, s72, v230\n"
  "s_add_u32 m0, s60, -256\n"
  "s_nop 0\n"
  "buffer_load_dwordx4 v221, s[48:51], s81 offen offset:256  lds\n"
  "v_and_b32_e64 v1, 31, v0\n"
  "v_lshlrev_b32_e64 v229, 3, v1\n"
  "v_lshrrev_b32_e64 v1, 5, v0\n"
  "v_mul_u32_u24 v1, 0x460, v1\n"
  "v_add3_u32 v229, v1, v229, s72\n"
  "s_waitcnt vmcnt(16)\n"
  "s_barrier\n"
  "ds_read_b128 a[164:167], v216 offset:24704\n"
  "ds_read_b128 a[168:171], v216 offset:24768\n"
  "ds_read_b128 a[172:175], v216 offset:25728\n"
  "ds_read_b128 a[176:179], v216 offset:25792\n"
  "ds_read_b128 a[180:183], v216 offset:26752\n"
  "ds_read_b128 a[184:187], v216 offset:26816\n"
  "ds_read_b128 a[96:99], v233 offset:17920\n"
  "ds_read_b128 a[100:103], v233 offset:17984\n"
  "ds_read_b128 a[104:107], v233 offset:19040\n"
  "ds_read_b128 a[108:111], v233 offset:19104\n"
  "ds_read_b128 a[112:115], v233 offset:20160\n"
  "ds_read_b128 a[116:119], v233 offset:20224\n"
  "v_bfe_u32 v5, v231, 1, 8\n"
  "v_add_u32_e64 v5, s29, v5\n"
  "ds_read_b128 a[188:191], v235 offset:24704\n"
  "ds_read_b128 a[192:195], v235 offset:24768\n"
  "ds_read_b128 a[196:199], v235 offset:25728\n"
  "ds_read_b128 a[200:203], v235 offset:25792\n"
  "ds_read_b128 a[204:207], v235 offset:26752\n"
  "ds_read_b128 a[208:211], v235 offset:26816\n"
  "ds_read_b32 v4, v5\n"
  "v_bfe_u32 v234, v231, 1, 6\n"
  "s_waitcnt lgkmcnt(12)\n"
  "v_mfma_f32_16x16x32_bf16 v[64:67], a[164:167], a[96:99], v[248:251]\n"
  "s_waitcnt lgkmcnt(11)\n"
  "v_mfma_f32_16x16x32_bf16 v[64:67], a[168:171], a[100:103], v[64:67]\n"
  "s_add_u32 m0, s60, -2304\n"
  "s_nop 0\n"
  "buffer_load_dwordx4 v221, s[48:51], s81 offen offset:3328  lds\n"
  "s_waitcnt lgkmcnt(10)\n"
  "v_mfma_f32_16x16x32_bf16 v[64:67], a[172:175], a[104:107], v[64:67]\n"
  "s_waitcnt lgkmcnt(9)\n"
  "v_mfma_f32_16x16x32_bf16 v[64:67], a[176:179], a[108:111], v[64:67]\n"
  "s_waitcnt lgkmcnt(8)\n"
  "v_mfma_f32_16x16x32_bf16 v[64:67], a[180:183], a[112:115], v[64:67]\n"
  "s_waitcnt lgkmcnt(0)\n"
  "v_and_b32 v4, 0x7fff7fff, v4\n"
  "v_lshlrev_b32 v5, 16, v4\n"
  "v_mfma_f32_16x16x32_bf16 v[64:67], a[184:187], a[116:119], v[64:67]\n"
  "s_add_u32 m0, s72, 8704\n"
  "s_nop 0\n"
  "buffer_load_dwordx4 v221, s[48:51], s82 offen offset:256  lds\n"
  "v_mfma_f32_16x16x32_bf16 v[240:243], a[188:191], a[96:99], v[248:251]\n"
  "s_waitcnt vmcnt(12)\n"
  "ds_read_b128 a[20:23], v74 offset:2112\n"
  "ds_read_b128 a[16:19], v74 offset:2048\n"
  "ds_read_b128 a[12:15], v73 offset:64\n"
  "v_mfma_f32_16x16x32_bf16 v[240:243], a[192:195], a[100:103], v[240:243]\n"
  "ds_read_b128 a[8:11], v73 offset:0\n"
  "ds_read_b128 a[4:7], v72 offset:64\n"
  "ds_read_b128 a[0:3], v72 offset:0\n"
  "v_max_u32_e64 v4, v4, v5\n"
  "v_mfma_f32_16x16x32_bf16 v[240:243], a[196:199], a[104:107], v[240:243]\n"
  "s_nop 0\n"
  "v_max_u32_dpp v4, v4, v4 quad_perm:[1,0,3,2] row_mask:0xf bank_mask:0xf\n"
  "v_max3_f32 v227, v64, v65, v66\n"
  "v_mfma_f32_16x16x32_bf16 v[240:243], a[200:203], a[108:111], v[240:243]\n"
  "s_add_u32 m0, s72, 6720\n"
  "s_nop 0\n"
  "buffer_load_dwordx4 v221, s[48:51], s82 offen offset:3328  lds\n"
  "v_max_u32_dpp v4, v4, v4 quad_perm:[2,3,0,1] row_mask:0xf bank_mask:0xf\n"
  "v_add_u32 v4, -1, v4\n"
  "v_mfma_f32_16x16x32_bf16 v[240:243], a[204:207], a[112:115], v[240:243]\n"
  "s_nop 0\n"
  "v_min_u32_dpp v4, v4, v4 row_shr:4 row_mask:0xf bank_mask:0xf\n"
  "v_mfma_f32_16x16x32_bf16 v[240:243], a[208:211], a[116:119], v[240:243]\n"
  "s_waitcnt lgkmcnt(5)\n"
  "v_min_u32_dpp v4, v4, v4 row_shr:8 row_mask:0xf bank_mask:0xf\n"
  "v_mfma_f32_16x16x32_bf16 v[0:3], a[184:187], a[20:23], v[248:251]\n"
  "s_waitcnt lgkmcnt(4)\n"
  "v_min_u32_dpp v4, v4, v4 row_bcast:15 row_mask:0xa bank_mask:0xf\n"
  "v_mfma_f32_16x16x32_bf16 v[0:3], a[180:183], a[16:19], v[0:3]\n"
  "s_add_u32 m0, s72, 6592\n"
  "s_nop 0\n"
  "buffer_load_dwordx4 v221, s[48:51], s82 offen offset:128  lds\n"
  "s_waitcnt lgkmcnt(3)\n"
  "v_min_u32_dpp v4, v4, v4 row_bcast:31 row_mask:0xc bank_mask:0xf\n"
  "v_mfma_f32_16x16x32_bf16 v[0:3], a[176:179], a[12:15], v[0:3]\n"
  "s_waitcnt lgkmcnt(2)\n"
  "v_readlane_b32 s65, v4, 63\n"
  "v_mfma_f32_16x16x32_bf16 v[0:3], a[172:175], a[8:11], v[0:3]\n"
  "s_waitcnt lgkmcnt(1)\n"
  "v_max3_f32 v227, v227, v67, v240\n"
  "v_max3_f32 v227, v227, v241, v242\n"
  "v_mfma_f32_16x16x32_bf16 v[0:3], a[168:171], a[4:7], v[0:3]\n"
  "s_add_u32 s65, s65, 1\n"
  "s_lshr_b32 s65, s65, 23\n"
  "s_sub_i32 s65, 0x77, s65\n"
  "s_and_b32 s65, s65, -4\n"
  "s_max_i32 s65, s65, 0xffffffc4\n"
  "s_waitcnt lgkmcnt(0)\n"
  "v_cvt_f32_i32 v4, s65\n"
  "v_max_f32 v227, v227, v243\n"
  "v_mfma_f32_16x16x32_bf16 v[0:3], a[164:167], a[0:3], v[0:3]\n"
  "s_add_u32 m0, s72, 4608\n"
  "s_nop 0\n"
  "buffer_load_dwordx4 v221, s[48:51], s82 offen offset:3200  lds\n"
  "v_readfirstlane_b32 s64, v4\n"
  "v_mul_f32 v227, s74, v227\n"
  "s_nop 0\n"
  "v_add_f32 v227, s64, v227\n"
  "v_mfma_f32_16x16x32_bf16 v[4:7], a[208:211], a[20:23], v[248:251]\n"
  "s_add_u32 s65, s65, 0xab\n"
  "ds_bpermute_b32 v227, v234, v227\n"
  "v_mfma_f32_16x16x32_bf16 v[4:7], a[204:207], a[16:19], v[4:7]\n"
  "s_cmpk_gt_u32 s65, 0xd3\n"
  "s_cselect_b32 s65, 0, s65\n"
  "v_mfma_f32_16x16x32_bf16 v[4:7], a[200:203], a[12:15], v[4:7]\n"
  "s_lshl_b32 s65, s65, 23\n"
  "v_max3_f32 v223, v0, v1, v2\n"
  "v_max_f32 v223, v223, v3\n"
  "v_mfma_f32_16x16x32_bf16 v[4:7], a[196:199], a[8:11], v[4:7]\n"
  "s_add_u32 m0, s72, 4480\n"
  "s_nop 0\n"
  "buffer_load_dwordx4 v221, s[48:51], s82 offen offset:0  lds\n"
  "v_mfma_f32_16x16x32_bf16 v[4:7], a[192:195], a[4:7], v[4:7]\n"
  "s_mov_b32 s75, s65\n"
  "v_mul_f32 v223, s74, v223\n"
  "v_add_f32_e64 v223, s64, v223\n"
  "v_mfma_f32_16x16x32_bf16 v[4:7], a[188:191], a[0:3], v[4:7]\n"
  "ds_bpermute_b32 v223, v234, v223\n"
  "s_waitcnt lgkmcnt(1)\n"
  "v_fma_f32 v64, v64, s73, v227\n"
  "v_exp_f32 v64, v64\n"
  "v_fma_f32 v65, v65, s73, v227\n"
  "v_exp_f32 v65, v65\n"
  "v_fma_f32 v66, v66, s73, v227\n"
  "v_exp_f32 v66, v66\n"
  "v_fma_f32 v67, v67, s73, v227\n"
  "v_exp_f32_e64 v67, v67\n"
  "v_cvt_pk_bf16_f32 v64, v64, v65\n"
  "v_cvt_pk_bf16_f32 v65, v66, v67\n"
  "ds_write_b64 v231, v[64:65] offset:0\n"
  "s_waitcnt vmcnt(11) lgkmcnt(0)\n"
  "s_barrier\n"
  "ds_read_b128 a[212:215], v216 offset:12352\n"
  "ds_read_b128 a[216:219], v216 offset:12416\n"
  "ds_read_b128 a[220:223], v216 offset:13376\n"
  "ds_read_b128 a[224:227], v216 offset:13440\n"
  "ds_read_b128 a[228:231], v216 offset:14400\n"
  "ds_read_b128 a[232:235], v216 offset:14464\n"
  "ds_read_b64 a[156:157], v231 offset:0\n"
  "ds_read_b64 a[158:159], v232 offset:0\n"
  "s_waitcnt lgkmcnt(7)\n"
  "v_mfma_f32_16x16x32_bf16 v[64:67], a[212:215], a[96:99], v[248:251]\n"
  "s_add_u32 m0, s72, 2496\n"
  "s_nop 0\n"
  "buffer_load_dwordx4 v221, s[48:51], s82 offen offset:3072  lds\n"
  "ds_read_b64_tr_b16 a[144:145], v217 offset:14400\n"
  "ds_read_b64_tr_b16 a[146:147], v218 offset:14400\n"
  "s_waitcnt lgkmcnt(8)\n"
  "v_mfma_f32_16x16x32_bf16 v[64:67], a[216:219], a[100:103], v[64:67]\n"
  "ds_read_b64_tr_b16 a[148:149], v217 offset:14416\n"
  "ds_read_b64_tr_b16 a[150:151], v218 offset:14416\n"
  "s_waitcnt lgkmcnt(9)\n"
  "v_mfma_f32_16x16x32_bf16 v[64:67], a[220:223], a[104:107], v[64:67]\n"
  "ds_read_b64_tr_b16 v[76:77], v217 offset:13376\n"
  "ds_read_b64_tr_b16 v[78:79], v218 offset:13376\n"
  "s_waitcnt lgkmcnt(10)\n"
  "v_mfma_f32_16x16x32_bf16 v[64:67], a[224:227], a[108:111], v[64:67]\n"
  "ds_read_b64_tr_b16 v[80:81], v217 offset:13392\n"
  "ds_read_b64_tr_b16 v[82:83], v218 offset:13392\n"
  "s_waitcnt lgkmcnt(11)\n"
  "v_mfma_f32_16x16x32_bf16 v[64:67], a[228:231], a[112:115], v[64:67]\n"
  "s_add_u32 m0, s72, 11200\n"
  "s_nop 0\n"
  "buffer_load_dwordx4 v221, s[48:51], s83 offen offset:0  lds\n"
  "ds_read_b128 a[120:123], v235 offset:12352\n"
  "ds_read_b128 a[124:127], v235 offset:12416\n"
  "s_waitcnt lgkmcnt(12)\n"
  "v_mfma_f32_16x16x32_bf16 v[64:67], a[232:235], a[116:119], v[64:67]\n"
  "ds_read_b128 a[128:131], v235 offset:13376\n"
  "ds_read_b128 a[132:135], v235 offset:13440\n"
  "ds_read_b128 a[136:139], v235 offset:14400\n"
  "ds_read_b128 a[140:143], v235 offset:14464\n"
  "s_waitcnt lgkmcnt(12)\n"
  "v_mfma_f32_16x16x32_bf16 a[244:247], a[156:159], a[144:147], 1.0\n"
  "ds_read_b64_tr_b16 v[84:85], v219 offset:14400\n"
  "ds_read_b64_tr_b16 v[86:87], v220 offset:14400\n"
  "s_waitcnt lgkmcnt(12)\n"
  "v_mfma_f32_16x16x32_bf16 a[248:251], a[156:159], a[148:151], 1.0\n"
  "s_add_u32 m0, s72, 9216\n"
  "s_nop 0\n"
  "buffer_load_dwordx4 v221, s[48:51], s83 offen offset:3072  lds\n"
  "ds_read_b64_tr_b16 v[244:245], v219 offset:14416\n"
  "ds_read_b64_tr_b16 v[246:247], v220 offset:14416\n"
  "s_waitcnt lgkmcnt(12)\n"
  "v_mfma_f32_16x16x32_bf16 a[236:239], a[156:159], v[76:79], 1.0\n"
  "ds_read_b64_tr_b16 a[252:253], v219 offset:13376\n"
  "ds_read_b64_tr_b16 a[254:255], v220 offset:13376\n"
  "s_waitcnt lgkmcnt(12)\n"
  "v_fma_f32 v0, v0, s73, v223\n"
  "v_exp_f32 v0, v0\n"
  "v_fma_f32 v1, v1, s73, v223\n"
  "v_exp_f32 v1, v1\n"
  "v_fma_f32 v2, v2, s73, v223\n"
  "v_exp_f32 v2, v2\n"
  "v_fma_f32 v3, v3, s73, v223\n"
  "v_exp_f32_e64 v3, v3\n"
  "v_mfma_f32_16x16x32_bf16 a[240:243], a[156:159], v[80:83], 1.0\n"
  "ds_read_b64_tr_b16 v[236:237], v219 offset:13392\n"
  "ds_read_b64_tr_b16 v[238:239], v220 offset:13392\n"
  "v_cvt_pk_bf16_f32 v8, v0, v1\n"
  "v_cvt_pk_bf16_f32 v9, v2, v3\n"
  "v_mfma_f32_16x16x32_bf16 v[0:3], a[232:235], a[20:23], v[248:251]\n"
  "v_fma_f32 v4, v4, s73, v223\n"
  "v_fma_f32 v5, v5, s73, v223\n"
  "v_mfma_f32_16x16x32_bf16 v[0:3], a[228:231], a[16:19], v[0:3]\n"
  "s_add_u32 m0, s72, 13312\n"
  "s_nop 0\n"
  "buffer_load_dwordx4 v221, s[48:51], s83 offen offset:128  lds\n"
  "v_mfma_f32_16x16x32_bf16 v[0:3], a[224:227], a[12:15], v[0:3]\n"
  "v_exp_f32 v4, v4\n"
  "v_mfma_f32_16x16x32_bf16 v[0:3], a[220:223], a[8:11], v[0:3]\n"
  "v_exp_f32_e64 v5, v5\n"
  "v_mfma_f32_16x16x32_bf16 v[0:3], a[216:219], a[4:7], v[0:3]\n"
  "v_fma_f32 v6, v6, s73, v223\n"
  "v_exp_f32 v6, v6\n"
  "v_fma_f32 v7, v7, s73, v223\n"
  "v_exp_f32 v7, v7\n"
  "v_mfma_f32_16x16x32_bf16 v[0:3], a[212:215], a[0:3], v[0:3]\n"
  "s_add_u32 m0, s72, 11328\n"
  "s_nop 0\n"
  "buffer_load_dwordx4 v221, s[48:51], s83 offen offset:3200  lds\n"
  "s_waitcnt lgkmcnt(13)\n"
  "v_cvt_pk_bf16_f32 v10, v4, v5\n"
  "v_cvt_pk_bf16_f32 v11, v6, v7\n"
  "v_mfma_f32_16x16x32_bf16 v[4:7], a[120:123], a[0:3], v[248:251]\n"
  "s_waitcnt vmcnt(10)\n"
  "ds_read_b128 a[24:27], v222 offset:0\n"
  "ds_read_b128 a[28:31], v222 offset:64\n"
  "ds_read_b128 a[32:35], v222 offset:2240\n"
  "s_waitcnt lgkmcnt(15)\n"
  "v_mfma_f32_16x16x32_bf16 v[4:7], a[124:127], a[4:7], v[4:7]\n"
  "ds_read_b128 a[36:39], v222 offset:2304\n"
  "ds_read_b128 a[40:43], v72 offset:24704\n"
  "ds_read_b128 a[44:47], v72 offset:24768\n"
  "s_waitcnt lgkmcnt(15)\n"
  "v_mfma_f32_16x16x32_bf16 v[4:7], a[128:131], a[8:11], v[4:7]\n"
  "v_fma_f32 v64, v64, s73, v227\n"
  "v_fma_f32 v65, v65, s73, v227\n"
  "v_mfma_f32_16x16x32_bf16 v[4:7], a[132:135], a[12:15], v[4:7]\n"
  "s_add_u32 m0, s72, 15424\n"
  "s_nop 0\n"
  "buffer_load_dwordx4 v221, s[48:51], s83 offen offset:256  lds\n"
  "v_mfma_f32_16x16x32_bf16 v[4:7], a[136:139], a[16:19], v[4:7]\n"
  "s_waitcnt lgkmcnt(14)\n"
  "v_exp_f32 v64, v64\n"
  "v_mfma_f32_16x16x32_bf16 v[4:7], a[140:143], a[20:23], v[4:7]\n"
  "s_waitcnt lgkmcnt(5)\n"
  "v_exp_f32 v65, v65\n"
  "v_mfma_f32_16x16x32_bf16 v[16:19], a[164:167], a[24:27], v[248:251]\n"
  "s_waitcnt lgkmcnt(4)\n"
  "v_fma_f32 v66, v66, s73, v227\n"
  "v_fma_f32 v67, v67, s73, v227\n"
  "v_mfma_f32_16x16x32_bf16 v[16:19], a[168:171], a[28:31], v[16:19]\n"
  "s_add_u32 m0, s72, 13440\n"
  "s_nop 0\n"
  "buffer_load_dwordx4 v221, s[48:51], s83 offen offset:3328  lds\n"
  "s_waitcnt lgkmcnt(3)\n"
  "v_mfma_f32_16x16x32_bf16 v[16:19], a[172:175], a[32:35], v[16:19]\n"
  "s_waitcnt lgkmcnt(2)\n"
  "v_exp_f32 v66, v66\n"
  "v_mfma_f32_16x16x32_bf16 v[16:19], a[176:179], a[36:39], v[16:19]\n"
  "s_waitcnt lgkmcnt(1)\n"
  "v_exp_f32 v67, v67\n"
  "v_mfma_f32_16x16x32_bf16 v[16:19], a[180:183], a[40:43], v[16:19]\n"
  "s_waitcnt lgkmcnt(0)\n"
  "v_cvt_pk_bf16_f32 v64, v64, v65\n"
  "v_cvt_pk_bf16_f32 v65, v66, v67\n"
  "v_mfma_f32_16x16x32_bf16 v[16:19], a[184:187], a[44:47], v[16:19]\n"
  "s_mov_b32 m0, s66\n"
  "s_nop 0\n"
  "buffer_load_dwordx4 v221, s[40:43], 0 offen offset:0 lds\n"
  "v_mfma_f32_16x16x32_bf16 v[20:23], a[188:191], a[24:27], v[248:251]\n"
  "v_fma_f32 v0, v0, s73, v223\n"
  "v_fma_f32 v1, v1, s73, v223\n"
  "v_mfma_f32_16x16x32_bf16 v[20:23], a[192:195], a[28:31], v[20:23]\n"
  "v_exp_f32 v0, v0\n"
  "v_mfma_f32_16x16x32_bf16 v[20:23], a[196:199], a[32:35], v[20:23]\n"
  "v_max3_f32 v224, v16, v17, v18\n"
  "v_max_f32 v224, v224, v19\n"
  "v_mfma_f32_16x16x32_bf16 v[20:23], a[200:203], a[36:39], v[20:23]\n"
  "s_addk_i32 m0, 896\n"
  "s_nop 0\n"
  "buffer_load_dwordx4 v221, s[40:43], 0 offen offset:128 lds\n"
  "v_mfma_f32_16x16x32_bf16 v[20:23], a[204:207], a[40:43], v[20:23]\n"
  "v_mul_f32 v224, s74, v224\n"
  "v_add_f32 v224, s64, v224\n"
  "v_mfma_f32_16x16x32_bf16 v[20:23], a[208:211], a[44:47], v[20:23]\n"
  "ds_bpermute_b32 v224, v234, v224\n"
  "v_mfma_f32_16x16x32_bf16 v[104:107], v[8:11], a[144:147], 1.0\n"
  "v_exp_f32 v1, v1\n"
  "v_mfma_f32_16x16x32_bf16 v[108:111], v[8:11], a[148:151], 1.0\n"
  "s_addk_i32 m0, 896\n"
  "s_nop 0\n"
  "buffer_load_dwordx4 v221, s[40:43], 0 offen offset:256 lds\n"
  "s_add_u32 s40, s40, s31\n"
  "s_addc_u32 s41, s41, 0\n"
  "v_mfma_f32_16x16x32_bf16 v[112:115], v[8:11], v[84:87], 1.0\n"
  "v_fma_f32 v2, v2, s73, v223\n"
  "v_fma_f32 v3, v3, s73, v223\n"
  "v_mfma_f32_16x16x32_bf16 v[116:119], v[8:11], v[244:247], 1.0\n"
  "v_exp_f32 v2, v2\n"
  "v_mfma_f32_16x16x32_bf16 v[88:91], v[8:11], v[76:79], 1.0\n"
  "v_exp_f32_e64 v3, v3\n"
  "v_mfma_f32_16x16x32_bf16 v[92:95], v[8:11], v[80:83], 1.0\n"
  "s_mov_b32 m0, s58\n"
  "s_nop 0\n"
  "buffer_load_dwordx4 v221, s[32:35], 0 offen offset:0 lds\n"
  "v_mfma_f32_16x16x32_bf16 v[96:99], v[8:11], a[252:255], 1.0\n"
  "s_waitcnt lgkmcnt(0)\n"
  "v_fma_f32 v16, v16, s73, v224\n"
  "v_exp_f32 v16, v16\n"
  "v_fma_f32 v17, v17, s73, v224\n"
  "v_exp_f32 v17, v17\n"
  "v_fma_f32 v18, v18, s73, v224\n"
  "v_exp_f32 v18, v18\n"
  "v_fma_f32 v19, v19, s73, v224\n"
  "v_exp_f32_e64 v19, v19\n"
  "v_mfma_f32_16x16x32_bf16 v[100:103], v[8:11], v[236:239], 1.0\n"
  "v_cvt_pk_bf16_f32 v24, v16, v17\n"
  "v_cvt_pk_bf16_f32 v25, v18, v19\n"
  "v_mfma_f32_16x16x32_bf16 v[16:19], a[212:215], a[24:27], v[248:251]\n"
  "v_fma_f32 v20, v20, s73, v224\n"
  "v_fma_f32 v21, v21, s73, v224\n"
  "v_mfma_f32_16x16x32_bf16 v[16:19], a[216:219], a[28:31], v[16:19]\n"
  "s_addk_i32 m0, 1024\n"
  "s_nop 0\n"
  "buffer_load_dwordx4 v221, s[44:47], 0 offen offset:0 lds\n"
  "v_mfma_f32_16x16x32_bf16 v[16:19], a[220:223], a[32:35], v[16:19]\n"
  "v_exp_f32 v20, v20\n"
  "v_mfma_f32_16x16x32_bf16 v[16:19], a[224:227], a[36:39], v[16:19]\n"
  "v_exp_f32 v21, v21\n"
  "v_mfma_f32_16x16x32_bf16 v[16:19], a[228:231], a[40:43], v[16:19]\n"
  "v_fma_f32 v22, v22, s73, v224\n"
  "v_exp_f32 v22, v22\n"
  "v_fma_f32 v23, v23, s73, v224\n"
  "v_exp_f32 v23, v23\n"
  "v_mfma_f32_16x16x32_bf16 v[16:19], a[232:235], a[44:47], v[16:19]\n"
  "s_addk_i32 m0, 768\n"
  "s_nop 0\n"
  "buffer_load_dwordx4 v221, s[44:47], 0 offen offset:256 lds\n"
  "s_mov_b64 s[32:33], s[44:45]\n"
  "s_add_u32 s44, s44, s31\n"
  "s_addc_u32 s45, s45, 0\n"
  "v_cvt_pk_bf16_f32 v26, v20, v21\n"
  "v_cvt_pk_bf16_f32 v27, v22, v23\n"
  "v_mfma_f32_16x16x32_bf16 v[20:23], a[140:143], a[44:47], v[248:251]\n"
  "s_waitcnt vmcnt(12)\n"
  "ds_read_b128 a[68:71], v222 offset:9024\n"
  "ds_read_b128 a[64:67], v222 offset:8960\n"
  "ds_read_b128 a[60:63], v222 offset:6784\n"
  "v_mfma_f32_16x16x32_bf16 v[20:23], a[136:139], a[40:43], v[20:23]\n"
  "ds_read_b128 a[56:59], v222 offset:6720\n"
  "ds_read_b128 a[52:55], v222 offset:4544\n"
  "ds_read_b128 a[48:51], v222 offset:4480\n"
  "v_mfma_f32_16x16x32_bf16 v[20:23], a[132:135], a[36:39], v[20:23]\n"
  "v_fma_f32 v4, v4, s73, v223\n"
  "v_fma_f32 v5, v5, s73, v223\n"
  "v_mfma_f32_16x16x32_bf16 v[20:23], a[128:131], a[32:35], v[20:23]\n"
  "v_exp_f32 v4, v4\n"
  "v_mfma_f32_16x16x32_bf16 v[20:23], a[124:127], a[28:31], v[20:23]\n"
  "v_exp_f32 v5, v5\n"
  "v_mfma_f32_16x16x32_bf16 v[20:23], a[120:123], a[24:27], v[20:23]\n"
  "s_waitcnt lgkmcnt(5)\n"
  "v_fma_f32 v6, v6, s73, v223\n"
  "v_fma_f32 v7, v7, s73, v223\n"
  "v_mfma_f32_16x16x32_bf16 v[32:35], a[184:187], a[68:71], v[248:251]\n"
  "s_waitcnt lgkmcnt(4)\n"
  "v_exp_f32 v6, v6\n"
  "v_mfma_f32_16x16x32_bf16 v[32:35], a[180:183], a[64:67], v[32:35]\n"
  "s_waitcnt lgkmcnt(3)\n"
  "v_exp_f32 v7, v7\n"
  "v_mfma_f32_16x16x32_bf16 v[32:35], a[176:179], a[60:63], v[32:35]\n"
  "s_waitcnt lgkmcnt(2)\n"
  "v_fma_f32 v16, v16, s73, v224\n"
  "v_fma_f32 v17, v17, s73, v224\n"
  "v_mfma_f32_16x16x32_bf16 v[32:35], a[172:175], a[56:59], v[32:35]\n"
  "s_waitcnt lgkmcnt(1)\n"
  "v_exp_f32 v16, v16\n"
  "v_mfma_f32_16x16x32_bf16 v[32:35], a[168:171], a[52:55], v[32:35]\n"
  "s_waitcnt lgkmcnt(0)\n"
  "v_exp_f32 v17, v17\n"
  "v_mfma_f32_16x16x32_bf16 v[32:35], a[164:167], a[48:51], v[32:35]\n"
  "v_fma_f32 v18, v18, s73, v224\n"
  "v_fma_f32 v19, v19, s73, v224\n"
  "v_mfma_f32_16x16x32_bf16 v[36:39], a[208:211], a[68:71], v[248:251]\n"
  "v_exp_f32 v18, v18\n"
  "v_mfma_f32_16x16x32_bf16 v[36:39], a[204:207], a[64:67], v[36:39]\n"
  "v_exp_f32 v19, v19\n"
  "v_mfma_f32_16x16x32_bf16 v[36:39], a[200:203], a[60:63], v[36:39]\n"
  "v_max3_f32 v225, v32, v33, v34\n"
  "v_max_f32 v225, v225, v35\n"
  "v_mfma_f32_16x16x32_bf16 v[36:39], a[196:199], a[56:59], v[36:39]\n"
  "v_mul_f32 v225, s74, v225\n"
  "v_add_f32 v225, s64, v225\n"
  "v_mfma_f32_16x16x32_bf16 v[36:39], a[192:195], a[52:55], v[36:39]\n"
  "ds_bpermute_b32 v225, v234, v225\n"
  "v_mfma_f32_16x16x32_bf16 v[36:39], a[188:191], a[48:51], v[36:39]\n"
  "v_fma_f32 v20, v20, s73, v224\n"
  "v_fma_f32 v21, v21, s73, v224\n"
  "v_mfma_f32_16x16x32_bf16 v[136:139], v[24:27], a[144:147], 1.0\n"
  "v_exp_f32 v20, v20\n"
  "v_mfma_f32_16x16x32_bf16 v[140:143], v[24:27], a[148:151], 1.0\n"
  "v_exp_f32 v21, v21\n"
  "v_mfma_f32_16x16x32_bf16 v[144:147], v[24:27], v[84:87], 1.0\n"
  "v_fma_f32 v22, v22, s73, v224\n"
  "v_fma_f32 v23, v23, s73, v224\n"
  "v_mfma_f32_16x16x32_bf16 v[148:151], v[24:27], v[244:247], 1.0\n"
  "v_exp_f32 v22, v22\n"
  "v_mfma_f32_16x16x32_bf16 v[120:123], v[24:27], v[76:79], 1.0\n"
  "s_waitcnt lgkmcnt(0)\n"
  "v_fma_f32 v32, v32, s73, v225\n"
  "v_fma_f32 v33, v33, s73, v225\n"
  "v_mfma_f32_16x16x32_bf16 v[124:127], v[24:27], v[80:83], 1.0\n"
  "v_exp_f32 v32, v32\n"
  "v_mfma_f32_16x16x32_bf16 v[128:131], v[24:27], a[252:255], 1.0\n"
  "v_exp_f32 v33, v33\n"
  "v_fma_f32 v34, v34, s73, v225\n"
  "v_exp_f32 v34, v34\n"
  "v_fma_f32 v35, v35, s73, v225\n"
  "v_exp_f32 v35, v35\n"
  "v_mfma_f32_16x16x32_bf16 v[132:135], v[24:27], v[236:239], 1.0\n"
  "v_cvt_pk_bf16_f32 v40, v32, v33\n"
  "v_cvt_pk_bf16_f32 v41, v34, v35\n"
  "v_mfma_f32_16x16x32_bf16 v[32:35], a[232:235], a[68:71], v[248:251]\n"
  "v_fma_f32 v36, v36, s73, v225\n"
  "v_fma_f32 v37, v37, s73, v225\n"
  "v_mfma_f32_16x16x32_bf16 v[32:35], a[228:231], a[64:67], v[32:35]\n"
  "v_exp_f32 v36, v36\n"
  "v_mfma_f32_16x16x32_bf16 v[32:35], a[224:227], a[60:63], v[32:35]\n"
  "v_exp_f32 v37, v37\n"
  "v_mfma_f32_16x16x32_bf16 v[32:35], a[220:223], a[56:59], v[32:35]\n"
  "v_fma_f32 v38, v38, s73, v225\n"
  "v_fma_f32 v39, v39, s73, v225\n"
  "v_mfma_f32_16x16x32_bf16 v[32:35], a[216:219], a[52:55], v[32:35]\n"
  "v_exp_f32 v38, v38\n"
  "v_exp_f32 v39, v39\n"
  "v_mfma_f32_16x16x32_bf16 v[32:35], a[212:215], a[48:51], v[32:35]\n"
  "v_cvt_pk_bf16_f32 v42, v36, v37\n"
  "v_cvt_pk_bf16_f32 v43, v38, v39\n"
  "v_mfma_f32_16x16x32_bf16 v[36:39], a[120:123], a[48:51], v[248:251]\n"
  "s_waitcnt vmcnt(6)\n"
  "ds_read_b128 a[72:75], v222 offset:11200\n"
  "ds_read_b128 a[76:79], v222 offset:11264\n"
  "ds_read_b128 a[80:83], v222 offset:13440\n"
  "v_mfma_f32_16x16x32_bf16 v[36:39], a[124:127], a[52:55], v[36:39]\n"
  "ds_read_b128 a[84:87], v222 offset:13504\n"
  "ds_read_b128 a[88:91], v222 offset:15680\n"
  "ds_read_b128 a[92:95], v222 offset:15744\n"
  "v_mfma_f32_16x16x32_bf16 v[36:39], a[128:131], a[56:59], v[36:39]\n"
  "v_exp_f32 v23, v23\n"
  "v_mfma_f32_16x16x32_bf16 v[36:39], a[132:135], a[60:63], v[36:39]\n"
  "v_fma_f32 v32, v32, s73, v225\n"
  "v_fma_f32 v33, v33, s73, v225\n"
  "v_mfma_f32_16x16x32_bf16 v[36:39], a[136:139], a[64:67], v[36:39]\n"
  "v_exp_f32 v32, v32\n"
  "v_mfma_f32_16x16x32_bf16 v[36:39], a[140:143], a[68:71], v[36:39]\n"
  "s_waitcnt lgkmcnt(5)\n"
  "v_exp_f32 v33, v33\n"
  "v_mfma_f32_16x16x32_bf16 v[48:51], a[164:167], a[72:75], v[248:251]\n"
  "s_waitcnt lgkmcnt(4)\n"
  "v_fma_f32 v34, v34, s73, v225\n"
  "v_fma_f32 v35, v35, s73, v225\n"
  "v_mfma_f32_16x16x32_bf16 v[48:51], a[168:171], a[76:79], v[48:51]\n"
  "s_waitcnt lgkmcnt(3)\n"
  "v_exp_f32 v34, v34\n"
  "v_mfma_f32_16x16x32_bf16 v[48:51], a[172:175], a[80:83], v[48:51]\n"
  "s_waitcnt lgkmcnt(2)\n"
  "v_exp_f32 v35, v35\n"
  "v_mfma_f32_16x16x32_bf16 v[48:51], a[176:179], a[84:87], v[48:51]\n"
  "s_waitcnt lgkmcnt(1)\n"
  "v_fma_f32 v36, v36, s73, v225\n"
  "v_fma_f32 v37, v37, s73, v225\n"
  "v_mfma_f32_16x16x32_bf16 v[48:51], a[180:183], a[88:91], v[48:51]\n"
  "s_waitcnt lgkmcnt(0)\n"
  "v_exp_f32 v36, v36\n"
  "v_mfma_f32_16x16x32_bf16 v[48:51], a[184:187], a[92:95], v[48:51]\n"
  "v_exp_f32 v37, v37\n"
  "v_mfma_f32_16x16x32_bf16 v[52:55], a[188:191], a[72:75], v[248:251]\n"
  "v_fma_f32 v38, v38, s73, v225\n"
  "v_fma_f32 v39, v39, s73, v225\n"
  "v_mfma_f32_16x16x32_bf16 v[52:55], a[192:195], a[76:79], v[52:55]\n"
  "v_exp_f32 v38, v38\n"
  "v_mfma_f32_16x16x32_bf16 v[52:55], a[196:199], a[80:83], v[52:55]\n"
  "v_max3_f32 v226, v48, v49, v50\n"
  "v_max_f32 v226, v226, v51\n"
  "v_mfma_f32_16x16x32_bf16 v[52:55], a[200:203], a[84:87], v[52:55]\n"
  "v_mul_f32 v226, s74, v226\n"
  "v_add_f32 v226, s64, v226\n"
  "v_mfma_f32_16x16x32_bf16 v[52:55], a[204:207], a[88:91], v[52:55]\n"
  "ds_bpermute_b32 v226, v234, v226\n"
  "v_mfma_f32_16x16x32_bf16 v[52:55], a[208:211], a[92:95], v[52:55]\n"
  "v_exp_f32 v39, v39\n"
  "v_mfma_f32_16x16x32_bf16 v[168:171], v[40:43], a[144:147], 1.0\n"
  "ds_write_b64 v231, v[64:65] offset:2048\n"
  "v_mfma_f32_16x16x32_bf16 v[172:175], v[40:43], a[148:151], 1.0\n"
  "v_cvt_pk_bf16_f32 v12, v0, v1\n"
  "v_cvt_pk_bf16_f32 v13, v2, v3\n"
  "v_mfma_f32_16x16x32_bf16 v[176:179], v[40:43], v[84:87], 1.0\n"
  "v_cvt_pk_bf16_f32 v14, v4, v5\n"
  "v_cvt_pk_bf16_f32 v15, v6, v7\n"
  "v_mfma_f32_16x16x32_bf16 v[180:183], v[40:43], v[244:247], 1.0\n"
  "v_cvt_pk_bf16_f32 v28, v16, v17\n"
  "v_cvt_pk_bf16_f32 v29, v18, v19\n"
  "v_mfma_f32_16x16x32_bf16 v[152:155], v[40:43], v[76:79], 1.0\n"
  "s_waitcnt lgkmcnt(1)\n"
  "v_fma_f32 v48, v48, s73, v226\n"
  "v_fma_f32 v49, v49, s73, v226\n"
  "v_mfma_f32_16x16x32_bf16 v[156:159], v[40:43], v[80:83], 1.0\n"
  "v_exp_f32 v48, v48\n"
  "v_mfma_f32_16x16x32_bf16 v[160:163], v[40:43], a[252:255], 1.0\n"
  "v_exp_f32 v49, v49\n"
  "v_fma_f32 v50, v50, s73, v226\n"
  "v_exp_f32 v50, v50\n"
  "v_fma_f32 v51, v51, s73, v226\n"
  "v_exp_f32 v51, v51\n"
  "v_mfma_f32_16x16x32_bf16 v[164:167], v[40:43], v[236:239], 1.0\n"
  "v_cvt_pk_bf16_f32 v56, v48, v49\n"
  "v_cvt_pk_bf16_f32 v57, v50, v51\n"
  "v_mfma_f32_16x16x32_bf16 v[48:51], a[212:215], a[72:75], v[248:251]\n"
  "v_fma_f32 v52, v52, s73, v226\n"
  "v_fma_f32 v53, v53, s73, v226\n"
  "v_mfma_f32_16x16x32_bf16 v[48:51], a[216:219], a[76:79], v[48:51]\n"
  "v_exp_f32 v52, v52\n"
  "v_mfma_f32_16x16x32_bf16 v[48:51], a[220:223], a[80:83], v[48:51]\n"
  "v_exp_f32 v53, v53\n"
  "v_mfma_f32_16x16x32_bf16 v[48:51], a[224:227], a[84:87], v[48:51]\n"
  "v_fma_f32 v54, v54, s73, v226\n"
  "v_fma_f32 v55, v55, s73, v226\n"
  "v_mfma_f32_16x16x32_bf16 v[48:51], a[228:231], a[88:91], v[48:51]\n"
  "v_exp_f32 v54, v54\n"
  "v_exp_f32_e64 v55, v55\n"
  "v_mfma_f32_16x16x32_bf16 v[48:51], a[232:235], a[92:95], v[48:51]\n"
  "v_cvt_pk_bf16_f32 v58, v52, v53\n"
  "v_cvt_pk_bf16_f32 v59, v54, v55\n"
  "v_mfma_f32_16x16x32_bf16 v[52:55], a[140:143], a[92:95], v[248:251]\n"
  "v_cvt_pk_bf16_f32 v30, v20, v21\n"
  "v_cvt_pk_bf16_f32 v31, v22, v23\n"
  "v_mfma_f32_16x16x32_bf16 v[52:55], a[136:139], a[88:91], v[52:55]\n"
  "v_cvt_pk_bf16_f32 v44, v32, v33\n"
  "v_cvt_pk_bf16_f32 v45, v34, v35\n"
  "v_mfma_f32_16x16x32_bf16 v[52:55], a[132:135], a[84:87], v[52:55]\n"
  "v_fma_f32 v48, v48, s73, v226\n"
  "v_fma_f32 v49, v49, s73, v226\n"
  "v_mfma_f32_16x16x32_bf16 v[52:55], a[128:131], a[80:83], v[52:55]\n"
  "v_exp_f32 v48, v48\n"
  "v_mfma_f32_16x16x32_bf16 v[52:55], a[124:127], a[76:79], v[52:55]\n"
  "v_exp_f32 v49, v49\n"
  "v_mfma_f32_16x16x32_bf16 v[52:55], a[120:123], a[72:75], v[52:55]\n"
  "v_fma_f32 v50, v50, s73, v226\n"
  "v_fma_f32 v51, v51, s73, v226\n"
  "v_mfma_f32_16x16x32_bf16 v[200:203], v[56:59], a[144:147], 1.0\n"
  "v_exp_f32 v50, v50\n"
  "v_mfma_f32_16x16x32_bf16 v[204:207], v[56:59], a[148:151], 1.0\n"
  "v_exp_f32 v51, v51\n"
  "v_mfma_f32_16x16x32_bf16 v[208:211], v[56:59], v[84:87], 1.0\n"
  "v_fma_f32 v52, v52, s73, v226\n"
  "v_fma_f32 v53, v53, s73, v226\n"
  "v_mfma_f32_16x16x32_bf16 v[212:215], v[56:59], v[244:247], 1.0\n"
  "v_exp_f32 v52, v52\n"
  "v_mfma_f32_16x16x32_bf16 v[184:187], v[56:59], v[76:79], 1.0\n"
  "v_exp_f32 v53, v53\n"
  "v_mfma_f32_16x16x32_bf16 v[188:191], v[56:59], v[80:83], 1.0\n"
  "v_fma_f32 v54, v54, s73, v226\n"
  "v_fma_f32 v55, v55, s73, v226\n"
  "v_mfma_f32_16x16x32_bf16 v[192:195], v[56:59], a[252:255], 1.0\n"
  "v_exp_f32_e64 v54, v54\n"
  "v_mfma_f32_16x16x32_bf16 v[196:199], v[56:59], v[236:239], 1.0\n"
  "v_exp_f32_e64 v55, v55\n"
  "v_cvt_pk_bf16_f32 v46, v36, v37\n"
  "v_cvt_pk_bf16_f32 v47, v38, v39\n"
  "v_cvt_pk_bf16_f32 v60, v48, v49\n"
  "v_cvt_pk_bf16_f32 v61, v50, v51\n"
  "v_cvt_pk_bf16_f32 v62, v52, v53\n"
  "v_cvt_pk_bf16_f32 v63, v54, v55\n"
  "s_add_i32 s76, s76, 1\n"
  "s_mov_b32 m0, s67\n"
  "s_waitcnt vmcnt(0) lgkmcnt(0)\n"
  "s_barrier\n"
  "ds_read_b128 a[124:127], v216 offset:0\n"
  "ds_read_b64 a[160:161], v231 offset:2048\n"
  "ds_read_b64 a[162:163], v232 offset:2048\n"
  "buffer_load_dwordx4 v221, s[40:43], 0 offen offset:0 lds\n"
  "ds_read_b128 a[120:123], v216 offset:64\n"
  "s_addk_i32 m0, 896\n"
  "ds_read_b128 a[140:143], v216 offset:1024\n"
  "ds_read_b128 a[136:139], v216 offset:1088\n"
  "buffer_load_dwordx4 v221, s[40:43], 0 offen offset:128 lds\n"
  "s_addk_i32 m0, 896\n"
  "ds_read_b128 a[144:147], v216 offset:2048\n"
  "ds_read_b128 a[148:151], v216 offset:2112\n"
  "buffer_load_dwordx4 v221, s[40:43], 0 offen offset:256 lds\n"
  "s_waitcnt lgkmcnt(4)\n"
  "v_mfma_f32_16x16x32_bf16 v[64:67], a[124:127], a[96:99], v[248:251]\n"
  "s_add_u32 m0, s59, -128\n"
  "v_mfma_f32_16x16x32_bf16 v[64:67], a[120:123], a[100:103], v[64:67]\n"
  "s_waitcnt lgkmcnt(2)\n"
  "v_mfma_f32_16x16x32_bf16 v[64:67], a[140:143], a[104:107], v[64:67]\n"
  "buffer_load_dwordx4 v221, s[32:35], 0 offen offset:128 lds\n"
  "v_mfma_f32_16x16x32_bf16 v[64:67], a[136:139], a[108:111], v[64:67]\n"
  "s_waitcnt lgkmcnt(0)\n"
  "v_mfma_f32_16x16x32_bf16 v[64:67], a[144:147], a[112:115], v[64:67]\n"
  "s_addk_i32 m0, 1024\n"
  "v_mfma_f32_16x16x32_bf16 v[64:67], a[148:151], a[116:119], v[64:67]\n"
  "s_mov_b64 s[32:33], s[44:45]\n"
  "v_mfma_f32_16x16x32_bf16 v[0:3], a[148:151], a[20:23], v[248:251]\n"
  "buffer_load_dwordx4 v221, s[44:47], 0 offen offset:128 lds\n"
  "v_mfma_f32_16x16x32_bf16 v[0:3], a[144:147], a[16:19], v[0:3]\n"
  "s_addk_i32 m0, 896\n"
  "v_mfma_f32_16x16x32_bf16 v[0:3], a[136:139], a[12:15], v[0:3]\n"
  "s_add_u32 s40, s40, s31\n"
  "v_mfma_f32_16x16x32_bf16 v[0:3], a[140:143], a[8:11], v[0:3]\n"
  "s_addc_u32 s41, s41, 0\n"
  "v_mfma_f32_16x16x32_bf16 v[0:3], a[120:123], a[4:7], v[0:3]\n"
  "buffer_load_dwordx4 v221, s[44:47], 0 offen offset:256 lds\n"
  "v_mfma_f32_16x16x32_bf16 v[0:3], a[124:127], a[0:3], v[0:3]\n"
  "s_add_u32 s44, s44, s31\n"
  "v_mfma_f32_16x16x32_bf16 v[16:19], a[124:127], a[24:27], v[248:251]\n"
  "s_addc_u32 s45, s45, 0\n"
  "v_mfma_f32_16x16x32_bf16 v[16:19], a[120:123], a[28:31], v[16:19]\n"
  "v_mfma_f32_16x16x32_bf16 v[16:19], a[140:143], a[32:35], v[16:19]\n"
  "ds_read_b128 a[132:135], v235 offset:0\n"
  "v_mfma_f32_16x16x32_bf16 v[16:19], a[136:139], a[36:39], v[16:19]\n"
  "ds_read_b128 a[128:131], v235 offset:64\n"
  "v_mfma_f32_16x16x32_bf16 v[16:19], a[144:147], a[40:43], v[16:19]\n"
  "v_mfma_f32_16x16x32_bf16 v[16:19], a[148:151], a[44:47], v[16:19]\n"
  "v_mfma_f32_16x16x32_bf16 v[32:35], a[148:151], a[68:71], v[248:251]\n"
  "v_mfma_f32_16x16x32_bf16 v[32:35], a[144:147], a[64:67], v[32:35]\n"
  "v_mfma_f32_16x16x32_bf16 v[32:35], a[136:139], a[60:63], v[32:35]\n"
  "v_mfma_f32_16x16x32_bf16 v[32:35], a[140:143], a[56:59], v[32:35]\n"
  "v_mfma_f32_16x16x32_bf16 v[32:35], a[120:123], a[52:55], v[32:35]\n"
  "v_mfma_f32_16x16x32_bf16 v[32:35], a[124:127], a[48:51], v[32:35]\n"
  "v_mfma_f32_16x16x32_bf16 v[48:51], a[124:127], a[72:75], v[248:251]\n"
  "ds_read_b128 a[124:127], v235 offset:1024\n"
  "v_mfma_f32_16x16x32_bf16 v[48:51], a[120:123], a[76:79], v[48:51]\n"
  "ds_read_b128 a[120:123], v235 offset:1088\n"
  "v_mfma_f32_16x16x32_bf16 v[48:51], a[140:143], a[80:83], v[48:51]\n"
  "ds_read_b128 a[140:143], v235 offset:2048\n"
  "v_mfma_f32_16x16x32_bf16 v[48:51], a[136:139], a[84:87], v[48:51]\n"
  "ds_read_b128 a[136:139], v235 offset:2112\n"
  "v_mfma_f32_16x16x32_bf16 v[48:51], a[144:147], a[88:91], v[48:51]\n"
  "v_mfma_f32_16x16x32_bf16 v[48:51], a[148:151], a[92:95], v[48:51]\n"
  "s_waitcnt lgkmcnt(4)\n"
  "v_mfma_f32_16x16x32_bf16 v[4:7], a[132:135], a[0:3], v[248:251]\n"
  "v_mfma_f32_16x16x32_bf16 v[4:7], a[128:131], a[4:7], v[4:7]\n"
  "s_waitcnt lgkmcnt(2)\n"
  "v_mfma_f32_16x16x32_bf16 v[4:7], a[124:127], a[8:11], v[4:7]\n"
  "v_mfma_f32_16x16x32_bf16 v[4:7], a[120:123], a[12:15], v[4:7]\n"
  "s_waitcnt lgkmcnt(0)\n"
  "v_mfma_f32_16x16x32_bf16 v[4:7], a[140:143], a[16:19], v[4:7]\n"
  "v_mfma_f32_16x16x32_bf16 v[4:7], a[136:139], a[20:23], v[4:7]\n"
  "v_mfma_f32_16x16x32_bf16 v[20:23], a[136:139], a[44:47], v[248:251]\n"
  "v_mfma_f32_16x16x32_bf16 v[20:23], a[140:143], a[40:43], v[20:23]\n"
  "v_mfma_f32_16x16x32_bf16 v[20:23], a[120:123], a[36:39], v[20:23]\n"
  "ds_read_b64_tr_b16 a[144:145], v217 offset:2048\n"
  "v_mfma_f32_16x16x32_bf16 v[20:23], a[124:127], a[32:35], v[20:23]\n"
  "ds_read_b64_tr_b16 a[146:147], v218 offset:2048\n"
  "v_mfma_f32_16x16x32_bf16 v[20:23], a[128:131], a[28:31], v[20:23]\n"
  "ds_read_b64_tr_b16 a[148:149], v217 offset:2064\n"
  "v_mfma_f32_16x16x32_bf16 v[20:23], a[132:135], a[24:27], v[20:23]\n"
  "ds_read_b64_tr_b16 a[150:151], v218 offset:2064\n"
  "v_mfma_f32_16x16x32_bf16 v[36:39], a[132:135], a[48:51], v[248:251]\n"
  "v_mfma_f32_16x16x32_bf16 v[36:39], a[128:131], a[52:55], v[36:39]\n"
  "v_mfma_f32_16x16x32_bf16 v[36:39], a[124:127], a[56:59], v[36:39]\n"
  "v_mfma_f32_16x16x32_bf16 v[36:39], a[120:123], a[60:63], v[36:39]\n"
  "v_mfma_f32_16x16x32_bf16 v[36:39], a[140:143], a[64:67], v[36:39]\n"
  "v_mfma_f32_16x16x32_bf16 v[36:39], a[136:139], a[68:71], v[36:39]\n"
  "v_mfma_f32_16x16x32_bf16 v[52:55], a[136:139], a[92:95], v[248:251]\n"
  "ds_read_b64_tr_b16 a[136:137], v219 offset:2048\n"
  "v_mfma_f32_16x16x32_bf16 v[52:55], a[140:143], a[88:91], v[52:55]\n"
  "ds_read_b64_tr_b16 a[138:139], v220 offset:2048\n"
  "v_fma_f32 v64, v64, s73, v227\n"
  "v_mfma_f32_16x16x32_bf16 v[52:55], a[120:123], a[84:87], v[52:55]\n"
  "ds_read_b64_tr_b16 a[140:141], v219 offset:2064\n"
  "v_fma_f32 v65, v65, s73, v227\n"
  "v_mfma_f32_16x16x32_bf16 v[52:55], a[124:127], a[80:83], v[52:55]\n"
  "ds_read_b64_tr_b16 a[142:143], v220 offset:2064\n"
  "v_fma_f32 v66, v66, s73, v227\n"
  "v_mfma_f32_16x16x32_bf16 v[52:55], a[128:131], a[76:79], v[52:55]\n"
  "v_exp_f32 v64, v64\n"
  "v_mfma_f32_16x16x32_bf16 v[52:55], a[132:135], a[72:75], v[52:55]\n"
  "s_waitcnt lgkmcnt(4)\n"
  "v_exp_f32 v65, v65\n"
  "v_mfma_f32_16x16x32_bf16 v[104:107], v[12:15], a[144:147], v[104:107]\n"
  "v_exp_f32 v66, v66\n"
  "v_mfma_f32_16x16x32_bf16 v[108:111], v[12:15], a[148:151], v[108:111]\n"
  "v_mfma_f32_16x16x32_bf16 v[136:139], v[28:31], a[144:147], v[136:139]\n"
  "ds_read_b64_tr_b16 a[120:121], v217 offset:0\n"
  "v_mfma_f32_16x16x32_bf16 v[140:143], v[28:31], a[148:151], v[140:143]\n"
  "ds_read_b64_tr_b16 a[122:123], v218 offset:0\n"
  "v_mfma_f32_16x16x32_bf16 v[168:171], v[44:47], a[144:147], v[168:171]\n"
  "ds_read_b64_tr_b16 a[124:125], v217 offset:1024\n"
  "v_mfma_f32_16x16x32_bf16 v[172:175], v[44:47], a[148:151], v[172:175]\n"
  "ds_read_b64_tr_b16 a[126:127], v218 offset:1024\n"
  "v_mfma_f32_16x16x32_bf16 v[200:203], v[60:63], a[144:147], v[200:203]\n"
  "v_mfma_f32_16x16x32_bf16 v[204:207], v[60:63], a[148:151], v[204:207]\n"
  "v_fma_f32 v67, v67, s73, v227\n"
  "v_cvt_pk_bf16_f32 v64, v64, v65\n"
  "v_mfma_f32_16x16x32_bf16 a[244:247], a[160:163], a[144:147], a[244:247]\n"
  "v_mfma_f32_16x16x32_bf16 a[248:251], a[160:163], a[148:151], a[248:251]\n"
  "s_waitcnt lgkmcnt(4)\n"
  "v_mfma_f32_16x16x32_bf16 v[208:211], v[60:63], a[136:139], v[208:211]\n"
  "v_mfma_f32_16x16x32_bf16 v[212:215], v[60:63], a[140:143], v[212:215]\n"
  "v_mfma_f32_16x16x32_bf16 v[176:179], v[44:47], a[136:139], v[176:179]\n"
  "ds_read_b64_tr_b16 a[128:129], v217 offset:16\n"
  "v_mfma_f32_16x16x32_bf16 v[180:183], v[44:47], a[140:143], v[180:183]\n"
  "ds_read_b64_tr_b16 a[130:131], v218 offset:16\n"
  "v_mfma_f32_16x16x32_bf16 v[144:147], v[28:31], a[136:139], v[144:147]\n"
  "ds_read_b64_tr_b16 a[132:133], v217 offset:1040\n"
  "v_mfma_f32_16x16x32_bf16 v[148:151], v[28:31], a[140:143], v[148:151]\n"
  "ds_read_b64_tr_b16 a[134:135], v218 offset:1040\n"
  "v_mfma_f32_16x16x32_bf16 v[112:115], v[12:15], a[136:139], v[112:115]\n"
  "v_exp_f32 v67, v67\n"
  "v_mfma_f32_16x16x32_bf16 v[116:119], v[12:15], a[140:143], v[116:119]\n"
  "s_waitcnt lgkmcnt(4)\n"
  "v_mfma_f32_16x16x32_bf16 a[164:167], v[8:11], a[120:123], 1.0\n"
  "v_mfma_f32_16x16x32_bf16 a[164:167], v[12:15], a[124:127], a[164:167]\n"
  "v_mfma_f32_16x16x32_bf16 a[180:183], v[24:27], a[120:123], 1.0\n"
  "v_mfma_f32_16x16x32_bf16 a[180:183], v[28:31], a[124:127], a[180:183]\n"
  "v_cvt_pk_bf16_f32 v65, v66, v67\n"
  "v_fma_f32 v0, v0, s73, v223\n"
  "v_mfma_f32_16x16x32_bf16 a[196:199], v[40:43], a[120:123], 1.0\n"
  "s_mov_b32 m0, s68\n"
  "ds_read_b64_tr_b16 a[136:137], v219 offset:0\n"
  "v_mfma_f32_16x16x32_bf16 a[196:199], v[44:47], a[124:127], a[196:199]\n"
  "ds_read_b64_tr_b16 a[138:139], v220 offset:0\n"
  "v_fma_f32 v1, v1, s73, v223\n"
  "v_mfma_f32_16x16x32_bf16 a[212:215], v[56:59], a[120:123], 1.0\n"
  "ds_read_b64_tr_b16 a[140:141], v219 offset:1024\n"
  "v_fma_f32 v2, v2, s73, v223\n"
  "v_mfma_f32_16x16x32_bf16 a[212:215], v[60:63], a[124:127], a[212:215]\n"
  "ds_read_b64_tr_b16 a[142:143], v220 offset:1024\n"
  "v_fma_f32 v3, v3, s73, v223\n"
  "v_mfma_f32_16x16x32_bf16 a[228:231], a[156:159], a[120:123], 1.0\n"
  "buffer_load_dwordx4 v221, s[40:43], 0 offen offset:0 lds\n"
  "v_mfma_f32_16x16x32_bf16 a[228:231], a[160:163], a[124:127], a[228:231]\n"
  "s_waitcnt lgkmcnt(4)\n"
  "v_mfma_f32_16x16x32_bf16 a[232:235], a[156:159], a[128:131], 1.0\n"
  "s_addk_i32 m0, 896\n"
  "v_exp_f32 v0, v0\n"
  "v_mfma_f32_16x16x32_bf16 a[232:235], a[160:163], a[132:135], a[232:235]\n"
  "ds_write_b64 v231, v[64:65] offset:0\n"
  "v_mfma_f32_16x16x32_bf16 a[216:219], v[56:59], a[128:131], 1.0\n"
  "buffer_load_dwordx4 v221, s[40:43], 0 offen offset:128 lds\n"
  "v_fma_f32 v16, v16, s73, v224\n"
  "v_mfma_f32_16x16x32_bf16 a[216:219], v[60:63], a[132:135], a[216:219]\n"
  "s_addk_i32 m0, 896\n"
  "ds_read_b64_tr_b16 a[120:121], v219 offset:16\n"
  "v_fma_f32 v17, v17, s73, v224\n"
  "v_mfma_f32_16x16x32_bf16 a[200:203], v[40:43], a[128:131], 1.0\n"
  "ds_read_b64_tr_b16 a[122:123], v220 offset:16\n"
  "v_fma_f32 v18, v18, s73, v224\n"
  "v_mfma_f32_16x16x32_bf16 a[200:203], v[44:47], a[132:135], a[200:203]\n"
  "s_nop 0\n"
  "ds_read_b64_tr_b16 a[124:125], v219 offset:1040\n"
  "v_fma_f32 v19, v19, s73, v224\n"
  "v_mfma_f32_16x16x32_bf16 a[184:187], v[24:27], a[128:131], 1.0\n"
  "buffer_load_dwordx4 v221, s[40:43], 0 offen offset:256 lds\n"
  "v_fma_f32 v32, v32, s73, v225\n"
  "v_mfma_f32_16x16x32_bf16 a[184:187], v[28:31], a[132:135], a[184:187]\n"
  "s_mov_b32 m0, s60\n"
  "ds_read_b64_tr_b16 a[126:127], v220 offset:1040\n"
  "v_fma_f32 v33, v33, s73, v225\n"
  "v_mfma_f32_16x16x32_bf16 a[168:171], v[8:11], a[128:131], 1.0\n"
  "s_add_u32 s28, s96, 184448\n"
  "v_exp_f32 v1, v1\n"
  "v_mfma_f32_16x16x32_bf16 a[168:171], v[12:15], a[132:135], a[168:171]\n"
  "s_waitcnt lgkmcnt(5)\n"
  "v_exp_f32 v2, v2\n"
  "v_mfma_f32_16x16x32_bf16 a[172:175], v[8:11], a[136:139], 1.0\n"
  "buffer_load_dwordx4 v221, s[32:35], 0 offen offset:0 lds\n"
  "v_add_f32 v252, v0, v1\n"
  "v_mfma_f32_16x16x32_bf16 a[172:175], v[12:15], a[140:143], a[172:175]\n"
  "s_addk_i32 m0, 1024\n"
  "v_exp_f32 v3, v3\n"
  "v_mfma_f32_16x16x32_bf16 a[188:191], v[24:27], a[136:139], 1.0\n"
  "s_add_u32 s40, s40, s31\n"
  "v_exp_f32 v16, v16\n"
  "v_mfma_f32_16x16x32_bf16 a[188:191], v[28:31], a[140:143], a[188:191]\n"
  "s_addc_u32 s41, s41, 0\n"
  "v_exp_f32 v17, v17\n"
  "v_mfma_f32_16x16x32_bf16 a[204:207], v[40:43], a[136:139], 1.0\n"
  "buffer_load_dwordx4 v221, s[44:47], 0 offen offset:0 lds\n"
  "v_add_f32 v252, v252, v2\n"
  "v_mfma_f32_16x16x32_bf16 a[204:207], v[44:47], a[140:143], a[204:207]\n"
  "s_addk_i32 m0, 768\n"
  "v_exp_f32 v18, v18\n"
  "v_mfma_f32_16x16x32_bf16 a[220:223], v[56:59], a[136:139], 1.0\n"
  "s_mov_b64 s[32:33], s[44:45]\n"
  "v_exp_f32 v19, v19\n"
  "v_mfma_f32_16x16x32_bf16 a[220:223], v[60:63], a[140:143], a[220:223]\n"
  "s_waitcnt lgkmcnt(0)\n"
  "v_exp_f32 v32, v32\n"
  "v_mfma_f32_16x16x32_bf16 a[224:227], v[56:59], a[120:123], 1.0\n"
  "buffer_load_dwordx4 v221, s[44:47], 0 offen offset:256 lds\n"
  "v_add_f32_e64 v252, v252, v3\n"
  "v_mfma_f32_16x16x32_bf16 a[224:227], v[60:63], a[124:127], a[224:227]\n"
  "s_add_u32 s44, s44, s31\n"
  "v_exp_f32 v33, v33\n"
  "v_mfma_f32_16x16x32_bf16 a[208:211], v[40:43], a[120:123], 1.0\n"
  "s_addc_u32 s45, s45, 0\n"
  "v_add_f32 v253, v16, v17\n"
  "v_add_f32 v253, v253, v18\n"
  "v_mfma_f32_16x16x32_bf16 a[208:211], v[44:47], a[124:127], a[208:211]\n"
  "v_add_f32 v253, v253, v19\n"
  "v_fma_f32 v34, v34, s73, v225\n"
  "v_mfma_f32_16x16x32_bf16 a[192:195], v[24:27], a[120:123], 1.0\n"
  "s_cmp_eq_u32 s95, 0\n"
  "s_cselect_b32 s95, 3, s95\n"
  "s_subb_u32 s100, s100, -11\n"
  "v_exp_f32 v34, v34\n"
  "v_mfma_f32_16x16x32_bf16 a[192:195], v[28:31], a[124:127], a[192:195]\n"
  "s_waitcnt vmcnt(6)\n"
  "s_barrier\n"
  "ds_read_b128 a[128:131], v216 offset:12352\n"
  "v_mfma_f32_16x16x32_bf16 a[176:179], v[8:11], a[120:123], 1.0\n"
  "s_sub_u32 s95, s95, 1\n"
  "ds_read_b128 a[132:135], v216 offset:12416\n"
  "v_fma_f32 v35, v35, s73, v225\n"
  "v_mfma_f32_16x16x32_bf16 a[176:179], v[12:15], a[124:127], a[176:179]\n"
  "s_lshr_b32 s79, s100, 0\n"
  "ds_read_b128 a[120:123], v216 offset:13376\n"
  "v_add_f32 v254, v32, v33\n"
  "v_mfma_f32_16x16x32_bf16 v[68:71], v[8:11], a[152:155], 0\n"
  "s_mul_i32 s79, s79, 7\n"
  "ds_read_b128 a[124:127], v216 offset:13440\n"
  "v_cvt_pk_bf16_f32 v8, v0, v1\n"
  "v_mfma_f32_16x16x32_bf16 v[68:71], v[12:15], a[152:155], v[68:71]\n"
  "s_add_u32 s79, s79, s100\n"
  "ds_read_b128 a[136:139], v216 offset:14400\n"
  "v_cvt_pk_bf16_f32 v9, v2, v3\n"
  "v_mfma_f32_16x16x32_bf16 v[72:75], v[24:27], a[152:155], 0\n"
  "s_add_u32 s79, s79, s101\n"
  "ds_read_b128 a[140:143], v216 offset:14464\n"
  "v_cvt_pk_bf16_f32 v24, v16, v17\n"
  "v_mfma_f32_16x16x32_bf16 v[72:75], v[28:31], a[152:155], v[72:75]\n"
  "s_mov_b32 m0, s66\n"
  "v_exp_f32 v35, v35\n"
  "v_mfma_f32_16x16x32_bf16 v[76:79], v[40:43], a[152:155], 0\n"
  "s_min_u32 s79, s79, 0x1ff\n"
  "v_cvt_pk_bf16_f32 v25, v18, v19\n"
  "v_add_f32 v254, v254, v34\n"
  "v_mfma_f32_16x16x32_bf16 v[76:79], v[44:47], a[152:155], v[76:79]\n"
  "s_mul_i32 s96, s79, 196608\n"
  "v_add_f32 v254, v254, v35\n"
  "v_cvt_pk_bf16_f32 v40, v32, v33\n"
  "v_mfma_f32_16x16x32_bf16 v[80:83], v[56:59], a[152:155], 0\n"
  "s_add_u32 s96, s96, s39\n"
  "v_cvt_pk_bf16_f32 v41, v34, v35\n"
  "v_fma_f32 v48, v48, s73, v226\n"
  "v_mfma_f32_16x16x32_bf16 v[80:83], v[60:63], a[152:155], v[80:83]\n"
  "buffer_load_dwordx4 v221, s[40:43], 0 offen offset:0 lds\n"
  "v_mfma_f32_16x16x32_bf16 v[84:87], a[156:159], a[152:155], 0\n"
  "ds_read_b64 a[156:157], v231 offset:0\n"
  "v_fma_f32 v49, v49, s73, v226\n"
  "v_mfma_f32_16x16x32_bf16 v[84:87], a[160:163], a[152:155], v[84:87]\n"
  "s_branch L_CUT_%=\n"
  ".p2align 5\n"
  "L_BIG_%=:\n"
  "v_mfma_f32_16x16x32_bf16 v[16:19], a[120:123], a[24:27], v[248:251]\n"
  "s_mov_b32 m0, s60\n"
  "v_fma_f32 v64, v64, s73, v227\n"
  "v_fma_f32 v65, v65, s73, v227\n"
  "v_mfma_f32_16x16x32_bf16 v[16:19], a[124:127], a[28:31], v[16:19]\n"
  "buffer_load_dwordx4 v221, s[32:35], 0 offen offset:0 lds\n"
  "v_fma_f32 v66, v66, s73, v227\n"
  "v_mfma_f32_16x16x32_bf16 v[16:19], a[128:131], a[32:35], v[16:19]\n"
  "s_addk_i32 m0, 1024\n"
  "ds_read_b64_tr_b16 a[144:145], v217 offset:0\n"
  "v_fma_f32 v67, v67, s73, v227\n"
  "v_mfma_f32_16x16x32_bf16 v[16:19], a[132:135], a[36:39], v[16:19]\n"
  "s_mov_b64 s[36:37], s[44:45]\n"
  "ds_read_b64_tr_b16 a[146:147], v218 offset:0\n"
  "v_fma_f32 v0, v0, s73, v223\n"
  "v_mfma_f32_16x16x32_bf16 v[16:19], a[136:139], a[40:43], v[16:19]\n"
  "ds_read_b64_tr_b16 a[148:149], v217 offset:1024\n"
  "v_fma_f32 v1, v1, s73, v223\n"
  "v_mfma_f32_16x16x32_bf16 v[16:19], a[140:143], a[44:47], v[16:19]\n"
  "buffer_load_dwordx4 v221, s[44:47], 0 offen offset:0 lds\n"
  "v_fma_f32 v2, v2, s73, v223\n"
  "v_mfma_f32_16x16x32_bf16 v[32:35], a[140:143], a[68:71], v[248:251]\n"
  "s_addk_i32 m0, 768\n"
  "ds_read_b64_tr_b16 a[150:151], v218 offset:1024\n"
  "v_fma_f32 v3, v3, s73, v223\n"
  "v_mfma_f32_16x16x32_bf16 v[32:35], a[136:139], a[64:67], v[32:35]\n"
  "s_add_u32 s32, s88, s28\n"
  "v_exp_f32 v64, v64\n"
  "v_mfma_f32_16x16x32_bf16 v[32:35], a[132:135], a[60:63], v[32:35]\n"
  "s_addc_u32 s33, s89, 0\n"
  "v_exp_f32 v65, v65\n"
  "v_mfma_f32_16x16x32_bf16 v[32:35], a[128:131], a[56:59], v[32:35]\n"
  "buffer_load_dwordx4 v221, s[44:47], 0 offen offset:256 lds\n"
  "v_cvt_pk_bf16_f32 v64, v64, v65\n"
  "v_mfma_f32_16x16x32_bf16 v[32:35], a[124:127], a[52:55], v[32:35]\n"
  "s_add_u32 s44, s32, s31\n"
  "v_exp_f32 v66, v66\n"
  "v_mfma_f32_16x16x32_bf16 v[32:35], a[120:123], a[48:51], v[32:35]\n"
  "s_addc_u32 s45, s33, 0\n"
  "v_exp_f32 v67, v67\n"
  "v_mfma_f32_16x16x32_bf16 v[48:51], a[120:123], a[72:75], v[248:251]\n"
  "s_mov_b32 s46, s30\n"
  "v_exp_f32 v0, v0\n"
  "v_mfma_f32_16x16x32_bf16 v[48:51], a[124:127], a[76:79], v[48:51]\n"
  "v_exp_f32 v1, v1\n"
  "v_mfma_f32_16x16x32_bf16 v[48:51], a[128:131], a[80:83], v[48:51]\n"
  "ds_read_b64_tr_b16 a[120:121], v217 offset:16\n"
  "v_cvt_pk_bf16_f32 v65, v66, v67\n"
  "v_mfma_f32_16x16x32_bf16 v[48:51], a[132:135], a[84:87], v[48:51]\n"
  "ds_read_b64_tr_b16 a[122:123], v218 offset:16\n"
  "v_add_f32 v252, v252, v0\n"
  "v_mfma_f32_16x16x32_bf16 v[48:51], a[136:139], a[88:91], v[48:51]\n"
  "ds_read_b64_tr_b16 a[124:125], v217 offset:1040\n"
  "v_add_f32 v252, v252, v1\n"
  "v_mfma_f32_16x16x32_bf16 v[48:51], a[140:143], a[92:95], v[48:51]\n"
  "s_waitcnt lgkmcnt(3)\n"
  "ds_read_b64_tr_b16 a[126:127], v218 offset:1040\n"
  "v_fma_f32 v16, v16, s73, v224\n"
  "v_mfma_f32_16x16x32_bf16 a[228:231], a[156:159], a[144:147], a[228:231]\n"
  "v_exp_f32 v2, v2\n"
  "v_mfma_f32_16x16x32_bf16 a[228:231], a[160:163], a[148:151], a[228:231]\n"
  "v_exp_f32 v3, v3\n"
  "v_mfma_f32_16x16x32_bf16 a[212:215], v[56:59], a[144:147], a[212:215]\n"
  "v_exp_f32 v16, v16\n"
  "v_mfma_f32_16x16x32_bf16 a[212:215], v[60:63], a[148:151], a[212:215]\n"
  "v_add_f32 v252, v252, v2\n"
  "v_add_f32 v252, v252, v3\n"
  "v_mfma_f32_16x16x32_bf16 a[196:199], v[40:43], a[144:147], a[196:199]\n"
  "v_fma_f32 v17, v17, s73, v224\n"
  "v_fma_f32 v18, v18, s73, v224\n"
  "v_mfma_f32_16x16x32_bf16 a[196:199], v[44:47], a[148:151], a[196:199]\n"
  "v_exp_f32 v17, v17\n"
  "v_mfma_f32_16x16x32_bf16 a[180:183], v[24:27], a[144:147], a[180:183]\n"
  "ds_read_b64_tr_b16 a[128:129], v219 offset:0\n"
  "v_fma_f32 v19, v19, s73, v224\n"
  "v_mfma_f32_16x16x32_bf16 a[180:183], v[28:31], a[148:151], a[180:183]\n"
  "s_waitcnt vmcnt(14)\n"
  "ds_read_b64_tr_b16 a[130:131], v220 offset:0\n"
  "v_add_f32 v253, v253, v16\n"
  "v_mfma_f32_16x16x32_bf16 a[164:167], v[8:11], a[144:147], a[164:167]\n"
  "s_waitcnt vmcnt(12)\n"
  "ds_read_b64_tr_b16 a[132:133], v219 offset:1024\n"
  "v_add_f32 v253, v253, v17\n"
  "v_mfma_f32_16x16x32_bf16 a[164:167], v[12:15], a[148:151], a[164:167]\n"
  "ds_read_b64_tr_b16 a[134:135], v220 offset:1024\n"
  "v_fma_f32 v32, v32, s73, v225\n"
  "v_mfma_f32_16x16x32_bf16 v[84:87], a[156:159], a[152:155], v[84:87]\n"
  "v_exp_f32 v18, v18\n"
  "v_mfma_f32_16x16x32_bf16 v[84:87], a[160:163], a[152:155], v[84:87]\n"
  "s_waitcnt lgkmcnt(4)\n"
  "v_exp_f32 v19, v19\n"
  "v_mfma_f32_16x16x32_bf16 a[168:171], v[8:11], a[120:123], a[168:171]\n"
  "v_exp_f32 v32, v32\n"
  "v_mfma_f32_16x16x32_bf16 a[168:171], v[12:15], a[124:127], a[168:171]\n"
  "v_add_f32 v253, v253, v18\n"
  "v_add_f32 v253, v253, v19\n"
  "v_mfma_f32_16x16x32_bf16 a[184:187], v[24:27], a[120:123], a[184:187]\n"
  "ds_read_b128 a[136:139], v235 offset:0\n"
  "v_fma_f32 v33, v33, s73, v225\n"
  "v_mfma_f32_16x16x32_bf16 a[184:187], v[28:31], a[124:127], a[184:187]\n"
  "ds_read_b128 a[140:143], v235 offset:64\n"
  "v_fma_f32 v34, v34, s73, v225\n"
  "v_mfma_f32_16x16x32_bf16 a[200:203], v[40:43], a[120:123], a[200:203]\n"
  "ds_read_b128 a[144:147], v235 offset:1024\n"
  "v_fma_f32 v35, v35, s73, v225\n"
  "v_mfma_f32_16x16x32_bf16 a[200:203], v[44:47], a[124:127], a[200:203]\n"
  "ds_read_b128 a[148:151], v235 offset:1088\n"
  "v_add_f32 v254, v254, v32\n"
  "v_mfma_f32_16x16x32_bf16 a[216:219], v[56:59], a[120:123], a[216:219]\n"
  "v_exp_f32 v33, v33\n"
  "v_mfma_f32_16x16x32_bf16 a[216:219], v[60:63], a[124:127], a[216:219]\n"
  "v_exp_f32 v34, v34\n"
  "v_mfma_f32_16x16x32_bf16 a[232:235], a[156:159], a[120:123], a[232:235]\n"
  "ds_read_b128 a[120:123], v235 offset:2048\n"
  "v_add_f32 v254, v254, v33\n"
  "v_mfma_f32_16x16x32_bf16 a[232:235], a[160:163], a[124:127], a[232:235]\n"
  "s_waitcnt lgkmcnt(5)\n"
  "ds_read_b128 a[124:127], v235 offset:2112\n"
  "v_add_f32 v254, v254, v34\n"
  "v_mfma_f32_16x16x32_bf16 a[220:223], v[56:59], a[128:131], a[220:223]\n"
  "v_exp_f32 v35, v35\n"
  "v_mfma_f32_16x16x32_bf16 a[220:223], v[60:63], a[132:135], a[220:223]\n"
  "ds_write_b64 v231, v[64:65] offset:0\n"
  "v_mfma_f32_16x16x32_bf16 a[204:207], v[40:43], a[128:131], a[204:207]\n"
  "v_add_f32 v254, v254, v35\n"
  "v_fma_f32 v48, v48, s73, v226\n"
  "v_mfma_f32_16x16x32_bf16 a[204:207], v[44:47], a[132:135], a[204:207]\n"
  "v_exp_f32 v48, v48\n"
  "v_mfma_f32_16x16x32_bf16 a[188:191], v[24:27], a[128:131], a[188:191]\n"
  "v_fma_f32 v49, v49, s73, v226\n"
  "v_fma_f32 v50, v50, s73, v226\n"
  "v_mfma_f32_16x16x32_bf16 a[188:191], v[28:31], a[132:135], a[188:191]\n"
  "v_exp_f32 v49, v49\n"
  "v_mfma_f32_16x16x32_bf16 a[172:175], v[8:11], a[128:131], a[172:175]\n"
  "v_exp_f32 v50, v50\n"
  "v_mfma_f32_16x16x32_bf16 a[172:175], v[12:15], a[132:135], a[172:175]\n"
  "s_waitcnt lgkmcnt(5)\n"
  "v_fma_f32 v51, v51, s73, v226\n"
  "v_add_f32 v255, v255, v48\n"
  "v_mfma_f32_16x16x32_bf16 v[4:7], a[136:139], a[0:3], v[248:251]\n"
  "v_exp_f32 v51, v51\n"
  "v_mfma_f32_16x16x32_bf16 v[4:7], a[140:143], a[4:7], v[4:7]\n"
  "s_waitcnt lgkmcnt(3)\n"
  "v_add_f32 v255, v255, v49\n"
  "v_add_f32 v255, v255, v50\n"
  "v_mfma_f32_16x16x32_bf16 v[4:7], a[144:147], a[8:11], v[4:7]\n"
  "v_add_f32 v255, v255, v51\n"
  "v_mov_b32 v237, 0\n"
  "v_mfma_f32_16x16x32_bf16 v[4:7], a[148:151], a[12:15], v[4:7]\n"
  "s_waitcnt lgkmcnt(1)\n"
  "v_mov_b32 v238, 0\n"
  "v_mov_b32 v239, 0\n"
  "v_mfma_f32_16x16x32_bf16 v[4:7], a[120:123], a[16:19], v[4:7]\n"
  "v_mov_b32 v241, 0\n"
  "v_mov_b32 v242, 0\n"
  "v_mfma_f32_16x16x32_bf16 v[4:7], a[124:127], a[20:23], v[4:7]\n"
  "v_mov_b32 v243, 0\n"
  "v_bfe_u32 v234, v231, 1, 6\n"
  "v_mfma_f32_16x16x32_bf16 v[20:23], a[124:127], a[44:47], v[248:251]\n"
  "v_mfma_f32_16x16x32_bf16 v[20:23], a[120:123], a[40:43], v[20:23]\n"
  "v_mfma_f32_16x16x32_bf16 v[20:23], a[148:151], a[36:39], v[20:23]\n"
  "ds_read_b64_tr_b16 a[128:129], v219 offset:16\n"
  "v_fma_f32 v4, v4, s73, v223\n"
  "v_mfma_f32_16x16x32_bf16 v[20:23], a[144:147], a[32:35], v[20:23]\n"
  "ds_read_b64_tr_b16 a[130:131], v220 offset:16\n"
  "v_fma_f32 v5, v5, s73, v223\n"
  "v_mfma_f32_16x16x32_bf16 v[20:23], a[140:143], a[28:31], v[20:23]\n"
  "ds_read_b64_tr_b16 a[132:133], v219 offset:1040\n"
  "v_fma_f32 v6, v6, s73, v223\n"
  "v_mfma_f32_16x16x32_bf16 v[20:23], a[136:139], a[24:27], v[20:23]\n"
  "ds_read_b64_tr_b16 a[134:135], v220 offset:1040\n"
  "v_fma_f32 v7, v7, s73, v223\n"
  "v_mfma_f32_16x16x32_bf16 v[36:39], a[136:139], a[48:51], v[248:251]\n"
  "v_exp_f32 v4, v4\n"
  "v_mfma_f32_16x16x32_bf16 v[36:39], a[140:143], a[52:55], v[36:39]\n"
  "v_exp_f32 v5, v5\n"
  "v_mfma_f32_16x16x32_bf16 v[36:39], a[144:147], a[56:59], v[36:39]\n"
  "v_exp_f32 v6, v6\n"
  "v_mfma_f32_16x16x32_bf16 v[36:39], a[148:151], a[60:63], v[36:39]\n"
  "v_exp_f32 v7, v7\n"
  "v_mfma_f32_16x16x32_bf16 v[36:39], a[120:123], a[64:67], v[36:39]\n"
  "v_add_f32 v252, v252, v4\n"
  "v_add_f32 v252, v252, v5\n"
  "v_mfma_f32_16x16x32_bf16 v[36:39], a[124:127], a[68:71], v[36:39]\n"
  "v_add_f32 v252, v252, v6\n"
  "v_add_f32 v252, v252, v7\n"
  "v_mfma_f32_16x16x32_bf16 v[52:55], a[124:127], a[92:95], v[248:251]\n"
  "ds_read_b64_tr_b16 a[124:125], v217 offset:2048\n"
  "v_fma_f32 v20, v20, s73, v224\n"
  "v_mfma_f32_16x16x32_bf16 v[52:55], a[120:123], a[88:91], v[52:55]\n"
  "ds_read_b64_tr_b16 a[126:127], v218 offset:2048\n"
  "v_fma_f32 v21, v21, s73, v224\n"
  "v_mfma_f32_16x16x32_bf16 v[52:55], a[148:151], a[84:87], v[52:55]\n"
  "ds_read_b64_tr_b16 a[120:121], v217 offset:2064\n"
  "v_fma_f32 v22, v22, s73, v224\n"
  "v_mfma_f32_16x16x32_bf16 v[52:55], a[144:147], a[80:83], v[52:55]\n"
  "ds_read_b64_tr_b16 a[122:123], v218 offset:2064\n"
  "v_fma_f32 v23, v23, s73, v224\n"
  "v_mfma_f32_16x16x32_bf16 v[52:55], a[140:143], a[76:79], v[52:55]\n"
  "v_exp_f32 v20, v20\n"
  "v_mfma_f32_16x16x32_bf16 v[52:55], a[136:139], a[72:75], v[52:55]\n"
  "s_waitcnt lgkmcnt(4)\n"
  "v_exp_f32 v21, v21\n"
  "v_mfma_f32_16x16x32_bf16 a[176:179], v[8:11], a[128:131], a[176:179]\n"
  "v_cvt_pk_bf16_f32 v8, v0, v1\n"
  "v_cvt_pk_bf16_f32 v9, v2, v3\n"
  "v_mfma_f32_16x16x32_bf16 a[176:179], v[12:15], a[132:135], a[176:179]\n"
  "v_exp_f32 v22, v22\n"
  "v_mfma_f32_16x16x32_bf16 a[192:195], v[24:27], a[128:131], a[192:195]\n"
  "ds_read_b64_tr_b16 a[140:141], v219 offset:2048\n"
  "v_cvt_pk_bf16_f32 v24, v16, v17\n"
  "v_mfma_f32_16x16x32_bf16 a[192:195], v[28:31], a[132:135], a[192:195]\n"
  "ds_read_b64_tr_b16 a[142:143], v220 offset:2048\n"
  "v_cvt_pk_bf16_f32 v25, v18, v19\n"
  "v_mfma_f32_16x16x32_bf16 a[208:211], v[40:43], a[128:131], a[208:211]\n"
  "ds_read_b64_tr_b16 a[136:137], v219 offset:2064\n"
  "v_cvt_pk_bf16_f32 v40, v32, v33\n"
  "v_mfma_f32_16x16x32_bf16 a[208:211], v[44:47], a[132:135], a[208:211]\n"
  "ds_read_b64_tr_b16 a[138:139], v220 offset:2064\n"
  "v_cvt_pk_bf16_f32 v41, v34, v35\n"
  "v_mfma_f32_16x16x32_bf16 a[224:227], v[56:59], a[128:131], a[224:227]\n"
  "v_exp_f32 v23, v23\n"
  "v_mfma_f32_16x16x32_bf16 a[224:227], v[60:63], a[132:135], a[224:227]\n"
  "s_waitcnt lgkmcnt(4)\n"
  "v_cvt_pk_bf16_f32 v56, v48, v49\n"
  "v_cvt_pk_bf16_f32 v57, v50, v51\n"
  "v_mfma_f32_16x16x32_bf16 a[244:247], a[160:163], a[124:127], a[244:247]\n"
  "v_cvt_pk_bf16_f32 v10, v4, v5\n"
  "v_cvt_pk_bf16_f32 v11, v6, v7\n"
  "v_mfma_f32_16x16x32_bf16 a[248:251], a[160:163], a[120:123], a[248:251]\n"
  "v_add_f32 v253, v253, v20\n"
  "v_add_f32 v253, v253, v21\n"
  "v_mfma_f32_16x16x32_bf16 v[200:203], v[60:63], a[124:127], v[200:203]\n"
  "v_add_f32 v253, v253, v22\n"
  "v_add_f32 v253, v253, v23\n"
  "v_mfma_f32_16x16x32_bf16 v[204:207], v[60:63], a[120:123], v[204:207]\n"
  "v_cvt_pk_bf16_f32 v26, v20, v21\n"
  "v_cvt_pk_bf16_f32 v27, v22, v23\n"
  "v_mfma_f32_16x16x32_bf16 v[168:171], v[44:47], a[124:127], v[168:171]\n"
  "v_fma_f32 v36, v36, s73, v225\n"
  "v_fma_f32 v37, v37, s73, v225\n"
  "v_mfma_f32_16x16x32_bf16 v[172:175], v[44:47], a[120:123], v[172:175]\n"
  "s_waitcnt vmcnt(6) lgkmcnt(0)\n"
  "s_barrier\n"
  "ds_read_b128 a[128:131], v216 offset:12352\n"
  "v_mfma_f32_16x16x32_bf16 v[136:139], v[28:31], a[124:127], v[136:139]\n"
  "ds_read_b64 a[156:157], v231 offset:0\n"
  "v_fma_f32 v38, v38, s73, v225\n"
  "v_mfma_f32_16x16x32_bf16 v[140:143], v[28:31], a[120:123], v[140:143]\n"
  "ds_read_b64 a[158:159], v232 offset:0\n"
  "v_fma_f32 v39, v39, s73, v225\n"
  "v_mfma_f32_16x16x32_bf16 v[104:107], v[12:15], a[124:127], v[104:107]\n"
  "ds_read_b128 a[132:135], v216 offset:12416\n"
  "v_fma_f32 v52, v52, s73, v226\n"
  "v_mfma_f32_16x16x32_bf16 v[108:111], v[12:15], a[120:123], v[108:111]\n"
  "ds_read_b128 a[124:127], v216 offset:13376\n"
  "v_fma_f32 v53, v53, s73, v226\n"
  "v_mfma_f32_16x16x32_bf16 v[112:115], v[12:15], a[140:143], v[112:115]\n"
  "ds_read_b128 a[120:123], v216 offset:13440\n"
  "v_fma_f32 v54, v54, s73, v226\n"
  "v_mfma_f32_16x16x32_bf16 v[116:119], v[12:15], a[136:139], v[116:119]\n"
  "s_mov_b32 m0, s66\n"
  "ds_read_b128 a[148:151], v216 offset:14400\n"
  "v_fma_f32 v55, v55, s73, v226\n"
  "v_mfma_f32_16x16x32_bf16 v[144:147], v[28:31], a[140:143], v[144:147]\n"
  "ds_read_b128 a[144:147], v216 offset:14464\n"
  "v_and_b32 v236, 0xffff0000, v252\n"
  "v_mfma_f32_16x16x32_bf16 v[148:151], v[28:31], a[136:139], v[148:151]\n"
  "v_exp_f32 v36, v36\n"
  "v_mfma_f32_16x16x32_bf16 v[176:179], v[44:47], a[140:143], v[176:179]\n"
  "v_exp_f32 v37, v37\n"
  "v_mfma_f32_16x16x32_bf16 v[180:183], v[44:47], a[136:139], v[180:183]\n"
  "buffer_load_dwordx4 v221, s[40:43], 0 offen offset:0 lds\n"
  "v_mfma_f32_16x16x32_bf16 v[208:211], v[60:63], a[140:143], v[208:211]\n"
  "s_addk_i32 m0, 896\n"
  "v_exp_f32 v38, v38\n"
  "v_mfma_f32_16x16x32_bf16 v[212:215], v[60:63], a[136:139], v[212:215]\n"
  "s_waitcnt lgkmcnt(4)\n"
  "v_exp_f32 v39, v39\n"
  "v_mfma_f32_16x16x32_bf16 v[64:67], a[128:131], a[96:99], v[248:251]\n"
  "v_exp_f32 v52, v52\n"
  "v_mfma_f32_16x16x32_bf16 v[64:67], a[132:135], a[100:103], v[64:67]\n"
  "s_waitcnt lgkmcnt(2)\n"
  "buffer_load_dwordx4 v221, s[40:43], 0 offen offset:128 lds\n"
  "v_add_f32 v254, v254, v36\n"
  "v_mfma_f32_16x16x32_bf16 v[64:67], a[124:127], a[104:107], v[64:67]\n"
  "s_addk_i32 m0, 896\n"
  "v_exp_f32 v53, v53\n"
  "v_mfma_f32_16x16x32_bf16 v[64:67], a[120:123], a[108:111], v[64:67]\n"
  "s_waitcnt lgkmcnt(0)\n"
  "v_exp_f32 v54, v54\n"
  "v_mfma_f32_16x16x32_bf16 v[64:67], a[148:151], a[112:115], v[64:67]\n"
  "v_exp_f32 v55, v55\n"
  "v_mfma_f32_16x16x32_bf16 v[64:67], a[144:147], a[116:119], v[64:67]\n"
  "buffer_load_dwordx4 v221, s[40:43], 0 offen offset:256 lds\n"
  "v_add_f32 v254, v254, v37\n"
  "v_mfma_f32_16x16x32_bf16 v[0:3], a[144:147], a[20:23], v[248:251]\n"
  "s_add_u32 s40, s40, -12288\n"
  "v_add_f32 v254, v254, v38\n"
  "v_add_f32 v254, v254, v39\n"
  "v_mfma_f32_16x16x32_bf16 v[0:3], a[148:151], a[16:19], v[0:3]\n"
  "s_addc_u32 s41, s41, -1\n"
  "v_cvt_pk_bf16_f32 v42, v36, v37\n"
  "v_cvt_pk_bf16_f32 v43, v38, v39\n"
  "v_mfma_f32_16x16x32_bf16 v[0:3], a[120:123], a[12:15], v[0:3]\n"
  "v_add_f32 v255, v255, v52\n"
  "v_add_f32 v255, v255, v53\n"
  "v_mfma_f32_16x16x32_bf16 v[0:3], a[124:127], a[8:11], v[0:3]\n"
  "s_mov_b32 m0, s58\n"
  "v_add_f32 v255, v255, v54\n"
  "v_add_f32 v255, v255, v55\n"
  "v_mfma_f32_16x16x32_bf16 v[0:3], a[132:135], a[4:7], v[0:3]\n"
  "v_cvt_pk_bf16_f32 v58, v52, v53\n"
  "v_cvt_pk_bf16_f32 v59, v54, v55\n"
  "v_mfma_f32_16x16x32_bf16 v[0:3], a[128:131], a[0:3], v[0:3]\n"
  "v_sub_f32 v252, v252, v236\n"
  "v_or_b32_sdwa v236, v236, v252 dst_sel:DWORD dst_unused:UNUSED_PAD src0_sel:DWORD src1_sel:WORD_1\n"
  "v_mfma_f32_16x16x32_bf16 v[16:19], a[128:131], a[24:27], v[248:251]\n"
  "s_add_u32 s71, s92, s83\n"
  "v_and_b32 v240, 0xffff0000, v253\n"
  "v_sub_f32 v253, v253, v240\n"
  "v_mfma_f32_16x16x32_bf16 v[16:19], a[132:135], a[28:31], v[16:19]\n"
  "buffer_load_dwordx4 v221, s[32:35], 0 offen offset:0 lds\n"
  "v_or_b32_sdwa v240, v240, v253 dst_sel:DWORD dst_unused:UNUSED_PAD src0_sel:DWORD src1_sel:WORD_1\n"
  "v_mfma_f32_16x16x32_bf16 v[16:19], a[124:127], a[32:35], v[16:19]\n"
  "s_addk_i32 m0, 1024\n"
  "ds_read_b128 a[140:143], v235 offset:12352\n"
  "v_fma_f32 v64, v64, s73, v227\n"
  "v_mfma_f32_16x16x32_bf16 v[16:19], a[120:123], a[36:39], v[16:19]\n"
  "s_mov_b64 s[32:33], s[36:37]\n"
  "ds_read_b128 a[136:139], v235 offset:12416\n"
  "v_fma_f32 v65, v65, s73, v227\n"
  "v_mfma_f32_16x16x32_bf16 v[16:19], a[148:151], a[40:43], v[16:19]\n"
  "v_exp_f32 v64, v64\n"
  "v_mfma_f32_16x16x32_bf16 v[16:19], a[144:147], a[44:47], v[16:19]\n"
  "buffer_load_dwordx4 v221, s[44:47], 0 offen offset:0 lds\n"
  "v_fma_f32 v66, v66, s73, v227\n"
  "v_mfma_f32_16x16x32_bf16 v[32:35], a[144:147], a[68:71], v[248:251]\n"
  "s_addk_i32 m0, 768\n"
  "v_exp_f32 v65, v65\n"
  "v_mfma_f32_16x16x32_bf16 v[32:35], a[148:151], a[64:67], v[32:35]\n"
  "v_exp_f32 v66, v66\n"
  "v_mfma_f32_16x16x32_bf16 v[32:35], a[120:123], a[60:63], v[32:35]\n"
  "v_fma_f32 v67, v67, s73, v227\n"
  "v_cvt_pk_bf16_f32 v64, v64, v65\n"
  "v_mfma_f32_16x16x32_bf16 v[32:35], a[124:127], a[56:59], v[32:35]\n"
  "buffer_load_dwordx4 v221, s[44:47], 0 offen offset:256 lds\n"
  "v_fma_f32 v0, v0, s73, v223\n"
  "v_mfma_f32_16x16x32_bf16 v[32:35], a[132:135], a[52:55], v[32:35]\n"
  "s_add_u32 s44, s44, -12288\n"
  "v_exp_f32 v67, v67\n"
  "v_mfma_f32_16x16x32_bf16 v[32:35], a[128:131], a[48:51], v[32:35]\n"
  "s_addc_u32 s45, s45, -1\n"
  "v_exp_f32 v0, v0\n"
  "v_mfma_f32_16x16x32_bf16 v[48:51], a[128:131], a[72:75], v[248:251]\n"
  "ds_read_b128 a[128:131], v235 offset:13376\n"
  "v_cvt_pk_bf16_f32 v65, v66, v67\n"
  "v_mfma_f32_16x16x32_bf16 v[48:51], a[132:135], a[76:79], v[48:51]\n"
  "ds_read_b128 a[132:135], v235 offset:13440\n"
  "v_fma_f32 v1, v1, s73, v223\n"
  "v_mfma_f32_16x16x32_bf16 v[48:51], a[124:127], a[80:83], v[48:51]\n"
  "ds_read_b128 a[124:127], v235 offset:14400\n"
  "v_fma_f32 v2, v2, s73, v223\n"
  "v_mfma_f32_16x16x32_bf16 v[48:51], a[120:123], a[84:87], v[48:51]\n"
  "ds_read_b128 a[120:123], v235 offset:14464\n"
  "v_fma_f32 v3, v3, s73, v223\n"
  "v_mfma_f32_16x16x32_bf16 v[48:51], a[148:151], a[88:91], v[48:51]\n"
  "v_exp_f32 v1, v1\n"
  "v_mfma_f32_16x16x32_bf16 v[48:51], a[144:147], a[92:95], v[48:51]\n"
  "s_waitcnt lgkmcnt(4)\n"
  "v_exp_f32 v2, v2\n"
  "v_mfma_f32_16x16x32_bf16 v[4:7], a[140:143], a[0:3], v[248:251]\n"
  "ds_read_b128 a[0:3], v222 offset:0\n"
  "v_fma_f32 v16, v16, s73, v224\n"
  "v_mfma_f32_16x16x32_bf16 v[4:7], a[136:139], a[4:7], v[4:7]\n"
  "s_waitcnt lgkmcnt(3)\n"
  "ds_read_b128 a[4:7], v222 offset:64\n"
  "v_fma_f32 v17, v17, s73, v224\n"
  "v_mfma_f32_16x16x32_bf16 v[4:7], a[128:131], a[8:11], v[4:7]\n"
  "ds_read_b128 a[8:11], v222 offset:2240\n"
  "v_fma_f32 v18, v18, s73, v224\n"
  "v_mfma_f32_16x16x32_bf16 v[4:7], a[132:135], a[12:15], v[4:7]\n"
  "s_waitcnt lgkmcnt(3)\n"
  "ds_read_b128 a[12:15], v222 offset:2304\n"
  "v_fma_f32 v19, v19, s73, v224\n"
  "v_mfma_f32_16x16x32_bf16 v[4:7], a[124:127], a[16:19], v[4:7]\n"
  "ds_read_b128 a[16:19], v222 offset:4480\n"
  "v_fma_f32 v32, v32, s73, v225\n"
  "v_mfma_f32_16x16x32_bf16 v[4:7], a[120:123], a[20:23], v[4:7]\n"
  "ds_read_b128 a[20:23], v222 offset:4544\n"
  "v_fma_f32 v33, v33, s73, v225\n"
  "v_mfma_f32_16x16x32_bf16 v[20:23], a[120:123], a[44:47], v[248:251]\n"
  "ds_read_b128 a[44:47], v222 offset:6784\n"
  "v_fma_f32 v34, v34, s73, v225\n"
  "v_mfma_f32_16x16x32_bf16 v[20:23], a[124:127], a[40:43], v[20:23]\n"
  "ds_read_b128 a[40:43], v222 offset:6720\n"
  "v_fma_f32 v35, v35, s73, v225\n"
  "v_mfma_f32_16x16x32_bf16 v[20:23], a[132:135], a[36:39], v[20:23]\n"
  "ds_read_b64_tr_b16 a[148:149], v217 offset:12352\n"
  "v_fma_f32 v4, v4, s73, v223\n"
  "v_mfma_f32_16x16x32_bf16 v[20:23], a[128:131], a[32:35], v[20:23]\n"
  "ds_read_b64_tr_b16 a[150:151], v218 offset:12352\n"
  "v_fma_f32 v5, v5, s73, v223\n"
  "v_mfma_f32_16x16x32_bf16 v[20:23], a[136:139], a[28:31], v[20:23]\n"
  "s_add_u32 m0, s72, -256\n"
  "ds_read_b64_tr_b16 a[144:145], v217 offset:13376\n"
  "v_mfma_f32_16x16x32_bf16 v[20:23], a[140:143], a[24:27], v[20:23]\n"
  "s_add_u32 s52, s62, s57\n"
  "ds_read_b64_tr_b16 a[146:147], v218 offset:13376\n"
  "v_fma_f32 v6, v6, s73, v223\n"
  "v_mfma_f32_16x16x32_bf16 v[36:39], a[140:143], a[48:51], v[248:251]\n"
  "s_addc_u32 s53, s63, 0\n"
  "v_exp_f32 v3, v3\n"
  "v_mfma_f32_16x16x32_bf16 v[36:39], a[136:139], a[52:55], v[36:39]\n"
  "s_mov_b32 s54, s93\n"
  "v_exp_f32 v4, v4\n"
  "v_mfma_f32_16x16x32_bf16 v[36:39], a[128:131], a[56:59], v[36:39]\n"
  "s_waitcnt lgkmcnt(10)\n"
  "buffer_load_dwordx4 v221, s[48:51], 0 offen offset:256  lds\n"
  "v_mfma_f32_16x16x32_bf16 v[36:39], a[132:135], a[60:63], v[36:39]\n"
  "s_add_u32 m0, s72, -2240\n"
  "v_exp_f32 v5, v5\n"
  "v_mfma_f32_16x16x32_bf16 v[36:39], a[124:127], a[64:67], v[36:39]\n"
  "s_mov_b32 s57, s92\n"
  "v_exp_f32 v6, v6\n"
  "v_mfma_f32_16x16x32_bf16 v[36:39], a[120:123], a[68:71], v[36:39]\n"
  "s_mov_b32 s93, s94\n"
  "v_exp_f32 v16, v16\n"
  "v_mfma_f32_16x16x32_bf16 v[52:55], a[120:123], a[92:95], v[248:251]\n"
  "buffer_load_dwordx4 v221, s[48:51], 0 offen offset:3328  lds\n"
  "v_fma_f32 v7, v7, s73, v223\n"
  "v_mfma_f32_16x16x32_bf16 v[52:55], a[124:127], a[88:91], v[52:55]\n"
  "s_add_u32 s48, s84, s71\n"
  "ds_read_b64_tr_b16 a[120:121], v217 offset:12368\n"
  "v_fma_f32 v20, v20, s73, v224\n"
  "v_mfma_f32_16x16x32_bf16 v[52:55], a[132:135], a[84:87], v[52:55]\n"
  "s_addc_u32 s49, s85, 0\n"
  "ds_read_b64_tr_b16 a[122:123], v218 offset:12368\n"
  "v_fma_f32 v21, v21, s73, v224\n"
  "v_mfma_f32_16x16x32_bf16 v[52:55], a[128:131], a[80:83], v[52:55]\n"
  "s_add_u32 m0, s72, 2240\n"
  "ds_read_b64_tr_b16 a[124:125], v217 offset:13392\n"
  "v_mfma_f32_16x16x32_bf16 v[52:55], a[136:139], a[76:79], v[52:55]\n"
  "s_sub_i32 s71, s38, s83\n"
  "s_max_i32 s50, s71, 0\n"
  "s_waitcnt lgkmcnt(11)\n"
  "buffer_load_dwordx4 v221, s[48:51], 0 offen offset:0 nt lds\n"
  "v_fma_f32 v22, v22, s73, v224\n"
  "v_mfma_f32_16x16x32_bf16 v[52:55], a[140:143], a[72:75], v[52:55]\n"
  "s_waitcnt lgkmcnt(3)\n"
  "ds_read_b64_tr_b16 a[126:127], v218 offset:13392\n"
  "v_fma_f32 v23, v23, s73, v224\n"
  "v_mfma_f32_16x16x32_bf16 a[236:239], a[160:163], a[148:151], a[236:239]\n"
  "v_exp_f32 v7, v7\n"
  "v_mfma_f32_16x16x32_bf16 a[236:239], a[156:159], a[144:147], a[236:239]\n"
  "s_add_u32 m0, s72, 256\n"
  "ds_write_b64 v231, v[64:65] offset:2048\n"
  "v_mfma_f32_16x16x32_bf16 v[184:187], v[60:63], a[148:151], v[184:187]\n"
  "buffer_load_dwordx4 v221, s[48:51], 0 offen offset:3072 nt lds\n"
  "v_fma_f32 v36, v36, s73, v225\n"
  "v_mfma_f32_16x16x32_bf16 v[184:187], v[56:59], a[144:147], v[184:187]\n"
  "s_add_u32 m0, s72, 4352\n"
  "v_exp_f32 v17, v17\n"
  "v_mfma_f32_16x16x32_bf16 v[152:155], v[44:47], a[148:151], v[152:155]\n"
  "v_exp_f32 v18, v18\n"
  "v_mfma_f32_16x16x32_bf16 v[152:155], v[40:43], a[144:147], v[152:155]\n"
  "ds_read_b64_tr_b16 a[132:133], v219 offset:12352\n"
  "v_fma_f32 v37, v37, s73, v225\n"
  "v_mfma_f32_16x16x32_bf16 v[120:123], v[28:31], a[148:151], v[120:123]\n"
  "buffer_load_dwordx4 v221, s[48:51], 0 offen offset:128 nt lds\n"
  "v_fma_f32 v38, v38, s73, v225\n"
  "v_mfma_f32_16x16x32_bf16 v[120:123], v[24:27], a[144:147], v[120:123]\n"
  "s_add_u32 m0, s72, 2368\n"
  "ds_read_b64_tr_b16 a[134:135], v220 offset:12352\n"
  "v_mfma_f32_16x16x32_bf16 v[88:91], v[12:15], a[148:151], v[88:91]\n"
  "ds_read_b64_tr_b16 a[128:129], v219 offset:13376\n"
  "v_fma_f32 v39, v39, s73, v225\n"
  "v_mfma_f32_16x16x32_bf16 v[88:91], v[8:11], a[144:147], v[88:91]\n"
  "s_waitcnt lgkmcnt(4)\n"
  "ds_read_b64_tr_b16 a[130:131], v220 offset:13376\n"
  "v_fma_f32 v48, v48, s73, v226\n"
  "v_mfma_f32_16x16x32_bf16 v[92:95], v[12:15], a[120:123], v[92:95]\n"
  "buffer_load_dwordx4 v221, s[48:51], 0 offen offset:3200 nt lds\n"
  "v_fma_f32 v49, v49, s73, v226\n"
  "v_mfma_f32_16x16x32_bf16 v[92:95], v[8:11], a[124:127], v[92:95]\n"
  "s_add_u32 m0, s72, 6464\n"
  "v_exp_f32 v19, v19\n"
  "v_mfma_f32_16x16x32_bf16 v[124:127], v[28:31], a[120:123], v[124:127]\n"
  "v_exp_f32 v20, v20\n"
  "v_mfma_f32_16x16x32_bf16 v[124:127], v[24:27], a[124:127], v[124:127]\n"
  "v_exp_f32 v21, v21\n"
  "v_mfma_f32_16x16x32_bf16 v[156:159], v[44:47], a[120:123], v[156:159]\n"
  "buffer_load_dwordx4 v221, s[48:51], 0 offen offset:256 nt lds\n"
  "v_fma_f32 v50, v50, s73, v226\n"
  "v_mfma_f32_16x16x32_bf16 v[156:159], v[40:43], a[124:127], v[156:159]\n"
  "s_add_u32 m0, s72, 4480\n"
  "ds_read_b64_tr_b16 a[136:137], v219 offset:12368\n"
  "v_mfma_f32_16x16x32_bf16 v[188:191], v[60:63], a[120:123], v[188:191]\n"
  "ds_read_b64_tr_b16 a[138:139], v220 offset:12368\n"
  "v_fma_f32 v51, v51, s73, v226\n"
  "v_mfma_f32_16x16x32_bf16 v[188:191], v[56:59], a[124:127], v[188:191]\n"
  "ds_read_b64_tr_b16 a[140:141], v219 offset:13392\n"
  "v_fma_f32 v52, v52, s73, v226\n"
  "v_mfma_f32_16x16x32_bf16 a[240:243], a[160:163], a[120:123], a[240:243]\n"
  "buffer_load_dwordx4 v221, s[48:51], 0 offen offset:3328 nt lds\n"
  "v_fma_f32 v53, v53, s73, v226\n"
  "v_mfma_f32_16x16x32_bf16 a[240:243], a[156:159], a[124:127], a[240:243]\n"
  "ds_read_b64_tr_b16 a[142:143], v220 offset:13392\n"
  "v_fma_f32 v54, v54, s73, v226\n"
  "v_mfma_f32_16x16x32_bf16 v[68:71], v[236:239], a[152:155], v[68:71]\n"
  "s_waitcnt lgkmcnt(4)\n"
  "v_exp_f32 v22, v22\n"
  "v_mfma_f32_16x16x32_bf16 v[192:195], v[60:63], a[132:135], v[192:195]\n"
  "v_exp_f32 v23, v23\n"
  "v_mfma_f32_16x16x32_bf16 v[192:195], v[56:59], a[128:131], v[192:195]\n"
  "v_exp_f32 v32, v32\n"
  "v_mfma_f32_16x16x32_bf16 v[160:163], v[44:47], a[132:135], v[160:163]\n"
  "ds_read_b64_tr_b16 a[120:121], v217 offset:14400\n"
  "v_and_b32 v236, 0xffff0000, v254\n"
  "v_mfma_f32_16x16x32_bf16 v[160:163], v[40:43], a[128:131], v[160:163]\n"
  "ds_read_b64_tr_b16 a[122:123], v218 offset:14400\n"
  "v_sub_f32 v254, v254, v236\n"
  "v_mfma_f32_16x16x32_bf16 v[128:131], v[28:31], a[132:135], v[128:131]\n"
  "ds_read_b64_tr_b16 a[124:125], v217 offset:14416\n"
  "v_or_b32_sdwa v236, v236, v254 dst_sel:DWORD dst_unused:UNUSED_PAD src0_sel:DWORD src1_sel:WORD_1\n"
  "v_mfma_f32_16x16x32_bf16 v[128:131], v[24:27], a[128:131], v[128:131]\n"
  "ds_read_b64_tr_b16 a[126:127], v218 offset:14416\n"
  "v_fma_f32 v55, v55, s73, v226\n"
  "v_mfma_f32_16x16x32_bf16 v[96:99], v[12:15], a[132:135], v[96:99]\n"
  "v_exp_f32 v33, v33\n"
  "v_mfma_f32_16x16x32_bf16 v[96:99], v[8:11], a[128:131], v[96:99]\n"
  "v_exp_f32 v34, v34\n"
  "v_mfma_f32_16x16x32_bf16 v[72:75], v[240:243], a[152:155], v[72:75]\n"
  "s_waitcnt lgkmcnt(4)\n"
  "v_exp_f32 v35, v35\n"
  "v_mfma_f32_16x16x32_bf16 v[100:103], v[12:15], a[136:139], v[100:103]\n"
  "v_exp_f32 v36, v36\n"
  "v_mfma_f32_16x16x32_bf16 v[100:103], v[8:11], a[140:143], v[100:103]\n"
  "v_exp_f32 v37, v37\n"
  "v_mfma_f32_16x16x32_bf16 v[132:135], v[28:31], a[136:139], v[132:135]\n"
  "v_exp_f32 v38, v38\n"
  "v_mfma_f32_16x16x32_bf16 v[132:135], v[24:27], a[140:143], v[132:135]\n"
  "v_exp_f32 v39, v39\n"
  "v_mfma_f32_16x16x32_bf16 v[164:167], v[44:47], a[136:139], v[164:167]\n"
  "ds_read_b64_tr_b16 a[132:133], v219 offset:14400\n"
  "v_and_b32 v240, 0xffff0000, v255\n"
  "v_mfma_f32_16x16x32_bf16 v[164:167], v[40:43], a[140:143], v[164:167]\n"
  "ds_read_b64_tr_b16 a[134:135], v220 offset:14400\n"
  "v_sub_f32 v255, v255, v240\n"
  "v_mfma_f32_16x16x32_bf16 v[196:199], v[60:63], a[136:139], v[196:199]\n"
  "ds_read_b64_tr_b16 a[128:129], v219 offset:14416\n"
  "v_or_b32_sdwa v240, v240, v255 dst_sel:DWORD dst_unused:UNUSED_PAD src0_sel:DWORD src1_sel:WORD_1\n"
  "v_mfma_f32_16x16x32_bf16 v[196:199], v[56:59], a[140:143], v[196:199]\n"
  "ds_read_b64_tr_b16 a[130:131], v220 offset:14416\n"
  "v_cvt_pk_bf16_f32 v12, v0, v1\n"
  "v_mfma_f32_16x16x32_bf16 v[76:79], v[236:239], a[152:155], v[76:79]\n"
  "s_waitcnt lgkmcnt(4)\n"
  "v_exp_f32 v48, v48\n"
  "v_mfma_f32_16x16x32_bf16 a[244:247], a[156:159], a[120:123], a[244:247]\n"
  "v_exp_f32 v49, v49\n"
  "v_mfma_f32_16x16x32_bf16 a[248:251], a[156:159], a[124:127], a[248:251]\n"
  "v_exp_f32_e64 v50, v50\n"
  "v_mfma_f32_16x16x32_bf16 v[200:203], v[56:59], a[120:123], v[200:203]\n"
  "v_add_u32 v233, s97, v222\n"
  "s_mov_b64 exec, s[98:99]\n"
  "v_add_u32 v233, 0x4f00, v233\n"
  "s_mov_b64 exec, -1\n"
  "v_mfma_f32_16x16x32_bf16 v[204:207], v[56:59], a[124:127], v[204:207]\n"
  "ds_read_b128 a[96:99], v233 offset:17920\n"
  "ds_read_b128 a[100:103], v233 offset:17984\n"
  "ds_read_b128 a[104:107], v233 offset:19040\n"
  "ds_read_b128 a[108:111], v233 offset:19104\n"
  "ds_read_b128 a[112:115], v233 offset:20160\n"
  "ds_read_b128 a[116:119], v233 offset:20224\n"
  "v_mfma_f32_16x16x32_bf16 v[168:171], v[40:43], a[120:123], v[168:171]\n"
  "v_exp_f32 v51, v51\n"
  "v_mfma_f32_16x16x32_bf16 v[172:175], v[40:43], a[124:127], v[172:175]\n"
  "s_mov_b32 m0, s67\n"
  "v_exp_f32 v52, v52\n"
  "v_mfma_f32_16x16x32_bf16 v[136:139], v[24:27], a[120:123], v[136:139]\n"
  "s_waitcnt vmcnt(14) lgkmcnt(0)\n"
  "s_barrier\n"
  "v_mfma_f32_16x16x32_bf16 v[140:143], v[24:27], a[124:127], v[140:143]\n"
  "ds_read_b64 a[160:161], v231 offset:2048\n"
  "v_cvt_pk_bf16_f32 v13, v2, v3\n"
  "v_mfma_f32_16x16x32_bf16 v[104:107], v[8:11], a[120:123], v[104:107]\n"
  "ds_read_b64 a[162:163], v232 offset:2048\n"
  "v_cvt_pk_bf16_f32 v14, v4, v5\n"
  "v_mfma_f32_16x16x32_bf16 v[108:111], v[8:11], a[124:127], v[108:111]\n"
  "buffer_load_dwordx4 v221, s[40:43], 0 offen offset:0 lds\n"
  "v_mfma_f32_16x16x32_bf16 v[80:83], v[240:243], a[152:155], v[80:83]\n"
  "ds_read_b64_tr_b16 a[120:121], v217 offset:26752\n"
  "v_cvt_pk_bf16_f32 v15, v6, v7\n"
  "v_mfma_f32_16x16x32_bf16 v[112:115], v[8:11], a[132:135], v[112:115]\n"
  "s_addk_i32 m0, 896\n"
  "ds_read_b64_tr_b16 a[122:123], v218 offset:26752\n"
  "v_cvt_pk_bf16_f32 v28, v16, v17\n"
  "v_mfma_f32_16x16x32_bf16 v[116:119], v[8:11], a[128:131], v[116:119]\n"
  "ds_read_b64_tr_b16 a[124:125], v217 offset:26768\n"
  "v_cvt_pk_bf16_f32 v29, v18, v19\n"
  "v_mfma_f32_16x16x32_bf16 v[144:147], v[24:27], a[132:135], v[144:147]\n"
  "buffer_load_dwordx4 v221, s[40:43], 0 offen offset:128 lds\n"
  "v_cvt_pk_bf16_f32 v30, v20, v21\n"
  "v_mfma_f32_16x16x32_bf16 v[148:151], v[24:27], a[128:131], v[148:151]\n"
  "s_addk_i32 m0, 896\n"
  "ds_read_b64_tr_b16 a[126:127], v218 offset:26768\n"
  "v_cvt_pk_bf16_f32 v31, v22, v23\n"
  "v_mfma_f32_16x16x32_bf16 v[176:179], v[40:43], a[132:135], v[176:179]\n"
  "v_exp_f32 v53, v53\n"
  "v_mfma_f32_16x16x32_bf16 v[180:183], v[40:43], a[128:131], v[180:183]\n"
  "v_exp_f32 v54, v54\n"
  "v_mfma_f32_16x16x32_bf16 v[208:211], v[56:59], a[132:135], v[208:211]\n"
  "buffer_load_dwordx4 v221, s[40:43], 0 offen offset:256 lds\n"
  "v_cvt_pk_bf16_f32 v44, v32, v33\n"
  "v_mfma_f32_16x16x32_bf16 v[212:215], v[56:59], a[128:131], v[212:215]\n"
  "s_cmp_eq_i32 s76, 5\n"
  "s_cbranch_scc1 L_TAIL_%=\n"
  "v_exp_f32 v55, v55\n"
  "v_mfma_f32_16x16x32_bf16 v[68:71], v[12:15], a[152:155], v[68:71]\n"
  "ds_read_b64_tr_b16 a[132:133], v219 offset:26752\n"
  "v_cvt_pk_bf16_f32 v45, v34, v35\n"
  "v_cvt_pk_bf16_f32 v46, v36, v37\n"
  "v_cvt_pk_bf16_f32 v47, v38, v39\n"
  "v_mfma_f32_16x16x32_bf16 v[72:75], v[28:31], a[152:155], v[72:75]\n"
  "s_add_u32 m0, s59, -128\n"
  "ds_read_b64_tr_b16 a[134:135], v220 offset:26752\n"
  "v_cvt_pk_bf16_f32 v60, v48, v49\n"
  "v_cvt_pk_bf16_f32 v61, v50, v51\n"
  "v_cvt_pk_bf16_f32 v62, v52, v53\n"
  "v_cvt_pk_bf16_f32 v63, v54, v55\n"
  "v_mfma_f32_16x16x32_bf16 v[76:79], v[44:47], a[152:155], v[76:79]\n"
  "buffer_load_dwordx4 v221, s[32:35], 0 offen offset:128 lds\n"
  "ds_read_b128 a[24:27], v222 offset:11200\n"
  "v_mfma_f32_16x16x32_bf16 v[80:83], v[60:63], a[152:155], v[80:83]\n"
  "s_addk_i32 m0, 1024\n"
  "ds_read_b64_tr_b16 a[128:129], v219 offset:26768\n"
  "ds_read_b128 a[28:31], v222 offset:11264\n"
  "v_mfma_f32_16x16x32_bf16 v[84:87], a[156:159], a[152:155], v[84:87]\n"
  "s_waitcnt lgkmcnt(9)\n"
  "ds_read_b64_tr_b16 a[130:131], v220 offset:26768\n"
  "ds_read_b128 a[32:35], v222 offset:8960\n"
  "v_mfma_f32_16x16x32_bf16 v[84:87], a[160:163], a[152:155], v[84:87]\n"
  "s_waitcnt lgkmcnt(7)\n"
  "ds_read_b128 a[36:39], v222 offset:9024\n"
  "ds_read_b128 a[48:51], v222 offset:13440\n"
  "v_mfma_f32_16x16x32_bf16 a[244:247], a[160:163], a[120:123], a[244:247]\n"
  "buffer_load_dwordx4 v221, s[44:47], 0 offen offset:128 lds\n"
  "ds_read_b128 a[52:55], v222 offset:13504\n"
  "v_mfma_f32_16x16x32_bf16 a[248:251], a[160:163], a[124:127], a[248:251]\n"
  "s_addk_i32 m0, 896\n"
  "ds_read_b128 a[56:59], v222 offset:15680\n"
  "ds_read_b128 a[60:63], v222 offset:15744\n"
  "v_mfma_f32_16x16x32_bf16 v[200:203], v[60:63], a[120:123], v[200:203]\n"
  "ds_read_b64_tr_b16 a[136:137], v217 offset:24704\n"
  "v_maximum3_f32 v236, v68, v69, v70\n"
  "v_mfma_f32_16x16x32_bf16 v[204:207], v[60:63], a[124:127], v[204:207]\n"
  "ds_read_b64_tr_b16 a[138:139], v218 offset:24704\n"
  "v_maximum3_f32 v236, v236, v71, v72\n"
  "v_mfma_f32_16x16x32_bf16 v[168:171], v[44:47], a[120:123], v[168:171]\n"
  "buffer_load_dwordx4 v221, s[44:47], 0 offen offset:256 lds\n"
  "v_maximum3_f32 v236, v236, v73, v74\n"
  "v_mfma_f32_16x16x32_bf16 v[172:175], v[44:47], a[124:127], v[172:175]\n"
  "ds_read_b64_tr_b16 a[140:141], v217 offset:25728\n"
  "v_maximum3_f32 v236, v236, v75, v76\n"
  "v_mfma_f32_16x16x32_bf16 v[136:139], v[28:31], a[120:123], v[136:139]\n"
  "ds_read_b64_tr_b16 a[142:143], v218 offset:25728\n"
  "v_maximum3_f32 v236, v236, v77, v78\n"
  "v_mfma_f32_16x16x32_bf16 v[140:143], v[28:31], a[124:127], v[140:143]\n"
  "v_maximum3_f32 v236, v236, v79, v80\n"
  "v_maximum3_f32 v236, v236, v81, v82\n"
  "v_mfma_f32_16x16x32_bf16 v[104:107], v[12:15], a[120:123], v[104:107]\n"
  "v_maximum3_f32 v236, v236, v83, v84\n"
  "v_maximum3_f32 v236, v236, v85, v86\n"
  "v_mfma_f32_16x16x32_bf16 v[108:111], v[12:15], a[124:127], v[108:111]\n"
  "s_waitcnt lgkmcnt(10)\n"
  "v_rcp_f32 v84, v84\n"
  "v_mfma_f32_16x16x32_bf16 v[112:115], v[12:15], a[132:135], v[112:115]\n"
  "s_add_u32 s40, s40, 24576\n"
  "v_rcp_f32 v85, v85\n"
  "v_mfma_f32_16x16x32_bf16 v[116:119], v[12:15], a[128:131], v[116:119]\n"
  "s_addc_u32 s41, s41, 0\n"
  "v_accvgpr_read_b32 v240, a244\n"
  "v_accvgpr_read_b32 v241, a245\n"
  "v_mfma_f32_16x16x32_bf16 v[144:147], v[28:31], a[132:135], v[144:147]\n"
  "ds_read_b64_tr_b16 a[120:121], v217 offset:24720\n"
  "v_accvgpr_read_b32 v242, a248\n"
  "v_mfma_f32_16x16x32_bf16 v[148:151], v[28:31], a[128:131], v[148:151]\n"
  "ds_read_b64_tr_b16 a[122:123], v218 offset:24720\n"
  "v_accvgpr_read_b32 v243, a249\n"
  "v_mfma_f32_16x16x32_bf16 v[176:179], v[44:47], a[132:135], v[176:179]\n"
  "ds_read_b64_tr_b16 a[124:125], v217 offset:25744\n"
  "v_maximum3_f32 v236, v236, v87, v87\n"
  "v_mfma_f32_16x16x32_bf16 v[180:183], v[44:47], a[128:131], v[180:183]\n"
  "ds_read_b64_tr_b16 a[126:127], v218 offset:25744\n"
  "v_cmp_ngt_f32 vcc, s75, v236\n"
  "v_mfma_f32_16x16x32_bf16 v[208:211], v[60:63], a[132:135], v[208:211]\n"
  "s_mov_b32 s75, s65\n"
  "v_rcp_f32 v68, v68\n"
  "v_mfma_f32_16x16x32_bf16 v[212:215], v[60:63], a[128:131], v[212:215]\n"
  "s_waitcnt lgkmcnt(4)\n"
  "v_rcp_f32 v69, v69\n"
  "v_mfma_f32_16x16x32_bf16 a[228:231], a[156:159], a[136:139], a[228:231]\n"
  "s_add_u32 s32, s44, s31\n"
  "v_rcp_f32 v70, v70\n"
  "v_mfma_f32_16x16x32_bf16 a[228:231], a[160:163], a[140:143], a[228:231]\n"
  "s_addc_u32 s33, s45, 0\n"
  "v_rcp_f32 v71, v71\n"
  "v_mfma_f32_16x16x32_bf16 a[212:215], v[56:59], a[136:139], a[212:215]\n"
  "s_add_u32 s44, s44, 24576\n"
  "v_rcp_f32 v72, v72\n"
  "v_mfma_f32_16x16x32_bf16 a[212:215], v[60:63], a[140:143], a[212:215]\n"
  "s_addc_u32 s45, s45, 0\n"
  "v_rcp_f32 v73, v73\n"
  "v_mfma_f32_16x16x32_bf16 a[196:199], v[40:43], a[136:139], a[196:199]\n"
  "ds_read_b128 a[132:135], v216 offset:24704\n"
  "v_accvgpr_read_b32 v236, a246\n"
  "v_mfma_f32_16x16x32_bf16 a[196:199], v[44:47], a[140:143], a[196:199]\n"
  "ds_read_b128 a[128:131], v216 offset:24768\n"
  "v_accvgpr_read_b32 v237, a247\n"
  "v_mfma_f32_16x16x32_bf16 a[180:183], v[24:27], a[136:139], a[180:183]\n"
  "ds_read_b128 a[148:151], v216 offset:25728\n"
  "v_accvgpr_read_b32 v238, a250\n"
  "v_mfma_f32_16x16x32_bf16 a[180:183], v[28:31], a[140:143], a[180:183]\n"
  "ds_read_b128 a[144:147], v216 offset:25792\n"
  "v_add_u32 v233, s61, v229\n"
  "s_mov_b32 exec_lo, 0\n"
  "v_add_u32 v233, s69, v233\n"
  "s_mov_b32 exec_lo, -1\n"
  "v_rcp_f32_e64 v74, v74\n"
  "v_pk_fma_f32 v[240:241], v[240:241], v[84:85], v[84:85] neg_lo:[0,0,1] neg_hi:[0,0,1]\n"
  "v_pk_fma_f32 v[242:243], v[242:243], v[84:85], v[84:85] neg_lo:[0,0,1] neg_hi:[0,0,1]\n"
  "v_pk_fma_f32 v[104:105], v[104:105], v[68:69], v[68:69] neg_lo:[0,0,1] neg_hi:[0,0,1]\n"
  "v_pk_fma_f32 v[108:109], v[108:109], v[68:69], v[68:69] neg_lo:[0,0,1] neg_hi:[0,0,1]\n"
  "v_mfma_f32_16x16x32_bf16 a[164:167], v[8:11], a[136:139], a[164:167]\n"
  "s_nop 0\n"
  "ds_read_b128 a[136:139], v216 offset:26752\n"
  "v_cvt_pk_bf16_f32 v240, v240, v242\n"
  "v_mfma_f32_16x16x32_bf16 a[164:167], v[12:15], a[140:143], a[164:167]\n"
  "s_waitcnt lgkmcnt(5)\n"
  "ds_read_b128 a[140:143], v216 offset:26816\n"
  "v_cvt_pk_bf16_f32 v241, v241, v243\n"
  "v_cvt_pk_bf16_f32 v104, v104, v108\n"
  "v_rcp_f32_e64 v75, v75\n"
  "v_pk_fma_f32 v[112:113], v[112:113], v[68:69], v[68:69] neg_lo:[0,0,1] neg_hi:[0,0,1]\n"
  "v_pk_fma_f32 v[116:117], v[116:117], v[68:69], v[68:69] neg_lo:[0,0,1] neg_hi:[0,0,1]\n"
  "v_pk_fma_f32 v[106:107], v[106:107], v[70:71], v[70:71] neg_lo:[0,0,1] neg_hi:[0,0,1]\n"
  "v_pk_fma_f32 v[110:111], v[110:111], v[70:71], v[70:71] neg_lo:[0,0,1] neg_hi:[0,0,1]\n"
  "v_mfma_f32_16x16x32_bf16 a[168:171], v[8:11], a[120:123], a[168:171]\n"
  "s_cmp_lg_u64 vcc, 0\n"
  "s_cselect_b32 s79, s77, 0\n"
  "s_or_b32 s78, s78, s79\n"
  "s_lshl_b32 s77, s77, 1\n"
  "v_rcp_f32 v76, v76\n"
  "v_mfma_f32_16x16x32_bf16 a[168:171], v[12:15], a[124:127], a[168:171]\n"
  "v_cvt_pk_bf16_f32 v108, v105, v109\n"
  "v_rcp_f32 v77, v77\n"
  "v_rcp_f32_e64 v78, v78\n"
  "v_pk_fma_f32 v[114:115], v[114:115], v[70:71], v[70:71] neg_lo:[0,0,1] neg_hi:[0,0,1]\n"
  "v_pk_fma_f32 v[118:119], v[118:119], v[70:71], v[70:71] neg_lo:[0,0,1] neg_hi:[0,0,1]\n"
  "v_pk_fma_f32 v[136:137], v[136:137], v[72:73], v[72:73] neg_lo:[0,0,1] neg_hi:[0,0,1]\n"
  "v_pk_fma_f32 v[140:141], v[140:141], v[72:73], v[72:73] neg_lo:[0,0,1] neg_hi:[0,0,1]\n"
  "v_mfma_f32_16x16x32_bf16 a[184:187], v[24:27], a[120:123], a[184:187]\n"
  "v_rcp_f32_e64 v79, v79\n"
  "v_mfma_f32_16x16x32_bf16 a[184:187], v[28:31], a[124:127], a[184:187]\n"
  "v_cvt_pk_bf16_f32 v105, v112, v116\n"
  "v_rcp_f32_e64 v80, v80\n"
  "v_rcp_f32_e64 v81, v81\n"
  "v_pk_fma_f32 v[144:145], v[144:145], v[72:73], v[72:73] neg_lo:[0,0,1] neg_hi:[0,0,1]\n"
  "v_pk_fma_f32 v[148:149], v[148:149], v[72:73], v[72:73] neg_lo:[0,0,1] neg_hi:[0,0,1]\n"
  "v_pk_fma_f32 v[138:139], v[138:139], v[74:75], v[74:75] neg_lo:[0,0,1] neg_hi:[0,0,1]\n"
  "v_pk_fma_f32 v[142:143], v[142:143], v[74:75], v[74:75] neg_lo:[0,0,1] neg_hi:[0,0,1]\n"
  "v_mfma_f32_16x16x32_bf16 a[200:203], v[40:43], a[120:123], a[200:203]\n"
  "v_rcp_f32_e64 v82, v82\n"
  "v_mfma_f32_16x16x32_bf16 a[200:203], v[44:47], a[124:127], a[200:203]\n"
  "ds_write_b32 v233, v240 offset:20160\n"
  "v_cvt_pk_bf16_f32 v109, v113, v117\n"
  "v_rcp_f32_e64 v83, v83\n"
  "v_pk_fma_f32 v[146:147], v[146:147], v[74:75], v[74:75] neg_lo:[0,0,1] neg_hi:[0,0,1]\n"
  "v_pk_fma_f32 v[150:151], v[150:151], v[74:75], v[74:75] neg_lo:[0,0,1] neg_hi:[0,0,1]\n"
  "v_pk_fma_f32 v[168:169], v[168:169], v[76:77], v[76:77] neg_lo:[0,0,1] neg_hi:[0,0,1]\n"
  "v_pk_fma_f32 v[172:173], v[172:173], v[76:77], v[76:77] neg_lo:[0,0,1] neg_hi:[0,0,1]\n"
  "v_mfma_f32_16x16x32_bf16 a[216:219], v[56:59], a[120:123], a[216:219]\n"
  "ds_write_b32 v233, v241 offset:20448\n"
  "v_mfma_f32_16x16x32_bf16 a[216:219], v[60:63], a[124:127], a[216:219]\n"
  "ds_write_b64 v229, v[104:105] offset:8960\n"
  "v_cvt_pk_bf16_f32 v106, v106, v110\n"
  "v_cvt_pk_bf16_f32 v110, v107, v111\n"
  "v_pk_fma_f32 v[176:177], v[176:177], v[76:77], v[76:77] neg_lo:[0,0,1] neg_hi:[0,0,1]\n"
  "v_pk_fma_f32 v[180:181], v[180:181], v[76:77], v[76:77] neg_lo:[0,0,1] neg_hi:[0,0,1]\n"
  "v_pk_fma_f32 v[170:171], v[170:171], v[78:79], v[78:79] neg_lo:[0,0,1] neg_hi:[0,0,1]\n"
  "v_pk_fma_f32 v[174:175], v[174:175], v[78:79], v[78:79] neg_lo:[0,0,1] neg_hi:[0,0,1]\n"
  "v_mfma_f32_16x16x32_bf16 a[232:235], a[156:159], a[120:123], a[232:235]\n"
  "s_nop 0\n"
  "v_cvt_pk_bf16_f32 v107, v114, v118\n"
  "v_cvt_pk_bf16_f32 v111, v115, v119\n"
  "v_mfma_f32_16x16x32_bf16 a[232:235], a[160:163], a[124:127], a[232:235]\n"
  "s_waitcnt lgkmcnt(7)\n"
  "ds_write_b64 v229, v[108:109] offset:9248\n"
  "v_cvt_pk_bf16_f32 v136, v136, v140\n"
  "v_cvt_pk_bf16_f32 v140, v137, v141\n"
  "v_pk_fma_f32 v[178:179], v[178:179], v[78:79], v[78:79] neg_lo:[0,0,1] neg_hi:[0,0,1]\n"
  "v_pk_fma_f32 v[182:183], v[182:183], v[78:79], v[78:79] neg_lo:[0,0,1] neg_hi:[0,0,1]\n"
  "v_pk_fma_f32 v[200:201], v[200:201], v[80:81], v[80:81] neg_lo:[0,0,1] neg_hi:[0,0,1]\n"
  "v_pk_fma_f32 v[204:205], v[204:205], v[80:81], v[80:81] neg_lo:[0,0,1] neg_hi:[0,0,1]\n"
  "v_mfma_f32_16x16x32_bf16 v[64:67], a[132:135], a[96:99], v[248:251]\n"
  "v_cvt_pk_bf16_f32 v137, v144, v148\n"
  "v_cvt_pk_bf16_f32 v141, v145, v149\n"
  "v_mfma_f32_16x16x32_bf16 v[64:67], a[128:131], a[100:103], v[64:67]\n"
  "s_waitcnt lgkmcnt(6)\n"
  "v_cvt_pk_bf16_f32 v138, v138, v142\n"
  "v_cvt_pk_bf16_f32 v142, v139, v143\n"
  "v_mfma_f32_16x16x32_bf16 v[64:67], a[148:151], a[104:107], v[64:67]\n"
  "s_waitcnt vmcnt(12)\n"
  "ds_read_b128 a[64:67], v222 offset:0\n"
  "ds_read_b128 a[68:71], v222 offset:64\n"
  "v_mfma_f32_16x16x32_bf16 v[64:67], a[144:147], a[108:111], v[64:67]\n"
  "s_waitcnt lgkmcnt(6)\n"
  "v_cvt_pk_bf16_f32 v139, v146, v150\n"
  "v_cvt_pk_bf16_f32 v143, v147, v151\n"
  "v_mfma_f32_16x16x32_bf16 v[64:67], a[136:139], a[112:115], v[64:67]\n"
  "v_rcp_f32 v86, v86\n"
  "v_mfma_f32_16x16x32_bf16 v[64:67], a[140:143], a[116:119], v[64:67]\n"
  "ds_write_b64 v229, v[106:107] offset:9536\n"
  "v_rcp_f32_e64 v87, v87\n"
  "v_pk_fma_f32 v[208:209], v[208:209], v[80:81], v[80:81] neg_lo:[0,0,1] neg_hi:[0,0,1]\n"
  "v_pk_fma_f32 v[212:213], v[212:213], v[80:81], v[80:81] neg_lo:[0,0,1] neg_hi:[0,0,1]\n"
  "v_pk_fma_f32 v[202:203], v[202:203], v[82:83], v[82:83] neg_lo:[0,0,1] neg_hi:[0,0,1]\n"
  "v_pk_fma_f32 v[206:207], v[206:207], v[82:83], v[82:83] neg_lo:[0,0,1] neg_hi:[0,0,1]\n"
  "v_mfma_f32_16x16x32_bf16 v[0:3], a[140:143], a[20:23], v[248:251]\n"
  "v_cvt_pk_bf16_f32 v168, v168, v172\n"
  "v_cvt_pk_bf16_f32 v172, v169, v173\n"
  "v_mfma_f32_16x16x32_bf16 v[0:3], a[136:139], a[16:19], v[0:3]\n"
  "v_cvt_pk_bf16_f32 v169, v176, v180\n"
  "v_cvt_pk_bf16_f32 v173, v177, v181\n"
  "v_mfma_f32_16x16x32_bf16 v[0:3], a[144:147], a[12:15], v[0:3]\n"
  "s_waitcnt vmcnt(10)\n"
  "ds_read_b128 a[72:75], v222 offset:2240\n"
  "ds_read_b128 a[76:79], v222 offset:2304\n"
  "v_mfma_f32_16x16x32_bf16 v[0:3], a[148:151], a[8:11], v[0:3]\n"
  "s_waitcnt vmcnt(8)\n"
  "ds_read_b128 a[80:83], v222 offset:4480\n"
  "ds_read_b128 a[84:87], v222 offset:4544\n"
  "v_mfma_f32_16x16x32_bf16 v[0:3], a[128:131], a[4:7], v[0:3]\n"
  "s_waitcnt vmcnt(6)\n"
  "ds_read_b128 a[88:91], v222 offset:6720\n"
  "ds_read_b128 a[92:95], v222 offset:6784\n"
  "v_mfma_f32_16x16x32_bf16 v[0:3], a[132:135], a[0:3], v[0:3]\n"
  "s_nop 0\n"
  "ds_write_b64 v229, v[110:111] offset:9824\n"
  "v_max3_f32 v227, v64, v65, v66\n"
  "v_cvt_pk_bf16_f32 v170, v170, v174\n"
  "v_pk_fma_f32 v[210:211], v[210:211], v[82:83], v[82:83] neg_lo:[0,0,1] neg_hi:[0,0,1]\n"
  "v_pk_fma_f32 v[214:215], v[214:215], v[82:83], v[82:83] neg_lo:[0,0,1] neg_hi:[0,0,1]\n"
  "v_pk_fma_f32 v[236:237], v[236:237], v[86:87], v[86:87] neg_lo:[0,0,1] neg_hi:[0,0,1]\n"
  "v_mfma_f32_16x16x32_bf16 v[16:19], a[132:135], a[24:27], v[248:251]\n"
  "v_cvt_pk_bf16_f32 v174, v171, v175\n"
  "v_cvt_pk_bf16_f32 v171, v178, v182\n"
  "v_mfma_f32_16x16x32_bf16 v[16:19], a[128:131], a[28:31], v[16:19]\n"
  "ds_read_b64_tr_b16 a[252:253], v230 offset:8960\n"
  "ds_read_b64_tr_b16 a[254:255], v230 offset:8992\n"
  "v_mfma_f32_16x16x32_bf16 v[16:19], a[148:151], a[32:35], v[16:19]\n"
  "ds_read_b128 a[120:123], v235 offset:24704\n"
  "v_max3_f32 v223, v0, v1, v2\n"
  "v_mfma_f32_16x16x32_bf16 v[16:19], a[144:147], a[36:39], v[16:19]\n"
  "ds_read_b128 a[124:127], v235 offset:24768\n"
  "v_max_f32 v223, v223, v3\n"
  "v_mfma_f32_16x16x32_bf16 v[16:19], a[136:139], a[40:43], v[16:19]\n"
  "v_mul_f32 v223, s74, v223\n"
  "v_add_f32 v223, s64, v223\n"
  "v_mfma_f32_16x16x32_bf16 v[16:19], a[140:143], a[44:47], v[16:19]\n"
  "s_waitcnt lgkmcnt(12)\n"
  "ds_bpermute_b32 v223, v234, v223\n"
  "ds_write_b64 v229, v[136:137] offset:11200\n"
  "v_mfma_f32_16x16x32_bf16 v[32:35], a[140:143], a[68:71], v[248:251]\n"
  "ds_read_b64_tr_b16 v[244:245], v230 offset:10080\n"
  "ds_read_b64_tr_b16 v[246:247], v230 offset:10112\n"
  "v_mfma_f32_16x16x32_bf16 v[32:35], a[136:139], a[64:67], v[32:35]\n"
  "v_cvt_pk_bf16_f32 v175, v179, v183\n"
  "v_cvt_pk_bf16_f32 v200, v200, v204\n"
  "v_mfma_f32_16x16x32_bf16 v[32:35], a[144:147], a[60:63], v[32:35]\n"
  "v_max3_f32 v224, v16, v17, v18\n"
  "v_max_f32 v224, v224, v19\n"
  "v_mfma_f32_16x16x32_bf16 v[32:35], a[148:151], a[56:59], v[32:35]\n"
  "v_mul_f32 v224, s74, v224\n"
  "v_add_f32_e64 v224, s64, v224\n"
  "v_mfma_f32_16x16x32_bf16 v[32:35], a[128:131], a[52:55], v[32:35]\n"
  "ds_bpermute_b32 v224, v234, v224\n"
  "v_mfma_f32_16x16x32_bf16 v[32:35], a[132:135], a[48:51], v[32:35]\n"
  "s_waitcnt lgkmcnt(10)\n"
  "s_waitcnt lgkmcnt(7)\n"
  "buffer_store_dwordx4 a[252:255], v228, s[52:55], s80 offen offset:256 nt\n"
  "ds_write_b64 v229, v[140:141] offset:11488\n"
  "v_cvt_pk_bf16_f32 v204, v201, v205\n"
  "v_cvt_pk_bf16_f32 v201, v208, v212\n"
  "v_cvt_pk_bf16_f32 v205, v209, v213\n"
  "v_mfma_f32_16x16x32_bf16 v[48:51], a[132:135], a[72:75], v[248:251]\n"
  "s_waitcnt lgkmcnt(5)\n"
  "ds_read_b128 a[132:135], v235 offset:25728\n"
  "v_fma_f32 v0, v0, s73, v223\n"
  "v_mfma_f32_16x16x32_bf16 v[48:51], a[128:131], a[76:79], v[48:51]\n"
  "ds_read_b128 a[128:131], v235 offset:25792\n"
  "v_fma_f32 v1, v1, s73, v223\n"
  "v_mfma_f32_16x16x32_bf16 v[48:51], a[148:151], a[80:83], v[48:51]\n"
  "ds_read_b128 a[148:151], v235 offset:26752\n"
  "v_fma_f32 v2, v2, s73, v223\n"
  "v_mfma_f32_16x16x32_bf16 v[48:51], a[144:147], a[84:87], v[48:51]\n"
  "ds_read_b128 a[144:147], v235 offset:26816\n"
  "v_fma_f32 v3, v3, s73, v223\n"
  "v_mfma_f32_16x16x32_bf16 v[48:51], a[136:139], a[88:91], v[48:51]\n"
  "s_waitcnt lgkmcnt(5)\n"
  "v_exp_f32 v0, v0\n"
  "v_mfma_f32_16x16x32_bf16 v[48:51], a[140:143], a[92:95], v[48:51]\n"
  "ds_write_b64 v229, v[138:139] offset:11776\n"
  "v_exp_f32 v1, v1\n"
  "v_mfma_f32_16x16x32_bf16 v[240:243], a[120:123], a[96:99], v[248:251]\n"
  "v_exp_f32 v2, v2\n"
  "v_mfma_f32_16x16x32_bf16 v[240:243], a[124:127], a[100:103], v[240:243]\n"
  "s_waitcnt lgkmcnt(3)\n"
  "v_exp_f32 v3, v3\n"
  "v_mfma_f32_16x16x32_bf16 v[240:243], a[132:135], a[104:107], v[240:243]\n"
  "v_max3_f32 v225, v32, v33, v34\n"
  "v_max_f32 v225, v225, v35\n"
  "v_mfma_f32_16x16x32_bf16 v[240:243], a[128:131], a[108:111], v[240:243]\n"
  "s_waitcnt lgkmcnt(1)\n"
  "v_mul_f32 v225, s74, v225\n"
  "v_add_f32_e64 v225, s64, v225\n"
  "v_mfma_f32_16x16x32_bf16 v[240:243], a[148:151], a[112:115], v[240:243]\n"
  "ds_bpermute_b32 v225, v234, v225\n"
  "v_mfma_f32_16x16x32_bf16 v[240:243], a[144:147], a[116:119], v[240:243]\n"
  "buffer_store_dwordx4 v[244:247], v228, s[52:55], s80 offen offset:3328 nt\n"
  "ds_write_b64 v229, v[142:143] offset:12064\n"
  "v_fma_f32 v16, v16, s73, v224\n"
  "v_exp_f32 v16, v16\n"
  "v_mfma_f32_16x16x32_bf16 v[4:7], a[144:147], a[20:23], v[248:251]\n"
  "v_fma_f32 v17, v17, s73, v224\n"
  "v_fma_f32 v18, v18, s73, v224\n"
  "v_mfma_f32_16x16x32_bf16 v[4:7], a[148:151], a[16:19], v[4:7]\n"
  "v_exp_f32 v17, v17\n"
  "v_mfma_f32_16x16x32_bf16 v[4:7], a[128:131], a[12:15], v[4:7]\n"
  "v_max3_f32 v227, v227, v67, v240\n"
  "v_max3_f32 v227, v227, v241, v242\n"
  "v_mfma_f32_16x16x32_bf16 v[4:7], a[132:135], a[8:11], v[4:7]\n"
  "v_max_f32 v227, v227, v243\n"
  "v_mul_f32 v227, s74, v227\n"
  "v_mfma_f32_16x16x32_bf16 v[4:7], a[124:127], a[4:7], v[4:7]\n"
  "s_waitcnt lgkmcnt(1)\n"
  "v_add_f32 v227, s64, v227\n"
  "v_fma_f32 v19, v19, s73, v224\n"
  "v_mfma_f32_16x16x32_bf16 v[4:7], a[120:123], a[0:3], v[4:7]\n"
  "ds_bpermute_b32 v227, v234, v227\n"
  "ds_write_b64 v229, v[168:169] offset:13440\n"
  "v_mfma_f32_16x16x32_bf16 v[20:23], a[120:123], a[24:27], v[248:251]\n"
  "v_exp_f32 v18, v18\n"
  "v_mfma_f32_16x16x32_bf16 v[20:23], a[124:127], a[28:31], v[20:23]\n"
  "v_exp_f32 v19, v19\n"
  "v_mfma_f32_16x16x32_bf16 v[20:23], a[132:135], a[32:35], v[20:23]\n"
  "ds_read_b64_tr_b16 a[136:137], v219 offset:24704\n"
  "v_max3_f32 v226, v48, v49, v50\n"
  "v_mfma_f32_16x16x32_bf16 v[20:23], a[128:131], a[36:39], v[20:23]\n"
  "ds_read_b64_tr_b16 a[138:139], v220 offset:24704\n"
  "v_max_f32 v226, v226, v51\n"
  "v_mfma_f32_16x16x32_bf16 v[20:23], a[148:151], a[40:43], v[20:23]\n"
  "ds_read_b64_tr_b16 a[140:141], v219 offset:25728\n"
  "v_mul_f32_e64 v226, s74, v226\n"
  "v_mfma_f32_16x16x32_bf16 v[20:23], a[144:147], a[44:47], v[20:23]\n"
  "s_waitcnt lgkmcnt(4)\n"
  "ds_read_b64_tr_b16 a[142:143], v220 offset:25728\n"
  "ds_write_b64 v229, v[172:173] offset:13728\n"
  "v_fma_f32 v64, v64, s73, v227\n"
  "v_mfma_f32_16x16x32_bf16 v[36:39], a[144:147], a[68:71], v[248:251]\n"
  "v_exp_f32 v64, v64\n"
  "v_mfma_f32_16x16x32_bf16 v[36:39], a[148:151], a[64:67], v[36:39]\n"
  "v_fma_f32 v65, v65, s73, v227\n"
  "v_fma_f32 v66, v66, s73, v227\n"
  "v_mfma_f32_16x16x32_bf16 v[36:39], a[128:131], a[60:63], v[36:39]\n"
  "v_exp_f32 v65, v65\n"
  "v_mfma_f32_16x16x32_bf16 v[36:39], a[132:135], a[56:59], v[36:39]\n"
  "v_exp_f32 v66, v66\n"
  "v_mfma_f32_16x16x32_bf16 v[36:39], a[124:127], a[52:55], v[36:39]\n"
  "v_fma_f32 v67, v67, s73, v227\n"
  "v_cvt_pk_bf16_f32 v64, v64, v65\n"
  "v_mfma_f32_16x16x32_bf16 v[36:39], a[120:123], a[48:51], v[36:39]\n"
  "ds_write_b64 v229, v[170:171] offset:14016\n"
  "v_exp_f32 v67, v67\n"
  "v_mfma_f32_16x16x32_bf16 v[52:55], a[120:123], a[72:75], v[248:251]\n"
  "ds_read_b64_tr_b16 a[120:121], v219 offset:24720\n"
  "v_cvt_pk_bf16_f32 v65, v66, v67\n"
  "v_mfma_f32_16x16x32_bf16 v[52:55], a[124:127], a[76:79], v[52:55]\n"
  "ds_read_b64_tr_b16 a[122:123], v220 offset:24720\n"
  "v_add_f32_e64 v226, s64, v226\n"
  "v_mfma_f32_16x16x32_bf16 v[52:55], a[132:135], a[80:83], v[52:55]\n"
  "ds_read_b64_tr_b16 a[124:125], v219 offset:25744\n"
  "v_fma_f32 v32, v32, s73, v225\n"
  "v_mfma_f32_16x16x32_bf16 v[52:55], a[128:131], a[84:87], v[52:55]\n"
  "ds_read_b64_tr_b16 a[126:127], v220 offset:25744\n"
  "v_fma_f32 v33, v33, s73, v225\n"
  "v_mfma_f32_16x16x32_bf16 v[52:55], a[148:151], a[88:91], v[52:55]\n"
  "v_exp_f32 v32, v32\n"
  "v_mfma_f32_16x16x32_bf16 v[52:55], a[144:147], a[92:95], v[52:55]\n"
  "s_waitcnt lgkmcnt(6)\n"
  "ds_read_b64_tr_b16 a[252:253], v230 offset:11200\n"
  "ds_read_b64_tr_b16 a[254:255], v230 offset:11232\n"
  "ds_write_b64 v231, v[64:65] offset:0\n"
  "v_mfma_f32_16x16x32_bf16 a[220:223], v[56:59], a[136:139], a[220:223]\n"
  "v_exp_f32_e64 v33, v33\n"
  "v_mfma_f32_16x16x32_bf16 a[220:223], v[60:63], a[140:143], a[220:223]\n"
  "ds_bpermute_b32 v226, v234, v226\n"
  "ds_write_b64 v229, v[174:175] offset:14304\n"
  "v_mfma_f32_16x16x32_bf16 a[204:207], v[40:43], a[136:139], a[204:207]\n"
  "v_fma_f32 v34, v34, s73, v225\n"
  "v_fma_f32 v35, v35, s73, v225\n"
  "v_mfma_f32_16x16x32_bf16 a[204:207], v[44:47], a[140:143], a[204:207]\n"
  "s_waitcnt vmcnt(2) lgkmcnt(2)\n"
  "s_barrier\n"
  "ds_read_b64_tr_b16 a[132:133], v217 offset:12352\n"
  "ds_read_b64_tr_b16 v[244:245], v230 offset:12320\n"
  "ds_read_b64_tr_b16 v[246:247], v230 offset:12352\n"
  "v_mfma_f32_16x16x32_bf16 a[188:191], v[24:27], a[136:139], a[188:191]\n"
  "ds_read_b64 a[156:157], v231 offset:0\n"
  "v_fma_f32 v4, v4, s73, v223\n"
  "v_mfma_f32_16x16x32_bf16 a[188:191], v[28:31], a[140:143], a[188:191]\n"
  "buffer_store_dwordx4 a[252:255], v228, s[52:55], s81 offen offset:256 nt\n"
  "ds_read_b64 a[158:159], v232 offset:0\n"
  "ds_write_b64 v229, v[200:201] offset:15680\n"
  "v_exp_f32_e64 v34, v34\n"
  "v_mfma_f32_16x16x32_bf16 a[172:175], v[8:11], a[136:139], a[172:175]\n"
  "ds_read_b64_tr_b16 a[134:135], v218 offset:12352\n"
  "v_fma_f32 v5, v5, s73, v223\n"
  "v_mfma_f32_16x16x32_bf16 a[172:175], v[12:15], a[140:143], a[172:175]\n"
  "ds_read_b64_tr_b16 a[128:129], v217 offset:12368\n"
  "ds_write_b64 v229, v[204:205] offset:15968\n"
  "v_fma_f32 v6, v6, s73, v223\n"
  "v_mfma_f32_16x16x32_bf16 a[176:179], v[8:11], a[120:123], a[176:179]\n"
  "s_waitcnt lgkmcnt(10)\n"
  "ds_read_b64_tr_b16 a[130:131], v218 offset:12368\n"
  "v_cvt_pk_bf16_f32 v8, v0, v1\n"
  "v_mfma_f32_16x16x32_bf16 a[176:179], v[12:15], a[124:127], a[176:179]\n"
  "s_nop 0\n"
  "ds_read_b64_tr_b16 a[252:253], v230 offset:13440\n"
  "ds_read_b64_tr_b16 a[254:255], v230 offset:13472\n"
  "v_cvt_pk_bf16_f32 v9, v2, v3\n"
  "v_fma_f32 v48, v48, s73, v226\n"
  "v_cvt_pk_bf16_f32 v202, v202, v206\n"
  "v_mfma_f32_16x16x32_bf16 a[192:195], v[24:27], a[120:123], a[192:195]\n"
  "ds_read_b128 a[136:139], v216 offset:12352\n"
  "v_cvt_pk_bf16_f32 v24, v16, v17\n"
  "v_mfma_f32_16x16x32_bf16 a[192:195], v[28:31], a[124:127], a[192:195]\n"
  "ds_read_b128 a[140:143], v216 offset:12416\n"
  "v_cvt_pk_bf16_f32 v25, v18, v19\n"
  "v_cvt_pk_bf16_f32 v206, v203, v207\n"
  "v_cvt_pk_bf16_f32 v203, v210, v214\n"
  "v_cvt_pk_bf16_f32 v207, v211, v215\n"
  "v_mfma_f32_16x16x32_bf16 a[208:211], v[40:43], a[120:123], a[208:211]\n"
  "ds_read_b128 a[148:151], v216 offset:13376\n"
  "v_cvt_pk_bf16_f32 v40, v32, v33\n"
  "v_mfma_f32_16x16x32_bf16 a[208:211], v[44:47], a[124:127], a[208:211]\n"
  "ds_read_b128 a[144:147], v216 offset:13440\n"
  "ds_write_b64 v229, v[202:203] offset:16256\n"
  "v_fma_f32 v49, v49, s73, v226\n"
  "v_mfma_f32_16x16x32_bf16 a[224:227], v[56:59], a[120:123], a[224:227]\n"
  "s_nop 0\n"
  "ds_read_b128 a[120:123], v216 offset:14400\n"
  "v_fma_f32 v50, v50, s73, v226\n"
  "v_mfma_f32_16x16x32_bf16 a[224:227], v[60:63], a[124:127], a[224:227]\n"
  "L_JOIN_%=:\n"
  "s_waitcnt lgkmcnt(8)\n"
  "ds_read_b128 a[124:127], v216 offset:14464\n"
  "ds_write_b64 v229, v[206:207] offset:16544\n"
  "v_fma_f32 v51, v51, s73, v226\n"
  "v_mfma_f32_16x16x32_bf16 a[236:239], a[160:163], a[132:135], a[236:239]\n"
  "s_add_i32 s76, s76, 1\n"
  "buffer_store_dwordx4 v[244:247], v228, s[52:55], s81 offen offset:3328 nt\n"
  "v_exp_f32 v35, v35\n"
  "v_fma_f32 v7, v7, s73, v223\n"
  "v_mfma_f32_16x16x32_bf16 a[240:243], a[160:163], a[128:131], a[240:243]\n"
  "ds_read_b64_tr_b16 v[244:245], v230 offset:14560\n"
  "ds_read_b64_tr_b16 v[246:247], v230 offset:14592\n"
  "v_cvt_pk_bf16_f32 v41, v34, v35\n"
  "v_fma_f32 v20, v20, s73, v224\n"
  "v_accvgpr_read_b32 v239, a251\n"
  "v_pk_fma_f32 v[238:239], v[238:239], v[86:87], v[86:87] neg_lo:[0,0,1] neg_hi:[0,0,1]\n"
  "v_mfma_f32_16x16x32_bf16 v[184:187], v[60:63], a[132:135], v[184:187]\n"
  "v_exp_f32_e64 v48, v48\n"
  "v_accvgpr_read_b32 v136, a180\n"
  "v_accvgpr_read_b32 v137, a181\n"
  "v_accvgpr_read_b32 v140, a184\n"
  "v_pk_fma_f32 v[136:137], v[136:137], v[72:73], v[72:73] neg_lo:[0,0,1] neg_hi:[0,0,1]\n"
  "v_mfma_f32_16x16x32_bf16 v[188:191], v[60:63], a[128:131], v[188:191]\n"
  "v_exp_f32 v49, v49\n"
  "v_mfma_f32_16x16x32_bf16 v[152:155], v[44:47], a[132:135], v[152:155]\n"
  "v_exp_f32 v50, v50\n"
  "v_accvgpr_read_b32 v141, a185\n"
  "v_accvgpr_read_b32 v144, a188\n"
  "v_accvgpr_read_b32 v145, a189\n"
  "v_pk_fma_f32 v[140:141], v[140:141], v[72:73], v[72:73] neg_lo:[0,0,1] neg_hi:[0,0,1]\n"
  "v_pk_fma_f32 v[144:145], v[144:145], v[72:73], v[72:73] neg_lo:[0,0,1] neg_hi:[0,0,1]\n"
  "v_mfma_f32_16x16x32_bf16 v[156:159], v[44:47], a[128:131], v[156:159]\n"
  "v_exp_f32_e64 v51, v51\n"
  "v_accvgpr_read_b32 v148, a192\n"
  "v_accvgpr_read_b32 v149, a193\n"
  "v_accvgpr_read_b32 v138, a182\n"
  "v_pk_fma_f32 v[148:149], v[148:149], v[72:73], v[72:73] neg_lo:[0,0,1] neg_hi:[0,0,1]\n"
  "v_pk_fma_f32 v[184:185], v[184:185], v[80:81], v[80:81] neg_lo:[0,0,1] neg_hi:[0,0,1]\n"
  "v_pk_fma_f32 v[186:187], v[186:187], v[82:83], v[82:83] neg_lo:[0,0,1] neg_hi:[0,0,1]\n"
  "v_mfma_f32_16x16x32_bf16 v[120:123], v[28:31], a[132:135], v[120:123]\n"
  "v_exp_f32_e64 v4, v4\n"
  "v_mfma_f32_16x16x32_bf16 v[124:127], v[28:31], a[128:131], v[124:127]\n"
  "v_cvt_pk_bf16_f32 v56, v48, v49\n"
  "v_cvt_pk_bf16_f32 v57, v50, v51\n"
  "v_accvgpr_read_b32 v139, a183\n"
  "v_accvgpr_read_b32 v142, a186\n"
  "v_accvgpr_read_b32 v143, a187\n"
  "v_pk_fma_f32 v[138:139], v[138:139], v[74:75], v[74:75] neg_lo:[0,0,1] neg_hi:[0,0,1]\n"
  "v_pk_fma_f32 v[142:143], v[142:143], v[74:75], v[74:75] neg_lo:[0,0,1] neg_hi:[0,0,1]\n"
  "v_pk_fma_f32 v[188:189], v[188:189], v[80:81], v[80:81] neg_lo:[0,0,1] neg_hi:[0,0,1]\n"
  "v_pk_fma_f32 v[190:191], v[190:191], v[82:83], v[82:83] neg_lo:[0,0,1] neg_hi:[0,0,1]\n"
  "v_mfma_f32_16x16x32_bf16 v[88:91], v[12:15], a[132:135], v[88:91]\n"
  "v_exp_f32_e64 v5, v5\n"
  "v_accvgpr_read_b32 v146, a190\n"
  "v_accvgpr_read_b32 v147, a191\n"
  "v_accvgpr_read_b32 v150, a194\n"
  "v_pk_fma_f32 v[146:147], v[146:147], v[74:75], v[74:75] neg_lo:[0,0,1] neg_hi:[0,0,1]\n"
  "v_pk_fma_f32 v[152:153], v[152:153], v[76:77], v[76:77] neg_lo:[0,0,1] neg_hi:[0,0,1]\n"
  "v_pk_fma_f32 v[156:157], v[156:157], v[76:77], v[76:77] neg_lo:[0,0,1] neg_hi:[0,0,1]\n"
  "v_pk_fma_f32 v[154:155], v[154:155], v[78:79], v[78:79] neg_lo:[0,0,1] neg_hi:[0,0,1]\n"
  "v_mfma_f32_16x16x32_bf16 v[92:95], v[12:15], a[128:131], v[92:95]\n"
  "s_waitcnt lgkmcnt(8)\n"
  "v_exp_f32 v6, v6\n"
  "v_accvgpr_read_b32 v151, a195\n"
  "v_cvt_pk_bf16_f32 v136, v136, v140\n"
  "v_cvt_pk_bf16_f32 v140, v137, v141\n"
  "v_pk_fma_f32 v[150:151], v[150:151], v[74:75], v[74:75] neg_lo:[0,0,1] neg_hi:[0,0,1]\n"
  "v_pk_fma_f32 v[158:159], v[158:159], v[78:79], v[78:79] neg_lo:[0,0,1] neg_hi:[0,0,1]\n"
  "v_pk_fma_f32 v[120:121], v[120:121], v[72:73], v[72:73] neg_lo:[0,0,1] neg_hi:[0,0,1]\n"
  "v_pk_fma_f32 v[122:123], v[122:123], v[74:75], v[74:75] neg_lo:[0,0,1] neg_hi:[0,0,1]\n"
  "v_mfma_f32_16x16x32_bf16 v[64:67], a[136:139], a[96:99], v[248:251]\n"
  "v_exp_f32 v7, v7\n"
  "v_mfma_f32_16x16x32_bf16 v[64:67], a[140:143], a[100:103], v[64:67]\n"
  "s_waitcnt lgkmcnt(6)\n"
  "v_exp_f32 v20, v20\n"
  "v_mfma_f32_16x16x32_bf16 v[64:67], a[148:151], a[104:107], v[64:67]\n"
  "v_cvt_pk_bf16_f32 v10, v4, v5\n"
  "v_cvt_pk_bf16_f32 v11, v6, v7\n"
  "v_mfma_f32_16x16x32_bf16 v[64:67], a[144:147], a[108:111], v[64:67]\n"
  "s_waitcnt lgkmcnt(3)\n"
  "v_fma_f32 v21, v21, s73, v224\n"
  "v_fma_f32 v22, v22, s73, v224\n"
  "v_mfma_f32_16x16x32_bf16 v[64:67], a[120:123], a[112:115], v[64:67]\n"
  "v_exp_f32_e64 v21, v21\n"
  "v_mfma_f32_16x16x32_bf16 v[64:67], a[124:127], a[116:119], v[64:67]\n"
  "buffer_store_dwordx4 a[252:255], v228, s[52:55], s82 offen offset:256 nt\n"
  "v_exp_f32_e64 v22, v22\n"
  "v_fma_f32 v23, v23, s73, v224\n"
  "v_mfma_f32_16x16x32_bf16 v[0:3], a[124:127], a[20:23], v[248:251]\n"
  "v_exp_f32_e64 v23, v23\n"
  "v_mfma_f32_16x16x32_bf16 v[0:3], a[120:123], a[16:19], v[0:3]\n"
  "v_cvt_pk_bf16_f32 v26, v20, v21\n"
  "v_cvt_pk_bf16_f32 v27, v22, v23\n"
  "v_mfma_f32_16x16x32_bf16 v[0:3], a[144:147], a[12:15], v[0:3]\n"
  "v_fma_f32 v36, v36, s73, v225\n"
  "v_fma_f32 v37, v37, s73, v225\n"
  "v_mfma_f32_16x16x32_bf16 v[0:3], a[148:151], a[8:11], v[0:3]\n"
  "v_exp_f32_e64 v36, v36\n"
  "v_mfma_f32_16x16x32_bf16 v[0:3], a[140:143], a[4:7], v[0:3]\n"
  "v_exp_f32_e64 v37, v37\n"
  "v_mfma_f32_16x16x32_bf16 v[0:3], a[136:139], a[0:3], v[0:3]\n"
  "ds_read_b64_tr_b16 a[252:253], v230 offset:15680\n"
  "ds_read_b64_tr_b16 a[254:255], v230 offset:15712\n"
  "v_fma_f32 v38, v38, s73, v225\n"
  "v_fma_f32 v39, v39, s73, v225\n"
  "v_cvt_pk_bf16_f32 v137, v144, v148\n"
  "v_pk_fma_f32 v[124:125], v[124:125], v[72:73], v[72:73] neg_lo:[0,0,1] neg_hi:[0,0,1]\n"
  "v_pk_fma_f32 v[126:127], v[126:127], v[74:75], v[74:75] neg_lo:[0,0,1] neg_hi:[0,0,1]\n"
  "v_pk_fma_f32 v[88:89], v[88:89], v[68:69], v[68:69] neg_lo:[0,0,1] neg_hi:[0,0,1]\n"
  "v_pk_fma_f32 v[92:93], v[92:93], v[68:69], v[68:69] neg_lo:[0,0,1] neg_hi:[0,0,1]\n"
  "v_mfma_f32_16x16x32_bf16 v[16:19], a[136:139], a[24:27], v[248:251]\n"
  "v_exp_f32 v38, v38\n"
  "v_mfma_f32_16x16x32_bf16 v[16:19], a[140:143], a[28:31], v[16:19]\n"
  "v_exp_f32_e64 v39, v39\n"
  "v_mfma_f32_16x16x32_bf16 v[16:19], a[148:151], a[32:35], v[16:19]\n"
  "ds_read_b64_tr_b16 a[132:133], v219 offset:12352\n"
  "v_cvt_pk_bf16_f32 v42, v36, v37\n"
  "v_mfma_f32_16x16x32_bf16 v[16:19], a[144:147], a[36:39], v[16:19]\n"
  "ds_read_b64_tr_b16 a[134:135], v220 offset:12352\n"
  "v_cvt_pk_bf16_f32 v43, v38, v39\n"
  "v_mfma_f32_16x16x32_bf16 v[16:19], a[120:123], a[40:43], v[16:19]\n"
  "ds_read_b64_tr_b16 a[128:129], v219 offset:12368\n"
  "v_fma_f32 v52, v52, s73, v226\n"
  "v_mfma_f32_16x16x32_bf16 v[16:19], a[124:127], a[44:47], v[16:19]\n"
  "s_waitcnt lgkmcnt(5)\n"
  "buffer_store_dwordx4 v[244:247], v228, s[52:55], s82 offen offset:3328 nt\n"
  "ds_read_b64_tr_b16 a[130:131], v220 offset:12368\n"
  "ds_write_b64 v229, v[136:137] offset:2240\n"
  "v_exp_f32_e64 v52, v52\n"
  "v_pk_fma_f32 v[90:91], v[90:91], v[70:71], v[70:71] neg_lo:[0,0,1] neg_hi:[0,0,1]\n"
  "v_pk_fma_f32 v[94:95], v[94:95], v[70:71], v[70:71] neg_lo:[0,0,1] neg_hi:[0,0,1]\n"
  "v_mfma_f32_16x16x32_bf16 v[32:35], a[124:127], a[68:71], v[248:251]\n"
  "v_fma_f32 v53, v53, s73, v226\n"
  "v_fma_f32 v54, v54, s73, v226\n"
  "v_mfma_f32_16x16x32_bf16 v[32:35], a[120:123], a[64:67], v[32:35]\n"
  "v_exp_f32 v53, v53\n"
  "v_mfma_f32_16x16x32_bf16 v[32:35], a[144:147], a[60:63], v[32:35]\n"
  "v_exp_f32 v54, v54\n"
  "v_mfma_f32_16x16x32_bf16 v[32:35], a[148:151], a[56:59], v[32:35]\n"
  "v_fma_f32 v55, v55, s73, v226\n"
  "v_cvt_pk_bf16_f32 v58, v52, v53\n"
  "v_mfma_f32_16x16x32_bf16 v[32:35], a[140:143], a[52:55], v[32:35]\n"
  "v_exp_f32 v55, v55\n"
  "v_mfma_f32_16x16x32_bf16 v[32:35], a[136:139], a[48:51], v[32:35]\n"
  "ds_read_b64_tr_b16 v[244:245], v230 offset:16800\n"
  "v_cvt_pk_bf16_f32 v59, v54, v55\n"
  "v_mfma_f32_16x16x32_bf16 v[48:51], a[136:139], a[72:75], v[248:251]\n"
  "ds_read_b128 a[136:139], v235 offset:12352\n"
  "ds_read_b64_tr_b16 v[246:247], v230 offset:16832\n"
  "v_mfma_f32_16x16x32_bf16 v[48:51], a[140:143], a[76:79], v[48:51]\n"
  "ds_read_b128 a[140:143], v235 offset:12416\n"
  "v_cvt_pk_bf16_f32 v138, v138, v142\n"
  "v_mfma_f32_16x16x32_bf16 v[48:51], a[148:151], a[80:83], v[48:51]\n"
  "ds_read_b128 a[148:151], v235 offset:13376\n"
  "v_cvt_pk_bf16_f32 v142, v139, v143\n"
  "v_mfma_f32_16x16x32_bf16 v[48:51], a[144:147], a[84:87], v[48:51]\n"
  "ds_read_b128 a[144:147], v235 offset:13440\n"
  "v_cvt_pk_bf16_f32 v139, v146, v150\n"
  "v_mfma_f32_16x16x32_bf16 v[48:51], a[120:123], a[88:91], v[48:51]\n"
  "ds_read_b128 a[120:123], v235 offset:14400\n"
  "v_accvgpr_read_b32 v104, a164\n"
  "v_mfma_f32_16x16x32_bf16 v[48:51], a[124:127], a[92:95], v[48:51]\n"
  "s_waitcnt lgkmcnt(8)\n"
  "buffer_store_dwordx4 a[252:255], v228, s[52:55], s83 offen offset:256 nt\n"
  "ds_read_b128 a[124:127], v235 offset:14464\n"
  "ds_write_b64 v229, v[138:139] offset:2816\n"
  "v_accvgpr_read_b32 v105, a165\n"
  "v_accvgpr_read_b32 v108, a168\n"
  "v_pk_fma_f32 v[104:105], v[104:105], v[68:69], v[68:69] neg_lo:[0,0,1] neg_hi:[0,0,1]\n"
  "v_mfma_f32_16x16x32_bf16 v[96:99], v[12:15], a[132:135], v[96:99]\n"
  "v_accvgpr_read_b32 v109, a169\n"
  "v_accvgpr_read_b32 v112, a172\n"
  "v_accvgpr_read_b32 v113, a173\n"
  "v_accvgpr_read_b32 v116, a176\n"
  "v_accvgpr_read_b32 v117, a177\n"
  "v_pk_fma_f32 v[108:109], v[108:109], v[68:69], v[68:69] neg_lo:[0,0,1] neg_hi:[0,0,1]\n"
  "v_pk_fma_f32 v[112:113], v[112:113], v[68:69], v[68:69] neg_lo:[0,0,1] neg_hi:[0,0,1]\n"
  "v_pk_fma_f32 v[116:117], v[116:117], v[68:69], v[68:69] neg_lo:[0,0,1] neg_hi:[0,0,1]\n"
  "v_mfma_f32_16x16x32_bf16 v[100:103], v[12:15], a[128:131], v[100:103]\n"
  "v_accvgpr_read_b32 v106, a166\n"
  "v_accvgpr_read_b32 v107, a167\n"
  "v_mfma_f32_16x16x32_bf16 v[128:131], v[28:31], a[132:135], v[128:131]\n"
  "v_accvgpr_read_b32 v110, a170\n"
  "v_accvgpr_read_b32 v111, a171\n"
  "v_accvgpr_read_b32 v114, a174\n"
  "v_accvgpr_read_b32 v115, a175\n"
  "v_accvgpr_read_b32 v118, a178\n"
  "v_pk_fma_f32 v[106:107], v[106:107], v[70:71], v[70:71] neg_lo:[0,0,1] neg_hi:[0,0,1]\n"
  "v_pk_fma_f32 v[110:111], v[110:111], v[70:71], v[70:71] neg_lo:[0,0,1] neg_hi:[0,0,1]\n"
  "v_pk_fma_f32 v[114:115], v[114:115], v[70:71], v[70:71] neg_lo:[0,0,1] neg_hi:[0,0,1]\n"
  "v_mfma_f32_16x16x32_bf16 v[132:135], v[28:31], a[128:131], v[132:135]\n"
  "v_accvgpr_read_b32 v119, a179\n"
  "v_cvt_pk_bf16_f32 v141, v145, v149\n"
  "v_cvt_pk_bf16_f32 v143, v147, v151\n"
  "v_cvt_pk_bf16_f32 v104, v104, v108\n"
  "v_cvt_pk_bf16_f32 v108, v105, v109\n"
  "v_pk_fma_f32 v[118:119], v[118:119], v[70:71], v[70:71] neg_lo:[0,0,1] neg_hi:[0,0,1]\n"
  "v_pk_fma_f32 v[96:97], v[96:97], v[68:69], v[68:69] neg_lo:[0,0,1] neg_hi:[0,0,1]\n"
  "v_pk_fma_f32 v[98:99], v[98:99], v[70:71], v[70:71] neg_lo:[0,0,1] neg_hi:[0,0,1]\n"
  "v_mfma_f32_16x16x32_bf16 v[160:163], v[44:47], a[132:135], v[160:163]\n"
  "ds_write_b64 v229, v[140:141] offset:2528\n"
  "v_cvt_pk_bf16_f32 v105, v112, v116\n"
  "v_cvt_pk_bf16_f32 v109, v113, v117\n"
  "v_pk_fma_f32 v[100:101], v[100:101], v[68:69], v[68:69] neg_lo:[0,0,1] neg_hi:[0,0,1]\n"
  "v_pk_fma_f32 v[102:103], v[102:103], v[70:71], v[70:71] neg_lo:[0,0,1] neg_hi:[0,0,1]\n"
  "v_mfma_f32_16x16x32_bf16 v[164:167], v[44:47], a[128:131], v[164:167]\n"
  "v_cvt_pk_bf16_f32 v106, v106, v110\n"
  "v_cvt_pk_bf16_f32 v110, v107, v111\n"
  "v_mfma_f32_16x16x32_bf16 v[192:195], v[60:63], a[132:135], v[192:195]\n"
  "ds_write_b64 v229, v[142:143] offset:3104\n"
  "v_cvt_pk_bf16_f32 v107, v114, v118\n"
  "v_cvt_pk_bf16_f32 v111, v115, v119\n"
  "v_pk_fma_f32 v[128:129], v[128:129], v[72:73], v[72:73] neg_lo:[0,0,1] neg_hi:[0,0,1]\n"
  "v_pk_fma_f32 v[132:133], v[132:133], v[72:73], v[72:73] neg_lo:[0,0,1] neg_hi:[0,0,1]\n"
  "v_pk_fma_f32 v[130:131], v[130:131], v[74:75], v[74:75] neg_lo:[0,0,1] neg_hi:[0,0,1]\n"
  "v_pk_fma_f32 v[134:135], v[134:135], v[74:75], v[74:75] neg_lo:[0,0,1] neg_hi:[0,0,1]\n"
  "v_mfma_f32_16x16x32_bf16 v[196:199], v[60:63], a[128:131], v[196:199]\n"
  "s_nop 0\n"
  "s_waitcnt lgkmcnt(7)\n"
  "ds_write_b64 v229, v[104:105] offset:0\n"
  "v_cvt_pk_bf16_f32 v236, v236, v238\n"
  "v_cvt_pk_bf16_f32 v237, v237, v239\n"
  "v_pk_fma_f32 v[160:161], v[160:161], v[76:77], v[76:77] neg_lo:[0,0,1] neg_hi:[0,0,1]\n"
  "v_pk_fma_f32 v[162:163], v[162:163], v[78:79], v[78:79] neg_lo:[0,0,1] neg_hi:[0,0,1]\n"
  "v_mfma_f32_16x16x32_bf16 v[4:7], a[136:139], a[0:3], v[248:251]\n"
  "ds_write_b32 v233, v236 offset:20736\n"
  "v_mfma_f32_16x16x32_bf16 v[4:7], a[140:143], a[4:7], v[4:7]\n"
  "s_waitcnt lgkmcnt(7)\n"
  "ds_write_b32 v233, v237 offset:21024\n"
  "v_mfma_f32_16x16x32_bf16 v[4:7], a[148:151], a[8:11], v[4:7]\n"
  "v_cvt_pk_bf16_f32 v184, v184, v188\n"
  "v_cvt_pk_bf16_f32 v188, v185, v189\n"
  "v_mfma_f32_16x16x32_bf16 v[4:7], a[144:147], a[12:15], v[4:7]\n"
  "s_waitcnt lgkmcnt(6)\n"
  "v_cvt_pk_bf16_f32 v186, v186, v190\n"
  "v_cvt_pk_bf16_f32 v190, v187, v191\n"
  "v_mfma_f32_16x16x32_bf16 v[4:7], a[120:123], a[16:19], v[4:7]\n"
  "v_accvgpr_read_b32 v240, a236\n"
  "v_accvgpr_read_b32 v241, a237\n"
  "v_mfma_f32_16x16x32_bf16 v[4:7], a[124:127], a[20:23], v[4:7]\n"
  "buffer_store_dwordx4 v[244:247], v228, s[52:55], s83 offen offset:3328 nt\n"
  "ds_write_b64 v229, v[108:109] offset:288\n"
  "v_mfma_f32_16x16x32_bf16 v[20:23], a[124:127], a[44:47], v[248:251]\n"
  "v_accvgpr_read_b32 v236, a238\n"
  "v_accvgpr_read_b32 v237, a239\n"
  "v_mfma_f32_16x16x32_bf16 v[20:23], a[120:123], a[40:43], v[20:23]\n"
  "v_accvgpr_read_b32 v242, a240\n"
  "v_accvgpr_read_b32 v243, a241\n"
  "v_mfma_f32_16x16x32_bf16 v[20:23], a[144:147], a[36:39], v[20:23]\n"
  "ds_read_b64_tr_b16 a[132:133], v217 offset:14400\n"
  "v_accvgpr_read_b32 v238, a242\n"
  "v_mfma_f32_16x16x32_bf16 v[20:23], a[148:151], a[32:35], v[20:23]\n"
  "ds_read_b64_tr_b16 a[134:135], v218 offset:14400\n"
  "v_accvgpr_read_b32 v239, a243\n"
  "v_mfma_f32_16x16x32_bf16 v[20:23], a[140:143], a[28:31], v[20:23]\n"
  "ds_read_b64_tr_b16 a[128:129], v217 offset:14416\n"
  "v_fma_f32 v64, v64, s73, v227\n"
  "v_mfma_f32_16x16x32_bf16 v[20:23], a[136:139], a[24:27], v[20:23]\n"
  "ds_read_b64_tr_b16 a[130:131], v218 offset:14416\n"
  "ds_write_b64 v229, v[106:107] offset:576\n"
  "v_fma_f32 v65, v65, s73, v227\n"
  "v_pk_fma_f32 v[192:193], v[192:193], v[80:81], v[80:81] neg_lo:[0,0,1] neg_hi:[0,0,1]\n"
  "v_pk_fma_f32 v[196:197], v[196:197], v[80:81], v[80:81] neg_lo:[0,0,1] neg_hi:[0,0,1]\n"
  "v_pk_fma_f32 v[194:195], v[194:195], v[82:83], v[82:83] neg_lo:[0,0,1] neg_hi:[0,0,1]\n"
  "v_pk_fma_f32 v[198:199], v[198:199], v[82:83], v[82:83] neg_lo:[0,0,1] neg_hi:[0,0,1]\n"
  "v_mfma_f32_16x16x32_bf16 v[36:39], a[136:139], a[48:51], v[248:251]\n"
  "v_exp_f32_e64 v64, v64\n"
  "v_mfma_f32_16x16x32_bf16 v[36:39], a[140:143], a[52:55], v[36:39]\n"
  "v_exp_f32_e64 v65, v65\n"
  "v_mfma_f32_16x16x32_bf16 v[36:39], a[148:151], a[56:59], v[36:39]\n"
  "v_cvt_pk_bf16_f32 v185, v192, v196\n"
  "v_cvt_pk_bf16_f32 v187, v194, v198\n"
  "v_mfma_f32_16x16x32_bf16 v[36:39], a[144:147], a[60:63], v[36:39]\n"
  "v_cvt_pk_bf16_f32 v189, v193, v197\n"
  "v_cvt_pk_bf16_f32 v191, v195, v199\n"
  "v_mfma_f32_16x16x32_bf16 v[36:39], a[120:123], a[64:67], v[36:39]\n"
  "v_fma_f32 v66, v66, s73, v227\n"
  "v_fma_f32 v67, v67, s73, v227\n"
  "v_mfma_f32_16x16x32_bf16 v[36:39], a[124:127], a[68:71], v[36:39]\n"
  "ds_write_b64 v229, v[110:111] offset:864\n"
  "v_exp_f32_e64 v66, v66\n"
  "v_pk_fma_f32 v[164:165], v[164:165], v[76:77], v[76:77] neg_lo:[0,0,1] neg_hi:[0,0,1]\n"
  "v_pk_fma_f32 v[166:167], v[166:167], v[78:79], v[78:79] neg_lo:[0,0,1] neg_hi:[0,0,1]\n"
  "v_pk_fma_f32 v[240:241], v[240:241], v[84:85], v[84:85] neg_lo:[0,0,1] neg_hi:[0,0,1]\n"
  "v_pk_fma_f32 v[242:243], v[242:243], v[84:85], v[84:85] neg_lo:[0,0,1] neg_hi:[0,0,1]\n"
  "v_mfma_f32_16x16x32_bf16 v[52:55], a[124:127], a[92:95], v[248:251]\n"
  "ds_read_b64_tr_b16 a[124:125], v219 offset:14400\n"
  "v_cvt_pk_bf16_f32 v152, v152, v156\n"
  "v_mfma_f32_16x16x32_bf16 v[52:55], a[120:123], a[88:91], v[52:55]\n"
  "ds_read_b64_tr_b16 a[126:127], v220 offset:14400\n"
  "ds_read_b64_tr_b16 a[252:253], v230 offset:0\n"
  "v_mfma_f32_16x16x32_bf16 v[52:55], a[144:147], a[84:87], v[52:55]\n"
  "ds_read_b64_tr_b16 a[120:121], v219 offset:14416\n"
  "ds_read_b64_tr_b16 a[254:255], v230 offset:32\n"
  "v_mfma_f32_16x16x32_bf16 v[52:55], a[148:151], a[80:83], v[52:55]\n"
  "ds_read_b64_tr_b16 a[122:123], v220 offset:14416\n"
  "ds_read_b64_tr_b16 v[244:245], v230 offset:1120\n"
  "v_mfma_f32_16x16x32_bf16 v[52:55], a[140:143], a[76:79], v[52:55]\n"
  "v_exp_f32 v67, v67\n"
  "v_mfma_f32_16x16x32_bf16 v[52:55], a[136:139], a[72:75], v[52:55]\n"
  "s_waitcnt lgkmcnt(9)\n"
  "ds_read_b64_tr_b16 v[246:247], v230 offset:1152\n"
  "v_cvt_pk_bf16_f32 v156, v153, v157\n"
  "v_mfma_f32_16x16x32_bf16 a[244:247], a[156:159], a[132:135], 1.0\n"
  "ds_write_b64 v229, v[184:185] offset:15680\n"
  "v_cvt_pk_bf16_f32 v153, v160, v164\n"
  "v_cvt_pk_bf16_f32 v154, v154, v158\n"
  "v_pk_fma_f32 v[236:237], v[236:237], v[86:87], v[86:87] neg_lo:[0,0,1] neg_hi:[0,0,1]\n"
  "v_pk_fma_f32 v[238:239], v[238:239], v[86:87], v[86:87] neg_lo:[0,0,1] neg_hi:[0,0,1]\n"
  "v_mfma_f32_16x16x32_bf16 a[248:251], a[156:159], a[128:131], 1.0\n"
  "ds_write_b64 v229, v[186:187] offset:16256\n"
  "v_cvt_pk_bf16_f32 v158, v155, v159\n"
  "v_cvt_pk_bf16_f32 v155, v162, v166\n"
  "v_mfma_f32_16x16x32_bf16 v[200:203], v[56:59], a[132:135], 1.0\n"
  "s_nop 0\n"
  "s_waitcnt lgkmcnt(5)\n"
  "buffer_store_dwordx4 a[252:255], v228, s[52:55], s80 offen offset:0 nt\n"
  "ds_read_b64_tr_b16 a[140:141], v217 offset:13376\n"
  "ds_write_b64 v229, v[188:189] offset:15968\n"
  "v_cvt_pk_bf16_f32 v64, v64, v65\n"
  "v_cvt_pk_bf16_f32 v65, v66, v67\n"
  "v_mfma_f32_16x16x32_bf16 v[204:207], v[56:59], a[128:131], 1.0\n"
  "ds_read_b64_tr_b16 a[142:143], v218 offset:13376\n"
  "ds_read_b64_tr_b16 a[252:253], v230 offset:2240\n"
  "v_mfma_f32_16x16x32_bf16 v[168:171], v[40:43], a[132:135], 1.0\n"
  "ds_read_b64_tr_b16 a[136:137], v217 offset:13392\n"
  "ds_read_b64_tr_b16 a[254:255], v230 offset:2272\n"
  "ds_write_b64 v229, v[190:191] offset:16544\n"
  "v_mfma_f32_16x16x32_bf16 v[172:175], v[40:43], a[128:131], 1.0\n"
  "ds_read_b64_tr_b16 a[138:139], v218 offset:13392\n"
  "ds_write_b64 v229, v[152:153] offset:13440\n"
  "v_cvt_pk_bf16_f32 v157, v161, v165\n"
  "v_mfma_f32_16x16x32_bf16 v[136:139], v[24:27], a[132:135], 1.0\n"
  "v_cvt_pk_bf16_f32 v159, v163, v167\n"
  "v_cvt_pk_bf16_f32 v120, v120, v124\n"
  "v_mfma_f32_16x16x32_bf16 v[140:143], v[24:27], a[128:131], 1.0\n"
  "ds_write_b64 v229, v[154:155] offset:14016\n"
  "v_cvt_pk_bf16_f32 v124, v121, v125\n"
  "v_cvt_pk_bf16_f32 v121, v128, v132\n"
  "v_mfma_f32_16x16x32_bf16 v[104:107], v[8:11], a[132:135], 1.0\n"
  "ds_write_b64 v231, v[64:65] offset:2048\n"
  "v_cvt_pk_bf16_f32 v122, v122, v126\n"
  "v_cvt_pk_bf16_f32 v126, v123, v127\n"
  "v_mfma_f32_16x16x32_bf16 v[108:111], v[8:11], a[128:131], 1.0\n"
  "s_waitcnt lgkmcnt(15)\n"
  "v_cvt_pk_bf16_f32 v123, v130, v134\n"
  "v_cvt_pk_bf16_f32 v125, v129, v133\n"
  "v_mfma_f32_16x16x32_bf16 v[112:115], v[8:11], a[124:127], 1.0\n"
  "s_nop 0\n"
  "ds_write_b64 v229, v[156:157] offset:13728\n"
  "v_cvt_pk_bf16_f32 v127, v131, v135\n"
  "v_cvt_pk_bf16_f32 v88, v88, v92\n"
  "v_mfma_f32_16x16x32_bf16 v[116:119], v[8:11], a[120:123], 1.0\n"
  "ds_write_b64 v229, v[158:159] offset:14304\n"
  "v_cvt_pk_bf16_f32 v92, v89, v93\n"
  "v_cvt_pk_bf16_f32 v89, v96, v100\n"
  "v_mfma_f32_16x16x32_bf16 v[144:147], v[24:27], a[124:127], 1.0\n"
  "s_nop 0\n"
  "s_waitcnt lgkmcnt(15)\n"
  "buffer_store_dwordx4 v[244:247], v228, s[52:55], s80 offen offset:3072 nt\n"
  "ds_read_b64_tr_b16 a[132:133], v219 offset:13376\n"
  "ds_write_b64 v229, v[120:121] offset:11200\n"
  "v_cvt_pk_bf16_f32 v90, v90, v94\n"
  "v_cvt_pk_bf16_f32 v94, v91, v95\n"
  "v_mfma_f32_16x16x32_bf16 v[148:151], v[24:27], a[120:123], 1.0\n"
  "ds_read_b64_tr_b16 a[134:135], v220 offset:13376\n"
  "ds_read_b64_tr_b16 v[244:245], v230 offset:3360\n"
  "v_mfma_f32_16x16x32_bf16 v[176:179], v[40:43], a[124:127], 1.0\n"
  "ds_read_b64_tr_b16 a[128:129], v219 offset:13392\n"
  "ds_read_b64_tr_b16 v[246:247], v230 offset:3392\n"
  "ds_write_b64 v229, v[122:123] offset:11776\n"
  "v_mfma_f32_16x16x32_bf16 v[180:183], v[40:43], a[120:123], 1.0\n"
  "ds_read_b64_tr_b16 a[130:131], v220 offset:13392\n"
  "ds_write_b64 v229, v[124:125] offset:11488\n"
  "v_cvt_pk_bf16_f32 v91, v98, v102\n"
  "v_mfma_f32_16x16x32_bf16 v[208:211], v[56:59], a[124:127], 1.0\n"
  "s_nop 0\n"
  "ds_write_b64 v229, v[126:127] offset:12064\n"
  "v_mfma_f32_16x16x32_bf16 v[212:215], v[56:59], a[120:123], 1.0\n"
  "s_waitcnt lgkmcnt(15)\n"
  "ds_write_b64 v229, v[88:89] offset:8960\n"
  "v_cvt_pk_bf16_f32 v93, v97, v101\n"
  "v_cvt_pk_bf16_f32 v95, v99, v103\n"
  "v_mfma_f32_16x16x32_bf16 a[236:239], a[156:159], a[140:143], 1.0\n"
  "ds_write_b64 v229, v[90:91] offset:9536\n"
  "v_fma_f32 v0, v0, s73, v223\n"
  "v_fma_f32 v1, v1, s73, v223\n"
  "v_mfma_f32_16x16x32_bf16 a[240:243], a[156:159], a[136:139], 1.0\n"
  "ds_write_b64 v229, v[92:93] offset:9248\n"
  "v_mfma_f32_16x16x32_bf16 v[184:187], v[56:59], a[140:143], 1.0\n"
  "ds_write_b64 v229, v[94:95] offset:9824\n"
  "v_exp_f32_e64 v0, v0\n"
  "v_mfma_f32_16x16x32_bf16 v[188:191], v[56:59], a[136:139], 1.0\n"
  "buffer_store_dwordx4 a[252:255], v228, s[52:55], s81 offen offset:0 nt\n"
  "v_exp_f32_e64 v1, v1\n"
  "v_fma_f32 v2, v2, s73, v223\n"
  "v_fma_f32 v3, v3, s73, v223\n"
  "v_cvt_pk_bf16_f32 v240, v240, v242\n"
  "v_cvt_pk_bf16_f32 v241, v241, v243\n"
  "v_mfma_f32_16x16x32_bf16 v[152:155], v[40:43], a[140:143], 1.0\n"
  "s_mov_b32 m0, s67\n"
  "s_waitcnt lgkmcnt(8)\n"
  "buffer_store_dwordx4 v[244:247], v228, s[52:55], s81 offen offset:3072 nt\n"
  "ds_write_b32 v233, v240 offset:19040\n"
  "v_exp_f32 v2, v2\n"
  "v_exp_f32 v3, v3\n"
  "v_mfma_f32_16x16x32_bf16 v[156:159], v[40:43], a[136:139], 1.0\n"
  "s_waitcnt lgkmcnt(7)\n"
  "s_barrier\n"
  "ds_read_b128 a[124:127], v216 offset:0\n"
  "v_mfma_f32_16x16x32_bf16 v[120:123], v[24:27], a[140:143], 1.0\n"
  "ds_read_b64 a[160:161], v231 offset:2048\n"
  "ds_write_b32 v233, v241 offset:19328\n"
  "v_cvt_pk_bf16_f32 v12, v0, v1\n"
  "v_cvt_pk_bf16_f32 v236, v236, v238\n"
  "v_mfma_f32_16x16x32_bf16 v[124:127], v[24:27], a[136:139], 1.0\n"
  "ds_read_b64 a[162:163], v232 offset:2048\n"
  "ds_write_b32 v233, v236 offset:19616\n"
  "v_cvt_pk_bf16_f32 v13, v2, v3\n"
  "v_cvt_pk_bf16_f32 v237, v237, v239\n"
  "v_mfma_f32_16x16x32_bf16 v[88:91], v[8:11], a[140:143], 1.0\n"
  "buffer_load_dwordx4 v221, s[40:43], 0 offen offset:0 lds\n"
  "v_mfma_f32_16x16x32_bf16 v[92:95], v[8:11], a[136:139], 1.0\n"
  "s_nop 0\n"
  "ds_read_b128 a[120:123], v216 offset:64\n"
  "v_fma_f32 v16, v16, s73, v224\n"
  "v_mfma_f32_16x16x32_bf16 v[96:99], v[8:11], a[132:135], 1.0\n"
  "s_addk_i32 m0, 896\n"
  "ds_read_b128 a[140:143], v216 offset:1024\n"
  "ds_write_b32 v233, v237 offset:19904\n"
  "v_fma_f32 v17, v17, s73, v224\n"
  "v_accvgpr_read_b32 v240, a196\n"
  "v_mfma_f32_16x16x32_bf16 v[100:103], v[8:11], a[128:131], 1.0\n"
  "ds_read_b128 a[136:139], v216 offset:1088\n"
  "v_fma_f32 v18, v18, s73, v224\n"
  "v_accvgpr_read_b32 v241, a197\n"
  "v_accvgpr_read_b32 v242, a200\n"
  "v_accvgpr_read_b32 v243, a201\n"
  "v_pk_fma_f32 v[240:241], v[240:241], v[76:77], v[76:77] neg_lo:[0,0,1] neg_hi:[0,0,1]\n"
  "v_pk_fma_f32 v[242:243], v[242:243], v[76:77], v[76:77] neg_lo:[0,0,1] neg_hi:[0,0,1]\n"
  "v_mfma_f32_16x16x32_bf16 v[128:131], v[24:27], a[132:135], 1.0\n"
  "buffer_load_dwordx4 v221, s[40:43], 0 offen offset:128 lds\n"
  "v_fma_f32 v19, v19, s73, v224\n"
  "v_mfma_f32_16x16x32_bf16 v[132:135], v[24:27], a[128:131], 1.0\n"
  "s_addk_i32 m0, 896\n"
  "ds_read_b128 a[144:147], v216 offset:2048\n"
  "v_fma_f32 v32, v32, s73, v225\n"
  "v_mfma_f32_16x16x32_bf16 v[160:163], v[40:43], a[132:135], 1.0\n"
  "s_nop 0\n"
  "ds_read_b128 a[148:151], v216 offset:2112\n"
  "v_fma_f32 v33, v33, s73, v225\n"
  "v_accvgpr_read_b32 v236, a204\n"
  "v_accvgpr_read_b32 v237, a205\n"
  "v_accvgpr_read_b32 v238, a208\n"
  "v_pk_fma_f32 v[236:237], v[236:237], v[76:77], v[76:77] neg_lo:[0,0,1] neg_hi:[0,0,1]\n"
  "v_mfma_f32_16x16x32_bf16 v[164:167], v[40:43], a[128:131], 1.0\n"
  "v_exp_f32_e64 v16, v16\n"
  "v_accvgpr_read_b32 v239, a209\n"
  "v_cvt_pk_bf16_f32 v240, v240, v242\n"
  "v_cvt_pk_bf16_f32 v242, v241, v243\n"
  "v_pk_fma_f32 v[238:239], v[238:239], v[76:77], v[76:77] neg_lo:[0,0,1] neg_hi:[0,0,1]\n"
  "v_mfma_f32_16x16x32_bf16 v[192:195], v[56:59], a[132:135], 1.0\n"
  "buffer_load_dwordx4 v221, s[40:43], 0 offen offset:256 lds\n"
  "v_fma_f32 v34, v34, s73, v225\n"
  "v_mfma_f32_16x16x32_bf16 v[196:199], v[56:59], a[128:131], 1.0\n"
  "s_waitcnt lgkmcnt(5)\n"
  "v_exp_f32 v17, v17\n"
  "v_cvt_pk_bf16_f32 v241, v236, v238\n"
  "v_cvt_pk_bf16_f32 v243, v237, v239\n"
  "v_accvgpr_read_b32 v236, a198\n"
  "v_mfma_f32_16x16x32_bf16 v[64:67], a[124:127], a[96:99], v[248:251]\n"
  "s_add_u32 m0, s59, -128\n"
  "v_exp_f32 v18, v18\n"
  "v_mfma_f32_16x16x32_bf16 v[64:67], a[120:123], a[100:103], v[64:67]\n"
  "s_waitcnt lgkmcnt(2)\n"
  "v_exp_f32 v19, v19\n"
  "v_mfma_f32_16x16x32_bf16 v[64:67], a[140:143], a[104:107], v[64:67]\n"
  "buffer_load_dwordx4 v221, s[32:35], 0 offen offset:128 lds\n"
  "v_cvt_pk_bf16_f32 v28, v16, v17\n"
  "v_mfma_f32_16x16x32_bf16 v[64:67], a[136:139], a[108:111], v[64:67]\n"
  "s_waitcnt lgkmcnt(0)\n"
  "v_exp_f32 v32, v32\n"
  "v_mfma_f32_16x16x32_bf16 v[64:67], a[144:147], a[112:115], v[64:67]\n"
  "s_addk_i32 m0, 1024\n"
  "v_exp_f32 v33, v33\n"
  "v_mfma_f32_16x16x32_bf16 v[64:67], a[148:151], a[116:119], v[64:67]\n"
  "s_mov_b64 s[32:33], s[44:45]\n"
  "v_exp_f32 v34, v34\n"
  "v_mfma_f32_16x16x32_bf16 v[0:3], a[148:151], a[20:23], v[248:251]\n"
  "buffer_load_dwordx4 v221, s[44:47], 0 offen offset:128 lds\n"
  "v_cvt_pk_bf16_f32 v29, v18, v19\n"
  "v_mfma_f32_16x16x32_bf16 v[0:3], a[144:147], a[16:19], v[0:3]\n"
  "s_addk_i32 m0, 896\n"
  "v_fma_f32 v35, v35, s73, v225\n"
  "v_cvt_pk_bf16_f32 v44, v32, v33\n"
  "v_mfma_f32_16x16x32_bf16 v[0:3], a[136:139], a[12:15], v[0:3]\n"
  "s_add_u32 s40, s40, s31\n"
  "v_exp_f32 v35, v35\n"
  "v_mfma_f32_16x16x32_bf16 v[0:3], a[140:143], a[8:11], v[0:3]\n"
  "s_addc_u32 s41, s41, 0\n"
  "v_cvt_pk_bf16_f32 v45, v34, v35\n"
  "v_fma_f32 v48, v48, s73, v226\n"
  "v_mfma_f32_16x16x32_bf16 v[0:3], a[120:123], a[4:7], v[0:3]\n"
  "buffer_load_dwordx4 v221, s[44:47], 0 offen offset:256 lds\n"
  "v_fma_f32 v49, v49, s73, v226\n"
  "v_mfma_f32_16x16x32_bf16 v[0:3], a[124:127], a[0:3], v[0:3]\n"
  "s_add_u32 s44, s44, s31\n"
  "v_exp_f32_e64 v48, v48\n"
  "v_accvgpr_read_b32 v237, a199\n"
  "v_accvgpr_read_b32 v238, a202\n"
  "v_accvgpr_read_b32 v239, a203\n"
  "v_pk_fma_f32 v[236:237], v[236:237], v[78:79], v[78:79] neg_lo:[0,0,1] neg_hi:[0,0,1]\n"
  "v_pk_fma_f32 v[238:239], v[238:239], v[78:79], v[78:79] neg_lo:[0,0,1] neg_hi:[0,0,1]\n"
  "v_mfma_f32_16x16x32_bf16 v[16:19], a[124:127], a[24:27], v[248:251]\n"
  "s_addc_u32 s45, s45, 0\n"
  "v_exp_f32 v49, v49\n"
  "v_mfma_f32_16x16x32_bf16 v[16:19], a[120:123], a[28:31], v[16:19]\n"
  "v_fma_f32 v50, v50, s73, v226\n"
  "v_fma_f32 v51, v51, s73, v226\n"
  "v_mfma_f32_16x16x32_bf16 v[16:19], a[140:143], a[32:35], v[16:19]\n"
  "ds_read_b128 a[132:135], v235 offset:0\n"
  "v_cvt_pk_bf16_f32 v60, v48, v49\n"
  "v_mfma_f32_16x16x32_bf16 v[16:19], a[136:139], a[36:39], v[16:19]\n"
  "ds_read_b128 a[128:131], v235 offset:64\n"
  "v_fma_f32 v4, v4, s73, v223\n"
  "v_mfma_f32_16x16x32_bf16 v[16:19], a[144:147], a[40:43], v[16:19]\n"
  "v_exp_f32 v50, v50\n"
  "v_mfma_f32_16x16x32_bf16 v[16:19], a[148:151], a[44:47], v[16:19]\n"
  "v_exp_f32 v51, v51\n"
  "v_mfma_f32_16x16x32_bf16 v[32:35], a[148:151], a[68:71], v[248:251]\n"
  "v_exp_f32 v4, v4\n"
  "v_mfma_f32_16x16x32_bf16 v[32:35], a[144:147], a[64:67], v[32:35]\n"
  "v_cvt_pk_bf16_f32 v61, v50, v51\n"
  "v_fma_f32 v5, v5, s73, v223\n"
  "v_mfma_f32_16x16x32_bf16 v[32:35], a[136:139], a[60:63], v[32:35]\n"
  "v_exp_f32 v5, v5\n"
  "v_mfma_f32_16x16x32_bf16 v[32:35], a[140:143], a[56:59], v[32:35]\n"
  "v_fma_f32 v6, v6, s73, v223\n"
  "v_fma_f32 v7, v7, s73, v223\n"
  "v_mfma_f32_16x16x32_bf16 v[32:35], a[120:123], a[52:55], v[32:35]\n"
  "v_exp_f32 v6, v6\n"
  "v_mfma_f32_16x16x32_bf16 v[32:35], a[124:127], a[48:51], v[32:35]\n"
  "v_exp_f32 v7, v7\n"
  "v_cvt_pk_bf16_f32 v236, v236, v238\n"
  "v_cvt_pk_bf16_f32 v238, v237, v239\n"
  "v_mfma_f32_16x16x32_bf16 v[48:51], a[124:127], a[72:75], v[248:251]\n"
  "ds_read_b128 a[124:127], v235 offset:1024\n"
  "v_cvt_pk_bf16_f32 v14, v4, v5\n"
  "v_mfma_f32_16x16x32_bf16 v[48:51], a[120:123], a[76:79], v[48:51]\n"
  "ds_read_b128 a[120:123], v235 offset:1088\n"
  "v_cvt_pk_bf16_f32 v15, v6, v7\n"
  "v_mfma_f32_16x16x32_bf16 v[48:51], a[140:143], a[80:83], v[48:51]\n"
  "ds_read_b128 a[140:143], v235 offset:2048\n"
  "v_fma_f32 v20, v20, s73, v224\n"
  "v_mfma_f32_16x16x32_bf16 v[48:51], a[136:139], a[84:87], v[48:51]\n"
  "ds_read_b128 a[136:139], v235 offset:2112\n"
  "v_fma_f32 v21, v21, s73, v224\n"
  "v_mfma_f32_16x16x32_bf16 v[48:51], a[144:147], a[88:91], v[48:51]\n"
  "v_exp_f32 v20, v20\n"
  "v_mfma_f32_16x16x32_bf16 v[48:51], a[148:151], a[92:95], v[48:51]\n"
  "s_waitcnt lgkmcnt(4)\n"
  "v_exp_f32 v21, v21\n"
  "v_mfma_f32_16x16x32_bf16 v[4:7], a[132:135], a[0:3], v[248:251]\n"
  "v_fma_f32 v22, v22, s73, v224\n"
  "v_fma_f32 v23, v23, s73, v224\n"
  "v_mfma_f32_16x16x32_bf16 v[4:7], a[128:131], a[4:7], v[4:7]\n"
  "s_waitcnt lgkmcnt(2)\n"
  "v_exp_f32 v22, v22\n"
  "v_mfma_f32_16x16x32_bf16 v[4:7], a[124:127], a[8:11], v[4:7]\n"
  "v_exp_f32 v23, v23\n"
  "v_mfma_f32_16x16x32_bf16 v[4:7], a[120:123], a[12:15], v[4:7]\n"
  "s_waitcnt lgkmcnt(0)\n"
  "v_cvt_pk_bf16_f32 v30, v20, v21\n"
  "v_cvt_pk_bf16_f32 v31, v22, v23\n"
  "v_mfma_f32_16x16x32_bf16 v[4:7], a[140:143], a[16:19], v[4:7]\n"
  "v_fma_f32 v36, v36, s73, v225\n"
  "v_fma_f32 v37, v37, s73, v225\n"
  "v_mfma_f32_16x16x32_bf16 v[4:7], a[136:139], a[20:23], v[4:7]\n"
  "v_exp_f32 v36, v36\n"
  "v_exp_f32_e64 v37, v37\n"
  "v_mfma_f32_16x16x32_bf16 v[20:23], a[136:139], a[44:47], v[248:251]\n"
  "v_fma_f32 v38, v38, s73, v225\n"
  "v_fma_f32 v39, v39, s73, v225\n"
  "v_mfma_f32_16x16x32_bf16 v[20:23], a[140:143], a[40:43], v[20:23]\n"
  "v_exp_f32_e64 v38, v38\n"
  "v_mfma_f32_16x16x32_bf16 v[20:23], a[120:123], a[36:39], v[20:23]\n"
  "ds_read_b64_tr_b16 a[144:145], v217 offset:2048\n"
  "v_cvt_pk_bf16_f32 v46, v36, v37\n"
  "v_mfma_f32_16x16x32_bf16 v[20:23], a[124:127], a[32:35], v[20:23]\n"
  "ds_read_b64_tr_b16 a[146:147], v218 offset:2048\n"
  "v_fma_f32 v52, v52, s73, v226\n"
  "v_mfma_f32_16x16x32_bf16 v[20:23], a[128:131], a[28:31], v[20:23]\n"
  "ds_read_b64_tr_b16 a[148:149], v217 offset:2064\n"
  "v_exp_f32_e64 v39, v39\n"
  "v_mfma_f32_16x16x32_bf16 v[20:23], a[132:135], a[24:27], v[20:23]\n"
  "ds_read_b64_tr_b16 a[150:151], v218 offset:2064\n"
  "ds_write_b64 v229, v[240:241] offset:4480\n"
  "v_cvt_pk_bf16_f32 v47, v38, v39\n"
  "v_exp_f32_e64 v52, v52\n"
  "v_mfma_f32_16x16x32_bf16 v[36:39], a[132:135], a[48:51], v[248:251]\n"
  "v_fma_f32 v53, v53, s73, v226\n"
  "v_fma_f32 v54, v54, s73, v226\n"
  "v_mfma_f32_16x16x32_bf16 v[36:39], a[128:131], a[52:55], v[36:39]\n"
  "v_exp_f32_e64 v53, v53\n"
  "v_mfma_f32_16x16x32_bf16 v[36:39], a[124:127], a[56:59], v[36:39]\n"
  "v_exp_f32 v54, v54\n"
  "v_mfma_f32_16x16x32_bf16 v[36:39], a[120:123], a[60:63], v[36:39]\n"
  "v_fma_f32 v55, v55, s73, v226\n"
  "v_cvt_pk_bf16_f32 v62, v52, v53\n"
  "v_mfma_f32_16x16x32_bf16 v[36:39], a[140:143], a[64:67], v[36:39]\n"
  "v_exp_f32_e64 v55, v55\n"
  "v_mfma_f32_16x16x32_bf16 v[36:39], a[136:139], a[68:71], v[36:39]\n"
  "v_cvt_pk_bf16_f32 v63, v54, v55\n"
  "v_accvgpr_read_b32 v240, a206\n"
  "v_mfma_f32_16x16x32_bf16 v[52:55], a[136:139], a[92:95], v[248:251]\n"
  "ds_read_b64_tr_b16 a[136:137], v219 offset:2048\n"
  "v_accvgpr_read_b32 v241, a207\n"
  "v_mfma_f32_16x16x32_bf16 v[52:55], a[140:143], a[88:91], v[52:55]\n"
  "ds_read_b64_tr_b16 a[138:139], v220 offset:2048\n"
  "v_fma_f32 v64, v64, s73, v227\n"
  "v_mfma_f32_16x16x32_bf16 v[52:55], a[120:123], a[84:87], v[52:55]\n"
  "ds_read_b64_tr_b16 a[140:141], v219 offset:2064\n"
  "v_fma_f32 v65, v65, s73, v227\n"
  "v_mfma_f32_16x16x32_bf16 v[52:55], a[124:127], a[80:83], v[52:55]\n"
  "ds_read_b64_tr_b16 a[142:143], v220 offset:2064\n"
  "v_fma_f32 v66, v66, s73, v227\n"
  "v_mfma_f32_16x16x32_bf16 v[52:55], a[128:131], a[76:79], v[52:55]\n"
  "v_exp_f32_e64 v64, v64\n"
  "v_mfma_f32_16x16x32_bf16 v[52:55], a[132:135], a[72:75], v[52:55]\n"
  "s_waitcnt lgkmcnt(5)\n"
  "ds_write_b64 v229, v[242:243] offset:4768\n"
  "v_exp_f32_e64 v65, v65\n"
  "v_pk_fma_f32 v[240:241], v[240:241], v[78:79], v[78:79] neg_lo:[0,0,1] neg_hi:[0,0,1]\n"
  "v_mfma_f32_16x16x32_bf16 v[104:107], v[12:15], a[144:147], v[104:107]\n"
  "v_accvgpr_read_b32 v242, a210\n"
  "v_accvgpr_read_b32 v243, a211\n"
  "v_exp_f32_e64 v66, v66\n"
  "v_pk_fma_f32 v[242:243], v[242:243], v[78:79], v[78:79] neg_lo:[0,0,1] neg_hi:[0,0,1]\n"
  "v_mfma_f32_16x16x32_bf16 v[108:111], v[12:15], a[148:151], v[108:111]\n"
  "v_cvt_pk_bf16_f32 v237, v240, v242\n"
  "v_cvt_pk_bf16_f32 v239, v241, v243\n"
  "v_mfma_f32_16x16x32_bf16 v[136:139], v[28:31], a[144:147], v[136:139]\n"
  "ds_read_b64_tr_b16 a[120:121], v217 offset:0\n"
  "ds_write_b64 v229, v[236:237] offset:5056\n"
  "v_accvgpr_read_b32 v240, a212\n"
  "v_mfma_f32_16x16x32_bf16 v[140:143], v[28:31], a[148:151], v[140:143]\n"
  "ds_read_b64_tr_b16 a[122:123], v218 offset:0\n"
  "v_accvgpr_read_b32 v241, a213\n"
  "v_mfma_f32_16x16x32_bf16 v[168:171], v[44:47], a[144:147], v[168:171]\n"
  "ds_read_b64_tr_b16 a[124:125], v217 offset:1024\n"
  "ds_write_b64 v229, v[238:239] offset:5344\n"
  "v_accvgpr_read_b32 v242, a216\n"
  "v_pk_fma_f32 v[240:241], v[240:241], v[80:81], v[80:81] neg_lo:[0,0,1] neg_hi:[0,0,1]\n"
  "v_mfma_f32_16x16x32_bf16 v[172:175], v[44:47], a[148:151], v[172:175]\n"
  "ds_read_b64_tr_b16 a[126:127], v218 offset:1024\n"
  "v_accvgpr_read_b32 v243, a217\n"
  "v_accvgpr_read_b32 v236, a220\n"
  "v_accvgpr_read_b32 v237, a221\n"
  "v_accvgpr_read_b32 v238, a224\n"
  "v_pk_fma_f32 v[242:243], v[242:243], v[80:81], v[80:81] neg_lo:[0,0,1] neg_hi:[0,0,1]\n"
  "v_pk_fma_f32 v[236:237], v[236:237], v[80:81], v[80:81] neg_lo:[0,0,1] neg_hi:[0,0,1]\n"
  "v_mfma_f32_16x16x32_bf16 v[200:203], v[60:63], a[144:147], v[200:203]\n"
  "v_accvgpr_read_b32 v239, a225\n"
  "v_cvt_pk_bf16_f32 v240, v240, v242\n"
  "v_mfma_f32_16x16x32_bf16 v[204:207], v[60:63], a[148:151], v[204:207]\n"
  "ds_read_b64_tr_b16 a[252:253], v230 offset:4480\n"
  "ds_read_b64_tr_b16 a[254:255], v230 offset:4512\n"
  "v_cvt_pk_bf16_f32 v242, v241, v243\n"
  "v_fma_f32 v67, v67, s73, v227\n"
  "v_cvt_pk_bf16_f32 v64, v64, v65\n"
  "v_pk_fma_f32 v[238:239], v[238:239], v[80:81], v[80:81] neg_lo:[0,0,1] neg_hi:[0,0,1]\n"
  "v_mfma_f32_16x16x32_bf16 a[244:247], a[160:163], a[144:147], a[244:247]\n"
  "s_nop 0\n"
  "v_cvt_pk_bf16_f32 v241, v236, v238\n"
  "v_cvt_pk_bf16_f32 v243, v237, v239\n"
  "v_mfma_f32_16x16x32_bf16 a[248:251], a[160:163], a[148:151], a[248:251]\n"
  "s_waitcnt lgkmcnt(9)\n"
  "ds_write_b64 v229, v[240:241] offset:6720\n"
  "v_accvgpr_read_b32 v236, a214\n"
  "v_accvgpr_read_b32 v237, a215\n"
  "v_pk_fma_f32 v[236:237], v[236:237], v[82:83], v[82:83] neg_lo:[0,0,1] neg_hi:[0,0,1]\n"
  "v_mfma_f32_16x16x32_bf16 v[208:211], v[60:63], a[136:139], v[208:211]\n"
  "ds_write_b64 v229, v[242:243] offset:7008\n"
  "v_accvgpr_read_b32 v238, a218\n"
  "v_accvgpr_read_b32 v239, a219\n"
  "v_pk_fma_f32 v[238:239], v[238:239], v[82:83], v[82:83] neg_lo:[0,0,1] neg_hi:[0,0,1]\n"
  "v_mfma_f32_16x16x32_bf16 v[212:215], v[60:63], a[140:143], v[212:215]\n"
  "v_accvgpr_read_b32 v240, a222\n"
  "v_accvgpr_read_b32 v241, a223\n"
  "v_mfma_f32_16x16x32_bf16 v[176:179], v[44:47], a[136:139], v[176:179]\n"
  "ds_read_b64_tr_b16 a[128:129], v217 offset:16\n"
  "v_accvgpr_read_b32 v242, a226\n"
  "v_accvgpr_read_b32 v243, a227\n"
  "v_cvt_pk_bf16_f32 v236, v236, v238\n"
  "v_cvt_pk_bf16_f32 v238, v237, v239\n"
  "v_pk_fma_f32 v[240:241], v[240:241], v[82:83], v[82:83] neg_lo:[0,0,1] neg_hi:[0,0,1]\n"
  "v_pk_fma_f32 v[242:243], v[242:243], v[82:83], v[82:83] neg_lo:[0,0,1] neg_hi:[0,0,1]\n"
  "v_mfma_f32_16x16x32_bf16 v[180:183], v[44:47], a[140:143], v[180:183]\n"
  "ds_read_b64_tr_b16 a[130:131], v218 offset:16\n"
  "v_cvt_pk_bf16_f32 v237, v240, v242\n"
  "v_mfma_f32_16x16x32_bf16 v[144:147], v[28:31], a[136:139], v[144:147]\n"
  "ds_read_b64_tr_b16 a[132:133], v217 offset:1040\n"
  "ds_write_b64 v229, v[236:237] offset:7296\n"
  "v_cvt_pk_bf16_f32 v239, v241, v243\n"
  "v_mfma_f32_16x16x32_bf16 v[148:151], v[28:31], a[140:143], v[148:151]\n"
  "ds_read_b64_tr_b16 a[134:135], v218 offset:1040\n"
  "ds_write_b64 v229, v[238:239] offset:7584\n"
  "v_accvgpr_read_b32 v240, a228\n"
  "v_mfma_f32_16x16x32_bf16 v[112:115], v[12:15], a[136:139], v[112:115]\n"
  "v_exp_f32 v67, v67\n"
  "v_mfma_f32_16x16x32_bf16 v[116:119], v[12:15], a[140:143], v[116:119]\n"
  "s_waitcnt lgkmcnt(10)\n"
  "v_accvgpr_read_b32 v241, a229\n"
  "v_accvgpr_read_b32 v236, a230\n"
  "v_accvgpr_read_b32 v237, a231\n"
  "v_accvgpr_read_b32 v242, a232\n"
  "v_accvgpr_read_b32 v243, a233\n"
  "v_pk_fma_f32 v[240:241], v[240:241], v[84:85], v[84:85] neg_lo:[0,0,1] neg_hi:[0,0,1]\n"
  "v_pk_fma_f32 v[242:243], v[242:243], v[84:85], v[84:85] neg_lo:[0,0,1] neg_hi:[0,0,1]\n"
  "v_pk_fma_f32 v[236:237], v[236:237], v[86:87], v[86:87] neg_lo:[0,0,1] neg_hi:[0,0,1]\n"
  "v_mfma_f32_16x16x32_bf16 a[164:167], v[8:11], a[120:123], 1.0\n"
  "v_accvgpr_read_b32 v238, a234\n"
  "v_accvgpr_read_b32 v239, a235\n"
  "v_mfma_f32_16x16x32_bf16 a[164:167], v[12:15], a[124:127], a[164:167]\n"
  "v_cvt_pk_bf16_f32 v240, v240, v242\n"
  "v_cvt_pk_bf16_f32 v241, v241, v243\n"
  "v_mfma_f32_16x16x32_bf16 a[180:183], v[24:27], a[120:123], 1.0\n"
  "ds_write_b32 v233, v240 offset:17920\n"
  "v_mfma_f32_16x16x32_bf16 a[180:183], v[28:31], a[124:127], a[180:183]\n"
  "ds_read_b64_tr_b16 v[244:245], v230 offset:5600\n"
  "ds_write_b32 v233, v241 offset:18208\n"
  "v_cvt_pk_bf16_f32 v65, v66, v67\n"
  "v_fma_f32 v0, v0, s73, v223\n"
  "v_pk_fma_f32 v[238:239], v[238:239], v[86:87], v[86:87] neg_lo:[0,0,1] neg_hi:[0,0,1]\n"
  "v_mfma_f32_16x16x32_bf16 a[196:199], v[40:43], a[120:123], 1.0\n"
  "s_mov_b32 m0, s68\n"
  "ds_read_b64_tr_b16 a[136:137], v219 offset:0\n"
  "v_cvt_pk_bf16_f32 v236, v236, v238\n"
  "v_mfma_f32_16x16x32_bf16 a[196:199], v[44:47], a[124:127], a[196:199]\n"
  "s_nop 0\n"
  "ds_read_b64_tr_b16 a[138:139], v220 offset:0\n"
  "ds_write_b32 v233, v236 offset:18496\n"
  "v_cvt_pk_bf16_f32 v237, v237, v239\n"
  "v_fma_f32 v1, v1, s73, v223\n"
  "v_mfma_f32_16x16x32_bf16 a[212:215], v[56:59], a[120:123], 1.0\n"
  "ds_read_b64_tr_b16 a[140:141], v219 offset:1024\n"
  "v_fma_f32 v2, v2, s73, v223\n"
  "v_mfma_f32_16x16x32_bf16 a[212:215], v[60:63], a[124:127], a[212:215]\n"
  "ds_read_b64_tr_b16 a[142:143], v220 offset:1024\n"
  "v_fma_f32 v3, v3, s73, v223\n"
  "v_mfma_f32_16x16x32_bf16 a[228:231], a[156:159], a[120:123], 1.0\n"
  "buffer_load_dwordx4 v221, s[40:43], 0 offen offset:0 lds\n"
  "v_mfma_f32_16x16x32_bf16 a[228:231], a[160:163], a[124:127], a[228:231]\n"
  "s_waitcnt lgkmcnt(9)\n"
  "ds_write_b32 v233, v237 offset:18784\n"
  "v_mfma_f32_16x16x32_bf16 a[232:235], a[156:159], a[128:131], 1.0\n"
  "s_addk_i32 m0, 896\n"
  "v_exp_f32_e64 v0, v0\n"
  "v_mfma_f32_16x16x32_bf16 a[232:235], a[160:163], a[132:135], a[232:235]\n"
  "ds_write_b64 v231, v[64:65] offset:0\n"
  "v_mfma_f32_16x16x32_bf16 a[216:219], v[56:59], a[128:131], 1.0\n"
  "buffer_load_dwordx4 v221, s[40:43], 0 offen offset:128 lds\n"
  "v_fma_f32 v16, v16, s73, v224\n"
  "v_mfma_f32_16x16x32_bf16 a[216:219], v[60:63], a[132:135], a[216:219]\n"
  "s_addk_i32 m0, 896\n"
  "ds_read_b64_tr_b16 a[120:121], v219 offset:16\n"
  "v_fma_f32 v17, v17, s73, v224\n"
  "v_mfma_f32_16x16x32_bf16 a[200:203], v[40:43], a[128:131], 1.0\n"
  "ds_read_b64_tr_b16 a[122:123], v220 offset:16\n"
  "v_fma_f32 v18, v18, s73, v224\n"
  "v_mfma_f32_16x16x32_bf16 a[200:203], v[44:47], a[132:135], a[200:203]\n"
  "ds_read_b64_tr_b16 a[124:125], v219 offset:1040\n"
  "v_fma_f32 v19, v19, s73, v224\n"
  "v_mfma_f32_16x16x32_bf16 a[184:187], v[24:27], a[128:131], 1.0\n"
  "buffer_load_dwordx4 v221, s[40:43], 0 offen offset:256 lds\n"
  "v_fma_f32 v32, v32, s73, v225\n"
  "v_mfma_f32_16x16x32_bf16 a[184:187], v[28:31], a[132:135], a[184:187]\n"
  "s_mov_b32 m0, s60\n"
  "ds_read_b64_tr_b16 a[126:127], v220 offset:1040\n"
  "v_fma_f32 v33, v33, s73, v225\n"
  "v_mfma_f32_16x16x32_bf16 a[168:171], v[8:11], a[128:131], 1.0\n"
  "s_add_u32 s28, s96, 184448\n"
  "v_exp_f32 v1, v1\n"
  "v_mfma_f32_16x16x32_bf16 a[168:171], v[12:15], a[132:135], a[168:171]\n"
  "s_waitcnt lgkmcnt(6)\n"
  "v_exp_f32 v2, v2\n"
  "v_mfma_f32_16x16x32_bf16 a[172:175], v[8:11], a[136:139], 1.0\n"
  "buffer_load_dwordx4 v221, s[32:35], 0 offen offset:0 lds\n"
  "v_add_f32 v252, v0, v1\n"
  "v_mfma_f32_16x16x32_bf16 a[172:175], v[12:15], a[140:143], a[172:175]\n"
  "s_addk_i32 m0, 1024\n"
  "v_exp_f32 v3, v3\n"
  "v_mfma_f32_16x16x32_bf16 a[188:191], v[24:27], a[136:139], 1.0\n"
  "s_add_u32 s40, s40, s31\n"
  "v_exp_f32 v16, v16\n"
  "v_mfma_f32_16x16x32_bf16 a[188:191], v[28:31], a[140:143], a[188:191]\n"
  "s_addc_u32 s41, s41, 0\n"
  "v_exp_f32 v17, v17\n"
  "v_mfma_f32_16x16x32_bf16 a[204:207], v[40:43], a[136:139], 1.0\n"
  "buffer_load_dwordx4 v221, s[44:47], 0 offen offset:0 lds\n"
  "v_add_f32 v252, v252, v2\n"
  "v_mfma_f32_16x16x32_bf16 a[204:207], v[44:47], a[140:143], a[204:207]\n"
  "s_addk_i32 m0, 768\n"
  "v_exp_f32 v18, v18\n"
  "v_mfma_f32_16x16x32_bf16 a[220:223], v[56:59], a[136:139], 1.0\n"
  "s_mov_b64 s[32:33], s[44:45]\n"
  "v_exp_f32 v19, v19\n"
  "v_mfma_f32_16x16x32_bf16 a[220:223], v[60:63], a[140:143], a[220:223]\n"
  "s_waitcnt lgkmcnt(0)\n"
  "v_exp_f32 v32, v32\n"
  "v_mfma_f32_16x16x32_bf16 a[224:227], v[56:59], a[120:123], 1.0\n"
  "buffer_load_dwordx4 v221, s[44:47], 0 offen offset:256 lds\n"
  "v_add_f32 v252, v252, v3\n"
  "v_mfma_f32_16x16x32_bf16 a[224:227], v[60:63], a[124:127], a[224:227]\n"
  "s_add_u32 s44, s44, s31\n"
  "v_exp_f32 v33, v33\n"
  "v_mfma_f32_16x16x32_bf16 a[208:211], v[40:43], a[120:123], 1.0\n"
  "s_addc_u32 s45, s45, 0\n"
  "v_add_f32 v253, v16, v17\n"
  "v_add_f32 v253, v253, v18\n"
  "v_mfma_f32_16x16x32_bf16 a[208:211], v[44:47], a[124:127], a[208:211]\n"
  "v_add_f32 v253, v253, v19\n"
  "v_fma_f32 v34, v34, s73, v225\n"
  "v_mfma_f32_16x16x32_bf16 a[192:195], v[24:27], a[120:123], 1.0\n"
  "s_cmp_eq_u32 s95, 0\n"
  "s_cselect_b32 s95, 3, s95\n"
  "s_subb_u32 s100, s100, -11\n"
  "v_exp_f32 v34, v34\n"
  "v_mfma_f32_16x16x32_bf16 a[192:195], v[28:31], a[124:127], a[192:195]\n"
  "s_waitcnt vmcnt(6)\n"
  "s_barrier\n"
  "ds_read_b128 a[128:131], v216 offset:12352\n"
  "v_mfma_f32_16x16x32_bf16 a[176:179], v[8:11], a[120:123], 1.0\n"
  "s_sub_u32 s95, s95, 1\n"
  "ds_read_b128 a[132:135], v216 offset:12416\n"
  "v_fma_f32 v35, v35, s73, v225\n"
  "v_mfma_f32_16x16x32_bf16 a[176:179], v[12:15], a[124:127], a[176:179]\n"
  "s_lshr_b32 s79, s100, 0\n"
  "ds_read_b128 a[120:123], v216 offset:13376\n"
  "v_add_f32 v254, v32, v33\n"
  "v_mfma_f32_16x16x32_bf16 v[68:71], v[8:11], a[152:155], 0\n"
  "s_mul_i32 s79, s79, 7\n"
  "ds_read_b128 a[124:127], v216 offset:13440\n"
  "v_cvt_pk_bf16_f32 v8, v0, v1\n"
  "v_mfma_f32_16x16x32_bf16 v[68:71], v[12:15], a[152:155], v[68:71]\n"
  "s_add_u32 s79, s79, s100\n"
  "ds_read_b128 a[136:139], v216 offset:14400\n"
  "v_cvt_pk_bf16_f32 v9, v2, v3\n"
  "v_mfma_f32_16x16x32_bf16 v[72:75], v[24:27], a[152:155], 0\n"
  "s_add_u32 s79, s79, s101\n"
  "ds_read_b128 a[140:143], v216 offset:14464\n"
  "v_cvt_pk_bf16_f32 v24, v16, v17\n"
  "v_mfma_f32_16x16x32_bf16 v[72:75], v[28:31], a[152:155], v[72:75]\n"
  "s_mov_b32 m0, s66\n"
  "v_exp_f32 v35, v35\n"
  "v_mfma_f32_16x16x32_bf16 v[76:79], v[40:43], a[152:155], 0\n"
  "s_min_u32 s79, s79, 0x1ff\n"
  "v_cvt_pk_bf16_f32 v25, v18, v19\n"
  "v_add_f32 v254, v254, v34\n"
  "v_mfma_f32_16x16x32_bf16 v[76:79], v[44:47], a[152:155], v[76:79]\n"
  "s_mul_i32 s96, s79, 196608\n"
  "v_add_f32 v254, v254, v35\n"
  "v_cvt_pk_bf16_f32 v40, v32, v33\n"
  "v_mfma_f32_16x16x32_bf16 v[80:83], v[56:59], a[152:155], 0\n"
  "s_add_u32 s96, s96, s39\n"
  "v_cvt_pk_bf16_f32 v41, v34, v35\n"
  "v_fma_f32 v48, v48, s73, v226\n"
  "v_mfma_f32_16x16x32_bf16 v[80:83], v[60:63], a[152:155], v[80:83]\n"
  "buffer_load_dwordx4 v221, s[40:43], 0 offen offset:0 lds\n"
  "v_mfma_f32_16x16x32_bf16 v[84:87], a[156:159], a[152:155], 0\n"
  "ds_read_b64 a[156:157], v231 offset:0\n"
  "v_fma_f32 v49, v49, s73, v226\n"
  "v_mfma_f32_16x16x32_bf16 v[84:87], a[160:163], a[152:155], v[84:87]\n"
  "L_CUT_%=:\n"
  "s_waitcnt lgkmcnt(5)\n"
  "ds_read_b64 a[158:159], v232 offset:0\n"
  "v_fma_f32 v50, v50, s73, v226\n"
  "v_mfma_f32_16x16x32_bf16 v[64:67], a[128:131], a[96:99], v[248:251]\n"
  "s_addk_i32 m0, 896\n"
  "v_exp_f32 v48, v48\n"
  "v_mfma_f32_16x16x32_bf16 v[64:67], a[132:135], a[100:103], v[64:67]\n"
  "s_waitcnt lgkmcnt(4)\n"
  "buffer_load_dwordx4 v221, s[40:43], 0 offen offset:128 lds\n"
  "v_fma_f32 v51, v51, s73, v226\n"
  "v_mfma_f32_16x16x32_bf16 v[64:67], a[120:123], a[104:107], v[64:67]\n"
  "s_addk_i32 m0, 896\n"
  "v_exp_f32 v49, v49\n"
  "v_mfma_f32_16x16x32_bf16 v[64:67], a[124:127], a[108:111], v[64:67]\n"
  "s_waitcnt lgkmcnt(2)\n"
  "v_exp_f32 v50, v50\n"
  "v_mfma_f32_16x16x32_bf16 v[64:67], a[136:139], a[112:115], v[64:67]\n"
  "s_mul_i32 s92, s79, 325632\n"
  "v_exp_f32 v51, v51\n"
  "v_mfma_f32_16x16x32_bf16 v[64:67], a[140:143], a[116:119], v[64:67]\n"
  "buffer_load_dwordx4 v221, s[40:43], 0 offen offset:256 lds\n"
  "v_add_f32 v255, v48, v49\n"
  "v_mfma_f32_16x16x32_bf16 v[0:3], a[140:143], a[20:23], v[248:251]\n"
  "s_mul_i32 s79, s95, 110592\n"
  "v_add_f32 v255, v255, v50\n"
  "v_add_f32 v255, v255, v51\n"
  "v_mfma_f32_16x16x32_bf16 v[0:3], a[136:139], a[16:19], v[0:3]\n"
  "s_add_u32 s92, s92, s79\n"
  "v_cvt_pk_bf16_f32 v56, v48, v49\n"
  "v_cvt_pk_bf16_f32 v57, v50, v51\n"
  "v_mfma_f32_16x16x32_bf16 v[0:3], a[124:127], a[12:15], v[0:3]\n"
  "s_add_u32 s71, s92, s80\n"
  "v_fma_f32 v52, v52, s73, v226\n"
  "v_fma_f32 v53, v53, s73, v226\n"
  "v_mfma_f32_16x16x32_bf16 v[0:3], a[120:123], a[8:11], v[0:3]\n"
  "s_add_u32 m0, s58, -128\n"
  "v_exp_f32 v52, v52\n"
  "v_mfma_f32_16x16x32_bf16 v[0:3], a[132:135], a[4:7], v[0:3]\n"
  "s_add_u32 s48, s84, s71\n"
  "v_exp_f32 v53, v53\n"
  "v_mfma_f32_16x16x32_bf16 v[0:3], a[128:131], a[0:3], v[0:3]\n"
  "s_addc_u32 s49, s85, 0\n"
  "v_fma_f32 v54, v54, s73, v226\n"
  "v_fma_f32 v55, v55, s73, v226\n"
  "v_mfma_f32_16x16x32_bf16 v[16:19], a[128:131], a[24:27], v[248:251]\n"
  "s_cmp_eq_u32 s95, 2\n"
  "s_cselect_b32 s94, 17, 18\n"
  "v_exp_f32 v54, v54\n"
  "v_mfma_f32_16x16x32_bf16 v[16:19], a[132:135], a[28:31], v[16:19]\n"
  "buffer_load_dwordx4 v221, s[32:35], 0 offen offset:128 lds\n"
  "v_cvt_pk_bf16_f32 v58, v52, v53\n"
  "v_mfma_f32_16x16x32_bf16 v[16:19], a[120:123], a[32:35], v[16:19]\n"
  "s_addk_i32 m0, 1024\n"
  "ds_read_b64_tr_b16 a[144:145], v217 offset:12352\n"
  "v_fma_f32 v36, v36, s73, v225\n"
  "v_mfma_f32_16x16x32_bf16 v[16:19], a[124:127], a[36:39], v[16:19]\n"
  "s_mul_i32 s94, s94, 6144\n"
  "ds_read_b64_tr_b16 a[146:147], v218 offset:12352\n"
  "v_add_f32 v255, v255, v52\n"
  "v_mfma_f32_16x16x32_bf16 v[16:19], a[136:139], a[40:43], v[16:19]\n"
  "s_cmp_lt_i32 s76, 5\n"
  "s_cselect_b32 s38, s94, 0\n"
  "s_cselect_b32 s28, s96, s28\n"
  "ds_read_b64_tr_b16 a[148:149], v217 offset:13376\n"
  "v_mfma_f32_16x16x32_bf16 v[16:19], a[140:143], a[44:47], v[16:19]\n"
  "buffer_load_dwordx4 v221, s[44:47], 0 offen offset:128 lds\n"
  "v_fma_f32 v37, v37, s73, v225\n"
  "v_mfma_f32_16x16x32_bf16 v[32:35], a[140:143], a[68:71], v[248:251]\n"
  "s_addk_i32 m0, 896\n"
  "ds_read_b64_tr_b16 a[150:151], v218 offset:13376\n"
  "v_fma_f32 v38, v38, s73, v225\n"
  "v_mfma_f32_16x16x32_bf16 v[32:35], a[136:139], a[64:67], v[32:35]\n"
  "s_sub_i32 s71, s38, s80\n"
  "v_exp_f32 v55, v55\n"
  "v_mfma_f32_16x16x32_bf16 v[32:35], a[124:127], a[60:63], v[32:35]\n"
  "s_max_i32 s50, s71, 0\n"
  "v_exp_f32 v36, v36\n"
  "v_mfma_f32_16x16x32_bf16 v[32:35], a[120:123], a[56:59], v[32:35]\n"
  "buffer_load_dwordx4 v221, s[44:47], 0 offen offset:256 lds\n"
  "v_cvt_pk_bf16_f32 v59, v54, v55\n"
  "v_mfma_f32_16x16x32_bf16 v[32:35], a[132:135], a[52:55], v[32:35]\n"
  "s_add_u32 s40, s40, s31\n"
  "v_exp_f32 v37, v37\n"
  "v_mfma_f32_16x16x32_bf16 v[32:35], a[128:131], a[48:51], v[32:35]\n"
  "s_addc_u32 s41, s41, 0\n"
  "v_exp_f32 v38, v38\n"
  "v_mfma_f32_16x16x32_bf16 v[48:51], a[128:131], a[72:75], v[248:251]\n"
  "s_mov_b64 s[32:33], s[44:45]\n"
  "ds_read_b64_tr_b16 a[128:129], v217 offset:12368\n"
  "v_fma_f32 v39, v39, s73, v225\n"
  "v_mfma_f32_16x16x32_bf16 v[48:51], a[132:135], a[76:79], v[48:51]\n"
  "ds_read_b64_tr_b16 a[130:131], v218 offset:12368\n"
  "v_cvt_pk_bf16_f32 v42, v36, v37\n"
  "v_mfma_f32_16x16x32_bf16 v[48:51], a[120:123], a[80:83], v[48:51]\n"
  "ds_read_b64_tr_b16 a[132:133], v217 offset:13392\n"
  "v_fma_f32 v20, v20, s73, v224\n"
  "v_mfma_f32_16x16x32_bf16 v[48:51], a[124:127], a[84:87], v[48:51]\n"
  "s_add_u32 m0, s72, 0\n"
  "ds_read_b64_tr_b16 a[134:135], v218 offset:13392\n"
  "v_fma_f32 v21, v21, s73, v224\n"
  "v_mfma_f32_16x16x32_bf16 v[48:51], a[136:139], a[88:91], v[48:51]\n"
  "s_min_u32 s30, s38, 0xc00\n"
  "v_exp_f32 v39, v39\n"
  "v_mfma_f32_16x16x32_bf16 v[48:51], a[140:143], a[92:95], v[48:51]\n"
  "s_waitcnt lgkmcnt(4)\n"
  "v_exp_f32 v20, v20\n"
  "v_mfma_f32_16x16x32_bf16 a[236:239], a[160:163], a[144:147], a[236:239]\n"
  "v_exp_f32 v21, v21\n"
  "v_mfma_f32_16x16x32_bf16 a[236:239], a[156:159], a[148:151], a[236:239]\n"
  "buffer_load_dwordx4 v221, s[48:51], 0 offen offset:0 nt lds\n"
  "v_cvt_pk_bf16_f32 v43, v38, v39\n"
  "v_mfma_f32_16x16x32_bf16 v[184:187], v[60:63], a[144:147], v[184:187]\n"
  "v_fma_f32 v22, v22, s73, v224\n"
  "v_fma_f32 v23, v23, s73, v224\n"
  "v_mfma_f32_16x16x32_bf16 v[184:187], v[56:59], a[148:151], v[184:187]\n"
  "s_add_u32 s44, s44, s31\n"
  "v_exp_f32 v22, v22\n"
  "v_exp_f32 v23, v23\n"
  "v_mfma_f32_16x16x32_bf16 v[152:155], v[44:47], a[144:147], v[152:155]\n"
  "s_addc_u32 s45, s45, 0\n"
  "ds_read_b64_tr_b16 a[120:121], v219 offset:12352\n"
  "v_cvt_pk_bf16_f32 v26, v20, v21\n"
  "v_mfma_f32_16x16x32_bf16 v[152:155], v[40:43], a[148:151], v[152:155]\n"
  "ds_read_b64_tr_b16 a[122:123], v220 offset:12352\n"
  "v_cvt_pk_bf16_f32 v27, v22, v23\n"
  "v_fma_f32 v4, v4, s73, v223\n"
  "v_exp_f32 v4, v4\n"
  "v_fma_f32 v5, v5, s73, v223\n"
  "v_exp_f32 v5, v5\n"
  "v_fma_f32 v6, v6, s73, v223\n"
  "v_exp_f32 v6, v6\n"
  "v_fma_f32 v7, v7, s73, v223\n"
  "v_exp_f32 v7, v7\n"
  "v_mfma_f32_16x16x32_bf16 v[120:123], v[28:31], a[144:147], v[120:123]\n"
  "ds_read_b64_tr_b16 a[124:125], v219 offset:13376\n"
  "v_cvt_pk_bf16_f32 v10, v4, v5\n"
  "v_mfma_f32_16x16x32_bf16 v[120:123], v[24:27], a[148:151], v[120:123]\n"
  "ds_read_b64_tr_b16 a[126:127], v220 offset:13376\n"
  "v_cvt_pk_bf16_f32 v11, v6, v7\n"
  "v_mfma_f32_16x16x32_bf16 v[88:91], v[12:15], a[144:147], v[88:91]\n"
  "s_add_u32 m0, s72, -1984\n"
  "v_add_f32 v252, v252, v4\n"
  "v_add_f32 v252, v252, v5\n"
  "v_mfma_f32_16x16x32_bf16 v[88:91], v[8:11], a[148:151], v[88:91]\n"
  "s_waitcnt lgkmcnt(4)\n"
  "buffer_load_dwordx4 v221, s[48:51], 0 offen offset:3072 nt lds\n"
  "v_add_f32 v252, v252, v6\n"
  "v_mfma_f32_16x16x32_bf16 v[92:95], v[12:15], a[128:131], v[92:95]\n"
  "v_add_f32 v252, v252, v7\n"
  "v_add_f32 v253, v253, v20\n"
  "v_mfma_f32_16x16x32_bf16 v[92:95], v[8:11], a[132:135], v[92:95]\n"
  "v_add_f32 v253, v253, v21\n"
  "v_add_f32 v253, v253, v22\n"
  "v_mfma_f32_16x16x32_bf16 v[124:127], v[28:31], a[128:131], v[124:127]\n"
  "ds_read_b128 a[136:139], v235 offset:12352\n"
  "v_add_f32 v253, v253, v23\n"
  "v_mfma_f32_16x16x32_bf16 v[124:127], v[24:27], a[132:135], v[124:127]\n"
  "s_add_u32 m0, s72, 2112\n"
  "ds_read_b128 a[140:143], v235 offset:12416\n"
  "v_add_f32 v254, v254, v36\n"
  "v_mfma_f32_16x16x32_bf16 v[156:159], v[44:47], a[128:131], v[156:159]\n"
  "ds_read_b128 a[144:147], v235 offset:13376\n"
  "v_add_f32 v254, v254, v37\n"
  "v_mfma_f32_16x16x32_bf16 v[156:159], v[40:43], a[132:135], v[156:159]\n"
  "ds_read_b128 a[148:151], v235 offset:13440\n"
  "v_add_f32 v254, v254, v38\n"
  "v_mfma_f32_16x16x32_bf16 v[188:191], v[60:63], a[128:131], v[188:191]\n"
  "v_add_f32 v254, v254, v39\n"
  "v_add_f32 v255, v255, v53\n"
  "v_mfma_f32_16x16x32_bf16 v[188:191], v[56:59], a[132:135], v[188:191]\n"
  "buffer_load_dwordx4 v221, s[48:51], 0 offen offset:128 nt lds\n"
  "v_add_f32 v255, v255, v54\n"
  "v_mfma_f32_16x16x32_bf16 a[240:243], a[160:163], a[128:131], a[240:243]\n"
  "ds_read_b128 a[128:131], v235 offset:14400\n"
  "v_add_f32 v255, v255, v55\n"
  "v_mfma_f32_16x16x32_bf16 a[240:243], a[156:159], a[132:135], a[240:243]\n"
  "s_waitcnt lgkmcnt(5)\n"
  "ds_read_b128 a[132:135], v235 offset:14464\n"
  "v_fma_f32 v64, v64, s73, v227\n"
  "v_mfma_f32_16x16x32_bf16 v[192:195], v[60:63], a[120:123], v[192:195]\n"
  "v_exp_f32 v64, v64\n"
  "v_mfma_f32_16x16x32_bf16 v[192:195], v[56:59], a[124:127], v[192:195]\n"
  "s_add_u32 m0, s72, 128\n"
  "v_fma_f32 v65, v65, s73, v227\n"
  "v_mfma_f32_16x16x32_bf16 v[160:163], v[44:47], a[120:123], v[160:163]\n"
  "v_exp_f32 v65, v65\n"
  "v_mfma_f32_16x16x32_bf16 v[160:163], v[40:43], a[124:127], v[160:163]\n"
  "v_fma_f32 v66, v66, s73, v227\n"
  "v_fma_f32 v67, v67, s73, v227\n"
  "v_mfma_f32_16x16x32_bf16 v[128:131], v[28:31], a[120:123], v[128:131]\n"
  "v_exp_f32 v66, v66\n"
  "v_mfma_f32_16x16x32_bf16 v[128:131], v[24:27], a[124:127], v[128:131]\n"
  "buffer_load_dwordx4 v221, s[48:51], 0 offen offset:3200 nt lds\n"
  "v_cvt_pk_bf16_f32 v64, v64, v65\n"
  "v_mfma_f32_16x16x32_bf16 v[96:99], v[12:15], a[120:123], v[96:99]\n"
  "v_exp_f32 v67, v67\n"
  "v_mfma_f32_16x16x32_bf16 v[96:99], v[8:11], a[124:127], v[96:99]\n"
  "s_waitcnt lgkmcnt(4)\n"
  "v_cvt_pk_bf16_f32 v65, v66, v67\n"
  "v_fma_f32 v0, v0, s73, v223\n"
  "v_mfma_f32_16x16x32_bf16 v[4:7], a[136:139], a[0:3], v[248:251]\n"
  "v_exp_f32 v0, v0\n"
  "v_mfma_f32_16x16x32_bf16 v[4:7], a[140:143], a[4:7], v[4:7]\n"
  "s_waitcnt lgkmcnt(2)\n"
  "v_fma_f32 v1, v1, s73, v223\n"
  "v_fma_f32 v2, v2, s73, v223\n"
  "v_mfma_f32_16x16x32_bf16 v[4:7], a[144:147], a[8:11], v[4:7]\n"
  "v_exp_f32 v1, v1\n"
  "v_mfma_f32_16x16x32_bf16 v[4:7], a[148:151], a[12:15], v[4:7]\n"
  "s_waitcnt lgkmcnt(0)\n"
  "v_exp_f32 v2, v2\n"
  "v_mfma_f32_16x16x32_bf16 v[4:7], a[128:131], a[16:19], v[4:7]\n"
  "v_fma_f32 v3, v3, s73, v223\n"
  "v_add_f32 v252, v252, v0\n"
  "v_mfma_f32_16x16x32_bf16 v[4:7], a[132:135], a[20:23], v[4:7]\n"
  "ds_write_b64 v231, v[64:65] offset:2048\n"
  "v_mfma_f32_16x16x32_bf16 v[20:23], a[132:135], a[44:47], v[248:251]\n"
  "v_exp_f32 v3, v3\n"
  "v_mfma_f32_16x16x32_bf16 v[20:23], a[128:131], a[40:43], v[20:23]\n"
  "v_add_f32 v252, v252, v1\n"
  "v_add_f32 v252, v252, v2\n"
  "v_mfma_f32_16x16x32_bf16 v[20:23], a[148:151], a[36:39], v[20:23]\n"
  "ds_read_b64_tr_b16 a[120:121], v219 offset:12368\n"
  "v_add_f32 v252, v252, v3\n"
  "v_mfma_f32_16x16x32_bf16 v[20:23], a[144:147], a[32:35], v[20:23]\n"
  "ds_read_b64_tr_b16 a[122:123], v220 offset:12368\n"
  "v_fma_f32 v16, v16, s73, v224\n"
  "v_mfma_f32_16x16x32_bf16 v[20:23], a[140:143], a[28:31], v[20:23]\n"
  "ds_read_b64_tr_b16 a[124:125], v219 offset:13392\n"
  "v_fma_f32 v17, v17, s73, v224\n"
  "v_mfma_f32_16x16x32_bf16 v[20:23], a[136:139], a[24:27], v[20:23]\n"
  "ds_read_b64_tr_b16 a[126:127], v220 offset:13392\n"
  "v_fma_f32 v18, v18, s73, v224\n"
  "v_mfma_f32_16x16x32_bf16 v[36:39], a[136:139], a[48:51], v[248:251]\n"
  "v_exp_f32 v16, v16\n"
  "v_mfma_f32_16x16x32_bf16 v[36:39], a[140:143], a[52:55], v[36:39]\n"
  "v_exp_f32 v17, v17\n"
  "v_mfma_f32_16x16x32_bf16 v[36:39], a[144:147], a[56:59], v[36:39]\n"
  "v_exp_f32 v18, v18\n"
  "v_mfma_f32_16x16x32_bf16 v[36:39], a[148:151], a[60:63], v[36:39]\n"
  "v_fma_f32 v19, v19, s73, v224\n"
  "v_add_f32 v253, v253, v16\n"
  "v_mfma_f32_16x16x32_bf16 v[36:39], a[128:131], a[64:67], v[36:39]\n"
  "v_exp_f32 v19, v19\n"
  "v_mfma_f32_16x16x32_bf16 v[36:39], a[132:135], a[68:71], v[36:39]\n"
  "v_add_f32 v253, v253, v17\n"
  "v_add_f32 v253, v253, v18\n"
  "v_mfma_f32_16x16x32_bf16 v[52:55], a[132:135], a[92:95], v[248:251]\n"
  "ds_read_b64_tr_b16 a[132:133], v217 offset:14400\n"
  "v_add_f32 v253, v253, v19\n"
  "v_mfma_f32_16x16x32_bf16 v[52:55], a[128:131], a[88:91], v[52:55]\n"
  "ds_read_b64_tr_b16 a[134:135], v218 offset:14400\n"
  "v_fma_f32 v32, v32, s73, v225\n"
  "v_mfma_f32_16x16x32_bf16 v[52:55], a[148:151], a[84:87], v[52:55]\n"
  "ds_read_b64_tr_b16 a[128:129], v217 offset:14416\n"
  "v_fma_f32 v33, v33, s73, v225\n"
  "v_mfma_f32_16x16x32_bf16 v[52:55], a[144:147], a[80:83], v[52:55]\n"
  "ds_read_b64_tr_b16 a[130:131], v218 offset:14416\n"
  "v_fma_f32 v34, v34, s73, v225\n"
  "v_mfma_f32_16x16x32_bf16 v[52:55], a[140:143], a[76:79], v[52:55]\n"
  "v_exp_f32 v32, v32\n"
  "v_mfma_f32_16x16x32_bf16 v[52:55], a[136:139], a[72:75], v[52:55]\n"
  "s_waitcnt lgkmcnt(4)\n"
  "v_exp_f32 v33, v33\n"
  "v_mfma_f32_16x16x32_bf16 v[100:103], v[12:15], a[120:123], v[100:103]\n"
  "v_exp_f32 v34, v34\n"
  "v_mfma_f32_16x16x32_bf16 v[100:103], v[8:11], a[124:127], v[100:103]\n"
  "v_cvt_pk_bf16_f32 v12, v0, v1\n"
  "v_cvt_pk_bf16_f32 v13, v2, v3\n"
  "v_mfma_f32_16x16x32_bf16 v[132:135], v[28:31], a[120:123], v[132:135]\n"
  "ds_read_b64_tr_b16 a[140:141], v219 offset:14400\n"
  "v_cvt_pk_bf16_f32 v28, v16, v17\n"
  "v_mfma_f32_16x16x32_bf16 v[132:135], v[24:27], a[124:127], v[132:135]\n"
  "ds_read_b64_tr_b16 a[142:143], v220 offset:14400\n"
  "v_cvt_pk_bf16_f32 v29, v18, v19\n"
  "v_mfma_f32_16x16x32_bf16 v[164:167], v[44:47], a[120:123], v[164:167]\n"
  "ds_read_b64_tr_b16 a[136:137], v219 offset:14416\n"
  "v_fma_f32 v35, v35, s73, v225\n"
  "v_mfma_f32_16x16x32_bf16 v[164:167], v[40:43], a[124:127], v[164:167]\n"
  "ds_read_b64_tr_b16 a[138:139], v220 offset:14416\n"
  "v_add_f32 v254, v254, v32\n"
  "v_mfma_f32_16x16x32_bf16 v[196:199], v[60:63], a[120:123], v[196:199]\n"
  "v_exp_f32 v35, v35\n"
  "v_mfma_f32_16x16x32_bf16 v[196:199], v[56:59], a[124:127], v[196:199]\n"
  "s_waitcnt lgkmcnt(4)\n"
  "v_add_f32 v254, v254, v33\n"
  "v_add_f32 v254, v254, v34\n"
  "v_mfma_f32_16x16x32_bf16 a[244:247], a[156:159], a[132:135], a[244:247]\n"
  "v_add_f32 v254, v254, v35\n"
  "v_cvt_pk_bf16_f32 v44, v32, v33\n"
  "v_mfma_f32_16x16x32_bf16 a[248:251], a[156:159], a[128:131], a[248:251]\n"
  "v_cvt_pk_bf16_f32 v45, v34, v35\n"
  "v_fma_f32 v48, v48, s73, v226\n"
  "v_mfma_f32_16x16x32_bf16 v[200:203], v[56:59], a[132:135], v[200:203]\n"
  "v_exp_f32 v48, v48\n"
  "v_mfma_f32_16x16x32_bf16 v[204:207], v[56:59], a[128:131], v[204:207]\n"
  "v_fma_f32 v49, v49, s73, v226\n"
  "v_fma_f32 v50, v50, s73, v226\n"
  "v_mfma_f32_16x16x32_bf16 v[168:171], v[40:43], a[132:135], v[168:171]\n"
  "v_exp_f32 v49, v49\n"
  "v_mfma_f32_16x16x32_bf16 v[172:175], v[40:43], a[128:131], v[172:175]\n"
  "s_waitcnt vmcnt(10) lgkmcnt(0)\n"
  "s_barrier\n"
  "ds_read_b128 a[120:123], v216 offset:24704\n"
  "v_mfma_f32_16x16x32_bf16 v[136:139], v[24:27], a[132:135], v[136:139]\n"
  "ds_read_b64 a[160:161], v231 offset:2048\n"
  "v_fma_f32 v51, v51, s73, v226\n"
  "v_mfma_f32_16x16x32_bf16 v[140:143], v[24:27], a[128:131], v[140:143]\n"
  "ds_read_b64 a[162:163], v232 offset:2048\n"
  "v_add_f32 v255, v255, v48\n"
  "v_mfma_f32_16x16x32_bf16 v[104:107], v[8:11], a[132:135], v[104:107]\n"
  "ds_read_b128 a[124:127], v216 offset:24768\n"
  "v_add_f32 v255, v255, v49\n"
  "v_mfma_f32_16x16x32_bf16 v[108:111], v[8:11], a[128:131], v[108:111]\n"
  "ds_read_b128 a[132:135], v216 offset:25728\n"
  "v_cvt_pk_bf16_f32 v60, v48, v49\n"
  "v_mfma_f32_16x16x32_bf16 v[112:115], v[8:11], a[140:143], v[112:115]\n"
  "ds_read_b128 a[128:131], v216 offset:25792\n"
  "v_fma_f32 v52, v52, s73, v226\n"
  "v_mfma_f32_16x16x32_bf16 v[116:119], v[8:11], a[136:139], v[116:119]\n"
  "s_mov_b32 m0, s67\n"
  "ds_read_b128 a[148:151], v216 offset:26752\n"
  "v_fma_f32 v53, v53, s73, v226\n"
  "v_mfma_f32_16x16x32_bf16 v[144:147], v[24:27], a[140:143], v[144:147]\n"
  "ds_read_b128 a[144:147], v216 offset:26816\n"
  "v_fma_f32 v54, v54, s73, v226\n"
  "v_mfma_f32_16x16x32_bf16 v[148:151], v[24:27], a[136:139], v[148:151]\n"
  "v_exp_f32 v50, v50\n"
  "v_mfma_f32_16x16x32_bf16 v[176:179], v[40:43], a[140:143], v[176:179]\n"
  "v_exp_f32 v51, v51\n"
  "v_mfma_f32_16x16x32_bf16 v[180:183], v[40:43], a[136:139], v[180:183]\n"
  "buffer_load_dwordx4 v221, s[40:43], 0 offen offset:0 lds\n"
  "v_mfma_f32_16x16x32_bf16 v[208:211], v[56:59], a[140:143], v[208:211]\n"
  "s_addk_i32 m0, 896\n"
  "v_exp_f32 v52, v52\n"
  "v_mfma_f32_16x16x32_bf16 v[212:215], v[56:59], a[136:139], v[212:215]\n"
  "s_waitcnt lgkmcnt(4)\n"
  "v_exp_f32 v53, v53\n"
  "v_mfma_f32_16x16x32_bf16 v[64:67], a[120:123], a[96:99], v[248:251]\n"
  "v_exp_f32 v54, v54\n"
  "v_mfma_f32_16x16x32_bf16 v[64:67], a[124:127], a[100:103], v[64:67]\n"
  "s_waitcnt lgkmcnt(2)\n"
  "buffer_load_dwordx4 v221, s[40:43], 0 offen offset:128 lds\n"
  "v_add_f32 v255, v255, v50\n"
  "v_mfma_f32_16x16x32_bf16 v[64:67], a[132:135], a[104:107], v[64:67]\n"
  "s_addk_i32 m0, 896\n"
  "v_add_f32 v255, v255, v51\n"
  "v_cvt_pk_bf16_f32 v61, v50, v51\n"
  "v_mfma_f32_16x16x32_bf16 v[64:67], a[128:131], a[108:111], v[64:67]\n"
  "s_waitcnt lgkmcnt(0)\n"
  "v_fma_f32 v55, v55, s73, v226\n"
  "v_cvt_pk_bf16_f32 v62, v52, v53\n"
  "v_mfma_f32_16x16x32_bf16 v[64:67], a[148:151], a[112:115], v[64:67]\n"
  "v_exp_f32 v55, v55\n"
  "v_mfma_f32_16x16x32_bf16 v[64:67], a[144:147], a[116:119], v[64:67]\n"
  "buffer_load_dwordx4 v221, s[40:43], 0 offen offset:256 lds\n"
  "v_cvt_pk_bf16_f32 v63, v54, v55\n"
  "v_mfma_f32_16x16x32_bf16 v[0:3], a[144:147], a[20:23], v[248:251]\n"
  "v_fma_f32 v36, v36, s73, v225\n"
  "v_fma_f32 v37, v37, s73, v225\n"
  "v_mfma_f32_16x16x32_bf16 v[0:3], a[148:151], a[16:19], v[0:3]\n"
  "s_add_u32 s40, s40, s31\n"
  "v_exp_f32 v36, v36\n"
  "v_mfma_f32_16x16x32_bf16 v[0:3], a[128:131], a[12:15], v[0:3]\n"
  "s_addc_u32 s41, s41, 0\n"
  "v_exp_f32 v37, v37\n"
  "v_mfma_f32_16x16x32_bf16 v[0:3], a[132:135], a[8:11], v[0:3]\n"
  "s_mov_b32 m0, s59\n"
  "v_fma_f32 v38, v38, s73, v225\n"
  "v_fma_f32 v39, v39, s73, v225\n"
  "v_mfma_f32_16x16x32_bf16 v[0:3], a[124:127], a[4:7], v[0:3]\n"
  "v_exp_f32 v38, v38\n"
  "v_mfma_f32_16x16x32_bf16 v[0:3], a[120:123], a[0:3], v[0:3]\n"
  "v_exp_f32 v39, v39\n"
  "v_mfma_f32_16x16x32_bf16 v[16:19], a[120:123], a[24:27], v[248:251]\n"
  "v_cvt_pk_bf16_f32 v46, v36, v37\n"
  "v_cvt_pk_bf16_f32 v47, v38, v39\n"
  "v_mfma_f32_16x16x32_bf16 v[16:19], a[124:127], a[28:31], v[16:19]\n"
  "buffer_load_dwordx4 v221, s[32:35], 0 offen offset:0 lds\n"
  "v_fma_f32 v20, v20, s73, v224\n"
  "v_mfma_f32_16x16x32_bf16 v[16:19], a[132:135], a[32:35], v[16:19]\n"
  "s_addk_i32 m0, 1024\n"
  "ds_read_b64_tr_b16 a[140:141], v217 offset:24704\n"
  "v_fma_f32 v21, v21, s73, v224\n"
  "v_mfma_f32_16x16x32_bf16 v[16:19], a[128:131], a[36:39], v[16:19]\n"
  "s_mov_b64 s[32:33], s[44:45]\n"
  "ds_read_b64_tr_b16 a[142:143], v218 offset:24704\n"
  "v_fma_f32 v22, v22, s73, v224\n"
  "v_mfma_f32_16x16x32_bf16 v[16:19], a[148:151], a[40:43], v[16:19]\n"
  "ds_read_b64_tr_b16 a[136:137], v217 offset:25728\n"
  "v_fma_f32 v23, v23, s73, v224\n"
  "v_mfma_f32_16x16x32_bf16 v[16:19], a[144:147], a[44:47], v[16:19]\n"
  "buffer_load_dwordx4 v221, s[44:47], 0 offen offset:0 lds\n"
  "v_fma_f32 v4, v4, s73, v223\n"
  "v_mfma_f32_16x16x32_bf16 v[32:35], a[144:147], a[68:71], v[248:251]\n"
  "s_addk_i32 m0, 768\n"
  "ds_read_b64_tr_b16 a[138:139], v218 offset:25728\n"
  "v_fma_f32 v5, v5, s73, v223\n"
  "v_mfma_f32_16x16x32_bf16 v[32:35], a[148:151], a[64:67], v[32:35]\n"
  "v_exp_f32 v20, v20\n"
  "v_mfma_f32_16x16x32_bf16 v[32:35], a[128:131], a[60:63], v[32:35]\n"
  "v_exp_f32 v21, v21\n"
  "v_mfma_f32_16x16x32_bf16 v[32:35], a[132:135], a[56:59], v[32:35]\n"
  "buffer_load_dwordx4 v221, s[44:47], 0 offen offset:256 lds\n"
  "v_cvt_pk_bf16_f32 v30, v20, v21\n"
  "v_mfma_f32_16x16x32_bf16 v[32:35], a[124:127], a[52:55], v[32:35]\n"
  "s_add_u32 s44, s44, s31\n"
  "v_exp_f32 v22, v22\n"
  "v_mfma_f32_16x16x32_bf16 v[32:35], a[120:123], a[48:51], v[32:35]\n"
  "s_addc_u32 s45, s45, 0\n"
  "v_exp_f32 v23, v23\n"
  "v_mfma_f32_16x16x32_bf16 v[48:51], a[120:123], a[72:75], v[248:251]\n"
  "v_exp_f32 v4, v4\n"
  "v_mfma_f32_16x16x32_bf16 v[48:51], a[124:127], a[76:79], v[48:51]\n"
  "v_exp_f32 v5, v5\n"
  "v_mfma_f32_16x16x32_bf16 v[48:51], a[132:135], a[80:83], v[48:51]\n"
  "ds_read_b64_tr_b16 a[120:121], v217 offset:24720\n"
  "v_cvt_pk_bf16_f32 v31, v22, v23\n"
  "v_mfma_f32_16x16x32_bf16 v[48:51], a[128:131], a[84:87], v[48:51]\n"
  "ds_read_b64_tr_b16 a[122:123], v218 offset:24720\n"
  "v_fma_f32 v6, v6, s73, v223\n"
  "v_mfma_f32_16x16x32_bf16 v[48:51], a[148:151], a[88:91], v[48:51]\n"
  "ds_read_b64_tr_b16 a[124:125], v217 offset:25744\n"
  "v_fma_f32 v7, v7, s73, v223\n"
  "v_mfma_f32_16x16x32_bf16 v[48:51], a[144:147], a[92:95], v[48:51]\n"
  "s_waitcnt lgkmcnt(3)\n"
  "ds_read_b64_tr_b16 a[126:127], v218 offset:25744\n"
  "v_cvt_pk_bf16_f32 v14, v4, v5\n"
  "v_mfma_f32_16x16x32_bf16 a[228:231], a[156:159], a[140:143], a[228:231]\n"
  "v_exp_f32 v6, v6\n"
  "v_mfma_f32_16x16x32_bf16 a[228:231], a[160:163], a[136:139], a[228:231]\n"
  "v_exp_f32 v7, v7\n"
  "v_mfma_f32_16x16x32_bf16 a[212:215], v[56:59], a[140:143], a[212:215]\n"
  "v_cvt_pk_bf16_f32 v15, v6, v7\n"
  "v_add_f32 v252, v252, v4\n"
  "v_mfma_f32_16x16x32_bf16 a[212:215], v[60:63], a[136:139], a[212:215]\n"
  "v_add_f32 v252, v252, v5\n"
  "v_add_f32 v252, v252, v6\n"
  "v_mfma_f32_16x16x32_bf16 a[196:199], v[40:43], a[140:143], a[196:199]\n"
  "v_add_f32 v252, v252, v7\n"
  "v_add_f32 v253, v253, v20\n"
  "v_mfma_f32_16x16x32_bf16 a[196:199], v[44:47], a[136:139], a[196:199]\n"
  "v_add_f32 v253, v253, v21\n"
  "v_add_f32 v253, v253, v22\n"
  "v_mfma_f32_16x16x32_bf16 a[180:183], v[24:27], a[140:143], a[180:183]\n"
  "ds_read_b64_tr_b16 a[132:133], v219 offset:24704\n"
  "v_add_f32 v253, v253, v23\n"
  "v_mfma_f32_16x16x32_bf16 a[180:183], v[28:31], a[136:139], a[180:183]\n"
  "ds_read_b64_tr_b16 a[134:135], v220 offset:24704\n"
  "v_add_f32 v254, v254, v36\n"
  "v_mfma_f32_16x16x32_bf16 a[164:167], v[8:11], a[140:143], a[164:167]\n"
  "ds_read_b64_tr_b16 a[128:129], v219 offset:25728\n"
  "v_add_f32 v254, v254, v37\n"
  "v_mfma_f32_16x16x32_bf16 a[164:167], v[12:15], a[136:139], a[164:167]\n"
  "ds_read_b64_tr_b16 a[130:131], v220 offset:25728\n"
  "v_add_f32 v254, v254, v38\n"
  "v_mfma_f32_16x16x32_bf16 v[84:87], a[156:159], a[152:155], v[84:87]\n"
  "v_add_f32 v254, v254, v39\n"
  "v_add_f32 v255, v255, v52\n"
  "v_mfma_f32_16x16x32_bf16 v[84:87], a[160:163], a[152:155], v[84:87]\n"
  "s_waitcnt lgkmcnt(4)\n"
  "v_add_f32 v255, v255, v53\n"
  "v_add_f32 v255, v255, v54\n"
  "v_mfma_f32_16x16x32_bf16 a[168:171], v[8:11], a[120:123], a[168:171]\n"
  "ds_read_b64_tr_b16 v[246:247], v230 offset:5632\n"
  "v_add_f32 v255, v255, v55\n"
  "v_mfma_f32_16x16x32_bf16 a[168:171], v[12:15], a[124:127], a[168:171]\n"
  "v_fma_f32 v64, v64, s73, v227\n"
  "v_fma_f32 v65, v65, s73, v227\n"
  "v_mfma_f32_16x16x32_bf16 a[184:187], v[24:27], a[120:123], a[184:187]\n"
  "ds_read_b128 a[140:143], v235 offset:24704\n"
  "v_fma_f32 v66, v66, s73, v227\n"
  "v_mfma_f32_16x16x32_bf16 a[184:187], v[28:31], a[124:127], a[184:187]\n"
  "ds_read_b128 a[136:139], v235 offset:24768\n"
  "v_fma_f32 v67, v67, s73, v227\n"
  "v_mfma_f32_16x16x32_bf16 a[200:203], v[40:43], a[120:123], a[200:203]\n"
  "ds_read_b128 a[148:151], v235 offset:25728\n"
  "v_fma_f32 v0, v0, s73, v223\n"
  "v_mfma_f32_16x16x32_bf16 a[200:203], v[44:47], a[124:127], a[200:203]\n"
  "ds_read_b128 a[144:147], v235 offset:25792\n"
  "v_fma_f32 v1, v1, s73, v223\n"
  "v_mfma_f32_16x16x32_bf16 a[216:219], v[56:59], a[120:123], a[216:219]\n"
  "v_exp_f32 v64, v64\n"
  "v_mfma_f32_16x16x32_bf16 a[216:219], v[60:63], a[124:127], a[216:219]\n"
  "v_exp_f32 v65, v65\n"
  "v_mfma_f32_16x16x32_bf16 a[232:235], a[156:159], a[120:123], a[232:235]\n"
  "ds_read_b128 a[120:123], v235 offset:26752\n"
  "v_cvt_pk_bf16_f32 v64, v64, v65\n"
  "v_mfma_f32_16x16x32_bf16 a[232:235], a[160:163], a[124:127], a[232:235]\n"
  "s_waitcnt lgkmcnt(6)\n"
  "ds_read_b128 a[124:127], v235 offset:26816\n"
  "v_fma_f32 v2, v2, s73, v223\n"
  "v_mfma_f32_16x16x32_bf16 a[220:223], v[56:59], a[132:135], a[220:223]\n"
  "v_exp_f32 v66, v66\n"
  "v_mfma_f32_16x16x32_bf16 a[220:223], v[60:63], a[128:131], a[220:223]\n"
  "v_exp_f32 v67, v67\n"
  "v_mfma_f32_16x16x32_bf16 a[204:207], v[40:43], a[132:135], a[204:207]\n"
  "v_exp_f32 v0, v0\n"
  "v_mfma_f32_16x16x32_bf16 a[204:207], v[44:47], a[128:131], a[204:207]\n"
  "buffer_store_dwordx4 a[252:255], v228, s[52:55], s82 offen offset:0 nt\n"
  "v_cvt_pk_bf16_f32 v65, v66, v67\n"
  "v_exp_f32 v1, v1\n"
  "v_mfma_f32_16x16x32_bf16 a[188:191], v[24:27], a[132:135], a[188:191]\n"
  "ds_read_b64_tr_b16 a[252:253], v230 offset:6720\n"
  "ds_read_b64_tr_b16 a[254:255], v230 offset:6752\n"
  "v_mfma_f32_16x16x32_bf16 a[188:191], v[28:31], a[128:131], a[188:191]\n"
  "ds_write_b64 v231, v[64:65] offset:0\n"
  "v_mfma_f32_16x16x32_bf16 a[172:175], v[8:11], a[132:135], a[172:175]\n"
  "v_exp_f32 v2, v2\n"
  "v_mfma_f32_16x16x32_bf16 a[172:175], v[12:15], a[128:131], a[172:175]\n"
  "s_waitcnt lgkmcnt(7)\n"
  "v_fma_f32 v3, v3, s73, v223\n"
  "v_add_f32 v252, v252, v0\n"
  "v_mfma_f32_16x16x32_bf16 v[4:7], a[140:143], a[0:3], v[248:251]\n"
  "v_exp_f32 v3, v3\n"
  "v_mfma_f32_16x16x32_bf16 v[4:7], a[136:139], a[4:7], v[4:7]\n"
  "s_waitcnt lgkmcnt(5)\n"
  "v_add_f32 v252, v252, v1\n"
  "v_add_f32 v252, v252, v2\n"
  "v_mfma_f32_16x16x32_bf16 v[4:7], a[148:151], a[8:11], v[4:7]\n"
  "v_add_f32 v252, v252, v3\n"
  "v_fma_f32 v16, v16, s73, v224\n"
  "v_mfma_f32_16x16x32_bf16 v[4:7], a[144:147], a[12:15], v[4:7]\n"
  "s_waitcnt lgkmcnt(3)\n"
  "v_exp_f32 v16, v16\n"
  "v_mfma_f32_16x16x32_bf16 v[4:7], a[120:123], a[16:19], v[4:7]\n"
  "v_fma_f32 v17, v17, s73, v224\n"
  "v_fma_f32 v18, v18, s73, v224\n"
  "v_mfma_f32_16x16x32_bf16 v[4:7], a[124:127], a[20:23], v[4:7]\n"
  "v_exp_f32 v17, v17\n"
  "v_mfma_f32_16x16x32_bf16 v[20:23], a[124:127], a[44:47], v[248:251]\n"
  "v_exp_f32 v18, v18\n"
  "v_mfma_f32_16x16x32_bf16 v[20:23], a[120:123], a[40:43], v[20:23]\n"
  "v_fma_f32 v19, v19, s73, v224\n"
  "v_add_f32 v253, v253, v16\n"
  "v_mfma_f32_16x16x32_bf16 v[20:23], a[144:147], a[36:39], v[20:23]\n"
  "ds_read_b64_tr_b16 a[132:133], v219 offset:24720\n"
  "v_add_f32 v253, v253, v17\n"
  "v_mfma_f32_16x16x32_bf16 v[20:23], a[148:151], a[32:35], v[20:23]\n"
  "ds_read_b64_tr_b16 a[134:135], v220 offset:24720\n"
  "v_add_f32 v253, v253, v18\n"
  "v_mfma_f32_16x16x32_bf16 v[20:23], a[136:139], a[28:31], v[20:23]\n"
  "ds_read_b64_tr_b16 a[128:129], v219 offset:25744\n"
  "v_fma_f32 v32, v32, s73, v225\n"
  "v_mfma_f32_16x16x32_bf16 v[20:23], a[140:143], a[24:27], v[20:23]\n"
  "ds_read_b64_tr_b16 a[130:131], v220 offset:25744\n"
  "v_fma_f32 v33, v33, s73, v225\n"
  "v_mfma_f32_16x16x32_bf16 v[36:39], a[140:143], a[48:51], v[248:251]\n"
  "v_exp_f32 v19, v19\n"
  "v_mfma_f32_16x16x32_bf16 v[36:39], a[136:139], a[52:55], v[36:39]\n"
  "v_exp_f32 v32, v32\n"
  "v_mfma_f32_16x16x32_bf16 v[36:39], a[148:151], a[56:59], v[36:39]\n"
  "v_exp_f32 v33, v33\n"
  "v_mfma_f32_16x16x32_bf16 v[36:39], a[144:147], a[60:63], v[36:39]\n"
  "v_add_f32 v253, v253, v19\n"
  "v_fma_f32 v34, v34, s73, v225\n"
  "v_mfma_f32_16x16x32_bf16 v[36:39], a[120:123], a[64:67], v[36:39]\n"
  "v_exp_f32_e64 v34, v34\n"
  "v_mfma_f32_16x16x32_bf16 v[36:39], a[124:127], a[68:71], v[36:39]\n"
  "buffer_store_dwordx4 v[244:247], v228, s[52:55], s82 offen offset:3072 nt\n"
  "v_fma_f32 v35, v35, s73, v225\n"
  "v_exp_f32_e64 v35, v35\n"
  "v_mfma_f32_16x16x32_bf16 v[52:55], a[124:127], a[92:95], v[248:251]\n"
  "ds_read_b64_tr_b16 a[124:125], v217 offset:26752\n"
  "v_add_f32 v254, v254, v32\n"
  "v_mfma_f32_16x16x32_bf16 v[52:55], a[120:123], a[88:91], v[52:55]\n"
  "ds_read_b64_tr_b16 a[126:127], v218 offset:26752\n"
  "v_add_f32 v254, v254, v33\n"
  "v_mfma_f32_16x16x32_bf16 v[52:55], a[144:147], a[84:87], v[52:55]\n"
  "ds_read_b64_tr_b16 a[120:121], v217 offset:26768\n"
  "v_add_f32 v254, v254, v34\n"
  "v_mfma_f32_16x16x32_bf16 v[52:55], a[148:151], a[80:83], v[52:55]\n"
  "ds_read_b64_tr_b16 a[122:123], v218 offset:26768\n"
  "v_add_f32 v254, v254, v35\n"
  "v_mfma_f32_16x16x32_bf16 v[52:55], a[136:139], a[76:79], v[52:55]\n"
  "v_fma_f32 v48, v48, s73, v226\n"
  "v_fma_f32 v49, v49, s73, v226\n"
  "v_mfma_f32_16x16x32_bf16 v[52:55], a[140:143], a[72:75], v[52:55]\n"
  "s_waitcnt lgkmcnt(4)\n"
  "v_exp_f32 v48, v48\n"
  "v_mfma_f32_16x16x32_bf16 a[176:179], v[8:11], a[132:135], a[176:179]\n"
  "v_exp_f32 v49, v49\n"
  "v_mfma_f32_16x16x32_bf16 a[176:179], v[12:15], a[128:131], a[176:179]\n"
  "v_cvt_pk_bf16_f32 v8, v0, v1\n"
  "v_cvt_pk_bf16_f32 v9, v2, v3\n"
  "v_mfma_f32_16x16x32_bf16 a[192:195], v[24:27], a[132:135], a[192:195]\n"
  "ds_read_b64_tr_b16 a[136:137], v219 offset:26752\n"
  "v_cvt_pk_bf16_f32 v24, v16, v17\n"
  "v_mfma_f32_16x16x32_bf16 a[192:195], v[28:31], a[128:131], a[192:195]\n"
  "ds_read_b64_tr_b16 a[138:139], v220 offset:26752\n"
  "v_cvt_pk_bf16_f32 v25, v18, v19\n"
  "v_mfma_f32_16x16x32_bf16 a[208:211], v[40:43], a[132:135], a[208:211]\n"
  "ds_read_b64_tr_b16 a[140:141], v219 offset:26768\n"
  "v_cvt_pk_bf16_f32 v40, v32, v33\n"
  "v_mfma_f32_16x16x32_bf16 a[208:211], v[44:47], a[128:131], a[208:211]\n"
  "ds_read_b64_tr_b16 a[142:143], v220 offset:26768\n"
  "v_cvt_pk_bf16_f32 v41, v34, v35\n"
  "v_mfma_f32_16x16x32_bf16 a[224:227], v[56:59], a[132:135], a[224:227]\n"
  "v_fma_f32 v50, v50, s73, v226\n"
  "v_fma_f32 v51, v51, s73, v226\n"
  "v_mfma_f32_16x16x32_bf16 a[224:227], v[60:63], a[128:131], a[224:227]\n"
  "s_waitcnt lgkmcnt(4)\n"
  "v_exp_f32 v50, v50\n"
  "v_mfma_f32_16x16x32_bf16 a[244:247], a[160:163], a[124:127], a[244:247]\n"
  "v_exp_f32 v51, v51\n"
  "v_mfma_f32_16x16x32_bf16 a[248:251], a[160:163], a[120:123], a[248:251]\n"
  "v_add_f32 v255, v255, v48\n"
  "v_add_f32 v255, v255, v49\n"
  "v_mfma_f32_16x16x32_bf16 v[200:203], v[60:63], a[124:127], v[200:203]\n"
  "v_add_f32 v255, v255, v50\n"
  "v_add_f32 v255, v255, v51\n"
  "v_mfma_f32_16x16x32_bf16 v[204:207], v[60:63], a[120:123], v[204:207]\n"
  "buffer_store_dwordx4 a[252:255], v228, s[52:55], s83 offen offset:0 nt\n"
  "v_cvt_pk_bf16_f32 v56, v48, v49\n"
  "v_cvt_pk_bf16_f32 v57, v50, v51\n"
  "v_fma_f32 v52, v52, s73, v226\n"
  "v_mfma_f32_16x16x32_bf16 v[168:171], v[44:47], a[124:127], v[168:171]\n"
  "v_exp_f32_e64 v52, v52\n"
  "v_mfma_f32_16x16x32_bf16 v[172:175], v[44:47], a[120:123], v[172:175]\n"
  "s_waitcnt vmcnt(13) lgkmcnt(0)\n"
  "s_barrier\n"
  "ds_read_b128 a[132:135], v216 offset:0\n"
  "v_mfma_f32_16x16x32_bf16 v[136:139], v[28:31], a[124:127], v[136:139]\n"
  "ds_read_b64 a[156:157], v231 offset:0\n"
  "v_fma_f32 v53, v53, s73, v226\n"
  "v_mfma_f32_16x16x32_bf16 v[140:143], v[28:31], a[120:123], v[140:143]\n"
  "ds_read_b64 a[158:159], v232 offset:0\n"
  "v_fma_f32 v54, v54, s73, v226\n"
  "v_mfma_f32_16x16x32_bf16 v[104:107], v[12:15], a[124:127], v[104:107]\n"
  "ds_read_b128 a[128:131], v216 offset:64\n"
  "v_fma_f32 v55, v55, s73, v226\n"
  "v_mfma_f32_16x16x32_bf16 v[108:111], v[12:15], a[120:123], v[108:111]\n"
  "ds_read_b128 a[124:127], v216 offset:1024\n"
  "v_fma_f32 v36, v36, s73, v225\n"
  "v_mfma_f32_16x16x32_bf16 v[112:115], v[12:15], a[136:139], v[112:115]\n"
  "ds_read_b128 a[120:123], v216 offset:1088\n"
  "v_fma_f32 v37, v37, s73, v225\n"
  "v_mfma_f32_16x16x32_bf16 v[116:119], v[12:15], a[140:143], v[116:119]\n"
  "s_mov_b32 m0, s68\n"
  "ds_read_b128 a[144:147], v216 offset:2048\n"
  "v_fma_f32 v38, v38, s73, v225\n"
  "v_mfma_f32_16x16x32_bf16 v[144:147], v[28:31], a[136:139], v[144:147]\n"
  "ds_read_b128 a[148:151], v216 offset:2112\n"
  "v_fma_f32 v39, v39, s73, v225\n"
  "v_mfma_f32_16x16x32_bf16 v[148:151], v[28:31], a[140:143], v[148:151]\n"
  "v_exp_f32 v53, v53\n"
  "v_mfma_f32_16x16x32_bf16 v[176:179], v[44:47], a[136:139], v[176:179]\n"
  "v_exp_f32 v54, v54\n"
  "v_mfma_f32_16x16x32_bf16 v[180:183], v[44:47], a[140:143], v[180:183]\n"
  "buffer_load_dwordx4 v221, s[40:43], 0 offen offset:0 lds\n"
  "v_mfma_f32_16x16x32_bf16 v[208:211], v[60:63], a[136:139], v[208:211]\n"
  "s_addk_i32 m0, 896\n"
  "v_exp_f32 v55, v55\n"
  "v_mfma_f32_16x16x32_bf16 v[212:215], v[60:63], a[140:143], v[212:215]\n"
  "s_waitcnt lgkmcnt(4)\n"
  "v_exp_f32 v36, v36\n"
  "v_mfma_f32_16x16x32_bf16 v[64:67], a[132:135], a[96:99], v[248:251]\n"
  "v_exp_f32 v37, v37\n"
  "v_mfma_f32_16x16x32_bf16 v[64:67], a[128:131], a[100:103], v[64:67]\n"
  "s_waitcnt lgkmcnt(2)\n"
  "buffer_load_dwordx4 v221, s[40:43], 0 offen offset:128 lds\n"
  "v_cvt_pk_bf16_f32 v58, v52, v53\n"
  "v_mfma_f32_16x16x32_bf16 v[64:67], a[124:127], a[104:107], v[64:67]\n"
  "s_addk_i32 m0, 896\n"
  "v_exp_f32 v38, v38\n"
  "v_mfma_f32_16x16x32_bf16 v[64:67], a[120:123], a[108:111], v[64:67]\n"
  "s_waitcnt lgkmcnt(0)\n"
  "v_exp_f32 v39, v39\n"
  "v_mfma_f32_16x16x32_bf16 v[64:67], a[144:147], a[112:115], v[64:67]\n"
  "v_cvt_pk_bf16_f32 v59, v54, v55\n"
  "v_cvt_pk_bf16_f32 v42, v36, v37\n"
  "v_mfma_f32_16x16x32_bf16 v[64:67], a[148:151], a[116:119], v[64:67]\n"
  "buffer_load_dwordx4 v221, s[40:43], 0 offen offset:256 lds\n"
  "v_cvt_pk_bf16_f32 v43, v38, v39\n"
  "v_mfma_f32_16x16x32_bf16 v[0:3], a[148:151], a[20:23], v[248:251]\n"
  "v_fma_f32 v20, v20, s73, v224\n"
  "v_fma_f32 v21, v21, s73, v224\n"
  "v_mfma_f32_16x16x32_bf16 v[0:3], a[144:147], a[16:19], v[0:3]\n"
  "s_add_u32 s40, s40, s31\n"
  "v_exp_f32 v20, v20\n"
  "v_mfma_f32_16x16x32_bf16 v[0:3], a[120:123], a[12:15], v[0:3]\n"
  "s_addc_u32 s41, s41, 0\n"
  "v_exp_f32 v21, v21\n"
  "v_mfma_f32_16x16x32_bf16 v[0:3], a[124:127], a[8:11], v[0:3]\n"
  "s_add_u32 m0, s60, -128\n"
  "v_fma_f32 v22, v22, s73, v224\n"
  "v_add_f32 v253, v253, v20\n"
  "v_mfma_f32_16x16x32_bf16 v[0:3], a[128:131], a[4:7], v[0:3]\n"
  "v_exp_f32 v22, v22\n"
  "v_mfma_f32_16x16x32_bf16 v[0:3], a[132:135], a[0:3], v[0:3]\n"
  "v_fma_f32 v23, v23, s73, v224\n"
  "v_cvt_pk_bf16_f32 v26, v20, v21\n"
  "v_mfma_f32_16x16x32_bf16 v[16:19], a[132:135], a[24:27], v[248:251]\n"
  "v_exp_f32 v23, v23\n"
  "v_mfma_f32_16x16x32_bf16 v[16:19], a[128:131], a[28:31], v[16:19]\n"
  "buffer_load_dwordx4 v221, s[32:35], 0 offen offset:128 lds\n"
  "v_cvt_pk_bf16_f32 v27, v22, v23\n"
  "v_mfma_f32_16x16x32_bf16 v[16:19], a[124:127], a[32:35], v[16:19]\n"
  "s_addk_i32 m0, 1024\n"
  "ds_read_b64_tr_b16 a[136:137], v217 offset:0\n"
  "v_fma_f32 v4, v4, s73, v223\n"
  "v_mfma_f32_16x16x32_bf16 v[16:19], a[120:123], a[36:39], v[16:19]\n"
  "s_mov_b64 s[32:33], s[44:45]\n"
  "ds_read_b64_tr_b16 a[138:139], v218 offset:0\n"
  "v_fma_f32 v5, v5, s73, v223\n"
  "v_mfma_f32_16x16x32_bf16 v[16:19], a[144:147], a[40:43], v[16:19]\n"
  "ds_read_b64_tr_b16 a[140:141], v217 offset:1024\n"
  "v_fma_f32 v6, v6, s73, v223\n"
  "v_mfma_f32_16x16x32_bf16 v[16:19], a[148:151], a[44:47], v[16:19]\n"
  "buffer_load_dwordx4 v221, s[44:47], 0 offen offset:128 lds\n"
  "v_fma_f32 v7, v7, s73, v223\n"
  "v_mfma_f32_16x16x32_bf16 v[32:35], a[148:151], a[68:71], v[248:251]\n"
  "s_addk_i32 m0, 896\n"
  "ds_read_b64_tr_b16 a[142:143], v218 offset:1024\n"
  "v_add_f32 v253, v253, v21\n"
  "v_mfma_f32_16x16x32_bf16 v[32:35], a[144:147], a[64:67], v[32:35]\n"
  "v_exp_f32 v4, v4\n"
  "v_mfma_f32_16x16x32_bf16 v[32:35], a[120:123], a[60:63], v[32:35]\n"
  "v_exp_f32 v5, v5\n"
  "v_mfma_f32_16x16x32_bf16 v[32:35], a[124:127], a[56:59], v[32:35]\n"
  "buffer_load_dwordx4 v221, s[44:47], 0 offen offset:256 lds\n"
  "v_cvt_pk_bf16_f32 v10, v4, v5\n"
  "v_mfma_f32_16x16x32_bf16 v[32:35], a[128:131], a[52:55], v[32:35]\n"
  "s_add_u32 s44, s44, s31\n"
  "v_exp_f32 v6, v6\n"
  "v_mfma_f32_16x16x32_bf16 v[32:35], a[132:135], a[48:51], v[32:35]\n"
  "s_addc_u32 s45, s45, 0\n"
  "v_exp_f32 v7, v7\n"
  "v_mfma_f32_16x16x32_bf16 v[48:51], a[132:135], a[72:75], v[248:251]\n"
  "ds_read_b64_tr_b16 a[132:133], v217 offset:16\n"
  "v_cvt_pk_bf16_f32 v11, v6, v7\n"
  "v_mfma_f32_16x16x32_bf16 v[48:51], a[128:131], a[76:79], v[48:51]\n"
  "ds_read_b64_tr_b16 a[134:135], v218 offset:16\n"
  "v_add_f32 v252, v252, v4\n"
  "v_mfma_f32_16x16x32_bf16 v[48:51], a[124:127], a[80:83], v[48:51]\n"
  "ds_read_b64_tr_b16 a[128:129], v217 offset:1040\n"
  "v_add_f32 v252, v252, v5\n"
  "v_mfma_f32_16x16x32_bf16 v[48:51], a[120:123], a[84:87], v[48:51]\n"
  "s_add_u32 m0, s72, 4224\n"
  "ds_read_b64_tr_b16 a[130:131], v218 offset:1040\n"
  "v_add_f32 v252, v252, v6\n"
  "v_mfma_f32_16x16x32_bf16 v[48:51], a[144:147], a[88:91], v[48:51]\n"
  "v_add_f32 v252, v252, v7\n"
  "v_add_f32 v253, v253, v22\n"
  "v_mfma_f32_16x16x32_bf16 v[48:51], a[148:151], a[92:95], v[48:51]\n"
  "s_waitcnt lgkmcnt(4)\n"
  "v_add_f32 v253, v253, v23\n"
  "v_add_f32 v254, v254, v36\n"
  "v_mfma_f32_16x16x32_bf16 a[236:239], a[160:163], a[136:139], a[236:239]\n"
  "v_add_f32 v254, v254, v37\n"
  "v_add_f32 v254, v254, v38\n"
  "v_mfma_f32_16x16x32_bf16 a[236:239], a[156:159], a[140:143], a[236:239]\n"
  "buffer_load_dwordx4 v221, s[48:51], 0 offen offset:256 nt lds\n"
  "v_add_f32 v254, v254, v39\n"
  "v_mfma_f32_16x16x32_bf16 v[184:187], v[60:63], a[136:139], v[184:187]\n"
  "v_add_f32 v255, v255, v52\n"
  "v_add_f32 v255, v255, v53\n"
  "v_mfma_f32_16x16x32_bf16 v[184:187], v[56:59], a[140:143], v[184:187]\n"
  "v_add_f32 v255, v255, v54\n"
  "v_add_f32 v255, v255, v55\n"
  "v_mfma_f32_16x16x32_bf16 v[152:155], v[44:47], a[136:139], v[152:155]\n"
  "ds_read_b64_tr_b16 a[124:125], v219 offset:0\n"
  "v_fma_f32 v64, v64, s73, v227\n"
  "v_mfma_f32_16x16x32_bf16 v[152:155], v[40:43], a[140:143], v[152:155]\n"
  "s_add_u32 m0, s72, 2240\n"
  "ds_read_b64_tr_b16 a[126:127], v220 offset:0\n"
  "v_mfma_f32_16x16x32_bf16 v[120:123], v[28:31], a[136:139], v[120:123]\n"
  "ds_read_b64_tr_b16 a[120:121], v219 offset:1024\n"
  "v_fma_f32 v65, v65, s73, v227\n"
  "v_mfma_f32_16x16x32_bf16 v[120:123], v[24:27], a[140:143], v[120:123]\n"
  "ds_read_b64_tr_b16 a[122:123], v220 offset:1024\n"
  "v_fma_f32 v66, v66, s73, v227\n"
  "v_mfma_f32_16x16x32_bf16 v[88:91], v[12:15], a[136:139], v[88:91]\n"
  "v_exp_f32 v64, v64\n"
  "v_mfma_f32_16x16x32_bf16 v[88:91], v[8:11], a[140:143], v[88:91]\n"
  "s_waitcnt lgkmcnt(4)\n"
  "buffer_load_dwordx4 v221, s[48:51], 0 offen offset:3328 nt lds\n"
  "v_fma_f32 v67, v67, s73, v227\n"
  "v_mfma_f32_16x16x32_bf16 v[92:95], v[12:15], a[132:135], v[92:95]\n"
  "v_exp_f32 v65, v65\n"
  "v_mfma_f32_16x16x32_bf16 v[92:95], v[8:11], a[128:131], v[92:95]\n"
  "v_exp_f32 v66, v66\n"
  "v_mfma_f32_16x16x32_bf16 v[124:127], v[28:31], a[132:135], v[124:127]\n"
  "ds_read_b128 a[136:139], v235 offset:0\n"
  "v_cvt_pk_bf16_f32 v64, v64, v65\n"
  "v_mfma_f32_16x16x32_bf16 v[124:127], v[24:27], a[128:131], v[124:127]\n"
  "ds_read_b128 a[140:143], v235 offset:64\n"
  "v_fma_f32 v0, v0, s73, v223\n"
  "v_mfma_f32_16x16x32_bf16 v[156:159], v[44:47], a[132:135], v[156:159]\n"
  "ds_read_b128 a[144:147], v235 offset:1024\n"
  "v_fma_f32 v1, v1, s73, v223\n"
  "v_mfma_f32_16x16x32_bf16 v[156:159], v[40:43], a[128:131], v[156:159]\n"
  "ds_read_b128 a[148:151], v235 offset:1088\n"
  "v_fma_f32 v2, v2, s73, v223\n"
  "v_mfma_f32_16x16x32_bf16 v[188:191], v[60:63], a[132:135], v[188:191]\n"
  "v_exp_f32 v67, v67\n"
  "v_mfma_f32_16x16x32_bf16 v[188:191], v[56:59], a[128:131], v[188:191]\n"
  "v_exp_f32 v0, v0\n"
  "v_mfma_f32_16x16x32_bf16 a[240:243], a[160:163], a[132:135], a[240:243]\n"
  "ds_read_b128 a[132:135], v235 offset:2048\n"
  "v_cvt_pk_bf16_f32 v65, v66, v67\n"
  "v_mfma_f32_16x16x32_bf16 a[240:243], a[156:159], a[128:131], a[240:243]\n"
  "s_waitcnt lgkmcnt(5)\n"
  "ds_read_b128 a[128:131], v235 offset:2112\n"
  "v_fma_f32 v3, v3, s73, v223\n"
  "v_mfma_f32_16x16x32_bf16 v[192:195], v[60:63], a[124:127], v[192:195]\n"
  "v_exp_f32_e64 v1, v1\n"
  "v_mfma_f32_16x16x32_bf16 v[192:195], v[56:59], a[120:123], v[192:195]\n"
  "ds_write_b64 v231, v[64:65] offset:2048\n"
  "v_mfma_f32_16x16x32_bf16 v[160:163], v[44:47], a[124:127], v[160:163]\n"
  "v_exp_f32 v2, v2\n"
  "v_mfma_f32_16x16x32_bf16 v[160:163], v[40:43], a[120:123], v[160:163]\n"
  "v_exp_f32 v3, v3\n"
  "v_mfma_f32_16x16x32_bf16 v[128:131], v[28:31], a[124:127], v[128:131]\n"
  "v_add_f32 v252, v252, v0\n"
  "v_add_f32 v252, v252, v1\n"
  "v_mfma_f32_16x16x32_bf16 v[128:131], v[24:27], a[120:123], v[128:131]\n"
  "v_add_f32 v252, v252, v2\n"
  "v_add_f32 v252, v252, v3\n"
  "v_mfma_f32_16x16x32_bf16 v[96:99], v[12:15], a[124:127], v[96:99]\n"
  "v_fma_f32 v16, v16, s73, v224\n"
  "v_fma_f32 v17, v17, s73, v224\n"
  "v_mfma_f32_16x16x32_bf16 v[96:99], v[8:11], a[120:123], v[96:99]\n"
  "s_waitcnt lgkmcnt(5)\n"
  "v_exp_f32 v16, v16\n"
  "v_mfma_f32_16x16x32_bf16 v[4:7], a[136:139], a[0:3], v[248:251]\n"
  "v_exp_f32 v17, v17\n"
  "v_mfma_f32_16x16x32_bf16 v[4:7], a[140:143], a[4:7], v[4:7]\n"
  "s_waitcnt lgkmcnt(3)\n"
  "v_fma_f32 v18, v18, s73, v224\n"
  "v_fma_f32 v19, v19, s73, v224\n"
  "v_mfma_f32_16x16x32_bf16 v[4:7], a[144:147], a[8:11], v[4:7]\n"
  "v_exp_f32 v18, v18\n"
  "v_mfma_f32_16x16x32_bf16 v[4:7], a[148:151], a[12:15], v[4:7]\n"
  "s_waitcnt lgkmcnt(1)\n"
  "v_exp_f32 v19, v19\n"
  "v_mfma_f32_16x16x32_bf16 v[4:7], a[132:135], a[16:19], v[4:7]\n"
  "v_add_f32 v253, v253, v16\n"
  "v_add_f32 v253, v253, v17\n"
  "v_mfma_f32_16x16x32_bf16 v[4:7], a[128:131], a[20:23], v[4:7]\n"
  "v_add_f32 v253, v253, v18\n"
  "v_add_f32 v253, v253, v19\n"
  "v_mfma_f32_16x16x32_bf16 v[20:23], a[128:131], a[44:47], v[248:251]\n"
  "v_fma_f32 v32, v32, s73, v225\n"
  "v_fma_f32 v33, v33, s73, v225\n"
  "v_mfma_f32_16x16x32_bf16 v[20:23], a[132:135], a[40:43], v[20:23]\n"
  "v_exp_f32 v32, v32\n"
  "v_mfma_f32_16x16x32_bf16 v[20:23], a[148:151], a[36:39], v[20:23]\n"
  "ds_read_b64_tr_b16 a[124:125], v219 offset:16\n"
  "v_fma_f32 v34, v34, s73, v225\n"
  "v_mfma_f32_16x16x32_bf16 v[20:23], a[144:147], a[32:35], v[20:23]\n"
  "ds_read_b64_tr_b16 a[126:127], v220 offset:16\n"
  "v_fma_f32 v35, v35, s73, v225\n"
  "v_mfma_f32_16x16x32_bf16 v[20:23], a[140:143], a[28:31], v[20:23]\n"
  "ds_read_b64_tr_b16 a[120:121], v219 offset:1040\n"
  "v_add_f32 v254, v254, v32\n"
  "v_mfma_f32_16x16x32_bf16 v[20:23], a[136:139], a[24:27], v[20:23]\n"
  "ds_read_b64_tr_b16 a[122:123], v220 offset:1040\n"
  "v_fma_f32 v48, v48, s73, v226\n"
  "v_mfma_f32_16x16x32_bf16 v[36:39], a[136:139], a[48:51], v[248:251]\n"
  "v_exp_f32 v33, v33\n"
  "v_mfma_f32_16x16x32_bf16 v[36:39], a[140:143], a[52:55], v[36:39]\n"
  "v_exp_f32 v34, v34\n"
  "v_mfma_f32_16x16x32_bf16 v[36:39], a[144:147], a[56:59], v[36:39]\n"
  "v_exp_f32 v35, v35\n"
  "v_mfma_f32_16x16x32_bf16 v[36:39], a[148:151], a[60:63], v[36:39]\n"
  "v_exp_f32 v48, v48\n"
  "v_mfma_f32_16x16x32_bf16 v[36:39], a[132:135], a[64:67], v[36:39]\n"
  "v_add_f32 v254, v254, v33\n"
  "v_add_f32 v254, v254, v34\n"
  "v_mfma_f32_16x16x32_bf16 v[36:39], a[128:131], a[68:71], v[36:39]\n"
  "v_add_f32 v254, v254, v35\n"
  "v_fma_f32 v49, v49, s73, v226\n"
  "v_mfma_f32_16x16x32_bf16 v[52:55], a[128:131], a[92:95], v[248:251]\n"
  "ds_read_b64_tr_b16 a[128:129], v217 offset:2048\n"
  "v_fma_f32 v50, v50, s73, v226\n"
  "v_mfma_f32_16x16x32_bf16 v[52:55], a[132:135], a[88:91], v[52:55]\n"
  "ds_read_b64_tr_b16 a[130:131], v218 offset:2048\n"
  "v_fma_f32 v51, v51, s73, v226\n"
  "v_mfma_f32_16x16x32_bf16 v[52:55], a[148:151], a[84:87], v[52:55]\n"
  "ds_read_b64_tr_b16 a[132:133], v217 offset:2064\n"
  "v_add_f32 v255, v255, v48\n"
  "v_mfma_f32_16x16x32_bf16 v[52:55], a[144:147], a[80:83], v[52:55]\n"
  "ds_read_b64_tr_b16 a[134:135], v218 offset:2064\n"
  "v_fma_f32 v36, v36, s73, v225\n"
  "v_mfma_f32_16x16x32_bf16 v[52:55], a[140:143], a[76:79], v[52:55]\n"
  "v_exp_f32 v49, v49\n"
  "v_mfma_f32_16x16x32_bf16 v[52:55], a[136:139], a[72:75], v[52:55]\n"
  "s_waitcnt lgkmcnt(4)\n"
  "v_exp_f32 v50, v50\n"
  "v_mfma_f32_16x16x32_bf16 v[100:103], v[12:15], a[124:127], v[100:103]\n"
  "v_exp_f32 v51, v51\n"
  "v_mfma_f32_16x16x32_bf16 v[100:103], v[8:11], a[120:123], v[100:103]\n"
  "v_cvt_pk_bf16_f32 v12, v0, v1\n"
  "v_cvt_pk_bf16_f32 v13, v2, v3\n"
  "v_mfma_f32_16x16x32_bf16 v[132:135], v[28:31], a[124:127], v[132:135]\n"
  "ds_read_b64_tr_b16 a[140:141], v219 offset:2048\n"
  "v_cvt_pk_bf16_f32 v28, v16, v17\n"
  "v_mfma_f32_16x16x32_bf16 v[132:135], v[24:27], a[120:123], v[132:135]\n"
  "ds_read_b64_tr_b16 a[142:143], v220 offset:2048\n"
  "v_cvt_pk_bf16_f32 v29, v18, v19\n"
  "v_mfma_f32_16x16x32_bf16 v[164:167], v[44:47], a[124:127], v[164:167]\n"
  "ds_read_b64_tr_b16 a[136:137], v219 offset:2064\n"
  "v_cvt_pk_bf16_f32 v44, v32, v33\n"
  "v_mfma_f32_16x16x32_bf16 v[164:167], v[40:43], a[120:123], v[164:167]\n"
  "ds_read_b64_tr_b16 a[138:139], v220 offset:2064\n"
  "v_cvt_pk_bf16_f32 v45, v34, v35\n"
  "v_mfma_f32_16x16x32_bf16 v[196:199], v[60:63], a[124:127], v[196:199]\n"
  "v_exp_f32 v36, v36\n"
  "v_mfma_f32_16x16x32_bf16 v[196:199], v[56:59], a[120:123], v[196:199]\n"
  "s_waitcnt lgkmcnt(4)\n"
  "v_add_f32 v255, v255, v49\n"
  "v_add_f32 v255, v255, v50\n"
  "v_mfma_f32_16x16x32_bf16 a[244:247], a[156:159], a[128:131], a[244:247]\n"
  "v_add_f32 v255, v255, v51\n"
  "v_cvt_pk_bf16_f32 v60, v48, v49\n"
  "v_mfma_f32_16x16x32_bf16 a[248:251], a[156:159], a[132:135], a[248:251]\n"
  "v_cvt_pk_bf16_f32 v61, v50, v51\n"
  "v_fma_f32 v52, v52, s73, v226\n"
  "v_mfma_f32_16x16x32_bf16 v[200:203], v[56:59], a[128:131], v[200:203]\n"
  "v_exp_f32 v52, v52\n"
  "v_mfma_f32_16x16x32_bf16 v[204:207], v[56:59], a[132:135], v[204:207]\n"
  "v_fma_f32 v53, v53, s73, v226\n"
  "v_fma_f32 v54, v54, s73, v226\n"
  "v_mfma_f32_16x16x32_bf16 v[168:171], v[40:43], a[128:131], v[168:171]\n"
  "v_exp_f32 v53, v53\n"
  "v_mfma_f32_16x16x32_bf16 v[172:175], v[40:43], a[132:135], v[172:175]\n"
  "s_waitcnt vmcnt(11) lgkmcnt(0)\n"
  "s_barrier\n"
  "ds_read_b128 a[124:127], v216 offset:12352\n"
  "v_mfma_f32_16x16x32_bf16 v[136:139], v[24:27], a[128:131], v[136:139]\n"
  "ds_read_b64 a[160:161], v231 offset:2048\n"
  "v_fma_f32 v55, v55, s73, v226\n"
  "v_mfma_f32_16x16x32_bf16 v[140:143], v[24:27], a[132:135], v[140:143]\n"
  "ds_read_b64 a[162:163], v232 offset:2048\n"
  "v_cvt_pk_bf16_f32 v62, v52, v53\n"
  "v_mfma_f32_16x16x32_bf16 v[104:107], v[8:11], a[128:131], v[104:107]\n"
  "ds_read_b128 a[120:123], v216 offset:12416\n"
  "v_fma_f32 v37, v37, s73, v225\n"
  "v_mfma_f32_16x16x32_bf16 v[108:111], v[8:11], a[132:135], v[108:111]\n"
  "ds_read_b128 a[128:131], v216 offset:13376\n"
  "v_fma_f32 v38, v38, s73, v225\n"
  "v_mfma_f32_16x16x32_bf16 v[112:115], v[8:11], a[140:143], v[112:115]\n"
  "ds_read_b128 a[132:135], v216 offset:13440\n"
  "v_fma_f32 v39, v39, s73, v225\n"
  "v_mfma_f32_16x16x32_bf16 v[116:119], v[8:11], a[136:139], v[116:119]\n"
  "s_mov_b32 m0, s66\n"
  "ds_read_b128 a[148:151], v216 offset:14400\n"
  "v_fma_f32 v20, v20, s73, v224\n"
  "v_mfma_f32_16x16x32_bf16 v[144:147], v[24:27], a[140:143], v[144:147]\n"
  "ds_read_b128 a[144:147], v216 offset:14464\n"
  "v_fma_f32 v21, v21, s73, v224\n"
  "v_mfma_f32_16x16x32_bf16 v[148:151], v[24:27], a[136:139], v[148:151]\n"
  "v_exp_f32 v54, v54\n"
  "v_mfma_f32_16x16x32_bf16 v[176:179], v[40:43], a[140:143], v[176:179]\n"
  "v_exp_f32 v55, v55\n"
  "v_mfma_f32_16x16x32_bf16 v[180:183], v[40:43], a[136:139], v[180:183]\n"
  "buffer_load_dwordx4 v221, s[40:43], 0 offen offset:0 lds\n"
  "v_mfma_f32_16x16x32_bf16 v[208:211], v[56:59], a[140:143], v[208:211]\n"
  "s_addk_i32 m0, 896\n"
  "v_exp_f32 v37, v37\n"
  "v_mfma_f32_16x16x32_bf16 v[212:215], v[56:59], a[136:139], v[212:215]\n"
  "s_waitcnt lgkmcnt(4)\n"
  "v_exp_f32 v38, v38\n"
  "v_mfma_f32_16x16x32_bf16 v[64:67], a[124:127], a[96:99], v[248:251]\n"
  "v_exp_f32 v39, v39\n"
  "v_mfma_f32_16x16x32_bf16 v[64:67], a[120:123], a[100:103], v[64:67]\n"
  "s_waitcnt lgkmcnt(2)\n"
  "buffer_load_dwordx4 v221, s[40:43], 0 offen offset:128 lds\n"
  "v_cvt_pk_bf16_f32 v63, v54, v55\n"
  "v_mfma_f32_16x16x32_bf16 v[64:67], a[128:131], a[104:107], v[64:67]\n"
  "s_addk_i32 m0, 896\n"
  "v_exp_f32 v20, v20\n"
  "v_mfma_f32_16x16x32_bf16 v[64:67], a[132:135], a[108:111], v[64:67]\n"
  "s_waitcnt lgkmcnt(0)\n"
  "v_exp_f32 v21, v21\n"
  "v_mfma_f32_16x16x32_bf16 v[64:67], a[148:151], a[112:115], v[64:67]\n"
  "v_cvt_pk_bf16_f32 v46, v36, v37\n"
  "v_cvt_pk_bf16_f32 v47, v38, v39\n"
  "v_mfma_f32_16x16x32_bf16 v[64:67], a[144:147], a[116:119], v[64:67]\n"
  "buffer_load_dwordx4 v221, s[40:43], 0 offen offset:256 lds\n"
  "v_fma_f32 v22, v22, s73, v224\n"
  "v_mfma_f32_16x16x32_bf16 v[0:3], a[144:147], a[20:23], v[248:251]\n"
  "s_add_u32 s40, s40, s31\n"
  "v_exp_f32 v22, v22\n"
  "v_mfma_f32_16x16x32_bf16 v[0:3], a[148:151], a[16:19], v[0:3]\n"
  "s_addc_u32 s41, s41, 0\n"
  "v_mbcnt_lo_u32_b32 v236, -1, 0\n"
  "v_mbcnt_hi_u32_b32 v236, -1, v236\n"
  "v_mfma_f32_16x16x32_bf16 v[0:3], a[132:135], a[12:15], v[0:3]\n"
  "s_sub_u32 s79, s96, s39\n"
  "v_mul_u32_u24 v236, 0x18c, v236\n"
  "v_fma_f32 v23, v23, s73, v224\n"
  "v_mfma_f32_16x16x32_bf16 v[0:3], a[128:131], a[8:11], v[0:3]\n"
  "s_mov_b32 m0, s58\n"
  "v_exp_f32 v23, v23\n"
  "v_mfma_f32_16x16x32_bf16 v[0:3], a[120:123], a[4:7], v[0:3]\n"
  "v_cvt_pk_bf16_f32 v30, v20, v21\n"
  "v_cvt_pk_bf16_f32 v31, v22, v23\n"
  "v_mfma_f32_16x16x32_bf16 v[0:3], a[124:127], a[0:3], v[0:3]\n"
  "v_fma_f32 v4, v4, s73, v223\n"
  "v_fma_f32 v5, v5, s73, v223\n"
  "v_mfma_f32_16x16x32_bf16 v[16:19], a[124:127], a[24:27], v[248:251]\n"
  "v_exp_f32 v4, v4\n"
  "v_mfma_f32_16x16x32_bf16 v[16:19], a[120:123], a[28:31], v[16:19]\n"
  "buffer_load_dwordx4 v221, s[32:35], 0 offen offset:0 lds\n"
  "v_fma_f32 v6, v6, s73, v223\n"
  "v_mfma_f32_16x16x32_bf16 v[16:19], a[128:131], a[32:35], v[16:19]\n"
  "s_addk_i32 m0, 1024\n"
  "ds_read_b64_tr_b16 a[140:141], v217 offset:12352\n"
  "v_fma_f32 v7, v7, s73, v223\n"
  "v_mfma_f32_16x16x32_bf16 v[16:19], a[132:135], a[36:39], v[16:19]\n"
  "s_mov_b64 s[32:33], s[44:45]\n"
  "ds_read_b64_tr_b16 a[142:143], v218 offset:12352\n"
  "v_add_f32 v252, v252, v4\n"
  "v_mfma_f32_16x16x32_bf16 v[16:19], a[148:151], a[40:43], v[16:19]\n"
  "ds_read_b64_tr_b16 a[136:137], v217 offset:13376\n"
  "v_add_f32 v253, v253, v20\n"
  "v_mfma_f32_16x16x32_bf16 v[16:19], a[144:147], a[44:47], v[16:19]\n"
  "buffer_load_dwordx4 v221, s[44:47], 0 offen offset:0 lds\n"
  "v_add_f32 v253, v253, v21\n"
  "v_mfma_f32_16x16x32_bf16 v[32:35], a[144:147], a[68:71], v[248:251]\n"
  "s_addk_i32 m0, 768\n"
  "ds_read_b64_tr_b16 a[138:139], v218 offset:13376\n"
  "v_add_f32 v253, v253, v22\n"
  "v_mfma_f32_16x16x32_bf16 v[32:35], a[148:151], a[64:67], v[32:35]\n"
  "v_exp_f32 v5, v5\n"
  "v_mfma_f32_16x16x32_bf16 v[32:35], a[132:135], a[60:63], v[32:35]\n"
  "v_exp_f32 v6, v6\n"
  "v_mfma_f32_16x16x32_bf16 v[32:35], a[128:131], a[56:59], v[32:35]\n"
  "buffer_load_dwordx4 v221, s[44:47], 0 offen offset:256 lds\n"
  "v_cvt_pk_bf16_f32 v14, v4, v5\n"
  "v_mfma_f32_16x16x32_bf16 v[32:35], a[120:123], a[52:55], v[32:35]\n"
  "s_add_u32 s44, s44, s31\n"
  "v_exp_f32 v7, v7\n"
  "v_mfma_f32_16x16x32_bf16 v[32:35], a[124:127], a[48:51], v[32:35]\n"
  "s_addc_u32 s45, s45, 0\n"
  "v_cvt_pk_bf16_f32 v15, v6, v7\n"
  "v_add_f32 v252, v252, v5\n"
  "v_mfma_f32_16x16x32_bf16 v[48:51], a[124:127], a[72:75], v[248:251]\n"
  "s_mov_b32 m0, s29\n"
  "v_add_f32 v252, v252, v6\n"
  "v_add_f32 v252, v252, v7\n"
  "v_mfma_f32_16x16x32_bf16 v[48:51], a[120:123], a[76:79], v[48:51]\n"
  "v_add_f32 v253, v253, v23\n"
  "v_add_f32 v254, v254, v36\n"
  "v_mfma_f32_16x16x32_bf16 v[48:51], a[128:131], a[80:83], v[48:51]\n"
  "ds_read_b64_tr_b16 a[124:125], v217 offset:12368\n"
  "v_add_f32 v254, v254, v37\n"
  "v_mfma_f32_16x16x32_bf16 v[48:51], a[132:135], a[84:87], v[48:51]\n"
  "buffer_load_dword v236, s[88:91], s79 offen lds\n"
  "v_add_f32 v254, v254, v38\n"
  "v_mfma_f32_16x16x32_bf16 v[48:51], a[148:151], a[88:91], v[48:51]\n"
  "ds_read_b64_tr_b16 a[126:127], v218 offset:12368\n"
  "v_add_f32 v254, v254, v39\n"
  "v_mfma_f32_16x16x32_bf16 v[48:51], a[144:147], a[92:95], v[48:51]\n"
  "s_waitcnt lgkmcnt(2)\n"
  "ds_read_b64_tr_b16 a[120:121], v217 offset:13392\n"
  "v_add_f32 v255, v255, v52\n"
  "v_mfma_f32_16x16x32_bf16 a[228:231], a[156:159], a[140:143], a[228:231]\n"
  "ds_read_b64_tr_b16 a[122:123], v218 offset:13392\n"
  "v_add_f32 v255, v255, v53\n"
  "v_mfma_f32_16x16x32_bf16 a[228:231], a[160:163], a[136:139], a[228:231]\n"
  "v_add_f32 v255, v255, v54\n"
  "v_add_f32 v255, v255, v55\n"
  "v_mfma_f32_16x16x32_bf16 a[212:215], v[56:59], a[140:143], a[212:215]\n"
  "ds_read_b64_tr_b16 v[244:245], v230 offset:7840\n"
  "ds_read_b64_tr_b16 v[246:247], v230 offset:7872\n"
  "v_mfma_f32_16x16x32_bf16 a[212:215], v[60:63], a[136:139], a[212:215]\n"
  "ds_read_b64_tr_b16 a[252:253], v230 offset:17920\n"
  "ds_read_b64_tr_b16 a[254:255], v230 offset:17952\n"
  "v_mfma_f32_16x16x32_bf16 a[196:199], v[40:43], a[140:143], a[196:199]\n"
  "v_fma_f32 v64, v64, s73, v227\n"
  "v_fma_f32 v65, v65, s73, v227\n"
  "v_mfma_f32_16x16x32_bf16 a[196:199], v[44:47], a[136:139], a[196:199]\n"
  "v_exp_f32 v64, v64\n"
  "v_mfma_f32_16x16x32_bf16 a[180:183], v[24:27], a[140:143], a[180:183]\n"
  "ds_read_b64_tr_b16 a[128:129], v219 offset:12352\n"
  "v_fma_f32 v66, v66, s73, v227\n"
  "v_mfma_f32_16x16x32_bf16 a[180:183], v[28:31], a[136:139], a[180:183]\n"
  "ds_read_b64_tr_b16 a[130:131], v220 offset:12352\n"
  "v_fma_f32 v67, v67, s73, v227\n"
  "v_mfma_f32_16x16x32_bf16 a[164:167], v[8:11], a[140:143], a[164:167]\n"
  "ds_read_b64_tr_b16 a[132:133], v219 offset:13376\n"
  "v_fma_f32 v0, v0, s73, v223\n"
  "v_mfma_f32_16x16x32_bf16 a[164:167], v[12:15], a[136:139], a[164:167]\n"
  "ds_read_b64_tr_b16 a[134:135], v220 offset:13376\n"
  "v_fma_f32 v1, v1, s73, v223\n"
  "v_mfma_f32_16x16x32_bf16 v[84:87], a[156:159], a[152:155], v[84:87]\n"
  "v_exp_f32 v65, v65\n"
  "v_mfma_f32_16x16x32_bf16 v[84:87], a[160:163], a[152:155], v[84:87]\n"
  "s_waitcnt lgkmcnt(8)\n"
  "v_exp_f32 v66, v66\n"
  "v_mfma_f32_16x16x32_bf16 a[168:171], v[8:11], a[124:127], a[168:171]\n"
  "v_exp_f32 v67, v67\n"
  "v_mfma_f32_16x16x32_bf16 a[168:171], v[12:15], a[120:123], a[168:171]\n"
  "v_exp_f32 v0, v0\n"
  "v_mfma_f32_16x16x32_bf16 a[184:187], v[24:27], a[124:127], a[184:187]\n"
  "ds_read_b128 a[140:143], v235 offset:12352\n"
  "v_cvt_pk_bf16_f32 v64, v64, v65\n"
  "v_mfma_f32_16x16x32_bf16 a[184:187], v[28:31], a[120:123], a[184:187]\n"
  "ds_read_b128 a[136:139], v235 offset:12416\n"
  "v_cvt_pk_bf16_f32 v65, v66, v67\n"
  "v_mfma_f32_16x16x32_bf16 a[200:203], v[40:43], a[124:127], a[200:203]\n"
  "ds_read_b128 a[148:151], v235 offset:13376\n"
  "v_fma_f32 v2, v2, s73, v223\n"
  "v_mfma_f32_16x16x32_bf16 a[200:203], v[44:47], a[120:123], a[200:203]\n"
  "ds_read_b128 a[144:147], v235 offset:13440\n"
  "v_fma_f32 v3, v3, s73, v223\n"
  "v_mfma_f32_16x16x32_bf16 a[216:219], v[56:59], a[124:127], a[216:219]\n"
  "v_exp_f32 v1, v1\n"
  "v_mfma_f32_16x16x32_bf16 a[216:219], v[60:63], a[120:123], a[216:219]\n"
  "v_exp_f32 v2, v2\n"
  "v_mfma_f32_16x16x32_bf16 a[232:235], a[156:159], a[124:127], a[232:235]\n"
  "ds_read_b128 a[124:127], v235 offset:14400\n"
  "v_add_f32 v252, v252, v0\n"
  "v_mfma_f32_16x16x32_bf16 a[232:235], a[160:163], a[120:123], a[232:235]\n"
  "s_waitcnt lgkmcnt(5)\n"
  "ds_read_b128 a[120:123], v235 offset:14464\n"
  "v_add_f32 v252, v252, v1\n"
  "v_mfma_f32_16x16x32_bf16 a[220:223], v[56:59], a[128:131], a[220:223]\n"
  "v_exp_f32 v3, v3\n"
  "v_mfma_f32_16x16x32_bf16 a[220:223], v[60:63], a[132:135], a[220:223]\n"
  "ds_write_b64 v231, v[64:65] offset:0\n"
  "v_mfma_f32_16x16x32_bf16 a[204:207], v[40:43], a[128:131], a[204:207]\n"
  "v_add_f32 v252, v252, v2\n"
  "v_add_f32 v252, v252, v3\n"
  "v_mfma_f32_16x16x32_bf16 a[204:207], v[44:47], a[132:135], a[204:207]\n"
  "buffer_store_dwordx4 v[244:247], v228, s[52:55], s83 offen offset:3072 nt\n"
  "v_fma_f32 v16, v16, s73, v224\n"
  "v_exp_f32 v16, v16\n"
  "v_mfma_f32_16x16x32_bf16 a[188:191], v[24:27], a[128:131], a[188:191]\n"
  "ds_read_b64_tr_b16 v[244:245], v230 offset:19040\n"
  "ds_read_b64_tr_b16 v[246:247], v230 offset:19072\n"
  "v_mfma_f32_16x16x32_bf16 a[188:191], v[28:31], a[132:135], a[188:191]\n"
  "v_fma_f32 v17, v17, s73, v224\n"
  "v_fma_f32 v18, v18, s73, v224\n"
  "v_mfma_f32_16x16x32_bf16 a[172:175], v[8:11], a[128:131], a[172:175]\n"
  "v_exp_f32 v17, v17\n"
  "v_mfma_f32_16x16x32_bf16 a[172:175], v[12:15], a[132:135], a[172:175]\n"
  "s_waitcnt lgkmcnt(7)\n"
  "v_exp_f32 v18, v18\n"
  "v_mfma_f32_16x16x32_bf16 v[4:7], a[140:143], a[0:3], v[248:251]\n"
  "v_fma_f32 v19, v19, s73, v224\n"
  "v_add_f32 v253, v253, v16\n"
  "v_mfma_f32_16x16x32_bf16 v[4:7], a[136:139], a[4:7], v[4:7]\n"
  "s_waitcnt lgkmcnt(5)\n"
  "v_exp_f32 v19, v19\n"
  "v_mfma_f32_16x16x32_bf16 v[4:7], a[148:151], a[8:11], v[4:7]\n"
  "v_add_f32 v253, v253, v17\n"
  "v_add_f32 v253, v253, v18\n"
  "v_mfma_f32_16x16x32_bf16 v[4:7], a[144:147], a[12:15], v[4:7]\n"
  "s_waitcnt lgkmcnt(3)\n"
  "v_add_f32 v253, v253, v19\n"
  "v_fma_f32 v32, v32, s73, v225\n"
  "v_mfma_f32_16x16x32_bf16 v[4:7], a[124:127], a[16:19], v[4:7]\n"
  "v_exp_f32 v32, v32\n"
  "v_mfma_f32_16x16x32_bf16 v[4:7], a[120:123], a[20:23], v[4:7]\n"
  "v_fma_f32 v33, v33, s73, v225\n"
  "v_fma_f32 v34, v34, s73, v225\n"
  "v_mfma_f32_16x16x32_bf16 v[20:23], a[120:123], a[44:47], v[248:251]\n"
  "v_exp_f32 v33, v33\n"
  "v_mfma_f32_16x16x32_bf16 v[20:23], a[124:127], a[40:43], v[20:23]\n"
  "v_exp_f32 v34, v34\n"
  "v_mfma_f32_16x16x32_bf16 v[20:23], a[144:147], a[36:39], v[20:23]\n"
  "ds_read_b64_tr_b16 a[128:129], v219 offset:12368\n"
  "v_fma_f32 v35, v35, s73, v225\n"
  "v_mfma_f32_16x16x32_bf16 v[20:23], a[148:151], a[32:35], v[20:23]\n"
  "ds_read_b64_tr_b16 a[130:131], v220 offset:12368\n"
  "v_add_f32 v254, v254, v32\n"
  "v_mfma_f32_16x16x32_bf16 v[20:23], a[136:139], a[28:31], v[20:23]\n"
  "ds_read_b64_tr_b16 a[132:133], v219 offset:13392\n"
  "v_add_f32 v254, v254, v33\n"
  "v_mfma_f32_16x16x32_bf16 v[20:23], a[140:143], a[24:27], v[20:23]\n"
  "ds_read_b64_tr_b16 a[134:135], v220 offset:13392\n"
  "v_add_f32 v254, v254, v34\n"
  "v_mfma_f32_16x16x32_bf16 v[36:39], a[140:143], a[48:51], v[248:251]\n"
  "v_exp_f32 v35, v35\n"
  "v_mfma_f32_16x16x32_bf16 v[36:39], a[136:139], a[52:55], v[36:39]\n"
  "v_add_f32 v254, v254, v35\n"
  "v_fma_f32 v48, v48, s73, v226\n"
  "v_mfma_f32_16x16x32_bf16 v[36:39], a[148:151], a[56:59], v[36:39]\n"
  "v_exp_f32 v48, v48\n"
  "v_mfma_f32_16x16x32_bf16 v[36:39], a[144:147], a[60:63], v[36:39]\n"
  "v_fma_f32 v49, v49, s73, v226\n"
  "v_fma_f32 v50, v50, s73, v226\n"
  "v_mfma_f32_16x16x32_bf16 v[36:39], a[124:127], a[64:67], v[36:39]\n"
  "v_exp_f32_e64 v49, v49\n"
  "v_mfma_f32_16x16x32_bf16 v[36:39], a[120:123], a[68:71], v[36:39]\n"
  "buffer_store_dwordx4 a[252:255], v228, s[52:55], s56 offen offset:0 nt\n"
  "v_exp_f32_e64 v50, v50\n"
  "v_fma_f32 v51, v51, s73, v226\n"
  "v_mfma_f32_16x16x32_bf16 v[52:55], a[120:123], a[92:95], v[248:251]\n"
  "ds_read_b64_tr_b16 a[120:121], v217 offset:14400\n"
  "v_add_f32 v255, v255, v48\n"
  "v_mfma_f32_16x16x32_bf16 v[52:55], a[124:127], a[88:91], v[52:55]\n"
  "ds_read_b64_tr_b16 a[122:123], v218 offset:14400\n"
  "v_add_f32 v255, v255, v49\n"
  "v_mfma_f32_16x16x32_bf16 v[52:55], a[144:147], a[84:87], v[52:55]\n"
  "ds_read_b64_tr_b16 a[124:125], v217 offset:14416\n"
  "v_add_f32 v255, v255, v50\n"
  "v_mfma_f32_16x16x32_bf16 v[52:55], a[148:151], a[80:83], v[52:55]\n"
  "ds_read_b64_tr_b16 a[126:127], v218 offset:14416\n"
  "v_fma_f32 v36, v36, s73, v225\n"
  "v_mfma_f32_16x16x32_bf16 v[52:55], a[136:139], a[76:79], v[52:55]\n"
  "v_exp_f32 v51, v51\n"
  "v_mfma_f32_16x16x32_bf16 v[52:55], a[140:143], a[72:75], v[52:55]\n"
  "s_waitcnt lgkmcnt(4)\n"
  "v_exp_f32 v36, v36\n"
  "v_mfma_f32_16x16x32_bf16 a[176:179], v[8:11], a[128:131], a[176:179]\n"
  "v_cvt_pk_bf16_f32 v8, v0, v1\n"
  "v_cvt_pk_bf16_f32 v9, v2, v3\n"
  "v_mfma_f32_16x16x32_bf16 a[176:179], v[12:15], a[132:135], a[176:179]\n"
  "v_add_f32 v255, v255, v51\n"
  "v_fma_f32 v37, v37, s73, v225\n"
  "v_mfma_f32_16x16x32_bf16 a[192:195], v[24:27], a[128:131], a[192:195]\n"
  "ds_read_b64_tr_b16 a[136:137], v219 offset:14400\n"
  "v_cvt_pk_bf16_f32 v24, v16, v17\n"
  "v_mfma_f32_16x16x32_bf16 a[192:195], v[28:31], a[132:135], a[192:195]\n"
  "ds_read_b64_tr_b16 a[138:139], v220 offset:14400\n"
  "v_cvt_pk_bf16_f32 v25, v18, v19\n"
  "v_mfma_f32_16x16x32_bf16 a[208:211], v[40:43], a[128:131], a[208:211]\n"
  "ds_read_b64_tr_b16 a[140:141], v219 offset:14416\n"
  "v_cvt_pk_bf16_f32 v40, v32, v33\n"
  "v_mfma_f32_16x16x32_bf16 a[208:211], v[44:47], a[132:135], a[208:211]\n"
  "ds_read_b64_tr_b16 a[142:143], v220 offset:14416\n"
  "v_cvt_pk_bf16_f32 v41, v34, v35\n"
  "v_mfma_f32_16x16x32_bf16 a[224:227], v[56:59], a[128:131], a[224:227]\n"
  "v_exp_f32 v37, v37\n"
  "v_mfma_f32_16x16x32_bf16 a[224:227], v[60:63], a[132:135], a[224:227]\n"
  "s_waitcnt lgkmcnt(4)\n"
  "v_cvt_pk_bf16_f32 v56, v48, v49\n"
  "v_cvt_pk_bf16_f32 v57, v50, v51\n"
  "v_mfma_f32_16x16x32_bf16 a[244:247], a[160:163], a[120:123], a[244:247]\n"
  "v_fma_f32 v52, v52, s73, v226\n"
  "v_fma_f32 v53, v53, s73, v226\n"
  "v_mfma_f32_16x16x32_bf16 a[248:251], a[160:163], a[124:127], a[248:251]\n"
  "v_exp_f32 v52, v52\n"
  "v_mfma_f32_16x16x32_bf16 v[200:203], v[60:63], a[120:123], v[200:203]\n"
  "v_exp_f32_e64 v53, v53\n"
  "v_mfma_f32_16x16x32_bf16 v[204:207], v[60:63], a[124:127], v[204:207]\n"
  "buffer_store_dwordx4 v[244:247], v228, s[52:55], s56 offen offset:128 nt\n"
  "v_fma_f32 v54, v54, s73, v226\n"
  "v_exp_f32_e64 v54, v54\n"
  "v_mfma_f32_16x16x32_bf16 v[168:171], v[44:47], a[120:123], v[168:171]\n"
  "v_fma_f32 v55, v55, s73, v226\n"
  "v_cvt_pk_bf16_f32 v58, v52, v53\n"
  "v_mfma_f32_16x16x32_bf16 v[172:175], v[44:47], a[124:127], v[172:175]\n"
  "s_waitcnt vmcnt(12) lgkmcnt(0)\n"
  "s_barrier\n"
  "ds_read_b128 a[128:131], v216 offset:24704\n"
  "v_mfma_f32_16x16x32_bf16 v[136:139], v[28:31], a[120:123], v[136:139]\n"
  "ds_read_b64 a[156:157], v231 offset:0\n"
  "v_fma_f32 v38, v38, s73, v225\n"
  "v_mfma_f32_16x16x32_bf16 v[140:143], v[28:31], a[124:127], v[140:143]\n"
  "ds_read_b64 a[158:159], v232 offset:0\n"
  "v_fma_f32 v39, v39, s73, v225\n"
  "v_mfma_f32_16x16x32_bf16 v[104:107], v[12:15], a[120:123], v[104:107]\n"
  "ds_read_b128 a[132:135], v216 offset:24768\n"
  "v_cvt_pk_bf16_f32 v42, v36, v37\n"
  "v_mfma_f32_16x16x32_bf16 v[108:111], v[12:15], a[124:127], v[108:111]\n"
  "ds_read_b128 a[120:123], v216 offset:25728\n"
  "v_fma_f32 v20, v20, s73, v224\n"
  "v_mfma_f32_16x16x32_bf16 v[112:115], v[12:15], a[136:139], v[112:115]\n"
  "s_add_u32 s71, s92, s81\n"
  "ds_read_b128 a[124:127], v216 offset:25792\n"
  "v_fma_f32 v21, v21, s73, v224\n"
  "v_mfma_f32_16x16x32_bf16 v[116:119], v[12:15], a[140:143], v[116:119]\n"
  "s_mov_b32 m0, s67\n"
  "ds_read_b128 a[144:147], v216 offset:26752\n"
  "v_fma_f32 v22, v22, s73, v224\n"
  "v_mfma_f32_16x16x32_bf16 v[144:147], v[28:31], a[136:139], v[144:147]\n"
  "s_add_u32 s48, s84, s71\n"
  "ds_read_b128 a[148:151], v216 offset:26816\n"
  "v_fma_f32 v23, v23, s73, v224\n"
  "v_mfma_f32_16x16x32_bf16 v[148:151], v[28:31], a[140:143], v[148:151]\n"
  "s_addc_u32 s49, s85, 0\n"
  "v_exp_f32 v55, v55\n"
  "v_mfma_f32_16x16x32_bf16 v[176:179], v[44:47], a[136:139], v[176:179]\n"
  "s_sub_i32 s71, s38, s81\n"
  "v_exp_f32 v38, v38\n"
  "v_mfma_f32_16x16x32_bf16 v[180:183], v[44:47], a[140:143], v[180:183]\n"
  "buffer_load_dwordx4 v221, s[40:43], 0 offen offset:0 lds\n"
  "v_mfma_f32_16x16x32_bf16 v[208:211], v[60:63], a[136:139], v[208:211]\n"
  "s_addk_i32 m0, 896\n"
  "v_exp_f32 v39, v39\n"
  "v_mfma_f32_16x16x32_bf16 v[212:215], v[60:63], a[140:143], v[212:215]\n"
  "s_waitcnt lgkmcnt(4)\n"
  "v_exp_f32 v20, v20\n"
  "v_mfma_f32_16x16x32_bf16 v[64:67], a[128:131], a[96:99], v[248:251]\n"
  "s_max_i32 s50, s71, 0\n"
  "v_exp_f32 v21, v21\n"
  "v_mfma_f32_16x16x32_bf16 v[64:67], a[132:135], a[100:103], v[64:67]\n"
  "s_waitcnt lgkmcnt(2)\n"
  "buffer_load_dwordx4 v221, s[40:43], 0 offen offset:128 lds\n"
  "v_cvt_pk_bf16_f32 v59, v54, v55\n"
  "v_mfma_f32_16x16x32_bf16 v[64:67], a[120:123], a[104:107], v[64:67]\n"
  "s_addk_i32 m0, 896\n"
  "v_exp_f32 v22, v22\n"
  "v_mfma_f32_16x16x32_bf16 v[64:67], a[124:127], a[108:111], v[64:67]\n"
  "s_waitcnt lgkmcnt(0)\n"
  "v_exp_f32 v23, v23\n"
  "v_mfma_f32_16x16x32_bf16 v[64:67], a[144:147], a[112:115], v[64:67]\n"
  "v_cvt_pk_bf16_f32 v43, v38, v39\n"
  "v_cvt_pk_bf16_f32 v26, v20, v21\n"
  "v_mfma_f32_16x16x32_bf16 v[64:67], a[148:151], a[116:119], v[64:67]\n"
  "buffer_load_dwordx4 v221, s[40:43], 0 offen offset:256 lds\n"
  "v_cvt_pk_bf16_f32 v27, v22, v23\n"
  "v_mfma_f32_16x16x32_bf16 v[0:3], a[148:151], a[20:23], v[248:251]\n"
  "v_fma_f32 v4, v4, s73, v223\n"
  "v_fma_f32 v5, v5, s73, v223\n"
  "v_mfma_f32_16x16x32_bf16 v[0:3], a[144:147], a[16:19], v[0:3]\n"
  "s_add_u32 s71, s92, s56\n"
  "v_exp_f32 v4, v4\n"
  "v_mfma_f32_16x16x32_bf16 v[0:3], a[124:127], a[12:15], v[0:3]\n"
  "s_add_u32 s40, s40, s31\n"
  "v_exp_f32 v5, v5\n"
  "v_mfma_f32_16x16x32_bf16 v[0:3], a[120:123], a[8:11], v[0:3]\n"
  "s_addc_u32 s41, s41, 0\n"
  "v_fma_f32 v6, v6, s73, v223\n"
  "v_fma_f32 v7, v7, s73, v223\n"
  "v_mfma_f32_16x16x32_bf16 v[0:3], a[132:135], a[4:7], v[0:3]\n"
  "s_add_u32 m0, s59, -128\n"
  "v_exp_f32 v6, v6\n"
  "v_mfma_f32_16x16x32_bf16 v[0:3], a[128:131], a[0:3], v[0:3]\n"
  "v_exp_f32 v7, v7\n"
  "v_mfma_f32_16x16x32_bf16 v[16:19], a[128:131], a[24:27], v[248:251]\n"
  "v_cvt_pk_bf16_f32 v10, v4, v5\n"
  "v_cvt_pk_bf16_f32 v11, v6, v7\n"
  "v_mfma_f32_16x16x32_bf16 v[16:19], a[132:135], a[28:31], v[16:19]\n"
  "buffer_load_dwordx4 v221, s[32:35], 0 offen offset:128 lds\n"
  "v_add_f32 v252, v252, v4\n"
  "v_mfma_f32_16x16x32_bf16 v[16:19], a[120:123], a[32:35], v[16:19]\n"
  "s_addk_i32 m0, 1024\n"
  "ds_read_b64_tr_b16 a[136:137], v217 offset:24704\n"
  "v_add_f32 v252, v252, v5\n"
  "v_mfma_f32_16x16x32_bf16 v[16:19], a[124:127], a[36:39], v[16:19]\n"
  "s_mov_b64 s[32:33], s[44:45]\n"
  "ds_read_b64_tr_b16 a[138:139], v218 offset:24704\n"
  "v_add_f32 v252, v252, v6\n"
  "v_mfma_f32_16x16x32_bf16 v[16:19], a[144:147], a[40:43], v[16:19]\n"
  "ds_read_b64_tr_b16 a[140:141], v217 offset:25728\n"
  "v_add_f32 v252, v252, v7\n"
  "v_mfma_f32_16x16x32_bf16 v[16:19], a[148:151], a[44:47], v[16:19]\n"
  "buffer_load_dwordx4 v221, s[44:47], 0 offen offset:128 lds\n"
  "v_add_f32 v253, v253, v20\n"
  "v_mfma_f32_16x16x32_bf16 v[32:35], a[148:151], a[68:71], v[248:251]\n"
  "s_addk_i32 m0, 896\n"
  "ds_read_b64_tr_b16 a[142:143], v218 offset:25728\n"
  "v_add_f32 v253, v253, v21\n"
  "v_mfma_f32_16x16x32_bf16 v[32:35], a[144:147], a[64:67], v[32:35]\n"
  "v_add_f32 v253, v253, v22\n"
  "v_add_f32 v253, v253, v23\n"
  "v_mfma_f32_16x16x32_bf16 v[32:35], a[124:127], a[60:63], v[32:35]\n"
  "v_add_f32 v254, v254, v36\n"
  "v_add_f32 v254, v254, v37\n"
  "v_mfma_f32_16x16x32_bf16 v[32:35], a[120:123], a[56:59], v[32:35]\n"
  "buffer_load_dwordx4 v221, s[44:47], 0 offen offset:256 lds\n"
  "v_add_f32 v254, v254, v38\n"
  "v_mfma_f32_16x16x32_bf16 v[32:35], a[132:135], a[52:55], v[32:35]\n"
  "s_add_u32 s44, s44, s31\n"
  "v_add_f32 v254, v254, v39\n"
  "v_add_f32 v255, v255, v52\n"
  "v_mfma_f32_16x16x32_bf16 v[32:35], a[128:131], a[48:51], v[32:35]\n"
  "s_addc_u32 s45, s45, 0\n"
  "v_add_f32 v255, v255, v53\n"
  "v_add_f32 v255, v255, v54\n"
  "v_mfma_f32_16x16x32_bf16 v[48:51], a[128:131], a[72:75], v[248:251]\n"
  "ds_read_b64_tr_b16 a[128:129], v217 offset:24720\n"
  "v_add_f32 v255, v255, v55\n"
  "v_mfma_f32_16x16x32_bf16 v[48:51], a[132:135], a[76:79], v[48:51]\n"
  "ds_read_b64_tr_b16 a[130:131], v218 offset:24720\n"
  "v_fma_f32 v64, v64, s73, v227\n"
  "v_mfma_f32_16x16x32_bf16 v[48:51], a[120:123], a[80:83], v[48:51]\n"
  "ds_read_b64_tr_b16 a[132:133], v217 offset:25744\n"
  "v_fma_f32 v65, v65, s73, v227\n"
  "v_mfma_f32_16x16x32_bf16 v[48:51], a[124:127], a[84:87], v[48:51]\n"
  "s_add_u32 m0, s72, 6464\n"
  "ds_read_b64_tr_b16 a[134:135], v218 offset:25744\n"
  "v_mfma_f32_16x16x32_bf16 v[48:51], a[144:147], a[88:91], v[48:51]\n"
  "v_exp_f32 v64, v64\n"
  "v_mfma_f32_16x16x32_bf16 v[48:51], a[148:151], a[92:95], v[48:51]\n"
  "s_waitcnt lgkmcnt(4)\n"
  "v_exp_f32 v65, v65\n"
  "v_mfma_f32_16x16x32_bf16 a[236:239], a[160:163], a[136:139], a[236:239]\n"
  "v_fma_f32 v66, v66, s73, v227\n"
  "v_fma_f32 v67, v67, s73, v227\n"
  "v_mfma_f32_16x16x32_bf16 a[236:239], a[156:159], a[140:143], a[236:239]\n"
  "buffer_load_dwordx4 v221, s[48:51], 0 offen offset:256 nt lds\n"
  "v_cvt_pk_bf16_f32 v64, v64, v65\n"
  "v_mfma_f32_16x16x32_bf16 v[184:187], v[60:63], a[136:139], v[184:187]\n"
  "v_exp_f32 v66, v66\n"
  "v_mfma_f32_16x16x32_bf16 v[184:187], v[56:59], a[140:143], v[184:187]\n"
  "v_exp_f32 v67, v67\n"
  "v_mfma_f32_16x16x32_bf16 v[152:155], v[44:47], a[136:139], v[152:155]\n"
  "ds_read_b64_tr_b16 a[120:121], v219 offset:24704\n"
  "v_cvt_pk_bf16_f32 v65, v66, v67\n"
  "v_mfma_f32_16x16x32_bf16 v[152:155], v[40:43], a[140:143], v[152:155]\n"
  "s_add_u32 m0, s72, 4480\n"
  "ds_read_b64_tr_b16 a[122:123], v220 offset:24704\n"
  "v_mfma_f32_16x16x32_bf16 v[120:123], v[28:31], a[136:139], v[120:123]\n"
  "ds_read_b64_tr_b16 a[124:125], v219 offset:25728\n"
  "v_fma_f32 v0, v0, s73, v223\n"
  "v_mfma_f32_16x16x32_bf16 v[120:123], v[24:27], a[140:143], v[120:123]\n"
  "ds_read_b64_tr_b16 a[126:127], v220 offset:25728\n"
  "v_fma_f32 v1, v1, s73, v223\n"
  "v_mfma_f32_16x16x32_bf16 v[88:91], v[12:15], a[136:139], v[88:91]\n"
  "v_exp_f32 v0, v0\n"
  "v_mfma_f32_16x16x32_bf16 v[88:91], v[8:11], a[140:143], v[88:91]\n"
  "s_waitcnt lgkmcnt(4)\n"
  "buffer_load_dwordx4 v221, s[48:51], 0 offen offset:3328 nt lds\n"
  "v_fma_f32 v2, v2, s73, v223\n"
  "v_mfma_f32_16x16x32_bf16 v[92:95], v[12:15], a[128:131], v[92:95]\n"
  "s_add_u32 s48, s84, s71\n"
  "v_exp_f32 v1, v1\n"
  "v_mfma_f32_16x16x32_bf16 v[92:95], v[8:11], a[132:135], v[92:95]\n"
  "s_addc_u32 s49, s85, 0\n"
  "v_exp_f32 v2, v2\n"
  "v_mfma_f32_16x16x32_bf16 v[124:127], v[28:31], a[128:131], v[124:127]\n"
  "s_sub_i32 s71, s38, s56\n"
  "ds_read_b128 a[136:139], v235 offset:24704\n"
  "v_fma_f32 v3, v3, s73, v223\n"
  "v_mfma_f32_16x16x32_bf16 v[124:127], v[24:27], a[132:135], v[124:127]\n"
  "s_add_u32 m0, s72, 17920\n"
  "ds_read_b128 a[140:143], v235 offset:24768\n"
  "v_add_f32 v252, v252, v0\n"
  "v_mfma_f32_16x16x32_bf16 v[156:159], v[44:47], a[128:131], v[156:159]\n"
  "s_max_i32 s50, s71, 0\n"
  "ds_read_b128 a[144:147], v235 offset:25728\n"
  "v_add_f32 v252, v252, v1\n"
  "v_mfma_f32_16x16x32_bf16 v[156:159], v[40:43], a[132:135], v[156:159]\n"
  "ds_read_b128 a[148:151], v235 offset:25792\n"
  "v_add_f32 v252, v252, v2\n"
  "v_mfma_f32_16x16x32_bf16 v[188:191], v[60:63], a[128:131], v[188:191]\n"
  "v_exp_f32 v3, v3\n"
  "v_mfma_f32_16x16x32_bf16 v[188:191], v[56:59], a[132:135], v[188:191]\n"
  "buffer_load_dwordx4 v221, s[48:51], 0 offen offset:0 nt lds\n"
  "v_add_f32 v252, v252, v3\n"
  "v_mfma_f32_16x16x32_bf16 a[240:243], a[160:163], a[128:131], a[240:243]\n"
  "ds_read_b128 a[128:131], v235 offset:26752\n"
  "v_fma_f32 v16, v16, s73, v224\n"
  "v_mfma_f32_16x16x32_bf16 a[240:243], a[156:159], a[132:135], a[240:243]\n"
  "s_waitcnt lgkmcnt(5)\n"
  "ds_read_b128 a[132:135], v235 offset:26816\n"
  "v_fma_f32 v17, v17, s73, v224\n"
  "v_mfma_f32_16x16x32_bf16 v[192:195], v[60:63], a[120:123], v[192:195]\n"
  "v_exp_f32 v16, v16\n"
  "v_mfma_f32_16x16x32_bf16 v[192:195], v[56:59], a[124:127], v[192:195]\n"
  "s_add_u32 m0, s72, 18912\n"
  "ds_write_b64 v231, v[64:65] offset:2048\n"
  "v_mfma_f32_16x16x32_bf16 v[160:163], v[44:47], a[120:123], v[160:163]\n"
  "v_exp_f32 v17, v17\n"
  "v_mfma_f32_16x16x32_bf16 v[160:163], v[40:43], a[124:127], v[160:163]\n"
  "v_fma_f32 v18, v18, s73, v224\n"
  "v_fma_f32 v19, v19, s73, v224\n"
  "v_mfma_f32_16x16x32_bf16 v[128:131], v[28:31], a[120:123], v[128:131]\n"
  "v_exp_f32 v18, v18\n"
  "v_mfma_f32_16x16x32_bf16 v[128:131], v[24:27], a[124:127], v[128:131]\n"
  "buffer_load_dwordx4 v221, s[48:51], 0 offen offset:128 nt lds\n"
  "v_add_f32 v253, v253, v16\n"
  "v_mfma_f32_16x16x32_bf16 v[96:99], v[12:15], a[120:123], v[96:99]\n"
  "v_exp_f32 v19, v19\n"
  "v_mfma_f32_16x16x32_bf16 v[96:99], v[8:11], a[124:127], v[96:99]\n"
  "s_waitcnt lgkmcnt(5)\n"
  "v_add_f32 v253, v253, v17\n"
  "v_add_f32 v253, v253, v18\n"
  "v_mfma_f32_16x16x32_bf16 v[4:7], a[136:139], a[0:3], v[248:251]\n"
  "v_add_f32 v253, v253, v19\n"
  "v_fma_f32 v32, v32, s73, v225\n"
  "v_mfma_f32_16x16x32_bf16 v[4:7], a[140:143], a[4:7], v[4:7]\n"
  "s_waitcnt lgkmcnt(3)\n"
  "v_exp_f32 v32, v32\n"
  "v_mfma_f32_16x16x32_bf16 v[4:7], a[144:147], a[8:11], v[4:7]\n"
  "v_fma_f32 v33, v33, s73, v225\n"
  "v_fma_f32 v34, v34, s73, v225\n"
  "v_mfma_f32_16x16x32_bf16 v[4:7], a[148:151], a[12:15], v[4:7]\n"
  "s_waitcnt lgkmcnt(1)\n"
  "v_exp_f32 v33, v33\n"
  "v_mfma_f32_16x16x32_bf16 v[4:7], a[128:131], a[16:19], v[4:7]\n"
  "v_exp_f32 v34, v34\n"
  "v_mfma_f32_16x16x32_bf16 v[4:7], a[132:135], a[20:23], v[4:7]\n"
  "v_fma_f32 v35, v35, s73, v225\n"
  "v_add_f32 v254, v254, v32\n"
  "v_mfma_f32_16x16x32_bf16 v[20:23], a[132:135], a[44:47], v[248:251]\n"
  "v_exp_f32 v35, v35\n"
  "v_mfma_f32_16x16x32_bf16 v[20:23], a[128:131], a[40:43], v[20:23]\n"
  "v_add_f32 v254, v254, v33\n"
  "v_add_f32 v254, v254, v34\n"
  "v_mfma_f32_16x16x32_bf16 v[20:23], a[148:151], a[36:39], v[20:23]\n"
  "ds_read_b64_tr_b16 a[120:121], v219 offset:24720\n"
  "v_add_f32 v254, v254, v35\n"
  "v_mfma_f32_16x16x32_bf16 v[20:23], a[144:147], a[32:35], v[20:23]\n"
  "ds_read_b64_tr_b16 a[122:123], v220 offset:24720\n"
  "v_fma_f32 v48, v48, s73, v226\n"
  "v_mfma_f32_16x16x32_bf16 v[20:23], a[140:143], a[28:31], v[20:23]\n"
  "ds_read_b64_tr_b16 a[124:125], v219 offset:25744\n"
  "v_fma_f32 v49, v49, s73, v226\n"
  "v_mfma_f32_16x16x32_bf16 v[20:23], a[136:139], a[24:27], v[20:23]\n"
  "ds_read_b64_tr_b16 a[126:127], v220 offset:25744\n"
  "v_fma_f32 v50, v50, s73, v226\n"
  "v_mfma_f32_16x16x32_bf16 v[36:39], a[136:139], a[48:51], v[248:251]\n"
  "v_exp_f32 v48, v48\n"
  "v_mfma_f32_16x16x32_bf16 v[36:39], a[140:143], a[52:55], v[36:39]\n"
  "v_exp_f32 v49, v49\n"
  "v_mfma_f32_16x16x32_bf16 v[36:39], a[144:147], a[56:59], v[36:39]\n"
  "v_exp_f32 v50, v50\n"
  "v_mfma_f32_16x16x32_bf16 v[36:39], a[148:151], a[60:63], v[36:39]\n"
  "v_fma_f32 v51, v51, s73, v226\n"
  "v_add_f32 v255, v255, v48\n"
  "v_mfma_f32_16x16x32_bf16 v[36:39], a[128:131], a[64:67], v[36:39]\n"
  "v_exp_f32 v51, v51\n"
  "v_mfma_f32_16x16x32_bf16 v[36:39], a[132:135], a[68:71], v[36:39]\n"
  "v_add_f32 v255, v255, v49\n"
  "v_add_f32 v255, v255, v50\n"
  "v_mfma_f32_16x16x32_bf16 v[52:55], a[132:135], a[92:95], v[248:251]\n"
  "ds_read_b64_tr_b16 a[132:133], v217 offset:26752\n"
  "v_add_f32 v255, v255, v51\n"
  "v_mfma_f32_16x16x32_bf16 v[52:55], a[128:131], a[88:91], v[52:55]\n"
  "ds_read_b64_tr_b16 a[134:135], v218 offset:26752\n"
  "v_fma_f32 v20, v20, s73, v224\n"
  "v_mfma_f32_16x16x32_bf16 v[52:55], a[148:151], a[84:87], v[52:55]\n"
  "ds_read_b64_tr_b16 a[128:129], v217 offset:26768\n"
  "v_fma_f32 v36, v36, s73, v225\n"
  "v_mfma_f32_16x16x32_bf16 v[52:55], a[144:147], a[80:83], v[52:55]\n"
  "ds_read_b64_tr_b16 a[130:131], v218 offset:26768\n"
  "v_fma_f32 v37, v37, s73, v225\n"
  "v_mfma_f32_16x16x32_bf16 v[52:55], a[140:143], a[76:79], v[52:55]\n"
  "v_exp_f32 v36, v36\n"
  "v_mfma_f32_16x16x32_bf16 v[52:55], a[136:139], a[72:75], v[52:55]\n"
  "s_waitcnt lgkmcnt(4)\n"
  "v_exp_f32 v37, v37\n"
  "v_mfma_f32_16x16x32_bf16 v[100:103], v[12:15], a[120:123], v[100:103]\n"
  "v_cvt_pk_bf16_f32 v12, v0, v1\n"
  "v_cvt_pk_bf16_f32 v13, v2, v3\n"
  "v_mfma_f32_16x16x32_bf16 v[100:103], v[8:11], a[124:127], v[100:103]\n"
  "v_exp_f32 v20, v20\n"
  "v_mfma_f32_16x16x32_bf16 v[132:135], v[28:31], a[120:123], v[132:135]\n"
  "ds_read_b64_tr_b16 a[140:141], v219 offset:26752\n"
  "v_cvt_pk_bf16_f32 v28, v16, v17\n"
  "v_mfma_f32_16x16x32_bf16 v[132:135], v[24:27], a[124:127], v[132:135]\n"
  "ds_read_b64_tr_b16 a[142:143], v220 offset:26752\n"
  "v_cvt_pk_bf16_f32 v29, v18, v19\n"
  "v_mfma_f32_16x16x32_bf16 v[164:167], v[44:47], a[120:123], v[164:167]\n"
  "ds_read_b64_tr_b16 a[136:137], v219 offset:26768\n"
  "v_cvt_pk_bf16_f32 v44, v32, v33\n"
  "v_mfma_f32_16x16x32_bf16 v[164:167], v[40:43], a[124:127], v[164:167]\n"
  "ds_read_b64_tr_b16 a[138:139], v220 offset:26768\n"
  "v_cvt_pk_bf16_f32 v45, v34, v35\n"
  "v_mfma_f32_16x16x32_bf16 v[196:199], v[60:63], a[120:123], v[196:199]\n"
  "v_cvt_pk_bf16_f32 v60, v48, v49\n"
  "v_cvt_pk_bf16_f32 v61, v50, v51\n"
  "v_mfma_f32_16x16x32_bf16 v[196:199], v[56:59], a[124:127], v[196:199]\n"
  "s_waitcnt lgkmcnt(4)\n"
  "v_fma_f32 v52, v52, s73, v226\n"
  "v_fma_f32 v53, v53, s73, v226\n"
  "v_mfma_f32_16x16x32_bf16 a[244:247], a[156:159], a[132:135], a[244:247]\n"
  "v_exp_f32 v52, v52\n"
  "v_mfma_f32_16x16x32_bf16 a[248:251], a[156:159], a[128:131], a[248:251]\n"
  "v_exp_f32 v53, v53\n"
  "v_mfma_f32_16x16x32_bf16 v[200:203], v[56:59], a[132:135], v[200:203]\n"
  "v_fma_f32 v54, v54, s73, v226\n"
  "v_fma_f32 v55, v55, s73, v226\n"
  "v_mfma_f32_16x16x32_bf16 v[204:207], v[56:59], a[128:131], v[204:207]\n"
  "v_exp_f32 v54, v54\n"
  "v_mfma_f32_16x16x32_bf16 v[168:171], v[40:43], a[132:135], v[168:171]\n"
  "v_exp_f32_e64 v55, v55\n"
  "v_mfma_f32_16x16x32_bf16 v[172:175], v[40:43], a[128:131], v[172:175]\n"
  "s_waitcnt vmcnt(14) lgkmcnt(0)\n"
  "s_barrier\n"
  "ds_read_b128 a[120:123], v216 offset:0\n"
  "v_mfma_f32_16x16x32_bf16 v[136:139], v[24:27], a[132:135], v[136:139]\n"
  "ds_read_b64 a[160:161], v231 offset:2048\n"
  "v_cvt_pk_bf16_f32 v62, v52, v53\n"
  "v_mfma_f32_16x16x32_bf16 v[140:143], v[24:27], a[128:131], v[140:143]\n"
  "ds_read_b64 a[162:163], v232 offset:2048\n"
  "v_cvt_pk_bf16_f32 v63, v54, v55\n"
  "v_mfma_f32_16x16x32_bf16 v[104:107], v[8:11], a[132:135], v[104:107]\n"
  "ds_read_b128 a[124:127], v216 offset:64\n"
  "v_fma_f32 v38, v38, s73, v225\n"
  "v_mfma_f32_16x16x32_bf16 v[108:111], v[8:11], a[128:131], v[108:111]\n"
  "ds_read_b128 a[132:135], v216 offset:1024\n"
  "v_fma_f32 v39, v39, s73, v225\n"
  "v_mfma_f32_16x16x32_bf16 v[112:115], v[8:11], a[140:143], v[112:115]\n"
  "ds_read_b128 a[128:131], v216 offset:1088\n"
  "v_cvt_pk_bf16_f32 v46, v36, v37\n"
  "v_mfma_f32_16x16x32_bf16 v[116:119], v[8:11], a[136:139], v[116:119]\n"
  "s_mov_b32 m0, s68\n"
  "ds_read_b128 a[148:151], v216 offset:2048\n"
  "v_fma_f32 v21, v21, s73, v224\n"
  "v_mfma_f32_16x16x32_bf16 v[144:147], v[24:27], a[140:143], v[144:147]\n"
  "ds_read_b128 a[144:147], v216 offset:2112\n"
  "v_fma_f32 v22, v22, s73, v224\n"
  "v_mfma_f32_16x16x32_bf16 v[148:151], v[24:27], a[136:139], v[148:151]\n"
  "v_exp_f32 v38, v38\n"
  "v_mfma_f32_16x16x32_bf16 v[176:179], v[40:43], a[140:143], v[176:179]\n"
  "v_exp_f32 v39, v39\n"
  "v_mfma_f32_16x16x32_bf16 v[180:183], v[40:43], a[136:139], v[180:183]\n"
  "buffer_load_dwordx4 v221, s[40:43], 0 offen offset:0 lds\n"
  "v_mfma_f32_16x16x32_bf16 v[208:211], v[56:59], a[140:143], v[208:211]\n"
  "s_addk_i32 m0, 896\n"
  "v_exp_f32 v21, v21\n"
  "v_mfma_f32_16x16x32_bf16 v[212:215], v[56:59], a[136:139], v[212:215]\n"
  "s_waitcnt lgkmcnt(4)\n"
  "v_exp_f32 v22, v22\n"
  "v_mfma_f32_16x16x32_bf16 v[64:67], a[120:123], a[96:99], v[248:251]\n"
  "v_cvt_pk_bf16_f32 v47, v38, v39\n"
  "v_fma_f32 v23, v23, s73, v224\n"
  "v_mfma_f32_16x16x32_bf16 v[64:67], a[124:127], a[100:103], v[64:67]\n"
  "s_waitcnt lgkmcnt(2)\n"
  "buffer_load_dwordx4 v221, s[40:43], 0 offen offset:128 lds\n"
  "v_cvt_pk_bf16_f32 v30, v20, v21\n"
  "v_mfma_f32_16x16x32_bf16 v[64:67], a[132:135], a[104:107], v[64:67]\n"
  "s_addk_i32 m0, 896\n"
  "v_exp_f32 v23, v23\n"
  "v_mfma_f32_16x16x32_bf16 v[64:67], a[128:131], a[108:111], v[64:67]\n"
  "s_waitcnt lgkmcnt(0)\n"
  "v_cvt_pk_bf16_f32 v31, v22, v23\n"
  "v_fma_f32 v4, v4, s73, v223\n"
  "v_mfma_f32_16x16x32_bf16 v[64:67], a[148:151], a[112:115], v[64:67]\n"
  "v_exp_f32 v4, v4\n"
  "v_mfma_f32_16x16x32_bf16 v[64:67], a[144:147], a[116:119], v[64:67]\n"
  "buffer_load_dwordx4 v221, s[40:43], 0 offen offset:256 lds\n"
  "v_fma_f32 v5, v5, s73, v223\n"
  "v_mfma_f32_16x16x32_bf16 v[0:3], a[144:147], a[20:23], v[248:251]\n"
  "s_add_u32 s40, s40, s31\n"
  "v_exp_f32 v5, v5\n"
  "v_mfma_f32_16x16x32_bf16 v[0:3], a[148:151], a[16:19], v[0:3]\n"
  "s_addc_u32 s41, s41, 0\n"
  "v_fma_f32 v6, v6, s73, v223\n"
  "v_fma_f32 v7, v7, s73, v223\n"
  "v_mfma_f32_16x16x32_bf16 v[0:3], a[128:131], a[12:15], v[0:3]\n"
  "v_exp_f32 v6, v6\n"
  "v_mfma_f32_16x16x32_bf16 v[0:3], a[132:135], a[8:11], v[0:3]\n"
  "s_mov_b32 m0, s60\n"
  "v_exp_f32 v7, v7\n"
  "v_mfma_f32_16x16x32_bf16 v[0:3], a[124:127], a[4:7], v[0:3]\n"
  "v_cvt_pk_bf16_f32 v14, v4, v5\n"
  "v_cvt_pk_bf16_f32 v15, v6, v7\n"
  "v_mfma_f32_16x16x32_bf16 v[0:3], a[120:123], a[0:3], v[0:3]\n"
  "v_add_f32 v252, v252, v4\n"
  "v_add_f32 v252, v252, v5\n"
  "v_mfma_f32_16x16x32_bf16 v[16:19], a[120:123], a[24:27], v[248:251]\n"
  "v_add_f32 v252, v252, v6\n"
  "v_add_f32 v252, v252, v7\n"
  "v_mfma_f32_16x16x32_bf16 v[16:19], a[124:127], a[28:31], v[16:19]\n"
  "buffer_load_dwordx4 v221, s[32:35], 0 offen offset:0 lds\n"
  "v_add_f32 v253, v253, v20\n"
  "v_mfma_f32_16x16x32_bf16 v[16:19], a[132:135], a[32:35], v[16:19]\n"
  "s_addk_i32 m0, 1024\n"
  "ds_read_b64_tr_b16 a[140:141], v217 offset:0\n"
  "v_add_f32 v253, v253, v21\n"
  "v_mfma_f32_16x16x32_bf16 v[16:19], a[128:131], a[36:39], v[16:19]\n"
  "s_mov_b64 s[32:33], s[44:45]\n"
  "ds_read_b64_tr_b16 a[142:143], v218 offset:0\n"
  "v_add_f32 v253, v253, v22\n"
  "v_mfma_f32_16x16x32_bf16 v[16:19], a[148:151], a[40:43], v[16:19]\n"
  "ds_read_b64_tr_b16 a[136:137], v217 offset:1024\n"
  "v_add_f32 v253, v253, v23\n"
  "v_mfma_f32_16x16x32_bf16 v[16:19], a[144:147], a[44:47], v[16:19]\n"
  "buffer_load_dwordx4 v221, s[44:47], 0 offen offset:0 lds\n"
  "v_add_f32 v254, v254, v36\n"
  "v_mfma_f32_16x16x32_bf16 v[32:35], a[144:147], a[68:71], v[248:251]\n"
  "s_addk_i32 m0, 768\n"
  "ds_read_b64_tr_b16 a[138:139], v218 offset:1024\n"
  "v_add_f32 v254, v254, v37\n"
  "v_mfma_f32_16x16x32_bf16 v[32:35], a[148:151], a[64:67], v[32:35]\n"
  "v_add_f32 v254, v254, v38\n"
  "v_add_f32 v254, v254, v39\n"
  "v_mfma_f32_16x16x32_bf16 v[32:35], a[128:131], a[60:63], v[32:35]\n"
  "v_add_f32 v255, v255, v52\n"
  "v_add_f32 v255, v255, v53\n"
  "v_mfma_f32_16x16x32_bf16 v[32:35], a[132:135], a[56:59], v[32:35]\n"
  "buffer_load_dwordx4 v221, s[44:47], 0 offen offset:256 lds\n"
  "v_add_f32 v255, v255, v54\n"
  "v_mfma_f32_16x16x32_bf16 v[32:35], a[124:127], a[52:55], v[32:35]\n"
  "s_add_u32 s44, s44, s31\n"
  "ds_read_b64_tr_b16 a[252:253], v230 offset:20160\n"
  "v_add_f32 v255, v255, v55\n"
  "v_mfma_f32_16x16x32_bf16 v[32:35], a[120:123], a[48:51], v[32:35]\n"
  "s_addc_u32 s45, s45, 0\n"
  "ds_read_b64_tr_b16 a[254:255], v230 offset:20192\n"
  "ds_read_b64_tr_b16 v[244:245], v230 offset:8960\n"
  "v_mfma_f32_16x16x32_bf16 v[48:51], a[120:123], a[72:75], v[248:251]\n"
  "ds_read_b64_tr_b16 v[246:247], v230 offset:8992\n"
  "v_fma_f32 v64, v64, s73, v227\n"
  "v_mfma_f32_16x16x32_bf16 v[48:51], a[124:127], a[76:79], v[48:51]\n"
  "v_exp_f32 v64, v64\n"
  "v_mfma_f32_16x16x32_bf16 v[48:51], a[132:135], a[80:83], v[48:51]\n"
  "ds_read_b64_tr_b16 a[120:121], v217 offset:16\n"
  "v_fma_f32 v65, v65, s73, v227\n"
  "v_mfma_f32_16x16x32_bf16 v[48:51], a[128:131], a[84:87], v[48:51]\n"
  "ds_read_b64_tr_b16 a[122:123], v218 offset:16\n"
  "v_fma_f32 v66, v66, s73, v227\n"
  "v_mfma_f32_16x16x32_bf16 v[48:51], a[148:151], a[88:91], v[48:51]\n"
  "ds_read_b64_tr_b16 a[124:125], v217 offset:1040\n"
  "v_fma_f32 v67, v67, s73, v227\n"
  "v_mfma_f32_16x16x32_bf16 v[48:51], a[144:147], a[92:95], v[48:51]\n"
  "s_waitcnt lgkmcnt(7)\n"
  "ds_read_b64_tr_b16 a[126:127], v218 offset:1040\n"
  "v_fma_f32 v0, v0, s73, v223\n"
  "v_mfma_f32_16x16x32_bf16 a[228:231], a[156:159], a[140:143], a[228:231]\n"
  "v_exp_f32 v65, v65\n"
  "v_mfma_f32_16x16x32_bf16 a[228:231], a[160:163], a[136:139], a[228:231]\n"
  "v_exp_f32 v66, v66\n"
  "v_mfma_f32_16x16x32_bf16 a[212:215], v[56:59], a[140:143], a[212:215]\n"
  "v_exp_f32 v67, v67\n"
  "v_mfma_f32_16x16x32_bf16 a[212:215], v[60:63], a[136:139], a[212:215]\n"
  "v_exp_f32 v0, v0\n"
  "v_mfma_f32_16x16x32_bf16 a[196:199], v[40:43], a[140:143], a[196:199]\n"
  "v_cvt_pk_bf16_f32 v64, v64, v65\n"
  "v_cvt_pk_bf16_f32 v65, v66, v67\n"
  "v_mfma_f32_16x16x32_bf16 a[196:199], v[44:47], a[136:139], a[196:199]\n"
  "v_fma_f32 v1, v1, s73, v223\n"
  "v_fma_f32 v2, v2, s73, v223\n"
  "v_mfma_f32_16x16x32_bf16 a[180:183], v[24:27], a[140:143], a[180:183]\n"
  "ds_read_b64_tr_b16 a[132:133], v219 offset:0\n"
  "v_fma_f32 v3, v3, s73, v223\n"
  "v_mfma_f32_16x16x32_bf16 a[180:183], v[28:31], a[136:139], a[180:183]\n"
  "ds_read_b64_tr_b16 a[134:135], v220 offset:0\n"
  "v_add_f32 v252, v252, v0\n"
  "v_mfma_f32_16x16x32_bf16 a[164:167], v[8:11], a[140:143], a[164:167]\n"
  "ds_read_b64_tr_b16 a[128:129], v219 offset:1024\n"
  "v_fma_f32 v16, v16, s73, v224\n"
  "v_mfma_f32_16x16x32_bf16 a[164:167], v[12:15], a[136:139], a[164:167]\n"
  "ds_read_b64_tr_b16 a[130:131], v220 offset:1024\n"
  "v_fma_f32 v17, v17, s73, v224\n"
  "v_mfma_f32_16x16x32_bf16 v[84:87], a[156:159], a[152:155], v[84:87]\n"
  "v_exp_f32 v1, v1\n"
  "v_mfma_f32_16x16x32_bf16 v[84:87], a[160:163], a[152:155], v[84:87]\n"
  "s_waitcnt lgkmcnt(4)\n"
  "v_exp_f32 v2, v2\n"
  "v_mfma_f32_16x16x32_bf16 a[168:171], v[8:11], a[120:123], a[168:171]\n"
  "v_exp_f32 v3, v3\n"
  "v_mfma_f32_16x16x32_bf16 a[168:171], v[12:15], a[124:127], a[168:171]\n"
  "v_exp_f32 v16, v16\n"
  "v_mfma_f32_16x16x32_bf16 a[184:187], v[24:27], a[120:123], a[184:187]\n"
  "ds_read_b128 a[140:143], v235 offset:0\n"
  "v_add_f32 v252, v252, v1\n"
  "v_mfma_f32_16x16x32_bf16 a[184:187], v[28:31], a[124:127], a[184:187]\n"
  "ds_read_b128 a[136:139], v235 offset:64\n"
  "v_add_f32 v252, v252, v2\n"
  "v_mfma_f32_16x16x32_bf16 a[200:203], v[40:43], a[120:123], a[200:203]\n"
  "ds_read_b128 a[148:151], v235 offset:1024\n"
  "v_add_f32 v252, v252, v3\n"
  "v_mfma_f32_16x16x32_bf16 a[200:203], v[44:47], a[124:127], a[200:203]\n"
  "ds_read_b128 a[144:147], v235 offset:1088\n"
  "v_fma_f32 v18, v18, s73, v224\n"
  "v_mfma_f32_16x16x32_bf16 a[216:219], v[56:59], a[120:123], a[216:219]\n"
  "v_exp_f32 v17, v17\n"
  "v_mfma_f32_16x16x32_bf16 a[216:219], v[60:63], a[124:127], a[216:219]\n"
  "v_exp_f32 v18, v18\n"
  "v_mfma_f32_16x16x32_bf16 a[232:235], a[156:159], a[120:123], a[232:235]\n"
  "ds_read_b128 a[120:123], v235 offset:2048\n"
  "v_fma_f32 v19, v19, s73, v224\n"
  "v_mfma_f32_16x16x32_bf16 a[232:235], a[160:163], a[124:127], a[232:235]\n"
  "s_waitcnt lgkmcnt(5)\n"
  "ds_read_b128 a[124:127], v235 offset:2112\n"
  "v_add_f32 v253, v253, v16\n"
  "v_mfma_f32_16x16x32_bf16 a[220:223], v[56:59], a[132:135], a[220:223]\n"
  "v_exp_f32_e64 v19, v19\n"
  "v_mfma_f32_16x16x32_bf16 a[220:223], v[60:63], a[128:131], a[220:223]\n"
  "ds_write_b64 v231, v[64:65] offset:0\n"
  "v_mfma_f32_16x16x32_bf16 a[204:207], v[40:43], a[132:135], a[204:207]\n"
  "v_add_f32 v253, v253, v17\n"
  "v_add_f32 v253, v253, v18\n"
  "v_mfma_f32_16x16x32_bf16 a[204:207], v[44:47], a[128:131], a[204:207]\n"
  "buffer_store_dwordx4 a[252:255], v228, s[52:55], s56 offen offset:256 nt\n"
  "v_add_f32 v253, v253, v19\n"
  "v_fma_f32 v32, v32, s73, v225\n"
  "v_fma_f32 v33, v33, s73, v225\n"
  "v_mfma_f32_16x16x32_bf16 a[188:191], v[24:27], a[132:135], a[188:191]\n"
  "ds_read_b64_tr_b16 a[252:253], v230 offset:10080\n"
  "ds_read_b64_tr_b16 a[254:255], v230 offset:10112\n"
  "v_mfma_f32_16x16x32_bf16 a[188:191], v[28:31], a[128:131], a[188:191]\n"
  "v_exp_f32 v32, v32\n"
  "v_mfma_f32_16x16x32_bf16 a[172:175], v[8:11], a[132:135], a[172:175]\n"
  "v_exp_f32 v33, v33\n"
  "v_mfma_f32_16x16x32_bf16 a[172:175], v[12:15], a[128:131], a[172:175]\n"
  "s_waitcnt lgkmcnt(7)\n"
  "v_fma_f32 v34, v34, s73, v225\n"
  "v_fma_f32 v35, v35, s73, v225\n"
  "v_mfma_f32_16x16x32_bf16 v[4:7], a[140:143], a[0:3], v[248:251]\n"
  "v_exp_f32 v34, v34\n"
  "v_mfma_f32_16x16x32_bf16 v[4:7], a[136:139], a[4:7], v[4:7]\n"
  "s_waitcnt lgkmcnt(5)\n"
  "v_exp_f32 v35, v35\n"
  "v_mfma_f32_16x16x32_bf16 v[4:7], a[148:151], a[8:11], v[4:7]\n"
  "v_add_f32 v254, v254, v32\n"
  "v_add_f32 v254, v254, v33\n"
  "v_mfma_f32_16x16x32_bf16 v[4:7], a[144:147], a[12:15], v[4:7]\n"
  "s_waitcnt lgkmcnt(3)\n"
  "v_add_f32 v254, v254, v34\n"
  "v_add_f32 v254, v254, v35\n"
  "v_mfma_f32_16x16x32_bf16 v[4:7], a[120:123], a[16:19], v[4:7]\n"
  "v_fma_f32 v48, v48, s73, v226\n"
  "v_fma_f32 v49, v49, s73, v226\n"
  "v_mfma_f32_16x16x32_bf16 v[4:7], a[124:127], a[20:23], v[4:7]\n"
  "v_exp_f32 v48, v48\n"
  "v_mfma_f32_16x16x32_bf16 v[20:23], a[124:127], a[44:47], v[248:251]\n"
  "v_exp_f32 v49, v49\n"
  "v_mfma_f32_16x16x32_bf16 v[20:23], a[120:123], a[40:43], v[20:23]\n"
  "v_fma_f32 v50, v50, s73, v226\n"
  "v_fma_f32 v51, v51, s73, v226\n"
  "v_mfma_f32_16x16x32_bf16 v[20:23], a[144:147], a[36:39], v[20:23]\n"
  "ds_read_b64_tr_b16 a[132:133], v219 offset:16\n"
  "v_add_f32 v255, v255, v48\n"
  "v_mfma_f32_16x16x32_bf16 v[20:23], a[148:151], a[32:35], v[20:23]\n"
  "ds_read_b64_tr_b16 a[134:135], v220 offset:16\n"
  "v_add_f32 v255, v255, v49\n"
  "v_mfma_f32_16x16x32_bf16 v[20:23], a[136:139], a[28:31], v[20:23]\n"
  "ds_read_b64_tr_b16 a[128:129], v219 offset:1040\n"
  "v_fma_f32 v4, v4, s73, v223\n"
  "v_mfma_f32_16x16x32_bf16 v[20:23], a[140:143], a[24:27], v[20:23]\n"
  "ds_read_b64_tr_b16 a[130:131], v220 offset:1040\n"
  "v_fma_f32 v5, v5, s73, v223\n"
  "v_mfma_f32_16x16x32_bf16 v[36:39], a[140:143], a[48:51], v[248:251]\n"
  "v_exp_f32 v50, v50\n"
  "v_mfma_f32_16x16x32_bf16 v[36:39], a[136:139], a[52:55], v[36:39]\n"
  "v_exp_f32 v51, v51\n"
  "v_mfma_f32_16x16x32_bf16 v[36:39], a[148:151], a[56:59], v[36:39]\n"
  "v_exp_f32 v4, v4\n"
  "v_mfma_f32_16x16x32_bf16 v[36:39], a[144:147], a[60:63], v[36:39]\n"
  "v_exp_f32 v5, v5\n"
  "v_mfma_f32_16x16x32_bf16 v[36:39], a[120:123], a[64:67], v[36:39]\n"
  "v_add_f32 v255, v255, v50\n"
  "v_add_f32 v255, v255, v51\n"
  "v_mfma_f32_16x16x32_bf16 v[36:39], a[124:127], a[68:71], v[36:39]\n"
  "buffer_store_dwordx4 v[244:247], v228, s[52:55], s80 offen offset:128 nt\n"
  "v_fma_f32 v20, v20, s73, v224\n"
  "v_exp_f32_e64 v20, v20\n"
  "v_mfma_f32_16x16x32_bf16 v[52:55], a[124:127], a[92:95], v[248:251]\n"
  "ds_read_b64_tr_b16 a[124:125], v217 offset:2048\n"
  "v_fma_f32 v21, v21, s73, v224\n"
  "v_mfma_f32_16x16x32_bf16 v[52:55], a[120:123], a[88:91], v[52:55]\n"
  "ds_read_b64_tr_b16 a[126:127], v218 offset:2048\n"
  "v_fma_f32 v22, v22, s73, v224\n"
  "v_mfma_f32_16x16x32_bf16 v[52:55], a[144:147], a[84:87], v[52:55]\n"
  "ds_read_b64_tr_b16 a[120:121], v217 offset:2064\n"
  "v_fma_f32 v36, v36, s73, v225\n"
  "v_mfma_f32_16x16x32_bf16 v[52:55], a[148:151], a[80:83], v[52:55]\n"
  "ds_read_b64_tr_b16 a[122:123], v218 offset:2064\n"
  "v_fma_f32 v37, v37, s73, v225\n"
  "v_mfma_f32_16x16x32_bf16 v[52:55], a[136:139], a[76:79], v[52:55]\n"
  "v_exp_f32 v36, v36\n"
  "v_mfma_f32_16x16x32_bf16 v[52:55], a[140:143], a[72:75], v[52:55]\n"
  "s_waitcnt lgkmcnt(4)\n"
  "v_exp_f32 v37, v37\n"
  "v_mfma_f32_16x16x32_bf16 a[176:179], v[8:11], a[132:135], a[176:179]\n"
  "v_cvt_pk_bf16_f32 v8, v0, v1\n"
  "v_cvt_pk_bf16_f32 v9, v2, v3\n"
  "v_mfma_f32_16x16x32_bf16 a[176:179], v[12:15], a[128:131], a[176:179]\n"
  "v_exp_f32 v21, v21\n"
  "v_mfma_f32_16x16x32_bf16 a[192:195], v[24:27], a[132:135], a[192:195]\n"
  "ds_read_b64_tr_b16 a[136:137], v219 offset:2048\n"
  "v_cvt_pk_bf16_f32 v24, v16, v17\n"
  "v_mfma_f32_16x16x32_bf16 a[192:195], v[28:31], a[128:131], a[192:195]\n"
  "ds_read_b64_tr_b16 a[138:139], v220 offset:2048\n"
  "v_cvt_pk_bf16_f32 v25, v18, v19\n"
  "v_mfma_f32_16x16x32_bf16 a[208:211], v[40:43], a[132:135], a[208:211]\n"
  "ds_read_b64_tr_b16 a[140:141], v219 offset:2064\n"
  "v_cvt_pk_bf16_f32 v40, v32, v33\n"
  "v_mfma_f32_16x16x32_bf16 a[208:211], v[44:47], a[128:131], a[208:211]\n"
  "ds_read_b64_tr_b16 a[142:143], v220 offset:2064\n"
  "v_cvt_pk_bf16_f32 v41, v34, v35\n"
  "v_mfma_f32_16x16x32_bf16 a[224:227], v[56:59], a[132:135], a[224:227]\n"
  "v_exp_f32 v22, v22\n"
  "v_mfma_f32_16x16x32_bf16 a[224:227], v[60:63], a[128:131], a[224:227]\n"
  "s_waitcnt lgkmcnt(4)\n"
  "v_cvt_pk_bf16_f32 v56, v48, v49\n"
  "v_cvt_pk_bf16_f32 v57, v50, v51\n"
  "v_mfma_f32_16x16x32_bf16 a[244:247], a[160:163], a[124:127], a[244:247]\n"
  "v_fma_f32 v52, v52, s73, v226\n"
  "v_fma_f32 v53, v53, s73, v226\n"
  "v_mfma_f32_16x16x32_bf16 a[248:251], a[160:163], a[120:123], a[248:251]\n"
  "v_exp_f32 v52, v52\n"
  "v_mfma_f32_16x16x32_bf16 v[200:203], v[60:63], a[124:127], v[200:203]\n"
  "v_exp_f32 v53, v53\n"
  "v_mfma_f32_16x16x32_bf16 v[204:207], v[60:63], a[120:123], v[204:207]\n"
  "buffer_store_dwordx4 a[252:255], v228, s[52:55], s80 offen offset:3200 nt\n"
  "v_fma_f32 v54, v54, s73, v226\n"
  "v_exp_f32_e64 v54, v54\n"
  "v_mfma_f32_16x16x32_bf16 v[168:171], v[44:47], a[124:127], v[168:171]\n"
  "v_fma_f32 v55, v55, s73, v226\n"
  "v_cvt_pk_bf16_f32 v58, v52, v53\n"
  "v_mfma_f32_16x16x32_bf16 v[172:175], v[44:47], a[120:123], v[172:175]\n"
  "s_waitcnt vmcnt(13) lgkmcnt(0)\n"
  "s_barrier\n"
  "ds_read_b128 a[132:135], v216 offset:12352\n"
  "v_mfma_f32_16x16x32_bf16 v[136:139], v[28:31], a[124:127], v[136:139]\n"
  "ds_read_b64 a[156:157], v231 offset:0\n"
  "v_fma_f32 v38, v38, s73, v225\n"
  "v_mfma_f32_16x16x32_bf16 v[140:143], v[28:31], a[120:123], v[140:143]\n"
  "ds_read_b64 a[158:159], v232 offset:0\n"
  "v_fma_f32 v39, v39, s73, v225\n"
  "v_mfma_f32_16x16x32_bf16 v[104:107], v[12:15], a[124:127], v[104:107]\n"
  "ds_read_b128 a[128:131], v216 offset:12416\n"
  "v_cvt_pk_bf16_f32 v42, v36, v37\n"
  "v_mfma_f32_16x16x32_bf16 v[108:111], v[12:15], a[120:123], v[108:111]\n"
  "ds_read_b128 a[124:127], v216 offset:13376\n"
  "v_fma_f32 v23, v23, s73, v224\n"
  "v_mfma_f32_16x16x32_bf16 v[112:115], v[12:15], a[136:139], v[112:115]\n"
  "ds_read_b128 a[120:123], v216 offset:13440\n"
  "v_cvt_pk_bf16_f32 v26, v20, v21\n"
  "v_mfma_f32_16x16x32_bf16 v[116:119], v[12:15], a[140:143], v[116:119]\n"
  "s_mov_b32 m0, s66\n"
  "ds_read_b128 a[144:147], v216 offset:14400\n"
  "v_fma_f32 v6, v6, s73, v223\n"
  "v_mfma_f32_16x16x32_bf16 v[144:147], v[28:31], a[136:139], v[144:147]\n"
  "ds_read_b128 a[148:151], v216 offset:14464\n"
  "v_fma_f32 v7, v7, s73, v223\n"
  "v_mfma_f32_16x16x32_bf16 v[148:151], v[28:31], a[140:143], v[148:151]\n"
  "v_exp_f32 v55, v55\n"
  "v_mfma_f32_16x16x32_bf16 v[176:179], v[44:47], a[136:139], v[176:179]\n"
  "v_exp_f32 v38, v38\n"
  "v_mfma_f32_16x16x32_bf16 v[180:183], v[44:47], a[140:143], v[180:183]\n"
  "buffer_load_dwordx4 v221, s[40:43], 0 offen offset:0 lds\n"
  "v_mfma_f32_16x16x32_bf16 v[208:211], v[60:63], a[136:139], v[208:211]\n"
  "s_addk_i32 m0, 896\n"
  "v_exp_f32 v39, v39\n"
  "v_mfma_f32_16x16x32_bf16 v[212:215], v[60:63], a[140:143], v[212:215]\n"
  "s_waitcnt lgkmcnt(4)\n"
  "v_exp_f32 v23, v23\n"
  "v_mfma_f32_16x16x32_bf16 v[64:67], a[132:135], a[96:99], v[248:251]\n"
  "s_add_u32 s71, s92, s81\n"
  "v_exp_f32 v6, v6\n"
  "v_mfma_f32_16x16x32_bf16 v[64:67], a[128:131], a[100:103], v[64:67]\n"
  "s_waitcnt lgkmcnt(2)\n"
  "buffer_load_dwordx4 v221, s[40:43], 0 offen offset:128 lds\n"
  "v_cvt_pk_bf16_f32 v59, v54, v55\n"
  "v_mfma_f32_16x16x32_bf16 v[64:67], a[124:127], a[104:107], v[64:67]\n"
  "s_addk_i32 m0, 896\n"
  "v_exp_f32 v7, v7\n"
  "v_mfma_f32_16x16x32_bf16 v[64:67], a[120:123], a[108:111], v[64:67]\n"
  "s_waitcnt lgkmcnt(0)\n"
  "v_cvt_pk_bf16_f32 v43, v38, v39\n"
  "v_cvt_pk_bf16_f32 v27, v22, v23\n"
  "v_mfma_f32_16x16x32_bf16 v[64:67], a[144:147], a[112:115], v[64:67]\n"
  "v_cvt_pk_bf16_f32 v10, v4, v5\n"
  "v_cvt_pk_bf16_f32 v11, v6, v7\n"
  "v_mfma_f32_16x16x32_bf16 v[64:67], a[148:151], a[116:119], v[64:67]\n"
  "buffer_load_dwordx4 v221, s[40:43], 0 offen offset:256 lds\n"
  "v_add_f32 v252, v252, v4\n"
  "v_mfma_f32_16x16x32_bf16 v[0:3], a[148:151], a[20:23], v[248:251]\n"
  "s_add_u32 s40, s40, s31\n"
  "v_add_f32 v252, v252, v5\n"
  "v_add_f32 v252, v252, v6\n"
  "v_mfma_f32_16x16x32_bf16 v[0:3], a[144:147], a[16:19], v[0:3]\n"
  "s_addc_u32 s41, s41, 0\n"
  "v_add_f32 v252, v252, v7\n"
  "v_add_f32 v253, v253, v20\n"
  "v_mfma_f32_16x16x32_bf16 v[0:3], a[120:123], a[12:15], v[0:3]\n"
  "v_add_f32 v253, v253, v21\n"
  "v_add_f32 v253, v253, v22\n"
  "v_mfma_f32_16x16x32_bf16 v[0:3], a[124:127], a[8:11], v[0:3]\n"
  "s_add_u32 m0, s58, -128\n"
  "v_add_f32 v253, v253, v23\n"
  "v_add_f32 v254, v254, v36\n"
  "v_mfma_f32_16x16x32_bf16 v[0:3], a[128:131], a[4:7], v[0:3]\n"
  "v_add_f32 v254, v254, v37\n"
  "v_add_f32 v254, v254, v38\n"
  "v_mfma_f32_16x16x32_bf16 v[0:3], a[132:135], a[0:3], v[0:3]\n"
  "v_add_f32 v254, v254, v39\n"
  "v_bfe_u32 v237, v231, 1, 8\n"
  "v_mfma_f32_16x16x32_bf16 v[16:19], a[132:135], a[24:27], v[248:251]\n"
  "v_add_u32 v237, s29, v237\n"
  "v_add_f32 v255, v255, v52\n"
  "v_mfma_f32_16x16x32_bf16 v[16:19], a[128:131], a[28:31], v[16:19]\n"
  "buffer_load_dwordx4 v221, s[32:35], 0 offen offset:128 lds\n"
  "ds_read_b32 v236, v237\n"
  "v_mfma_f32_16x16x32_bf16 v[16:19], a[124:127], a[32:35], v[16:19]\n"
  "s_addk_i32 m0, 1024\n"
  "ds_read_b64_tr_b16 a[136:137], v217 offset:12352\n"
  "v_add_f32 v255, v255, v53\n"
  "v_mfma_f32_16x16x32_bf16 v[16:19], a[120:123], a[36:39], v[16:19]\n"
  "s_mov_b64 s[32:33], s[44:45]\n"
  "ds_read_b64_tr_b16 a[138:139], v218 offset:12352\n"
  "v_add_f32 v255, v255, v54\n"
  "v_mfma_f32_16x16x32_bf16 v[16:19], a[144:147], a[40:43], v[16:19]\n"
  "ds_read_b64_tr_b16 a[140:141], v217 offset:13376\n"
  "v_add_f32 v255, v255, v55\n"
  "v_mfma_f32_16x16x32_bf16 v[16:19], a[148:151], a[44:47], v[16:19]\n"
  "buffer_load_dwordx4 v221, s[44:47], 0 offen offset:128 lds\n"
  "v_fma_f32 v64, v64, s73, v227\n"
  "v_mfma_f32_16x16x32_bf16 v[32:35], a[148:151], a[68:71], v[248:251]\n"
  "s_addk_i32 m0, 896\n"
  "ds_read_b64_tr_b16 a[142:143], v218 offset:13376\n"
  "v_fma_f32 v65, v65, s73, v227\n"
  "v_mfma_f32_16x16x32_bf16 v[32:35], a[144:147], a[64:67], v[32:35]\n"
  "s_waitcnt lgkmcnt(4)\n"
  "v_exp_f32 v64, v64\n"
  "v_mfma_f32_16x16x32_bf16 v[32:35], a[120:123], a[60:63], v[32:35]\n"
  "v_exp_f32 v65, v65\n"
  "v_mfma_f32_16x16x32_bf16 v[32:35], a[124:127], a[56:59], v[32:35]\n"
  "buffer_load_dwordx4 v221, s[44:47], 0 offen offset:256 lds\n"
  "v_fma_f32 v66, v66, s73, v227\n"
  "v_mfma_f32_16x16x32_bf16 v[32:35], a[128:131], a[52:55], v[32:35]\n"
  "s_add_u32 s44, s44, s31\n"
  "v_exp_f32 v66, v66\n"
  "v_mfma_f32_16x16x32_bf16 v[32:35], a[132:135], a[48:51], v[32:35]\n"
  "s_addc_u32 s45, s45, 0\n"
  "v_fma_f32 v67, v67, s73, v227\n"
  "v_cvt_pk_bf16_f32 v64, v64, v65\n"
  "v_mfma_f32_16x16x32_bf16 v[48:51], a[132:135], a[72:75], v[248:251]\n"
  "ds_read_b64_tr_b16 a[132:133], v217 offset:12368\n"
  "v_fma_f32 v0, v0, s73, v223\n"
  "v_mfma_f32_16x16x32_bf16 v[48:51], a[128:131], a[76:79], v[48:51]\n"
  "ds_read_b64_tr_b16 a[134:135], v218 offset:12368\n"
  "v_fma_f32 v1, v1, s73, v223\n"
  "v_mfma_f32_16x16x32_bf16 v[48:51], a[124:127], a[80:83], v[48:51]\n"
  "ds_read_b64_tr_b16 a[128:129], v217 offset:13392\n"
  "v_fma_f32 v2, v2, s73, v223\n"
  "v_mfma_f32_16x16x32_bf16 v[48:51], a[120:123], a[84:87], v[48:51]\n"
  "s_add_u32 m0, s72, 19904\n"
  "ds_read_b64_tr_b16 a[130:131], v218 offset:13392\n"
  "v_mfma_f32_16x16x32_bf16 v[48:51], a[144:147], a[88:91], v[48:51]\n"
  "v_exp_f32 v67, v67\n"
  "v_mfma_f32_16x16x32_bf16 v[48:51], a[148:151], a[92:95], v[48:51]\n"
  "s_waitcnt lgkmcnt(4)\n"
  "v_exp_f32 v0, v0\n"
  "v_mfma_f32_16x16x32_bf16 a[236:239], a[160:163], a[136:139], a[236:239]\n"
  "v_exp_f32 v1, v1\n"
  "v_mfma_f32_16x16x32_bf16 a[236:239], a[156:159], a[140:143], a[236:239]\n"
  "buffer_load_dwordx4 v221, s[48:51], 0 offen offset:256 nt lds\n"
  "v_cvt_pk_bf16_f32 v65, v66, v67\n"
  "v_mfma_f32_16x16x32_bf16 v[184:187], v[60:63], a[136:139], v[184:187]\n"
  "s_add_u32 s48, s84, s71\n"
  "v_exp_f32 v2, v2\n"
  "v_mfma_f32_16x16x32_bf16 v[184:187], v[56:59], a[140:143], v[184:187]\n"
  "s_addc_u32 s49, s85, 0\n"
  "v_fma_f32 v3, v3, s73, v223\n"
  "v_add_f32 v252, v252, v0\n"
  "v_mfma_f32_16x16x32_bf16 v[152:155], v[44:47], a[136:139], v[152:155]\n"
  "s_sub_i32 s71, s38, s81\n"
  "ds_read_b64_tr_b16 a[124:125], v219 offset:12352\n"
  "v_add_f32 v252, v252, v1\n"
  "v_mfma_f32_16x16x32_bf16 v[152:155], v[40:43], a[140:143], v[152:155]\n"
  "s_add_u32 m0, s72, 8832\n"
  "ds_read_b64_tr_b16 a[126:127], v220 offset:12352\n"
  "v_add_f32 v252, v252, v2\n"
  "v_mfma_f32_16x16x32_bf16 v[120:123], v[28:31], a[136:139], v[120:123]\n"
  "s_max_i32 s50, s71, 0\n"
  "ds_read_b64_tr_b16 a[120:121], v219 offset:13376\n"
  "v_fma_f32 v16, v16, s73, v224\n"
  "v_mfma_f32_16x16x32_bf16 v[120:123], v[24:27], a[140:143], v[120:123]\n"
  "ds_read_b64_tr_b16 a[122:123], v220 offset:13376\n"
  "v_fma_f32 v17, v17, s73, v224\n"
  "v_mfma_f32_16x16x32_bf16 v[88:91], v[12:15], a[136:139], v[88:91]\n"
  "v_exp_f32 v3, v3\n"
  "v_mfma_f32_16x16x32_bf16 v[88:91], v[8:11], a[140:143], v[88:91]\n"
  "s_waitcnt lgkmcnt(4)\n"
  "buffer_load_dwordx4 v221, s[48:51], 0 offen offset:128 nt lds\n"
  "v_add_f32 v252, v252, v3\n"
  "v_mfma_f32_16x16x32_bf16 v[92:95], v[12:15], a[132:135], v[92:95]\n"
  "v_exp_f32 v16, v16\n"
  "v_mfma_f32_16x16x32_bf16 v[92:95], v[8:11], a[128:131], v[92:95]\n"
  "v_exp_f32 v17, v17\n"
  "v_mfma_f32_16x16x32_bf16 v[124:127], v[28:31], a[132:135], v[124:127]\n"
  "ds_read_b128 a[136:139], v235 offset:12352\n"
  "v_fma_f32 v18, v18, s73, v224\n"
  "v_mfma_f32_16x16x32_bf16 v[124:127], v[24:27], a[128:131], v[124:127]\n"
  "s_add_u32 m0, s72, 6848\n"
  "ds_read_b128 a[140:143], v235 offset:12416\n"
  "v_add_f32 v253, v253, v16\n"
  "v_mfma_f32_16x16x32_bf16 v[156:159], v[44:47], a[132:135], v[156:159]\n"
  "ds_read_b128 a[144:147], v235 offset:13376\n"
  "v_fma_f32 v19, v19, s73, v224\n"
  "v_mfma_f32_16x16x32_bf16 v[156:159], v[40:43], a[128:131], v[156:159]\n"
  "ds_read_b128 a[148:151], v235 offset:13440\n"
  "v_add_f32 v253, v253, v17\n"
  "v_mfma_f32_16x16x32_bf16 v[188:191], v[60:63], a[132:135], v[188:191]\n"
  "v_exp_f32 v18, v18\n"
  "v_mfma_f32_16x16x32_bf16 v[188:191], v[56:59], a[128:131], v[188:191]\n"
  "buffer_load_dwordx4 v221, s[48:51], 0 offen offset:3200 nt lds\n"
  "v_add_f32 v253, v253, v18\n"
  "v_mfma_f32_16x16x32_bf16 a[240:243], a[160:163], a[132:135], a[240:243]\n"
  "ds_read_b128 a[132:135], v235 offset:14400\n"
  "v_fma_f32 v32, v32, s73, v225\n"
  "v_mfma_f32_16x16x32_bf16 a[240:243], a[156:159], a[128:131], a[240:243]\n"
  "s_waitcnt lgkmcnt(5)\n"
  "ds_read_b128 a[128:131], v235 offset:14464\n"
  "v_fma_f32 v33, v33, s73, v225\n"
  "v_mfma_f32_16x16x32_bf16 v[192:195], v[60:63], a[124:127], v[192:195]\n"
  "v_exp_f32 v19, v19\n"
  "v_mfma_f32_16x16x32_bf16 v[192:195], v[56:59], a[120:123], v[192:195]\n"
  "ds_write_b64 v231, v[64:65] offset:2048\n"
  "v_mfma_f32_16x16x32_bf16 v[160:163], v[44:47], a[124:127], v[160:163]\n"
  "v_exp_f32 v32, v32\n"
  "v_mfma_f32_16x16x32_bf16 v[160:163], v[40:43], a[120:123], v[160:163]\n"
  "v_exp_f32 v33, v33\n"
  "v_mfma_f32_16x16x32_bf16 v[128:131], v[28:31], a[124:127], v[128:131]\n"
  "v_add_f32 v253, v253, v19\n"
  "v_fma_f32 v34, v34, s73, v225\n"
  "v_mfma_f32_16x16x32_bf16 v[128:131], v[24:27], a[120:123], v[128:131]\n"
  "v_exp_f32 v34, v34\n"
  "v_mfma_f32_16x16x32_bf16 v[96:99], v[12:15], a[124:127], v[96:99]\n"
  "v_fma_f32 v35, v35, s73, v225\n"
  "v_add_f32 v254, v254, v32\n"
  "v_mfma_f32_16x16x32_bf16 v[96:99], v[8:11], a[120:123], v[96:99]\n"
  "s_waitcnt lgkmcnt(5)\n"
  "v_exp_f32 v35, v35\n"
  "v_mfma_f32_16x16x32_bf16 v[4:7], a[136:139], a[0:3], v[248:251]\n"
  "v_add_f32 v254, v254, v33\n"
  "v_add_f32 v254, v254, v34\n"
  "v_mfma_f32_16x16x32_bf16 v[4:7], a[140:143], a[4:7], v[4:7]\n"
  "s_waitcnt lgkmcnt(3)\n"
  "v_add_f32 v254, v254, v35\n"
  "v_fma_f32 v48, v48, s73, v226\n"
  "v_mfma_f32_16x16x32_bf16 v[4:7], a[144:147], a[8:11], v[4:7]\n"
  "v_exp_f32 v48, v48\n"
  "v_mfma_f32_16x16x32_bf16 v[4:7], a[148:151], a[12:15], v[4:7]\n"
  "s_waitcnt lgkmcnt(1)\n"
  "v_fma_f32 v49, v49, s73, v226\n"
  "v_fma_f32 v50, v50, s73, v226\n"
  "v_mfma_f32_16x16x32_bf16 v[4:7], a[132:135], a[16:19], v[4:7]\n"
  "v_exp_f32 v49, v49\n"
  "v_mfma_f32_16x16x32_bf16 v[4:7], a[128:131], a[20:23], v[4:7]\n"
  "v_exp_f32 v50, v50\n"
  "v_mfma_f32_16x16x32_bf16 v[20:23], a[128:131], a[44:47], v[248:251]\n"
  "v_fma_f32 v51, v51, s73, v226\n"
  "v_add_f32 v255, v255, v48\n"
  "v_mfma_f32_16x16x32_bf16 v[20:23], a[132:135], a[40:43], v[20:23]\n"
  "v_exp_f32 v51, v51\n"
  "v_mfma_f32_16x16x32_bf16 v[20:23], a[148:151], a[36:39], v[20:23]\n"
  "ds_read_b64_tr_b16 a[124:125], v219 offset:12368\n"
  "v_add_f32 v255, v255, v49\n"
  "v_mfma_f32_16x16x32_bf16 v[20:23], a[144:147], a[32:35], v[20:23]\n"
  "ds_read_b64_tr_b16 a[126:127], v220 offset:12368\n"
  "v_add_f32 v255, v255, v50\n"
  "v_mfma_f32_16x16x32_bf16 v[20:23], a[140:143], a[28:31], v[20:23]\n"
  "ds_read_b64_tr_b16 a[120:121], v219 offset:13392\n"
  "v_add_f32 v255, v255, v51\n"
  "v_mfma_f32_16x16x32_bf16 v[20:23], a[136:139], a[24:27], v[20:23]\n"
  "ds_read_b64_tr_b16 a[122:123], v220 offset:13392\n"
  "v_fma_f32 v4, v4, s73, v223\n"
  "v_mfma_f32_16x16x32_bf16 v[36:39], a[136:139], a[48:51], v[248:251]\n"
  "v_exp_f32 v4, v4\n"
  "v_mfma_f32_16x16x32_bf16 v[36:39], a[140:143], a[52:55], v[36:39]\n"
  "v_fma_f32 v5, v5, s73, v223\n"
  "v_fma_f32 v6, v6, s73, v223\n"
  "v_mfma_f32_16x16x32_bf16 v[36:39], a[144:147], a[56:59], v[36:39]\n"
  "v_exp_f32 v5, v5\n"
  "v_mfma_f32_16x16x32_bf16 v[36:39], a[148:151], a[60:63], v[36:39]\n"
  "v_exp_f32 v6, v6\n"
  "v_mfma_f32_16x16x32_bf16 v[36:39], a[132:135], a[64:67], v[36:39]\n"
  "v_fma_f32 v20, v20, s73, v224\n"
  "v_fma_f32 v21, v21, s73, v224\n"
  "v_mfma_f32_16x16x32_bf16 v[36:39], a[128:131], a[68:71], v[36:39]\n"
  "v_exp_f32 v20, v20\n"
  "v_mfma_f32_16x16x32_bf16 v[52:55], a[128:131], a[92:95], v[248:251]\n"
  "ds_read_b64_tr_b16 a[128:129], v217 offset:14400\n"
  "v_fma_f32 v22, v22, s73, v224\n"
  "v_mfma_f32_16x16x32_bf16 v[52:55], a[132:135], a[88:91], v[52:55]\n"
  "ds_read_b64_tr_b16 a[130:131], v218 offset:14400\n"
  "v_fma_f32 v23, v23, s73, v224\n"
  "v_mfma_f32_16x16x32_bf16 v[52:55], a[148:151], a[84:87], v[52:55]\n"
  "ds_read_b64_tr_b16 a[132:133], v217 offset:14416\n"
  "v_fma_f32 v36, v36, s73, v225\n"
  "v_mfma_f32_16x16x32_bf16 v[52:55], a[144:147], a[80:83], v[52:55]\n"
  "ds_read_b64_tr_b16 a[134:135], v218 offset:14416\n"
  "v_fma_f32 v37, v37, s73, v225\n"
  "v_mfma_f32_16x16x32_bf16 v[52:55], a[140:143], a[76:79], v[52:55]\n"
  "v_exp_f32 v36, v36\n"
  "v_mfma_f32_16x16x32_bf16 v[52:55], a[136:139], a[72:75], v[52:55]\n"
  "s_waitcnt lgkmcnt(4)\n"
  "v_exp_f32 v37, v37\n"
  "v_mfma_f32_16x16x32_bf16 v[100:103], v[12:15], a[124:127], v[100:103]\n"
  "v_cvt_pk_bf16_f32 v12, v0, v1\n"
  "v_cvt_pk_bf16_f32 v13, v2, v3\n"
  "v_mfma_f32_16x16x32_bf16 v[100:103], v[8:11], a[120:123], v[100:103]\n"
  "v_exp_f32 v21, v21\n"
  "v_mfma_f32_16x16x32_bf16 v[132:135], v[28:31], a[124:127], v[132:135]\n"
  "ds_read_b64_tr_b16 a[140:141], v219 offset:14400\n"
  "v_cvt_pk_bf16_f32 v28, v16, v17\n"
  "v_mfma_f32_16x16x32_bf16 v[132:135], v[24:27], a[120:123], v[132:135]\n"
  "ds_read_b64_tr_b16 a[142:143], v220 offset:14400\n"
  "v_cvt_pk_bf16_f32 v29, v18, v19\n"
  "v_mfma_f32_16x16x32_bf16 v[164:167], v[44:47], a[124:127], v[164:167]\n"
  "ds_read_b64_tr_b16 a[136:137], v219 offset:14416\n"
  "v_cvt_pk_bf16_f32 v44, v32, v33\n"
  "v_mfma_f32_16x16x32_bf16 v[164:167], v[40:43], a[120:123], v[164:167]\n"
  "ds_read_b64_tr_b16 a[138:139], v220 offset:14416\n"
  "v_cvt_pk_bf16_f32 v45, v34, v35\n"
  "v_mfma_f32_16x16x32_bf16 v[196:199], v[60:63], a[124:127], v[196:199]\n"
  "v_exp_f32 v22, v22\n"
  "v_mfma_f32_16x16x32_bf16 v[196:199], v[56:59], a[120:123], v[196:199]\n"
  "s_waitcnt lgkmcnt(4)\n"
  "v_exp_f32 v23, v23\n"
  "v_mfma_f32_16x16x32_bf16 a[244:247], a[156:159], a[128:131], a[244:247]\n"
  "v_cvt_pk_bf16_f32 v60, v48, v49\n"
  "v_cvt_pk_bf16_f32 v61, v50, v51\n"
  "v_mfma_f32_16x16x32_bf16 a[248:251], a[156:159], a[132:135], a[248:251]\n"
  "v_fma_f32 v52, v52, s73, v226\n"
  "v_fma_f32 v53, v53, s73, v226\n"
  "v_mfma_f32_16x16x32_bf16 v[200:203], v[56:59], a[128:131], v[200:203]\n"
  "v_exp_f32 v52, v52\n"
  "v_mfma_f32_16x16x32_bf16 v[204:207], v[56:59], a[132:135], v[204:207]\n"
  "v_exp_f32_e64 v53, v53\n"
  "v_mfma_f32_16x16x32_bf16 v[168:171], v[40:43], a[128:131], v[168:171]\n"
  "v_fma_f32 v54, v54, s73, v226\n"
  "v_fma_f32 v55, v55, s73, v226\n"
  "v_mfma_f32_16x16x32_bf16 v[172:175], v[40:43], a[132:135], v[172:175]\n"
  "s_waitcnt vmcnt(12) lgkmcnt(0)\n"
  "s_barrier\n"
  "ds_read_b128 a[124:127], v216 offset:24704\n"
  "v_mfma_f32_16x16x32_bf16 v[136:139], v[24:27], a[128:131], v[136:139]\n"
  "ds_read_b64 a[160:161], v231 offset:2048\n"
  "v_cvt_pk_bf16_f32 v62, v52, v53\n"
  "v_mfma_f32_16x16x32_bf16 v[140:143], v[24:27], a[132:135], v[140:143]\n"
  "ds_read_b64 a[162:163], v232 offset:2048\n"
  "v_fma_f32 v38, v38, s73, v225\n"
  "v_mfma_f32_16x16x32_bf16 v[104:107], v[8:11], a[128:131], v[104:107]\n"
  "ds_read_b128 a[120:123], v216 offset:24768\n"
  "v_fma_f32 v39, v39, s73, v225\n"
  "v_mfma_f32_16x16x32_bf16 v[108:111], v[8:11], a[132:135], v[108:111]\n"
  "ds_read_b128 a[128:131], v216 offset:25728\n"
  "v_cvt_pk_bf16_f32 v46, v36, v37\n"
  "v_mfma_f32_16x16x32_bf16 v[112:115], v[8:11], a[140:143], v[112:115]\n"
  "ds_read_b128 a[132:135], v216 offset:25792\n"
  "v_cvt_pk_bf16_f32 v30, v20, v21\n"
  "v_mfma_f32_16x16x32_bf16 v[116:119], v[8:11], a[136:139], v[116:119]\n"
  "s_mov_b32 m0, s67\n"
  "ds_read_b128 a[148:151], v216 offset:26752\n"
  "v_cvt_pk_bf16_f32 v31, v22, v23\n"
  "v_mfma_f32_16x16x32_bf16 v[144:147], v[24:27], a[140:143], v[144:147]\n"
  "ds_read_b128 a[144:147], v216 offset:26816\n"
  "v_fma_f32 v7, v7, s73, v223\n"
  "v_mfma_f32_16x16x32_bf16 v[148:151], v[24:27], a[136:139], v[148:151]\n"
  "v_exp_f32 v54, v54\n"
  "v_mfma_f32_16x16x32_bf16 v[176:179], v[40:43], a[140:143], v[176:179]\n"
  "v_exp_f32 v55, v55\n"
  "v_mfma_f32_16x16x32_bf16 v[180:183], v[40:43], a[136:139], v[180:183]\n"
  "buffer_load_dwordx4 v221, s[40:43], 0 offen offset:0 lds\n"
  "v_mfma_f32_16x16x32_bf16 v[208:211], v[56:59], a[140:143], v[208:211]\n"
  "s_addk_i32 m0, 896\n"
  "v_exp_f32 v38, v38\n"
  "v_mfma_f32_16x16x32_bf16 v[212:215], v[56:59], a[136:139], v[212:215]\n"
  "s_waitcnt lgkmcnt(4)\n"
  "v_exp_f32 v39, v39\n"
  "v_mfma_f32_16x16x32_bf16 v[64:67], a[124:127], a[96:99], v[248:251]\n"
  "v_exp_f32 v7, v7\n"
  "v_mfma_f32_16x16x32_bf16 v[64:67], a[120:123], a[100:103], v[64:67]\n"
  "s_waitcnt lgkmcnt(2)\n"
  "buffer_load_dwordx4 v221, s[40:43], 0 offen offset:128 lds\n"
  "v_cvt_pk_bf16_f32 v63, v54, v55\n"
  "v_mfma_f32_16x16x32_bf16 v[64:67], a[128:131], a[104:107], v[64:67]\n"
  "s_addk_i32 m0, 896\n"
  "v_cvt_pk_bf16_f32 v47, v38, v39\n"
  "v_cvt_pk_bf16_f32 v14, v4, v5\n"
  "v_mfma_f32_16x16x32_bf16 v[64:67], a[132:135], a[108:111], v[64:67]\n"
  "s_waitcnt lgkmcnt(0)\n"
  "v_cvt_pk_bf16_f32 v15, v6, v7\n"
  "v_add_f32 v252, v252, v4\n"
  "v_mfma_f32_16x16x32_bf16 v[64:67], a[148:151], a[112:115], v[64:67]\n"
  "v_add_f32 v252, v252, v5\n"
  "v_add_f32 v252, v252, v6\n"
  "v_mfma_f32_16x16x32_bf16 v[64:67], a[144:147], a[116:119], v[64:67]\n"
  "buffer_load_dwordx4 v221, s[40:43], 0 offen offset:256 lds\n"
  "v_add_f32 v252, v252, v7\n"
  "v_mfma_f32_16x16x32_bf16 v[0:3], a[144:147], a[20:23], v[248:251]\n"
  "s_add_u32 s40, s40, s31\n"
  "v_add_f32 v253, v253, v20\n"
  "v_add_f32 v253, v253, v21\n"
  "v_mfma_f32_16x16x32_bf16 v[0:3], a[148:151], a[16:19], v[0:3]\n"
  "s_addc_u32 s41, s41, 0\n"
  "v_add_f32 v253, v253, v22\n"
  "v_add_f32 v253, v253, v23\n"
  "v_mfma_f32_16x16x32_bf16 v[0:3], a[132:135], a[12:15], v[0:3]\n"
  "v_add_f32 v254, v254, v36\n"
  "v_add_f32 v254, v254, v37\n"
  "v_mfma_f32_16x16x32_bf16 v[0:3], a[128:131], a[8:11], v[0:3]\n"
  "s_mov_b32 m0, s59\n"
  "v_add_f32 v254, v254, v38\n"
  "v_add_f32 v254, v254, v39\n"
  "v_mfma_f32_16x16x32_bf16 v[0:3], a[120:123], a[4:7], v[0:3]\n"
  "v_add_f32 v255, v255, v52\n"
  "v_add_f32 v255, v255, v53\n"
  "v_mfma_f32_16x16x32_bf16 v[0:3], a[124:127], a[0:3], v[0:3]\n"
  "v_add_f32 v255, v255, v54\n"
  "v_add_f32 v255, v255, v55\n"
  "v_mfma_f32_16x16x32_bf16 v[16:19], a[124:127], a[24:27], v[248:251]\n"
  "ds_read_b64_tr_b16 v[244:245], v230 offset:11200\n"
  "ds_read_b64_tr_b16 v[246:247], v230 offset:11232\n"
  "v_mfma_f32_16x16x32_bf16 v[16:19], a[120:123], a[28:31], v[16:19]\n"
  "buffer_load_dwordx4 v221, s[32:35], 0 offen offset:0 lds\n"
  "ds_read_b64_tr_b16 a[252:253], v230 offset:12320\n"
  "v_mfma_f32_16x16x32_bf16 v[16:19], a[128:131], a[32:35], v[16:19]\n"
  "s_addk_i32 m0, 1024\n"
  "ds_read_b64_tr_b16 a[140:141], v217 offset:24704\n"
  "ds_read_b64_tr_b16 a[254:255], v230 offset:12352\n"
  "v_mfma_f32_16x16x32_bf16 v[16:19], a[132:135], a[36:39], v[16:19]\n"
  "s_mov_b64 s[32:33], s[44:45]\n"
  "ds_read_b64_tr_b16 a[142:143], v218 offset:24704\n"
  "v_fma_f32 v64, v64, s73, v227\n"
  "v_mfma_f32_16x16x32_bf16 v[16:19], a[148:151], a[40:43], v[16:19]\n"
  "ds_read_b64_tr_b16 a[136:137], v217 offset:25728\n"
  "v_fma_f32 v65, v65, s73, v227\n"
  "v_mfma_f32_16x16x32_bf16 v[16:19], a[144:147], a[44:47], v[16:19]\n"
  "buffer_load_dwordx4 v221, s[44:47], 0 offen offset:0 lds\n"
  "v_fma_f32 v66, v66, s73, v227\n"
  "v_mfma_f32_16x16x32_bf16 v[32:35], a[144:147], a[68:71], v[248:251]\n"
  "s_addk_i32 m0, 768\n"
  "ds_read_b64_tr_b16 a[138:139], v218 offset:25728\n"
  "v_fma_f32 v67, v67, s73, v227\n"
  "v_mfma_f32_16x16x32_bf16 v[32:35], a[148:151], a[64:67], v[32:35]\n"
  "v_exp_f32 v64, v64\n"
  "v_mfma_f32_16x16x32_bf16 v[32:35], a[132:135], a[60:63], v[32:35]\n"
  "v_exp_f32 v65, v65\n"
  "v_mfma_f32_16x16x32_bf16 v[32:35], a[128:131], a[56:59], v[32:35]\n"
  "buffer_load_dwordx4 v221, s[44:47], 0 offen offset:256 lds\n"
  "v_cvt_pk_bf16_f32 v64, v64, v65\n"
  "v_mfma_f32_16x16x32_bf16 v[32:35], a[120:123], a[52:55], v[32:35]\n"
  "s_add_u32 s44, s44, s31\n"
  "v_exp_f32 v66, v66\n"
  "v_mfma_f32_16x16x32_bf16 v[32:35], a[124:127], a[48:51], v[32:35]\n"
  "s_addc_u32 s45, s45, 0\n"
  "v_exp_f32 v67, v67\n"
  "v_mfma_f32_16x16x32_bf16 v[48:51], a[124:127], a[72:75], v[248:251]\n"
  "v_cvt_pk_bf16_f32 v65, v66, v67\n"
  "v_fma_f32 v0, v0, s73, v223\n"
  "v_mfma_f32_16x16x32_bf16 v[48:51], a[120:123], a[76:79], v[48:51]\n"
  "v_exp_f32 v0, v0\n"
  "v_mfma_f32_16x16x32_bf16 v[48:51], a[128:131], a[80:83], v[48:51]\n"
  "ds_read_b64_tr_b16 a[124:125], v217 offset:24720\n"
  "v_fma_f32 v1, v1, s73, v223\n"
  "v_mfma_f32_16x16x32_bf16 v[48:51], a[132:135], a[84:87], v[48:51]\n"
  "ds_read_b64_tr_b16 a[126:127], v218 offset:24720\n"
  "v_fma_f32 v2, v2, s73, v223\n"
  "v_mfma_f32_16x16x32_bf16 v[48:51], a[148:151], a[88:91], v[48:51]\n"
  "ds_read_b64_tr_b16 a[120:121], v217 offset:25744\n"
  "v_fma_f32 v3, v3, s73, v223\n"
  "v_mfma_f32_16x16x32_bf16 v[48:51], a[144:147], a[92:95], v[48:51]\n"
  "s_waitcnt lgkmcnt(3)\n"
  "ds_read_b64_tr_b16 a[122:123], v218 offset:25744\n"
  "v_add_f32 v252, v252, v0\n"
  "v_mfma_f32_16x16x32_bf16 a[228:231], a[156:159], a[140:143], a[228:231]\n"
  "v_exp_f32 v1, v1\n"
  "v_mfma_f32_16x16x32_bf16 a[228:231], a[160:163], a[136:139], a[228:231]\n"
  "v_exp_f32 v2, v2\n"
  "v_mfma_f32_16x16x32_bf16 a[212:215], v[56:59], a[140:143], a[212:215]\n"
  "v_exp_f32 v3, v3\n"
  "v_mfma_f32_16x16x32_bf16 a[212:215], v[60:63], a[136:139], a[212:215]\n"
  "v_add_f32 v252, v252, v1\n"
  "v_add_f32 v252, v252, v2\n"
  "v_mfma_f32_16x16x32_bf16 a[196:199], v[40:43], a[140:143], a[196:199]\n"
  "v_add_f32 v252, v252, v3\n"
  "v_fma_f32 v16, v16, s73, v224\n"
  "v_mfma_f32_16x16x32_bf16 a[196:199], v[44:47], a[136:139], a[196:199]\n"
  "v_exp_f32 v16, v16\n"
  "v_mfma_f32_16x16x32_bf16 a[180:183], v[24:27], a[140:143], a[180:183]\n"
  "ds_read_b64_tr_b16 a[128:129], v219 offset:24704\n"
  "v_fma_f32 v17, v17, s73, v224\n"
  "v_mfma_f32_16x16x32_bf16 a[180:183], v[28:31], a[136:139], a[180:183]\n"
  "ds_read_b64_tr_b16 a[130:131], v220 offset:24704\n"
  "v_fma_f32 v18, v18, s73, v224\n"
  "v_mfma_f32_16x16x32_bf16 a[164:167], v[8:11], a[140:143], a[164:167]\n"
  "ds_read_b64_tr_b16 a[132:133], v219 offset:25728\n"
  "v_fma_f32 v19, v19, s73, v224\n"
  "v_mfma_f32_16x16x32_bf16 a[164:167], v[12:15], a[136:139], a[164:167]\n"
  "ds_read_b64_tr_b16 a[134:135], v220 offset:25728\n"
  "v_add_f32 v253, v253, v16\n"
  "v_mfma_f32_16x16x32_bf16 v[84:87], a[156:159], a[152:155], v[84:87]\n"
  "v_exp_f32 v17, v17\n"
  "v_mfma_f32_16x16x32_bf16 v[84:87], a[160:163], a[152:155], v[84:87]\n"
  "s_waitcnt lgkmcnt(4)\n"
  "v_exp_f32 v18, v18\n"
  "v_mfma_f32_16x16x32_bf16 a[168:171], v[8:11], a[124:127], a[168:171]\n"
  "v_exp_f32 v19, v19\n"
  "v_mfma_f32_16x16x32_bf16 a[168:171], v[12:15], a[120:123], a[168:171]\n"
  "v_add_f32 v253, v253, v17\n"
  "v_add_f32 v253, v253, v18\n"
  "v_mfma_f32_16x16x32_bf16 a[184:187], v[24:27], a[124:127], a[184:187]\n"
  "ds_read_b128 a[140:143], v235 offset:24704\n"
  "v_add_f32 v253, v253, v19\n"
  "v_mfma_f32_16x16x32_bf16 a[184:187], v[28:31], a[120:123], a[184:187]\n"
  "ds_read_b128 a[136:139], v235 offset:24768\n"
  "v_fma_f32 v32, v32, s73, v225\n"
  "v_mfma_f32_16x16x32_bf16 a[200:203], v[40:43], a[124:127], a[200:203]\n"
  "ds_read_b128 a[148:151], v235 offset:25728\n"
  "v_fma_f32 v33, v33, s73, v225\n"
  "v_mfma_f32_16x16x32_bf16 a[200:203], v[44:47], a[120:123], a[200:203]\n"
  "ds_read_b128 a[144:147], v235 offset:25792\n"
  "v_fma_f32 v34, v34, s73, v225\n"
  "v_mfma_f32_16x16x32_bf16 a[216:219], v[56:59], a[124:127], a[216:219]\n"
  "v_exp_f32 v32, v32\n"
  "v_mfma_f32_16x16x32_bf16 a[216:219], v[60:63], a[120:123], a[216:219]\n"
  "v_exp_f32 v33, v33\n"
  "v_mfma_f32_16x16x32_bf16 a[232:235], a[156:159], a[124:127], a[232:235]\n"
  "ds_read_b128 a[124:127], v235 offset:26752\n"
  "v_fma_f32 v35, v35, s73, v225\n"
  "v_mfma_f32_16x16x32_bf16 a[232:235], a[160:163], a[120:123], a[232:235]\n"
  "s_waitcnt lgkmcnt(5)\n"
  "ds_read_b128 a[120:123], v235 offset:26816\n"
  "v_add_f32 v254, v254, v32\n"
  "v_mfma_f32_16x16x32_bf16 a[220:223], v[56:59], a[128:131], a[220:223]\n"
  "v_exp_f32 v34, v34\n"
  "v_mfma_f32_16x16x32_bf16 a[220:223], v[60:63], a[132:135], a[220:223]\n"
  "ds_write_b64 v231, v[64:65] offset:0\n"
  "v_mfma_f32_16x16x32_bf16 a[204:207], v[40:43], a[128:131], a[204:207]\n"
  "v_exp_f32_e64 v35, v35\n"
  "v_mfma_f32_16x16x32_bf16 a[204:207], v[44:47], a[132:135], a[204:207]\n"
  "buffer_store_dwordx4 v[244:247], v228, s[52:55], s81 offen offset:128 nt\n"
  "v_add_f32 v254, v254, v33\n"
  "v_add_f32 v254, v254, v34\n"
  "v_add_f32_e64 v254, v254, v35\n"
  "v_mfma_f32_16x16x32_bf16 a[188:191], v[24:27], a[128:131], a[188:191]\n"
  "ds_read_b64_tr_b16 v[244:245], v230 offset:13440\n"
  "ds_read_b64_tr_b16 v[246:247], v230 offset:13472\n"
  "v_mfma_f32_16x16x32_bf16 a[188:191], v[28:31], a[132:135], a[188:191]\n"
  "v_fma_f32 v48, v48, s73, v226\n"
  "v_fma_f32 v49, v49, s73, v226\n"
  "v_mfma_f32_16x16x32_bf16 a[172:175], v[8:11], a[128:131], a[172:175]\n"
  "v_exp_f32 v48, v48\n"
  "v_mfma_f32_16x16x32_bf16 a[172:175], v[12:15], a[132:135], a[172:175]\n"
  "s_waitcnt lgkmcnt(7)\n"
  "v_exp_f32 v49, v49\n"
  "v_mfma_f32_16x16x32_bf16 v[4:7], a[140:143], a[0:3], v[248:251]\n"
  "v_fma_f32 v50, v50, s73, v226\n"
  "v_fma_f32 v51, v51, s73, v226\n"
  "v_mfma_f32_16x16x32_bf16 v[4:7], a[136:139], a[4:7], v[4:7]\n"
  "s_waitcnt lgkmcnt(5)\n"
  "v_exp_f32 v50, v50\n"
  "v_mfma_f32_16x16x32_bf16 v[4:7], a[148:151], a[8:11], v[4:7]\n"
  "v_exp_f32 v51, v51\n"
  "v_mfma_f32_16x16x32_bf16 v[4:7], a[144:147], a[12:15], v[4:7]\n"
  "s_waitcnt lgkmcnt(3)\n"
  "v_add_f32 v255, v255, v48\n"
  "v_add_f32 v255, v255, v49\n"
  "v_mfma_f32_16x16x32_bf16 v[4:7], a[124:127], a[16:19], v[4:7]\n"
  "v_add_f32 v255, v255, v50\n"
  "v_add_f32 v255, v255, v51\n"
  "v_mfma_f32_16x16x32_bf16 v[4:7], a[120:123], a[20:23], v[4:7]\n"
  "v_and_b32 v236, 0x7fff7fff, v236\n"
  "v_lshlrev_b32 v237, 16, v236\n"
  "v_mfma_f32_16x16x32_bf16 v[20:23], a[120:123], a[44:47], v[248:251]\n"
  "v_max_u32 v236, v236, v237\n"
  "v_mfma_f32_16x16x32_bf16 v[20:23], a[124:127], a[40:43], v[20:23]\n"
  "s_nop 0\n"
  "v_max_u32_dpp v236, v236, v236 quad_perm:[1,0,3,2] row_mask:0xf bank_mask:0xf\n"
  "v_mfma_f32_16x16x32_bf16 v[20:23], a[144:147], a[36:39], v[20:23]\n"
  "ds_read_b64_tr_b16 a[128:129], v219 offset:24720\n"
  "v_fma_f32 v4, v4, s73, v223\n"
  "v_mfma_f32_16x16x32_bf16 v[20:23], a[148:151], a[32:35], v[20:23]\n"
  "ds_read_b64_tr_b16 a[130:131], v220 offset:24720\n"
  "v_fma_f32 v5, v5, s73, v223\n"
  "v_mfma_f32_16x16x32_bf16 v[20:23], a[136:139], a[28:31], v[20:23]\n"
  "ds_read_b64_tr_b16 a[132:133], v219 offset:25744\n"
  "v_fma_f32 v6, v6, s73, v223\n"
  "v_mfma_f32_16x16x32_bf16 v[20:23], a[140:143], a[24:27], v[20:23]\n"
  "ds_read_b64_tr_b16 a[134:135], v220 offset:25744\n"
  "v_fma_f32 v7, v7, s73, v223\n"
  "v_mfma_f32_16x16x32_bf16 v[36:39], a[140:143], a[48:51], v[248:251]\n"
  "v_exp_f32 v4, v4\n"
  "v_mfma_f32_16x16x32_bf16 v[36:39], a[136:139], a[52:55], v[36:39]\n"
  "v_exp_f32 v5, v5\n"
  "v_mfma_f32_16x16x32_bf16 v[36:39], a[148:151], a[56:59], v[36:39]\n"
  "v_exp_f32 v6, v6\n"
  "v_mfma_f32_16x16x32_bf16 v[36:39], a[144:147], a[60:63], v[36:39]\n"
  "v_exp_f32 v7, v7\n"
  "v_mfma_f32_16x16x32_bf16 v[36:39], a[124:127], a[64:67], v[36:39]\n"
  "v_fma_f32 v20, v20, s73, v224\n"
  "v_fma_f32 v21, v21, s73, v224\n"
  "v_mfma_f32_16x16x32_bf16 v[36:39], a[120:123], a[68:71], v[36:39]\n"
  "buffer_store_dwordx4 a[252:255], v228, s[52:55], s81 offen offset:3200 nt\n"
  "v_exp_f32_e64 v20, v20\n"
  "v_fma_f32 v22, v22, s73, v224\n"
  "v_mfma_f32_16x16x32_bf16 v[52:55], a[120:123], a[92:95], v[248:251]\n"
  "ds_read_b64_tr_b16 a[120:121], v217 offset:26752\n"
  "v_fma_f32 v23, v23, s73, v224\n"
  "v_mfma_f32_16x16x32_bf16 v[52:55], a[124:127], a[88:91], v[52:55]\n"
  "ds_read_b64_tr_b16 a[122:123], v218 offset:26752\n"
  "v_add_f32 v252, v252, v4\n"
  "v_mfma_f32_16x16x32_bf16 v[52:55], a[144:147], a[84:87], v[52:55]\n"
  "ds_read_b64_tr_b16 a[124:125], v217 offset:26768\n"
  "v_fma_f32 v36, v36, s73, v225\n"
  "v_mfma_f32_16x16x32_bf16 v[52:55], a[148:151], a[80:83], v[52:55]\n"
  "ds_read_b64_tr_b16 a[126:127], v218 offset:26768\n"
  "v_fma_f32 v37, v37, s73, v225\n"
  "v_mfma_f32_16x16x32_bf16 v[52:55], a[136:139], a[76:79], v[52:55]\n"
  "v_exp_f32 v36, v36\n"
  "v_mfma_f32_16x16x32_bf16 v[52:55], a[140:143], a[72:75], v[52:55]\n"
  "s_waitcnt lgkmcnt(4)\n"
  "v_exp_f32 v37, v37\n"
  "v_mfma_f32_16x16x32_bf16 a[176:179], v[8:11], a[128:131], a[176:179]\n"
  "v_cvt_pk_bf16_f32 v8, v0, v1\n"
  "v_cvt_pk_bf16_f32 v9, v2, v3\n"
  "v_mfma_f32_16x16x32_bf16 a[176:179], v[12:15], a[132:135], a[176:179]\n"
  "v_exp_f32 v21, v21\n"
  "v_mfma_f32_16x16x32_bf16 a[192:195], v[24:27], a[128:131], a[192:195]\n"
  "ds_read_b64_tr_b16 a[136:137], v219 offset:26752\n"
  "v_cvt_pk_bf16_f32 v24, v16, v17\n"
  "v_mfma_f32_16x16x32_bf16 a[192:195], v[28:31], a[132:135], a[192:195]\n"
  "ds_read_b64_tr_b16 a[138:139], v220 offset:26752\n"
  "v_cvt_pk_bf16_f32 v25, v18, v19\n"
  "v_mfma_f32_16x16x32_bf16 a[208:211], v[40:43], a[128:131], a[208:211]\n"
  "ds_read_b64_tr_b16 a[140:141], v219 offset:26768\n"
  "v_cvt_pk_bf16_f32 v40, v32, v33\n"
  "v_mfma_f32_16x16x32_bf16 a[208:211], v[44:47], a[132:135], a[208:211]\n"
  "ds_read_b64_tr_b16 a[142:143], v220 offset:26768\n"
  "v_cvt_pk_bf16_f32 v41, v34, v35\n"
  "v_mfma_f32_16x16x32_bf16 a[224:227], v[56:59], a[128:131], a[224:227]\n"
  "v_exp_f32 v22, v22\n"
  "v_mfma_f32_16x16x32_bf16 a[224:227], v[60:63], a[132:135], a[224:227]\n"
  "s_waitcnt lgkmcnt(4)\n"
  "v_exp_f32 v23, v23\n"
  "v_mfma_f32_16x16x32_bf16 a[244:247], a[160:163], a[120:123], a[244:247]\n"
  "v_cvt_pk_bf16_f32 v56, v48, v49\n"
  "v_cvt_pk_bf16_f32 v57, v50, v51\n"
  "v_mfma_f32_16x16x32_bf16 a[248:251], a[160:163], a[124:127], a[248:251]\n"
  "v_fma_f32 v52, v52, s73, v226\n"
  "v_fma_f32 v53, v53, s73, v226\n"
  "v_mfma_f32_16x16x32_bf16 v[200:203], v[60:63], a[120:123], v[200:203]\n"
  "v_exp_f32_e64 v52, v52\n"
  "v_mfma_f32_16x16x32_bf16 v[204:207], v[60:63], a[124:127], v[204:207]\n"
  "buffer_store_dwordx4 v[244:247], v228, s[52:55], s82 offen offset:128 nt\n"
  "v_exp_f32 v53, v53\n"
  "v_fma_f32 v54, v54, s73, v226\n"
  "v_mfma_f32_16x16x32_bf16 v[168:171], v[44:47], a[120:123], v[168:171]\n"
  "v_exp_f32 v54, v54\n"
  "v_mfma_f32_16x16x32_bf16 v[172:175], v[44:47], a[124:127], v[172:175]\n"
  "s_waitcnt vmcnt(12) lgkmcnt(0)\n"
  "s_barrier\n"
  "ds_read_b128 a[128:131], v216 offset:0\n"
  "v_mfma_f32_16x16x32_bf16 v[136:139], v[28:31], a[120:123], v[136:139]\n"
  "ds_read_b64 a[156:157], v231 offset:0\n"
  "v_fma_f32 v55, v55, s73, v226\n"
  "v_mfma_f32_16x16x32_bf16 v[140:143], v[28:31], a[124:127], v[140:143]\n"
  "ds_read_b64 a[158:159], v232 offset:0\n"
  "v_cvt_pk_bf16_f32 v58, v52, v53\n"
  "v_mfma_f32_16x16x32_bf16 v[104:107], v[12:15], a[120:123], v[104:107]\n"
  "ds_read_b128 a[132:135], v216 offset:64\n"
  "v_fma_f32 v38, v38, s73, v225\n"
  "v_mfma_f32_16x16x32_bf16 v[108:111], v[12:15], a[124:127], v[108:111]\n"
  "ds_read_b128 a[120:123], v216 offset:1024\n"
  "v_fma_f32 v39, v39, s73, v225\n"
  "v_mfma_f32_16x16x32_bf16 v[112:115], v[12:15], a[136:139], v[112:115]\n"
  "ds_read_b128 a[124:127], v216 offset:1088\n"
  "v_cvt_pk_bf16_f32 v42, v36, v37\n"
  "v_mfma_f32_16x16x32_bf16 v[116:119], v[12:15], a[140:143], v[116:119]\n"
  "s_mov_b32 m0, s68\n"
  "ds_read_b128 a[144:147], v216 offset:2048\n"
  "v_cvt_pk_bf16_f32 v26, v20, v21\n"
  "v_mfma_f32_16x16x32_bf16 v[144:147], v[28:31], a[136:139], v[144:147]\n"
  "ds_read_b128 a[148:151], v216 offset:2112\n"
  "v_cvt_pk_bf16_f32 v27, v22, v23\n"
  "v_mfma_f32_16x16x32_bf16 v[148:151], v[28:31], a[140:143], v[148:151]\n"
  "v_exp_f32 v55, v55\n"
  "v_mfma_f32_16x16x32_bf16 v[176:179], v[44:47], a[136:139], v[176:179]\n"
  "v_exp_f32 v38, v38\n"
  "v_mfma_f32_16x16x32_bf16 v[180:183], v[44:47], a[140:143], v[180:183]\n"
  "buffer_load_dwordx4 v221, s[40:43], 0 offen offset:0 lds\n"
  "v_mfma_f32_16x16x32_bf16 v[208:211], v[60:63], a[136:139], v[208:211]\n"
  "s_addk_i32 m0, 896\n"
  "v_exp_f32 v39, v39\n"
  "v_mfma_f32_16x16x32_bf16 v[212:215], v[60:63], a[140:143], v[212:215]\n"
  "s_waitcnt lgkmcnt(4)\n"
  "v_cvt_pk_bf16_f32 v59, v54, v55\n"
  "v_cvt_pk_bf16_f32 v43, v38, v39\n"
  "v_mfma_f32_16x16x32_bf16 v[64:67], a[128:131], a[96:99], v[248:251]\n"
  "v_cvt_pk_bf16_f32 v10, v4, v5\n"
  "v_cvt_pk_bf16_f32 v11, v6, v7\n"
  "v_mfma_f32_16x16x32_bf16 v[64:67], a[132:135], a[100:103], v[64:67]\n"
  "s_waitcnt lgkmcnt(2)\n"
  "buffer_load_dwordx4 v221, s[40:43], 0 offen offset:128 lds\n"
  "v_add_f32 v252, v252, v5\n"
  "v_mfma_f32_16x16x32_bf16 v[64:67], a[120:123], a[104:107], v[64:67]\n"
  "s_addk_i32 m0, 896\n"
  "v_add_f32 v252, v252, v6\n"
  "v_add_f32 v252, v252, v7\n"
  "v_mfma_f32_16x16x32_bf16 v[64:67], a[124:127], a[108:111], v[64:67]\n"
  "s_waitcnt lgkmcnt(0)\n"
  "v_add_f32 v253, v253, v20\n"
  "v_add_f32 v253, v253, v21\n"
  "v_mfma_f32_16x16x32_bf16 v[64:67], a[144:147], a[112:115], v[64:67]\n"
  "v_add_f32 v253, v253, v22\n"
  "v_add_f32 v253, v253, v23\n"
  "v_mfma_f32_16x16x32_bf16 v[64:67], a[148:151], a[116:119], v[64:67]\n"
  "buffer_load_dwordx4 v221, s[40:43], 0 offen offset:256 lds\n"
  "v_add_f32 v254, v254, v36\n"
  "v_mfma_f32_16x16x32_bf16 v[0:3], a[148:151], a[20:23], v[248:251]\n"
  "s_add_u32 s40, s40, s31\n"
  "v_add_f32 v254, v254, v37\n"
  "v_add_f32 v254, v254, v38\n"
  "v_mfma_f32_16x16x32_bf16 v[0:3], a[144:147], a[16:19], v[0:3]\n"
  "s_addc_u32 s41, s41, 0\n"
  "v_add_f32 v254, v254, v39\n"
  "v_add_f32 v255, v255, v52\n"
  "v_mfma_f32_16x16x32_bf16 v[0:3], a[124:127], a[12:15], v[0:3]\n"
  "v_add_f32 v255, v255, v53\n"
  "v_add_f32 v255, v255, v54\n"
  "v_mfma_f32_16x16x32_bf16 v[0:3], a[120:123], a[8:11], v[0:3]\n"
  "s_add_u32 m0, s60, -128\n"
  "ds_read_b64_tr_b16 a[252:253], v230 offset:14560\n"
  "v_add_f32 v255, v255, v55\n"
  "v_mfma_f32_16x16x32_bf16 v[0:3], a[132:135], a[4:7], v[0:3]\n"
  "ds_read_b64_tr_b16 a[254:255], v230 offset:14592\n"
  "ds_read_b64_tr_b16 v[244:245], v230 offset:15680\n"
  "v_mfma_f32_16x16x32_bf16 v[0:3], a[128:131], a[0:3], v[0:3]\n"
  "ds_read_b64_tr_b16 v[246:247], v230 offset:15712\n"
  "v_fma_f32 v64, v64, s73, v227\n"
  "v_mfma_f32_16x16x32_bf16 v[16:19], a[128:131], a[24:27], v[248:251]\n"
  "v_exp_f32 v64, v64\n"
  "v_mfma_f32_16x16x32_bf16 v[16:19], a[132:135], a[28:31], v[16:19]\n"
  "buffer_load_dwordx4 v221, s[32:35], 0 offen offset:128 lds\n"
  "v_fma_f32 v65, v65, s73, v227\n"
  "v_mfma_f32_16x16x32_bf16 v[16:19], a[120:123], a[32:35], v[16:19]\n"
  "s_addk_i32 m0, 1024\n"
  "ds_read_b64_tr_b16 a[136:137], v217 offset:0\n"
  "v_fma_f32 v66, v66, s73, v227\n"
  "v_mfma_f32_16x16x32_bf16 v[16:19], a[124:127], a[36:39], v[16:19]\n"
  "s_mov_b64 s[32:33], s[44:45]\n"
  "ds_read_b64_tr_b16 a[138:139], v218 offset:0\n"
  "v_fma_f32 v67, v67, s73, v227\n"
  "v_mfma_f32_16x16x32_bf16 v[16:19], a[144:147], a[40:43], v[16:19]\n"
  "ds_read_b64_tr_b16 a[140:141], v217 offset:1024\n"
  "v_fma_f32 v0, v0, s73, v223\n"
  "v_mfma_f32_16x16x32_bf16 v[16:19], a[148:151], a[44:47], v[16:19]\n"
  "buffer_load_dwordx4 v221, s[44:47], 0 offen offset:128 lds\n"
  "v_fma_f32 v1, v1, s73, v223\n"
  "v_mfma_f32_16x16x32_bf16 v[32:35], a[148:151], a[68:71], v[248:251]\n"
  "s_addk_i32 m0, 896\n"
  "ds_read_b64_tr_b16 a[142:143], v218 offset:1024\n"
  "v_fma_f32 v2, v2, s73, v223\n"
  "v_mfma_f32_16x16x32_bf16 v[32:35], a[144:147], a[64:67], v[32:35]\n"
  "v_exp_f32 v65, v65\n"
  "v_mfma_f32_16x16x32_bf16 v[32:35], a[124:127], a[60:63], v[32:35]\n"
  "v_exp_f32 v66, v66\n"
  "v_mfma_f32_16x16x32_bf16 v[32:35], a[120:123], a[56:59], v[32:35]\n"
  "buffer_load_dwordx4 v221, s[44:47], 0 offen offset:256 lds\n"
  "v_cvt_pk_bf16_f32 v64, v64, v65\n"
  "v_mfma_f32_16x16x32_bf16 v[32:35], a[132:135], a[52:55], v[32:35]\n"
  "s_add_u32 s44, s44, s31\n"
  "v_exp_f32 v67, v67\n"
  "v_mfma_f32_16x16x32_bf16 v[32:35], a[128:131], a[48:51], v[32:35]\n"
  "s_addc_u32 s45, s45, 0\n"
  "v_exp_f32 v0, v0\n"
  "v_mfma_f32_16x16x32_bf16 v[48:51], a[128:131], a[72:75], v[248:251]\n"
  "ds_read_b64_tr_b16 a[128:129], v217 offset:16\n"
  "v_cvt_pk_bf16_f32 v65, v66, v67\n"
  "v_mfma_f32_16x16x32_bf16 v[48:51], a[132:135], a[76:79], v[48:51]\n"
  "ds_read_b64_tr_b16 a[130:131], v218 offset:16\n"
  "v_fma_f32 v3, v3, s73, v223\n"
  "v_mfma_f32_16x16x32_bf16 v[48:51], a[120:123], a[80:83], v[48:51]\n"
  "ds_read_b64_tr_b16 a[132:133], v217 offset:1040\n"
  "v_add_f32 v252, v252, v0\n"
  "v_mfma_f32_16x16x32_bf16 v[48:51], a[124:127], a[84:87], v[48:51]\n"
  "s_add_u32 m0, s72, 11200\n"
  "ds_read_b64_tr_b16 a[134:135], v218 offset:1040\n"
  "v_mfma_f32_16x16x32_bf16 v[48:51], a[144:147], a[88:91], v[48:51]\n"
  "v_exp_f32 v1, v1\n"
  "v_mfma_f32_16x16x32_bf16 v[48:51], a[148:151], a[92:95], v[48:51]\n"
  "s_waitcnt lgkmcnt(4)\n"
  "v_exp_f32 v2, v2\n"
  "v_mfma_f32_16x16x32_bf16 a[236:239], a[160:163], a[136:139], a[236:239]\n"
  "v_exp_f32 v3, v3\n"
  "v_mfma_f32_16x16x32_bf16 a[236:239], a[156:159], a[140:143], a[236:239]\n"
  "buffer_load_dwordx4 v221, s[48:51], 0 offen offset:0 nt lds\n"
  "v_add_f32 v252, v252, v1\n"
  "v_mfma_f32_16x16x32_bf16 v[184:187], v[60:63], a[136:139], v[184:187]\n"
  "v_add_f32 v252, v252, v2\n"
  "v_add_f32 v252, v252, v3\n"
  "v_mfma_f32_16x16x32_bf16 v[184:187], v[56:59], a[140:143], v[184:187]\n"
  "v_fma_f32 v16, v16, s73, v224\n"
  "v_fma_f32 v17, v17, s73, v224\n"
  "v_mfma_f32_16x16x32_bf16 v[152:155], v[44:47], a[136:139], v[152:155]\n"
  "ds_read_b64_tr_b16 a[120:121], v219 offset:0\n"
  "v_fma_f32 v18, v18, s73, v224\n"
  "v_mfma_f32_16x16x32_bf16 v[152:155], v[40:43], a[140:143], v[152:155]\n"
  "s_add_u32 m0, s72, 9216\n"
  "ds_read_b64_tr_b16 a[122:123], v220 offset:0\n"
  "v_mfma_f32_16x16x32_bf16 v[120:123], v[28:31], a[136:139], v[120:123]\n"
  "ds_read_b64_tr_b16 a[124:125], v219 offset:1024\n"
  "v_fma_f32 v19, v19, s73, v224\n"
  "v_mfma_f32_16x16x32_bf16 v[120:123], v[24:27], a[140:143], v[120:123]\n"
  "ds_read_b64_tr_b16 a[126:127], v220 offset:1024\n"
  "v_fma_f32 v32, v32, s73, v225\n"
  "v_mfma_f32_16x16x32_bf16 v[88:91], v[12:15], a[136:139], v[88:91]\n"
  "v_exp_f32 v16, v16\n"
  "v_mfma_f32_16x16x32_bf16 v[88:91], v[8:11], a[140:143], v[88:91]\n"
  "s_waitcnt lgkmcnt(4)\n"
  "buffer_load_dwordx4 v221, s[48:51], 0 offen offset:3072 nt lds\n"
  "v_add_f32 v253, v253, v16\n"
  "v_mfma_f32_16x16x32_bf16 v[92:95], v[12:15], a[128:131], v[92:95]\n"
  "v_exp_f32 v17, v17\n"
  "v_mfma_f32_16x16x32_bf16 v[92:95], v[8:11], a[132:135], v[92:95]\n"
  "v_exp_f32 v18, v18\n"
  "v_mfma_f32_16x16x32_bf16 v[124:127], v[28:31], a[128:131], v[124:127]\n"
  "ds_read_b128 a[136:139], v235 offset:0\n"
  "v_add_f32 v253, v253, v17\n"
  "v_mfma_f32_16x16x32_bf16 v[124:127], v[24:27], a[132:135], v[124:127]\n"
  "ds_read_b128 a[140:143], v235 offset:64\n"
  "v_add_f32 v253, v253, v18\n"
  "v_mfma_f32_16x16x32_bf16 v[156:159], v[44:47], a[128:131], v[156:159]\n"
  "ds_read_b128 a[144:147], v235 offset:1024\n"
  "v_fma_f32 v33, v33, s73, v225\n"
  "v_mfma_f32_16x16x32_bf16 v[156:159], v[40:43], a[132:135], v[156:159]\n"
  "ds_read_b128 a[148:151], v235 offset:1088\n"
  "v_fma_f32 v34, v34, s73, v225\n"
  "v_mfma_f32_16x16x32_bf16 v[188:191], v[60:63], a[128:131], v[188:191]\n"
  "v_exp_f32 v19, v19\n"
  "v_mfma_f32_16x16x32_bf16 v[188:191], v[56:59], a[132:135], v[188:191]\n"
  "v_exp_f32 v32, v32\n"
  "v_mfma_f32_16x16x32_bf16 a[240:243], a[160:163], a[128:131], a[240:243]\n"
  "ds_read_b128 a[128:131], v235 offset:2048\n"
  "v_add_f32 v253, v253, v19\n"
  "v_mfma_f32_16x16x32_bf16 a[240:243], a[156:159], a[132:135], a[240:243]\n"
  "s_waitcnt lgkmcnt(5)\n"
  "ds_read_b128 a[132:135], v235 offset:2112\n"
  "v_fma_f32 v35, v35, s73, v225\n"
  "v_mfma_f32_16x16x32_bf16 v[192:195], v[60:63], a[120:123], v[192:195]\n"
  "v_exp_f32 v33, v33\n"
  "v_mfma_f32_16x16x32_bf16 v[192:195], v[56:59], a[124:127], v[192:195]\n"
  "ds_write_b64 v231, v[64:65] offset:2048\n"
  "v_mfma_f32_16x16x32_bf16 v[160:163], v[44:47], a[120:123], v[160:163]\n"
  "v_exp_f32 v34, v34\n"
  "v_mfma_f32_16x16x32_bf16 v[160:163], v[40:43], a[124:127], v[160:163]\n"
  "v_exp_f32 v35, v35\n"
  "v_mfma_f32_16x16x32_bf16 v[128:131], v[28:31], a[120:123], v[128:131]\n"
  "v_add_f32 v254, v254, v32\n"
  "v_add_f32 v254, v254, v33\n"
  "v_mfma_f32_16x16x32_bf16 v[128:131], v[24:27], a[124:127], v[128:131]\n"
  "buffer_store_dwordx4 a[252:255], v228, s[52:55], s82 offen offset:3200 nt\n"
  "v_add_f32 v254, v254, v34\n"
  "v_add_f32 v254, v254, v35\n"
  "v_max_u32_dpp v236, v236, v236 quad_perm:[2,3,0,1] row_mask:0xf bank_mask:0xf\n"
  "v_mfma_f32_16x16x32_bf16 v[96:99], v[12:15], a[120:123], v[96:99]\n"
  "ds_read_b64_tr_b16 a[252:253], v230 offset:16800\n"
  "ds_read_b64_tr_b16 a[254:255], v230 offset:16832\n"
  "v_mfma_f32_16x16x32_bf16 v[96:99], v[8:11], a[124:127], v[96:99]\n"
  "s_waitcnt lgkmcnt(7)\n"
  "v_add_u32 v236, -1, v236\n"
  "v_fma_f32 v48, v48, s73, v226\n"
  "v_mfma_f32_16x16x32_bf16 v[4:7], a[136:139], a[0:3], v[248:251]\n"
  "v_exp_f32 v48, v48\n"
  "v_mfma_f32_16x16x32_bf16 v[4:7], a[140:143], a[4:7], v[4:7]\n"
  "s_waitcnt lgkmcnt(5)\n"
  "v_min_u32_dpp v236, v236, v236 row_shr:4 row_mask:0xf bank_mask:0xf\n"
  "v_fma_f32 v49, v49, s73, v226\n"
  "v_mfma_f32_16x16x32_bf16 v[4:7], a[144:147], a[8:11], v[4:7]\n"
  "v_exp_f32 v49, v49\n"
  "v_mfma_f32_16x16x32_bf16 v[4:7], a[148:151], a[12:15], v[4:7]\n"
  "s_waitcnt lgkmcnt(3)\n"
  "v_min_u32_dpp v236, v236, v236 row_shr:8 row_mask:0xf bank_mask:0xf\n"
  "v_fma_f32 v50, v50, s73, v226\n"
  "v_mfma_f32_16x16x32_bf16 v[4:7], a[128:131], a[16:19], v[4:7]\n"
  "v_exp_f32 v50, v50\n"
  "v_mfma_f32_16x16x32_bf16 v[4:7], a[132:135], a[20:23], v[4:7]\n"
  "v_min_u32_dpp v236, v236, v236 row_bcast:15 row_mask:0xa bank_mask:0xf\n"
  "v_fma_f32 v51, v51, s73, v226\n"
  "v_mfma_f32_16x16x32_bf16 v[20:23], a[132:135], a[44:47], v[248:251]\n"
  "v_exp_f32 v51, v51\n"
  "v_mfma_f32_16x16x32_bf16 v[20:23], a[128:131], a[40:43], v[20:23]\n"
  "v_min_u32_dpp v236, v236, v236 row_bcast:31 row_mask:0xc bank_mask:0xf\n"
  "v_add_f32 v255, v255, v48\n"
  "v_mfma_f32_16x16x32_bf16 v[20:23], a[148:151], a[36:39], v[20:23]\n"
  "ds_read_b64_tr_b16 a[120:121], v219 offset:16\n"
  "v_add_f32 v255, v255, v49\n"
  "v_mfma_f32_16x16x32_bf16 v[20:23], a[144:147], a[32:35], v[20:23]\n"
  "ds_read_b64_tr_b16 a[122:123], v220 offset:16\n"
  "v_add_f32 v255, v255, v50\n"
  "v_mfma_f32_16x16x32_bf16 v[20:23], a[140:143], a[28:31], v[20:23]\n"
  "ds_read_b64_tr_b16 a[124:125], v219 offset:1040\n"
  "v_add_f32 v255, v255, v51\n"
  "v_mfma_f32_16x16x32_bf16 v[20:23], a[136:139], a[24:27], v[20:23]\n"
  "ds_read_b64_tr_b16 a[126:127], v220 offset:1040\n"
  "v_fma_f32 v4, v4, s73, v223\n"
  "v_mfma_f32_16x16x32_bf16 v[36:39], a[136:139], a[48:51], v[248:251]\n"
  "v_readlane_b32 s65, v236, 63\n"
  "v_mfma_f32_16x16x32_bf16 v[36:39], a[140:143], a[52:55], v[36:39]\n"
  "v_exp_f32 v4, v4\n"
  "v_mfma_f32_16x16x32_bf16 v[36:39], a[144:147], a[56:59], v[36:39]\n"
  "s_add_u32 s65, s65, 1\n"
  "v_fma_f32 v20, v20, s73, v224\n"
  "v_fma_f32 v21, v21, s73, v224\n"
  "v_mfma_f32_16x16x32_bf16 v[36:39], a[148:151], a[60:63], v[36:39]\n"
  "s_lshr_b32 s65, s65, 23\n"
  "v_exp_f32 v20, v20\n"
  "v_mfma_f32_16x16x32_bf16 v[36:39], a[128:131], a[64:67], v[36:39]\n"
  "s_sub_i32 s65, 0x77, s65\n"
  "v_exp_f32 v21, v21\n"
  "v_mfma_f32_16x16x32_bf16 v[36:39], a[132:135], a[68:71], v[36:39]\n"
  "s_and_b32 s65, s65, -4\n"
  "buffer_store_dwordx4 v[244:247], v228, s[52:55], s83 offen offset:128 nt\n"
  "v_fma_f32 v22, v22, s73, v224\n"
  "v_exp_f32 v22, v22\n"
  "v_mfma_f32_16x16x32_bf16 v[52:55], a[132:135], a[92:95], v[248:251]\n"
  "s_max_i32 s65, s65, 0xffffffc4\n"
  "ds_read_b64_tr_b16 a[132:133], v217 offset:2048\n"
  "v_add_f32 v252, v252, v4\n"
  "v_mfma_f32_16x16x32_bf16 v[52:55], a[128:131], a[88:91], v[52:55]\n"
  "ds_read_b64_tr_b16 a[134:135], v218 offset:2048\n"
  "v_cvt_f32_i32 v236, s65\n"
  "v_mfma_f32_16x16x32_bf16 v[52:55], a[148:151], a[84:87], v[52:55]\n"
  "ds_read_b64_tr_b16 a[128:129], v217 offset:2064\n"
  "v_fma_f32 v36, v36, s73, v225\n"
  "v_mfma_f32_16x16x32_bf16 v[52:55], a[144:147], a[80:83], v[52:55]\n"
  "ds_read_b64_tr_b16 a[130:131], v218 offset:2064\n"
  "v_fma_f32 v37, v37, s73, v225\n"
  "v_mfma_f32_16x16x32_bf16 v[52:55], a[140:143], a[76:79], v[52:55]\n"
  "s_add_u32 s65, s65, 0xab\n"
  "v_readfirstlane_b32 s64, v236\n"
  "v_mfma_f32_16x16x32_bf16 v[52:55], a[136:139], a[72:75], v[52:55]\n"
  "s_waitcnt lgkmcnt(4)\n"
  "v_exp_f32 v36, v36\n"
  "v_mfma_f32_16x16x32_bf16 v[100:103], v[12:15], a[120:123], v[100:103]\n"
  "v_cvt_pk_bf16_f32 v12, v0, v1\n"
  "v_cvt_pk_bf16_f32 v13, v2, v3\n"
  "v_mfma_f32_16x16x32_bf16 v[100:103], v[8:11], a[124:127], v[100:103]\n"
  "s_cmpk_gt_u32 s65, 0xd3\n"
  "s_cselect_b32 s65, 0, s65\n"
  "v_exp_f32 v37, v37\n"
  "v_mfma_f32_16x16x32_bf16 v[132:135], v[28:31], a[120:123], v[132:135]\n"
  "s_lshl_b32 s65, s65, 23\n"
  "ds_read_b64_tr_b16 a[140:141], v219 offset:2048\n"
  "v_cvt_pk_bf16_f32 v28, v16, v17\n"
  "v_mfma_f32_16x16x32_bf16 v[132:135], v[24:27], a[124:127], v[132:135]\n"
  "ds_read_b64_tr_b16 a[142:143], v220 offset:2048\n"
  "v_cvt_pk_bf16_f32 v29, v18, v19\n"
  "v_mfma_f32_16x16x32_bf16 v[164:167], v[44:47], a[120:123], v[164:167]\n"
  "ds_read_b64_tr_b16 a[136:137], v219 offset:2064\n"
  "v_cvt_pk_bf16_f32 v44, v32, v33\n"
  "v_mfma_f32_16x16x32_bf16 v[164:167], v[40:43], a[124:127], v[164:167]\n"
  "ds_read_b64_tr_b16 a[138:139], v220 offset:2064\n"
  "v_cvt_pk_bf16_f32 v45, v34, v35\n"
  "v_mfma_f32_16x16x32_bf16 v[196:199], v[60:63], a[120:123], v[196:199]\n"
  "v_cvt_pk_bf16_f32 v60, v48, v49\n"
  "v_cvt_pk_bf16_f32 v61, v50, v51\n"
  "v_mfma_f32_16x16x32_bf16 v[196:199], v[56:59], a[124:127], v[196:199]\n"
  "s_waitcnt lgkmcnt(4)\n"
  "v_fma_f32 v52, v52, s73, v226\n"
  "v_fma_f32 v53, v53, s73, v226\n"
  "v_mfma_f32_16x16x32_bf16 a[244:247], a[156:159], a[132:135], a[244:247]\n"
  "v_exp_f32 v52, v52\n"
  "v_mfma_f32_16x16x32_bf16 a[248:251], a[156:159], a[128:131], a[248:251]\n"
  "v_exp_f32_e64 v53, v53\n"
  "v_mfma_f32_16x16x32_bf16 v[200:203], v[56:59], a[132:135], v[200:203]\n"
  "v_fma_f32 v54, v54, s73, v226\n"
  "v_fma_f32 v55, v55, s73, v226\n"
  "v_mfma_f32_16x16x32_bf16 v[204:207], v[56:59], a[128:131], v[204:207]\n"
  "buffer_store_dwordx4 a[252:255], v228, s[52:55], s83 offen offset:3200 nt\n"
  "v_exp_f32_e64 v54, v54\n"
  "v_cvt_pk_bf16_f32 v62, v52, v53\n"
  "v_mfma_f32_16x16x32_bf16 v[168:171], v[40:43], a[132:135], v[168:171]\n"
  "v_exp_f32_e64 v55, v55\n"
  "v_mfma_f32_16x16x32_bf16 v[172:175], v[40:43], a[128:131], v[172:175]\n"
  "s_waitcnt vmcnt(14) lgkmcnt(0)\n"
  "s_barrier\n"
  "ds_read_b128 a[120:123], v216 offset:12352\n"
  "v_mfma_f32_16x16x32_bf16 v[136:139], v[24:27], a[132:135], v[136:139]\n"
  "ds_read_b64 a[160:161], v231 offset:2048\n"
  "v_cvt_pk_bf16_f32 v63, v54, v55\n"
  "v_mfma_f32_16x16x32_bf16 v[140:143], v[24:27], a[128:131], v[140:143]\n"
  "ds_read_b64 a[162:163], v232 offset:2048\n"
  "v_fma_f32 v38, v38, s73, v225\n"
  "v_mfma_f32_16x16x32_bf16 v[104:107], v[8:11], a[132:135], v[104:107]\n"
  "ds_read_b128 a[124:127], v216 offset:12416\n"
  "v_fma_f32 v39, v39, s73, v225\n"
  "v_mfma_f32_16x16x32_bf16 v[108:111], v[8:11], a[128:131], v[108:111]\n"
  "ds_read_b128 a[132:135], v216 offset:13376\n"
  "v_cvt_pk_bf16_f32 v46, v36, v37\n"
  "v_mfma_f32_16x16x32_bf16 v[112:115], v[8:11], a[140:143], v[112:115]\n"
  "s_add_u32 s71, s92, s82\n"
  "ds_read_b128 a[128:131], v216 offset:13440\n"
  "v_fma_f32 v23, v23, s73, v224\n"
  "v_mfma_f32_16x16x32_bf16 v[116:119], v[8:11], a[136:139], v[116:119]\n"
  "s_mov_b32 m0, s66\n"
  "ds_read_b128 a[148:151], v216 offset:14400\n"
  "v_cvt_pk_bf16_f32 v30, v20, v21\n"
  "v_mfma_f32_16x16x32_bf16 v[144:147], v[24:27], a[140:143], v[144:147]\n"
  "s_add_u32 s48, s84, s71\n"
  "ds_read_b128 a[144:147], v216 offset:14464\n"
  "v_fma_f32 v5, v5, s73, v223\n"
  "v_mfma_f32_16x16x32_bf16 v[148:151], v[24:27], a[136:139], v[148:151]\n"
  "s_addc_u32 s49, s85, 0\n"
  "v_exp_f32 v38, v38\n"
  "v_mfma_f32_16x16x32_bf16 v[176:179], v[40:43], a[140:143], v[176:179]\n"
  "s_sub_i32 s71, s38, s82\n"
  "v_exp_f32 v39, v39\n"
  "v_mfma_f32_16x16x32_bf16 v[180:183], v[40:43], a[136:139], v[180:183]\n"
  "buffer_load_dwordx4 v221, s[40:43], 0 offen offset:0 lds\n"
  "v_mfma_f32_16x16x32_bf16 v[208:211], v[56:59], a[140:143], v[208:211]\n"
  "s_addk_i32 m0, 896\n"
  "v_exp_f32 v23, v23\n"
  "v_mfma_f32_16x16x32_bf16 v[212:215], v[56:59], a[136:139], v[212:215]\n"
  "s_waitcnt lgkmcnt(4)\n"
  "v_exp_f32 v5, v5\n"
  "v_mfma_f32_16x16x32_bf16 v[64:67], a[120:123], a[96:99], v[248:251]\n"
  "s_max_i32 s50, s71, 0\n"
  "v_cvt_pk_bf16_f32 v47, v38, v39\n"
  "v_cvt_pk_bf16_f32 v31, v22, v23\n"
  "v_mfma_f32_16x16x32_bf16 v[64:67], a[124:127], a[100:103], v[64:67]\n"
  "s_waitcnt lgkmcnt(2)\n"
  "buffer_load_dwordx4 v221, s[40:43], 0 offen offset:128 lds\n"
  "v_fma_f32 v6, v6, s73, v223\n"
  "v_mfma_f32_16x16x32_bf16 v[64:67], a[132:135], a[104:107], v[64:67]\n"
  "s_addk_i32 m0, 896\n"
  "v_exp_f32 v6, v6\n"
  "v_mfma_f32_16x16x32_bf16 v[64:67], a[128:131], a[108:111], v[64:67]\n"
  "s_waitcnt lgkmcnt(0)\n"
  "v_fma_f32 v7, v7, s73, v223\n"
  "v_cvt_pk_bf16_f32 v14, v4, v5\n"
  "v_mfma_f32_16x16x32_bf16 v[64:67], a[148:151], a[112:115], v[64:67]\n"
  "v_exp_f32 v7, v7\n"
  "v_mfma_f32_16x16x32_bf16 v[64:67], a[144:147], a[116:119], v[64:67]\n"
  "buffer_load_dwordx4 v221, s[40:43], 0 offen offset:256 lds\n"
  "v_cvt_pk_bf16_f32 v15, v6, v7\n"
  "v_mfma_f32_16x16x32_bf16 v[0:3], a[144:147], a[20:23], v[248:251]\n"
  "s_add_u32 s40, s40, s31\n"
  "v_add_f32 v252, v252, v5\n"
  "v_add_f32 v252, v252, v6\n"
  "v_mfma_f32_16x16x32_bf16 v[0:3], a[148:151], a[16:19], v[0:3]\n"
  "s_addc_u32 s41, s41, 0\n"
  "v_bfe_u32 v236, v231, 3, 6\n"
  "v_lshlrev_b32 v236, 7, v236\n"
  "v_mfma_f32_16x16x32_bf16 v[0:3], a[128:131], a[12:15], v[0:3]\n"
  "s_cmp_eq_u32 s76, s24\n"
  "s_cselect_b32 s22, s25, 0\n"
  "v_add_f32 v252, v252, v7\n"
  "v_add_f32 v253, v253, v20\n"
  "v_mfma_f32_16x16x32_bf16 v[0:3], a[132:135], a[8:11], v[0:3]\n"
  "s_mov_b32 m0, s58\n"
  "v_add_f32 v253, v253, v21\n"
  "v_add_f32 v253, v253, v22\n"
  "v_mfma_f32_16x16x32_bf16 v[0:3], a[124:127], a[4:7], v[0:3]\n"
  "v_add_f32 v253, v253, v23\n"
  "v_add_f32 v254, v254, v36\n"
  "v_mfma_f32_16x16x32_bf16 v[0:3], a[120:123], a[0:3], v[0:3]\n"
  "v_add_f32 v254, v254, v37\n"
  "v_add_f32 v254, v254, v38\n"
  "v_mfma_f32_16x16x32_bf16 v[16:19], a[120:123], a[24:27], v[248:251]\n"
  "v_add_f32 v254, v254, v39\n"
  "v_add_f32 v255, v255, v52\n"
  "v_mfma_f32_16x16x32_bf16 v[16:19], a[124:127], a[28:31], v[16:19]\n"
  "buffer_load_dwordx4 v221, s[32:35], 0 offen offset:0 lds\n"
  "v_add_f32 v255, v255, v53\n"
  "v_mfma_f32_16x16x32_bf16 v[16:19], a[132:135], a[32:35], v[16:19]\n"
  "s_addk_i32 m0, 1024\n"
  "ds_read_b64_tr_b16 a[140:141], v217 offset:12352\n"
  "v_add_f32 v255, v255, v54\n"
  "v_mfma_f32_16x16x32_bf16 v[16:19], a[128:131], a[36:39], v[16:19]\n"
  "s_mov_b64 s[32:33], s[44:45]\n"
  "ds_read_b64_tr_b16 a[142:143], v218 offset:12352\n"
  "v_add_f32 v255, v255, v55\n"
  "v_mfma_f32_16x16x32_bf16 v[16:19], a[148:151], a[40:43], v[16:19]\n"
  "ds_read_b64_tr_b16 a[136:137], v217 offset:13376\n"
  "v_fma_f32 v64, v64, s73, v227\n"
  "v_mfma_f32_16x16x32_bf16 v[16:19], a[144:147], a[44:47], v[16:19]\n"
  "buffer_load_dwordx4 v221, s[44:47], 0 offen offset:0 lds\n"
  "v_fma_f32 v65, v65, s73, v227\n"
  "v_mfma_f32_16x16x32_bf16 v[32:35], a[144:147], a[68:71], v[248:251]\n"
  "s_addk_i32 m0, 768\n"
  "ds_read_b64_tr_b16 a[138:139], v218 offset:13376\n"
  "v_fma_f32 v66, v66, s73, v227\n"
  "v_mfma_f32_16x16x32_bf16 v[32:35], a[148:151], a[64:67], v[32:35]\n"
  "v_exp_f32 v64, v64\n"
  "v_mfma_f32_16x16x32_bf16 v[32:35], a[128:131], a[60:63], v[32:35]\n"
  "v_exp_f32 v65, v65\n"
  "v_mfma_f32_16x16x32_bf16 v[32:35], a[132:135], a[56:59], v[32:35]\n"
  "buffer_load_dwordx4 v221, s[44:47], 0 offen offset:256 lds\n"
  "v_fma_f32 v67, v67, s73, v227\n"
  "v_mfma_f32_16x16x32_bf16 v[32:35], a[124:127], a[52:55], v[32:35]\n"
  "s_add_u32 s44, s44, s31\n"
  "v_exp_f32 v66, v66\n"
  "v_mfma_f32_16x16x32_bf16 v[32:35], a[120:123], a[48:51], v[32:35]\n"
  "s_addc_u32 s45, s45, 0\n"
  "v_exp_f32 v67, v67\n"
  "v_mfma_f32_16x16x32_bf16 v[48:51], a[120:123], a[72:75], v[248:251]\n"
  "s_mov_b32 m0, s29\n"
  "v_cvt_pk_bf16_f32 v64, v64, v65\n"
  "v_cvt_pk_bf16_f32 v65, v66, v67\n"
  "v_mfma_f32_16x16x32_bf16 v[48:51], a[124:127], a[76:79], v[48:51]\n"
  "v_fma_f32 v0, v0, s73, v223\n"
  "v_fma_f32 v1, v1, s73, v223\n"
  "v_mfma_f32_16x16x32_bf16 v[48:51], a[132:135], a[80:83], v[48:51]\n"
  "ds_read_b64_tr_b16 a[120:121], v217 offset:12368\n"
  "v_fma_f32 v2, v2, s73, v223\n"
  "v_mfma_f32_16x16x32_bf16 v[48:51], a[128:131], a[84:87], v[48:51]\n"
  "buffer_load_dword v236, s[20:23], 0 offen lds\n"
  "v_fma_f32 v3, v3, s73, v223\n"
  "v_mfma_f32_16x16x32_bf16 v[48:51], a[148:151], a[88:91], v[48:51]\n"
  "s_add_u32 m0, s72, 13440\n"
  "ds_read_b64_tr_b16 a[122:123], v218 offset:12368\n"
  "v_mfma_f32_16x16x32_bf16 v[48:51], a[144:147], a[92:95], v[48:51]\n"
  "s_waitcnt lgkmcnt(2)\n"
  "ds_read_b64_tr_b16 a[124:125], v217 offset:13392\n"
  "v_fma_f32 v16, v16, s73, v224\n"
  "v_mfma_f32_16x16x32_bf16 a[228:231], a[156:159], a[140:143], a[228:231]\n"
  "ds_read_b64_tr_b16 a[126:127], v218 offset:13392\n"
  "v_fma_f32 v17, v17, s73, v224\n"
  "v_mfma_f32_16x16x32_bf16 a[228:231], a[160:163], a[136:139], a[228:231]\n"
  "buffer_load_dwordx4 v221, s[48:51], 0 offen offset:0 nt lds\n"
  "v_fma_f32 v18, v18, s73, v224\n"
  "v_mfma_f32_16x16x32_bf16 a[212:215], v[56:59], a[140:143], a[212:215]\n"
  "v_exp_f32 v0, v0\n"
  "v_mfma_f32_16x16x32_bf16 a[212:215], v[60:63], a[136:139], a[212:215]\n"
  "v_exp_f32 v1, v1\n"
  "v_mfma_f32_16x16x32_bf16 a[196:199], v[40:43], a[140:143], a[196:199]\n"
  "v_exp_f32 v2, v2\n"
  "v_mfma_f32_16x16x32_bf16 a[196:199], v[44:47], a[136:139], a[196:199]\n"
  "s_add_u32 m0, s72, 11456\n"
  "v_exp_f32 v3, v3\n"
  "v_mfma_f32_16x16x32_bf16 a[180:183], v[24:27], a[140:143], a[180:183]\n"
  "ds_read_b64_tr_b16 a[132:133], v219 offset:12352\n"
  "v_add_f32 v252, v252, v0\n"
  "v_mfma_f32_16x16x32_bf16 a[180:183], v[28:31], a[136:139], a[180:183]\n"
  "ds_read_b64_tr_b16 a[134:135], v220 offset:12352\n"
  "v_add_f32 v252, v252, v1\n"
  "v_mfma_f32_16x16x32_bf16 a[164:167], v[8:11], a[140:143], a[164:167]\n"
  "ds_read_b64_tr_b16 a[128:129], v219 offset:13376\n"
  "v_add_f32 v252, v252, v2\n"
  "v_mfma_f32_16x16x32_bf16 a[164:167], v[12:15], a[136:139], a[164:167]\n"
  "buffer_load_dwordx4 v221, s[48:51], 0 offen offset:3072 nt lds\n"
  "v_add_f32 v252, v252, v3\n"
  "v_mfma_f32_16x16x32_bf16 v[84:87], a[156:159], a[152:155], v[84:87]\n"
  "ds_read_b64_tr_b16 a[130:131], v220 offset:13376\n"
  "v_fma_f32 v19, v19, s73, v224\n"
  "v_mfma_f32_16x16x32_bf16 v[84:87], a[160:163], a[152:155], v[84:87]\n"
  "s_waitcnt lgkmcnt(4)\n"
  "v_exp_f32 v16, v16\n"
  "v_mfma_f32_16x16x32_bf16 a[168:171], v[8:11], a[120:123], a[168:171]\n"
  "v_exp_f32 v17, v17\n"
  "v_mfma_f32_16x16x32_bf16 a[168:171], v[12:15], a[124:127], a[168:171]\n"
  "s_add_u32 m0, s72, 15552\n"
  "v_exp_f32 v18, v18\n"
  "v_mfma_f32_16x16x32_bf16 a[184:187], v[24:27], a[120:123], a[184:187]\n"
  "ds_read_b128 a[140:143], v235 offset:12352\n"
  "v_add_f32 v253, v253, v16\n"
  "v_mfma_f32_16x16x32_bf16 a[184:187], v[28:31], a[124:127], a[184:187]\n"
  "ds_read_b128 a[136:139], v235 offset:12416\n"
  "v_add_f32 v253, v253, v17\n"
  "v_mfma_f32_16x16x32_bf16 a[200:203], v[40:43], a[120:123], a[200:203]\n"
  "ds_read_b128 a[148:151], v235 offset:13376\n"
  "v_add_f32 v253, v253, v18\n"
  "v_mfma_f32_16x16x32_bf16 a[200:203], v[44:47], a[124:127], a[200:203]\n"
  "buffer_load_dwordx4 v221, s[48:51], 0 offen offset:128 nt lds\n"
  "v_fma_f32 v32, v32, s73, v225\n"
  "v_mfma_f32_16x16x32_bf16 a[216:219], v[56:59], a[120:123], a[216:219]\n"
  "ds_read_b128 a[144:147], v235 offset:13440\n"
  "v_fma_f32 v33, v33, s73, v225\n"
  "v_mfma_f32_16x16x32_bf16 a[216:219], v[60:63], a[124:127], a[216:219]\n"
  "v_exp_f32 v19, v19\n"
  "v_mfma_f32_16x16x32_bf16 a[232:235], a[156:159], a[120:123], a[232:235]\n"
  "ds_read_b128 a[120:123], v235 offset:14400\n"
  "v_add_f32 v253, v253, v19\n"
  "v_mfma_f32_16x16x32_bf16 a[232:235], a[160:163], a[124:127], a[232:235]\n"
  "s_waitcnt lgkmcnt(5)\n"
  "ds_read_b128 a[124:127], v235 offset:14464\n"
  "v_fma_f32 v34, v34, s73, v225\n"
  "v_mfma_f32_16x16x32_bf16 a[220:223], v[56:59], a[132:135], a[220:223]\n"
  "s_add_u32 m0, s72, 13568\n"
  "v_exp_f32 v32, v32\n"
  "v_mfma_f32_16x16x32_bf16 a[220:223], v[60:63], a[128:131], a[220:223]\n"
  "ds_write_b64 v231, v[64:65] offset:0\n"
  "v_mfma_f32_16x16x32_bf16 a[204:207], v[40:43], a[132:135], a[204:207]\n"
  "v_exp_f32 v33, v33\n"
  "v_mfma_f32_16x16x32_bf16 a[204:207], v[44:47], a[128:131], a[204:207]\n"
  "buffer_load_dwordx4 v221, s[48:51], 0 offen offset:3200 nt lds\n"
  "v_fma_f32 v35, v35, s73, v225\n"
  "v_mfma_f32_16x16x32_bf16 a[188:191], v[24:27], a[132:135], a[188:191]\n"
  "v_exp_f32 v34, v34\n"
  "v_mfma_f32_16x16x32_bf16 a[188:191], v[28:31], a[128:131], a[188:191]\n"
  "v_exp_f32 v35, v35\n"
  "v_mfma_f32_16x16x32_bf16 a[172:175], v[8:11], a[132:135], a[172:175]\n"
  "v_add_f32 v254, v254, v32\n"
  "v_add_f32 v254, v254, v33\n"
  "v_mfma_f32_16x16x32_bf16 a[172:175], v[12:15], a[128:131], a[172:175]\n"
  "s_waitcnt lgkmcnt(5)\n"
  "v_add_f32 v254, v254, v34\n"
  "v_add_f32 v254, v254, v35\n"
  "v_mfma_f32_16x16x32_bf16 v[4:7], a[140:143], a[0:3], v[248:251]\n"
  "v_fma_f32 v48, v48, s73, v226\n"
  "v_fma_f32 v49, v49, s73, v226\n"
  "v_mfma_f32_16x16x32_bf16 v[4:7], a[136:139], a[4:7], v[4:7]\n"
  "s_waitcnt lgkmcnt(3)\n"
  "v_exp_f32 v48, v48\n"
  "v_mfma_f32_16x16x32_bf16 v[4:7], a[148:151], a[8:11], v[4:7]\n"
  "v_exp_f32 v49, v49\n"
  "v_mfma_f32_16x16x32_bf16 v[4:7], a[144:147], a[12:15], v[4:7]\n"
  "s_waitcnt lgkmcnt(1)\n"
  "v_fma_f32 v50, v50, s73, v226\n"
  "v_fma_f32 v51, v51, s73, v226\n"
  "v_mfma_f32_16x16x32_bf16 v[4:7], a[120:123], a[16:19], v[4:7]\n"
  "v_exp_f32 v50, v50\n"
  "v_mfma_f32_16x16x32_bf16 v[4:7], a[124:127], a[20:23], v[4:7]\n"
  "v_exp_f32 v51, v51\n"
  "v_mfma_f32_16x16x32_bf16 v[20:23], a[124:127], a[44:47], v[248:251]\n"
  "v_add_f32 v255, v255, v48\n"
  "v_add_f32 v255, v255, v49\n"
  "v_mfma_f32_16x16x32_bf16 v[20:23], a[120:123], a[40:43], v[20:23]\n"
  "v_add_f32 v255, v255, v50\n"
  "v_add_f32 v255, v255, v51\n"
  "v_mfma_f32_16x16x32_bf16 v[20:23], a[144:147], a[36:39], v[20:23]\n"
  "ds_read_b64_tr_b16 a[132:133], v219 offset:12368\n"
  "v_fma_f32 v4, v4, s73, v223\n"
  "v_mfma_f32_16x16x32_bf16 v[20:23], a[148:151], a[32:35], v[20:23]\n"
  "ds_read_b64_tr_b16 a[134:135], v220 offset:12368\n"
  "v_fma_f32 v5, v5, s73, v223\n"
  "v_mfma_f32_16x16x32_bf16 v[20:23], a[136:139], a[28:31], v[20:23]\n"
  "ds_read_b64_tr_b16 a[128:129], v219 offset:13392\n"
  "v_fma_f32 v6, v6, s73, v223\n"
  "v_mfma_f32_16x16x32_bf16 v[20:23], a[140:143], a[24:27], v[20:23]\n"
  "ds_read_b64_tr_b16 a[130:131], v220 offset:13392\n"
  "v_fma_f32 v7, v7, s73, v223\n"
  "v_mfma_f32_16x16x32_bf16 v[36:39], a[140:143], a[48:51], v[248:251]\n"
  "v_exp_f32 v4, v4\n"
  "v_mfma_f32_16x16x32_bf16 v[36:39], a[136:139], a[52:55], v[36:39]\n"
  "v_exp_f32 v5, v5\n"
  "v_mfma_f32_16x16x32_bf16 v[36:39], a[148:151], a[56:59], v[36:39]\n"
  "v_exp_f32 v6, v6\n"
  "v_mfma_f32_16x16x32_bf16 v[36:39], a[144:147], a[60:63], v[36:39]\n"
  "v_exp_f32 v7, v7\n"
  "v_mfma_f32_16x16x32_bf16 v[36:39], a[120:123], a[64:67], v[36:39]\n"
  "v_fma_f32 v20, v20, s73, v224\n"
  "v_fma_f32 v21, v21, s73, v224\n"
  "v_mfma_f32_16x16x32_bf16 v[36:39], a[124:127], a[68:71], v[36:39]\n"
  "v_exp_f32 v20, v20\n"
  "v_mfma_f32_16x16x32_bf16 v[52:55], a[124:127], a[92:95], v[248:251]\n"
  "ds_read_b64_tr_b16 a[124:125], v217 offset:14400\n"
  "v_fma_f32 v22, v22, s73, v224\n"
  "v_mfma_f32_16x16x32_bf16 v[52:55], a[120:123], a[88:91], v[52:55]\n"
  "ds_read_b64_tr_b16 a[126:127], v218 offset:14400\n"
  "v_fma_f32 v23, v23, s73, v224\n"
  "v_mfma_f32_16x16x32_bf16 v[52:55], a[144:147], a[84:87], v[52:55]\n"
  "ds_read_b64_tr_b16 a[120:121], v217 offset:14416\n"
  "v_fma_f32 v36, v36, s73, v225\n"
  "v_mfma_f32_16x16x32_bf16 v[52:55], a[148:151], a[80:83], v[52:55]\n"
  "ds_read_b64_tr_b16 a[122:123], v218 offset:14416\n"
  "v_fma_f32 v37, v37, s73, v225\n"
  "v_mfma_f32_16x16x32_bf16 v[52:55], a[136:139], a[76:79], v[52:55]\n"
  "v_exp_f32 v36, v36\n"
  "v_mfma_f32_16x16x32_bf16 v[52:55], a[140:143], a[72:75], v[52:55]\n"
  "s_waitcnt lgkmcnt(4)\n"
  "v_exp_f32 v37, v37\n"
  "v_mfma_f32_16x16x32_bf16 a[176:179], v[8:11], a[132:135], a[176:179]\n"
  "v_cvt_pk_bf16_f32 v8, v0, v1\n"
  "v_cvt_pk_bf16_f32 v9, v2, v3\n"
  "v_mfma_f32_16x16x32_bf16 a[176:179], v[12:15], a[128:131], a[176:179]\n"
  "v_exp_f32 v21, v21\n"
  "v_mfma_f32_16x16x32_bf16 a[192:195], v[24:27], a[132:135], a[192:195]\n"
  "ds_read_b64_tr_b16 a[136:137], v219 offset:14400\n"
  "v_cvt_pk_bf16_f32 v24, v16, v17\n"
  "v_mfma_f32_16x16x32_bf16 a[192:195], v[28:31], a[128:131], a[192:195]\n"
  "ds_read_b64_tr_b16 a[138:139], v220 offset:14400\n"
  "v_cvt_pk_bf16_f32 v25, v18, v19\n"
  "v_mfma_f32_16x16x32_bf16 a[208:211], v[40:43], a[132:135], a[208:211]\n"
  "ds_read_b64_tr_b16 a[140:141], v219 offset:14416\n"
  "v_cvt_pk_bf16_f32 v40, v32, v33\n"
  "v_mfma_f32_16x16x32_bf16 a[208:211], v[44:47], a[128:131], a[208:211]\n"
  "ds_read_b64_tr_b16 a[142:143], v220 offset:14416\n"
  "v_cvt_pk_bf16_f32 v41, v34, v35\n"
  "v_mfma_f32_16x16x32_bf16 a[224:227], v[56:59], a[132:135], a[224:227]\n"
  "v_exp_f32 v22, v22\n"
  "v_mfma_f32_16x16x32_bf16 a[224:227], v[60:63], a[128:131], a[224:227]\n"
  "s_waitcnt lgkmcnt(4)\n"
  "v_exp_f32 v23, v23\n"
  "v_mfma_f32_16x16x32_bf16 a[244:247], a[160:163], a[124:127], a[244:247]\n"
  "v_cvt_pk_bf16_f32 v56, v48, v49\n"
  "v_cvt_pk_bf16_f32 v57, v50, v51\n"
  "v_mfma_f32_16x16x32_bf16 a[248:251], a[160:163], a[120:123], a[248:251]\n"
  "v_fma_f32 v52, v52, s73, v226\n"
  "v_fma_f32 v53, v53, s73, v226\n"
  "v_mfma_f32_16x16x32_bf16 v[200:203], v[60:63], a[124:127], v[200:203]\n"
  "v_exp_f32 v52, v52\n"
  "v_mfma_f32_16x16x32_bf16 v[204:207], v[60:63], a[120:123], v[204:207]\n"
  "v_exp_f32 v53, v53\n"
  "v_mfma_f32_16x16x32_bf16 v[168:171], v[44:47], a[124:127], v[168:171]\n"
  "v_fma_f32 v54, v54, s73, v226\n"
  "v_fma_f32 v55, v55, s73, v226\n"
  "v_mfma_f32_16x16x32_bf16 v[172:175], v[44:47], a[120:123], v[172:175]\n"
  "s_waitcnt vmcnt(16) lgkmcnt(0)\n"
  "s_barrier\n"
  "ds_read_b128 a[132:135], v216 offset:24704\n"
  "v_mfma_f32_16x16x32_bf16 v[136:139], v[28:31], a[124:127], v[136:139]\n"
  "ds_read_b64 a[156:157], v231 offset:0\n"
  "v_cvt_pk_bf16_f32 v58, v52, v53\n"
  "v_mfma_f32_16x16x32_bf16 v[140:143], v[28:31], a[120:123], v[140:143]\n"
  "ds_read_b64 a[158:159], v232 offset:0\n"
  "v_fma_f32 v38, v38, s73, v225\n"
  "v_mfma_f32_16x16x32_bf16 v[104:107], v[12:15], a[124:127], v[104:107]\n"
  "ds_read_b128 a[128:131], v216 offset:24768\n"
  "v_fma_f32 v39, v39, s73, v225\n"
  "v_mfma_f32_16x16x32_bf16 v[108:111], v[12:15], a[120:123], v[108:111]\n"
  "ds_read_b128 a[124:127], v216 offset:25728\n"
  "v_cvt_pk_bf16_f32 v42, v36, v37\n"
  "v_mfma_f32_16x16x32_bf16 v[112:115], v[12:15], a[136:139], v[112:115]\n"
  "ds_read_b128 a[120:123], v216 offset:25792\n"
  "v_cvt_pk_bf16_f32 v26, v20, v21\n"
  "v_mfma_f32_16x16x32_bf16 v[116:119], v[12:15], a[140:143], v[116:119]\n"
  "s_mov_b32 m0, s67\n"
  "ds_read_b128 a[144:147], v216 offset:26752\n"
  "v_cvt_pk_bf16_f32 v27, v22, v23\n"
  "v_mfma_f32_16x16x32_bf16 v[144:147], v[28:31], a[136:139], v[144:147]\n"
  "ds_read_b128 a[148:151], v216 offset:26816\n"
  "v_cvt_pk_bf16_f32 v10, v4, v5\n"
  "v_mfma_f32_16x16x32_bf16 v[148:151], v[28:31], a[140:143], v[148:151]\n"
  "v_exp_f32 v54, v54\n"
  "v_mfma_f32_16x16x32_bf16 v[176:179], v[44:47], a[136:139], v[176:179]\n"
  "v_exp_f32 v55, v55\n"
  "v_mfma_f32_16x16x32_bf16 v[180:183], v[44:47], a[140:143], v[180:183]\n"
  "buffer_load_dwordx4 v221, s[40:43], 0 offen offset:0 lds\n"
  "v_mfma_f32_16x16x32_bf16 v[208:211], v[60:63], a[136:139], v[208:211]\n"
  "s_addk_i32 m0, 896\n"
  "v_exp_f32 v38, v38\n"
  "v_mfma_f32_16x16x32_bf16 v[212:215], v[60:63], a[140:143], v[212:215]\n"
  "s_waitcnt lgkmcnt(4)\n"
  "v_exp_f32 v39, v39\n"
  "v_mfma_f32_16x16x32_bf16 v[64:67], a[132:135], a[96:99], v[248:251]\n"
  "v_cvt_pk_bf16_f32 v59, v54, v55\n"
  "v_cvt_pk_bf16_f32 v43, v38, v39\n"
  "v_mfma_f32_16x16x32_bf16 v[64:67], a[128:131], a[100:103], v[64:67]\n"
  "s_waitcnt lgkmcnt(2)\n"
  "buffer_load_dwordx4 v221, s[40:43], 0 offen offset:128 lds\n"
  "v_cvt_pk_bf16_f32 v11, v6, v7\n"
  "v_mfma_f32_16x16x32_bf16 v[64:67], a[124:127], a[104:107], v[64:67]\n"
  "s_addk_i32 m0, 896\n"
  "v_add_f32 v252, v252, v4\n"
  "v_add_f32 v252, v252, v5\n"
  "v_mfma_f32_16x16x32_bf16 v[64:67], a[120:123], a[108:111], v[64:67]\n"
  "s_waitcnt lgkmcnt(0)\n"
  "v_add_f32 v252, v252, v6\n"
  "v_add_f32 v252, v252, v7\n"
  "v_mfma_f32_16x16x32_bf16 v[64:67], a[144:147], a[112:115], v[64:67]\n"
  "v_add_f32 v253, v253, v20\n"
  "v_add_f32 v253, v253, v21\n"
  "v_mfma_f32_16x16x32_bf16 v[64:67], a[148:151], a[116:119], v[64:67]\n"
  "buffer_load_dwordx4 v221, s[40:43], 0 offen offset:256 lds\n"
  "v_add_f32 v253, v253, v22\n"
  "v_mfma_f32_16x16x32_bf16 v[0:3], a[148:151], a[20:23], v[248:251]\n"
  "s_add_u32 s40, s86, s96\n"
  "v_add_f32 v253, v253, v23\n"
  "v_add_f32 v254, v254, v36\n"
  "v_mfma_f32_16x16x32_bf16 v[0:3], a[144:147], a[16:19], v[0:3]\n"
  "s_addc_u32 s41, s87, 0\n"
  "v_add_f32 v254, v254, v37\n"
  "v_add_f32 v254, v254, v38\n"
  "v_mfma_f32_16x16x32_bf16 v[0:3], a[120:123], a[12:15], v[0:3]\n"
  "s_mov_b32 s42, s30\n"
  "v_add_f32 v254, v254, v39\n"
  "v_add_f32 v255, v255, v52\n"
  "v_mfma_f32_16x16x32_bf16 v[0:3], a[124:127], a[8:11], v[0:3]\n"
  "s_add_u32 m0, s59, -128\n"
  "v_add_f32 v255, v255, v53\n"
  "v_add_f32 v255, v255, v54\n"
  "v_mfma_f32_16x16x32_bf16 v[0:3], a[128:131], a[4:7], v[0:3]\n"
  "v_add_f32 v255, v255, v55\n"
  "v_fma_f32 v64, v64, s73, v227\n"
  "v_mfma_f32_16x16x32_bf16 v[0:3], a[132:135], a[0:3], v[0:3]\n"
  "v_exp_f32 v64, v64\n"
  "v_mfma_f32_16x16x32_bf16 v[16:19], a[132:135], a[24:27], v[248:251]\n"
  "v_fma_f32 v65, v65, s73, v227\n"
  "v_fma_f32 v66, v66, s73, v227\n"
  "v_mfma_f32_16x16x32_bf16 v[16:19], a[128:131], a[28:31], v[16:19]\n"
  "buffer_load_dwordx4 v221, s[32:35], 0 offen offset:128 lds\n"
  "v_fma_f32 v67, v67, s73, v227\n"
  "v_mfma_f32_16x16x32_bf16 v[16:19], a[124:127], a[32:35], v[16:19]\n"
  "s_addk_i32 m0, 1024\n"
  "ds_read_b64_tr_b16 a[136:137], v217 offset:24704\n"
  "v_fma_f32 v0, v0, s73, v223\n"
  "v_mfma_f32_16x16x32_bf16 v[16:19], a[120:123], a[36:39], v[16:19]\n"
  "s_mov_b64 s[32:33], s[44:45]\n"
  "ds_read_b64_tr_b16 a[138:139], v218 offset:24704\n"
  "v_fma_f32 v1, v1, s73, v223\n"
  "v_mfma_f32_16x16x32_bf16 v[16:19], a[144:147], a[40:43], v[16:19]\n"
  "ds_read_b64_tr_b16 a[140:141], v217 offset:25728\n"
  "v_fma_f32 v2, v2, s73, v223\n"
  "v_mfma_f32_16x16x32_bf16 v[16:19], a[148:151], a[44:47], v[16:19]\n"
  "buffer_load_dwordx4 v221, s[44:47], 0 offen offset:128 lds\n"
  "v_fma_f32 v3, v3, s73, v223\n"
  "v_mfma_f32_16x16x32_bf16 v[32:35], a[148:151], a[68:71], v[248:251]\n"
  "s_addk_i32 m0, 896\n"
  "ds_read_b64_tr_b16 a[142:143], v218 offset:25728\n"
  "v_mfma_f32_16x16x32_bf16 v[32:35], a[144:147], a[64:67], v[32:35]\n"
  "v_exp_f32 v65, v65\n"
  "v_mfma_f32_16x16x32_bf16 v[32:35], a[120:123], a[60:63], v[32:35]\n"
  "v_exp_f32 v66, v66\n"
  "v_mfma_f32_16x16x32_bf16 v[32:35], a[124:127], a[56:59], v[32:35]\n"
  "buffer_load_dwordx4 v221, s[44:47], 0 offen offset:256 lds\n"
  "v_cvt_pk_bf16_f32 v64, v64, v65\n"
  "v_mfma_f32_16x16x32_bf16 v[32:35], a[128:131], a[52:55], v[32:35]\n"
  "s_add_u32 s44, s44, s31\n"
  "v_exp_f32 v67, v67\n"
  "v_mfma_f32_16x16x32_bf16 v[32:35], a[132:135], a[48:51], v[32:35]\n"
  "s_addc_u32 s45, s45, 0\n"
  "v_exp_f32 v0, v0\n"
  "v_mfma_f32_16x16x32_bf16 v[48:51], a[132:135], a[72:75], v[248:251]\n"
  "ds_read_b64_tr_b16 a[132:133], v217 offset:24720\n"
  "v_cvt_pk_bf16_f32 v65, v66, v67\n"
  "v_mfma_f32_16x16x32_bf16 v[48:51], a[128:131], a[76:79], v[48:51]\n"
  "ds_read_b64_tr_b16 a[134:135], v218 offset:24720\n"
  "v_add_f32 v252, v252, v0\n"
  "v_mfma_f32_16x16x32_bf16 v[48:51], a[124:127], a[80:83], v[48:51]\n"
  "ds_read_b64_tr_b16 a[128:129], v217 offset:25744\n"
  "v_fma_f32 v16, v16, s73, v224\n"
  "v_mfma_f32_16x16x32_bf16 v[48:51], a[120:123], a[84:87], v[48:51]\n"
  "ds_read_b64_tr_b16 a[130:131], v218 offset:25744\n"
  "v_fma_f32 v17, v17, s73, v224\n"
  "v_mfma_f32_16x16x32_bf16 v[48:51], a[144:147], a[88:91], v[48:51]\n"
  "v_exp_f32 v1, v1\n"
  "v_mfma_f32_16x16x32_bf16 v[48:51], a[148:151], a[92:95], v[48:51]\n"
  "s_waitcnt lgkmcnt(4)\n"
  "v_exp_f32 v2, v2\n"
  "v_mfma_f32_16x16x32_bf16 a[236:239], a[160:163], a[136:139], a[236:239]\n"
  "v_exp_f32 v3, v3\n"
  "v_mfma_f32_16x16x32_bf16 a[236:239], a[156:159], a[140:143], a[236:239]\n"
  "v_exp_f32 v16, v16\n"
  "v_mfma_f32_16x16x32_bf16 v[184:187], v[60:63], a[136:139], v[184:187]\n"
  "v_exp_f32 v17, v17\n"
  "v_mfma_f32_16x16x32_bf16 v[184:187], v[56:59], a[140:143], v[184:187]\n"
  "v_add_f32 v252, v252, v1\n"
  "v_add_f32 v252, v252, v2\n"
  "v_mfma_f32_16x16x32_bf16 v[152:155], v[44:47], a[136:139], v[152:155]\n"
  "ds_read_b64_tr_b16 a[124:125], v219 offset:24704\n"
  "v_add_f32 v252, v252, v3\n"
  "v_mfma_f32_16x16x32_bf16 v[152:155], v[40:43], a[140:143], v[152:155]\n"
  "ds_read_b64_tr_b16 a[126:127], v220 offset:24704\n"
  "v_fma_f32 v18, v18, s73, v224\n"
  "v_mfma_f32_16x16x32_bf16 v[120:123], v[28:31], a[136:139], v[120:123]\n"
  "ds_read_b64_tr_b16 a[120:121], v219 offset:25728\n"
  "v_fma_f32 v19, v19, s73, v224\n"
  "v_mfma_f32_16x16x32_bf16 v[120:123], v[24:27], a[140:143], v[120:123]\n"
  "ds_read_b64_tr_b16 a[122:123], v220 offset:25728\n"
  "v_fma_f32 v32, v32, s73, v225\n"
  "v_mfma_f32_16x16x32_bf16 v[88:91], v[12:15], a[136:139], v[88:91]\n"
  "v_exp_f32 v18, v18\n"
  "v_mfma_f32_16x16x32_bf16 v[88:91], v[8:11], a[140:143], v[88:91]\n"
  "s_waitcnt lgkmcnt(4)\n"
  "v_exp_f32 v19, v19\n"
  "v_mfma_f32_16x16x32_bf16 v[92:95], v[12:15], a[132:135], v[92:95]\n"
  "v_exp_f32 v32, v32\n"
  "v_mfma_f32_16x16x32_bf16 v[92:95], v[8:11], a[128:131], v[92:95]\n"
  "v_fma_f32 v33, v33, s73, v225\n"
  "v_fma_f32 v34, v34, s73, v225\n"
  "v_mfma_f32_16x16x32_bf16 v[124:127], v[28:31], a[132:135], v[124:127]\n"
  "ds_read_b128 a[136:139], v235 offset:24704\n"
  "v_fma_f32 v35, v35, s73, v225\n"
  "v_mfma_f32_16x16x32_bf16 v[124:127], v[24:27], a[128:131], v[124:127]\n"
  "ds_read_b128 a[140:143], v235 offset:24768\n"
  "v_fma_f32 v48, v48, s73, v226\n"
  "v_mfma_f32_16x16x32_bf16 v[156:159], v[44:47], a[132:135], v[156:159]\n"
  "ds_read_b128 a[144:147], v235 offset:25728\n"
  "v_fma_f32 v49, v49, s73, v226\n"
  "v_mfma_f32_16x16x32_bf16 v[156:159], v[40:43], a[128:131], v[156:159]\n"
  "ds_read_b128 a[148:151], v235 offset:25792\n"
  "v_fma_f32 v50, v50, s73, v226\n"
  "v_mfma_f32_16x16x32_bf16 v[188:191], v[60:63], a[132:135], v[188:191]\n"
  "v_exp_f32 v33, v33\n"
  "v_mfma_f32_16x16x32_bf16 v[188:191], v[56:59], a[128:131], v[188:191]\n"
  "v_exp_f32 v34, v34\n"
  "v_mfma_f32_16x16x32_bf16 a[240:243], a[160:163], a[132:135], a[240:243]\n"
  "ds_read_b128 a[132:135], v235 offset:26752\n"
  "v_fma_f32 v51, v51, s73, v226\n"
  "v_mfma_f32_16x16x32_bf16 a[240:243], a[156:159], a[128:131], a[240:243]\n"
  "s_waitcnt lgkmcnt(5)\n"
  "ds_read_b128 a[128:131], v235 offset:26816\n"
  "v_add_f32 v253, v253, v16\n"
  "v_mfma_f32_16x16x32_bf16 v[192:195], v[60:63], a[124:127], v[192:195]\n"
  "v_exp_f32 v35, v35\n"
  "v_mfma_f32_16x16x32_bf16 v[192:195], v[56:59], a[120:123], v[192:195]\n"
  "ds_write_b64 v231, v[64:65] offset:2048\n"
  "v_mfma_f32_16x16x32_bf16 v[160:163], v[44:47], a[124:127], v[160:163]\n"
  "v_exp_f32 v48, v48\n"
  "v_mfma_f32_16x16x32_bf16 v[160:163], v[40:43], a[120:123], v[160:163]\n"
  "v_exp_f32 v49, v49\n"
  "v_mfma_f32_16x16x32_bf16 v[128:131], v[28:31], a[124:127], v[128:131]\n"
  "v_exp_f32 v50, v50\n"
  "v_mfma_f32_16x16x32_bf16 v[128:131], v[24:27], a[120:123], v[128:131]\n"
  "v_exp_f32 v51, v51\n"
  "v_mfma_f32_16x16x32_bf16 v[96:99], v[12:15], a[124:127], v[96:99]\n"
  "v_add_f32 v253, v253, v17\n"
  "v_add_f32 v253, v253, v18\n"
  "v_mfma_f32_16x16x32_bf16 v[96:99], v[8:11], a[120:123], v[96:99]\n"
  "s_waitcnt lgkmcnt(5)\n"
  "v_add_f32 v253, v253, v19\n"
  "v_add_f32 v254, v254, v32\n"
  "v_mfma_f32_16x16x32_bf16 v[4:7], a[136:139], a[0:3], v[248:251]\n"
  "v_add_f32 v254, v254, v33\n"
  "v_add_f32 v254, v254, v34\n"
  "v_mfma_f32_16x16x32_bf16 v[4:7], a[140:143], a[4:7], v[4:7]\n"
  "s_waitcnt lgkmcnt(3)\n"
  "v_add_f32 v254, v254, v35\n"
  "v_add_f32 v255, v255, v48\n"
  "v_mfma_f32_16x16x32_bf16 v[4:7], a[144:147], a[8:11], v[4:7]\n"
  "v_add_f32 v255, v255, v49\n"
  "v_add_f32 v255, v255, v50\n"
  "v_mfma_f32_16x16x32_bf16 v[4:7], a[148:151], a[12:15], v[4:7]\n"
  "s_waitcnt lgkmcnt(1)\n"
  "v_add_f32 v255, v255, v51\n"
  "v_mfma_f32_16x16x32_bf16 v[4:7], a[132:135], a[16:19], v[4:7]\n"
  "v_mfma_f32_16x16x32_bf16 v[4:7], a[128:131], a[20:23], v[4:7]\n"
  "v_mfma_f32_16x16x32_bf16 v[20:23], a[128:131], a[44:47], v[248:251]\n"
  "v_mfma_f32_16x16x32_bf16 v[20:23], a[132:135], a[40:43], v[20:23]\n"
  "v_mfma_f32_16x16x32_bf16 v[20:23], a[148:151], a[36:39], v[20:23]\n"
  "ds_read_b64_tr_b16 a[124:125], v219 offset:24720\n"
  "v_fma_f32 v4, v4, s73, v223\n"
  "v_mfma_f32_16x16x32_bf16 v[20:23], a[144:147], a[32:35], v[20:23]\n"
  "ds_read_b64_tr_b16 a[126:127], v220 offset:24720\n"
  "v_fma_f32 v5, v5, s73, v223\n"
  "v_mfma_f32_16x16x32_bf16 v[20:23], a[140:143], a[28:31], v[20:23]\n"
  "ds_read_b64_tr_b16 a[120:121], v219 offset:25744\n"
  "v_fma_f32 v6, v6, s73, v223\n"
  "v_mfma_f32_16x16x32_bf16 v[20:23], a[136:139], a[24:27], v[20:23]\n"
  "ds_read_b64_tr_b16 a[122:123], v220 offset:25744\n"
  "v_fma_f32 v7, v7, s73, v223\n"
  "v_mfma_f32_16x16x32_bf16 v[36:39], a[136:139], a[48:51], v[248:251]\n"
  "v_exp_f32 v4, v4\n"
  "v_mfma_f32_16x16x32_bf16 v[36:39], a[140:143], a[52:55], v[36:39]\n"
  "v_exp_f32 v5, v5\n"
  "v_mfma_f32_16x16x32_bf16 v[36:39], a[144:147], a[56:59], v[36:39]\n"
  "v_exp_f32 v6, v6\n"
  "v_mfma_f32_16x16x32_bf16 v[36:39], a[148:151], a[60:63], v[36:39]\n"
  "v_exp_f32 v7, v7\n"
  "v_mfma_f32_16x16x32_bf16 v[36:39], a[132:135], a[64:67], v[36:39]\n"
  "v_fma_f32 v20, v20, s73, v224\n"
  "v_fma_f32 v21, v21, s73, v224\n"
  "v_mfma_f32_16x16x32_bf16 v[36:39], a[128:131], a[68:71], v[36:39]\n"
  "v_exp_f32 v20, v20\n"
  "v_mfma_f32_16x16x32_bf16 v[52:55], a[128:131], a[92:95], v[248:251]\n"
  "ds_read_b64_tr_b16 a[128:129], v217 offset:26752\n"
  "v_fma_f32 v22, v22, s73, v224\n"
  "v_mfma_f32_16x16x32_bf16 v[52:55], a[132:135], a[88:91], v[52:55]\n"
  "ds_read_b64_tr_b16 a[130:131], v218 offset:26752\n"
  "v_fma_f32 v23, v23, s73, v224\n"
  "v_mfma_f32_16x16x32_bf16 v[52:55], a[148:151], a[84:87], v[52:55]\n"
  "ds_read_b64_tr_b16 a[132:133], v217 offset:26768\n"
  "v_fma_f32 v36, v36, s73, v225\n"
  "v_mfma_f32_16x16x32_bf16 v[52:55], a[144:147], a[80:83], v[52:55]\n"
  "ds_read_b64_tr_b16 a[134:135], v218 offset:26768\n"
  "v_fma_f32 v37, v37, s73, v225\n"
  "v_mfma_f32_16x16x32_bf16 v[52:55], a[140:143], a[76:79], v[52:55]\n"
  "v_exp_f32 v21, v21\n"
  "v_mfma_f32_16x16x32_bf16 v[52:55], a[136:139], a[72:75], v[52:55]\n"
  "s_waitcnt lgkmcnt(4)\n"
  "v_exp_f32 v22, v22\n"
  "v_mfma_f32_16x16x32_bf16 v[100:103], v[12:15], a[124:127], v[100:103]\n"
  "v_exp_f32 v23, v23\n"
  "v_mfma_f32_16x16x32_bf16 v[100:103], v[8:11], a[120:123], v[100:103]\n"
  "v_exp_f32 v36, v36\n"
  "v_mfma_f32_16x16x32_bf16 v[132:135], v[28:31], a[124:127], v[132:135]\n"
  "ds_read_b64_tr_b16 a[148:149], v219 offset:26752\n"
  "v_cvt_pk_bf16_f32 v12, v0, v1\n"
  "v_mfma_f32_16x16x32_bf16 v[132:135], v[24:27], a[120:123], v[132:135]\n"
  "ds_read_b64_tr_b16 a[150:151], v220 offset:26752\n"
  "v_cvt_pk_bf16_f32 v13, v2, v3\n"
  "v_mfma_f32_16x16x32_bf16 v[164:167], v[44:47], a[124:127], v[164:167]\n"
  "ds_read_b64_tr_b16 a[144:145], v219 offset:26768\n"
  "v_fma_f32 v38, v38, s73, v225\n"
  "v_mfma_f32_16x16x32_bf16 v[164:167], v[40:43], a[120:123], v[164:167]\n"
  "ds_read_b64_tr_b16 a[146:147], v220 offset:26768\n"
  "v_fma_f32 v39, v39, s73, v225\n"
  "v_mfma_f32_16x16x32_bf16 v[196:199], v[60:63], a[124:127], v[196:199]\n"
  "v_exp_f32 v37, v37\n"
  "v_mfma_f32_16x16x32_bf16 v[196:199], v[56:59], a[120:123], v[196:199]\n"
  "s_waitcnt lgkmcnt(4)\n"
  "v_exp_f32 v38, v38\n"
  "v_mfma_f32_16x16x32_bf16 a[244:247], a[156:159], a[128:131], a[244:247]\n"
  "v_exp_f32 v39, v39\n"
  "v_mfma_f32_16x16x32_bf16 a[248:251], a[156:159], a[132:135], a[248:251]\n"
  "v_fma_f32 v52, v52, s73, v226\n"
  "v_fma_f32 v53, v53, s73, v226\n"
  "v_mfma_f32_16x16x32_bf16 v[200:203], v[56:59], a[128:131], v[200:203]\n"
  "v_exp_f32 v52, v52\n"
  "v_mfma_f32_16x16x32_bf16 v[204:207], v[56:59], a[132:135], v[204:207]\n"
  "v_exp_f32 v53, v53\n"
  "v_mfma_f32_16x16x32_bf16 v[168:171], v[40:43], a[128:131], v[168:171]\n"
  "v_fma_f32 v54, v54, s73, v226\n"
  "v_fma_f32 v55, v55, s73, v226\n"
  "v_mfma_f32_16x16x32_bf16 v[172:175], v[40:43], a[132:135], v[172:175]\n"
  "s_waitcnt vmcnt(11) lgkmcnt(0)\n"
  "s_barrier\n"
  "ds_read_b128 a[120:123], v216 offset:0\n"
  "v_mfma_f32_16x16x32_bf16 v[136:139], v[24:27], a[128:131], v[136:139]\n"
  "ds_read_b64 a[160:161], v231 offset:2048\n"
  "v_add_f32 v252, v252, v4\n"
  "v_mfma_f32_16x16x32_bf16 v[140:143], v[24:27], a[132:135], v[140:143]\n"
  "ds_read_b64 a[162:163], v232 offset:2048\n"
  "v_add_f32 v252, v252, v5\n"
  "v_mfma_f32_16x16x32_bf16 v[104:107], v[8:11], a[128:131], v[104:107]\n"
  "ds_read_b128 a[124:127], v216 offset:64\n"
  "v_add_f32 v252, v252, v6\n"
  "v_mfma_f32_16x16x32_bf16 v[108:111], v[8:11], a[132:135], v[108:111]\n"
  "ds_read_b128 a[128:131], v216 offset:1024\n"
  "v_add_f32 v252, v252, v7\n"
  "v_mfma_f32_16x16x32_bf16 v[112:115], v[8:11], a[148:151], v[112:115]\n"
  "ds_read_b128 a[132:135], v216 offset:1088\n"
  "v_add_f32 v253, v253, v20\n"
  "v_mfma_f32_16x16x32_bf16 v[116:119], v[8:11], a[144:147], v[116:119]\n"
  "s_mov_b32 m0, s68\n"
  "ds_read_b128 a[136:139], v216 offset:2048\n"
  "v_add_f32 v253, v253, v21\n"
  "v_mfma_f32_16x16x32_bf16 v[144:147], v[24:27], a[148:151], v[144:147]\n"
  "ds_read_b128 a[140:143], v216 offset:2112\n"
  "v_add_f32 v253, v253, v22\n"
  "v_mfma_f32_16x16x32_bf16 v[148:151], v[24:27], a[144:147], v[148:151]\n"
  "v_exp_f32 v54, v54\n"
  "v_mfma_f32_16x16x32_bf16 v[176:179], v[40:43], a[148:151], v[176:179]\n"
  "v_exp_f32 v55, v55\n"
  "v_mfma_f32_16x16x32_bf16 v[180:183], v[40:43], a[144:147], v[180:183]\n"
  "buffer_load_dwordx4 v221, s[40:43], 0 offen offset:0 lds\n"
  "v_mfma_f32_16x16x32_bf16 v[208:211], v[56:59], a[148:151], v[208:211]\n"
  "s_addk_i32 m0, 896\n"
  "v_add_f32 v253, v253, v23\n"
  "v_add_f32 v254, v254, v36\n"
  "v_mfma_f32_16x16x32_bf16 v[212:215], v[56:59], a[144:147], v[212:215]\n"
  "s_waitcnt lgkmcnt(4)\n"
  "v_add_f32 v254, v254, v37\n"
  "v_add_f32 v254, v254, v38\n"
  "v_mfma_f32_16x16x32_bf16 v[64:67], a[120:123], a[96:99], v[248:251]\n"
  "v_add_f32 v254, v254, v39\n"
  "v_add_f32 v255, v255, v52\n"
  "v_mfma_f32_16x16x32_bf16 v[64:67], a[124:127], a[100:103], v[64:67]\n"
  "s_waitcnt lgkmcnt(2)\n"
  "buffer_load_dwordx4 v221, s[40:43], 0 offen offset:128 lds\n"
  "v_add_f32 v255, v255, v53\n"
  "v_mfma_f32_16x16x32_bf16 v[64:67], a[128:131], a[104:107], v[64:67]\n"
  "s_addk_i32 m0, 896\n"
  "v_add_f32 v255, v255, v54\n"
  "v_add_f32 v255, v255, v55\n"
  "v_mfma_f32_16x16x32_bf16 v[64:67], a[132:135], a[108:111], v[64:67]\n"
  "s_waitcnt lgkmcnt(0)\n"
  "v_cvt_pk_bf16_f32 v14, v4, v5\n"
  "v_cvt_pk_bf16_f32 v15, v6, v7\n"
  "v_mfma_f32_16x16x32_bf16 v[64:67], a[136:139], a[112:115], v[64:67]\n"
  "v_cvt_pk_bf16_f32 v28, v16, v17\n"
  "v_cvt_pk_bf16_f32 v29, v18, v19\n"
  "v_mfma_f32_16x16x32_bf16 v[64:67], a[140:143], a[116:119], v[64:67]\n"
  "buffer_load_dwordx4 v221, s[40:43], 0 offen offset:256 lds\n"
  "v_cvt_pk_bf16_f32 v30, v20, v21\n"
  "v_mfma_f32_16x16x32_bf16 v[0:3], a[140:143], a[20:23], v[248:251]\n"
  "v_cvt_pk_bf16_f32 v31, v22, v23\n"
  "v_cvt_pk_bf16_f32 v44, v32, v33\n"
  "v_mfma_f32_16x16x32_bf16 v[0:3], a[136:139], a[16:19], v[0:3]\n"
  "v_cvt_pk_bf16_f32 v45, v34, v35\n"
  "v_cvt_pk_bf16_f32 v46, v36, v37\n"
  "v_mfma_f32_16x16x32_bf16 v[0:3], a[132:135], a[12:15], v[0:3]\n"
  "v_cvt_pk_bf16_f32 v47, v38, v39\n"
  "v_cvt_pk_bf16_f32 v60, v48, v49\n"
  "v_mfma_f32_16x16x32_bf16 v[0:3], a[128:131], a[8:11], v[0:3]\n"
  "v_cvt_pk_bf16_f32 v61, v50, v51\n"
  "v_cvt_pk_bf16_f32 v62, v52, v53\n"
  "v_mfma_f32_16x16x32_bf16 v[0:3], a[124:127], a[4:7], v[0:3]\n"
  "v_cvt_pk_bf16_f32 v63, v54, v55\n"
  "v_mfma_f32_16x16x32_bf16 v[0:3], a[120:123], a[0:3], v[0:3]\n"
  "s_add_u32 s40, s40, 24576\n"
  "s_addc_u32 s41, s41, 0\n"
  "s_branch L_BIG_%=\n"
  ".p2align 5\n"
  "L_TAIL_%=:\n"
  "ds_read_b64_tr_b16 a[120:121], v217 offset:26752\n"
  "ds_read_b64_tr_b16 a[122:123], v218 offset:26752\n"
  "v_exp_f32_e64 v55, v55\n"
  "v_cvt_pk_bf16_f32 v45, v34, v35\n"
  "ds_read_b64_tr_b16 a[124:125], v217 offset:26768\n"
  "ds_read_b64_tr_b16 a[126:127], v218 offset:26768\n"
  "v_cvt_pk_bf16_f32 v46, v36, v37\n"
  "v_cvt_pk_bf16_f32 v47, v38, v39\n"
  "v_cvt_pk_bf16_f32 v60, v48, v49\n"
  "s_waitcnt lgkmcnt(8)\n"
  "ds_read_b64_tr_b16 a[128:129], v219 offset:26752\n"
  "ds_read_b64_tr_b16 a[130:131], v220 offset:26752\n"
  "v_cvt_pk_bf16_f32 v61, v50, v51\n"
  "v_cvt_pk_bf16_f32 v62, v52, v53\n"
  "v_cvt_pk_bf16_f32 v63, v54, v55\n"
  "v_mfma_f32_16x16x32_bf16 v[84:87], a[156:159], a[152:155], v[84:87]\n"
  "s_nop 0\n"
  "ds_read_b64_tr_b16 a[132:133], v219 offset:26768\n"
  "ds_read_b64_tr_b16 a[134:135], v220 offset:26768\n"
  "v_add_u32 v233, s61, v229\n"
  "s_mov_b32 exec_lo, 0\n"
  "v_add_u32 v233, s69, v233\n"
  "s_mov_b32 exec_lo, -1\n"
  "v_mfma_f32_16x16x32_bf16 v[84:87], a[160:163], a[152:155], v[84:87]\n"
  "ds_read_b64_tr_b16 a[136:137], v217 offset:24704\n"
  "ds_read_b64_tr_b16 a[138:139], v218 offset:24704\n"
  "v_mfma_f32_16x16x32_bf16 v[68:71], v[12:15], a[152:155], v[68:71]\n"
  "ds_read_b64_tr_b16 a[140:141], v217 offset:25728\n"
  "ds_read_b64_tr_b16 a[142:143], v218 offset:25728\n"
  "v_mfma_f32_16x16x32_bf16 v[72:75], v[28:31], a[152:155], v[72:75]\n"
  "ds_read_b64_tr_b16 a[144:145], v217 offset:24720\n"
  "ds_read_b64_tr_b16 a[146:147], v218 offset:24720\n"
  "v_mfma_f32_16x16x32_bf16 v[76:79], v[44:47], a[152:155], v[76:79]\n"
  "ds_read_b64_tr_b16 a[148:149], v217 offset:25744\n"
  "ds_read_b64_tr_b16 a[150:151], v218 offset:25744\n"
  "v_mfma_f32_16x16x32_bf16 v[80:83], v[60:63], a[152:155], v[80:83]\n"
  "s_waitcnt lgkmcnt(12)\n"
  "v_maximum3_f32 v236, v68, v69, v70\n"
  "v_rcp_f32 v68, v68\n"
  "v_rcp_f32_e64 v69, v69\n"
  "v_mfma_f32_16x16x32_bf16 a[244:247], a[160:163], a[120:123], a[244:247]\n"
  "v_maximum3_f32 v236, v236, v71, v72\n"
  "v_rcp_f32 v70, v70\n"
  "v_rcp_f32 v71, v71\n"
  "v_mfma_f32_16x16x32_bf16 a[248:251], a[160:163], a[124:127], a[248:251]\n"
  "v_maximum3_f32 v236, v236, v73, v74\n"
  "v_rcp_f32 v72, v72\n"
  "v_rcp_f32 v73, v73\n"
  "v_mfma_f32_16x16x32_bf16 v[104:107], v[12:15], a[120:123], v[104:107]\n"
  "v_maximum3_f32 v236, v236, v75, v76\n"
  "v_rcp_f32 v74, v74\n"
  "v_rcp_f32 v75, v75\n"
  "v_mfma_f32_16x16x32_bf16 v[108:111], v[12:15], a[124:127], v[108:111]\n"
  "s_waitcnt lgkmcnt(8)\n"
  "v_maximum3_f32 v236, v236, v77, v78\n"
  "v_rcp_f32 v76, v76\n"
  "v_rcp_f32 v77, v77\n"
  "v_mfma_f32_16x16x32_bf16 v[112:115], v[12:15], a[128:131], v[112:115]\n"
  "v_maximum3_f32 v236, v236, v79, v80\n"
  "v_rcp_f32 v78, v78\n"
  "v_rcp_f32_e64 v79, v79\n"
  "v_mfma_f32_16x16x32_bf16 v[116:119], v[12:15], a[132:135], v[116:119]\n"
  "v_maximum3_f32 v236, v236, v81, v82\n"
  "v_rcp_f32 v80, v80\n"
  "v_rcp_f32 v81, v81\n"
  "v_pk_fma_f32 v[104:105], v[104:105], v[68:69], v[68:69] neg_lo:[0,0,1] neg_hi:[0,0,1]\n"
  "v_pk_fma_f32 v[106:107], v[106:107], v[70:71], v[70:71] neg_lo:[0,0,1] neg_hi:[0,0,1]\n"
  "v_mfma_f32_16x16x32_bf16 v[136:139], v[28:31], a[120:123], v[136:139]\n"
  "v_maximum3_f32 v236, v236, v83, v84\n"
  "v_rcp_f32 v82, v82\n"
  "v_rcp_f32 v83, v83\n"
  "v_pk_fma_f32 v[108:109], v[108:109], v[68:69], v[68:69] neg_lo:[0,0,1] neg_hi:[0,0,1]\n"
  "v_pk_fma_f32 v[110:111], v[110:111], v[70:71], v[70:71] neg_lo:[0,0,1] neg_hi:[0,0,1]\n"
  "v_mfma_f32_16x16x32_bf16 v[140:143], v[28:31], a[124:127], v[140:143]\n"
  "v_cvt_pk_bf16_f32 v104, v104, v108\n"
  "v_cvt_pk_bf16_f32 v108, v105, v109\n"
  "v_cvt_pk_bf16_f32 v106, v106, v110\n"
  "v_cvt_pk_bf16_f32 v110, v107, v111\n"
  "v_maximum3_f32 v236, v236, v85, v86\n"
  "v_pk_fma_f32 v[112:113], v[112:113], v[68:69], v[68:69] neg_lo:[0,0,1] neg_hi:[0,0,1]\n"
  "v_pk_fma_f32 v[114:115], v[114:115], v[70:71], v[70:71] neg_lo:[0,0,1] neg_hi:[0,0,1]\n"
  "v_mfma_f32_16x16x32_bf16 v[144:147], v[28:31], a[128:131], v[144:147]\n"
  "v_maximum3_f32 v236, v236, v87, v87\n"
  "v_rcp_f32_e64 v84, v84\n"
  "v_rcp_f32_e64 v85, v85\n"
  "v_pk_fma_f32 v[116:117], v[116:117], v[68:69], v[68:69] neg_lo:[0,0,1] neg_hi:[0,0,1]\n"
  "v_pk_fma_f32 v[118:119], v[118:119], v[70:71], v[70:71] neg_lo:[0,0,1] neg_hi:[0,0,1]\n"
  "v_mfma_f32_16x16x32_bf16 v[148:151], v[28:31], a[132:135], v[148:151]\n"
  "v_cvt_pk_bf16_f32 v105, v112, v116\n"
  "v_cvt_pk_bf16_f32 v109, v113, v117\n"
  "v_cvt_pk_bf16_f32 v107, v114, v118\n"
  "v_cvt_pk_bf16_f32 v111, v115, v119\n"
  "v_cmp_ngt_f32_e64 vcc, s75, v236\n"
  "v_pk_fma_f32 v[136:137], v[136:137], v[72:73], v[72:73] neg_lo:[0,0,1] neg_hi:[0,0,1]\n"
  "v_pk_fma_f32 v[138:139], v[138:139], v[74:75], v[74:75] neg_lo:[0,0,1] neg_hi:[0,0,1]\n"
  "v_mfma_f32_16x16x32_bf16 v[168:171], v[44:47], a[120:123], v[168:171]\n"
  "ds_write_b64 v229, v[104:105] offset:8960\n"
  "v_rcp_f32_e64 v86, v86\n"
  "v_pk_fma_f32 v[140:141], v[140:141], v[72:73], v[72:73] neg_lo:[0,0,1] neg_hi:[0,0,1]\n"
  "v_pk_fma_f32 v[142:143], v[142:143], v[74:75], v[74:75] neg_lo:[0,0,1] neg_hi:[0,0,1]\n"
  "v_mfma_f32_16x16x32_bf16 v[172:175], v[44:47], a[124:127], v[172:175]\n"
  "s_cmp_lg_u64 vcc, 0\n"
  "s_cselect_b32 s79, s77, 0\n"
  "s_or_b32 s78, s78, s79\n"
  "s_lshl_b32 s77, s77, 1\n"
  "ds_write_b64 v229, v[108:109] offset:9248\n"
  "v_cvt_pk_bf16_f32 v136, v136, v140\n"
  "v_cvt_pk_bf16_f32 v140, v137, v141\n"
  "v_pk_fma_f32 v[144:145], v[144:145], v[72:73], v[72:73] neg_lo:[0,0,1] neg_hi:[0,0,1]\n"
  "v_pk_fma_f32 v[146:147], v[146:147], v[74:75], v[74:75] neg_lo:[0,0,1] neg_hi:[0,0,1]\n"
  "v_mfma_f32_16x16x32_bf16 v[176:179], v[44:47], a[128:131], v[176:179]\n"
  "ds_write_b64 v229, v[106:107] offset:9536\n"
  "v_cvt_pk_bf16_f32 v138, v138, v142\n"
  "v_cvt_pk_bf16_f32 v142, v139, v143\n"
  "v_pk_fma_f32 v[148:149], v[148:149], v[72:73], v[72:73] neg_lo:[0,0,1] neg_hi:[0,0,1]\n"
  "v_pk_fma_f32 v[150:151], v[150:151], v[74:75], v[74:75] neg_lo:[0,0,1] neg_hi:[0,0,1]\n"
  "v_mfma_f32_16x16x32_bf16 v[180:183], v[44:47], a[132:135], v[180:183]\n"
  "ds_write_b64 v229, v[110:111] offset:9824\n"
  "v_cvt_pk_bf16_f32 v137, v144, v148\n"
  "v_cvt_pk_bf16_f32 v141, v145, v149\n"
  "v_pk_fma_f32 v[168:169], v[168:169], v[76:77], v[76:77] neg_lo:[0,0,1] neg_hi:[0,0,1]\n"
  "v_pk_fma_f32 v[170:171], v[170:171], v[78:79], v[78:79] neg_lo:[0,0,1] neg_hi:[0,0,1]\n"
  "v_mfma_f32_16x16x32_bf16 v[200:203], v[60:63], a[120:123], v[200:203]\n"
  "ds_read_b64_tr_b16 a[120:121], v219 offset:24704\n"
  "ds_read_b64_tr_b16 a[122:123], v220 offset:24704\n"
  "ds_write_b64 v229, v[136:137] offset:11200\n"
  "v_pk_fma_f32 v[172:173], v[172:173], v[76:77], v[76:77] neg_lo:[0,0,1] neg_hi:[0,0,1]\n"
  "v_pk_fma_f32 v[174:175], v[174:175], v[78:79], v[78:79] neg_lo:[0,0,1] neg_hi:[0,0,1]\n"
  "v_mfma_f32_16x16x32_bf16 v[204:207], v[60:63], a[124:127], v[204:207]\n"
  "ds_read_b64_tr_b16 a[124:125], v219 offset:25728\n"
  "ds_read_b64_tr_b16 a[126:127], v220 offset:25728\n"
  "ds_write_b64 v229, v[140:141] offset:11488\n"
  "v_pk_fma_f32 v[176:177], v[176:177], v[76:77], v[76:77] neg_lo:[0,0,1] neg_hi:[0,0,1]\n"
  "v_pk_fma_f32 v[178:179], v[178:179], v[78:79], v[78:79] neg_lo:[0,0,1] neg_hi:[0,0,1]\n"
  "v_mfma_f32_16x16x32_bf16 v[208:211], v[60:63], a[128:131], v[208:211]\n"
  "ds_read_b64_tr_b16 a[128:129], v219 offset:24720\n"
  "ds_read_b64_tr_b16 a[130:131], v220 offset:24720\n"
  "ds_read_b64_tr_b16 v[244:245], v230 offset:8960\n"
  "ds_read_b64_tr_b16 v[246:247], v230 offset:8992\n"
  "v_cvt_pk_bf16_f32 v139, v146, v150\n"
  "v_pk_fma_f32 v[180:181], v[180:181], v[76:77], v[76:77] neg_lo:[0,0,1] neg_hi:[0,0,1]\n"
  "v_pk_fma_f32 v[182:183], v[182:183], v[78:79], v[78:79] neg_lo:[0,0,1] neg_hi:[0,0,1]\n"
  "v_mfma_f32_16x16x32_bf16 v[212:215], v[60:63], a[132:135], v[212:215]\n"
  "s_nop 0\n"
  "s_waitcnt lgkmcnt(15)\n"
  "ds_read_b64_tr_b16 a[132:133], v219 offset:25744\n"
  "ds_read_b64_tr_b16 a[134:135], v220 offset:25744\n"
  "ds_write_b64 v229, v[138:139] offset:11776\n"
  "v_pk_fma_f32 v[200:201], v[200:201], v[80:81], v[80:81] neg_lo:[0,0,1] neg_hi:[0,0,1]\n"
  "v_pk_fma_f32 v[202:203], v[202:203], v[82:83], v[82:83] neg_lo:[0,0,1] neg_hi:[0,0,1]\n"
  "v_mfma_f32_16x16x32_bf16 a[228:231], a[156:159], a[136:139], a[228:231]\n"
  "ds_read_b64_tr_b16 a[252:253], v230 offset:10080\n"
  "ds_read_b64_tr_b16 a[254:255], v230 offset:10112\n"
  "v_cvt_pk_bf16_f32 v143, v147, v151\n"
  "v_rcp_f32 v87, v87\n"
  "v_pk_fma_f32 v[204:205], v[204:205], v[80:81], v[80:81] neg_lo:[0,0,1] neg_hi:[0,0,1]\n"
  "v_pk_fma_f32 v[206:207], v[206:207], v[82:83], v[82:83] neg_lo:[0,0,1] neg_hi:[0,0,1]\n"
  "v_mfma_f32_16x16x32_bf16 a[228:231], a[160:163], a[140:143], a[228:231]\n"
  "s_waitcnt lgkmcnt(15)\n"
  "ds_write_b64 v229, v[142:143] offset:12064\n"
  "v_cvt_pk_bf16_f32 v168, v168, v172\n"
  "v_cvt_pk_bf16_f32 v172, v169, v173\n"
  "v_pk_fma_f32 v[208:209], v[208:209], v[80:81], v[80:81] neg_lo:[0,0,1] neg_hi:[0,0,1]\n"
  "v_pk_fma_f32 v[210:211], v[210:211], v[82:83], v[82:83] neg_lo:[0,0,1] neg_hi:[0,0,1]\n"
  "v_mfma_f32_16x16x32_bf16 a[232:235], a[156:159], a[144:147], a[232:235]\n"
  "v_cvt_pk_bf16_f32 v169, v176, v180\n"
  "v_cvt_pk_bf16_f32 v173, v177, v181\n"
  "v_cvt_pk_bf16_f32 v170, v170, v174\n"
  "v_cvt_pk_bf16_f32 v174, v171, v175\n"
  "v_cvt_pk_bf16_f32 v171, v178, v182\n"
  "v_pk_fma_f32 v[212:213], v[212:213], v[80:81], v[80:81] neg_lo:[0,0,1] neg_hi:[0,0,1]\n"
  "v_pk_fma_f32 v[214:215], v[214:215], v[82:83], v[82:83] neg_lo:[0,0,1] neg_hi:[0,0,1]\n"
  "v_mfma_f32_16x16x32_bf16 a[232:235], a[160:163], a[148:151], a[232:235]\n"
  "ds_read_b64_tr_b16 v[0:1], v230 offset:11200\n"
  "ds_read_b64_tr_b16 v[2:3], v230 offset:11232\n"
  "ds_write_b64 v229, v[168:169] offset:13440\n"
  "v_mfma_f32_16x16x32_bf16 a[164:167], v[8:11], a[136:139], a[164:167]\n"
  "s_waitcnt lgkmcnt(9)\n"
  "buffer_store_dwordx4 v[244:247], v228, s[52:55], s80 offen offset:256 nt\n"
  "ds_write_b64 v229, v[172:173] offset:13728\n"
  "v_cvt_pk_bf16_f32 v175, v179, v183\n"
  "v_mfma_f32_16x16x32_bf16 a[164:167], v[12:15], a[140:143], a[164:167]\n"
  "ds_read_b64_tr_b16 v[4:5], v230 offset:12320\n"
  "ds_read_b64_tr_b16 v[6:7], v230 offset:12352\n"
  "ds_write_b64 v229, v[170:171] offset:14016\n"
  "v_mfma_f32_16x16x32_bf16 a[168:171], v[8:11], a[144:147], a[168:171]\n"
  "s_waitcnt lgkmcnt(8)\n"
  "buffer_store_dwordx4 a[252:255], v228, s[52:55], s80 offen offset:3328 nt\n"
  "ds_write_b64 v229, v[174:175] offset:14304\n"
  "v_cvt_pk_bf16_f32 v200, v200, v204\n"
  "v_mfma_f32_16x16x32_bf16 a[168:171], v[12:15], a[148:151], a[168:171]\n"
  "v_cvt_pk_bf16_f32 v204, v201, v205\n"
  "v_cvt_pk_bf16_f32 v201, v208, v212\n"
  "v_cvt_pk_bf16_f32 v205, v209, v213\n"
  "v_cvt_pk_bf16_f32 v202, v202, v206\n"
  "v_cvt_pk_bf16_f32 v206, v203, v207\n"
  "v_mfma_f32_16x16x32_bf16 a[180:183], v[24:27], a[136:139], a[180:183]\n"
  "ds_read_b64_tr_b16 v[16:17], v230 offset:13440\n"
  "ds_read_b64_tr_b16 v[18:19], v230 offset:13472\n"
  "ds_write_b64 v229, v[200:201] offset:15680\n"
  "v_mfma_f32_16x16x32_bf16 a[180:183], v[28:31], a[140:143], a[180:183]\n"
  "s_waitcnt lgkmcnt(9)\n"
  "buffer_store_dwordx4 v[0:3], v228, s[52:55], s81 offen offset:256 nt\n"
  "ds_write_b64 v229, v[204:205] offset:15968\n"
  "v_cvt_pk_bf16_f32 v203, v210, v214\n"
  "v_mfma_f32_16x16x32_bf16 a[184:187], v[24:27], a[144:147], a[184:187]\n"
  "ds_read_b64_tr_b16 v[20:21], v230 offset:14560\n"
  "ds_write_b64 v229, v[202:203] offset:16256\n"
  "v_cvt_pk_bf16_f32 v207, v211, v215\n"
  "v_mfma_f32_16x16x32_bf16 a[184:187], v[28:31], a[148:151], a[184:187]\n"
  "s_waitcnt lgkmcnt(8)\n"
  "buffer_store_dwordx4 v[4:7], v228, s[52:55], s81 offen offset:3328 nt\n"
  "ds_read_b64_tr_b16 v[22:23], v230 offset:14592\n"
  "ds_write_b64 v229, v[206:207] offset:16544\n"
  "v_mfma_f32_16x16x32_bf16 a[196:199], v[40:43], a[136:139], a[196:199]\n"
  "v_accvgpr_read_b32 v104, a164\n"
  "v_accvgpr_read_b32 v105, a165\n"
  "v_accvgpr_read_b32 v108, a168\n"
  "v_accvgpr_read_b32 v109, a169\n"
  "v_accvgpr_read_b32 v106, a166\n"
  "v_pk_fma_f32 v[104:105], v[104:105], v[68:69], v[68:69] neg_lo:[0,0,1] neg_hi:[0,0,1]\n"
  "v_pk_fma_f32 v[108:109], v[108:109], v[68:69], v[68:69] neg_lo:[0,0,1] neg_hi:[0,0,1]\n"
  "v_mfma_f32_16x16x32_bf16 a[196:199], v[44:47], a[140:143], a[196:199]\n"
  "ds_read_b64_tr_b16 v[244:245], v230 offset:15680\n"
  "ds_read_b64_tr_b16 v[246:247], v230 offset:15712\n"
  "v_accvgpr_read_b32 v107, a167\n"
  "v_accvgpr_read_b32 v110, a170\n"
  "v_accvgpr_read_b32 v111, a171\n"
  "v_pk_fma_f32 v[106:107], v[106:107], v[70:71], v[70:71] neg_lo:[0,0,1] neg_hi:[0,0,1]\n"
  "v_pk_fma_f32 v[110:111], v[110:111], v[70:71], v[70:71] neg_lo:[0,0,1] neg_hi:[0,0,1]\n"
  "v_mfma_f32_16x16x32_bf16 a[200:203], v[40:43], a[144:147], a[200:203]\n"
  "s_nop 0\n"
  "s_waitcnt lgkmcnt(8)\n"
  "buffer_store_dwordx4 v[16:19], v228, s[52:55], s82 offen offset:256 nt\n"
  "v_cvt_pk_bf16_f32 v104, v104, v108\n"
  "v_cvt_pk_bf16_f32 v108, v105, v109\n"
  "v_cvt_pk_bf16_f32 v106, v106, v110\n"
  "v_cvt_pk_bf16_f32 v110, v107, v111\n"
  "v_mfma_f32_16x16x32_bf16 a[200:203], v[44:47], a[148:151], a[200:203]\n"
  "ds_read_b64_tr_b16 a[252:253], v230 offset:16800\n"
  "ds_read_b64_tr_b16 a[254:255], v230 offset:16832\n"
  "v_accvgpr_read_b32 v136, a180\n"
  "v_accvgpr_read_b32 v137, a181\n"
  "v_accvgpr_read_b32 v140, a184\n"
  "v_pk_fma_f32 v[136:137], v[136:137], v[72:73], v[72:73] neg_lo:[0,0,1] neg_hi:[0,0,1]\n"
  "v_mfma_f32_16x16x32_bf16 a[212:215], v[56:59], a[136:139], a[212:215]\n"
  "v_accvgpr_read_b32 v141, a185\n"
  "v_accvgpr_read_b32 v138, a182\n"
  "v_accvgpr_read_b32 v139, a183\n"
  "v_accvgpr_read_b32 v142, a186\n"
  "v_accvgpr_read_b32 v143, a187\n"
  "v_pk_fma_f32 v[140:141], v[140:141], v[72:73], v[72:73] neg_lo:[0,0,1] neg_hi:[0,0,1]\n"
  "v_pk_fma_f32 v[138:139], v[138:139], v[74:75], v[74:75] neg_lo:[0,0,1] neg_hi:[0,0,1]\n"
  "v_pk_fma_f32 v[142:143], v[142:143], v[74:75], v[74:75] neg_lo:[0,0,1] neg_hi:[0,0,1]\n"
  "v_mfma_f32_16x16x32_bf16 a[212:215], v[60:63], a[140:143], a[212:215]\n"
  "s_nop 0\n"
  "s_waitcnt lgkmcnt(5)\n"
  "buffer_store_dwordx4 v[20:23], v228, s[52:55], s82 offen offset:3328 nt\n"
  "v_cvt_pk_bf16_f32 v136, v136, v140\n"
  "v_cvt_pk_bf16_f32 v140, v137, v141\n"
  "v_cvt_pk_bf16_f32 v138, v138, v142\n"
  "v_cvt_pk_bf16_f32 v142, v139, v143\n"
  "v_mfma_f32_16x16x32_bf16 a[216:219], v[56:59], a[144:147], a[216:219]\n"
  "v_accvgpr_read_b32 v168, a196\n"
  "v_accvgpr_read_b32 v169, a197\n"
  "v_accvgpr_read_b32 v172, a200\n"
  "v_accvgpr_read_b32 v173, a201\n"
  "v_accvgpr_read_b32 v170, a198\n"
  "v_pk_fma_f32 v[168:169], v[168:169], v[76:77], v[76:77] neg_lo:[0,0,1] neg_hi:[0,0,1]\n"
  "v_pk_fma_f32 v[172:173], v[172:173], v[76:77], v[76:77] neg_lo:[0,0,1] neg_hi:[0,0,1]\n"
  "v_mfma_f32_16x16x32_bf16 a[216:219], v[60:63], a[148:151], a[216:219]\n"
  "s_nop 0\n"
  "s_waitcnt lgkmcnt(2)\n"
  "buffer_store_dwordx4 v[244:247], v228, s[52:55], s83 offen offset:256 nt\n"
  "v_accvgpr_read_b32 v171, a199\n"
  "v_accvgpr_read_b32 v174, a202\n"
  "v_accvgpr_read_b32 v175, a203\n"
  "v_cvt_pk_bf16_f32 v168, v168, v172\n"
  "v_pk_fma_f32 v[170:171], v[170:171], v[78:79], v[78:79] neg_lo:[0,0,1] neg_hi:[0,0,1]\n"
  "v_pk_fma_f32 v[174:175], v[174:175], v[78:79], v[78:79] neg_lo:[0,0,1] neg_hi:[0,0,1]\n"
  "v_mfma_f32_16x16x32_bf16 a[172:175], v[8:11], a[120:123], a[172:175]\n"
  "s_waitcnt vmcnt(10)\n"
  "s_barrier\n"
  "ds_read_b64_tr_b16 a[136:137], v217 offset:0\n"
  "ds_read_b64_tr_b16 a[138:139], v218 offset:0\n"
  "v_cvt_pk_bf16_f32 v172, v169, v173\n"
  "v_mfma_f32_16x16x32_bf16 a[172:175], v[12:15], a[124:127], a[172:175]\n"
  "s_nop 0\n"
  "s_waitcnt lgkmcnt(2)\n"
  "buffer_store_dwordx4 a[252:255], v228, s[52:55], s83 offen offset:3328 nt\n"
  "ds_read_b64_tr_b16 a[140:141], v217 offset:16\n"
  "ds_read_b64_tr_b16 a[142:143], v218 offset:16\n"
  "v_cvt_pk_bf16_f32 v170, v170, v174\n"
  "v_cvt_pk_bf16_f32 v174, v171, v175\n"
  "v_mfma_f32_16x16x32_bf16 a[176:179], v[8:11], a[128:131], a[176:179]\n"
  "ds_read_b64_tr_b16 a[144:145], v219 offset:0\n"
  "ds_read_b64_tr_b16 a[146:147], v220 offset:0\n"
  "v_accvgpr_read_b32 v240, a244\n"
  "v_accvgpr_read_b32 v241, a245\n"
  "v_accvgpr_read_b32 v242, a248\n"
  "v_pk_fma_f32 v[240:241], v[240:241], v[84:85], v[84:85] neg_lo:[0,0,1] neg_hi:[0,0,1]\n"
  "v_mfma_f32_16x16x32_bf16 a[176:179], v[12:15], a[132:135], a[176:179]\n"
  "ds_read_b64_tr_b16 a[148:149], v219 offset:16\n"
  "ds_read_b64_tr_b16 a[150:151], v220 offset:16\n"
  "v_accvgpr_read_b32 v243, a249\n"
  "v_accvgpr_read_b32 v236, a246\n"
  "v_accvgpr_read_b32 v237, a247\n"
  "v_pk_fma_f32 v[242:243], v[242:243], v[84:85], v[84:85] neg_lo:[0,0,1] neg_hi:[0,0,1]\n"
  "v_pk_fma_f32 v[236:237], v[236:237], v[86:87], v[86:87] neg_lo:[0,0,1] neg_hi:[0,0,1]\n"
  "v_mfma_f32_16x16x32_bf16 a[188:191], v[24:27], a[120:123], a[188:191]\n"
  "v_accvgpr_read_b32 v112, a172\n"
  "v_accvgpr_read_b32 v113, a173\n"
  "v_accvgpr_read_b32 v114, a174\n"
  "v_accvgpr_read_b32 v115, a175\n"
  "v_accvgpr_read_b32 v238, a250\n"
  "v_pk_fma_f32 v[112:113], v[112:113], v[68:69], v[68:69] neg_lo:[0,0,1] neg_hi:[0,0,1]\n"
  "v_pk_fma_f32 v[114:115], v[114:115], v[70:71], v[70:71] neg_lo:[0,0,1] neg_hi:[0,0,1]\n"
  "v_mfma_f32_16x16x32_bf16 a[188:191], v[28:31], a[124:127], a[188:191]\n"
  "v_accvgpr_read_b32 v239, a251\n"
  "v_accvgpr_read_b32 v200, a212\n"
  "v_accvgpr_read_b32 v201, a213\n"
  "v_accvgpr_read_b32 v204, a216\n"
  "v_accvgpr_read_b32 v205, a217\n"
  "v_pk_fma_f32 v[238:239], v[238:239], v[86:87], v[86:87] neg_lo:[0,0,1] neg_hi:[0,0,1]\n"
  "v_pk_fma_f32 v[200:201], v[200:201], v[80:81], v[80:81] neg_lo:[0,0,1] neg_hi:[0,0,1]\n"
  "v_pk_fma_f32 v[204:205], v[204:205], v[80:81], v[80:81] neg_lo:[0,0,1] neg_hi:[0,0,1]\n"
  "v_mfma_f32_16x16x32_bf16 a[192:195], v[24:27], a[128:131], a[192:195]\n"
  "v_accvgpr_read_b32 v116, a176\n"
  "v_accvgpr_read_b32 v117, a177\n"
  "v_accvgpr_read_b32 v118, a178\n"
  "v_accvgpr_read_b32 v119, a179\n"
  "v_accvgpr_read_b32 v202, a214\n"
  "v_pk_fma_f32 v[116:117], v[116:117], v[68:69], v[68:69] neg_lo:[0,0,1] neg_hi:[0,0,1]\n"
  "v_pk_fma_f32 v[118:119], v[118:119], v[70:71], v[70:71] neg_lo:[0,0,1] neg_hi:[0,0,1]\n"
  "v_mfma_f32_16x16x32_bf16 a[192:195], v[28:31], a[132:135], a[192:195]\n"
  "v_cvt_pk_bf16_f32 v105, v112, v116\n"
  "v_cvt_pk_bf16_f32 v109, v113, v117\n"
  "v_cvt_pk_bf16_f32 v107, v114, v118\n"
  "v_cvt_pk_bf16_f32 v111, v115, v119\n"
  "v_accvgpr_read_b32 v203, a215\n"
  "v_pk_fma_f32 v[202:203], v[202:203], v[82:83], v[82:83] neg_lo:[0,0,1] neg_hi:[0,0,1]\n"
  "v_mfma_f32_16x16x32_bf16 a[204:207], v[40:43], a[120:123], a[204:207]\n"
  "ds_write_b64 v229, v[104:105] offset:0\n"
  "v_accvgpr_read_b32 v144, a188\n"
  "v_accvgpr_read_b32 v145, a189\n"
  "v_pk_fma_f32 v[144:145], v[144:145], v[72:73], v[72:73] neg_lo:[0,0,1] neg_hi:[0,0,1]\n"
  "v_mfma_f32_16x16x32_bf16 a[204:207], v[44:47], a[124:127], a[204:207]\n"
  "ds_write_b64 v229, v[108:109] offset:288\n"
  "v_accvgpr_read_b32 v146, a190\n"
  "v_accvgpr_read_b32 v147, a191\n"
  "v_pk_fma_f32 v[146:147], v[146:147], v[74:75], v[74:75] neg_lo:[0,0,1] neg_hi:[0,0,1]\n"
  "v_mfma_f32_16x16x32_bf16 a[208:211], v[40:43], a[128:131], a[208:211]\n"
  "ds_write_b64 v229, v[106:107] offset:576\n"
  "ds_write_b64 v229, v[110:111] offset:864\n"
  "v_mfma_f32_16x16x32_bf16 a[208:211], v[44:47], a[132:135], a[208:211]\n"
  "v_accvgpr_read_b32 v148, a192\n"
  "v_accvgpr_read_b32 v149, a193\n"
  "v_accvgpr_read_b32 v150, a194\n"
  "v_accvgpr_read_b32 v151, a195\n"
  "v_accvgpr_read_b32 v206, a218\n"
  "v_pk_fma_f32 v[148:149], v[148:149], v[72:73], v[72:73] neg_lo:[0,0,1] neg_hi:[0,0,1]\n"
  "v_pk_fma_f32 v[150:151], v[150:151], v[74:75], v[74:75] neg_lo:[0,0,1] neg_hi:[0,0,1]\n"
  "v_mfma_f32_16x16x32_bf16 a[220:223], v[56:59], a[120:123], a[220:223]\n"
  "ds_read_b64_tr_b16 v[0:1], v230 offset:0\n"
  "ds_read_b64_tr_b16 v[2:3], v230 offset:32\n"
  "v_cvt_pk_bf16_f32 v137, v144, v148\n"
  "v_cvt_pk_bf16_f32 v141, v145, v149\n"
  "v_cvt_pk_bf16_f32 v139, v146, v150\n"
  "v_mfma_f32_16x16x32_bf16 a[220:223], v[60:63], a[124:127], a[220:223]\n"
  "ds_read_b64_tr_b16 v[4:5], v230 offset:1120\n"
  "ds_write_b64 v229, v[136:137] offset:2240\n"
  "v_cvt_pk_bf16_f32 v143, v147, v151\n"
  "v_mfma_f32_16x16x32_bf16 a[224:227], v[56:59], a[128:131], a[224:227]\n"
  "ds_read_b64_tr_b16 v[6:7], v230 offset:1152\n"
  "ds_write_b64 v229, v[140:141] offset:2528\n"
  "v_accvgpr_read_b32 v176, a204\n"
  "v_mfma_f32_16x16x32_bf16 a[224:227], v[60:63], a[132:135], a[224:227]\n"
  "s_nop 0\n"
  "s_waitcnt lgkmcnt(14)\n"
  "ds_write_b64 v229, v[138:139] offset:2816\n"
  "ds_write_b64 v229, v[142:143] offset:3104\n"
  "v_mfma_f32_16x16x32_bf16 a[236:239], a[160:163], a[136:139], a[236:239]\n"
  "v_accvgpr_read_b32 v177, a205\n"
  "v_accvgpr_read_b32 v180, a208\n"
  "v_accvgpr_read_b32 v181, a209\n"
  "v_accvgpr_read_b32 v178, a206\n"
  "v_accvgpr_read_b32 v179, a207\n"
  "v_pk_fma_f32 v[176:177], v[176:177], v[76:77], v[76:77] neg_lo:[0,0,1] neg_hi:[0,0,1]\n"
  "v_pk_fma_f32 v[180:181], v[180:181], v[76:77], v[76:77] neg_lo:[0,0,1] neg_hi:[0,0,1]\n"
  "v_pk_fma_f32 v[178:179], v[178:179], v[78:79], v[78:79] neg_lo:[0,0,1] neg_hi:[0,0,1]\n"
  "v_mfma_f32_16x16x32_bf16 a[240:243], a[160:163], a[140:143], a[240:243]\n"
  "ds_read_b64_tr_b16 v[16:17], v230 offset:2240\n"
  "ds_read_b64_tr_b16 v[18:19], v230 offset:2272\n"
  "v_accvgpr_read_b32 v182, a210\n"
  "v_accvgpr_read_b32 v183, a211\n"
  "v_cvt_pk_bf16_f32 v169, v176, v180\n"
  "v_pk_fma_f32 v[182:183], v[182:183], v[78:79], v[78:79] neg_lo:[0,0,1] neg_hi:[0,0,1]\n"
  "v_mfma_f32_16x16x32_bf16 v[88:91], v[12:15], a[136:139], v[88:91]\n"
  "s_waitcnt lgkmcnt(8)\n"
  "buffer_store_dwordx4 v[0:3], v228, s[52:55], s80 offen offset:0 nt\n"
  "ds_write_b64 v229, v[168:169] offset:4480\n"
  "v_cvt_pk_bf16_f32 v173, v177, v181\n"
  "v_mfma_f32_16x16x32_bf16 v[92:95], v[12:15], a[140:143], v[92:95]\n"
  "ds_write_b64 v229, v[172:173] offset:4768\n"
  "v_cvt_pk_bf16_f32 v171, v178, v182\n"
  "v_cvt_pk_bf16_f32 v175, v179, v183\n"
  "v_mfma_f32_16x16x32_bf16 v[96:99], v[12:15], a[144:147], v[96:99]\n"
  "s_waitcnt lgkmcnt(7)\n"
  "buffer_store_dwordx4 v[4:7], v228, s[52:55], s80 offen offset:3072 nt\n"
  "ds_read_b64_tr_b16 v[20:21], v230 offset:3360\n"
  "ds_read_b64_tr_b16 v[22:23], v230 offset:3392\n"
  "ds_write_b64 v229, v[170:171] offset:5056\n"
  "ds_write_b64 v229, v[174:175] offset:5344\n"
  "v_mfma_f32_16x16x32_bf16 v[100:103], v[12:15], a[148:151], v[100:103]\n"
  "v_accvgpr_read_b32 v208, a220\n"
  "v_accvgpr_read_b32 v209, a221\n"
  "v_accvgpr_read_b32 v212, a224\n"
  "v_accvgpr_read_b32 v213, a225\n"
  "v_accvgpr_read_b32 v207, a219\n"
  "v_pk_fma_f32 v[208:209], v[208:209], v[80:81], v[80:81] neg_lo:[0,0,1] neg_hi:[0,0,1]\n"
  "v_pk_fma_f32 v[212:213], v[212:213], v[80:81], v[80:81] neg_lo:[0,0,1] neg_hi:[0,0,1]\n"
  "v_pk_fma_f32 v[206:207], v[206:207], v[82:83], v[82:83] neg_lo:[0,0,1] neg_hi:[0,0,1]\n"
  "v_pk_fma_f32 v[88:89], v[88:89], v[68:69], v[68:69] neg_lo:[0,0,1] neg_hi:[0,0,1]\n"
  "v_mfma_f32_16x16x32_bf16 v[120:123], v[28:31], a[136:139], v[120:123]\n"
  "ds_read_b64_tr_b16 v[244:245], v230 offset:4480\n"
  "ds_read_b64_tr_b16 v[246:247], v230 offset:4512\n"
  "v_accvgpr_read_b32 v210, a222\n"
  "v_accvgpr_read_b32 v211, a223\n"
  "v_accvgpr_read_b32 v214, a226\n"
  "v_pk_fma_f32 v[210:211], v[210:211], v[82:83], v[82:83] neg_lo:[0,0,1] neg_hi:[0,0,1]\n"
  "v_pk_fma_f32 v[92:93], v[92:93], v[68:69], v[68:69] neg_lo:[0,0,1] neg_hi:[0,0,1]\n"
  "v_pk_fma_f32 v[90:91], v[90:91], v[70:71], v[70:71] neg_lo:[0,0,1] neg_hi:[0,0,1]\n"
  "v_pk_fma_f32 v[94:95], v[94:95], v[70:71], v[70:71] neg_lo:[0,0,1] neg_hi:[0,0,1]\n"
  "v_mfma_f32_16x16x32_bf16 v[124:127], v[28:31], a[140:143], v[124:127]\n"
  "s_nop 0\n"
  "s_waitcnt lgkmcnt(8)\n"
  "buffer_store_dwordx4 v[16:19], v228, s[52:55], s81 offen offset:0 nt\n"
  "v_accvgpr_read_b32 v215, a227\n"
  "v_cvt_pk_bf16_f32 v240, v240, v242\n"
  "v_cvt_pk_bf16_f32 v241, v241, v243\n"
  "v_pk_fma_f32 v[214:215], v[214:215], v[82:83], v[82:83] neg_lo:[0,0,1] neg_hi:[0,0,1]\n"
  "v_pk_fma_f32 v[96:97], v[96:97], v[68:69], v[68:69] neg_lo:[0,0,1] neg_hi:[0,0,1]\n"
  "v_pk_fma_f32 v[98:99], v[98:99], v[70:71], v[70:71] neg_lo:[0,0,1] neg_hi:[0,0,1]\n"
  "v_mfma_f32_16x16x32_bf16 v[128:131], v[28:31], a[144:147], v[128:131]\n"
  "v_cvt_pk_bf16_f32 v236, v236, v238\n"
  "v_cvt_pk_bf16_f32 v237, v237, v239\n"
  "v_cvt_pk_bf16_f32 v200, v200, v204\n"
  "v_cvt_pk_bf16_f32 v204, v201, v205\n"
  "v_cvt_pk_bf16_f32 v201, v208, v212\n"
  "v_cvt_pk_bf16_f32 v205, v209, v213\n"
  "v_cvt_pk_bf16_f32 v202, v202, v206\n"
  "v_cvt_pk_bf16_f32 v206, v203, v207\n"
  "v_cvt_pk_bf16_f32 v203, v210, v214\n"
  "v_cvt_pk_bf16_f32 v207, v211, v215\n"
  "v_pk_fma_f32 v[100:101], v[100:101], v[68:69], v[68:69] neg_lo:[0,0,1] neg_hi:[0,0,1]\n"
  "v_pk_fma_f32 v[102:103], v[102:103], v[70:71], v[70:71] neg_lo:[0,0,1] neg_hi:[0,0,1]\n"
  "v_mfma_f32_16x16x32_bf16 v[132:135], v[28:31], a[148:151], v[132:135]\n"
  "ds_read_b64_tr_b16 a[252:253], v230 offset:5600\n"
  "ds_read_b64_tr_b16 a[254:255], v230 offset:5632\n"
  "ds_write_b32 v233, v240 offset:20160\n"
  "ds_write_b32 v233, v241 offset:20448\n"
  "ds_write_b32 v233, v236 offset:20736\n"
  "ds_write_b32 v233, v237 offset:21024\n"
  "ds_write_b64 v229, v[200:201] offset:6720\n"
  "ds_write_b64 v229, v[204:205] offset:7008\n"
  "ds_write_b64 v229, v[202:203] offset:7296\n"
  "ds_write_b64 v229, v[206:207] offset:7584\n"
  "v_pk_fma_f32 v[120:121], v[120:121], v[72:73], v[72:73] neg_lo:[0,0,1] neg_hi:[0,0,1]\n"
  "v_pk_fma_f32 v[122:123], v[122:123], v[74:75], v[74:75] neg_lo:[0,0,1] neg_hi:[0,0,1]\n"
  "v_mfma_f32_16x16x32_bf16 v[152:155], v[44:47], a[136:139], v[152:155]\n"
  "s_nop 0\n"
  "s_waitcnt lgkmcnt(14)\n"
  "buffer_store_dwordx4 v[20:23], v228, s[52:55], s81 offen offset:3072 nt\n"
  "v_accvgpr_read_b32 v240, a228\n"
  "v_accvgpr_read_b32 v241, a229\n"
  "v_accvgpr_read_b32 v242, a232\n"
  "v_accvgpr_read_b32 v243, a233\n"
  "v_accvgpr_read_b32 v236, a230\n"
  "v_accvgpr_read_b32 v237, a231\n"
  "v_accvgpr_read_b32 v238, a234\n"
  "v_accvgpr_read_b32 v239, a235\n"
  "v_pk_fma_f32 v[240:241], v[240:241], v[84:85], v[84:85] neg_lo:[0,0,1] neg_hi:[0,0,1]\n"
  "v_pk_fma_f32 v[242:243], v[242:243], v[84:85], v[84:85] neg_lo:[0,0,1] neg_hi:[0,0,1]\n"
  "v_pk_fma_f32 v[236:237], v[236:237], v[86:87], v[86:87] neg_lo:[0,0,1] neg_hi:[0,0,1]\n"
  "v_pk_fma_f32 v[238:239], v[238:239], v[86:87], v[86:87] neg_lo:[0,0,1] neg_hi:[0,0,1]\n"
  "v_mfma_f32_16x16x32_bf16 v[156:159], v[44:47], a[140:143], v[156:159]\n"
  "ds_read_b64_tr_b16 v[0:1], v230 offset:6720\n"
  "ds_read_b64_tr_b16 v[2:3], v230 offset:6752\n"
  "v_cvt_pk_bf16_f32 v240, v240, v242\n"
  "v_cvt_pk_bf16_f32 v241, v241, v243\n"
  "v_cvt_pk_bf16_f32 v236, v236, v238\n"
  "v_cvt_pk_bf16_f32 v237, v237, v239\n"
  "v_pk_fma_f32 v[124:125], v[124:125], v[72:73], v[72:73] neg_lo:[0,0,1] neg_hi:[0,0,1]\n"
  "v_pk_fma_f32 v[128:129], v[128:129], v[72:73], v[72:73] neg_lo:[0,0,1] neg_hi:[0,0,1]\n"
  "v_pk_fma_f32 v[126:127], v[126:127], v[74:75], v[74:75] neg_lo:[0,0,1] neg_hi:[0,0,1]\n"
  "v_pk_fma_f32 v[130:131], v[130:131], v[74:75], v[74:75] neg_lo:[0,0,1] neg_hi:[0,0,1]\n"
  "v_mfma_f32_16x16x32_bf16 v[160:163], v[44:47], a[144:147], v[160:163]\n"
  "s_nop 0\n"
  "s_waitcnt lgkmcnt(12)\n"
  "buffer_store_dwordx4 v[244:247], v228, s[52:55], s82 offen offset:0 nt\n"
  "ds_write_b32 v233, v240 offset:17920\n"
  "ds_write_b32 v233, v241 offset:18208\n"
  "ds_write_b32 v233, v236 offset:18496\n"
  "ds_write_b32 v233, v237 offset:18784\n"
  "v_pk_fma_f32 v[132:133], v[132:133], v[72:73], v[72:73] neg_lo:[0,0,1] neg_hi:[0,0,1]\n"
  "v_pk_fma_f32 v[134:135], v[134:135], v[74:75], v[74:75] neg_lo:[0,0,1] neg_hi:[0,0,1]\n"
  "v_mfma_f32_16x16x32_bf16 v[164:167], v[44:47], a[148:151], v[164:167]\n"
  "v_accvgpr_read_b32 v240, a236\n"
  "v_accvgpr_read_b32 v241, a237\n"
  "v_accvgpr_read_b32 v242, a240\n"
  "v_accvgpr_read_b32 v243, a241\n"
  "v_accvgpr_read_b32 v236, a238\n"
  "v_accvgpr_read_b32 v237, a239\n"
  "v_accvgpr_read_b32 v238, a242\n"
  "v_accvgpr_read_b32 v239, a243\n"
  "v_cvt_pk_bf16_f32 v88, v88, v92\n"
  "v_cvt_pk_bf16_f32 v92, v89, v93\n"
  "v_cvt_pk_bf16_f32 v89, v96, v100\n"
  "v_cvt_pk_bf16_f32 v93, v97, v101\n"
  "v_cvt_pk_bf16_f32 v90, v90, v94\n"
  "v_cvt_pk_bf16_f32 v94, v91, v95\n"
  "v_cvt_pk_bf16_f32 v91, v98, v102\n"
  "v_cvt_pk_bf16_f32 v95, v99, v103\n"
  "v_pk_fma_f32 v[240:241], v[240:241], v[84:85], v[84:85] neg_lo:[0,0,1] neg_hi:[0,0,1]\n"
  "v_pk_fma_f32 v[242:243], v[242:243], v[84:85], v[84:85] neg_lo:[0,0,1] neg_hi:[0,0,1]\n"
  "v_pk_fma_f32 v[236:237], v[236:237], v[86:87], v[86:87] neg_lo:[0,0,1] neg_hi:[0,0,1]\n"
  "v_pk_fma_f32 v[238:239], v[238:239], v[86:87], v[86:87] neg_lo:[0,0,1] neg_hi:[0,0,1]\n"
  "v_mfma_f32_16x16x32_bf16 v[184:187], v[60:63], a[136:139], v[184:187]\n"
  "ds_read_b64_tr_b16 v[4:5], v230 offset:7840\n"
  "ds_read_b64_tr_b16 v[6:7], v230 offset:7872\n"
  "ds_write_b64 v229, v[88:89] offset:8960\n"
  "ds_write_b64 v229, v[92:93] offset:9248\n"
  "ds_write_b64 v229, v[90:91] offset:9536\n"
  "ds_write_b64 v229, v[94:95] offset:9824\n"
  "v_cvt_pk_bf16_f32 v240, v240, v242\n"
  "v_cvt_pk_bf16_f32 v241, v241, v243\n"
  "v_cvt_pk_bf16_f32 v236, v236, v238\n"
  "v_cvt_pk_bf16_f32 v237, v237, v239\n"
  "v_pk_fma_f32 v[152:153], v[152:153], v[76:77], v[76:77] neg_lo:[0,0,1] neg_hi:[0,0,1]\n"
  "v_pk_fma_f32 v[156:157], v[156:157], v[76:77], v[76:77] neg_lo:[0,0,1] neg_hi:[0,0,1]\n"
  "v_pk_fma_f32 v[154:155], v[154:155], v[78:79], v[78:79] neg_lo:[0,0,1] neg_hi:[0,0,1]\n"
  "v_pk_fma_f32 v[158:159], v[158:159], v[78:79], v[78:79] neg_lo:[0,0,1] neg_hi:[0,0,1]\n"
  "v_mfma_f32_16x16x32_bf16 v[188:191], v[60:63], a[140:143], v[188:191]\n"
  "s_nop 0\n"
  "s_waitcnt lgkmcnt(15)\n"
  "buffer_store_dwordx4 a[252:255], v228, s[52:55], s82 offen offset:3072 nt\n"
  "ds_write_b32 v233, v240 offset:19040\n"
  "ds_write_b32 v233, v241 offset:19328\n"
  "ds_write_b32 v233, v236 offset:19616\n"
  "ds_write_b32 v233, v237 offset:19904\n"
  "v_pk_fma_f32 v[160:161], v[160:161], v[76:77], v[76:77] neg_lo:[0,0,1] neg_hi:[0,0,1]\n"
  "v_pk_fma_f32 v[162:163], v[162:163], v[78:79], v[78:79] neg_lo:[0,0,1] neg_hi:[0,0,1]\n"
  "v_mfma_f32_16x16x32_bf16 v[192:195], v[60:63], a[144:147], v[192:195]\n"
  "ds_read_b64_tr_b16 v[16:17], v230 offset:8960\n"
  "ds_read_b64_tr_b16 v[18:19], v230 offset:8992\n"
  "ds_read_b64_tr_b16 v[20:21], v230 offset:10080\n"
  "ds_read_b64_tr_b16 v[22:23], v230 offset:10112\n"
  "v_cvt_pk_bf16_f32 v120, v120, v124\n"
  "v_pk_fma_f32 v[164:165], v[164:165], v[76:77], v[76:77] neg_lo:[0,0,1] neg_hi:[0,0,1]\n"
  "v_pk_fma_f32 v[166:167], v[166:167], v[78:79], v[78:79] neg_lo:[0,0,1] neg_hi:[0,0,1]\n"
  "v_mfma_f32_16x16x32_bf16 v[196:199], v[60:63], a[148:151], v[196:199]\n"
  "s_waitcnt lgkmcnt(4)\n"
  "s_barrier\n"
  "buffer_store_dwordx4 v[0:3], v228, s[52:55], s83 offen offset:0 nt\n"
  "v_cvt_pk_bf16_f32 v124, v121, v125\n"
  "v_cvt_pk_bf16_f32 v121, v128, v132\n"
  "v_pk_fma_f32 v[184:185], v[184:185], v[80:81], v[80:81] neg_lo:[0,0,1] neg_hi:[0,0,1]\n"
  "v_pk_fma_f32 v[186:187], v[186:187], v[82:83], v[82:83] neg_lo:[0,0,1] neg_hi:[0,0,1]\n"
  "ds_read_b64_tr_b16 v[244:245], v230 offset:17920\n"
  "ds_read_b64_tr_b16 v[246:247], v230 offset:17952\n"
  "ds_write_b64 v229, v[120:121] offset:11200\n"
  "v_pk_fma_f32 v[188:189], v[188:189], v[80:81], v[80:81] neg_lo:[0,0,1] neg_hi:[0,0,1]\n"
  "v_pk_fma_f32 v[190:191], v[190:191], v[82:83], v[82:83] neg_lo:[0,0,1] neg_hi:[0,0,1]\n"
  "ds_read_b64_tr_b16 a[252:253], v230 offset:19040\n"
  "ds_read_b64_tr_b16 a[254:255], v230 offset:19072\n"
  "ds_read_b64_tr_b16 v[0:1], v230 offset:20160\n"
  "ds_read_b64_tr_b16 v[2:3], v230 offset:20192\n"
  "v_cvt_pk_bf16_f32 v125, v129, v133\n"
  "v_pk_fma_f32 v[192:193], v[192:193], v[80:81], v[80:81] neg_lo:[0,0,1] neg_hi:[0,0,1]\n"
  "v_pk_fma_f32 v[194:195], v[194:195], v[82:83], v[82:83] neg_lo:[0,0,1] neg_hi:[0,0,1]\n"
  "buffer_store_dwordx4 v[4:7], v228, s[52:55], s83 offen offset:3072 nt\n"
  "ds_write_b64 v229, v[124:125] offset:11488\n"
  "v_cvt_pk_bf16_f32 v122, v122, v126\n"
  "v_pk_fma_f32 v[196:197], v[196:197], v[80:81], v[80:81] neg_lo:[0,0,1] neg_hi:[0,0,1]\n"
  "v_pk_fma_f32 v[198:199], v[198:199], v[82:83], v[82:83] neg_lo:[0,0,1] neg_hi:[0,0,1]\n"
  "v_cvt_pk_bf16_f32 v126, v123, v127\n"
  "v_cvt_pk_bf16_f32 v123, v130, v134\n"
  "v_cvt_pk_bf16_f32 v127, v131, v135\n"
  "v_cvt_pk_bf16_f32 v152, v152, v156\n"
  "v_cvt_pk_bf16_f32 v156, v153, v157\n"
  "s_waitcnt lgkmcnt(10)\n"
  "buffer_store_dwordx4 v[16:19], v228, s[52:55], s80 offen offset:128 nt\n"
  "ds_write_b64 v229, v[122:123] offset:11776\n"
  "v_cvt_pk_bf16_f32 v153, v160, v164\n"
  "ds_write_b64 v229, v[126:127] offset:12064\n"
  "v_cvt_pk_bf16_f32 v157, v161, v165\n"
  "v_cvt_pk_bf16_f32 v154, v154, v158\n"
  "s_waitcnt lgkmcnt(10)\n"
  "buffer_store_dwordx4 v[20:23], v228, s[52:55], s80 offen offset:3200 nt\n"
  "ds_write_b64 v229, v[152:153] offset:13440\n"
  "v_cvt_pk_bf16_f32 v158, v155, v159\n"
  "ds_read_b64_tr_b16 v[4:5], v230 offset:11200\n"
  "ds_read_b64_tr_b16 v[6:7], v230 offset:11232\n"
  "ds_write_b64 v229, v[156:157] offset:13728\n"
  "s_waitcnt lgkmcnt(12)\n"
  "buffer_store_dwordx4 v[244:247], v228, s[52:55], s56 offen offset:0 nt\n"
  "ds_read_b64_tr_b16 v[16:17], v230 offset:12320\n"
  "ds_read_b64_tr_b16 v[18:19], v230 offset:12352\n"
  "v_cvt_pk_bf16_f32 v155, v162, v166\n"
  "v_cvt_pk_bf16_f32 v159, v163, v167\n"
  "ds_write_b64 v229, v[154:155] offset:14016\n"
  "v_cvt_pk_bf16_f32 v184, v184, v188\n"
  "v_cvt_pk_bf16_f32 v188, v185, v189\n"
  "s_waitcnt lgkmcnt(12)\n"
  "buffer_store_dwordx4 a[252:255], v228, s[52:55], s56 offen offset:128 nt\n"
  "ds_write_b64 v229, v[158:159] offset:14304\n"
  "v_cvt_pk_bf16_f32 v185, v192, v196\n"
  "ds_write_b64 v229, v[184:185] offset:15680\n"
  "v_cvt_pk_bf16_f32 v189, v193, v197\n"
  "v_cvt_pk_bf16_f32 v186, v186, v190\n"
  "s_waitcnt lgkmcnt(12)\n"
  "buffer_store_dwordx4 v[0:3], v228, s[52:55], s56 offen offset:256 nt\n"
  "ds_read_b64_tr_b16 v[20:21], v230 offset:13440\n"
  "ds_write_b64 v229, v[188:189] offset:15968\n"
  "ds_read_b64_tr_b16 v[22:23], v230 offset:13472\n"
  "ds_read_b64_tr_b16 v[244:245], v230 offset:14560\n"
  "ds_read_b64_tr_b16 v[246:247], v230 offset:14592\n"
  "v_cvt_pk_bf16_f32 v190, v187, v191\n"
  "v_cvt_pk_bf16_f32 v187, v194, v198\n"
  "s_waitcnt lgkmcnt(11)\n"
  "buffer_store_dwordx4 v[4:7], v228, s[52:55], s81 offen offset:128 nt\n"
  "ds_write_b64 v229, v[186:187] offset:16256\n"
  "v_cvt_pk_bf16_f32 v191, v195, v199\n"
  "ds_write_b64 v229, v[190:191] offset:16544\n"
  "s_waitcnt lgkmcnt(10)\n"
  "buffer_store_dwordx4 v[16:19], v228, s[52:55], s81 offen offset:3200 nt\n"
  "ds_read_b64_tr_b16 a[252:253], v230 offset:15680\n"
  "ds_read_b64_tr_b16 a[254:255], v230 offset:15712\n"
  "ds_read_b64_tr_b16 v[0:1], v230 offset:16800\n"
  "ds_read_b64_tr_b16 v[2:3], v230 offset:16832\n"
  "s_waitcnt lgkmcnt(8)\n"
  "buffer_store_dwordx4 v[20:23], v228, s[52:55], s82 offen offset:128 nt\n"
  "s_waitcnt lgkmcnt(6)\n"
  "buffer_store_dwordx4 v[244:247], v228, s[52:55], s82 offen offset:3200 nt\n"
  "s_waitcnt lgkmcnt(2)\n"
  "buffer_store_dwordx4 a[252:255], v228, s[52:55], s83 offen offset:128 nt\n"
  "s_waitcnt lgkmcnt(0)\n"
  "buffer_store_dwordx4 v[0:3], v228, s[52:55], s83 offen offset:3200 nt\n"
  "L_END_%=:\n"
  "s_mov_b32 %0, s78\n"
  : "=&s"(bad)
  : "s"((unsigned long long)__builtin_amdgcn_kernarg_segment_ptr()), "s"(wg), "s"(tid0), "s"((unsigned)(size_t)lds)
  : "v0", "v1", "v2", "v3", "v4", "v5", "v6", "v7", "v8", "v9", "v10", "v11", "v12", "v13", "v14", "v15", "v16", "v17", "v18", "v19", "v20", "v21", "v22", "v23", "v24", "v25", "v26", "v27", "v28", "v29", "v30", "v31", "v32", "v33", "v34", "v35", "v36", "v37", "v38", "v39", "v40", "v41", "v42", "v43", "v44", "v45", "v46", "v47", "v48", "v49", "v50", "v51", "v52", "v53", "v54", "v55", "v56", "v57", "v58", "v59", "v60", "v61", "v62", "v63", "v64", "v65", "v66", "v67", "v68", "v69", "v70", "v71", "v72", "v73", "v74", "v75", "v76", "v77", "v78", "v79", "v80", "v81", "v82", "v83", "v84", "v85", "v86", "v87", "v88", "v89", "v90", "v91", "v92", "v93", "v94", "v95", "v96", "v97", "v98", "v99", "v100", "v101", "v102", "v103", "v104", "v105", "v106", "v107", "v108", "v109", "v110", "v111", "v112", "v113", "v114", "v115", "v116", "v117", "v118", "v119", "v120", "v121", "v122", "v123", "v124", "v125", "v126", "v127", "v128", "v129", "v130", "v131", "v132", "v133", "v134", "v135", "v136", "v137", "v138", "v139", "v140", "v141", "v142", "v143", "v144", "v145", "v146", "v147", "v148", "v149", "v150", "v151", "v152", "v153", "v154", "v155", "v156", "v157", "v158", "v159", "v160", "v161", "v162", "v163", "v164", "v165", "v166", "v167", "v168", "v169", "v170", "v171", "v172", "v173", "v174", "v175", "v176", "v177", "v178", "v179", "v180", "v181", "v182", "v183", "v184", "v185", "v186", "v187", "v188", "v189", "v190", "v191", "v192", "v193", "v194", "v195", "v196", "v197", "v198", "v199", "v200", "v201", "v202", "v203", "v204", "v205", "v206", "v207", "v208", "v209", "v210", "v211", "v212", "v213", "v214", "v215", "v216", "v217", "v218", "v219", "v220", "v221", "v222", "v223", "v224", "v225", "v226", "v227", "v228", "v229", "v230", "v231", "v232", "v233", "v234", "v235", "v236", "v237", "v238", "v239", "v240", "v241", "v242", "v243", "v244", "v245", "v246", "v247", "v248", "v249", "v250", "v251", "v252", "v253", "v254", "v255", "a0", "a1", "a2", "a3", "a4", "a5", "a6", "a7", "a8", "a9", "a10", "a11", "a12", "a13", "a14", "a15", "a16", "a17", "a18", "a19", "a20", "a21", "a22", "a23", "a24", "a25", "a26", "a27", "a28", "a29", "a30", "a31", "a32", "a33", "a34", "a35", "a36", "a37", "a38", "a39", "a40", "a41", "a42", "a43", "a44", "a45", "a46", "a47", "a48", "a49", "a50", "a51", "a52", "a53", "a54", "a55", "a56", "a57", "a58", "a59", "a60", "a61", "a62", "a63", "a64", "a65", "a66", "a67", "a68", "a69", "a70", "a71", "a72", "a73", "a74", "a75", "a76", "a77", "a78", "a79", "a80", "a81", "a82", "a83", "a84", "a85", "a86", "a87", "a88", "a89", "a90", "a91", "a92", "a93", "a94", "a95", "a96", "a97", "a98", "a99", "a100", "a101", "a102", "a103", "a104", "a105", "a106", "a107", "a108", "a109", "a110", "a111", "a112", "a113", "a114", "a115", "a116", "a117", "a118", "a119", "a120", "a121", "a122", "a123", "a124", "a125", "a126", "a127", "a128", "a129", "a130", "a131", "a132", "a133", "a134", "a135", "a136", "a137", "a138", "a139", "a140", "a141", "a142", "a143", "a144", "a145", "a146", "a147", "a148", "a149", "a150", "a151", "a152", "a153", "a154", "a155", "a156", "a157", "a158", "a159", "a160", "a161", "a162", "a163", "a164", "a165", "a166", "a167", "a168", "a169", "a170", "a171", "a172", "a173", "a174", "a175", "a176", "a177", "a178", "a179", "a180", "a181", "a182", "a183", "a184", "a185", "a186", "a187", "a188", "a189", "a190", "a191", "a192", "a193", "a194", "a195", "a196", "a197", "a198", "a199", "a200", "a201", "a202", "a203", "a204", "a205", "a206", "a207", "a208", "a209", "a210", "a211", "a212", "a213", "a214", "a215", "a216", "a217", "a218", "a219", "a220", "a221", "a222", "a223", "a224", "a225", "a226", "a227", "a228", "a229", "a230", "a231", "a232", "a233", "a234", "a235", "a236", "a237", "a238", "a239", "a240", "a241", "a242", "a243", "a244", "a245", "a246", "a247", "a248", "a249", "a250", "a251", "a252", "a253", "a254", "a255", "s20", "s21", "s22", "s23", "s24", "s25", "s26", "s27", "s28", "s29", "s30", "s31", "s32", "s33", "s34", "s35", "s36", "s37", "s38", "s39", "s40", "s41", "s42", "s43", "s44", "s45", "s46", "s47", "s48", "s49", "s50", "s51", "s52", "s53", "s54", "s55", "s56", "s57", "s58", "s59", "s60", "s61", "s62", "s63", "s64", "s65", "s66", "s67", "s68", "s69", "s70", "s71", "s72", "s73", "s74", "s75", "s76", "s77", "s78", "s79", "s80", "s81", "s82", "s83", "s84", "s85", "s86", "s87", "s88", "s89", "s90", "s91", "s92", "s93", "s94", "s95", "s96", "s97", "s98", "s99", "s100", "s101", "vcc", "scc", "memory");
  if (bad) {
    unsigned long long ka = (unsigned long long)__builtin_amdgcn_kernarg_segment_ptr();
    asm volatile("s_waitcnt vmcnt(0)" : "+s"(ka) :: "memory");
    const unsigned short* const* arg = (const unsigned short* const*)ka;
    const int xcd = wg & 7, slot = wg >> 3;
    constexpr int HB = 0;
    auto head_of = [&](int lh) { return lh + (lh >> HB) * (7 << HB) + (xcd << HB); };
#pragma unroll 1
    for (int it = 0; it < 6; ++it) {
      if (!((bad >> it) & 1u)) continue;
      const int p = slot + 32 * it, head = head_of(p / 3), r0 = 288 * (p % 3);
#pragma unroll 1
      for (int Y = 0; Y < 5; ++Y) {
        const int row = r0 + (Y < 4 ? 64 * wid + 16 * Y : 256 + 16 * (wid >> 1)), lo = Y < 4 ? 0 : 8 * (wid & 1);
        if (row < SQ) exact_tile(arg[0], arg[1], arg[2], (unsigned short*)arg[3], head, row, lo, Y < 4 ? 16 : lo + 8);
      }
    }
  }
}

extern "C" void kernel_launch(void* const* d_in, const int* in_sizes, int n_in,
                              void* d_out, int out_size, void* d_ws, size_t ws_size,
                              hipStream_t stream) {
  (void)hipFuncSetAttribute((const void*)attn_fwd_g, hipFuncAttributeMaxDynamicSharedMemorySize, LDS_BYTES);
  hipLaunchKernelGGL(attn_fwd_g, dim3(256), dim3(256), LDS_BYTES, stream, (const unsigned short*)d_in[0], (const unsigned short*)d_in[1],
                     (const unsigned short*)d_in[2], (unsigned short*)d_out);
}
```

```cpp
#include <hip/hip_runtime.h>
#include <hip/hip_bf16.h>

typedef __bf16   bf16x8 __attribute__((ext_vector_type(8)));
typedef __bf16   bf16x2 __attribute__((ext_vector_type(2)));
typedef float    f32x4  __attribute__((ext_vector_type(4)));
typedef float    f32x2  __attribute__((ext_vector_type(2)));
typedef unsigned u32x4  __attribute__((ext_vector_type(4)));
typedef unsigned u32x2  __attribute__((ext_vector_type(2)));
typedef unsigned short u16x8 __attribute__((ext_vector_type(8)));

constexpr int SQ = 848, SK = 512, DH = 192, ROWB = 384;
constexpr int LDS_BYTES = 163840;
constexpr float CSC = 0.07216878364870322f * 1.4426950408889634f;

#define DI __device__ __forceinline__
DI unsigned cvtpk(float lo, float hi) { f32x2 v = {lo, hi}; bf16x2 b = __builtin_convertvector(v, bf16x2); return __builtin_bit_cast(unsigned, b); }
DI float xmax(float x) {
  auto r = __builtin_amdgcn_permlane32_swap(__float_as_uint(x), __float_as_uint(x), false, false);
  x = fmaxf(__uint_as_float(r[0]), __uint_as_float(r[1]));
  auto q = __builtin_amdgcn_permlane16_swap(__float_as_uint(x), __float_as_uint(x), false, false);
  return fmaxf(__uint_as_float(q[0]), __uint_as_float(q[1]));
}
DI float xsum(float x) {
  auto r = __builtin_amdgcn_permlane32_swap(__float_as_uint(x), __float_as_uint(x), false, false);
  x = __uint_as_float(r[0]) + __uint_as_float(r[1]);
  auto q = __builtin_amdgcn_permlane16_swap(__float_as_uint(x), __float_as_uint(x), false, false);
  return __uint_as_float(q[0]) + __uint_as_float(q[1]);
}
DI f32x4 score_tile(const unsigned short* krow, const bf16x8 (&qf)[6]) {
  f32x4 acc = {0.f, 0.f, 0.f, 0.f};
#pragma unroll
  for (int s = 0; s < 6; ++s) acc = __builtin_amdgcn_mfma_f32_16x16x32_bf16(*(const bf16x8*)(krow + 32 * s), qf[s], acc, 0, 0, 0);
  return acc;
}
DI void exact_tile(const unsigned short* Q, const unsigned short* K, const unsigned short* V, unsigned short* O, int head, int row0, int lo, int hi) {
  const unsigned lane = __builtin_amdgcn_mbcnt_hi(~0u, __builtin_amdgcn_mbcnt_lo(~0u, 0u));
  const int c = lane & 15, g4 = lane >> 4;
  const int qrow = min(row0 + c, SQ - 1);
  bf16x8 qf[6];
#pragma unroll
  for (int s = 0; s < 6; ++s) qf[s] = *(const bf16x8*)(Q + ((size_t)head * SQ + qrow) * DH + 32 * s + 8 * g4);
  const unsigned short* Kh = K + ((size_t)head * SK + c) * DH + 8 * g4;
  float m = -__builtin_huge_valf();
#pragma unroll 1
  for (int kb = 0; kb < SK / 16; ++kb) {
    const f32x4 a = score_tile(Kh + (size_t)kb * 16 * DH, qf);
    m = fmaxf(fmaxf(m, fmaxf(a[0], a[1])), fmaxf(a[2], a[3]));
  }
  const float mc = -xmax(m) * CSC;
  float l = 0.f;
  f32x4 o[12];
#pragma unroll
  for (int dt = 0; dt < 12; ++dt) o[dt] = f32x4{0.f, 0.f, 0.f, 0.f};
#pragma unroll 1
  for (int j = 0; j < SK / 32; ++j) {
    u32x4 p;
#pragma unroll
    for (int kt = 0; kt < 2; ++kt) {
      f32x4 a = score_tile(Kh + (size_t)(32 * j + 16 * kt) * DH, qf);
#pragma unroll
      for (int r = 0; r < 4; ++r) { a[r] = __builtin_amdgcn_exp2f(fmaf(a[r], CSC, mc)); l += a[r]; }
      p[2 * kt] = cvtpk(a[0], a[1]); p[2 * kt + 1] = cvtpk(a[2], a[3]);
    }
    const unsigned short* Vj = V + ((size_t)head * SK + 32 * j + 4 * g4) * DH + c;
#pragma unroll
    for (int dt = 0; dt < 12; ++dt) {
      u16x8 vf;
#pragma unroll
      for (int i = 0; i < 8; ++i) vf[i] = Vj[(16 * (i >> 2) + (i & 3)) * DH + 16 * dt];
      o[dt] = __builtin_amdgcn_mfma_f32_16x16x32_bf16(__builtin_bit_cast(bf16x8, vf), __builtin_bit_cast(bf16x8, p), o[dt], 0, 0, 0);
    }
  }
  const float inv = 1.0f / xsum(l);
  if (row0 + c < SQ && c >= lo && c < hi) {
#pragma unroll
    for (int dt = 0; dt < 12; ++dt)
      *(u32x2*)(O + ((size_t)head * SQ + qrow) * DH + 16 * dt + 4 * g4) = u32x2{cvtpk(o[dt][0] * inv, o[dt][1] * inv), cvtpk(o[dt][2] * inv, o[dt][3] * inv)};
  }
}

__global__ __launch_bounds__(256, 1)
void attn_fwd_g(const unsigned short* __restrict__ Qg, const unsigned short* __restrict__ Kg,
                const unsigned short* __restrict__ Vg, unsigned short* __restrict__ Og) {
  extern __shared__ __attribute__((aligned(512))) char lds[];
  const int tid0 = __builtin_amdgcn_readfirstlane(threadIdx.x), wg = blockIdx.x, wid = tid0 >> 6;
  unsigned bad;
  asm volatile(
  ".p2align 4\n"
  "s_load_dwordx8 s[84:91], %1, 0\n"
  "L_PC_%=:\n"
  "s_getpc_b64 s[52:53]\n"
  "s_lshr_b32 s79, %2, 3\n"
  "s_lshl_b32 s79, s79, 15\n"
  "s_add_u32 s52, s52, s79\n"
  "s_addc_u32 s53, s53, 0\n"
  "s_sub_i32 s54, L_END_%=-L_PC_%=, s79\n"
  "s_max_i32 s54, s54, 0\n"
  "s_mov_b32 s55, 0x20000\n"
  "s_add_u32 m0, %4, 2048\n"
  "v_mbcnt_lo_u32_b32 v0, -1, 0\n"
  "v_mbcnt_hi_u32_b32 v0, -1, v0\n"
  "v_add_lshl_u32 v1, v0, %3, 7\n"
  "buffer_load_dword v1, s[52:55], 0 offen lds\n"
  "s_bitcmp1_b32 %2, 0\n"
  "s_cbranch_scc1 2\n"
  "s_waitcnt vmcnt(0) lgkmcnt(0)\n"
  "s_sleep 110\n"
  "s_lshr_b32 s38, %3, 6\n"
  "s_lshr_b32 s79, %2, 3\n"
  "s_mul_i32 s100, s79, 43\n"
  "s_lshr_b32 s100, s100, 7\n"
  "s_mul_i32 s71, s100, 3\n"
  "s_sub_u32 s95, s79, s71\n"
  "s_and_b32 s101, %2, 7\n"
  "s_lshl_b32 s101, s101, 0\n"
  "s_lshr_b32 s79, s100, 0\n"
  "s_mul_i32 s79, s79, 7\n"
  "s_add_u32 s79, s79, s100\n"
  "s_add_u32 s79, s79, s101\n"
  "s_mul_i32 s37, s79, 196608\n"
  "s_mul_i32 s92, s79, 325632\n"
  "s_mul_i32 s79, s95, 110592\n"
  "s_add_u32 s92, s92, s79\n"
  "s_mul_i32 s79, s38, 3072\n"
  "s_add_u32 s96, s79, s37\n"
  "s_lshr_b32 s71, s38, 1\n"
  "s_lshl_b32 s71, s71, 6\n"
  "s_add_u32 s79, s79, s71\n"
  "s_add_u32 s79, s79, %4\n"
  "s_add_u32 s66, s79, 41152\n"
  "s_add_u32 s67, s79, 53504\n"
  "s_add_u32 s68, s79, 65856\n"
  "s_mul_i32 s79, s38, 3088\n"
  "s_add_u32 s79, s79, %4\n"
  "s_add_u32 s58, s79, 4096\n"
  "s_add_u32 s59, s79, 16448\n"
  "s_add_u32 s60, s79, 28800\n"
  "s_mul_i32 s72, s38, 21312\n"
  "s_add_u32 s72, s72, %4\n"
  "s_add_u32 s72, s72, 78208\n"
  "s_movk_i32 s42, 0xc00\n"
  "s_movk_i32 s31, 12288\n"
  "s_mov_b32 s43, 0x20000\n"
  "s_mov_b32 s51, 0x20000\n"
  "s_bitcmp1_b32 s95, 1\n"
  "s_movk_i32 s93, 288\n"
  "s_cmovk_i32 s93, 272\n"
  "s_mul_i32 s93, s93, 384\n"
  "s_mov_b32 s50, s93\n"
  "s_mul_i32 s80, s38, 24576\n"
  "s_add_u32 s81, s80, 6144\n"
  "v_lshrrev_b32 v1, 3, v0\n"
  "v_and_b32 v2, 2, v1\n"
  "v_and_b32 v3, 7, v0\n"
  "v_xor_b32 v2, v2, v3\n"
  "v_mul_u32_u24 v221, 0x180, v1\n"
  "v_lshl_add_u32 v221, v2, 4, v221\n"
  "s_waitcnt lgkmcnt(0)\n"
  "s_add_u32 s40, s86, s96\n"
  "s_addc_u32 s41, s87, 0\n"
  "s_add_u32 s48, s84, s92\n"
  "s_addc_u32 s49, s85, 0\n"
  "s_mov_b32 m0, s68\n"
  "s_nop 0\n"
  "buffer_load_dwordx4 v221, s[40:43], 0 offen offset:0 lds\n"
  "s_addk_i32 m0, 896\n"
  "s_nop 0\n"
  "buffer_load_dwordx4 v221, s[40:43], 0 offen offset:128 lds\n"
  "s_addk_i32 m0, 896\n"
  "s_nop 0\n"
  "buffer_load_dwordx4 v221, s[40:43], 0 offen offset:256 lds\n"
  "s_add_u32 s40, s40, s31\n"
  "s_addc_u32 s41, s41, 0\n"
  "s_add_u32 s44, s88, s96\n"
  "s_addc_u32 s45, s89, 0\n"
  "s_mov_b64 s[62:63], s[90:91]\n"
  "s_mov_b32 s90, 100663296\n"
  "s_mov_b32 s91, 0x20000\n"
  "s_add_u32 s29, %4, 163456\n"
  "v_mbcnt_lo_u32_b32 v4, -1, 0\n"
  "v_mbcnt_hi_u32_b32 v4, -1, v4\n"
  "v_mul_u32_u24 v4, 0x18c, v4\n"
  "s_mov_b32 m0, s29\n"
  "s_nop 0\n"
  "buffer_load_dword v4, s[88:91], s37 offen lds\n"
  "s_mul_i32 s56, s38, 3072\n"
  "s_add_u32 s56, s56, 98304\n"
  "s_add_u32 m0, s72, 17920\n"
  "s_nop 0\n"
  "buffer_load_dwordx4 v221, s[48:51], s56 offen offset:0  lds\n"
  "s_add_u32 s82, s80, 12288\n"
  "s_add_u32 s83, s80, 18432\n"
  "s_mov_b64 s[46:47], s[42:43]\n"
  "s_mov_b64 s[34:35], s[42:43]\n"
  "s_bitcmp1_b32 s38, 0\n"
  "s_movk_i32 s70, 512\n"
  "s_cmovk_i32 s70, -512\n"
  "s_movk_i32 s97, 0\n"
  "s_cmovk_i32 s97, -21312\n"
  "s_movk_i32 s61, 0\n"
  "s_cmovk_i32 s61, -21308\n"
  "s_movk_i32 s69, 20196\n"
  "s_cmovk_i32 s69, 20188\n"
  "s_add_u32 m0, s72, 18912\n"
  "s_nop 0\n"
  "buffer_load_dwordx4 v221, s[48:51], s56 offen offset:128  lds\n"
  "s_mov_b32 s98, 0xff00ff00\n"
  "s_mov_b32 s99, s98\n"
  "s_mov_b32 s73, 0x3dd53b94\n"
  "s_mov_b32 s74, 0xbdd53b94\n"
  "s_mov_b32 s75, 0x53800000\n"
  "s_mov_b32 s76, -1\n"
  "s_add_u32 m0, s72, 19904\n"
  "s_nop 0\n"
  "buffer_load_dwordx4 v221, s[48:51], s56 offen offset:256  lds\n"
  "s_mov_b32 s77, 1\n"
  "s_mov_b32 s78, 0\n"
  "s_mov_b32 s57, s92\n"
  "s_mul_i32 s39, s38, 3072\n"
  "s_mov_b32 s54, 0\n"
  "s_add_u32 m0, s58, 1792\n"
  "s_nop 0\n"
  "buffer_load_dwordx4 v221, s[48:51], s80 offen offset:256  lds\n"
  "s_mov_b32 s55, 0x20000\n"
  "L_PC2_%=:\n"
  "s_getpc_b64 s[20:21]\n"
  "s_lshl_b32 s24, s38, 13\n"
  "s_add_u32 s24, s24, L_BIG_%=-L_PC2_%=-4\n"
  "s_cmp_lt_u32 s38, 2\n"
  "s_cselect_b32 s24, s24, L_TAIL_%=-L_PC2_%=-4\n"
  "s_sub_u32 s25, L_END_%=-L_PC2_%=-4, s24\n"
  "s_add_u32 s20, s20, s24\n"
  "s_addc_u32 s21, s21, 0\n"
  "s_mov_b32 s23, 0x20000\n"
  "s_lshr_b32 s24, s38, 1\n"
  "s_mul_i32 s24, s24, s38\n"
  "s_mul_i32 s24, s24, 5\n"
  "s_lshr_b32 s24, s24, 1\n"
  "v_lshrrev_b32 v1, 4, v0\n"
  "v_and_b32 v2, 2, v0\n"
  "v_xor_b32_e64 v1, v1, v2\n"
  "s_add_u32 m0, s60, -1280\n"
  "s_nop 0\n"
  "buffer_load_dwordx4 v221, s[48:51], s80 offen offset:3328  lds\n"
  "v_lshlrev_b32 v1, 4, v1\n"
  "v_and_b32 v2, 7, v0\n"
  "v_lshl_add_u32 v72, v2, 7, v1\n"
  "v_bfe_u32 v3, v0, 3, 1\n"
  "v_mul_u32_u24 v2, 0x440, v3\n"
  "s_add_u32 m0, s66, -128\n"
  "s_nop 0\n"
  "buffer_load_dwordx4 v221, s[48:51], s80 offen offset:128  lds\n"
  "v_add3_u32 v222, v72, v2, s72\n"
  "v_mul_u32_u24 v2, 0x6080, v3\n"
  "v_add3_u32 v74, v72, v2, s58\n"
  "v_lshl_add_u32 v72, v3, 10, v72\n"
  "v_add_u32 v73, s66, v72\n"
  "s_add_u32 m0, s66, -2176\n"
  "s_nop 0\n"
  "buffer_load_dwordx4 v221, s[48:51], s80 offen offset:3200  lds\n"
  "v_add_u32 v72, s58, v72\n"
  "v_lshl_add_u32 v1, v3, 6, v1\n"
  "v_and_b32 v2, 3, v0\n"
  "v_lshl_add_u32 v1, v2, 7, v1\n"
  "v_bfe_u32 v2, v0, 2, 2\n"
  "s_add_u32 m0, s58, 0\n"
  "s_nop 0\n"
  "buffer_load_dwordx4 v221, s[48:51], s80 offen offset:0  lds\n"
  "v_mul_u32_u24 v2, 0xc00, v2\n"
  "s_and_b32 s79, s38, 1\n"
  "s_lshl_b32 s79, s79, 9\n"
  "s_add_u32 s79, s79, %4\n"
  "s_add_u32 s71, s79, 41152\n"
  "s_add_u32 m0, s58, -2048\n"
  "s_nop 0\n"
  "buffer_load_dwordx4 v221, s[48:51], s80 offen offset:3072  lds\n"
  "v_add3_u32 v216, v1, v2, s71\n"
  "v_add_u32 v235, s70, v216\n"
  "v_add_u32 v233, s97, v222\n"
  "s_mov_b64 exec, s[98:99]\n"
  "v_add_u32 v233, 0x4f00, v233\n"
  "s_mov_b64 exec, -1\n"
  "v_lshrrev_b32_e64 v1, 4, v0\n"
  "v_mul_u32_u24 v1, 0xc10, v1\n"
  "s_mov_b32 m0, s67\n"
  "s_nop 0\n"
  "buffer_load_dwordx4 v221, s[40:43], 0 offen offset:0 lds\n"
  "v_bfe_u32 v2, v0, 2, 2\n"
  "v_lshl_add_u32 v1, v2, 7, v1\n"
  "v_bfe_u32 v2, v0, 1, 1\n"
  "v_lshl_add_u32 v1, v2, 6, v1\n"
  "v_and_b32_e64 v2, 1, v0\n"
  "s_addk_i32 m0, 896\n"
  "s_nop 0\n"
  "buffer_load_dwordx4 v221, s[40:43], 0 offen offset:128 lds\n"
  "v_lshl_add_u32 v1, v2, 3, v1\n"
  "s_add_u32 s71, s79, 4096\n"
  "v_add_u32 v1, s71, v1\n"
  "v_lshrrev_b32 v2, 3, v0\n"
  "v_xor_b32_e64 v2, s38, v2\n"
  "s_addk_i32 m0, 896\n"
  "s_nop 0\n"
  "buffer_load_dwordx4 v221, s[40:43], 0 offen offset:256 lds\n"
  "s_add_u32 s40, s40, s31\n"
  "s_addc_u32 s41, s41, 0\n"
  "v_and_b32 v2, 1, v2\n"
  "v_lshl_add_u32 v217, v2, 5, v1\n"
  "v_xor_b32 v2, 1, v2\n"
  "v_lshl_add_u32 v219, v2, 5, v1\n"
  "v_add_u32 v218, s70, v217\n"
  "s_add_u32 m0, s59, 896\n"
  "s_nop 0\n"
  "buffer_load_dwordx4 v221, s[44:47], 0 offen offset:128 lds\n"
  "v_add_u32_e64 v220, s70, v219\n"
  "s_lshl_b32 s71, s38, 9\n"
  "s_add_u32 s71, s71, %4\n"
  "s_add_u32 s71, s71, 0\n"
  "v_lshl_add_u32 v231, v0, 3, s71\n"
  "s_addk_i32 m0, 896\n"
  "s_nop 0\n"
  "buffer_load_dwordx4 v221, s[44:47], 0 offen offset:256 lds\n"
  "s_mov_b64 s[32:33], s[44:45]\n"
  "s_add_u32 s44, s44, s31\n"
  "s_addc_u32 s45, s45, 0\n"
  "v_add_u32_e64 v232, s70, v231\n"
  "v_mov_b32 v248, 0x43400000\n"
  "v_mov_b32 v249, 0x43400000\n"
  "v_mov_b32 v250, 0x43400000\n"
  "v_mov_b32 v251, 0x43400000\n"
  "s_add_u32 m0, s72, 0\n"
  "s_nop 0\n"
  "buffer_load_dwordx4 v221, s[48:51], s81 offen offset:0  lds\n"
  "v_mov_b32 v1, 0x3f803f80\n"
  "v_accvgpr_write_b32 a152, v1\n"
  "v_accvgpr_write_b32 a153, v1\n"
  "v_accvgpr_write_b32 a154, v1\n"
  "v_accvgpr_write_b32 a155, v1\n"
  "s_add_u32 m0, s72, -1984\n"
  "s_nop 0\n"
  "buffer_load_dwordx4 v221, s[48:51], s81 offen offset:3072  lds\n"
  "v_bfe_u32 v1, v0, 3, 1\n"
  "v_lshrrev_b32 v230, 4, v0\n"
  "v_lshl_add_u32 v1, v1, 2, v230\n"
  "v_mul_u32_u24 v228, 0x180, v1\n"
  "s_and_b32 s71, s38, 1\n"
  "s_add_u32 m0, s72, 2112\n"
  "s_nop 0\n"
  "buffer_load_dwordx4 v221, s[48:51], s81 offen offset:128  lds\n"
  "s_lshl_b32 s71, s71, 1\n"
  "v_and_b32 v1, 7, v0\n"
  "v_xor_b32_e64 v1, s71, v1\n"
  "v_lshl_add_u32 v228, v1, 4, v228\n"
  "v_mul_u32_u24 v230, 0x120, v230\n"
  "s_add_u32 m0, s72, 128\n"
  "s_nop 0\n"
  "buffer_load_dwordx4 v221, s[48:51], s81 offen offset:3200  lds\n"
  "v_and_b32 v1, 3, v0\n"
  "v_lshl_add_u32 v230, v1, 6, v230\n"
  "v_bfe_u32 v1, v0, 2, 2\n"
  "v_lshl_add_u32 v230, v1, 3, v230\n"
  "v_add_u32 v230, s72, v230\n"
  "s_add_u32 m0, s60, -256\n"
  "s_nop 0\n"
  "buffer_load_dwordx4 v221, s[48:51], s81 offen offset:256  lds\n"
  "v_and_b32_e64 v1, 31, v0\n"
  "v_lshlrev_b32_e64 v229, 3, v1\n"
  "v_lshrrev_b32_e64 v1, 5, v0\n"
  "v_mul_u32_u24 v1, 0x460, v1\n"
  "v_add3_u32 v229, v1, v229, s72\n"
  "s_waitcnt vmcnt(16)\n"
  "s_barrier\n"
  "ds_read_b128 a[164:167], v216 offset:24704\n"
  "ds_read_b128 a[168:171], v216 offset:24768\n"
  "ds_read_b128 a[172:175], v216 offset:25728\n"
  "ds_read_b128 a[176:179], v216 offset:25792\n"
  "ds_read_b128 a[180:183], v216 offset:26752\n"
  "ds_read_b128 a[184:187], v216 offset:26816\n"
  "ds_read_b128 a[96:99], v233 offset:17920\n"
  "ds_read_b128 a[100:103], v233 offset:17984\n"
  "ds_read_b128 a[104:107], v233 offset:19040\n"
  "ds_read_b128 a[108:111], v233 offset:19104\n"
  "ds_read_b128 a[112:115], v233 offset:20160\n"
  "ds_read_b128 a[116:119], v233 offset:20224\n"
  "v_bfe_u32 v5, v231, 1, 8\n"
  "v_add_u32_e64 v5, s29, v5\n"
  "ds_read_b128 a[188:191], v235 offset:24704\n"
  "ds_read_b128 a[192:195], v235 offset:24768\n"
  "ds_read_b128 a[196:199], v235 offset:25728\n"
  "ds_read_b128 a[200:203], v235 offset:25792\n"
  "ds_read_b128 a[204:207], v235 offset:26752\n"
  "ds_read_b128 a[208:211], v235 offset:26816\n"
  "ds_read_b32 v4, v5\n"
  "v_bfe_u32 v234, v231, 1, 6\n"
  "s_waitcnt lgkmcnt(12)\n"
  "v_mfma_f32_16x16x32_bf16 v[64:67], a[164:167], a[96:99], v[248:251]\n"
  "s_waitcnt lgkmcnt(11)\n"
  "v_mfma_f32_16x16x32_bf16 v[64:67], a[168:171], a[100:103], v[64:67]\n"
  "s_add_u32 m0, s60, -2304\n"
  "s_nop 0\n"
  "buffer_load_dwordx4 v221, s[48:51], s81 offen offset:3328  lds\n"
  "s_waitcnt lgkmcnt(10)\n"
  "v_mfma_f32_16x16x32_bf16 v[64:67], a[172:175], a[104:107], v[64:67]\n"
  "s_waitcnt lgkmcnt(9)\n"
  "v_mfma_f32_16x16x32_bf16 v[64:67], a[176:179], a[108:111], v[64:67]\n"
  "s_waitcnt lgkmcnt(8)\n"
  "v_mfma_f32_16x16x32_bf16 v[64:67], a[180:183], a[112:115], v[64:67]\n"
  "s_waitcnt lgkmcnt(0)\n"
  "v_and_b32 v4, 0x7fff7fff, v4\n"
  "v_lshlrev_b32 v5, 16, v4\n"
  "v_mfma_f32_16x16x32_bf16 v[64:67], a[184:187], a[116:119], v[64:67]\n"
  "s_add_u32 m0, s72, 8704\n"
  "s_nop 0\n"
  "buffer_load_dwordx4 v221, s[48:51], s82 offen offset:256  lds\n"
  "v_mfma_f32_16x16x32_bf16 v[240:243], a[188:191], a[96:99], v[248:251]\n"
  "s_waitcnt vmcnt(12)\n"
  "ds_read_b128 a[20:23], v74 offset:2112\n"
  "ds_read_b128 a[16:19], v74 offset:2048\n"
  "ds_read_b128 a[12:15], v73 offset:64\n"
  "v_mfma_f32_16x16x32_bf16 v[240:243], a[192:195], a[100:103], v[240:243]\n"
  "ds_read_b128 a[8:11], v73 offset:0\n"
  "ds_read_b128 a[4:7], v72 offset:64\n"
  "ds_read_b128 a[0:3], v72 offset:0\n"
  "v_max_u32_e64 v4, v4, v5\n"
  "v_mfma_f32_16x16x32_bf16 v[240:243], a[196:199], a[104:107], v[240:243]\n"
  "s_nop 0\n"
  "v_max_u32_dpp v4, v4, v4 quad_perm:[1,0,3,2] row_mask:0xf bank_mask:0xf\n"
  "v_max3_f32 v227, v64, v65, v66\n"
  "v_mfma_f32_16x16x32_bf16 v[240:243], a[200:203], a[108:111], v[240:243]\n"
  "s_add_u32 m0, s72, 6720\n"
  "s_nop 0\n"
  "buffer_load_dwordx4 v221, s[48:51], s82 offen offset:3328  lds\n"
  "v_max_u32_dpp v4, v4, v4 quad_perm:[2,3,0,1] row_mask:0xf bank_mask:0xf\n"
  "v_add_u32 v4, -1, v4\n"
  "v_mfma_f32_16x16x32_bf16 v[240:243], a[204:207], a[112:115], v[240:243]\n"
  "s_nop 0\n"
  "v_min_u32_dpp v4, v4, v4 row_shr:4 row_mask:0xf bank_mask:0xf\n"
  "v_mfma_f32_16x16x32_bf16 v[240:243], a[208:211], a[116:119], v[240:243]\n"
  "s_waitcnt lgkmcnt(5)\n"
  "v_min_u32_dpp v4, v4, v4 row_shr:8 row_mask:0xf bank_mask:0xf\n"
  "v_mfma_f32_16x16x32_bf16 v[0:3], a[184:187], a[20:23], v[248:251]\n"
  "s_waitcnt lgkmcnt(4)\n"
  "v_min_u32_dpp v4, v4, v4 row_bcast:15 row_mask:0xa bank_mask:0xf\n"
  "v_mfma_f32_16x16x32_bf16 v[0:3], a[180:183], a[16:19], v[0:3]\n"
  "s_add_u32 m0, s72, 6592\n"
  "s_nop 0\n"
  "buffer_load_dwordx4 v221, s[48:51], s82 offen offset:128  lds\n"
  "s_waitcnt lgkmcnt(3)\n"
  "v_min_u32_dpp v4, v4, v4 row_bcast:31 row_mask:0xc bank_mask:0xf\n"
  "v_mfma_f32_16x16x32_bf16 v[0:3], a[176:179], a[12:15], v[0:3]\n"
  "s_waitcnt lgkmcnt(2)\n"
  "v_readlane_b32 s65, v4, 63\n"
  "v_mfma_f32_16x16x32_bf16 v[0:3], a[172:175], a[8:11], v[0:3]\n"
  "s_waitcnt lgkmcnt(1)\n"
  "v_max3_f32 v227, v227, v67, v240\n"
  "v_max3_f32 v227, v227, v241, v242\n"
  "v_mfma_f32_16x16x32_bf16 v[0:3], a[168:171], a[4:7], v[0:3]\n"
  "s_add_u32 s65, s65, 1\n"
  "s_lshr_b32 s65, s65, 23\n"
  "s_sub_i32 s65, 0x77, s65\n"
  "s_and_b32 s65, s65, -4\n"
  "s_max_i32 s65, s65, 0xffffffc4\n"
  "s_waitcnt lgkmcnt(0)\n"
  "v_cvt_f32_i32 v4, s65\n"
  "v_max_f32 v227, v227, v243\n"
  "v_mfma_f32_16x16x32_bf16 v[0:3], a[164:167], a[0:3], v[0:3]\n"
  "s_add_u32 m0, s72, 4608\n"
  "s_nop 0\n"
  "buffer_load_dwordx4 v221, s[48:51], s82 offen offset:3200  lds\n"
  "v_readfirstlane_b32 s64, v4\n"
  "v_mul_f32 v227, s74, v227\n"
  "s_nop 0\n"
  "v_add_f32 v227, s64, v227\n"
  "v_mfma_f32_16x16x32_bf16 v[4:7], a[208:211], a[20:23], v[248:251]\n"
  "s_add_u32 s65, s65, 0xab\n"
  "ds_bpermute_b32 v227, v234, v227\n"
  "v_mfma_f32_16x16x32_bf16 v[4:7], a[204:207], a[16:19], v[4:7]\n"
  "s_cmpk_gt_u32 s65, 0xd3\n"
  "s_cselect_b32 s65, 0, s65\n"
  "v_mfma_f32_16x16x32_bf16 v[4:7], a[200:203], a[12:15], v[4:7]\n"
  "s_lshl_b32 s65, s65, 23\n"
  "v_max3_f32 v223, v0, v1, v2\n"
  "v_max_f32 v223, v223, v3\n"
  "v_mfma_f32_16x16x32_bf16 v[4:7], a[196:199], a[8:11], v[4:7]\n"
  "s_add_u32 m0, s72, 4480\n"
  "s_nop 0\n"
  "buffer_load_dwordx4 v221, s[48:51], s82 offen offset:0  lds\n"
  "v_mfma_f32_16x16x32_bf16 v[4:7], a[192:195], a[4:7], v[4:7]\n"
  "s_mov_b32 s75, s65\n"
  "v_mul_f32 v223, s74, v223\n"
  "v_add_f32_e64 v223, s64, v223\n"
  "v_mfma_f32_16x16x32_bf16 v[4:7], a[188:191], a[0:3], v[4:7]\n"
  "ds_bpermute_b32 v223, v234, v223\n"
  "s_waitcnt lgkmcnt(1)\n"
  "v_fma_f32 v64, v64, s73, v227\n"
  "v_exp_f32 v64, v64\n"
  "v_fma_f32 v65, v65, s73, v227\n"
  "v_exp_f32 v65, v65\n"
  "v_fma_f32 v66, v66, s73, v227\n"
  "v_exp_f32 v66, v66\n"
  "v_fma_f32 v67, v67, s73, v227\n"
  "v_exp_f32_e64 v67, v67\n"
  "v_cvt_pk_bf16_f32 v64, v64, v65\n"
  "v_cvt_pk_bf16_f32 v65, v66, v67\n"
  "ds_write_b64 v231, v[64:65] offset:0\n"
  "s_waitcnt vmcnt(11) lgkmcnt(0)\n"
  "s_barrier\n"
  "ds_read_b128 a[212:215], v216 offset:12352\n"
  "ds_read_b128 a[216:219], v216 offset:12416\n"
  "ds_read_b128 a[220:223], v216 offset:13376\n"
  "ds_read_b128 a[224:227], v216 offset:13440\n"
  "ds_read_b128 a[228:231], v216 offset:14400\n"
  "ds_read_b128 a[232:235], v216 offset:14464\n"
  "ds_read_b64 a[156:157], v231 offset:0\n"
  "ds_read_b64 a[158:159], v232 offset:0\n"
  "s_waitcnt lgkmcnt(7)\n"
  "v_mfma_f32_16x16x32_bf16 v[64:67], a[212:215], a[96:99], v[248:251]\n"
  "s_add_u32 m0, s72, 2496\n"
  "s_nop 0\n"
  "buffer_load_dwordx4 v221, s[48:51], s82 offen offset:3072  lds\n"
  "ds_read_b64_tr_b16 a[144:145], v217 offset:14400\n"
  "ds_read_b64_tr_b16 a[146:147], v218 offset:14400\n"
  "s_waitcnt lgkmcnt(8)\n"
  "v_mfma_f32_16x16x32_bf16 v[64:67], a[216:219], a[100:103], v[64:67]\n"
  "ds_read_b64_tr_b16 a[148:149], v217 offset:14416\n"
  "ds_read_b64_tr_b16 a[150:151], v218 offset:14416\n"
  "s_waitcnt lgkmcnt(9)\n"
  "v_mfma_f32_16x16x32_bf16 v[64:67], a[220:223], a[104:107], v[64:67]\n"
  "ds_read_b64_tr_b16 v[76:77], v217 offset:13376\n"
  "ds_read_b64_tr_b16 v[78:79], v218 offset:13376\n"
  "s_waitcnt lgkmcnt(10)\n"
  "v_mfma_f32_16x16x32_bf16 v[64:67], a[224:227], a[108:111], v[64:67]\n"
  "ds_read_b64_tr_b16 v[80:81], v217 offset:13392\n"
  "ds_read_b64_tr_b16 v[82:83], v218 offset:13392\n"
  "s_waitcnt lgkmcnt(11)\n"
  "v_mfma_f32_16x16x32_bf16 v[64:67], a[228:231], a[112:115], v[64:67]\n"
  "s_add_u32 m0, s72, 11200\n"
  "s_nop 0\n"
  "buffer_load_dwordx4 v221, s[48:51], s83 offen offset:0  lds\n"
  "ds_read_b128 a[120:123], v235 offset:12352\n"
  "ds_read_b128 a[124:127], v235 offset:12416\n"
  "s_waitcnt lgkmcnt(12)\n"
  "v_mfma_f32_16x16x32_bf16 v[64:67], a[232:235], a[116:119], v[64:67]\n"
  "ds_read_b128 a[128:131], v235 offset:13376\n"
  "ds_read_b128 a[132:135], v235 offset:13440\n"
  "ds_read_b128 a[136:139], v235 offset:14400\n"
  "ds_read_b128 a[140:143], v235 offset:14464\n"
  "s_waitcnt lgkmcnt(12)\n"
  "v_mfma_f32_16x16x32_bf16 a[244:247], a[156:159], a[144:147], 1.0\n"
  "ds_read_b64_tr_b16 v[84:85], v219 offset:14400\n"
  "ds_read_b64_tr_b16 v[86:87], v220 offset:14400\n"
  "s_waitcnt lgkmcnt(12)\n"
  "v_mfma_f32_16x16x32_bf16 a[248:251], a[156:159], a[148:151], 1.0\n"
  "s_add_u32 m0, s72, 9216\n"
  "s_nop 0\n"
  "buffer_load_dwordx4 v221, s[48:51], s83 offen offset:3072  lds\n"
  "ds_read_b64_tr_b16 v[244:245], v219 offset:14416\n"
  "ds_read_b64_tr_b16 v[246:247], v220 offset:14416\n"
  "s_waitcnt lgkmcnt(12)\n"
  "v_mfma_f32_16x16x32_bf16 a[236:239], a[156:159], v[76:79], 1.0\n"
  "ds_read_b64_tr_b16 a[252:253], v219 offset:13376\n"
  "ds_read_b64_tr_b16 a[254:255], v220 offset:13376\n"
  "s_waitcnt lgkmcnt(12)\n"
  "v_fma_f32 v0, v0, s73, v223\n"
  "v_exp_f32 v0, v0\n"
  "v_fma_f32 v1, v1, s73, v223\n"
  "v_exp_f32 v1, v1\n"
  "v_fma_f32 v2, v2, s73, v223\n"
  "v_exp_f32 v2, v2\n"
  "v_fma_f32 v3, v3, s73, v223\n"
  "v_exp_f32_e64 v3, v3\n"
  "v_mfma_f32_16x16x32_bf16 a[240:243], a[156:159], v[80:83], 1.0\n"
  "ds_read_b64_tr_b16 v[236:237], v219 offset:13392\n"
  "ds_read_b64_tr_b16 v[238:239], v220 offset:13392\n"
  "v_cvt_pk_bf16_f32 v8, v0, v1\n"
  "v_cvt_pk_bf16_f32 v9, v2, v3\n"
  "v_mfma_f32_16x16x32_bf16 v[0:3], a[232:235], a[20:23], v[248:251]\n"
  "v_fma_f32 v4, v4, s73, v223\n"
  "v_fma_f32 v5, v5, s73, v223\n"
  "v_mfma_f32_16x16x32_bf16 v[0:3], a[228:231], a[16:19], v[0:3]\n"
  "s_add_u32 m0, s72, 13312\n"
  "s_nop 0\n"
  "buffer_load_dwordx4 v221, s[48:51], s83 offen offset:128  lds\n"
  "v_mfma_f32_16x16x32_bf16 v[0:3], a[224:227], a[12:15], v[0:3]\n"
  "v_exp_f32 v4, v4\n"
  "v_mfma_f32_16x16x32_bf16 v[0:3], a[220:223], a[8:11], v[0:3]\n"
  "v_exp_f32_e64 v5, v5\n"
  "v_mfma_f32_16x16x32_bf16 v[0:3], a[216:219], a[4:7], v[0:3]\n"
  "v_fma_f32 v6, v6, s73, v223\n"
  "v_exp_f32 v6, v6\n"
  "v_fma_f32 v7, v7, s73, v223\n"
  "v_exp_f32 v7, v7\n"
  "v_mfma_f32_16x16x32_bf16 v[0:3], a[212:215], a[0:3], v[0:3]\n"
  "s_add_u32 m0, s72, 11328\n"
  "s_nop 0\n"
  "buffer_load_dwordx4 v221, s[48:51], s83 offen offset:3200  lds\n"
  "s_waitcnt lgkmcnt(13)\n"
  "v_cvt_pk_bf16_f32 v10, v4, v5\n"
  "v_cvt_pk_bf16_f32 v11, v6, v7\n"
  "v_mfma_f32_16x16x32_bf16 v[4:7], a[120:123], a[0:3], v[248:251]\n"
  "s_waitcnt vmcnt(10)\n"
  "ds_read_b128 a[24:27], v222 offset:0\n"
  "ds_read_b128 a[28:31], v222 offset:64\n"
  "ds_read_b128 a[32:35], v222 offset:2240\n"
  "s_waitcnt lgkmcnt(15)\n"
  "v_mfma_f32_16x16x32_bf16 v[4:7], a[124:127], a[4:7], v[4:7]\n"
  "ds_read_b128 a[36:39], v222 offset:2304\n"
  "ds_read_b128 a[40:43], v72 offset:24704\n"
  "ds_read_b128 a[44:47], v72 offset:24768\n"
  "s_waitcnt lgkmcnt(15)\n"
  "v_mfma_f32_16x16x32_bf16 v[4:7], a[128:131], a[8:11], v[4:7]\n"
  "v_fma_f32 v64, v64, s73, v227\n"
  "v_fma_f32 v65, v65, s73, v227\n"
  "v_mfma_f32_16x16x32_bf16 v[4:7], a[132:135], a[12:15], v[4:7]\n"
  "s_add_u32 m0, s72, 15424\n"
  "s_nop 0\n"
  "buffer_load_dwordx4 v221, s[48:51], s83 offen offset:256  lds\n"
  "v_mfma_f32_16x16x32_bf16 v[4:7], a[136:139], a[16:19], v[4:7]\n"
  "s_waitcnt lgkmcnt(14)\n"
  "v_exp_f32 v64, v64\n"
  "v_mfma_f32_16x16x32_bf16 v[4:7], a[140:143], a[20:23], v[4:7]\n"
  "s_waitcnt lgkmcnt(5)\n"
  "v_exp_f32 v65, v65\n"
  "v_mfma_f32_16x16x32_bf16 v[16:19], a[164:167], a[24:27], v[248:251]\n"
  "s_waitcnt lgkmcnt(4)\n"
  "v_fma_f32 v66, v66, s73, v227\n"
  "v_fma_f32 v67, v67, s73, v227\n"
  "v_mfma_f32_16x16x32_bf16 v[16:19], a[168:171], a[28:31], v[16:19]\n"
  "s_add_u32 m0, s72, 13440\n"
  "s_nop 0\n"
  "buffer_load_dwordx4 v221, s[48:51], s83 offen offset:3328  lds\n"
  "s_waitcnt lgkmcnt(3)\n"
  "v_mfma_f32_16x16x32_bf16 v[16:19], a[172:175], a[32:35], v[16:19]\n"
  "s_waitcnt lgkmcnt(2)\n"
  "v_exp_f32 v66, v66\n"
  "v_mfma_f32_16x16x32_bf16 v[16:19], a[176:179], a[36:39], v[16:19]\n"
  "s_waitcnt lgkmcnt(1)\n"
  "v_exp_f32 v67, v67\n"
  "v_mfma_f32_16x16x32_bf16 v[16:19], a[180:183], a[40:43], v[16:19]\n"
  "s_waitcnt lgkmcnt(0)\n"
  "v_cvt_pk_bf16_f32 v64, v64, v65\n"
  "v_cvt_pk_bf16_f32 v65, v66, v67\n"
  "v_mfma_f32_16x16x32_bf16 v[16:19], a[184:187], a[44:47], v[16:19]\n"
  "s_mov_b32 m0, s66\n"
  "s_nop 0\n"
  "buffer_load_dwordx4 v221, s[40:43], 0 offen offset:0 lds\n"
  "v_mfma_f32_16x16x32_bf16 v[20:23], a[188:191], a[24:27], v[248:251]\n"
  "v_fma_f32 v0, v0, s73, v223\n"
  "v_fma_f32 v1, v1, s73, v223\n"
  "v_mfma_f32_16x16x32_bf16 v[20:23], a[192:195], a[28:31], v[20:23]\n"
  "v_exp_f32 v0, v0\n"
  "v_mfma_f32_16x16x32_bf16 v[20:23], a[196:199], a[32:35], v[20:23]\n"
  "v_max3_f32 v224, v16, v17, v18\n"
  "v_max_f32 v224, v224, v19\n"
  "v_mfma_f32_16x16x32_bf16 v[20:23], a[200:203], a[36:39], v[20:23]\n"
  "s_addk_i32 m0, 896\n"
  "s_nop 0\n"
  "buffer_load_dwordx4 v221, s[40:43], 0 offen offset:128 lds\n"
  "v_mfma_f32_16x16x32_bf16 v[20:23], a[204:207], a[40:43], v[20:23]\n"
  "v_mul_f32 v224, s74, v224\n"
  "v_add_f32 v224, s64, v224\n"
  "v_mfma_f32_16x16x32_bf16 v[20:23], a[208:211], a[44:47], v[20:23]\n"
  "ds_bpermute_b32 v224, v234, v224\n"
  "v_mfma_f32_16x16x32_bf16 v[104:107], v[8:11], a[144:147], 1.0\n"
  "v_exp_f32 v1, v1\n"
  "v_mfma_f32_16x16x32_bf16 v[108:111], v[8:11], a[148:151], 1.0\n"
  "s_addk_i32 m0, 896\n"
  "s_nop 0\n"
  "buffer_load_dwordx4 v221, s[40:43], 0 offen offset:256 lds\n"
  "s_add_u32 s40, s40, s31\n"
  "s_addc_u32 s41, s41, 0\n"
  "v_mfma_f32_16x16x32_bf16 v[112:115], v[8:11], v[84:87], 1.0\n"
  "v_fma_f32 v2, v2, s73, v223\n"
  "v_fma_f32 v3, v3, s73, v223\n"
  "v_mfma_f32_16x16x32_bf16 v[116:119], v[8:11], v[244:247], 1.0\n"
  "v_exp_f32 v2, v2\n"
  "v_mfma_f32_16x16x32_bf16 v[88:91], v[8:11], v[76:79], 1.0\n"
  "v_exp_f32_e64 v3, v3\n"
  "v_mfma_f32_16x16x32_bf16 v[92:95], v[8:11], v[80:83], 1.0\n"
  "s_mov_b32 m0, s58\n"
  "s_nop 0\n"
  "buffer_load_dwordx4 v221, s[32:35], 0 offen offset:0 lds\n"
  "v_mfma_f32_16x16x32_bf16 v[96:99], v[8:11], a[252:255], 1.0\n"
  "s_waitcnt lgkmcnt(0)\n"
  "v_fma_f32 v16, v16, s73, v224\n"
  "v_exp_f32 v16, v16\n"
  "v_fma_f32 v17, v17, s73, v224\n"
  "v_exp_f32 v17, v17\n"
  "v_fma_f32 v18, v18, s73, v224\n"
  "v_exp_f32 v18, v18\n"
  "v_fma_f32 v19, v19, s73, v224\n"
  "v_exp_f32_e64 v19, v19\n"
  "v_mfma_f32_16x16x32_bf16 v[100:103], v[8:11], v[236:239], 1.0\n"
  "v_cvt_pk_bf16_f32 v24, v16, v17\n"
  "v_cvt_pk_bf16_f32 v25, v18, v19\n"
  "v_mfma_f32_16x16x32_bf16 v[16:19], a[212:215], a[24:27], v[248:251]\n"
  "v_fma_f32 v20, v20, s73, v224\n"
  "v_fma_f32 v21, v21, s73, v224\n"
  "v_mfma_f32_16x16x32_bf16 v[16:19], a[216:219], a[28:31], v[16:19]\n"
  "s_addk_i32 m0, 1024\n"
  "s_nop 0\n"
  "buffer_load_dwordx4 v221, s[44:47], 0 offen offset:0 lds\n"
  "v_mfma_f32_16x16x32_bf16 v[16:19], a[220:223], a[32:35], v[16:19]\n"
  "v_exp_f32 v20, v20\n"
  "v_mfma_f32_16x16x32_bf16 v[16:19], a[224:227], a[36:39], v[16:19]\n"
  "v_exp_f32 v21, v21\n"
  "v_mfma_f32_16x16x32_bf16 v[16:19], a[228:231], a[40:43], v[16:19]\n"
  "v_fma_f32 v22, v22, s73, v224\n"
  "v_exp_f32 v22, v22\n"
  "v_fma_f32 v23, v23, s73, v224\n"
  "v_exp_f32 v23, v23\n"
  "v_mfma_f32_16x16x32_bf16 v[16:19], a[232:235], a[44:47], v[16:19]\n"
  "s_addk_i32 m0, 768\n"
  "s_nop 0\n"
  "buffer_load_dwordx4 v221, s[44:47], 0 offen offset:256 lds\n"
  "s_mov_b64 s[32:33], s[44:45]\n"
  "s_add_u32 s44, s44, s31\n"
  "s_addc_u32 s45, s45, 0\n"
  "v_cvt_pk_bf16_f32 v26, v20, v21\n"
  "v_cvt_pk_bf16_f32 v27, v22, v23\n"
  "v_mfma_f32_16x16x32_bf16 v[20:23], a[140:143], a[44:47], v[248:251]\n"
  "s_waitcnt vmcnt(12)\n"
  "ds_read_b128 a[68:71], v222 offset:9024\n"
  "ds_read_b128 a[64:67], v222 offset:8960\n"
  "ds_read_b128 a[60:63], v222 offset:6784\n"
  "v_mfma_f32_16x16x32_bf16 v[20:23], a[136:139], a[40:43], v[20:23]\n"
  "ds_read_b128 a[56:59], v222 offset:6720\n"
  "ds_read_b128 a[52:55], v222 offset:4544\n"
  "ds_read_b128 a[48:51], v222 offset:4480\n"
  "v_mfma_f32_16x16x32_bf16 v[20:23], a[132:135], a[36:39], v[20:23]\n"
  "v_fma_f32 v4, v4, s73, v223\n"
  "v_fma_f32 v5, v5, s73, v223\n"
  "v_mfma_f32_16x16x32_bf16 v[20:23], a[128:131], a[32:35], v[20:23]\n"
  "v_exp_f32 v4, v4\n"
  "v_mfma_f32_16x16x32_bf16 v[20:23], a[124:127], a[28:31], v[20:23]\n"
  "v_exp_f32 v5, v5\n"
  "v_mfma_f32_16x16x32_bf16 v[20:23], a[120:123], a[24:27], v[20:23]\n"
  "s_waitcnt lgkmcnt(5)\n"
  "v_fma_f32 v6, v6, s73, v223\n"
  "v_fma_f32 v7, v7, s73, v223\n"
  "v_mfma_f32_16x16x32_bf16 v[32:35], a[184:187], a[68:71], v[248:251]\n"
  "s_waitcnt lgkmcnt(4)\n"
  "v_exp_f32 v6, v6\n"
  "v_mfma_f32_16x16x32_bf16 v[32:35], a[180:183], a[64:67], v[32:35]\n"
  "s_waitcnt lgkmcnt(3)\n"
  "v_exp_f32 v7, v7\n"
  "v_mfma_f32_16x16x32_bf16 v[32:35], a[176:179], a[60:63], v[32:35]\n"
  "s_waitcnt lgkmcnt(2)\n"
  "v_fma_f32 v16, v16, s73, v224\n"
  "v_fma_f32 v17, v17, s73, v224\n"
  "v_mfma_f32_16x16x32_bf16 v[32:35], a[172:175], a[56:59], v[32:35]\n"
  "s_waitcnt lgkmcnt(1)\n"
  "v_exp_f32 v16, v16\n"
  "v_mfma_f32_16x16x32_bf16 v[32:35], a[168:171], a[52:55], v[32:35]\n"
  "s_waitcnt lgkmcnt(0)\n"
  "v_exp_f32 v17, v17\n"
  "v_mfma_f32_16x16x32_bf16 v[32:35], a[164:167], a[48:51], v[32:35]\n"
  "v_fma_f32 v18, v18, s73, v224\n"
  "v_fma_f32 v19, v19, s73, v224\n"
  "v_mfma_f32_16x16x32_bf16 v[36:39], a[208:211], a[68:71], v[248:251]\n"
  "v_exp_f32 v18, v18\n"
  "v_mfma_f32_16x16x32_bf16 v[36:39], a[204:207], a[64:67], v[36:39]\n"
  "v_exp_f32 v19, v19\n"
  "v_mfma_f32_16x16x32_bf16 v[36:39], a[200:203], a[60:63], v[36:39]\n"
  "v_max3_f32 v225, v32, v33, v34\n"
  "v_max_f32 v225, v225, v35\n"
  "v_mfma_f32_16x16x32_bf16 v[36:39], a[196:199], a[56:59], v[36:39]\n"
  "v_mul_f32 v225, s74, v225\n"
  "v_add_f32 v225, s64, v225\n"
  "v_mfma_f32_16x16x32_bf16 v[36:39], a[192:195], a[52:55], v[36:39]\n"
  "ds_bpermute_b32 v225, v234, v225\n"
  "v_mfma_f32_16x16x32_bf16 v[36:39], a[188:191], a[48:51], v[36:39]\n"
  "v_fma_f32 v20, v20, s73, v224\n"
  "v_fma_f32 v21, v21, s73, v224\n"
  "v_mfma_f32_16x16x32_bf16 v[136:139], v[24:27], a[144:147], 1.0\n"
  "v_exp_f32 v20, v20\n"
  "v_mfma_f32_16x16x32_bf16 v[140:143], v[24:27], a[148:151], 1.0\n"
  "v_exp_f32 v21, v21\n"
  "v_mfma_f32_16x16x32_bf16 v[144:147], v[24:27], v[84:87], 1.0\n"
  "v_fma_f32 v22, v22, s73, v224\n"
  "v_fma_f32 v23, v23, s73, v224\n"
  "v_mfma_f32_16x16x32_bf16 v[148:151], v[24:27], v[244:247], 1.0\n"
  "v_exp_f32 v22, v22\n"
  "v_mfma_f32_16x16x32_bf16 v[120:123], v[24:27], v[76:79], 1.0\n"
  "s_waitcnt lgkmcnt(0)\n"
  "v_fma_f32 v32, v32, s73, v225\n"
  "v_fma_f32 v33, v33, s73, v225\n"
  "v_mfma_f32_16x16x32_bf16 v[124:127], v[24:27], v[80:83], 1.0\n"
  "v_exp_f32 v32, v32\n"
  "v_mfma_f32_16x16x32_bf16 v[128:131], v[24:27], a[252:255], 1.0\n"
  "v_exp_f32 v33, v33\n"
  "v_fma_f32 v34, v34, s73, v225\n"
  "v_exp_f32 v34, v34\n"
  "v_fma_f32 v35, v35, s73, v225\n"
  "v_exp_f32 v35, v35\n"
  "v_mfma_f32_16x16x32_bf16 v[132:135], v[24:27], v[236:239], 1.0\n"
  "v_cvt_pk_bf16_f32 v40, v32, v33\n"
  "v_cvt_pk_bf16_f32 v41, v34, v35\n"
  "v_mfma_f32_16x16x32_bf16 v[32:35], a[232:235], a[68:71], v[248:251]\n"
  "v_fma_f32 v36, v36, s73, v225\n"
  "v_fma_f32 v37, v37, s73, v225\n"
  "v_mfma_f32_16x16x32_bf16 v[32:35], a[228:231], a[64:67], v[32:35]\n"
  "v_exp_f32 v36, v36\n"
  "v_mfma_f32_16x16x32_bf16 v[32:35], a[224:227], a[60:63], v[32:35]\n"
  "v_exp_f32 v37, v37\n"
  "v_mfma_f32_16x16x32_bf16 v[32:35], a[220:223], a[56:59], v[32:35]\n"
  "v_fma_f32 v38, v38, s73, v225\n"
  "v_fma_f32 v39, v39, s73, v225\n"
  "v_mfma_f32_16x16x32_bf16 v[32:35], a[216:219], a[52:55], v[32:35]\n"
  "v_exp_f32 v38, v38\n"
  "v_exp_f32 v39, v39\n"
  "v_mfma_f32_16x16x32_bf16 v[32:35], a[212:215], a[48:51], v[32:35]\n"
  "v_cvt_pk_bf16_f32 v42, v36, v37\n"
  "v_cvt_pk_bf16_f32 v43, v38, v39\n"
  "v_mfma_f32_16x16x32_bf16 v[36:39], a[120:123], a[48:51], v[248:251]\n"
  "s_waitcnt vmcnt(6)\n"
  "ds_read_b128 a[72:75], v222 offset:11200\n"
  "ds_read_b128 a[76:79], v222 offset:11264\n"
  "ds_read_b128 a[80:83], v222 offset:13440\n"
  "v_mfma_f32_16x16x32_bf16 v[36:39], a[124:127], a[52:55], v[36:39]\n"
  "ds_read_b128 a[84:87], v222 offset:13504\n"
  "ds_read_b128 a[88:91], v222 offset:15680\n"
  "ds_read_b128 a[92:95], v222 offset:15744\n"
  "v_mfma_f32_16x16x32_bf16 v[36:39], a[128:131], a[56:59], v[36:39]\n"
  "v_exp_f32 v23, v23\n"
  "v_mfma_f32_16x16x32_bf16 v[36:39], a[132:135], a[60:63], v[36:39]\n"
  "v_fma_f32 v32, v32, s73, v225\n"
  "v_fma_f32 v33, v33, s73, v225\n"
  "v_mfma_f32_16x16x32_bf16 v[36:39], a[136:139], a[64:67], v[36:39]\n"
  "v_exp_f32 v32, v32\n"
  "v_mfma_f32_16x16x32_bf16 v[36:39], a[140:143], a[68:71], v[36:39]\n"
  "s_waitcnt lgkmcnt(5)\n"
  "v_exp_f32 v33, v33\n"
  "v_mfma_f32_16x16x32_bf16 v[48:51], a[164:167], a[72:75], v[248:251]\n"
  "s_waitcnt lgkmcnt(4)\n"
  "v_fma_f32 v34, v34, s73, v225\n"
  "v_fma_f32 v35, v35, s73, v225\n"
  "v_mfma_f32_16x16x32_bf16 v[48:51], a[168:171], a[76:79], v[48:51]\n"
  "s_waitcnt lgkmcnt(3)\n"
  "v_exp_f32 v34, v34\n"
  "v_mfma_f32_16x16x32_bf16 v[48:51], a[172:175], a[80:83], v[48:51]\n"
  "s_waitcnt lgkmcnt(2)\n"
  "v_exp_f32 v35, v35\n"
  "v_mfma_f32_16x16x32_bf16 v[48:51], a[176:179], a[84:87], v[48:51]\n"
  "s_waitcnt lgkmcnt(1)\n"
  "v_fma_f32 v36, v36, s73, v225\n"
  "v_fma_f32 v37, v37, s73, v225\n"
  "v_mfma_f32_16x16x32_bf16 v[48:51], a[180:183], a[88:91], v[48:51]\n"
  "s_waitcnt lgkmcnt(0)\n"
  "v_exp_f32 v36, v36\n"
  "v_mfma_f32_16x16x32_bf16 v[48:51], a[184:187], a[92:95], v[48:51]\n"
  "v_exp_f32 v37, v37\n"
  "v_mfma_f32_16x16x32_bf16 v[52:55], a[188:191], a[72:75], v[248:251]\n"
  "v_fma_f32 v38, v38, s73, v225\n"
  "v_fma_f32 v39, v39, s73, v225\n"
  "v_mfma_f32_16x16x32_bf16 v[52:55], a[192:195], a[76:79], v[52:55]\n"
  "v_exp_f32 v38, v38\n"
  "v_mfma_f32_16x16x32_bf16 v[52:55], a[196:199], a[80:83], v[52:55]\n"
  "v_max3_f32 v226, v48, v49, v50\n"
  "v_max_f32 v226, v226, v51\n"
  "v_mfma_f32_16x16x32_bf16 v[52:55], a[200:203], a[84:87], v[52:55]\n"
  "v_mul_f32 v226, s74, v226\n"
  "v_add_f32 v226, s64, v226\n"
  "v_mfma_f32_16x16x32_bf16 v[52:55], a[204:207], a[88:91], v[52:55]\n"
  "ds_bpermute_b32 v226, v234, v226\n"
  "v_mfma_f32_16x16x32_bf16 v[52:55], a[208:211], a[92:95], v[52:55]\n"
  "v_exp_f32 v39, v39\n"
  "v_mfma_f32_16x16x32_bf16 v[168:171], v[40:43], a[144:147], 1.0\n"
  "ds_write_b64 v231, v[64:65] offset:2048\n"
  "v_mfma_f32_16x16x32_bf16 v[172:175], v[40:43], a[148:151], 1.0\n"
  "v_cvt_pk_bf16_f32 v12, v0, v1\n"
  "v_cvt_pk_bf16_f32 v13, v2, v3\n"
  "v_mfma_f32_16x16x32_bf16 v[176:179], v[40:43], v[84:87], 1.0\n"
  "v_cvt_pk_bf16_f32 v14, v4, v5\n"
  "v_cvt_pk_bf16_f32 v15, v6, v7\n"
  "v_mfma_f32_16x16x32_bf16 v[180:183], v[40:43], v[244:247], 1.0\n"
  "v_cvt_pk_bf16_f32 v28, v16, v17\n"
  "v_cvt_pk_bf16_f32 v29, v18, v19\n"
  "v_mfma_f32_16x16x32_bf16 v[152:155], v[40:43], v[76:79], 1.0\n"
  "s_waitcnt lgkmcnt(1)\n"
  "v_fma_f32 v48, v48, s73, v226\n"
  "v_fma_f32 v49, v49, s73, v226\n"
  "v_mfma_f32_16x16x32_bf16 v[156:159], v[40:43], v[80:83], 1.0\n"
  "v_exp_f32 v48, v48\n"
  "v_mfma_f32_16x16x32_bf16 v[160:163], v[40:43], a[252:255], 1.0\n"
  "v_exp_f32 v49, v49\n"
  "v_fma_f32 v50, v50, s73, v226\n"
  "v_exp_f32 v50, v50\n"
  "v_fma_f32 v51, v51, s73, v226\n"
  "v_exp_f32 v51, v51\n"
  "v_mfma_f32_16x16x32_bf16 v[164:167], v[40:43], v[236:239], 1.0\n"
  "v_cvt_pk_bf16_f32 v56, v48, v49\n"
  "v_cvt_pk_bf16_f32 v57, v50, v51\n"
  "v_mfma_f32_16x16x32_bf16 v[48:51], a[212:215], a[72:75], v[248:251]\n"
  "v_fma_f32 v52, v52, s73, v226\n"
  "v_fma_f32 v53, v53, s73, v226\n"
  "v_mfma_f32_16x16x32_bf16 v[48:51], a[216:219], a[76:79], v[48:51]\n"
  "v_exp_f32 v52, v52\n"
  "v_mfma_f32_16x16x32_bf16 v[48:51], a[220:223], a[80:83], v[48:51]\n"
  "v_exp_f32 v53, v53\n"
  "v_mfma_f32_16x16x32_bf16 v[48:51], a[224:227], a[84:87], v[48:51]\n"
  "v_fma_f32 v54, v54, s73, v226\n"
  "v_fma_f32 v55, v55, s73, v226\n"
  "v_mfma_f32_16x16x32_bf16 v[48:51], a[228:231], a[88:91], v[48:51]\n"
  "v_exp_f32 v54, v54\n"
  "v_exp_f32_e64 v55, v55\n"
  "v_mfma_f32_16x16x32_bf16 v[48:51], a[232:235], a[92:95], v[48:51]\n"
  "v_cvt_pk_bf16_f32 v58, v52, v53\n"
  "v_cvt_pk_bf16_f32 v59, v54, v55\n"
  "v_mfma_f32_16x16x32_bf16 v[52:55], a[140:143], a[92:95], v[248:251]\n"
  "v_cvt_pk_bf16_f32 v30, v20, v21\n"
  "v_cvt_pk_bf16_f32 v31, v22, v23\n"
  "v_mfma_f32_16x16x32_bf16 v[52:55], a[136:139], a[88:91], v[52:55]\n"
  "v_cvt_pk_bf16_f32 v44, v32, v33\n"
  "v_cvt_pk_bf16_f32 v45, v34, v35\n"
  "v_mfma_f32_16x16x32_bf16 v[52:55], a[132:135], a[84:87], v[52:55]\n"
  "v_fma_f32 v48, v48, s73, v226\n"
  "v_fma_f32 v49, v49, s73, v226\n"
  "v_mfma_f32_16x16x32_bf16 v[52:55], a[128:131], a[80:83], v[52:55]\n"
  "v_exp_f32 v48, v48\n"
  "v_mfma_f32_16x16x32_bf16 v[52:55], a[124:127], a[76:79], v[52:55]\n"
  "v_exp_f32 v49, v49\n"
  "v_mfma_f32_16x16x32_bf16 v[52:55], a[120:123], a[72:75], v[52:55]\n"
  "v_fma_f32 v50, v50, s73, v226\n"
  "v_fma_f32 v51, v51, s73, v226\n"
  "v_mfma_f32_16x16x32_bf16 v[200:203], v[56:59], a[144:147], 1.0\n"
  "v_exp_f32 v50, v50\n"
  "v_mfma_f32_16x16x32_bf16 v[204:207], v[56:59], a[148:151], 1.0\n"
  "v_exp_f32 v51, v51\n"
  "v_mfma_f32_16x16x32_bf16 v[208:211], v[56:59], v[84:87], 1.0\n"
  "v_fma_f32 v52, v52, s73, v226\n"
  "v_fma_f32 v53, v53, s73, v226\n"
  "v_mfma_f32_16x16x32_bf16 v[212:215], v[56:59], v[244:247], 1.0\n"
  "v_exp_f32 v52, v52\n"
  "v_mfma_f32_16x16x32_bf16 v[184:187], v[56:59], v[76:79], 1.0\n"
  "v_exp_f32 v53, v53\n"
  "v_mfma_f32_16x16x32_bf16 v[188:191], v[56:59], v[80:83], 1.0\n"
  "v_fma_f32 v54, v54, s73, v226\n"
  "v_fma_f32 v55, v55, s73, v226\n"
  "v_mfma_f32_16x16x32_bf16 v[192:195], v[56:59], a[252:255], 1.0\n"
  "v_exp_f32_e64 v54, v54\n"
  "v_mfma_f32_16x16x32_bf16 v[196:199], v[56:59], v[236:239], 1.0\n"
  "v_exp_f32_e64 v55, v55\n"
  "v_cvt_pk_bf16_f32 v46, v36, v37\n"
  "v_cvt_pk_bf16_f32 v47, v38, v39\n"
  "v_cvt_pk_bf16_f32 v60, v48, v49\n"
  "v_cvt_pk_bf16_f32 v61, v50, v51\n"
  "v_cvt_pk_bf16_f32 v62, v52, v53\n"
  "v_cvt_pk_bf16_f32 v63, v54, v55\n"
  "s_add_i32 s76, s76, 1\n"
  "s_mov_b32 m0, s67\n"
  "s_waitcnt vmcnt(0) lgkmcnt(0)\n"
  "s_barrier\n"
  "ds_read_b128 a[124:127], v216 offset:0\n"
  "ds_read_b64 a[160:161], v231 offset:2048\n"
  "ds_read_b64 a[162:163], v232 offset:2048\n"
  "buffer_load_dwordx4 v221, s[40:43], 0 offen offset:0 lds\n"
  "ds_read_b128 a[120:123], v216 offset:64\n"
  "s_addk_i32 m0, 896\n"
  "ds_read_b128 a[140:143], v216 offset:1024\n"
  "ds_read_b128 a[136:139], v216 offset:1088\n"
  "buffer_load_dwordx4 v221, s[40:43], 0 offen offset:128 lds\n"
  "s_addk_i32 m0, 896\n"
  "ds_read_b128 a[144:147], v216 offset:2048\n"
  "ds_read_b128 a[148:151], v216 offset:2112\n"
  "buffer_load_dwordx4 v221, s[40:43], 0 offen offset:256 lds\n"
  "s_waitcnt lgkmcnt(4)\n"
  "v_mfma_f32_16x16x32_bf16 v[64:67], a[124:127], a[96:99], v[248:251]\n"
  "s_add_u32 m0, s59, -128\n"
  "v_mfma_f32_16x16x32_bf16 v[64:67], a[120:123], a[100:103], v[64:67]\n"
  "s_waitcnt lgkmcnt(2)\n"
  "v_mfma_f32_16x16x32_bf16 v[64:67], a[140:143], a[104:107], v[64:67]\n"
  "buffer_load_dwordx4 v221, s[32:35], 0 offen offset:128 lds\n"
  "v_mfma_f32_16x16x32_bf16 v[64:67], a[136:139], a[108:111], v[64:67]\n"
  "s_waitcnt lgkmcnt(0)\n"
  "v_mfma_f32_16x16x32_bf16 v[64:67], a[144:147], a[112:115], v[64:67]\n"
  "s_addk_i32 m0, 1024\n"
  "v_mfma_f32_16x16x32_bf16 v[64:67], a[148:151], a[116:119], v[64:67]\n"
  "s_mov_b64 s[32:33], s[44:45]\n"
  "v_mfma_f32_16x16x32_bf16 v[0:3], a[148:151], a[20:23], v[248:251]\n"
  "buffer_load_dwordx4 v221, s[44:47], 0 offen offset:128 lds\n"
  "v_mfma_f32_16x16x32_bf16 v[0:3], a[144:147], a[16:19], v[0:3]\n"
  "s_addk_i32 m0, 896\n"
  "v_mfma_f32_16x16x32_bf16 v[0:3], a[136:139], a[12:15], v[0:3]\n"
  "s_add_u32 s40, s40, s31\n"
  "v_mfma_f32_16x16x32_bf16 v[0:3], a[140:143], a[8:11], v[0:3]\n"
  "s_addc_u32 s41, s41, 0\n"
  "v_mfma_f32_16x16x32_bf16 v[0:3], a[120:123], a[4:7], v[0:3]\n"
  "buffer_load_dwordx4 v221, s[44:47], 0 offen offset:256 lds\n"
  "v_mfma_f32_16x16x32_bf16 v[0:3], a[124:127], a[0:3], v[0:3]\n"
  "s_add_u32 s44, s44, s31\n"
  "v_mfma_f32_16x16x32_bf16 v[16:19], a[124:127], a[24:27], v[248:251]\n"
  "s_addc_u32 s45, s45, 0\n"
  "v_mfma_f32_16x16x32_bf16 v[16:19], a[120:123], a[28:31], v[16:19]\n"
  "v_mfma_f32_16x16x32_bf16 v[16:19], a[140:143], a[32:35], v[16:19]\n"
  "ds_read_b128 a[132:135], v235 offset:0\n"
  "v_mfma_f32_16x16x32_bf16 v[16:19], a[136:139], a[36:39], v[16:19]\n"
  "ds_read_b128 a[128:131], v235 offset:64\n"
  "v_mfma_f32_16x16x32_bf16 v[16:19], a[144:147], a[40:43], v[16:19]\n"
  "v_mfma_f32_16x16x32_bf16 v[16:19], a[148:151], a[44:47], v[16:19]\n"
  "v_mfma_f32_16x16x32_bf16 v[32:35], a[148:151], a[68:71], v[248:251]\n"
  "v_mfma_f32_16x16x32_bf16 v[32:35], a[144:147], a[64:67], v[32:35]\n"
  "v_mfma_f32_16x16x32_bf16 v[32:35], a[136:139], a[60:63], v[32:35]\n"
  "v_mfma_f32_16x16x32_bf16 v[32:35], a[140:143], a[56:59], v[32:35]\n"
  "v_mfma_f32_16x16x32_bf16 v[32:35], a[120:123], a[52:55], v[32:35]\n"
  "v_mfma_f32_16x16x32_bf16 v[32:35], a[124:127], a[48:51], v[32:35]\n"
  "v_mfma_f32_16x16x32_bf16 v[48:51], a[124:127], a[72:75], v[248:251]\n"
  "ds_read_b128 a[124:127], v235 offset:1024\n"
  "v_mfma_f32_16x16x32_bf16 v[48:51], a[120:123], a[76:79], v[48:51]\n"
  "ds_read_b128 a[120:123], v235 offset:1088\n"
  "v_mfma_f32_16x16x32_bf16 v[48:51], a[140:143], a[80:83], v[48:51]\n"
  "ds_read_b128 a[140:143], v235 offset:2048\n"
  "v_mfma_f32_16x16x32_bf16 v[48:51], a[136:139], a[84:87], v[48:51]\n"
  "ds_read_b128 a[136:139], v235 offset:2112\n"
  "v_mfma_f32_16x16x32_bf16 v[48:51], a[144:147], a[88:91], v[48:51]\n"
  "v_mfma_f32_16x16x32_bf16 v[48:51], a[148:151], a[92:95], v[48:51]\n"
  "s_waitcnt lgkmcnt(4)\n"
  "v_mfma_f32_16x16x32_bf16 v[4:7], a[132:135], a[0:3], v[248:251]\n"
  "v_mfma_f32_16x16x32_bf16 v[4:7], a[128:131], a[4:7], v[4:7]\n"
  "s_waitcnt lgkmcnt(2)\n"
  "v_mfma_f32_16x16x32_bf16 v[4:7], a[124:127], a[8:11], v[4:7]\n"
  "v_mfma_f32_16x16x32_bf16 v[4:7], a[120:123], a[12:15], v[4:7]\n"
  "s_waitcnt lgkmcnt(0)\n"
  "v_mfma_f32_16x16x32_bf16 v[4:7], a[140:143], a[16:19], v[4:7]\n"
  "v_mfma_f32_16x16x32_bf16 v[4:7], a[136:139], a[20:23], v[4:7]\n"
  "v_mfma_f32_16x16x32_bf16 v[20:23], a[136:139], a[44:47], v[248:251]\n"
  "v_mfma_f32_16x16x32_bf16 v[20:23], a[140:143], a[40:43], v[20:23]\n"
  "v_mfma_f32_16x16x32_bf16 v[20:23], a[120:123], a[36:39], v[20:23]\n"
  "ds_read_b64_tr_b16 a[144:145], v217 offset:2048\n"
  "v_mfma_f32_16x16x32_bf16 v[20:23], a[124:127], a[32:35], v[20:23]\n"
  "ds_read_b64_tr_b16 a[146:147], v218 offset:2048\n"
  "v_mfma_f32_16x16x32_bf16 v[20:23], a[128:131], a[28:31], v[20:23]\n"
  "ds_read_b64_tr_b16 a[148:149], v217 offset:2064\n"
  "v_mfma_f32_16x16x32_bf16 v[20:23], a[132:135], a[24:27], v[20:23]\n"
  "ds_read_b64_tr_b16 a[150:151], v218 offset:2064\n"
  "v_mfma_f32_16x16x32_bf16 v[36:39], a[132:135], a[48:51], v[248:251]\n"
  "v_mfma_f32_16x16x32_bf16 v[36:39], a[128:131], a[52:55], v[36:39]\n"
  "v_mfma_f32_16x16x32_bf16 v[36:39], a[124:127], a[56:59], v[36:39]\n"
  "v_mfma_f32_16x16x32_bf16 v[36:39], a[120:123], a[60:63], v[36:39]\n"
  "v_mfma_f32_16x16x32_bf16 v[36:39], a[140:143], a[64:67], v[36:39]\n"
  "v_mfma_f32_16x16x32_bf16 v[36:39], a[136:139], a[68:71], v[36:39]\n"
  "v_mfma_f32_16x16x32_bf16 v[52:55], a[136:139], a[92:95], v[248:251]\n"
  "ds_read_b64_tr_b16 a[136:137], v219 offset:2048\n"
  "v_mfma_f32_16x16x32_bf16 v[52:55], a[140:143], a[88:91], v[52:55]\n"
  "ds_read_b64_tr_b16 a[138:139], v220 offset:2048\n"
  "v_mfma_f32_16x16x32_bf16 v[52:55], a[120:123], a[84:87], v[52:55]\n"
  "ds_read_b64_tr_b16 a[140:141], v219 offset:2064\n"
  "v_mfma_f32_16x16x32_bf16 v[52:55], a[124:127], a[80:83], v[52:55]\n"
  "ds_read_b64_tr_b16 a[142:143], v220 offset:2064\n"
  "v_fma_f32 v64, v64, s73, v227\n"
  "v_mfma_f32_16x16x32_bf16 v[52:55], a[128:131], a[76:79], v[52:55]\n"
  "v_exp_f32 v64, v64\n"
  "v_mfma_f32_16x16x32_bf16 v[52:55], a[132:135], a[72:75], v[52:55]\n"
  "s_waitcnt lgkmcnt(4)\n"
  "v_fma_f32 v65, v65, s73, v227\n"
  "v_fma_f32 v66, v66, s73, v227\n"
  "v_mfma_f32_16x16x32_bf16 v[104:107], v[12:15], a[144:147], v[104:107]\n"
  "v_exp_f32 v65, v65\n"
  "v_mfma_f32_16x16x32_bf16 v[108:111], v[12:15], a[148:151], v[108:111]\n"
  "v_mfma_f32_16x16x32_bf16 v[136:139], v[28:31], a[144:147], v[136:139]\n"
  "ds_read_b64_tr_b16 a[120:121], v217 offset:0\n"
  "v_mfma_f32_16x16x32_bf16 v[140:143], v[28:31], a[148:151], v[140:143]\n"
  "ds_read_b64_tr_b16 a[122:123], v218 offset:0\n"
  "v_mfma_f32_16x16x32_bf16 v[168:171], v[44:47], a[144:147], v[168:171]\n"
  "ds_read_b64_tr_b16 a[124:125], v217 offset:1024\n"
  "v_mfma_f32_16x16x32_bf16 v[172:175], v[44:47], a[148:151], v[172:175]\n"
  "ds_read_b64_tr_b16 a[126:127], v218 offset:1024\n"
  "v_mfma_f32_16x16x32_bf16 v[200:203], v[60:63], a[144:147], v[200:203]\n"
  "v_mfma_f32_16x16x32_bf16 v[204:207], v[60:63], a[148:151], v[204:207]\n"
  "v_exp_f32_e64 v66, v66\n"
  "v_mfma_f32_16x16x32_bf16 a[244:247], a[160:163], a[144:147], a[244:247]\n"
  "v_mfma_f32_16x16x32_bf16 a[248:251], a[160:163], a[148:151], a[248:251]\n"
  "s_waitcnt lgkmcnt(4)\n"
  "v_mfma_f32_16x16x32_bf16 v[208:211], v[60:63], a[136:139], v[208:211]\n"
  "v_mfma_f32_16x16x32_bf16 v[212:215], v[60:63], a[140:143], v[212:215]\n"
  "v_mfma_f32_16x16x32_bf16 v[176:179], v[44:47], a[136:139], v[176:179]\n"
  "ds_read_b64_tr_b16 a[128:129], v217 offset:16\n"
  "v_mfma_f32_16x16x32_bf16 v[180:183], v[44:47], a[140:143], v[180:183]\n"
  "ds_read_b64_tr_b16 a[130:131], v218 offset:16\n"
  "v_mfma_f32_16x16x32_bf16 v[144:147], v[28:31], a[136:139], v[144:147]\n"
  "ds_read_b64_tr_b16 a[132:133], v217 offset:1040\n"
  "v_mfma_f32_16x16x32_bf16 v[148:151], v[28:31], a[140:143], v[148:151]\n"
  "ds_read_b64_tr_b16 a[134:135], v218 offset:1040\n"
  "v_mfma_f32_16x16x32_bf16 v[112:115], v[12:15], a[136:139], v[112:115]\n"
  "v_mfma_f32_16x16x32_bf16 v[116:119], v[12:15], a[140:143], v[116:119]\n"
  "s_waitcnt lgkmcnt(4)\n"
  "v_mfma_f32_16x16x32_bf16 a[164:167], v[8:11], a[120:123], 1.0\n"
  "v_fma_f32 v67, v67, s73, v227\n"
  "v_mfma_f32_16x16x32_bf16 a[164:167], v[12:15], a[124:127], a[164:167]\n"
  "v_exp_f32 v67, v67\n"
  "v_mfma_f32_16x16x32_bf16 a[180:183], v[24:27], a[120:123], 1.0\n"
  "v_mfma_f32_16x16x32_bf16 a[180:183], v[28:31], a[124:127], a[180:183]\n"
  "v_cvt_pk_bf16_f32 v64, v64, v65\n"
  "v_cvt_pk_bf16_f32 v65, v66, v67\n"
  "v_mfma_f32_16x16x32_bf16 a[196:199], v[40:43], a[120:123], 1.0\n"
  "s_mov_b32 m0, s68\n"
  "ds_read_b64_tr_b16 a[136:137], v219 offset:0\n"
  "v_fma_f32 v0, v0, s73, v223\n"
  "v_mfma_f32_16x16x32_bf16 a[196:199], v[44:47], a[124:127], a[196:199]\n"
  "ds_read_b64_tr_b16 a[138:139], v220 offset:0\n"
  "v_fma_f32 v1, v1, s73, v223\n"
  "v_mfma_f32_16x16x32_bf16 a[212:215], v[56:59], a[120:123], 1.0\n"
  "ds_read_b64_tr_b16 a[140:141], v219 offset:1024\n"
  "v_fma_f32 v2, v2, s73, v223\n"
  "v_mfma_f32_16x16x32_bf16 a[212:215], v[60:63], a[124:127], a[212:215]\n"
  "ds_read_b64_tr_b16 a[142:143], v220 offset:1024\n"
  "v_fma_f32 v3, v3, s73, v223\n"
  "v_mfma_f32_16x16x32_bf16 a[228:231], a[156:159], a[120:123], 1.0\n"
  "buffer_load_dwordx4 v221, s[40:43], 0 offen offset:0 lds\n"
  "v_mfma_f32_16x16x32_bf16 a[228:231], a[160:163], a[124:127], a[228:231]\n"
  "s_waitcnt lgkmcnt(4)\n"
  "v_mfma_f32_16x16x32_bf16 a[232:235], a[156:159], a[128:131], 1.0\n"
  "s_addk_i32 m0, 896\n"
  "v_mfma_f32_16x16x32_bf16 a[232:235], a[160:163], a[132:135], a[232:235]\n"
  "ds_write_b64 v231, v[64:65] offset:0\n"
  "v_mfma_f32_16x16x32_bf16 a[216:219], v[56:59], a[128:131], 1.0\n"
  "buffer_load_dwordx4 v221, s[40:43], 0 offen offset:128 lds\n"
  "v_fma_f32 v16, v16, s73, v224\n"
  "v_mfma_f32_16x16x32_bf16 a[216:219], v[60:63], a[132:135], a[216:219]\n"
  "s_addk_i32 m0, 896\n"
  "ds_read_b64_tr_b16 a[120:121], v219 offset:16\n"
  "v_fma_f32 v17, v17, s73, v224\n"
  "v_mfma_f32_16x16x32_bf16 a[200:203], v[40:43], a[128:131], 1.0\n"
  "ds_read_b64_tr_b16 a[122:123], v220 offset:16\n"
  "v_fma_f32 v18, v18, s73, v224\n"
  "v_mfma_f32_16x16x32_bf16 a[200:203], v[44:47], a[132:135], a[200:203]\n"
  "s_nop 0\n"
  "ds_read_b64_tr_b16 a[124:125], v219 offset:1040\n"
  "v_fma_f32 v19, v19, s73, v224\n"
  "v_mfma_f32_16x16x32_bf16 a[184:187], v[24:27], a[128:131], 1.0\n"
  "buffer_load_dwordx4 v221, s[40:43], 0 offen offset:256 lds\n"
  "v_fma_f32 v32, v32, s73, v225\n"
  "v_mfma_f32_16x16x32_bf16 a[184:187], v[28:31], a[132:135], a[184:187]\n"
  "s_mov_b32 m0, s60\n"
  "ds_read_b64_tr_b16 a[126:127], v220 offset:1040\n"
  "v_fma_f32 v33, v33, s73, v225\n"
  "v_mfma_f32_16x16x32_bf16 a[168:171], v[8:11], a[128:131], 1.0\n"
  "s_add_u32 s28, s96, 184448\n"
  "v_exp_f32 v0, v0\n"
  "v_mfma_f32_16x16x32_bf16 a[168:171], v[12:15], a[132:135], a[168:171]\n"
  "s_waitcnt lgkmcnt(5)\n"
  "v_exp_f32 v1, v1\n"
  "v_mfma_f32_16x16x32_bf16 a[172:175], v[8:11], a[136:139], 1.0\n"
  "buffer_load_dwordx4 v221, s[32:35], 0 offen offset:0 lds\n"
  "v_add_f32 v252, v0, v1\n"
  "v_mfma_f32_16x16x32_bf16 a[172:175], v[12:15], a[140:143], a[172:175]\n"
  "s_addk_i32 m0, 1024\n"
  "v_exp_f32 v2, v2\n"
  "v_mfma_f32_16x16x32_bf16 a[188:191], v[24:27], a[136:139], 1.0\n"
  "s_add_u32 s40, s40, s31\n"
  "v_exp_f32 v3, v3\n"
  "v_mfma_f32_16x16x32_bf16 a[188:191], v[28:31], a[140:143], a[188:191]\n"
  "s_addc_u32 s41, s41, 0\n"
  "v_exp_f32 v16, v16\n"
  "v_mfma_f32_16x16x32_bf16 a[204:207], v[40:43], a[136:139], 1.0\n"
  "buffer_load_dwordx4 v221, s[44:47], 0 offen offset:0 lds\n"
  "v_add_f32 v252, v252, v2\n"
  "v_mfma_f32_16x16x32_bf16 a[204:207], v[44:47], a[140:143], a[204:207]\n"
  "s_addk_i32 m0, 768\n"
  "v_exp_f32 v17, v17\n"
  "v_mfma_f32_16x16x32_bf16 a[220:223], v[56:59], a[136:139], 1.0\n"
  "s_mov_b64 s[32:33], s[44:45]\n"
  "v_exp_f32 v18, v18\n"
  "v_mfma_f32_16x16x32_bf16 a[220:223], v[60:63], a[140:143], a[220:223]\n"
  "s_waitcnt lgkmcnt(0)\n"
  "v_exp_f32 v19, v19\n"
  "v_mfma_f32_16x16x32_bf16 a[224:227], v[56:59], a[120:123], 1.0\n"
  "buffer_load_dwordx4 v221, s[44:47], 0 offen offset:256 lds\n"
  "v_add_f32_e64 v252, v252, v3\n"
  "v_mfma_f32_16x16x32_bf16 a[224:227], v[60:63], a[124:127], a[224:227]\n"
  "s_add_u32 s44, s44, s31\n"
  "v_exp_f32 v32, v32\n"
  "v_mfma_f32_16x16x32_bf16 a[208:211], v[40:43], a[120:123], 1.0\n"
  "s_addc_u32 s45, s45, 0\n"
  "v_exp_f32 v33, v33\n"
  "v_mfma_f32_16x16x32_bf16 a[208:211], v[44:47], a[124:127], a[208:211]\n"
  "s_cmp_eq_u32 s95, 0\n"
  "s_cselect_b32 s95, 3, s95\n"
  "s_subb_u32 s100, s100, -11\n"
  "v_add_f32 v253, v16, v17\n"
  "v_add_f32 v253, v253, v18\n"
  "v_mfma_f32_16x16x32_bf16 a[192:195], v[24:27], a[120:123], 1.0\n"
  "s_sub_u32 s95, s95, 1\n"
  "v_add_f32 v253, v253, v19\n"
  "v_fma_f32 v34, v34, s73, v225\n"
  "v_mfma_f32_16x16x32_bf16 a[192:195], v[28:31], a[124:127], a[192:195]\n"
  "s_waitcnt vmcnt(6)\n"
  "s_barrier\n"
  "ds_read_b128 a[128:131], v216 offset:12352\n"
  "v_mfma_f32_16x16x32_bf16 a[176:179], v[8:11], a[120:123], 1.0\n"
  "s_lshr_b32 s79, s100, 0\n"
  "ds_read_b128 a[132:135], v216 offset:12416\n"
  "v_fma_f32 v35, v35, s73, v225\n"
  "v_mfma_f32_16x16x32_bf16 a[176:179], v[12:15], a[124:127], a[176:179]\n"
  "s_mul_i32 s79, s79, 7\n"
  "ds_read_b128 a[120:123], v216 offset:13376\n"
  "v_add_f32 v254, v32, v33\n"
  "v_mfma_f32_16x16x32_bf16 v[68:71], v[8:11], a[152:155], 0\n"
  "s_add_u32 s79, s79, s100\n"
  "ds_read_b128 a[124:127], v216 offset:13440\n"
  "v_cvt_pk_bf16_f32 v8, v0, v1\n"
  "v_mfma_f32_16x16x32_bf16 v[68:71], v[12:15], a[152:155], v[68:71]\n"
  "s_add_u32 s79, s79, s101\n"
  "ds_read_b128 a[136:139], v216 offset:14400\n"
  "v_cvt_pk_bf16_f32 v9, v2, v3\n"
  "v_mfma_f32_16x16x32_bf16 v[72:75], v[24:27], a[152:155], 0\n"
  "ds_read_b128 a[140:143], v216 offset:14464\n"
  "v_cvt_pk_bf16_f32 v24, v16, v17\n"
  "v_mfma_f32_16x16x32_bf16 v[72:75], v[28:31], a[152:155], v[72:75]\n"
  "s_mov_b32 m0, s66\n"
  "v_exp_f32 v34, v34\n"
  "v_mfma_f32_16x16x32_bf16 v[76:79], v[40:43], a[152:155], 0\n"
  "s_min_u32 s79, s79, 0x1ff\n"
  "v_exp_f32 v35, v35\n"
  "v_mfma_f32_16x16x32_bf16 v[76:79], v[44:47], a[152:155], v[76:79]\n"
  "s_mul_i32 s96, s79, 196608\n"
  "v_cvt_pk_bf16_f32 v25, v18, v19\n"
  "v_add_f32 v254, v254, v34\n"
  "v_mfma_f32_16x16x32_bf16 v[80:83], v[56:59], a[152:155], 0\n"
  "s_mul_i32 s92, s79, 325632\n"
  "v_add_f32 v254, v254, v35\n"
  "v_cvt_pk_bf16_f32 v40, v32, v33\n"
  "v_mfma_f32_16x16x32_bf16 v[80:83], v[60:63], a[152:155], v[80:83]\n"
  "buffer_load_dwordx4 v221, s[40:43], 0 offen offset:0 lds\n"
  "v_mfma_f32_16x16x32_bf16 v[84:87], a[156:159], a[152:155], 0\n"
  "s_add_u32 s96, s96, s39\n"
  "ds_read_b64 a[156:157], v231 offset:0\n"
  "v_cvt_pk_bf16_f32 v41, v34, v35\n"
  "v_mfma_f32_16x16x32_bf16 v[84:87], a[160:163], a[152:155], v[84:87]\n"
  "s_branch L_CUT_%=\n"
  ".p2align 5\n"
  "L_BIG_%=:\n"
  "v_mfma_f32_16x16x32_bf16 v[16:19], a[120:123], a[24:27], v[248:251]\n"
  "s_mov_b32 m0, s60\n"
  "v_fma_f32 v64, v64, s73, v227\n"
  "v_fma_f32 v65, v65, s73, v227\n"
  "v_mfma_f32_16x16x32_bf16 v[16:19], a[124:127], a[28:31], v[16:19]\n"
  "buffer_load_dwordx4 v221, s[32:35], 0 offen offset:0 lds\n"
  "v_fma_f32 v66, v66, s73, v227\n"
  "v_mfma_f32_16x16x32_bf16 v[16:19], a[128:131], a[32:35], v[16:19]\n"
  "s_addk_i32 m0, 1024\n"
  "ds_read_b64_tr_b16 a[144:145], v217 offset:0\n"
  "v_fma_f32 v67, v67, s73, v227\n"
  "v_mfma_f32_16x16x32_bf16 v[16:19], a[132:135], a[36:39], v[16:19]\n"
  "s_mov_b64 s[36:37], s[44:45]\n"
  "ds_read_b64_tr_b16 a[146:147], v218 offset:0\n"
  "v_fma_f32 v0, v0, s73, v223\n"
  "v_mfma_f32_16x16x32_bf16 v[16:19], a[136:139], a[40:43], v[16:19]\n"
  "ds_read_b64_tr_b16 a[148:149], v217 offset:1024\n"
  "v_fma_f32 v1, v1, s73, v223\n"
  "v_mfma_f32_16x16x32_bf16 v[16:19], a[140:143], a[44:47], v[16:19]\n"
  "buffer_load_dwordx4 v221, s[44:47], 0 offen offset:0 lds\n"
  "v_fma_f32 v2, v2, s73, v223\n"
  "v_mfma_f32_16x16x32_bf16 v[32:35], a[140:143], a[68:71], v[248:251]\n"
  "s_addk_i32 m0, 768\n"
  "ds_read_b64_tr_b16 a[150:151], v218 offset:1024\n"
  "v_fma_f32 v3, v3, s73, v223\n"
  "v_mfma_f32_16x16x32_bf16 v[32:35], a[136:139], a[64:67], v[32:35]\n"
  "s_add_u32 s32, s88, s28\n"
  "v_exp_f32 v64, v64\n"
  "v_mfma_f32_16x16x32_bf16 v[32:35], a[132:135], a[60:63], v[32:35]\n"
  "s_addc_u32 s33, s89, 0\n"
  "v_exp_f32 v65, v65\n"
  "v_mfma_f32_16x16x32_bf16 v[32:35], a[128:131], a[56:59], v[32:35]\n"
  "buffer_load_dwordx4 v221, s[44:47], 0 offen offset:256 lds\n"
  "v_cvt_pk_bf16_f32 v64, v64, v65\n"
  "v_mfma_f32_16x16x32_bf16 v[32:35], a[124:127], a[52:55], v[32:35]\n"
  "s_add_u32 s44, s32, s31\n"
  "v_exp_f32 v66, v66\n"
  "v_mfma_f32_16x16x32_bf16 v[32:35], a[120:123], a[48:51], v[32:35]\n"
  "s_addc_u32 s45, s33, 0\n"
  "v_exp_f32 v67, v67\n"
  "v_mfma_f32_16x16x32_bf16 v[48:51], a[120:123], a[72:75], v[248:251]\n"
  "s_mov_b32 s46, s30\n"
  "v_exp_f32 v0, v0\n"
  "v_mfma_f32_16x16x32_bf16 v[48:51], a[124:127], a[76:79], v[48:51]\n"
  "v_exp_f32 v1, v1\n"
  "v_mfma_f32_16x16x32_bf16 v[48:51], a[128:131], a[80:83], v[48:51]\n"
  "ds_read_b64_tr_b16 a[120:121], v217 offset:16\n"
  "v_cvt_pk_bf16_f32 v65, v66, v67\n"
  "v_mfma_f32_16x16x32_bf16 v[48:51], a[132:135], a[84:87], v[48:51]\n"
  "ds_read_b64_tr_b16 a[122:123], v218 offset:16\n"
  "v_add_f32 v252, v252, v0\n"
  "v_mfma_f32_16x16x32_bf16 v[48:51], a[136:139], a[88:91], v[48:51]\n"
  "ds_read_b64_tr_b16 a[124:125], v217 offset:1040\n"
  "v_add_f32 v252, v252, v1\n"
  "v_mfma_f32_16x16x32_bf16 v[48:51], a[140:143], a[92:95], v[48:51]\n"
  "s_waitcnt lgkmcnt(3)\n"
  "ds_read_b64_tr_b16 a[126:127], v218 offset:1040\n"
  "v_fma_f32 v16, v16, s73, v224\n"
  "v_mfma_f32_16x16x32_bf16 a[228:231], a[156:159], a[144:147], a[228:231]\n"
  "v_exp_f32 v2, v2\n"
  "v_mfma_f32_16x16x32_bf16 a[228:231], a[160:163], a[148:151], a[228:231]\n"
  "v_exp_f32 v3, v3\n"
  "v_mfma_f32_16x16x32_bf16 a[212:215], v[56:59], a[144:147], a[212:215]\n"
  "v_exp_f32 v16, v16\n"
  "v_mfma_f32_16x16x32_bf16 a[212:215], v[60:63], a[148:151], a[212:215]\n"
  "v_add_f32 v252, v252, v2\n"
  "v_add_f32 v252, v252, v3\n"
  "v_mfma_f32_16x16x32_bf16 a[196:199], v[40:43], a[144:147], a[196:199]\n"
  "v_fma_f32 v17, v17, s73, v224\n"
  "v_fma_f32 v18, v18, s73, v224\n"
  "v_mfma_f32_16x16x32_bf16 a[196:199], v[44:47], a[148:151], a[196:199]\n"
  "v_exp_f32 v17, v17\n"
  "v_mfma_f32_16x16x32_bf16 a[180:183], v[24:27], a[144:147], a[180:183]\n"
  "ds_read_b64_tr_b16 a[128:129], v219 offset:0\n"
  "v_fma_f32 v19, v19, s73, v224\n"
  "v_mfma_f32_16x16x32_bf16 a[180:183], v[28:31], a[148:151], a[180:183]\n"
  "s_waitcnt vmcnt(14)\n"
  "ds_read_b64_tr_b16 a[130:131], v220 offset:0\n"
  "v_add_f32 v253, v253, v16\n"
  "v_mfma_f32_16x16x32_bf16 a[164:167], v[8:11], a[144:147], a[164:167]\n"
  "s_waitcnt vmcnt(12)\n"
  "ds_read_b64_tr_b16 a[132:133], v219 offset:1024\n"
  "v_add_f32 v253, v253, v17\n"
  "v_mfma_f32_16x16x32_bf16 a[164:167], v[12:15], a[148:151], a[164:167]\n"
  "ds_read_b64_tr_b16 a[134:135], v220 offset:1024\n"
  "v_fma_f32 v32, v32, s73, v225\n"
  "v_mfma_f32_16x16x32_bf16 v[84:87], a[156:159], a[152:155], v[84:87]\n"
  "v_exp_f32 v18, v18\n"
  "v_mfma_f32_16x16x32_bf16 v[84:87], a[160:163], a[152:155], v[84:87]\n"
  "s_waitcnt lgkmcnt(4)\n"
  "v_exp_f32 v19, v19\n"
  "v_mfma_f32_16x16x32_bf16 a[168:171], v[8:11], a[120:123], a[168:171]\n"
  "v_exp_f32 v32, v32\n"
  "v_mfma_f32_16x16x32_bf16 a[168:171], v[12:15], a[124:127], a[168:171]\n"
  "v_add_f32 v253, v253, v18\n"
  "v_add_f32 v253, v253, v19\n"
  "v_mfma_f32_16x16x32_bf16 a[184:187], v[24:27], a[120:123], a[184:187]\n"
  "ds_read_b128 a[136:139], v235 offset:0\n"
  "v_fma_f32 v33, v33, s73, v225\n"
  "v_mfma_f32_16x16x32_bf16 a[184:187], v[28:31], a[124:127], a[184:187]\n"
  "ds_read_b128 a[140:143], v235 offset:64\n"
  "v_fma_f32 v34, v34, s73, v225\n"
  "v_mfma_f32_16x16x32_bf16 a[200:203], v[40:43], a[120:123], a[200:203]\n"
  "ds_read_b128 a[144:147], v235 offset:1024\n"
  "v_fma_f32 v35, v35, s73, v225\n"
  "v_mfma_f32_16x16x32_bf16 a[200:203], v[44:47], a[124:127], a[200:203]\n"
  "ds_read_b128 a[148:151], v235 offset:1088\n"
  "v_add_f32 v254, v254, v32\n"
  "v_mfma_f32_16x16x32_bf16 a[216:219], v[56:59], a[120:123], a[216:219]\n"
  "v_exp_f32 v33, v33\n"
  "v_mfma_f32_16x16x32_bf16 a[216:219], v[60:63], a[124:127], a[216:219]\n"
  "v_exp_f32 v34, v34\n"
  "v_mfma_f32_16x16x32_bf16 a[232:235], a[156:159], a[120:123], a[232:235]\n"
  "ds_read_b128 a[120:123], v235 offset:2048\n"
  "v_add_f32 v254, v254, v33\n"
  "v_mfma_f32_16x16x32_bf16 a[232:235], a[160:163], a[124:127], a[232:235]\n"
  "s_waitcnt lgkmcnt(5)\n"
  "ds_read_b128 a[124:127], v235 offset:2112\n"
  "v_add_f32 v254, v254, v34\n"
  "v_mfma_f32_16x16x32_bf16 a[220:223], v[56:59], a[128:131], a[220:223]\n"
  "v_exp_f32 v35, v35\n"
  "v_mfma_f32_16x16x32_bf16 a[220:223], v[60:63], a[132:135], a[220:223]\n"
  "ds_write_b64 v231, v[64:65] offset:0\n"
  "v_mfma_f32_16x16x32_bf16 a[204:207], v[40:43], a[128:131], a[204:207]\n"
  "v_add_f32 v254, v254, v35\n"
  "v_fma_f32 v48, v48, s73, v226\n"
  "v_mfma_f32_16x16x32_bf16 a[204:207], v[44:47], a[132:135], a[204:207]\n"
  "v_exp_f32 v48, v48\n"
  "v_mfma_f32_16x16x32_bf16 a[188:191], v[24:27], a[128:131], a[188:191]\n"
  "v_fma_f32 v49, v49, s73, v226\n"
  "v_fma_f32 v50, v50, s73, v226\n"
  "v_mfma_f32_16x16x32_bf16 a[188:191], v[28:31], a[132:135], a[188:191]\n"
  "v_exp_f32 v49, v49\n"
  "v_mfma_f32_16x16x32_bf16 a[172:175], v[8:11], a[128:131], a[172:175]\n"
  "v_exp_f32 v50, v50\n"
  "v_mfma_f32_16x16x32_bf16 a[172:175], v[12:15], a[132:135], a[172:175]\n"
  "s_waitcnt lgkmcnt(5)\n"
  "v_fma_f32 v51, v51, s73, v226\n"
  "v_add_f32 v255, v255, v48\n"
  "v_mfma_f32_16x16x32_bf16 v[4:7], a[136:139], a[0:3], v[248:251]\n"
  "v_exp_f32 v51, v51\n"
  "v_mfma_f32_16x16x32_bf16 v[4:7], a[140:143], a[4:7], v[4:7]\n"
  "s_waitcnt lgkmcnt(3)\n"
  "v_add_f32 v255, v255, v49\n"
  "v_add_f32 v255, v255, v50\n"
  "v_mfma_f32_16x16x32_bf16 v[4:7], a[144:147], a[8:11], v[4:7]\n"
  "v_add_f32 v255, v255, v51\n"
  "v_mov_b32 v237, 0\n"
  "v_mfma_f32_16x16x32_bf16 v[4:7], a[148:151], a[12:15], v[4:7]\n"
  "s_waitcnt lgkmcnt(1)\n"
  "v_mov_b32 v238, 0\n"
  "v_mov_b32 v239, 0\n"
  "v_mfma_f32_16x16x32_bf16 v[4:7], a[120:123], a[16:19], v[4:7]\n"
  "v_mov_b32 v241, 0\n"
  "v_mov_b32 v242, 0\n"
  "v_mfma_f32_16x16x32_bf16 v[4:7], a[124:127], a[20:23], v[4:7]\n"
  "v_mov_b32 v243, 0\n"
  "v_bfe_u32 v234, v231, 1, 6\n"
  "v_mfma_f32_16x16x32_bf16 v[20:23], a[124:127], a[44:47], v[248:251]\n"
  "v_mfma_f32_16x16x32_bf16 v[20:23], a[120:123], a[40:43], v[20:23]\n"
  "v_mfma_f32_16x16x32_bf16 v[20:23], a[148:151], a[36:39], v[20:23]\n"
  "ds_read_b64_tr_b16 a[128:129], v219 offset:16\n"
  "v_fma_f32 v4, v4, s73, v223\n"
  "v_mfma_f32_16x16x32_bf16 v[20:23], a[144:147], a[32:35], v[20:23]\n"
  "ds_read_b64_tr_b16 a[130:131], v220 offset:16\n"
  "v_fma_f32 v5, v5, s73, v223\n"
  "v_mfma_f32_16x16x32_bf16 v[20:23], a[140:143], a[28:31], v[20:23]\n"
  "ds_read_b64_tr_b16 a[132:133], v219 offset:1040\n"
  "v_fma_f32 v6, v6, s73, v223\n"
  "v_mfma_f32_16x16x32_bf16 v[20:23], a[136:139], a[24:27], v[20:23]\n"
  "ds_read_b64_tr_b16 a[134:135], v220 offset:1040\n"
  "v_fma_f32 v7, v7, s73, v223\n"
  "v_mfma_f32_16x16x32_bf16 v[36:39], a[136:139], a[48:51], v[248:251]\n"
  "v_exp_f32 v4, v4\n"
  "v_mfma_f32_16x16x32_bf16 v[36:39], a[140:143], a[52:55], v[36:39]\n"
  "v_exp_f32 v5, v5\n"
  "v_mfma_f32_16x16x32_bf16 v[36:39], a[144:147], a[56:59], v[36:39]\n"
  "v_exp_f32 v6, v6\n"
  "v_mfma_f32_16x16x32_bf16 v[36:39], a[148:151], a[60:63], v[36:39]\n"
  "v_exp_f32 v7, v7\n"
  "v_mfma_f32_16x16x32_bf16 v[36:39], a[120:123], a[64:67], v[36:39]\n"
  "v_add_f32 v252, v252, v4\n"
  "v_add_f32 v252, v252, v5\n"
  "v_mfma_f32_16x16x32_bf16 v[36:39], a[124:127], a[68:71], v[36:39]\n"
  "v_add_f32 v252, v252, v6\n"
  "v_add_f32 v252, v252, v7\n"
  "v_mfma_f32_16x16x32_bf16 v[52:55], a[124:127], a[92:95], v[248:251]\n"
  "ds_read_b64_tr_b16 a[124:125], v217 offset:2048\n"
  "v_fma_f32 v20, v20, s73, v224\n"
  "v_mfma_f32_16x16x32_bf16 v[52:55], a[120:123], a[88:91], v[52:55]\n"
  "ds_read_b64_tr_b16 a[126:127], v218 offset:2048\n"
  "v_fma_f32 v21, v21, s73, v224\n"
  "v_mfma_f32_16x16x32_bf16 v[52:55], a[148:151], a[84:87], v[52:55]\n"
  "ds_read_b64_tr_b16 a[120:121], v217 offset:2064\n"
  "v_fma_f32 v22, v22, s73, v224\n"
  "v_mfma_f32_16x16x32_bf16 v[52:55], a[144:147], a[80:83], v[52:55]\n"
  "ds_read_b64_tr_b16 a[122:123], v218 offset:2064\n"
  "v_fma_f32 v23, v23, s73, v224\n"
  "v_mfma_f32_16x16x32_bf16 v[52:55], a[140:143], a[76:79], v[52:55]\n"
  "v_exp_f32 v20, v20\n"
  "v_mfma_f32_16x16x32_bf16 v[52:55], a[136:139], a[72:75], v[52:55]\n"
  "s_waitcnt lgkmcnt(4)\n"
  "v_exp_f32 v21, v21\n"
  "v_mfma_f32_16x16x32_bf16 a[176:179], v[8:11], a[128:131], a[176:179]\n"
  "v_cvt_pk_bf16_f32 v8, v0, v1\n"
  "v_cvt_pk_bf16_f32 v9, v2, v3\n"
  "v_mfma_f32_16x16x32_bf16 a[176:179], v[12:15], a[132:135], a[176:179]\n"
  "v_exp_f32 v22, v22\n"
  "v_mfma_f32_16x16x32_bf16 a[192:195], v[24:27], a[128:131], a[192:195]\n"
  "ds_read_b64_tr_b16 a[140:141], v219 offset:2048\n"
  "v_cvt_pk_bf16_f32 v24, v16, v17\n"
  "v_mfma_f32_16x16x32_bf16 a[192:195], v[28:31], a[132:135], a[192:195]\n"
  "ds_read_b64_tr_b16 a[142:143], v220 offset:2048\n"
  "v_cvt_pk_bf16_f32 v25, v18, v19\n"
  "v_mfma_f32_16x16x32_bf16 a[208:211], v[40:43], a[128:131], a[208:211]\n"
  "ds_read_b64_tr_b16 a[136:137], v219 offset:2064\n"
  "v_cvt_pk_bf16_f32 v40, v32, v33\n"
  "v_mfma_f32_16x16x32_bf16 a[208:211], v[44:47], a[132:135], a[208:211]\n"
  "ds_read_b64_tr_b16 a[138:139], v220 offset:2064\n"
  "v_cvt_pk_bf16_f32 v41, v34, v35\n"
  "v_mfma_f32_16x16x32_bf16 a[224:227], v[56:59], a[128:131], a[224:227]\n"
  "v_exp_f32 v23, v23\n"
  "v_mfma_f32_16x16x32_bf16 a[224:227], v[60:63], a[132:135], a[224:227]\n"
  "s_waitcnt lgkmcnt(4)\n"
  "v_cvt_pk_bf16_f32 v56, v48, v49\n"
  "v_cvt_pk_bf16_f32 v57, v50, v51\n"
  "v_mfma_f32_16x16x32_bf16 a[244:247], a[160:163], a[124:127], a[244:247]\n"
  "v_cvt_pk_bf16_f32 v10, v4, v5\n"
  "v_cvt_pk_bf16_f32 v11, v6, v7\n"
  "v_mfma_f32_16x16x32_bf16 a[248:251], a[160:163], a[120:123], a[248:251]\n"
  "v_add_f32 v253, v253, v20\n"
  "v_add_f32 v253, v253, v21\n"
  "v_mfma_f32_16x16x32_bf16 v[200:203], v[60:63], a[124:127], v[200:203]\n"
  "v_add_f32 v253, v253, v22\n"
  "v_add_f32 v253, v253, v23\n"
  "v_mfma_f32_16x16x32_bf16 v[204:207], v[60:63], a[120:123], v[204:207]\n"
  "v_cvt_pk_bf16_f32 v26, v20, v21\n"
  "v_cvt_pk_bf16_f32 v27, v22, v23\n"
  "v_mfma_f32_16x16x32_bf16 v[168:171], v[44:47], a[124:127], v[168:171]\n"
  "v_fma_f32 v36, v36, s73, v225\n"
  "v_fma_f32 v37, v37, s73, v225\n"
  "v_mfma_f32_16x16x32_bf16 v[172:175], v[44:47], a[120:123], v[172:175]\n"
  "s_waitcnt vmcnt(6) lgkmcnt(0)\n"
  "s_barrier\n"
  "ds_read_b128 a[128:131], v216 offset:12352\n"
  "v_mfma_f32_16x16x32_bf16 v[136:139], v[28:31], a[124:127], v[136:139]\n"
  "ds_read_b64 a[156:157], v231 offset:0\n"
  "v_fma_f32 v38, v38, s73, v225\n"
  "v_mfma_f32_16x16x32_bf16 v[140:143], v[28:31], a[120:123], v[140:143]\n"
  "ds_read_b64 a[158:159], v232 offset:0\n"
  "v_fma_f32 v39, v39, s73, v225\n"
  "v_mfma_f32_16x16x32_bf16 v[104:107], v[12:15], a[124:127], v[104:107]\n"
  "ds_read_b128 a[132:135], v216 offset:12416\n"
  "v_fma_f32 v52, v52, s73, v226\n"
  "v_mfma_f32_16x16x32_bf16 v[108:111], v[12:15], a[120:123], v[108:111]\n"
  "ds_read_b128 a[124:127], v216 offset:13376\n"
  "v_fma_f32 v53, v53, s73, v226\n"
  "v_mfma_f32_16x16x32_bf16 v[112:115], v[12:15], a[140:143], v[112:115]\n"
  "ds_read_b128 a[120:123], v216 offset:13440\n"
  "v_fma_f32 v54, v54, s73, v226\n"
  "v_mfma_f32_16x16x32_bf16 v[116:119], v[12:15], a[136:139], v[116:119]\n"
  "s_mov_b32 m0, s66\n"
  "ds_read_b128 a[148:151], v216 offset:14400\n"
  "v_fma_f32 v55, v55, s73, v226\n"
  "v_mfma_f32_16x16x32_bf16 v[144:147], v[28:31], a[140:143], v[144:147]\n"
  "ds_read_b128 a[144:147], v216 offset:14464\n"
  "v_and_b32 v236, 0xffff0000, v252\n"
  "v_mfma_f32_16x16x32_bf16 v[148:151], v[28:31], a[136:139], v[148:151]\n"
  "v_exp_f32 v36, v36\n"
  "v_mfma_f32_16x16x32_bf16 v[176:179], v[44:47], a[140:143], v[176:179]\n"
  "v_exp_f32 v37, v37\n"
  "v_mfma_f32_16x16x32_bf16 v[180:183], v[44:47], a[136:139], v[180:183]\n"
  "buffer_load_dwordx4 v221, s[40:43], 0 offen offset:0 lds\n"
  "v_mfma_f32_16x16x32_bf16 v[208:211], v[60:63], a[140:143], v[208:211]\n"
  "s_addk_i32 m0, 896\n"
  "v_exp_f32 v38, v38\n"
  "v_mfma_f32_16x16x32_bf16 v[212:215], v[60:63], a[136:139], v[212:215]\n"
  "s_waitcnt lgkmcnt(4)\n"
  "v_exp_f32 v39, v39\n"
  "v_mfma_f32_16x16x32_bf16 v[64:67], a[128:131], a[96:99], v[248:251]\n"
  "v_exp_f32 v52, v52\n"
  "v_mfma_f32_16x16x32_bf16 v[64:67], a[132:135], a[100:103], v[64:67]\n"
  "s_waitcnt lgkmcnt(2)\n"
  "buffer_load_dwordx4 v221, s[40:43], 0 offen offset:128 lds\n"
  "v_add_f32 v254, v254, v36\n"
  "v_mfma_f32_16x16x32_bf16 v[64:67], a[124:127], a[104:107], v[64:67]\n"
  "s_addk_i32 m0, 896\n"
  "v_exp_f32 v53, v53\n"
  "v_mfma_f32_16x16x32_bf16 v[64:67], a[120:123], a[108:111], v[64:67]\n"
  "s_waitcnt lgkmcnt(0)\n"
  "v_exp_f32 v54, v54\n"
  "v_mfma_f32_16x16x32_bf16 v[64:67], a[148:151], a[112:115], v[64:67]\n"
  "v_exp_f32 v55, v55\n"
  "v_mfma_f32_16x16x32_bf16 v[64:67], a[144:147], a[116:119], v[64:67]\n"
  "buffer_load_dwordx4 v221, s[40:43], 0 offen offset:256 lds\n"
  "v_add_f32 v254, v254, v37\n"
  "v_mfma_f32_16x16x32_bf16 v[0:3], a[144:147], a[20:23], v[248:251]\n"
  "s_add_u32 s40, s40, -12288\n"
  "v_add_f32 v254, v254, v38\n"
  "v_add_f32 v254, v254, v39\n"
  "v_mfma_f32_16x16x32_bf16 v[0:3], a[148:151], a[16:19], v[0:3]\n"
  "s_addc_u32 s41, s41, -1\n"
  "v_cvt_pk_bf16_f32 v42, v36, v37\n"
  "v_cvt_pk_bf16_f32 v43, v38, v39\n"
  "v_mfma_f32_16x16x32_bf16 v[0:3], a[120:123], a[12:15], v[0:3]\n"
  "v_add_f32 v255, v255, v52\n"
  "v_add_f32 v255, v255, v53\n"
  "v_mfma_f32_16x16x32_bf16 v[0:3], a[124:127], a[8:11], v[0:3]\n"
  "s_mov_b32 m0, s58\n"
  "v_add_f32 v255, v255, v54\n"
  "v_add_f32 v255, v255, v55\n"
  "v_mfma_f32_16x16x32_bf16 v[0:3], a[132:135], a[4:7], v[0:3]\n"
  "v_cvt_pk_bf16_f32 v58, v52, v53\n"
  "v_cvt_pk_bf16_f32 v59, v54, v55\n"
  "v_mfma_f32_16x16x32_bf16 v[0:3], a[128:131], a[0:3], v[0:3]\n"
  "v_sub_f32 v252, v252, v236\n"
  "v_or_b32_sdwa v236, v236, v252 dst_sel:DWORD dst_unused:UNUSED_PAD src0_sel:DWORD src1_sel:WORD_1\n"
  "v_mfma_f32_16x16x32_bf16 v[16:19], a[128:131], a[24:27], v[248:251]\n"
  "s_add_u32 s71, s92, s83\n"
  "v_and_b32 v240, 0xffff0000, v253\n"
  "v_sub_f32 v253, v253, v240\n"
  "v_mfma_f32_16x16x32_bf16 v[16:19], a[132:135], a[28:31], v[16:19]\n"
  "buffer_load_dwordx4 v221, s[32:35], 0 offen offset:0 lds\n"
  "v_or_b32_sdwa v240, v240, v253 dst_sel:DWORD dst_unused:UNUSED_PAD src0_sel:DWORD src1_sel:WORD_1\n"
  "v_mfma_f32_16x16x32_bf16 v[16:19], a[124:127], a[32:35], v[16:19]\n"
  "s_addk_i32 m0, 1024\n"
  "ds_read_b128 a[140:143], v235 offset:12352\n"
  "v_fma_f32 v64, v64, s73, v227\n"
  "v_mfma_f32_16x16x32_bf16 v[16:19], a[120:123], a[36:39], v[16:19]\n"
  "s_mov_b64 s[32:33], s[36:37]\n"
  "ds_read_b128 a[136:139], v235 offset:12416\n"
  "v_fma_f32 v65, v65, s73, v227\n"
  "v_mfma_f32_16x16x32_bf16 v[16:19], a[148:151], a[40:43], v[16:19]\n"
  "v_exp_f32 v64, v64\n"
  "v_mfma_f32_16x16x32_bf16 v[16:19], a[144:147], a[44:47], v[16:19]\n"
  "buffer_load_dwordx4 v221, s[44:47], 0 offen offset:0 lds\n"
  "v_fma_f32 v66, v66, s73, v227\n"
  "v_mfma_f32_16x16x32_bf16 v[32:35], a[144:147], a[68:71], v[248:251]\n"
  "s_addk_i32 m0, 768\n"
  "v_exp_f32 v65, v65\n"
  "v_mfma_f32_16x16x32_bf16 v[32:35], a[148:151], a[64:67], v[32:35]\n"
  "v_exp_f32 v66, v66\n"
  "v_mfma_f32_16x16x32_bf16 v[32:35], a[120:123], a[60:63], v[32:35]\n"
  "v_fma_f32 v67, v67, s73, v227\n"
  "v_cvt_pk_bf16_f32 v64, v64, v65\n"
  "v_mfma_f32_16x16x32_bf16 v[32:35], a[124:127], a[56:59], v[32:35]\n"
  "buffer_load_dwordx4 v221, s[44:47], 0 offen offset:256 lds\n"
  "v_fma_f32 v0, v0, s73, v223\n"
  "v_mfma_f32_16x16x32_bf16 v[32:35], a[132:135], a[52:55], v[32:35]\n"
  "s_add_u32 s44, s44, -12288\n"
  "v_exp_f32 v67, v67\n"
  "v_mfma_f32_16x16x32_bf16 v[32:35], a[128:131], a[48:51], v[32:35]\n"
  "s_addc_u32 s45, s45, -1\n"
  "v_exp_f32 v0, v0\n"
  "v_mfma_f32_16x16x32_bf16 v[48:51], a[128:131], a[72:75], v[248:251]\n"
  "ds_read_b128 a[128:131], v235 offset:13376\n"
  "v_cvt_pk_bf16_f32 v65, v66, v67\n"
  "v_mfma_f32_16x16x32_bf16 v[48:51], a[132:135], a[76:79], v[48:51]\n"
  "ds_read_b128 a[132:135], v235 offset:13440\n"
  "v_fma_f32 v1, v1, s73, v223\n"
  "v_mfma_f32_16x16x32_bf16 v[48:51], a[124:127], a[80:83], v[48:51]\n"
  "ds_read_b128 a[124:127], v235 offset:14400\n"
  "v_fma_f32 v2, v2, s73, v223\n"
  "v_mfma_f32_16x16x32_bf16 v[48:51], a[120:123], a[84:87], v[48:51]\n"
  "ds_read_b128 a[120:123], v235 offset:14464\n"
  "v_fma_f32 v3, v3, s73, v223\n"
  "v_mfma_f32_16x16x32_bf16 v[48:51], a[148:151], a[88:91], v[48:51]\n"
  "v_exp_f32 v1, v1\n"
  "v_mfma_f32_16x16x32_bf16 v[48:51], a[144:147], a[92:95], v[48:51]\n"
  "s_waitcnt lgkmcnt(4)\n"
  "v_exp_f32 v2, v2\n"
  "v_mfma_f32_16x16x32_bf16 v[4:7], a[140:143], a[0:3], v[248:251]\n"
  "ds_read_b128 a[0:3], v222 offset:0\n"
  "v_fma_f32 v16, v16, s73, v224\n"
  "v_mfma_f32_16x16x32_bf16 v[4:7], a[136:139], a[4:7], v[4:7]\n"
  "s_waitcnt lgkmcnt(3)\n"
  "ds_read_b128 a[4:7], v222 offset:64\n"
  "v_fma_f32 v17, v17, s73, v224\n"
  "v_mfma_f32_16x16x32_bf16 v[4:7], a[128:131], a[8:11], v[4:7]\n"
  "ds_read_b128 a[8:11], v222 offset:2240\n"
  "v_fma_f32 v18, v18, s73, v224\n"
  "v_mfma_f32_16x16x32_bf16 v[4:7], a[132:135], a[12:15], v[4:7]\n"
  "s_waitcnt lgkmcnt(3)\n"
  "ds_read_b128 a[12:15], v222 offset:2304\n"
  "v_fma_f32 v19, v19, s73, v224\n"
  "v_mfma_f32_16x16x32_bf16 v[4:7], a[124:127], a[16:19], v[4:7]\n"
  "ds_read_b128 a[16:19], v222 offset:4480\n"
  "v_fma_f32 v32, v32, s73, v225\n"
  "v_mfma_f32_16x16x32_bf16 v[4:7], a[120:123], a[20:23], v[4:7]\n"
  "ds_read_b128 a[20:23], v222 offset:4544\n"
  "v_fma_f32 v33, v33, s73, v225\n"
  "v_mfma_f32_16x16x32_bf16 v[20:23], a[120:123], a[44:47], v[248:251]\n"
  "ds_read_b128 a[44:47], v222 offset:6784\n"
  "v_fma_f32 v34, v34, s73, v225\n"
  "v_mfma_f32_16x16x32_bf16 v[20:23], a[124:127], a[40:43], v[20:23]\n"
  "ds_read_b128 a[40:43], v222 offset:6720\n"
  "v_fma_f32 v35, v35, s73, v225\n"
  "v_mfma_f32_16x16x32_bf16 v[20:23], a[132:135], a[36:39], v[20:23]\n"
  "ds_read_b64_tr_b16 a[148:149], v217 offset:12352\n"
  "v_fma_f32 v4, v4, s73, v223\n"
  "v_mfma_f32_16x16x32_bf16 v[20:23], a[128:131], a[32:35], v[20:23]\n"
  "ds_read_b64_tr_b16 a[150:151], v218 offset:12352\n"
  "v_fma_f32 v5, v5, s73, v223\n"
  "v_mfma_f32_16x16x32_bf16 v[20:23], a[136:139], a[28:31], v[20:23]\n"
  "s_add_u32 m0, s72, -256\n"
  "ds_read_b64_tr_b16 a[144:145], v217 offset:13376\n"
  "v_mfma_f32_16x16x32_bf16 v[20:23], a[140:143], a[24:27], v[20:23]\n"
  "s_add_u32 s52, s62, s57\n"
  "ds_read_b64_tr_b16 a[146:147], v218 offset:13376\n"
  "v_fma_f32 v6, v6, s73, v223\n"
  "v_mfma_f32_16x16x32_bf16 v[36:39], a[140:143], a[48:51], v[248:251]\n"
  "s_addc_u32 s53, s63, 0\n"
  "v_exp_f32 v3, v3\n"
  "v_mfma_f32_16x16x32_bf16 v[36:39], a[136:139], a[52:55], v[36:39]\n"
  "s_mov_b32 s54, s93\n"
  "v_exp_f32 v4, v4\n"
  "v_mfma_f32_16x16x32_bf16 v[36:39], a[128:131], a[56:59], v[36:39]\n"
  "s_waitcnt lgkmcnt(10)\n"
  "buffer_load_dwordx4 v221, s[48:51], 0 offen offset:256  lds\n"
  "v_mfma_f32_16x16x32_bf16 v[36:39], a[132:135], a[60:63], v[36:39]\n"
  "s_add_u32 m0, s72, -2240\n"
  "v_exp_f32 v5, v5\n"
  "v_mfma_f32_16x16x32_bf16 v[36:39], a[124:127], a[64:67], v[36:39]\n"
  "s_mov_b32 s57, s92\n"
  "v_exp_f32 v6, v6\n"
  "v_mfma_f32_16x16x32_bf16 v[36:39], a[120:123], a[68:71], v[36:39]\n"
  "s_mov_b32 s93, s94\n"
  "v_exp_f32 v16, v16\n"
  "v_mfma_f32_16x16x32_bf16 v[52:55], a[120:123], a[92:95], v[248:251]\n"
  "buffer_load_dwordx4 v221, s[48:51], 0 offen offset:3328  lds\n"
  "v_fma_f32 v7, v7, s73, v223\n"
  "v_mfma_f32_16x16x32_bf16 v[52:55], a[124:127], a[88:91], v[52:55]\n"
  "s_add_u32 s48, s84, s71\n"
  "ds_read_b64_tr_b16 a[120:121], v217 offset:12368\n"
  "v_fma_f32 v20, v20, s73, v224\n"
  "v_mfma_f32_16x16x32_bf16 v[52:55], a[132:135], a[84:87], v[52:55]\n"
  "s_addc_u32 s49, s85, 0\n"
  "ds_read_b64_tr_b16 a[122:123], v218 offset:12368\n"
  "v_fma_f32 v21, v21, s73, v224\n"
  "v_mfma_f32_16x16x32_bf16 v[52:55], a[128:131], a[80:83], v[52:55]\n"
  "s_add_u32 m0, s72, 2240\n"
  "ds_read_b64_tr_b16 a[124:125], v217 offset:13392\n"
  "v_mfma_f32_16x16x32_bf16 v[52:55], a[136:139], a[76:79], v[52:55]\n"
  "s_sub_i32 s71, s38, s83\n"
  "s_max_i32 s50, s71, 0\n"
  "s_waitcnt lgkmcnt(11)\n"
  "buffer_load_dwordx4 v221, s[48:51], 0 offen offset:0 nt lds\n"
  "v_fma_f32 v22, v22, s73, v224\n"
  "v_mfma_f32_16x16x32_bf16 v[52:55], a[140:143], a[72:75], v[52:55]\n"
  "s_waitcnt lgkmcnt(3)\n"
  "ds_read_b64_tr_b16 a[126:127], v218 offset:13392\n"
  "v_fma_f32 v23, v23, s73, v224\n"
  "v_mfma_f32_16x16x32_bf16 a[236:239], a[160:163], a[148:151], a[236:239]\n"
  "v_exp_f32 v7, v7\n"
  "v_mfma_f32_16x16x32_bf16 a[236:239], a[156:159], a[144:147], a[236:239]\n"
  "s_add_u32 m0, s72, 256\n"
  "ds_write_b64 v231, v[64:65] offset:2048\n"
  "v_mfma_f32_16x16x32_bf16 v[184:187], v[60:63], a[148:151], v[184:187]\n"
  "buffer_load_dwordx4 v221, s[48:51], 0 offen offset:3072 nt lds\n"
  "v_fma_f32 v36, v36, s73, v225\n"
  "v_mfma_f32_16x16x32_bf16 v[184:187], v[56:59], a[144:147], v[184:187]\n"
  "s_add_u32 m0, s72, 4352\n"
  "v_exp_f32 v17, v17\n"
  "v_mfma_f32_16x16x32_bf16 v[152:155], v[44:47], a[148:151], v[152:155]\n"
  "v_exp_f32 v18, v18\n"
  "v_mfma_f32_16x16x32_bf16 v[152:155], v[40:43], a[144:147], v[152:155]\n"
  "ds_read_b64_tr_b16 a[132:133], v219 offset:12352\n"
  "v_fma_f32 v37, v37, s73, v225\n"
  "v_mfma_f32_16x16x32_bf16 v[120:123], v[28:31], a[148:151], v[120:123]\n"
  "buffer_load_dwordx4 v221, s[48:51], 0 offen offset:128 nt lds\n"
  "v_fma_f32 v38, v38, s73, v225\n"
  "v_mfma_f32_16x16x32_bf16 v[120:123], v[24:27], a[144:147], v[120:123]\n"
  "s_add_u32 m0, s72, 2368\n"
  "ds_read_b64_tr_b16 a[134:135], v220 offset:12352\n"
  "v_mfma_f32_16x16x32_bf16 v[88:91], v[12:15], a[148:151], v[88:91]\n"
  "ds_read_b64_tr_b16 a[128:129], v219 offset:13376\n"
  "v_fma_f32 v39, v39, s73, v225\n"
  "v_mfma_f32_16x16x32_bf16 v[88:91], v[8:11], a[144:147], v[88:91]\n"
  "s_waitcnt lgkmcnt(4)\n"
  "ds_read_b64_tr_b16 a[130:131], v220 offset:13376\n"
  "v_fma_f32 v48, v48, s73, v226\n"
  "v_mfma_f32_16x16x32_bf16 v[92:95], v[12:15], a[120:123], v[92:95]\n"
  "buffer_load_dwordx4 v221, s[48:51], 0 offen offset:3200 nt lds\n"
  "v_fma_f32 v49, v49, s73, v226\n"
  "v_mfma_f32_16x16x32_bf16 v[92:95], v[8:11], a[124:127], v[92:95]\n"
  "s_add_u32 m0, s72, 6464\n"
  "v_exp_f32 v19, v19\n"
  "v_mfma_f32_16x16x32_bf16 v[124:127], v[28:31], a[120:123], v[124:127]\n"
  "v_exp_f32 v20, v20\n"
  "v_mfma_f32_16x16x32_bf16 v[124:127], v[24:27], a[124:127], v[124:127]\n"
  "v_exp_f32 v21, v21\n"
  "v_mfma_f32_16x16x32_bf16 v[156:159], v[44:47], a[120:123], v[156:159]\n"
  "buffer_load_dwordx4 v221, s[48:51], 0 offen offset:256 nt lds\n"
  "v_fma_f32 v50, v50, s73, v226\n"
  "v_mfma_f32_16x16x32_bf16 v[156:159], v[40:43], a[124:127], v[156:159]\n"
  "s_add_u32 m0, s72, 4480\n"
  "ds_read_b64_tr_b16 a[136:137], v219 offset:12368\n"
  "v_mfma_f32_16x16x32_bf16 v[188:191], v[60:63], a[120:123], v[188:191]\n"
  "ds_read_b64_tr_b16 a[138:139], v220 offset:12368\n"
  "v_fma_f32 v51, v51, s73, v226\n"
  "v_mfma_f32_16x16x32_bf16 v[188:191], v[56:59], a[124:127], v[188:191]\n"
  "ds_read_b64_tr_b16 a[140:141], v219 offset:13392\n"
  "v_fma_f32 v52, v52, s73, v226\n"
  "v_mfma_f32_16x16x32_bf16 a[240:243], a[160:163], a[120:123], a[240:243]\n"
  "buffer_load_dwordx4 v221, s[48:51], 0 offen offset:3328 nt lds\n"
  "v_fma_f32 v53, v53, s73, v226\n"
  "v_mfma_f32_16x16x32_bf16 a[240:243], a[156:159], a[124:127], a[240:243]\n"
  "ds_read_b64_tr_b16 a[142:143], v220 offset:13392\n"
  "v_fma_f32 v54, v54, s73, v226\n"
  "v_mfma_f32_16x16x32_bf16 v[68:71], v[236:239], a[152:155], v[68:71]\n"
  "s_waitcnt lgkmcnt(4)\n"
  "v_exp_f32 v22, v22\n"
  "v_mfma_f32_16x16x32_bf16 v[192:195], v[60:63], a[132:135], v[192:195]\n"
  "v_exp_f32 v23, v23\n"
  "v_mfma_f32_16x16x32_bf16 v[192:195], v[56:59], a[128:131], v[192:195]\n"
  "v_exp_f32 v32, v32\n"
  "v_mfma_f32_16x16x32_bf16 v[160:163], v[44:47], a[132:135], v[160:163]\n"
  "ds_read_b64_tr_b16 a[120:121], v217 offset:14400\n"
  "v_and_b32 v236, 0xffff0000, v254\n"
  "v_mfma_f32_16x16x32_bf16 v[160:163], v[40:43], a[128:131], v[160:163]\n"
  "ds_read_b64_tr_b16 a[122:123], v218 offset:14400\n"
  "v_sub_f32 v254, v254, v236\n"
  "v_mfma_f32_16x16x32_bf16 v[128:131], v[28:31], a[132:135], v[128:131]\n"
  "ds_read_b64_tr_b16 a[124:125], v217 offset:14416\n"
  "v_or_b32_sdwa v236, v236, v254 dst_sel:DWORD dst_unused:UNUSED_PAD src0_sel:DWORD src1_sel:WORD_1\n"
  "v_mfma_f32_16x16x32_bf16 v[128:131], v[24:27], a[128:131], v[128:131]\n"
  "ds_read_b64_tr_b16 a[126:127], v218 offset:14416\n"
  "v_fma_f32 v55, v55, s73, v226\n"
  "v_mfma_f32_16x16x32_bf16 v[96:99], v[12:15], a[132:135], v[96:99]\n"
  "v_exp_f32 v33, v33\n"
  "v_mfma_f32_16x16x32_bf16 v[96:99], v[8:11], a[128:131], v[96:99]\n"
  "v_exp_f32 v34, v34\n"
  "v_mfma_f32_16x16x32_bf16 v[72:75], v[240:243], a[152:155], v[72:75]\n"
  "s_waitcnt lgkmcnt(4)\n"
  "v_exp_f32 v35, v35\n"
  "v_mfma_f32_16x16x32_bf16 v[100:103], v[12:15], a[136:139], v[100:103]\n"
  "v_exp_f32 v36, v36\n"
  "v_mfma_f32_16x16x32_bf16 v[100:103], v[8:11], a[140:143], v[100:103]\n"
  "v_exp_f32 v37, v37\n"
  "v_mfma_f32_16x16x32_bf16 v[132:135], v[28:31], a[136:139], v[132:135]\n"
  "v_exp_f32 v38, v38\n"
  "v_mfma_f32_16x16x32_bf16 v[132:135], v[24:27], a[140:143], v[132:135]\n"
  "v_exp_f32 v39, v39\n"
  "v_mfma_f32_16x16x32_bf16 v[164:167], v[44:47], a[136:139], v[164:167]\n"
  "ds_read_b64_tr_b16 a[132:133], v219 offset:14400\n"
  "v_and_b32 v240, 0xffff0000, v255\n"
  "v_mfma_f32_16x16x32_bf16 v[164:167], v[40:43], a[140:143], v[164:167]\n"
  "ds_read_b64_tr_b16 a[134:135], v220 offset:14400\n"
  "v_sub_f32 v255, v255, v240\n"
  "v_mfma_f32_16x16x32_bf16 v[196:199], v[60:63], a[136:139], v[196:199]\n"
  "ds_read_b64_tr_b16 a[128:129], v219 offset:14416\n"
  "v_or_b32_sdwa v240, v240, v255 dst_sel:DWORD dst_unused:UNUSED_PAD src0_sel:DWORD src1_sel:WORD_1\n"
  "v_mfma_f32_16x16x32_bf16 v[196:199], v[56:59], a[140:143], v[196:199]\n"
  "ds_read_b64_tr_b16 a[130:131], v220 offset:14416\n"
  "v_cvt_pk_bf16_f32 v12, v0, v1\n"
  "v_mfma_f32_16x16x32_bf16 v[76:79], v[236:239], a[152:155], v[76:79]\n"
  "s_waitcnt lgkmcnt(4)\n"
  "v_exp_f32 v48, v48\n"
  "v_mfma_f32_16x16x32_bf16 a[244:247], a[156:159], a[120:123], a[244:247]\n"
  "v_exp_f32 v49, v49\n"
  "v_mfma_f32_16x16x32_bf16 a[248:251], a[156:159], a[124:127], a[248:251]\n"
  "v_exp_f32_e64 v50, v50\n"
  "v_mfma_f32_16x16x32_bf16 v[200:203], v[56:59], a[120:123], v[200:203]\n"
  "v_add_u32 v233, s97, v222\n"
  "s_mov_b64 exec, s[98:99]\n"
  "v_add_u32 v233, 0x4f00, v233\n"
  "s_mov_b64 exec, -1\n"
  "v_mfma_f32_16x16x32_bf16 v[204:207], v[56:59], a[124:127], v[204:207]\n"
  "ds_read_b128 a[96:99], v233 offset:17920\n"
  "ds_read_b128 a[100:103], v233 offset:17984\n"
  "ds_read_b128 a[104:107], v233 offset:19040\n"
  "ds_read_b128 a[108:111], v233 offset:19104\n"
  "ds_read_b128 a[112:115], v233 offset:20160\n"
  "ds_read_b128 a[116:119], v233 offset:20224\n"
  "v_mfma_f32_16x16x32_bf16 v[168:171], v[40:43], a[120:123], v[168:171]\n"
  "v_exp_f32 v51, v51\n"
  "v_mfma_f32_16x16x32_bf16 v[172:175], v[40:43], a[124:127], v[172:175]\n"
  "s_mov_b32 m0, s67\n"
  "v_exp_f32 v52, v52\n"
  "v_mfma_f32_16x16x32_bf16 v[136:139], v[24:27], a[120:123], v[136:139]\n"
  "s_waitcnt vmcnt(14) lgkmcnt(0)\n"
  "s_barrier\n"
  "v_mfma_f32_16x16x32_bf16 v[140:143], v[24:27], a[124:127], v[140:143]\n"
  "ds_read_b64 a[160:161], v231 offset:2048\n"
  "v_cvt_pk_bf16_f32 v13, v2, v3\n"
  "v_mfma_f32_16x16x32_bf16 v[104:107], v[8:11], a[120:123], v[104:107]\n"
  "ds_read_b64 a[162:163], v232 offset:2048\n"
  "v_cvt_pk_bf16_f32 v14, v4, v5\n"
  "v_mfma_f32_16x16x32_bf16 v[108:111], v[8:11], a[124:127], v[108:111]\n"
  "buffer_load_dwordx4 v221, s[40:43], 0 offen offset:0 lds\n"
  "v_mfma_f32_16x16x32_bf16 v[80:83], v[240:243], a[152:155], v[80:83]\n"
  "ds_read_b64_tr_b16 a[120:121], v217 offset:26752\n"
  "v_cvt_pk_bf16_f32 v15, v6, v7\n"
  "v_mfma_f32_16x16x32_bf16 v[112:115], v[8:11], a[132:135], v[112:115]\n"
  "s_addk_i32 m0, 896\n"
  "ds_read_b64_tr_b16 a[122:123], v218 offset:26752\n"
  "v_cvt_pk_bf16_f32 v28, v16, v17\n"
  "v_mfma_f32_16x16x32_bf16 v[116:119], v[8:11], a[128:131], v[116:119]\n"
  "ds_read_b64_tr_b16 a[124:125], v217 offset:26768\n"
  "v_cvt_pk_bf16_f32 v29, v18, v19\n"
  "v_mfma_f32_16x16x32_bf16 v[144:147], v[24:27], a[132:135], v[144:147]\n"
  "buffer_load_dwordx4 v221, s[40:43], 0 offen offset:128 lds\n"
  "v_cvt_pk_bf16_f32 v30, v20, v21\n"
  "v_mfma_f32_16x16x32_bf16 v[148:151], v[24:27], a[128:131], v[148:151]\n"
  "s_addk_i32 m0, 896\n"
  "ds_read_b64_tr_b16 a[126:127], v218 offset:26768\n"
  "v_cvt_pk_bf16_f32 v31, v22, v23\n"
  "v_mfma_f32_16x16x32_bf16 v[176:179], v[40:43], a[132:135], v[176:179]\n"
  "v_exp_f32 v53, v53\n"
  "v_mfma_f32_16x16x32_bf16 v[180:183], v[40:43], a[128:131], v[180:183]\n"
  "v_exp_f32 v54, v54\n"
  "v_mfma_f32_16x16x32_bf16 v[208:211], v[56:59], a[132:135], v[208:211]\n"
  "buffer_load_dwordx4 v221, s[40:43], 0 offen offset:256 lds\n"
  "v_cvt_pk_bf16_f32 v44, v32, v33\n"
  "v_mfma_f32_16x16x32_bf16 v[212:215], v[56:59], a[128:131], v[212:215]\n"
  "s_cmp_eq_i32 s76, 5\n"
  "s_cbranch_scc1 L_TAIL_%=\n"
  "v_exp_f32 v55, v55\n"
  "v_mfma_f32_16x16x32_bf16 v[68:71], v[12:15], a[152:155], v[68:71]\n"
  "ds_read_b64_tr_b16 a[132:133], v219 offset:26752\n"
  "v_cvt_pk_bf16_f32 v45, v34, v35\n"
  "v_cvt_pk_bf16_f32 v46, v36, v37\n"
  "v_cvt_pk_bf16_f32 v47, v38, v39\n"
  "v_mfma_f32_16x16x32_bf16 v[72:75], v[28:31], a[152:155], v[72:75]\n"
  "s_add_u32 m0, s59, -128\n"
  "ds_read_b64_tr_b16 a[134:135], v220 offset:26752\n"
  "v_cvt_pk_bf16_f32 v60, v48, v49\n"
  "v_cvt_pk_bf16_f32 v61, v50, v51\n"
  "v_cvt_pk_bf16_f32 v62, v52, v53\n"
  "v_cvt_pk_bf16_f32 v63, v54, v55\n"
  "v_mfma_f32_16x16x32_bf16 v[76:79], v[44:47], a[152:155], v[76:79]\n"
  "buffer_load_dwordx4 v221, s[32:35], 0 offen offset:128 lds\n"
  "ds_read_b128 a[24:27], v222 offset:11200\n"
  "v_mfma_f32_16x16x32_bf16 v[80:83], v[60:63], a[152:155], v[80:83]\n"
  "s_addk_i32 m0, 1024\n"
  "ds_read_b64_tr_b16 a[128:129], v219 offset:26768\n"
  "ds_read_b128 a[28:31], v222 offset:11264\n"
  "v_mfma_f32_16x16x32_bf16 v[84:87], a[156:159], a[152:155], v[84:87]\n"
  "s_waitcnt lgkmcnt(9)\n"
  "ds_read_b64_tr_b16 a[130:131], v220 offset:26768\n"
  "ds_read_b128 a[32:35], v222 offset:8960\n"
  "v_mfma_f32_16x16x32_bf16 v[84:87], a[160:163], a[152:155], v[84:87]\n"
  "s_waitcnt lgkmcnt(7)\n"
  "ds_read_b128 a[36:39], v222 offset:9024\n"
  "ds_read_b128 a[48:51], v222 offset:13440\n"
  "v_mfma_f32_16x16x32_bf16 a[244:247], a[160:163], a[120:123], a[244:247]\n"
  "buffer_load_dwordx4 v221, s[44:47], 0 offen offset:128 lds\n"
  "ds_read_b128 a[52:55], v222 offset:13504\n"
  "v_mfma_f32_16x16x32_bf16 a[248:251], a[160:163], a[124:127], a[248:251]\n"
  "s_addk_i32 m0, 896\n"
  "ds_read_b128 a[56:59], v222 offset:15680\n"
  "ds_read_b128 a[60:63], v222 offset:15744\n"
  "v_mfma_f32_16x16x32_bf16 v[200:203], v[60:63], a[120:123], v[200:203]\n"
  "ds_read_b64_tr_b16 a[136:137], v217 offset:24704\n"
  "v_maximum3_f32 v236, v68, v69, v70\n"
  "v_mfma_f32_16x16x32_bf16 v[204:207], v[60:63], a[124:127], v[204:207]\n"
  "ds_read_b64_tr_b16 a[138:139], v218 offset:24704\n"
  "v_maximum3_f32 v236, v236, v71, v72\n"
  "v_mfma_f32_16x16x32_bf16 v[168:171], v[44:47], a[120:123], v[168:171]\n"
  "buffer_load_dwordx4 v221, s[44:47], 0 offen offset:256 lds\n"
  "v_maximum3_f32 v236, v236, v73, v74\n"
  "v_mfma_f32_16x16x32_bf16 v[172:175], v[44:47], a[124:127], v[172:175]\n"
  "ds_read_b64_tr_b16 a[140:141], v217 offset:25728\n"
  "v_maximum3_f32 v236, v236, v75, v76\n"
  "v_mfma_f32_16x16x32_bf16 v[136:139], v[28:31], a[120:123], v[136:139]\n"
  "ds_read_b64_tr_b16 a[142:143], v218 offset:25728\n"
  "v_maximum3_f32 v236, v236, v77, v78\n"
  "v_mfma_f32_16x16x32_bf16 v[140:143], v[28:31], a[124:127], v[140:143]\n"
  "v_maximum3_f32 v236, v236, v79, v80\n"
  "v_maximum3_f32 v236, v236, v81, v82\n"
  "v_mfma_f32_16x16x32_bf16 v[104:107], v[12:15], a[120:123], v[104:107]\n"
  "v_maximum3_f32 v236, v236, v83, v84\n"
  "v_maximum3_f32 v236, v236, v85, v86\n"
  "v_mfma_f32_16x16x32_bf16 v[108:111], v[12:15], a[124:127], v[108:111]\n"
  "s_waitcnt lgkmcnt(10)\n"
  "v_rcp_f32 v84, v84\n"
  "v_mfma_f32_16x16x32_bf16 v[112:115], v[12:15], a[132:135], v[112:115]\n"
  "s_add_u32 s40, s40, 24576\n"
  "v_rcp_f32 v85, v85\n"
  "v_mfma_f32_16x16x32_bf16 v[116:119], v[12:15], a[128:131], v[116:119]\n"
  "s_addc_u32 s41, s41, 0\n"
  "v_accvgpr_read_b32 v240, a244\n"
  "v_accvgpr_read_b32 v241, a245\n"
  "v_mfma_f32_16x16x32_bf16 v[144:147], v[28:31], a[132:135], v[144:147]\n"
  "ds_read_b64_tr_b16 a[120:121], v217 offset:24720\n"
  "v_accvgpr_read_b32 v242, a248\n"
  "v_mfma_f32_16x16x32_bf16 v[148:151], v[28:31], a[128:131], v[148:151]\n"
  "ds_read_b64_tr_b16 a[122:123], v218 offset:24720\n"
  "v_accvgpr_read_b32 v243, a249\n"
  "v_mfma_f32_16x16x32_bf16 v[176:179], v[44:47], a[132:135], v[176:179]\n"
  "ds_read_b64_tr_b16 a[124:125], v217 offset:25744\n"
  "v_maximum3_f32 v236, v236, v87, v87\n"
  "v_mfma_f32_16x16x32_bf16 v[180:183], v[44:47], a[128:131], v[180:183]\n"
  "ds_read_b64_tr_b16 a[126:127], v218 offset:25744\n"
  "v_cmp_ngt_f32 vcc, s75, v236\n"
  "v_mfma_f32_16x16x32_bf16 v[208:211], v[60:63], a[132:135], v[208:211]\n"
  "s_mov_b32 s75, s65\n"
  "v_rcp_f32 v68, v68\n"
  "v_mfma_f32_16x16x32_bf16 v[212:215], v[60:63], a[128:131], v[212:215]\n"
  "s_waitcnt lgkmcnt(4)\n"
  "v_rcp_f32 v69, v69\n"
  "v_mfma_f32_16x16x32_bf16 a[228:231], a[156:159], a[136:139], a[228:231]\n"
  "s_add_u32 s32, s44, s31\n"
  "v_rcp_f32 v70, v70\n"
  "v_mfma_f32_16x16x32_bf16 a[228:231], a[160:163], a[140:143], a[228:231]\n"
  "s_addc_u32 s33, s45, 0\n"
  "v_rcp_f32 v71, v71\n"
  "v_mfma_f32_16x16x32_bf16 a[212:215], v[56:59], a[136:139], a[212:215]\n"
  "s_add_u32 s44, s44, 24576\n"
  "v_rcp_f32 v72, v72\n"
  "v_mfma_f32_16x16x32_bf16 a[212:215], v[60:63], a[140:143], a[212:215]\n"
  "s_addc_u32 s45, s45, 0\n"
  "v_rcp_f32 v73, v73\n"
  "v_mfma_f32_16x16x32_bf16 a[196:199], v[40:43], a[136:139], a[196:199]\n"
  "ds_read_b128 a[132:135], v216 offset:24704\n"
  "v_accvgpr_read_b32 v236, a246\n"
  "v_mfma_f32_16x16x32_bf16 a[196:199], v[44:47], a[140:143], a[196:199]\n"
  "ds_read_b128 a[128:131], v216 offset:24768\n"
  "v_accvgpr_read_b32 v237, a247\n"
  "v_mfma_f32_16x16x32_bf16 a[180:183], v[24:27], a[136:139], a[180:183]\n"
  "ds_read_b128 a[148:151], v216 offset:25728\n"
  "v_accvgpr_read_b32 v238, a250\n"
  "v_mfma_f32_16x16x32_bf16 a[180:183], v[28:31], a[140:143], a[180:183]\n"
  "ds_read_b128 a[144:147], v216 offset:25792\n"
  "v_add_u32 v233, s61, v229\n"
  "s_mov_b32 exec_lo, 0\n"
  "v_add_u32 v233, s69, v233\n"
  "s_mov_b32 exec_lo, -1\n"
  "v_rcp_f32_e64 v74, v74\n"
  "v_pk_fma_f32 v[240:241], v[240:241], v[84:85], v[84:85] neg_lo:[0,0,1] neg_hi:[0,0,1]\n"
  "v_pk_fma_f32 v[242:243], v[242:243], v[84:85], v[84:85] neg_lo:[0,0,1] neg_hi:[0,0,1]\n"
  "v_pk_fma_f32 v[104:105], v[104:105], v[68:69], v[68:69] neg_lo:[0,0,1] neg_hi:[0,0,1]\n"
  "v_pk_fma_f32 v[108:109], v[108:109], v[68:69], v[68:69] neg_lo:[0,0,1] neg_hi:[0,0,1]\n"
  "v_mfma_f32_16x16x32_bf16 a[164:167], v[8:11], a[136:139], a[164:167]\n"
  "s_nop 0\n"
  "ds_read_b128 a[136:139], v216 offset:26752\n"
  "v_cvt_pk_bf16_f32 v240, v240, v242\n"
  "v_mfma_f32_16x16x32_bf16 a[164:167], v[12:15], a[140:143], a[164:167]\n"
  "s_waitcnt lgkmcnt(5)\n"
  "ds_read_b128 a[140:143], v216 offset:26816\n"
  "v_cvt_pk_bf16_f32 v241, v241, v243\n"
  "v_cvt_pk_bf16_f32 v104, v104, v108\n"
  "v_rcp_f32_e64 v75, v75\n"
  "v_pk_fma_f32 v[112:113], v[112:113], v[68:69], v[68:69] neg_lo:[0,0,1] neg_hi:[0,0,1]\n"
  "v_pk_fma_f32 v[116:117], v[116:117], v[68:69], v[68:69] neg_lo:[0,0,1] neg_hi:[0,0,1]\n"
  "v_pk_fma_f32 v[106:107], v[106:107], v[70:71], v[70:71] neg_lo:[0,0,1] neg_hi:[0,0,1]\n"
  "v_pk_fma_f32 v[110:111], v[110:111], v[70:71], v[70:71] neg_lo:[0,0,1] neg_hi:[0,0,1]\n"
  "v_mfma_f32_16x16x32_bf16 a[168:171], v[8:11], a[120:123], a[168:171]\n"
  "s_cmp_lg_u64 vcc, 0\n"
  "s_cselect_b32 s79, s77, 0\n"
  "s_or_b32 s78, s78, s79\n"
  "s_lshl_b32 s77, s77, 1\n"
  "v_rcp_f32 v76, v76\n"
  "v_mfma_f32_16x16x32_bf16 a[168:171], v[12:15], a[124:127], a[168:171]\n"
  "v_cvt_pk_bf16_f32 v108, v105, v109\n"
  "v_rcp_f32 v77, v77\n"
  "v_rcp_f32_e64 v78, v78\n"
  "v_pk_fma_f32 v[114:115], v[114:115], v[70:71], v[70:71] neg_lo:[0,0,1] neg_hi:[0,0,1]\n"
  "v_pk_fma_f32 v[118:119], v[118:119], v[70:71], v[70:71] neg_lo:[0,0,1] neg_hi:[0,0,1]\n"
  "v_pk_fma_f32 v[136:137], v[136:137], v[72:73], v[72:73] neg_lo:[0,0,1] neg_hi:[0,0,1]\n"
  "v_pk_fma_f32 v[140:141], v[140:141], v[72:73], v[72:73] neg_lo:[0,0,1] neg_hi:[0,0,1]\n"
  "v_mfma_f32_16x16x32_bf16 a[184:187], v[24:27], a[120:123], a[184:187]\n"
  "v_rcp_f32_e64 v79, v79\n"
  "v_mfma_f32_16x16x32_bf16 a[184:187], v[28:31], a[124:127], a[184:187]\n"
  "v_cvt_pk_bf16_f32 v105, v112, v116\n"
  "v_rcp_f32_e64 v80, v80\n"
  "v_rcp_f32_e64 v81, v81\n"
  "v_pk_fma_f32 v[144:145], v[144:145], v[72:73], v[72:73] neg_lo:[0,0,1] neg_hi:[0,0,1]\n"
  "v_pk_fma_f32 v[148:149], v[148:149], v[72:73], v[72:73] neg_lo:[0,0,1] neg_hi:[0,0,1]\n"
  "v_pk_fma_f32 v[138:139], v[138:139], v[74:75], v[74:75] neg_lo:[0,0,1] neg_hi:[0,0,1]\n"
  "v_pk_fma_f32 v[142:143], v[142:143], v[74:75], v[74:75] neg_lo:[0,0,1] neg_hi:[0,0,1]\n"
  "v_mfma_f32_16x16x32_bf16 a[200:203], v[40:43], a[120:123], a[200:203]\n"
  "v_rcp_f32_e64 v82, v82\n"
  "v_mfma_f32_16x16x32_bf16 a[200:203], v[44:47], a[124:127], a[200:203]\n"
  "ds_write_b32 v233, v240 offset:20160\n"
  "v_cvt_pk_bf16_f32 v109, v113, v117\n"
  "v_rcp_f32_e64 v83, v83\n"
  "v_pk_fma_f32 v[146:147], v[146:147], v[74:75], v[74:75] neg_lo:[0,0,1] neg_hi:[0,0,1]\n"
  "v_pk_fma_f32 v[150:151], v[150:151], v[74:75], v[74:75] neg_lo:[0,0,1] neg_hi:[0,0,1]\n"
  "v_pk_fma_f32 v[168:169], v[168:169], v[76:77], v[76:77] neg_lo:[0,0,1] neg_hi:[0,0,1]\n"
  "v_pk_fma_f32 v[172:173], v[172:173], v[76:77], v[76:77] neg_lo:[0,0,1] neg_hi:[0,0,1]\n"
  "v_mfma_f32_16x16x32_bf16 a[216:219], v[56:59], a[120:123], a[216:219]\n"
  "ds_write_b32 v233, v241 offset:20448\n"
  "v_mfma_f32_16x16x32_bf16 a[216:219], v[60:63], a[124:127], a[216:219]\n"
  "ds_write_b64 v229, v[104:105] offset:8960\n"
  "v_cvt_pk_bf16_f32 v106, v106, v110\n"
  "v_cvt_pk_bf16_f32 v110, v107, v111\n"
  "v_pk_fma_f32 v[176:177], v[176:177], v[76:77], v[76:77] neg_lo:[0,0,1] neg_hi:[0,0,1]\n"
  "v_pk_fma_f32 v[180:181], v[180:181], v[76:77], v[76:77] neg_lo:[0,0,1] neg_hi:[0,0,1]\n"
  "v_pk_fma_f32 v[170:171], v[170:171], v[78:79], v[78:79] neg_lo:[0,0,1] neg_hi:[0,0,1]\n"
  "v_pk_fma_f32 v[174:175], v[174:175], v[78:79], v[78:79] neg_lo:[0,0,1] neg_hi:[0,0,1]\n"
  "v_mfma_f32_16x16x32_bf16 a[232:235], a[156:159], a[120:123], a[232:235]\n"
  "s_nop 0\n"
  "v_cvt_pk_bf16_f32 v107, v114, v118\n"
  "v_cvt_pk_bf16_f32 v111, v115, v119\n"
  "v_mfma_f32_16x16x32_bf16 a[232:235], a[160:163], a[124:127], a[232:235]\n"
  "s_waitcnt lgkmcnt(7)\n"
  "ds_write_b64 v229, v[108:109] offset:9248\n"
  "v_cvt_pk_bf16_f32 v136, v136, v140\n"
  "v_cvt_pk_bf16_f32 v140, v137, v141\n"
  "v_pk_fma_f32 v[178:179], v[178:179], v[78:79], v[78:79] neg_lo:[0,0,1] neg_hi:[0,0,1]\n"
  "v_pk_fma_f32 v[182:183], v[182:183], v[78:79], v[78:79] neg_lo:[0,0,1] neg_hi:[0,0,1]\n"
  "v_pk_fma_f32 v[200:201], v[200:201], v[80:81], v[80:81] neg_lo:[0,0,1] neg_hi:[0,0,1]\n"
  "v_pk_fma_f32 v[204:205], v[204:205], v[80:81], v[80:81] neg_lo:[0,0,1] neg_hi:[0,0,1]\n"
  "v_mfma_f32_16x16x32_bf16 v[64:67], a[132:135], a[96:99], v[248:251]\n"
  "v_cvt_pk_bf16_f32 v137, v144, v148\n"
  "v_cvt_pk_bf16_f32 v141, v145, v149\n"
  "v_mfma_f32_16x16x32_bf16 v[64:67], a[128:131], a[100:103], v[64:67]\n"
  "s_waitcnt lgkmcnt(6)\n"
  "v_cvt_pk_bf16_f32 v138, v138, v142\n"
  "v_cvt_pk_bf16_f32 v142, v139, v143\n"
  "v_mfma_f32_16x16x32_bf16 v[64:67], a[148:151], a[104:107], v[64:67]\n"
  "s_waitcnt vmcnt(12)\n"
  "ds_read_b128 a[64:67], v222 offset:0\n"
  "ds_read_b128 a[68:71], v222 offset:64\n"
  "v_mfma_f32_16x16x32_bf16 v[64:67], a[144:147], a[108:111], v[64:67]\n"
  "s_waitcnt lgkmcnt(6)\n"
  "v_cvt_pk_bf16_f32 v139, v146, v150\n"
  "v_cvt_pk_bf16_f32 v143, v147, v151\n"
  "v_mfma_f32_16x16x32_bf16 v[64:67], a[136:139], a[112:115], v[64:67]\n"
  "s_nop 0\n"
  "v_cvt_pk_bf16_f32 v168, v168, v172\n"
  "v_cvt_pk_bf16_f32 v172, v169, v173\n"
  "v_mfma_f32_16x16x32_bf16 v[64:67], a[140:143], a[116:119], v[64:67]\n"
  "ds_write_b64 v229, v[106:107] offset:9536\n"
  "v_cvt_pk_bf16_f32 v169, v176, v180\n"
  "v_cvt_pk_bf16_f32 v173, v177, v181\n"
  "v_pk_fma_f32 v[208:209], v[208:209], v[80:81], v[80:81] neg_lo:[0,0,1] neg_hi:[0,0,1]\n"
  "v_pk_fma_f32 v[212:213], v[212:213], v[80:81], v[80:81] neg_lo:[0,0,1] neg_hi:[0,0,1]\n"
  "v_pk_fma_f32 v[202:203], v[202:203], v[82:83], v[82:83] neg_lo:[0,0,1] neg_hi:[0,0,1]\n"
  "v_pk_fma_f32 v[206:207], v[206:207], v[82:83], v[82:83] neg_lo:[0,0,1] neg_hi:[0,0,1]\n"
  "v_mfma_f32_16x16x32_bf16 v[0:3], a[140:143], a[20:23], v[248:251]\n"
  "v_cvt_pk_bf16_f32 v170, v170, v174\n"
  "v_cvt_pk_bf16_f32 v174, v171, v175\n"
  "v_mfma_f32_16x16x32_bf16 v[0:3], a[136:139], a[16:19], v[0:3]\n"
  "v_cvt_pk_bf16_f32 v171, v178, v182\n"
  "v_cvt_pk_bf16_f32 v175, v179, v183\n"
  "v_mfma_f32_16x16x32_bf16 v[0:3], a[144:147], a[12:15], v[0:3]\n"
  "s_waitcnt vmcnt(10)\n"
  "ds_read_b128 a[72:75], v222 offset:2240\n"
  "ds_read_b128 a[76:79], v222 offset:2304\n"
  "v_mfma_f32_16x16x32_bf16 v[0:3], a[148:151], a[8:11], v[0:3]\n"
  "s_waitcnt vmcnt(8)\n"
  "ds_read_b128 a[80:83], v222 offset:4480\n"
  "ds_read_b128 a[84:87], v222 offset:4544\n"
  "v_mfma_f32_16x16x32_bf16 v[0:3], a[128:131], a[4:7], v[0:3]\n"
  "s_waitcnt vmcnt(6)\n"
  "ds_read_b128 a[88:91], v222 offset:6720\n"
  "ds_read_b128 a[92:95], v222 offset:6784\n"
  "v_mfma_f32_16x16x32_bf16 v[0:3], a[132:135], a[0:3], v[0:3]\n"
  "s_nop 0\n"
  "ds_write_b64 v229, v[110:111] offset:9824\n"
  "v_max3_f32 v227, v64, v65, v66\n"
  "v_cvt_pk_bf16_f32 v200, v200, v204\n"
  "v_pk_fma_f32 v[210:211], v[210:211], v[82:83], v[82:83] neg_lo:[0,0,1] neg_hi:[0,0,1]\n"
  "v_pk_fma_f32 v[214:215], v[214:215], v[82:83], v[82:83] neg_lo:[0,0,1] neg_hi:[0,0,1]\n"
  "v_mfma_f32_16x16x32_bf16 v[16:19], a[132:135], a[24:27], v[248:251]\n"
  "v_rcp_f32 v86, v86\n"
  "v_mfma_f32_16x16x32_bf16 v[16:19], a[128:131], a[28:31], v[16:19]\n"
  "ds_read_b64_tr_b16 a[252:253], v230 offset:8960\n"
  "ds_read_b64_tr_b16 a[254:255], v230 offset:8992\n"
  "v_mfma_f32_16x16x32_bf16 v[16:19], a[148:151], a[32:35], v[16:19]\n"
  "ds_read_b128 a[120:123], v235 offset:24704\n"
  "v_max3_f32 v223, v0, v1, v2\n"
  "v_mfma_f32_16x16x32_bf16 v[16:19], a[144:147], a[36:39], v[16:19]\n"
  "ds_read_b128 a[124:127], v235 offset:24768\n"
  "v_max_f32 v223, v223, v3\n"
  "v_mfma_f32_16x16x32_bf16 v[16:19], a[136:139], a[40:43], v[16:19]\n"
  "v_mul_f32 v223, s74, v223\n"
  "v_add_f32 v223, s64, v223\n"
  "v_mfma_f32_16x16x32_bf16 v[16:19], a[140:143], a[44:47], v[16:19]\n"
  "s_waitcnt lgkmcnt(12)\n"
  "ds_bpermute_b32 v223, v234, v223\n"
  "ds_write_b64 v229, v[136:137] offset:11200\n"
  "v_mfma_f32_16x16x32_bf16 v[32:35], a[140:143], a[68:71], v[248:251]\n"
  "ds_read_b64_tr_b16 v[244:245], v230 offset:10080\n"
  "ds_read_b64_tr_b16 v[246:247], v230 offset:10112\n"
  "v_mfma_f32_16x16x32_bf16 v[32:35], a[136:139], a[64:67], v[32:35]\n"
  "v_rcp_f32 v87, v87\n"
  "v_mfma_f32_16x16x32_bf16 v[32:35], a[144:147], a[60:63], v[32:35]\n"
  "v_max3_f32 v224, v16, v17, v18\n"
  "v_max_f32 v224, v224, v19\n"
  "v_mfma_f32_16x16x32_bf16 v[32:35], a[148:151], a[56:59], v[32:35]\n"
  "v_mul_f32 v224, s74, v224\n"
  "v_add_f32_e64 v224, s64, v224\n"
  "v_mfma_f32_16x16x32_bf16 v[32:35], a[128:131], a[52:55], v[32:35]\n"
  "ds_bpermute_b32 v224, v234, v224\n"
  "v_mfma_f32_16x16x32_bf16 v[32:35], a[132:135], a[48:51], v[32:35]\n"
  "s_waitcnt lgkmcnt(10)\n"
  "s_waitcnt lgkmcnt(7)\n"
  "buffer_store_dwordx4 a[252:255], v228, s[52:55], s80 offen offset:256 nt\n"
  "ds_write_b64 v229, v[140:141] offset:11488\n"
  "v_cvt_pk_bf16_f32 v204, v201, v205\n"
  "v_cvt_pk_bf16_f32 v201, v208, v212\n"
  "v_cvt_pk_bf16_f32 v205, v209, v213\n"
  "v_pk_fma_f32 v[236:237], v[236:237], v[86:87], v[86:87] neg_lo:[0,0,1] neg_hi:[0,0,1]\n"
  "v_mfma_f32_16x16x32_bf16 v[48:51], a[132:135], a[72:75], v[248:251]\n"
  "s_waitcnt lgkmcnt(5)\n"
  "ds_read_b128 a[132:135], v235 offset:25728\n"
  "v_fma_f32 v0, v0, s73, v223\n"
  "v_mfma_f32_16x16x32_bf16 v[48:51], a[128:131], a[76:79], v[48:51]\n"
  "ds_read_b128 a[128:131], v235 offset:25792\n"
  "v_fma_f32 v1, v1, s73, v223\n"
  "v_mfma_f32_16x16x32_bf16 v[48:51], a[148:151], a[80:83], v[48:51]\n"
  "ds_read_b128 a[148:151], v235 offset:26752\n"
  "v_fma_f32 v2, v2, s73, v223\n"
  "v_mfma_f32_16x16x32_bf16 v[48:51], a[144:147], a[84:87], v[48:51]\n"
  "ds_read_b128 a[144:147], v235 offset:26816\n"
  "v_fma_f32 v3, v3, s73, v223\n"
  "v_mfma_f32_16x16x32_bf16 v[48:51], a[136:139], a[88:91], v[48:51]\n"
  "s_waitcnt lgkmcnt(5)\n"
  "v_exp_f32 v0, v0\n"
  "v_mfma_f32_16x16x32_bf16 v[48:51], a[140:143], a[92:95], v[48:51]\n"
  "buffer_store_dwordx4 v[244:247], v228, s[52:55], s80 offen offset:3328 nt\n"
  "ds_write_b64 v229, v[138:139] offset:11776\n"
  "v_exp_f32 v1, v1\n"
  "v_max3_f32 v225, v32, v33, v34\n"
  "v_mfma_f32_16x16x32_bf16 v[240:243], a[120:123], a[96:99], v[248:251]\n"
  "v_exp_f32 v2, v2\n"
  "v_mfma_f32_16x16x32_bf16 v[240:243], a[124:127], a[100:103], v[240:243]\n"
  "s_waitcnt lgkmcnt(3)\n"
  "v_exp_f32 v3, v3\n"
  "v_mfma_f32_16x16x32_bf16 v[240:243], a[132:135], a[104:107], v[240:243]\n"
  "v_max_f32 v225, v225, v35\n"
  "v_mul_f32 v225, s74, v225\n"
  "v_mfma_f32_16x16x32_bf16 v[240:243], a[128:131], a[108:111], v[240:243]\n"
  "s_waitcnt lgkmcnt(1)\n"
  "v_add_f32 v225, s64, v225\n"
  "v_fma_f32 v16, v16, s73, v224\n"
  "v_mfma_f32_16x16x32_bf16 v[240:243], a[148:151], a[112:115], v[240:243]\n"
  "v_exp_f32 v16, v16\n"
  "v_mfma_f32_16x16x32_bf16 v[240:243], a[144:147], a[116:119], v[240:243]\n"
  "ds_bpermute_b32 v225, v234, v225\n"
  "ds_write_b64 v229, v[142:143] offset:12064\n"
  "v_mfma_f32_16x16x32_bf16 v[4:7], a[144:147], a[20:23], v[248:251]\n"
  "v_fma_f32 v17, v17, s73, v224\n"
  "v_fma_f32 v18, v18, s73, v224\n"
  "v_mfma_f32_16x16x32_bf16 v[4:7], a[148:151], a[16:19], v[4:7]\n"
  "v_exp_f32 v17, v17\n"
  "v_mfma_f32_16x16x32_bf16 v[4:7], a[128:131], a[12:15], v[4:7]\n"
  "v_max3_f32 v227, v227, v67, v240\n"
  "v_max3_f32 v227, v227, v241, v242\n"
  "v_mfma_f32_16x16x32_bf16 v[4:7], a[132:135], a[8:11], v[4:7]\n"
  "v_max_f32 v227, v227, v243\n"
  "v_mul_f32 v227, s74, v227\n"
  "v_mfma_f32_16x16x32_bf16 v[4:7], a[124:127], a[4:7], v[4:7]\n"
  "v_add_f32_e64 v227, s64, v227\n"
  "v_fma_f32 v19, v19, s73, v224\n"
  "v_mfma_f32_16x16x32_bf16 v[4:7], a[120:123], a[0:3], v[4:7]\n"
  "s_waitcnt lgkmcnt(1)\n"
  "ds_bpermute_b32 v227, v234, v227\n"
  "ds_write_b64 v229, v[168:169] offset:13440\n"
  "v_mfma_f32_16x16x32_bf16 v[20:23], a[120:123], a[24:27], v[248:251]\n"
  "v_exp_f32 v18, v18\n"
  "v_mfma_f32_16x16x32_bf16 v[20:23], a[124:127], a[28:31], v[20:23]\n"
  "v_exp_f32 v19, v19\n"
  "v_mfma_f32_16x16x32_bf16 v[20:23], a[132:135], a[32:35], v[20:23]\n"
  "ds_read_b64_tr_b16 a[136:137], v219 offset:24704\n"
  "v_max3_f32 v226, v48, v49, v50\n"
  "v_mfma_f32_16x16x32_bf16 v[20:23], a[128:131], a[36:39], v[20:23]\n"
  "ds_read_b64_tr_b16 a[138:139], v220 offset:24704\n"
  "v_max_f32 v226, v226, v51\n"
  "v_mfma_f32_16x16x32_bf16 v[20:23], a[148:151], a[40:43], v[20:23]\n"
  "ds_read_b64_tr_b16 a[140:141], v219 offset:25728\n"
  "v_mul_f32_e64 v226, s74, v226\n"
  "v_mfma_f32_16x16x32_bf16 v[20:23], a[144:147], a[44:47], v[20:23]\n"
  "s_waitcnt lgkmcnt(4)\n"
  "ds_read_b64_tr_b16 a[142:143], v220 offset:25728\n"
  "ds_write_b64 v229, v[172:173] offset:13728\n"
  "v_fma_f32 v64, v64, s73, v227\n"
  "v_mfma_f32_16x16x32_bf16 v[36:39], a[144:147], a[68:71], v[248:251]\n"
  "v_exp_f32 v64, v64\n"
  "v_mfma_f32_16x16x32_bf16 v[36:39], a[148:151], a[64:67], v[36:39]\n"
  "v_fma_f32 v65, v65, s73, v227\n"
  "v_fma_f32 v66, v66, s73, v227\n"
  "v_mfma_f32_16x16x32_bf16 v[36:39], a[128:131], a[60:63], v[36:39]\n"
  "v_exp_f32 v65, v65\n"
  "v_mfma_f32_16x16x32_bf16 v[36:39], a[132:135], a[56:59], v[36:39]\n"
  "v_exp_f32_e64 v66, v66\n"
  "v_mfma_f32_16x16x32_bf16 v[36:39], a[124:127], a[52:55], v[36:39]\n"
  "v_fma_f32 v67, v67, s73, v227\n"
  "v_cvt_pk_bf16_f32 v64, v64, v65\n"
  "v_mfma_f32_16x16x32_bf16 v[36:39], a[120:123], a[48:51], v[36:39]\n"
  "ds_write_b64 v229, v[170:171] offset:14016\n"
  "v_exp_f32_e64 v67, v67\n"
  "v_mfma_f32_16x16x32_bf16 v[52:55], a[120:123], a[72:75], v[248:251]\n"
  "ds_read_b64_tr_b16 a[120:121], v219 offset:24720\n"
  "v_cvt_pk_bf16_f32 v65, v66, v67\n"
  "v_mfma_f32_16x16x32_bf16 v[52:55], a[124:127], a[76:79], v[52:55]\n"
  "ds_read_b64_tr_b16 a[122:123], v220 offset:24720\n"
  "v_add_f32 v226, s64, v226\n"
  "v_mfma_f32_16x16x32_bf16 v[52:55], a[132:135], a[80:83], v[52:55]\n"
  "ds_read_b64_tr_b16 a[124:125], v219 offset:25744\n"
  "v_fma_f32 v32, v32, s73, v225\n"
  "v_mfma_f32_16x16x32_bf16 v[52:55], a[128:131], a[84:87], v[52:55]\n"
  "ds_read_b64_tr_b16 a[126:127], v220 offset:25744\n"
  "v_fma_f32 v33, v33, s73, v225\n"
  "v_mfma_f32_16x16x32_bf16 v[52:55], a[148:151], a[88:91], v[52:55]\n"
  "v_exp_f32_e64 v32, v32\n"
  "v_mfma_f32_16x16x32_bf16 v[52:55], a[144:147], a[92:95], v[52:55]\n"
  "s_waitcnt lgkmcnt(6)\n"
  "ds_read_b64_tr_b16 a[252:253], v230 offset:11200\n"
  "ds_read_b64_tr_b16 a[254:255], v230 offset:11232\n"
  "ds_write_b64 v231, v[64:65] offset:0\n"
  "v_mfma_f32_16x16x32_bf16 a[220:223], v[56:59], a[136:139], a[220:223]\n"
  "v_exp_f32_e64 v33, v33\n"
  "v_mfma_f32_16x16x32_bf16 a[220:223], v[60:63], a[140:143], a[220:223]\n"
  "ds_bpermute_b32 v226, v234, v226\n"
  "ds_write_b64 v229, v[174:175] offset:14304\n"
  "v_mfma_f32_16x16x32_bf16 a[204:207], v[40:43], a[136:139], a[204:207]\n"
  "v_fma_f32 v34, v34, s73, v225\n"
  "v_fma_f32 v35, v35, s73, v225\n"
  "v_mfma_f32_16x16x32_bf16 a[204:207], v[44:47], a[140:143], a[204:207]\n"
  "s_waitcnt vmcnt(2) lgkmcnt(2)\n"
  "s_barrier\n"
  "ds_read_b64_tr_b16 a[132:133], v217 offset:12352\n"
  "ds_read_b64_tr_b16 v[244:245], v230 offset:12320\n"
  "ds_read_b64_tr_b16 v[246:247], v230 offset:12352\n"
  "v_mfma_f32_16x16x32_bf16 a[188:191], v[24:27], a[136:139], a[188:191]\n"
  "ds_read_b64 a[156:157], v231 offset:0\n"
  "v_fma_f32 v4, v4, s73, v223\n"
  "v_mfma_f32_16x16x32_bf16 a[188:191], v[28:31], a[140:143], a[188:191]\n"
  "buffer_store_dwordx4 a[252:255], v228, s[52:55], s81 offen offset:256 nt\n"
  "ds_read_b64 a[158:159], v232 offset:0\n"
  "ds_write_b64 v229, v[200:201] offset:15680\n"
  "v_exp_f32_e64 v34, v34\n"
  "v_mfma_f32_16x16x32_bf16 a[172:175], v[8:11], a[136:139], a[172:175]\n"
  "ds_read_b64_tr_b16 a[134:135], v218 offset:12352\n"
  "v_fma_f32 v5, v5, s73, v223\n"
  "v_mfma_f32_16x16x32_bf16 a[172:175], v[12:15], a[140:143], a[172:175]\n"
  "ds_read_b64_tr_b16 a[128:129], v217 offset:12368\n"
  "ds_write_b64 v229, v[204:205] offset:15968\n"
  "v_fma_f32 v6, v6, s73, v223\n"
  "v_mfma_f32_16x16x32_bf16 a[176:179], v[8:11], a[120:123], a[176:179]\n"
  "s_waitcnt lgkmcnt(10)\n"
  "ds_read_b64_tr_b16 a[130:131], v218 offset:12368\n"
  "v_cvt_pk_bf16_f32 v8, v0, v1\n"
  "v_mfma_f32_16x16x32_bf16 a[176:179], v[12:15], a[124:127], a[176:179]\n"
  "s_nop 0\n"
  "ds_read_b64_tr_b16 a[252:253], v230 offset:13440\n"
  "ds_read_b64_tr_b16 a[254:255], v230 offset:13472\n"
  "v_cvt_pk_bf16_f32 v9, v2, v3\n"
  "v_fma_f32 v48, v48, s73, v226\n"
  "v_cvt_pk_bf16_f32 v202, v202, v206\n"
  "v_mfma_f32_16x16x32_bf16 a[192:195], v[24:27], a[120:123], a[192:195]\n"
  "ds_read_b128 a[136:139], v216 offset:12352\n"
  "v_cvt_pk_bf16_f32 v24, v16, v17\n"
  "v_mfma_f32_16x16x32_bf16 a[192:195], v[28:31], a[124:127], a[192:195]\n"
  "s_waitcnt lgkmcnt(10)\n"
  "buffer_store_dwordx4 v[244:247], v228, s[52:55], s81 offen offset:3328 nt\n"
  "ds_read_b128 a[140:143], v216 offset:12416\n"
  "v_cvt_pk_bf16_f32 v25, v18, v19\n"
  "v_exp_f32 v35, v35\n"
  "v_cvt_pk_bf16_f32 v206, v203, v207\n"
  "v_cvt_pk_bf16_f32 v203, v210, v214\n"
  "v_mfma_f32_16x16x32_bf16 a[208:211], v[40:43], a[120:123], a[208:211]\n"
  "ds_read_b128 a[148:151], v216 offset:13376\n"
  "v_cvt_pk_bf16_f32 v40, v32, v33\n"
  "v_mfma_f32_16x16x32_bf16 a[208:211], v[44:47], a[124:127], a[208:211]\n"
  "ds_read_b128 a[144:147], v216 offset:13440\n"
  "ds_write_b64 v229, v[202:203] offset:16256\n"
  "v_cvt_pk_bf16_f32 v41, v34, v35\n"
  "v_mfma_f32_16x16x32_bf16 a[224:227], v[56:59], a[120:123], a[224:227]\n"
  "s_nop 0\n"
  "ds_read_b128 a[120:123], v216 offset:14400\n"
  "v_fma_f32 v49, v49, s73, v226\n"
  "v_mfma_f32_16x16x32_bf16 a[224:227], v[60:63], a[124:127], a[224:227]\n"
  "L_JOIN_%=:\n"
  "s_waitcnt lgkmcnt(8)\n"
  "ds_read_b128 a[124:127], v216 offset:14464\n"
  "ds_read_b64_tr_b16 v[244:245], v230 offset:14560\n"
  "ds_read_b64_tr_b16 v[246:247], v230 offset:14592\n"
  "v_fma_f32 v50, v50, s73, v226\n"
  "v_cvt_pk_bf16_f32 v207, v211, v215\n"
  "v_mfma_f32_16x16x32_bf16 a[236:239], a[160:163], a[132:135], a[236:239]\n"
  "s_add_i32 s76, s76, 1\n"
  "v_exp_f32_e64 v48, v48\n"
  "v_mfma_f32_16x16x32_bf16 a[240:243], a[160:163], a[128:131], a[240:243]\n"
  "s_waitcnt lgkmcnt(9)\n"
  "buffer_store_dwordx4 a[252:255], v228, s[52:55], s82 offen offset:256 nt\n"
  "ds_write_b64 v229, v[206:207] offset:16544\n"
  "v_exp_f32 v49, v49\n"
  "v_fma_f32 v51, v51, s73, v226\n"
  "v_mfma_f32_16x16x32_bf16 v[184:187], v[60:63], a[132:135], v[184:187]\n"
  "v_exp_f32 v50, v50\n"
  "v_accvgpr_read_b32 v239, a251\n"
  "v_accvgpr_read_b32 v136, a180\n"
  "v_accvgpr_read_b32 v137, a181\n"
  "v_pk_fma_f32 v[136:137], v[136:137], v[72:73], v[72:73] neg_lo:[0,0,1] neg_hi:[0,0,1]\n"
  "v_pk_fma_f32 v[238:239], v[238:239], v[86:87], v[86:87] neg_lo:[0,0,1] neg_hi:[0,0,1]\n"
  "v_mfma_f32_16x16x32_bf16 v[188:191], v[60:63], a[128:131], v[188:191]\n"
  "v_exp_f32_e64 v51, v51\n"
  "v_mfma_f32_16x16x32_bf16 v[152:155], v[44:47], a[132:135], v[152:155]\n"
  "ds_read_b64_tr_b16 a[252:253], v230 offset:15680\n"
  "ds_read_b64_tr_b16 a[254:255], v230 offset:15712\n"
  "v_cvt_pk_bf16_f32 v56, v48, v49\n"
  "v_cvt_pk_bf16_f32 v57, v50, v51\n"
  "v_accvgpr_read_b32 v140, a184\n"
  "v_mfma_f32_16x16x32_bf16 v[156:159], v[44:47], a[128:131], v[156:159]\n"
  "v_exp_f32_e64 v4, v4\n"
  "v_accvgpr_read_b32 v141, a185\n"
  "v_accvgpr_read_b32 v144, a188\n"
  "v_accvgpr_read_b32 v145, a189\n"
  "v_pk_fma_f32 v[140:141], v[140:141], v[72:73], v[72:73] neg_lo:[0,0,1] neg_hi:[0,0,1]\n"
  "v_pk_fma_f32 v[144:145], v[144:145], v[72:73], v[72:73] neg_lo:[0,0,1] neg_hi:[0,0,1]\n"
  "v_pk_fma_f32 v[184:185], v[184:185], v[80:81], v[80:81] neg_lo:[0,0,1] neg_hi:[0,0,1]\n"
  "v_pk_fma_f32 v[186:187], v[186:187], v[82:83], v[82:83] neg_lo:[0,0,1] neg_hi:[0,0,1]\n"
  "v_mfma_f32_16x16x32_bf16 v[120:123], v[28:31], a[132:135], v[120:123]\n"
  "v_exp_f32_e64 v5, v5\n"
  "v_mfma_f32_16x16x32_bf16 v[124:127], v[28:31], a[128:131], v[124:127]\n"
  "s_waitcnt lgkmcnt(3)\n"
  "buffer_store_dwordx4 v[244:247], v228, s[52:55], s82 offen offset:3328 nt\n"
  "v_exp_f32 v6, v6\n"
  "v_fma_f32 v7, v7, s73, v223\n"
  "v_cvt_pk_bf16_f32 v10, v4, v5\n"
  "v_accvgpr_read_b32 v148, a192\n"
  "v_accvgpr_read_b32 v149, a193\n"
  "v_pk_fma_f32 v[148:149], v[148:149], v[72:73], v[72:73] neg_lo:[0,0,1] neg_hi:[0,0,1]\n"
  "v_pk_fma_f32 v[188:189], v[188:189], v[80:81], v[80:81] neg_lo:[0,0,1] neg_hi:[0,0,1]\n"
  "v_pk_fma_f32 v[190:191], v[190:191], v[82:83], v[82:83] neg_lo:[0,0,1] neg_hi:[0,0,1]\n"
  "v_pk_fma_f32 v[152:153], v[152:153], v[76:77], v[76:77] neg_lo:[0,0,1] neg_hi:[0,0,1]\n"
  "v_mfma_f32_16x16x32_bf16 v[88:91], v[12:15], a[132:135], v[88:91]\n"
  "ds_read_b64_tr_b16 v[244:245], v230 offset:16800\n"
  "ds_read_b64_tr_b16 v[246:247], v230 offset:16832\n"
  "v_exp_f32_e64 v7, v7\n"
  "v_accvgpr_read_b32 v138, a182\n"
  "v_pk_fma_f32 v[156:157], v[156:157], v[76:77], v[76:77] neg_lo:[0,0,1] neg_hi:[0,0,1]\n"
  "v_pk_fma_f32 v[154:155], v[154:155], v[78:79], v[78:79] neg_lo:[0,0,1] neg_hi:[0,0,1]\n"
  "v_pk_fma_f32 v[158:159], v[158:159], v[78:79], v[78:79] neg_lo:[0,0,1] neg_hi:[0,0,1]\n"
  "v_mfma_f32_16x16x32_bf16 v[92:95], v[12:15], a[128:131], v[92:95]\n"
  "v_cvt_pk_bf16_f32 v11, v6, v7\n"
  "v_fma_f32 v20, v20, s73, v224\n"
  "v_accvgpr_read_b32 v139, a183\n"
  "v_accvgpr_read_b32 v142, a186\n"
  "v_accvgpr_read_b32 v143, a187\n"
  "v_pk_fma_f32 v[138:139], v[138:139], v[74:75], v[74:75] neg_lo:[0,0,1] neg_hi:[0,0,1]\n"
  "v_pk_fma_f32 v[142:143], v[142:143], v[74:75], v[74:75] neg_lo:[0,0,1] neg_hi:[0,0,1]\n"
  "v_pk_fma_f32 v[120:121], v[120:121], v[72:73], v[72:73] neg_lo:[0,0,1] neg_hi:[0,0,1]\n"
  "v_pk_fma_f32 v[122:123], v[122:123], v[74:75], v[74:75] neg_lo:[0,0,1] neg_hi:[0,0,1]\n"
  "v_mfma_f32_16x16x32_bf16 v[64:67], a[136:139], a[96:99], v[248:251]\n"
  "v_exp_f32 v20, v20\n"
  "v_mfma_f32_16x16x32_bf16 v[64:67], a[140:143], a[100:103], v[64:67]\n"
  "v_fma_f32 v21, v21, s73, v224\n"
  "v_fma_f32 v22, v22, s73, v224\n"
  "v_mfma_f32_16x16x32_bf16 v[64:67], a[148:151], a[104:107], v[64:67]\n"
  "v_exp_f32 v21, v21\n"
  "v_mfma_f32_16x16x32_bf16 v[64:67], a[144:147], a[108:111], v[64:67]\n"
  "v_exp_f32_e64 v22, v22\n"
  "v_mfma_f32_16x16x32_bf16 v[64:67], a[120:123], a[112:115], v[64:67]\n"
  "v_fma_f32 v23, v23, s73, v224\n"
  "v_cvt_pk_bf16_f32 v26, v20, v21\n"
  "v_mfma_f32_16x16x32_bf16 v[64:67], a[124:127], a[116:119], v[64:67]\n"
  "s_waitcnt lgkmcnt(2)\n"
  "buffer_store_dwordx4 a[252:255], v228, s[52:55], s83 offen offset:256 nt\n"
  "v_exp_f32 v23, v23\n"
  "v_fma_f32 v36, v36, s73, v225\n"
  "v_mfma_f32_16x16x32_bf16 v[0:3], a[124:127], a[20:23], v[248:251]\n"
  "v_exp_f32 v36, v36\n"
  "v_mfma_f32_16x16x32_bf16 v[0:3], a[120:123], a[16:19], v[0:3]\n"
  "v_cvt_pk_bf16_f32 v27, v22, v23\n"
  "v_fma_f32 v37, v37, s73, v225\n"
  "v_mfma_f32_16x16x32_bf16 v[0:3], a[144:147], a[12:15], v[0:3]\n"
  "v_exp_f32 v37, v37\n"
  "v_mfma_f32_16x16x32_bf16 v[0:3], a[148:151], a[8:11], v[0:3]\n"
  "v_fma_f32 v38, v38, s73, v225\n"
  "v_fma_f32 v39, v39, s73, v225\n"
  "v_mfma_f32_16x16x32_bf16 v[0:3], a[140:143], a[4:7], v[0:3]\n"
  "v_exp_f32 v38, v38\n"
  "v_mfma_f32_16x16x32_bf16 v[0:3], a[136:139], a[0:3], v[0:3]\n"
  "s_waitcnt lgkmcnt(0)\n"
  "buffer_store_dwordx4 v[244:247], v228, s[52:55], s83 offen offset:3328 nt\n"
  "v_exp_f32_e64 v39, v39\n"
  "v_cvt_pk_bf16_f32 v42, v36, v37\n"
  "v_fma_f32 v52, v52, s73, v226\n"
  "v_accvgpr_read_b32 v146, a190\n"
  "v_accvgpr_read_b32 v147, a191\n"
  "v_pk_fma_f32 v[146:147], v[146:147], v[74:75], v[74:75] neg_lo:[0,0,1] neg_hi:[0,0,1]\n"
  "v_pk_fma_f32 v[124:125], v[124:125], v[72:73], v[72:73] neg_lo:[0,0,1] neg_hi:[0,0,1]\n"
  "v_pk_fma_f32 v[126:127], v[126:127], v[74:75], v[74:75] neg_lo:[0,0,1] neg_hi:[0,0,1]\n"
  "v_pk_fma_f32 v[88:89], v[88:89], v[68:69], v[68:69] neg_lo:[0,0,1] neg_hi:[0,0,1]\n"
  "v_mfma_f32_16x16x32_bf16 v[16:19], a[136:139], a[24:27], v[248:251]\n"
  "v_exp_f32_e64 v52, v52\n"
  "v_mfma_f32_16x16x32_bf16 v[16:19], a[140:143], a[28:31], v[16:19]\n"
  "v_cvt_pk_bf16_f32 v43, v38, v39\n"
  "v_fma_f32 v53, v53, s73, v226\n"
  "v_mfma_f32_16x16x32_bf16 v[16:19], a[148:151], a[32:35], v[16:19]\n"
  "ds_read_b64_tr_b16 a[132:133], v219 offset:12352\n"
  "v_fma_f32 v54, v54, s73, v226\n"
  "v_mfma_f32_16x16x32_bf16 v[16:19], a[144:147], a[36:39], v[16:19]\n"
  "ds_read_b64_tr_b16 a[134:135], v220 offset:12352\n"
  "v_fma_f32 v55, v55, s73, v226\n"
  "v_mfma_f32_16x16x32_bf16 v[16:19], a[120:123], a[40:43], v[16:19]\n"
  "ds_read_b64_tr_b16 a[128:129], v219 offset:12368\n"
  "v_accvgpr_read_b32 v150, a194\n"
  "v_mfma_f32_16x16x32_bf16 v[16:19], a[124:127], a[44:47], v[16:19]\n"
  "ds_read_b64_tr_b16 a[130:131], v220 offset:12368\n"
  "v_accvgpr_read_b32 v151, a195\n"
  "v_cvt_pk_bf16_f32 v136, v136, v140\n"
  "v_cvt_pk_bf16_f32 v140, v137, v141\n"
  "v_cvt_pk_bf16_f32 v137, v144, v148\n"
  "v_pk_fma_f32 v[150:151], v[150:151], v[74:75], v[74:75] neg_lo:[0,0,1] neg_hi:[0,0,1]\n"
  "v_pk_fma_f32 v[92:93], v[92:93], v[68:69], v[68:69] neg_lo:[0,0,1] neg_hi:[0,0,1]\n"
  "v_pk_fma_f32 v[90:91], v[90:91], v[70:71], v[70:71] neg_lo:[0,0,1] neg_hi:[0,0,1]\n"
  "v_pk_fma_f32 v[94:95], v[94:95], v[70:71], v[70:71] neg_lo:[0,0,1] neg_hi:[0,0,1]\n"
  "v_mfma_f32_16x16x32_bf16 v[32:35], a[124:127], a[68:71], v[248:251]\n"
  "v_exp_f32 v53, v53\n"
  "v_mfma_f32_16x16x32_bf16 v[32:35], a[120:123], a[64:67], v[32:35]\n"
  "v_exp_f32 v54, v54\n"
  "v_mfma_f32_16x16x32_bf16 v[32:35], a[144:147], a[60:63], v[32:35]\n"
  "v_exp_f32 v55, v55\n"
  "v_mfma_f32_16x16x32_bf16 v[32:35], a[148:151], a[56:59], v[32:35]\n"
  "v_cvt_pk_bf16_f32 v58, v52, v53\n"
  "v_cvt_pk_bf16_f32 v59, v54, v55\n"
  "v_mfma_f32_16x16x32_bf16 v[32:35], a[140:143], a[52:55], v[32:35]\n"
  "v_cvt_pk_bf16_f32 v138, v138, v142\n"
  "v_cvt_pk_bf16_f32 v142, v139, v143\n"
  "v_mfma_f32_16x16x32_bf16 v[32:35], a[136:139], a[48:51], v[32:35]\n"
  "v_cvt_pk_bf16_f32 v139, v146, v150\n"
  "v_accvgpr_read_b32 v104, a164\n"
  "v_mfma_f32_16x16x32_bf16 v[48:51], a[136:139], a[72:75], v[248:251]\n"
  "ds_read_b128 a[136:139], v235 offset:12352\n"
  "v_accvgpr_read_b32 v105, a165\n"
  "v_mfma_f32_16x16x32_bf16 v[48:51], a[140:143], a[76:79], v[48:51]\n"
  "ds_read_b128 a[140:143], v235 offset:12416\n"
  "v_accvgpr_read_b32 v108, a168\n"
  "v_mfma_f32_16x16x32_bf16 v[48:51], a[148:151], a[80:83], v[48:51]\n"
  "ds_read_b128 a[148:151], v235 offset:13376\n"
  "v_accvgpr_read_b32 v109, a169\n"
  "v_mfma_f32_16x16x32_bf16 v[48:51], a[144:147], a[84:87], v[48:51]\n"
  "ds_read_b128 a[144:147], v235 offset:13440\n"
  "v_accvgpr_read_b32 v112, a172\n"
  "v_mfma_f32_16x16x32_bf16 v[48:51], a[120:123], a[88:91], v[48:51]\n"
  "ds_read_b128 a[120:123], v235 offset:14400\n"
  "v_accvgpr_read_b32 v113, a173\n"
  "v_mfma_f32_16x16x32_bf16 v[48:51], a[124:127], a[92:95], v[48:51]\n"
  "s_waitcnt lgkmcnt(5)\n"
  "ds_read_b128 a[124:127], v235 offset:14464\n"
  "ds_write_b64 v229, v[136:137] offset:2240\n"
  "v_accvgpr_read_b32 v116, a176\n"
  "v_pk_fma_f32 v[104:105], v[104:105], v[68:69], v[68:69] neg_lo:[0,0,1] neg_hi:[0,0,1]\n"
  "v_pk_fma_f32 v[108:109], v[108:109], v[68:69], v[68:69] neg_lo:[0,0,1] neg_hi:[0,0,1]\n"
  "v_pk_fma_f32 v[112:113], v[112:113], v[68:69], v[68:69] neg_lo:[0,0,1] neg_hi:[0,0,1]\n"
  "v_mfma_f32_16x16x32_bf16 v[96:99], v[12:15], a[132:135], v[96:99]\n"
  "ds_write_b64 v229, v[138:139] offset:2816\n"
  "v_accvgpr_read_b32 v117, a177\n"
  "v_accvgpr_read_b32 v106, a166\n"
  "v_pk_fma_f32 v[116:117], v[116:117], v[68:69], v[68:69] neg_lo:[0,0,1] neg_hi:[0,0,1]\n"
  "v_mfma_f32_16x16x32_bf16 v[100:103], v[12:15], a[128:131], v[100:103]\n"
  "v_accvgpr_read_b32 v107, a167\n"
  "v_accvgpr_read_b32 v110, a170\n"
  "v_mfma_f32_16x16x32_bf16 v[128:131], v[28:31], a[132:135], v[128:131]\n"
  "v_accvgpr_read_b32 v111, a171\n"
  "v_accvgpr_read_b32 v114, a174\n"
  "v_accvgpr_read_b32 v115, a175\n"
  "v_accvgpr_read_b32 v118, a178\n"
  "v_accvgpr_read_b32 v119, a179\n"
  "v_pk_fma_f32 v[106:107], v[106:107], v[70:71], v[70:71] neg_lo:[0,0,1] neg_hi:[0,0,1]\n"
  "v_pk_fma_f32 v[110:111], v[110:111], v[70:71], v[70:71] neg_lo:[0,0,1] neg_hi:[0,0,1]\n"
  "v_pk_fma_f32 v[114:115], v[114:115], v[70:71], v[70:71] neg_lo:[0,0,1] neg_hi:[0,0,1]\n"
  "v_pk_fma_f32 v[118:119], v[118:119], v[70:71], v[70:71] neg_lo:[0,0,1] neg_hi:[0,0,1]\n"
  "v_mfma_f32_16x16x32_bf16 v[132:135], v[28:31], a[128:131], v[132:135]\n"
  "v_cvt_pk_bf16_f32 v141, v145, v149\n"
  "v_cvt_pk_bf16_f32 v143, v147, v151\n"
  "v_cvt_pk_bf16_f32 v104, v104, v108\n"
  "v_cvt_pk_bf16_f32 v108, v105, v109\n"
  "v_cvt_pk_bf16_f32 v105, v112, v116\n"
  "v_pk_fma_f32 v[96:97], v[96:97], v[68:69], v[68:69] neg_lo:[0,0,1] neg_hi:[0,0,1]\n"
  "v_pk_fma_f32 v[98:99], v[98:99], v[70:71], v[70:71] neg_lo:[0,0,1] neg_hi:[0,0,1]\n"
  "v_mfma_f32_16x16x32_bf16 v[160:163], v[44:47], a[132:135], v[160:163]\n"
  "ds_write_b64 v229, v[140:141] offset:2528\n"
  "v_cvt_pk_bf16_f32 v109, v113, v117\n"
  "v_cvt_pk_bf16_f32 v106, v106, v110\n"
  "v_pk_fma_f32 v[100:101], v[100:101], v[68:69], v[68:69] neg_lo:[0,0,1] neg_hi:[0,0,1]\n"
  "v_pk_fma_f32 v[102:103], v[102:103], v[70:71], v[70:71] neg_lo:[0,0,1] neg_hi:[0,0,1]\n"
  "v_mfma_f32_16x16x32_bf16 v[164:167], v[44:47], a[128:131], v[164:167]\n"
  "v_cvt_pk_bf16_f32 v110, v107, v111\n"
  "v_cvt_pk_bf16_f32 v107, v114, v118\n"
  "v_mfma_f32_16x16x32_bf16 v[192:195], v[60:63], a[132:135], v[192:195]\n"
  "ds_write_b64 v229, v[142:143] offset:3104\n"
  "v_cvt_pk_bf16_f32 v111, v115, v119\n"
  "v_cvt_pk_bf16_f32 v236, v236, v238\n"
  "v_pk_fma_f32 v[128:129], v[128:129], v[72:73], v[72:73] neg_lo:[0,0,1] neg_hi:[0,0,1]\n"
  "v_pk_fma_f32 v[132:133], v[132:133], v[72:73], v[72:73] neg_lo:[0,0,1] neg_hi:[0,0,1]\n"
  "v_pk_fma_f32 v[130:131], v[130:131], v[74:75], v[74:75] neg_lo:[0,0,1] neg_hi:[0,0,1]\n"
  "v_pk_fma_f32 v[134:135], v[134:135], v[74:75], v[74:75] neg_lo:[0,0,1] neg_hi:[0,0,1]\n"
  "v_mfma_f32_16x16x32_bf16 v[196:199], v[60:63], a[128:131], v[196:199]\n"
  "s_nop 0\n"
  "s_waitcnt lgkmcnt(8)\n"
  "ds_write_b64 v229, v[104:105] offset:0\n"
  "v_cvt_pk_bf16_f32 v237, v237, v239\n"
  "v_cvt_pk_bf16_f32 v184, v184, v188\n"
  "v_pk_fma_f32 v[160:161], v[160:161], v[76:77], v[76:77] neg_lo:[0,0,1] neg_hi:[0,0,1]\n"
  "v_pk_fma_f32 v[162:163], v[162:163], v[78:79], v[78:79] neg_lo:[0,0,1] neg_hi:[0,0,1]\n"
  "v_mfma_f32_16x16x32_bf16 v[4:7], a[136:139], a[0:3], v[248:251]\n"
  "ds_write_b32 v233, v236 offset:20736\n"
  "v_mfma_f32_16x16x32_bf16 v[4:7], a[140:143], a[4:7], v[4:7]\n"
  "s_waitcnt lgkmcnt(8)\n"
  "ds_write_b32 v233, v237 offset:21024\n"
  "v_mfma_f32_16x16x32_bf16 v[4:7], a[148:151], a[8:11], v[4:7]\n"
  "v_cvt_pk_bf16_f32 v188, v185, v189\n"
  "v_cvt_pk_bf16_f32 v186, v186, v190\n"
  "v_mfma_f32_16x16x32_bf16 v[4:7], a[144:147], a[12:15], v[4:7]\n"
  "s_waitcnt lgkmcnt(7)\n"
  "v_cvt_pk_bf16_f32 v190, v187, v191\n"
  "v_accvgpr_read_b32 v240, a236\n"
  "v_mfma_f32_16x16x32_bf16 v[4:7], a[120:123], a[16:19], v[4:7]\n"
  "v_accvgpr_read_b32 v241, a237\n"
  "v_accvgpr_read_b32 v236, a238\n"
  "v_mfma_f32_16x16x32_bf16 v[4:7], a[124:127], a[20:23], v[4:7]\n"
  "ds_write_b64 v229, v[108:109] offset:288\n"
  "v_mfma_f32_16x16x32_bf16 v[20:23], a[124:127], a[44:47], v[248:251]\n"
  "v_accvgpr_read_b32 v237, a239\n"
  "v_accvgpr_read_b32 v242, a240\n"
  "v_mfma_f32_16x16x32_bf16 v[20:23], a[120:123], a[40:43], v[20:23]\n"
  "v_accvgpr_read_b32 v243, a241\n"
  "v_accvgpr_read_b32 v238, a242\n"
  "v_mfma_f32_16x16x32_bf16 v[20:23], a[144:147], a[36:39], v[20:23]\n"
  "ds_read_b64_tr_b16 a[132:133], v217 offset:14400\n"
  "v_accvgpr_read_b32 v239, a243\n"
  "v_mfma_f32_16x16x32_bf16 v[20:23], a[148:151], a[32:35], v[20:23]\n"
  "ds_read_b64_tr_b16 a[134:135], v218 offset:14400\n"
  "v_fma_f32 v64, v64, s73, v227\n"
  "v_mfma_f32_16x16x32_bf16 v[20:23], a[140:143], a[28:31], v[20:23]\n"
  "ds_read_b64_tr_b16 a[128:129], v217 offset:14416\n"
  "v_fma_f32 v65, v65, s73, v227\n"
  "v_mfma_f32_16x16x32_bf16 v[20:23], a[136:139], a[24:27], v[20:23]\n"
  "ds_read_b64_tr_b16 a[130:131], v218 offset:14416\n"
  "ds_write_b64 v229, v[106:107] offset:576\n"
  "v_fma_f32 v66, v66, s73, v227\n"
  "v_pk_fma_f32 v[192:193], v[192:193], v[80:81], v[80:81] neg_lo:[0,0,1] neg_hi:[0,0,1]\n"
  "v_pk_fma_f32 v[196:197], v[196:197], v[80:81], v[80:81] neg_lo:[0,0,1] neg_hi:[0,0,1]\n"
  "v_pk_fma_f32 v[194:195], v[194:195], v[82:83], v[82:83] neg_lo:[0,0,1] neg_hi:[0,0,1]\n"
  "v_pk_fma_f32 v[198:199], v[198:199], v[82:83], v[82:83] neg_lo:[0,0,1] neg_hi:[0,0,1]\n"
  "v_mfma_f32_16x16x32_bf16 v[36:39], a[136:139], a[48:51], v[248:251]\n"
  "v_exp_f32 v64, v64\n"
  "v_mfma_f32_16x16x32_bf16 v[36:39], a[140:143], a[52:55], v[36:39]\n"
  "v_exp_f32 v65, v65\n"
  "v_mfma_f32_16x16x32_bf16 v[36:39], a[148:151], a[56:59], v[36:39]\n"
  "v_exp_f32_e64 v66, v66\n"
  "v_mfma_f32_16x16x32_bf16 v[36:39], a[144:147], a[60:63], v[36:39]\n"
  "v_cvt_pk_bf16_f32 v185, v192, v196\n"
  "v_cvt_pk_bf16_f32 v187, v194, v198\n"
  "v_mfma_f32_16x16x32_bf16 v[36:39], a[120:123], a[64:67], v[36:39]\n"
  "v_cvt_pk_bf16_f32 v189, v193, v197\n"
  "v_cvt_pk_bf16_f32 v191, v195, v199\n"
  "v_mfma_f32_16x16x32_bf16 v[36:39], a[124:127], a[68:71], v[36:39]\n"
  "ds_write_b64 v229, v[110:111] offset:864\n"
  "v_fma_f32 v67, v67, s73, v227\n"
  "v_cvt_pk_bf16_f32 v152, v152, v156\n"
  "v_pk_fma_f32 v[164:165], v[164:165], v[76:77], v[76:77] neg_lo:[0,0,1] neg_hi:[0,0,1]\n"
  "v_pk_fma_f32 v[166:167], v[166:167], v[78:79], v[78:79] neg_lo:[0,0,1] neg_hi:[0,0,1]\n"
  "v_pk_fma_f32 v[240:241], v[240:241], v[84:85], v[84:85] neg_lo:[0,0,1] neg_hi:[0,0,1]\n"
  "v_pk_fma_f32 v[242:243], v[242:243], v[84:85], v[84:85] neg_lo:[0,0,1] neg_hi:[0,0,1]\n"
  "v_mfma_f32_16x16x32_bf16 v[52:55], a[124:127], a[92:95], v[248:251]\n"
  "ds_read_b64_tr_b16 a[124:125], v219 offset:14400\n"
  "v_cvt_pk_bf16_f32 v156, v153, v157\n"
  "v_mfma_f32_16x16x32_bf16 v[52:55], a[120:123], a[88:91], v[52:55]\n"
  "ds_read_b64_tr_b16 a[126:127], v220 offset:14400\n"
  "ds_read_b64_tr_b16 a[252:253], v230 offset:0\n"
  "v_mfma_f32_16x16x32_bf16 v[52:55], a[144:147], a[84:87], v[52:55]\n"
  "ds_read_b64_tr_b16 a[120:121], v219 offset:14416\n"
  "ds_read_b64_tr_b16 a[254:255], v230 offset:32\n"
  "v_mfma_f32_16x16x32_bf16 v[52:55], a[148:151], a[80:83], v[52:55]\n"
  "ds_read_b64_tr_b16 a[122:123], v220 offset:14416\n"
  "ds_read_b64_tr_b16 v[244:245], v230 offset:1120\n"
  "v_mfma_f32_16x16x32_bf16 v[52:55], a[140:143], a[76:79], v[52:55]\n"
  "v_exp_f32 v67, v67\n"
  "v_mfma_f32_16x16x32_bf16 v[52:55], a[136:139], a[72:75], v[52:55]\n"
  "s_waitcnt lgkmcnt(9)\n"
  "ds_read_b64_tr_b16 v[246:247], v230 offset:1152\n"
  "v_cvt_pk_bf16_f32 v153, v160, v164\n"
  "v_mfma_f32_16x16x32_bf16 a[244:247], a[156:159], a[132:135], 1.0\n"
  "ds_write_b64 v229, v[184:185] offset:15680\n"
  "v_cvt_pk_bf16_f32 v154, v154, v158\n"
  "v_cvt_pk_bf16_f32 v158, v155, v159\n"
  "v_pk_fma_f32 v[236:237], v[236:237], v[86:87], v[86:87] neg_lo:[0,0,1] neg_hi:[0,0,1]\n"
  "v_pk_fma_f32 v[238:239], v[238:239], v[86:87], v[86:87] neg_lo:[0,0,1] neg_hi:[0,0,1]\n"
  "v_mfma_f32_16x16x32_bf16 a[248:251], a[156:159], a[128:131], 1.0\n"
  "ds_write_b64 v229, v[186:187] offset:16256\n"
  "v_cvt_pk_bf16_f32 v155, v162, v166\n"
  "v_cvt_pk_bf16_f32 v64, v64, v65\n"
  "v_mfma_f32_16x16x32_bf16 v[200:203], v[56:59], a[132:135], 1.0\n"
  "s_nop 0\n"
  "s_waitcnt lgkmcnt(5)\n"
  "buffer_store_dwordx4 a[252:255], v228, s[52:55], s80 offen offset:0 nt\n"
  "ds_read_b64_tr_b16 a[140:141], v217 offset:13376\n"
  "ds_write_b64 v229, v[188:189] offset:15968\n"
  "v_cvt_pk_bf16_f32 v65, v66, v67\n"
  "v_cvt_pk_bf16_f32 v157, v161, v165\n"
  "v_mfma_f32_16x16x32_bf16 v[204:207], v[56:59], a[128:131], 1.0\n"
  "ds_read_b64_tr_b16 a[142:143], v218 offset:13376\n"
  "ds_read_b64_tr_b16 a[252:253], v230 offset:2240\n"
  "v_mfma_f32_16x16x32_bf16 v[168:171], v[40:43], a[132:135], 1.0\n"
  "ds_read_b64_tr_b16 a[136:137], v217 offset:13392\n"
  "ds_read_b64_tr_b16 a[254:255], v230 offset:2272\n"
  "ds_write_b64 v229, v[190:191] offset:16544\n"
  "v_mfma_f32_16x16x32_bf16 v[172:175], v[40:43], a[128:131], 1.0\n"
  "ds_read_b64_tr_b16 a[138:139], v218 offset:13392\n"
  "ds_write_b64 v229, v[152:153] offset:13440\n"
  "v_cvt_pk_bf16_f32 v159, v163, v167\n"
  "v_mfma_f32_16x16x32_bf16 v[136:139], v[24:27], a[132:135], 1.0\n"
  "ds_write_b64 v229, v[154:155] offset:14016\n"
  "v_mfma_f32_16x16x32_bf16 v[140:143], v[24:27], a[128:131], 1.0\n"
  "ds_write_b64 v231, v[64:65] offset:2048\n"
  "v_cvt_pk_bf16_f32 v120, v120, v124\n"
  "v_cvt_pk_bf16_f32 v124, v121, v125\n"
  "v_mfma_f32_16x16x32_bf16 v[104:107], v[8:11], a[132:135], 1.0\n"
  "s_nop 0\n"
  "s_waitcnt lgkmcnt(13)\n"
  "buffer_store_dwordx4 v[244:247], v228, s[52:55], s80 offen offset:3072 nt\n"
  "ds_write_b64 v229, v[156:157] offset:13728\n"
  "v_cvt_pk_bf16_f32 v121, v128, v132\n"
  "v_cvt_pk_bf16_f32 v122, v122, v126\n"
  "v_cvt_pk_bf16_f32 v126, v123, v127\n"
  "v_mfma_f32_16x16x32_bf16 v[108:111], v[8:11], a[128:131], 1.0\n"
  "ds_read_b64_tr_b16 v[244:245], v230 offset:3360\n"
  "ds_read_b64_tr_b16 v[246:247], v230 offset:3392\n"
  "v_mfma_f32_16x16x32_bf16 v[112:115], v[8:11], a[124:127], 1.0\n"
  "ds_write_b64 v229, v[158:159] offset:14304\n"
  "v_cvt_pk_bf16_f32 v123, v130, v134\n"
  "v_cvt_pk_bf16_f32 v125, v129, v133\n"
  "v_mfma_f32_16x16x32_bf16 v[116:119], v[8:11], a[120:123], 1.0\n"
  "ds_write_b64 v229, v[120:121] offset:11200\n"
  "v_cvt_pk_bf16_f32 v127, v131, v135\n"
  "v_cvt_pk_bf16_f32 v88, v88, v92\n"
  "v_mfma_f32_16x16x32_bf16 v[144:147], v[24:27], a[124:127], 1.0\n"
  "ds_read_b64_tr_b16 a[132:133], v219 offset:13376\n"
  "ds_write_b64 v229, v[122:123] offset:11776\n"
  "v_cvt_pk_bf16_f32 v92, v89, v93\n"
  "v_mfma_f32_16x16x32_bf16 v[148:151], v[24:27], a[120:123], 1.0\n"
  "s_nop 0\n"
  "ds_read_b64_tr_b16 a[134:135], v220 offset:13376\n"
  "v_cvt_pk_bf16_f32 v89, v96, v100\n"
  "v_mfma_f32_16x16x32_bf16 v[176:179], v[40:43], a[124:127], 1.0\n"
  "s_waitcnt lgkmcnt(13)\n"
  "buffer_store_dwordx4 a[252:255], v228, s[52:55], s81 offen offset:0 nt\n"
  "ds_read_b64_tr_b16 a[128:129], v219 offset:13392\n"
  "ds_write_b64 v229, v[124:125] offset:11488\n"
  "v_cvt_pk_bf16_f32 v90, v90, v94\n"
  "v_cvt_pk_bf16_f32 v94, v91, v95\n"
  "v_mfma_f32_16x16x32_bf16 v[180:183], v[40:43], a[120:123], 1.0\n"
  "ds_read_b64_tr_b16 a[130:131], v220 offset:13392\n"
  "ds_write_b64 v229, v[126:127] offset:12064\n"
  "v_cvt_pk_bf16_f32 v91, v98, v102\n"
  "v_mfma_f32_16x16x32_bf16 v[208:211], v[56:59], a[124:127], 1.0\n"
  "s_nop 0\n"
  "v_cvt_pk_bf16_f32 v93, v97, v101\n"
  "v_cvt_pk_bf16_f32 v95, v99, v103\n"
  "v_mfma_f32_16x16x32_bf16 v[212:215], v[56:59], a[120:123], 1.0\n"
  "s_waitcnt lgkmcnt(15)\n"
  "ds_write_b64 v229, v[88:89] offset:8960\n"
  "v_fma_f32 v0, v0, s73, v223\n"
  "v_fma_f32 v1, v1, s73, v223\n"
  "v_mfma_f32_16x16x32_bf16 a[236:239], a[156:159], a[140:143], 1.0\n"
  "ds_write_b64 v229, v[90:91] offset:9536\n"
  "v_exp_f32_e64 v0, v0\n"
  "v_mfma_f32_16x16x32_bf16 a[240:243], a[156:159], a[136:139], 1.0\n"
  "s_nop 0\n"
  "ds_write_b64 v229, v[92:93] offset:9248\n"
  "v_mfma_f32_16x16x32_bf16 v[184:187], v[56:59], a[140:143], 1.0\n"
  "s_waitcnt lgkmcnt(12)\n"
  "buffer_store_dwordx4 v[244:247], v228, s[52:55], s81 offen offset:3072 nt\n"
  "ds_write_b64 v229, v[94:95] offset:9824\n"
  "v_exp_f32_e64 v1, v1\n"
  "v_fma_f32 v2, v2, s73, v223\n"
  "v_mfma_f32_16x16x32_bf16 v[188:191], v[56:59], a[136:139], 1.0\n"
  "v_exp_f32 v2, v2\n"
  "v_cvt_pk_bf16_f32 v240, v240, v242\n"
  "v_cvt_pk_bf16_f32 v241, v241, v243\n"
  "v_cvt_pk_bf16_f32 v236, v236, v238\n"
  "v_mfma_f32_16x16x32_bf16 v[152:155], v[40:43], a[140:143], 1.0\n"
  "s_mov_b32 m0, s67\n"
  "ds_write_b32 v233, v240 offset:19040\n"
  "v_fma_f32 v3, v3, s73, v223\n"
  "v_cvt_pk_bf16_f32 v12, v0, v1\n"
  "v_cvt_pk_bf16_f32 v237, v237, v239\n"
  "v_mfma_f32_16x16x32_bf16 v[156:159], v[40:43], a[136:139], 1.0\n"
  "s_waitcnt lgkmcnt(6)\n"
  "s_barrier\n"
  "ds_read_b128 a[124:127], v216 offset:0\n"
  "v_mfma_f32_16x16x32_bf16 v[120:123], v[24:27], a[140:143], 1.0\n"
  "ds_read_b64 a[160:161], v231 offset:2048\n"
  "ds_write_b32 v233, v241 offset:19328\n"
  "v_fma_f32 v16, v16, s73, v224\n"
  "v_accvgpr_read_b32 v240, a196\n"
  "v_mfma_f32_16x16x32_bf16 v[124:127], v[24:27], a[136:139], 1.0\n"
  "ds_read_b64 a[162:163], v232 offset:2048\n"
  "ds_write_b32 v233, v236 offset:19616\n"
  "v_fma_f32 v17, v17, s73, v224\n"
  "v_accvgpr_read_b32 v241, a197\n"
  "v_pk_fma_f32 v[240:241], v[240:241], v[76:77], v[76:77] neg_lo:[0,0,1] neg_hi:[0,0,1]\n"
  "v_mfma_f32_16x16x32_bf16 v[88:91], v[8:11], a[140:143], 1.0\n"
  "buffer_load_dwordx4 v221, s[40:43], 0 offen offset:0 lds\n"
  "v_mfma_f32_16x16x32_bf16 v[92:95], v[8:11], a[136:139], 1.0\n"
  "s_nop 0\n"
  "ds_read_b128 a[120:123], v216 offset:64\n"
  "v_fma_f32 v18, v18, s73, v224\n"
  "v_mfma_f32_16x16x32_bf16 v[96:99], v[8:11], a[132:135], 1.0\n"
  "s_addk_i32 m0, 896\n"
  "ds_read_b128 a[140:143], v216 offset:1024\n"
  "ds_write_b32 v233, v237 offset:19904\n"
  "v_fma_f32 v19, v19, s73, v224\n"
  "v_accvgpr_read_b32 v242, a200\n"
  "v_mfma_f32_16x16x32_bf16 v[100:103], v[8:11], a[128:131], 1.0\n"
  "ds_read_b128 a[136:139], v216 offset:1088\n"
  "v_fma_f32 v32, v32, s73, v225\n"
  "v_accvgpr_read_b32 v243, a201\n"
  "v_accvgpr_read_b32 v236, a204\n"
  "v_accvgpr_read_b32 v237, a205\n"
  "v_pk_fma_f32 v[242:243], v[242:243], v[76:77], v[76:77] neg_lo:[0,0,1] neg_hi:[0,0,1]\n"
  "v_pk_fma_f32 v[236:237], v[236:237], v[76:77], v[76:77] neg_lo:[0,0,1] neg_hi:[0,0,1]\n"
  "v_mfma_f32_16x16x32_bf16 v[128:131], v[24:27], a[132:135], 1.0\n"
  "buffer_load_dwordx4 v221, s[40:43], 0 offen offset:128 lds\n"
  "v_fma_f32 v33, v33, s73, v225\n"
  "v_mfma_f32_16x16x32_bf16 v[132:135], v[24:27], a[128:131], 1.0\n"
  "s_addk_i32 m0, 896\n"
  "ds_read_b128 a[144:147], v216 offset:2048\n"
  "v_fma_f32 v34, v34, s73, v225\n"
  "v_mfma_f32_16x16x32_bf16 v[160:163], v[40:43], a[132:135], 1.0\n"
  "s_nop 0\n"
  "ds_read_b128 a[148:151], v216 offset:2112\n"
  "v_fma_f32 v35, v35, s73, v225\n"
  "v_accvgpr_read_b32 v238, a208\n"
  "v_accvgpr_read_b32 v239, a209\n"
  "v_cvt_pk_bf16_f32 v240, v240, v242\n"
  "v_pk_fma_f32 v[238:239], v[238:239], v[76:77], v[76:77] neg_lo:[0,0,1] neg_hi:[0,0,1]\n"
  "v_mfma_f32_16x16x32_bf16 v[164:167], v[40:43], a[128:131], 1.0\n"
  "v_exp_f32_e64 v3, v3\n"
  "v_cvt_pk_bf16_f32 v242, v241, v243\n"
  "v_cvt_pk_bf16_f32 v241, v236, v238\n"
  "v_cvt_pk_bf16_f32 v243, v237, v239\n"
  "v_mfma_f32_16x16x32_bf16 v[192:195], v[56:59], a[132:135], 1.0\n"
  "buffer_load_dwordx4 v221, s[40:43], 0 offen offset:256 lds\n"
  "v_cvt_pk_bf16_f32 v13, v2, v3\n"
  "v_mfma_f32_16x16x32_bf16 v[196:199], v[56:59], a[128:131], 1.0\n"
  "s_waitcnt lgkmcnt(5)\n"
  "v_exp_f32 v16, v16\n"
  "v_accvgpr_read_b32 v236, a198\n"
  "v_accvgpr_read_b32 v237, a199\n"
  "v_accvgpr_read_b32 v238, a202\n"
  "v_pk_fma_f32 v[236:237], v[236:237], v[78:79], v[78:79] neg_lo:[0,0,1] neg_hi:[0,0,1]\n"
  "v_mfma_f32_16x16x32_bf16 v[64:67], a[124:127], a[96:99], v[248:251]\n"
  "s_add_u32 m0, s59, -128\n"
  "v_exp_f32 v17, v17\n"
  "v_mfma_f32_16x16x32_bf16 v[64:67], a[120:123], a[100:103], v[64:67]\n"
  "s_waitcnt lgkmcnt(2)\n"
  "v_exp_f32 v18, v18\n"
  "v_mfma_f32_16x16x32_bf16 v[64:67], a[140:143], a[104:107], v[64:67]\n"
  "buffer_load_dwordx4 v221, s[32:35], 0 offen offset:128 lds\n"
  "v_cvt_pk_bf16_f32 v28, v16, v17\n"
  "v_mfma_f32_16x16x32_bf16 v[64:67], a[136:139], a[108:111], v[64:67]\n"
  "s_waitcnt lgkmcnt(0)\n"
  "v_exp_f32 v19, v19\n"
  "v_mfma_f32_16x16x32_bf16 v[64:67], a[144:147], a[112:115], v[64:67]\n"
  "s_addk_i32 m0, 1024\n"
  "v_exp_f32 v32, v32\n"
  "v_mfma_f32_16x16x32_bf16 v[64:67], a[148:151], a[116:119], v[64:67]\n"
  "s_mov_b64 s[32:33], s[44:45]\n"
  "v_exp_f32 v33, v33\n"
  "v_mfma_f32_16x16x32_bf16 v[0:3], a[148:151], a[20:23], v[248:251]\n"
  "buffer_load_dwordx4 v221, s[44:47], 0 offen offset:128 lds\n"
  "v_cvt_pk_bf16_f32 v29, v18, v19\n"
  "v_mfma_f32_16x16x32_bf16 v[0:3], a[144:147], a[16:19], v[0:3]\n"
  "s_addk_i32 m0, 896\n"
  "v_exp_f32 v34, v34\n"
  "v_mfma_f32_16x16x32_bf16 v[0:3], a[136:139], a[12:15], v[0:3]\n"
  "s_add_u32 s40, s40, s31\n"
  "v_exp_f32 v35, v35\n"
  "v_mfma_f32_16x16x32_bf16 v[0:3], a[140:143], a[8:11], v[0:3]\n"
  "s_addc_u32 s41, s41, 0\n"
  "v_cvt_pk_bf16_f32 v44, v32, v33\n"
  "v_cvt_pk_bf16_f32 v45, v34, v35\n"
  "v_mfma_f32_16x16x32_bf16 v[0:3], a[120:123], a[4:7], v[0:3]\n"
  "buffer_load_dwordx4 v221, s[44:47], 0 offen offset:256 lds\n"
  "v_fma_f32 v48, v48, s73, v226\n"
  "v_mfma_f32_16x16x32_bf16 v[0:3], a[124:127], a[0:3], v[0:3]\n"
  "s_add_u32 s44, s44, s31\n"
  "v_exp_f32 v48, v48\n"
  "v_accvgpr_read_b32 v239, a203\n"
  "v_mfma_f32_16x16x32_bf16 v[16:19], a[124:127], a[24:27], v[248:251]\n"
  "s_addc_u32 s45, s45, 0\n"
  "v_fma_f32 v49, v49, s73, v226\n"
  "v_fma_f32 v50, v50, s73, v226\n"
  "v_mfma_f32_16x16x32_bf16 v[16:19], a[120:123], a[28:31], v[16:19]\n"
  "v_exp_f32 v49, v49\n"
  "v_mfma_f32_16x16x32_bf16 v[16:19], a[140:143], a[32:35], v[16:19]\n"
  "ds_read_b128 a[132:135], v235 offset:0\n"
  "v_fma_f32 v51, v51, s73, v226\n"
  "v_mfma_f32_16x16x32_bf16 v[16:19], a[136:139], a[36:39], v[16:19]\n"
  "ds_read_b128 a[128:131], v235 offset:64\n"
  "v_cvt_pk_bf16_f32 v60, v48, v49\n"
  "v_mfma_f32_16x16x32_bf16 v[16:19], a[144:147], a[40:43], v[16:19]\n"
  "v_exp_f32 v50, v50\n"
  "v_mfma_f32_16x16x32_bf16 v[16:19], a[148:151], a[44:47], v[16:19]\n"
  "v_exp_f32 v51, v51\n"
  "v_mfma_f32_16x16x32_bf16 v[32:35], a[148:151], a[68:71], v[248:251]\n"
  "v_cvt_pk_bf16_f32 v61, v50, v51\n"
  "v_fma_f32 v4, v4, s73, v223\n"
  "v_mfma_f32_16x16x32_bf16 v[32:35], a[144:147], a[64:67], v[32:35]\n"
  "v_exp_f32 v4, v4\n"
  "v_mfma_f32_16x16x32_bf16 v[32:35], a[136:139], a[60:63], v[32:35]\n"
  "v_fma_f32 v5, v5, s73, v223\n"
  "v_fma_f32 v6, v6, s73, v223\n"
  "v_mfma_f32_16x16x32_bf16 v[32:35], a[140:143], a[56:59], v[32:35]\n"
  "v_exp_f32 v5, v5\n"
  "v_mfma_f32_16x16x32_bf16 v[32:35], a[120:123], a[52:55], v[32:35]\n"
  "v_exp_f32 v6, v6\n"
  "v_mfma_f32_16x16x32_bf16 v[32:35], a[124:127], a[48:51], v[32:35]\n"
  "v_fma_f32 v7, v7, s73, v223\n"
  "v_cvt_pk_bf16_f32 v14, v4, v5\n"
  "v_mfma_f32_16x16x32_bf16 v[48:51], a[124:127], a[72:75], v[248:251]\n"
  "ds_read_b128 a[124:127], v235 offset:1024\n"
  "v_fma_f32 v20, v20, s73, v224\n"
  "v_mfma_f32_16x16x32_bf16 v[48:51], a[120:123], a[76:79], v[48:51]\n"
  "ds_read_b128 a[120:123], v235 offset:1088\n"
  "v_fma_f32 v21, v21, s73, v224\n"
  "v_mfma_f32_16x16x32_bf16 v[48:51], a[140:143], a[80:83], v[48:51]\n"
  "ds_read_b128 a[140:143], v235 offset:2048\n"
  "v_fma_f32 v22, v22, s73, v224\n"
  "v_mfma_f32_16x16x32_bf16 v[48:51], a[136:139], a[84:87], v[48:51]\n"
  "ds_read_b128 a[136:139], v235 offset:2112\n"
  "v_fma_f32 v23, v23, s73, v224\n"
  "v_mfma_f32_16x16x32_bf16 v[48:51], a[144:147], a[88:91], v[48:51]\n"
  "v_exp_f32 v7, v7\n"
  "v_mfma_f32_16x16x32_bf16 v[48:51], a[148:151], a[92:95], v[48:51]\n"
  "s_waitcnt lgkmcnt(4)\n"
  "v_cvt_pk_bf16_f32 v15, v6, v7\n"
  "v_fma_f32 v36, v36, s73, v225\n"
  "v_mfma_f32_16x16x32_bf16 v[4:7], a[132:135], a[0:3], v[248:251]\n"
  "v_exp_f32 v20, v20\n"
  "v_mfma_f32_16x16x32_bf16 v[4:7], a[128:131], a[4:7], v[4:7]\n"
  "s_waitcnt lgkmcnt(2)\n"
  "v_exp_f32 v21, v21\n"
  "v_mfma_f32_16x16x32_bf16 v[4:7], a[124:127], a[8:11], v[4:7]\n"
  "v_exp_f32 v22, v22\n"
  "v_mfma_f32_16x16x32_bf16 v[4:7], a[120:123], a[12:15], v[4:7]\n"
  "s_waitcnt lgkmcnt(0)\n"
  "v_exp_f32_e64 v23, v23\n"
  "v_mfma_f32_16x16x32_bf16 v[4:7], a[140:143], a[16:19], v[4:7]\n"
  "v_cvt_pk_bf16_f32 v30, v20, v21\n"
  "v_cvt_pk_bf16_f32 v31, v22, v23\n"
  "v_mfma_f32_16x16x32_bf16 v[4:7], a[136:139], a[20:23], v[4:7]\n"
  "v_exp_f32 v36, v36\n"
  "v_fma_f32 v37, v37, s73, v225\n"
  "v_exp_f32 v37, v37\n"
  "v_mfma_f32_16x16x32_bf16 v[20:23], a[136:139], a[44:47], v[248:251]\n"
  "v_fma_f32 v38, v38, s73, v225\n"
  "v_fma_f32 v39, v39, s73, v225\n"
  "v_mfma_f32_16x16x32_bf16 v[20:23], a[140:143], a[40:43], v[20:23]\n"
  "v_exp_f32 v38, v38\n"
  "v_mfma_f32_16x16x32_bf16 v[20:23], a[120:123], a[36:39], v[20:23]\n"
  "ds_read_b64_tr_b16 a[144:145], v217 offset:2048\n"
  "v_cvt_pk_bf16_f32 v46, v36, v37\n"
  "v_mfma_f32_16x16x32_bf16 v[20:23], a[124:127], a[32:35], v[20:23]\n"
  "ds_read_b64_tr_b16 a[146:147], v218 offset:2048\n"
  "v_fma_f32 v52, v52, s73, v226\n"
  "v_mfma_f32_16x16x32_bf16 v[20:23], a[128:131], a[28:31], v[20:23]\n"
  "ds_read_b64_tr_b16 a[148:149], v217 offset:2064\n"
  "v_exp_f32 v39, v39\n"
  "v_mfma_f32_16x16x32_bf16 v[20:23], a[132:135], a[24:27], v[20:23]\n"
  "ds_read_b64_tr_b16 a[150:151], v218 offset:2064\n"
  "ds_write_b64 v229, v[240:241] offset:4480\n"
  "v_cvt_pk_bf16_f32 v47, v38, v39\n"
  "v_exp_f32 v52, v52\n"
  "v_pk_fma_f32 v[238:239], v[238:239], v[78:79], v[78:79] neg_lo:[0,0,1] neg_hi:[0,0,1]\n"
  "v_mfma_f32_16x16x32_bf16 v[36:39], a[132:135], a[48:51], v[248:251]\n"
  "v_fma_f32 v53, v53, s73, v226\n"
  "v_fma_f32 v54, v54, s73, v226\n"
  "v_mfma_f32_16x16x32_bf16 v[36:39], a[128:131], a[52:55], v[36:39]\n"
  "v_exp_f32 v53, v53\n"
  "v_mfma_f32_16x16x32_bf16 v[36:39], a[124:127], a[56:59], v[36:39]\n"
  "v_exp_f32 v54, v54\n"
  "v_mfma_f32_16x16x32_bf16 v[36:39], a[120:123], a[60:63], v[36:39]\n"
  "v_fma_f32 v55, v55, s73, v226\n"
  "v_cvt_pk_bf16_f32 v62, v52, v53\n"
  "v_mfma_f32_16x16x32_bf16 v[36:39], a[140:143], a[64:67], v[36:39]\n"
  "v_exp_f32 v55, v55\n"
  "v_mfma_f32_16x16x32_bf16 v[36:39], a[136:139], a[68:71], v[36:39]\n"
  "v_cvt_pk_bf16_f32 v63, v54, v55\n"
  "v_accvgpr_read_b32 v240, a206\n"
  "v_mfma_f32_16x16x32_bf16 v[52:55], a[136:139], a[92:95], v[248:251]\n"
  "ds_read_b64_tr_b16 a[136:137], v219 offset:2048\n"
  "v_accvgpr_read_b32 v241, a207\n"
  "v_mfma_f32_16x16x32_bf16 v[52:55], a[140:143], a[88:91], v[52:55]\n"
  "ds_read_b64_tr_b16 a[138:139], v220 offset:2048\n"
  "v_cvt_pk_bf16_f32 v236, v236, v238\n"
  "v_mfma_f32_16x16x32_bf16 v[52:55], a[120:123], a[84:87], v[52:55]\n"
  "ds_read_b64_tr_b16 a[140:141], v219 offset:2064\n"
  "v_cvt_pk_bf16_f32 v238, v237, v239\n"
  "v_mfma_f32_16x16x32_bf16 v[52:55], a[124:127], a[80:83], v[52:55]\n"
  "ds_read_b64_tr_b16 a[142:143], v220 offset:2064\n"
  "v_fma_f32 v64, v64, s73, v227\n"
  "v_mfma_f32_16x16x32_bf16 v[52:55], a[128:131], a[76:79], v[52:55]\n"
  "v_exp_f32 v64, v64\n"
  "v_mfma_f32_16x16x32_bf16 v[52:55], a[132:135], a[72:75], v[52:55]\n"
  "s_waitcnt lgkmcnt(5)\n"
  "ds_write_b64 v229, v[242:243] offset:4768\n"
  "v_fma_f32 v65, v65, s73, v227\n"
  "v_fma_f32 v66, v66, s73, v227\n"
  "v_pk_fma_f32 v[240:241], v[240:241], v[78:79], v[78:79] neg_lo:[0,0,1] neg_hi:[0,0,1]\n"
  "v_mfma_f32_16x16x32_bf16 v[104:107], v[12:15], a[144:147], v[104:107]\n"
  "v_accvgpr_read_b32 v242, a210\n"
  "v_accvgpr_read_b32 v243, a211\n"
  "v_exp_f32_e64 v65, v65\n"
  "v_pk_fma_f32 v[242:243], v[242:243], v[78:79], v[78:79] neg_lo:[0,0,1] neg_hi:[0,0,1]\n"
  "v_mfma_f32_16x16x32_bf16 v[108:111], v[12:15], a[148:151], v[108:111]\n"
  "v_cvt_pk_bf16_f32 v237, v240, v242\n"
  "v_cvt_pk_bf16_f32 v239, v241, v243\n"
  "v_mfma_f32_16x16x32_bf16 v[136:139], v[28:31], a[144:147], v[136:139]\n"
  "ds_read_b64_tr_b16 a[120:121], v217 offset:0\n"
  "ds_write_b64 v229, v[236:237] offset:5056\n"
  "v_accvgpr_read_b32 v240, a212\n"
  "v_mfma_f32_16x16x32_bf16 v[140:143], v[28:31], a[148:151], v[140:143]\n"
  "ds_read_b64_tr_b16 a[122:123], v218 offset:0\n"
  "v_accvgpr_read_b32 v241, a213\n"
  "v_mfma_f32_16x16x32_bf16 v[168:171], v[44:47], a[144:147], v[168:171]\n"
  "ds_read_b64_tr_b16 a[124:125], v217 offset:1024\n"
  "ds_write_b64 v229, v[238:239] offset:5344\n"
  "v_accvgpr_read_b32 v242, a216\n"
  "v_pk_fma_f32 v[240:241], v[240:241], v[80:81], v[80:81] neg_lo:[0,0,1] neg_hi:[0,0,1]\n"
  "v_mfma_f32_16x16x32_bf16 v[172:175], v[44:47], a[148:151], v[172:175]\n"
  "ds_read_b64_tr_b16 a[126:127], v218 offset:1024\n"
  "v_accvgpr_read_b32 v243, a217\n"
  "v_accvgpr_read_b32 v236, a220\n"
  "v_accvgpr_read_b32 v237, a221\n"
  "v_accvgpr_read_b32 v238, a224\n"
  "v_pk_fma_f32 v[242:243], v[242:243], v[80:81], v[80:81] neg_lo:[0,0,1] neg_hi:[0,0,1]\n"
  "v_pk_fma_f32 v[236:237], v[236:237], v[80:81], v[80:81] neg_lo:[0,0,1] neg_hi:[0,0,1]\n"
  "v_mfma_f32_16x16x32_bf16 v[200:203], v[60:63], a[144:147], v[200:203]\n"
  "v_accvgpr_read_b32 v239, a225\n"
  "v_cvt_pk_bf16_f32 v240, v240, v242\n"
  "v_mfma_f32_16x16x32_bf16 v[204:207], v[60:63], a[148:151], v[204:207]\n"
  "ds_read_b64_tr_b16 a[252:253], v230 offset:4480\n"
  "ds_read_b64_tr_b16 a[254:255], v230 offset:4512\n"
  "v_cvt_pk_bf16_f32 v242, v241, v243\n"
  "v_exp_f32 v66, v66\n"
  "v_pk_fma_f32 v[238:239], v[238:239], v[80:81], v[80:81] neg_lo:[0,0,1] neg_hi:[0,0,1]\n"
  "v_mfma_f32_16x16x32_bf16 a[244:247], a[160:163], a[144:147], a[244:247]\n"
  "v_cvt_pk_bf16_f32 v241, v236, v238\n"
  "v_cvt_pk_bf16_f32 v243, v237, v239\n"
  "v_mfma_f32_16x16x32_bf16 a[248:251], a[160:163], a[148:151], a[248:251]\n"
  "s_waitcnt lgkmcnt(9)\n"
  "ds_write_b64 v229, v[240:241] offset:6720\n"
  "v_accvgpr_read_b32 v236, a214\n"
  "v_accvgpr_read_b32 v237, a215\n"
  "v_pk_fma_f32 v[236:237], v[236:237], v[82:83], v[82:83] neg_lo:[0,0,1] neg_hi:[0,0,1]\n"
  "v_mfma_f32_16x16x32_bf16 v[208:211], v[60:63], a[136:139], v[208:211]\n"
  "ds_write_b64 v229, v[242:243] offset:7008\n"
  "v_accvgpr_read_b32 v238, a218\n"
  "v_accvgpr_read_b32 v239, a219\n"
  "v_pk_fma_f32 v[238:239], v[238:239], v[82:83], v[82:83] neg_lo:[0,0,1] neg_hi:[0,0,1]\n"
  "v_mfma_f32_16x16x32_bf16 v[212:215], v[60:63], a[140:143], v[212:215]\n"
  "v_accvgpr_read_b32 v240, a222\n"
  "v_accvgpr_read_b32 v241, a223\n"
  "v_mfma_f32_16x16x32_bf16 v[176:179], v[44:47], a[136:139], v[176:179]\n"
  "ds_read_b64_tr_b16 a[128:129], v217 offset:16\n"
  "v_accvgpr_read_b32 v242, a226\n"
  "v_accvgpr_read_b32 v243, a227\n"
  "v_cvt_pk_bf16_f32 v236, v236, v238\n"
  "v_cvt_pk_bf16_f32 v238, v237, v239\n"
  "v_pk_fma_f32 v[240:241], v[240:241], v[82:83], v[82:83] neg_lo:[0,0,1] neg_hi:[0,0,1]\n"
  "v_pk_fma_f32 v[242:243], v[242:243], v[82:83], v[82:83] neg_lo:[0,0,1] neg_hi:[0,0,1]\n"
  "v_mfma_f32_16x16x32_bf16 v[180:183], v[44:47], a[140:143], v[180:183]\n"
  "ds_read_b64_tr_b16 a[130:131], v218 offset:16\n"
  "v_cvt_pk_bf16_f32 v237, v240, v242\n"
  "v_mfma_f32_16x16x32_bf16 v[144:147], v[28:31], a[136:139], v[144:147]\n"
  "ds_read_b64_tr_b16 a[132:133], v217 offset:1040\n"
  "ds_write_b64 v229, v[236:237] offset:7296\n"
  "v_cvt_pk_bf16_f32 v239, v241, v243\n"
  "v_mfma_f32_16x16x32_bf16 v[148:151], v[28:31], a[140:143], v[148:151]\n"
  "ds_read_b64_tr_b16 a[134:135], v218 offset:1040\n"
  "ds_write_b64 v229, v[238:239] offset:7584\n"
  "v_accvgpr_read_b32 v240, a228\n"
  "v_mfma_f32_16x16x32_bf16 v[112:115], v[12:15], a[136:139], v[112:115]\n"
  "s_nop 0\n"
  "v_accvgpr_read_b32 v241, a229\n"
  "v_accvgpr_read_b32 v236, a230\n"
  "v_mfma_f32_16x16x32_bf16 v[116:119], v[12:15], a[140:143], v[116:119]\n"
  "s_waitcnt lgkmcnt(10)\n"
  "v_accvgpr_read_b32 v237, a231\n"
  "v_accvgpr_read_b32 v242, a232\n"
  "v_accvgpr_read_b32 v243, a233\n"
  "v_accvgpr_read_b32 v238, a234\n"
  "v_accvgpr_read_b32 v239, a235\n"
  "v_pk_fma_f32 v[240:241], v[240:241], v[84:85], v[84:85] neg_lo:[0,0,1] neg_hi:[0,0,1]\n"
  "v_pk_fma_f32 v[242:243], v[242:243], v[84:85], v[84:85] neg_lo:[0,0,1] neg_hi:[0,0,1]\n"
  "v_pk_fma_f32 v[236:237], v[236:237], v[86:87], v[86:87] neg_lo:[0,0,1] neg_hi:[0,0,1]\n"
  "v_pk_fma_f32 v[238:239], v[238:239], v[86:87], v[86:87] neg_lo:[0,0,1] neg_hi:[0,0,1]\n"
  "v_mfma_f32_16x16x32_bf16 a[164:167], v[8:11], a[120:123], 1.0\n"
  "v_fma_f32 v67, v67, s73, v227\n"
  "v_cvt_pk_bf16_f32 v240, v240, v242\n"
  "v_mfma_f32_16x16x32_bf16 a[164:167], v[12:15], a[124:127], a[164:167]\n"
  "v_exp_f32_e64 v67, v67\n"
  "v_mfma_f32_16x16x32_bf16 a[180:183], v[24:27], a[120:123], 1.0\n"
  "ds_write_b32 v233, v240 offset:17920\n"
  "v_mfma_f32_16x16x32_bf16 a[180:183], v[28:31], a[124:127], a[180:183]\n"
  "v_cvt_pk_bf16_f32 v241, v241, v243\n"
  "v_cvt_pk_bf16_f32 v236, v236, v238\n"
  "v_cvt_pk_bf16_f32 v237, v237, v239\n"
  "v_cvt_pk_bf16_f32 v64, v64, v65\n"
  "v_cvt_pk_bf16_f32 v65, v66, v67\n"
  "v_mfma_f32_16x16x32_bf16 a[196:199], v[40:43], a[120:123], 1.0\n"
  "s_mov_b32 m0, s68\n"
  "ds_read_b64_tr_b16 a[136:137], v219 offset:0\n"
  "v_fma_f32 v0, v0, s73, v223\n"
  "v_mfma_f32_16x16x32_bf16 a[196:199], v[44:47], a[124:127], a[196:199]\n"
  "s_nop 0\n"
  "ds_read_b64_tr_b16 a[138:139], v220 offset:0\n"
  "ds_read_b64_tr_b16 v[244:245], v230 offset:5600\n"
  "ds_write_b32 v233, v241 offset:18208\n"
  "v_fma_f32 v1, v1, s73, v223\n"
  "v_mfma_f32_16x16x32_bf16 a[212:215], v[56:59], a[120:123], 1.0\n"
  "ds_read_b64_tr_b16 a[140:141], v219 offset:1024\n"
  "v_fma_f32 v2, v2, s73, v223\n"
  "v_mfma_f32_16x16x32_bf16 a[212:215], v[60:63], a[124:127], a[212:215]\n"
  "ds_read_b64_tr_b16 a[142:143], v220 offset:1024\n"
  "v_fma_f32 v3, v3, s73, v223\n"
  "v_mfma_f32_16x16x32_bf16 a[228:231], a[156:159], a[120:123], 1.0\n"
  "buffer_load_dwordx4 v221, s[40:43], 0 offen offset:0 lds\n"
  "v_mfma_f32_16x16x32_bf16 a[228:231], a[160:163], a[124:127], a[228:231]\n"
  "s_waitcnt lgkmcnt(8)\n"
  "ds_write_b32 v233, v236 offset:18496\n"
  "v_mfma_f32_16x16x32_bf16 a[232:235], a[156:159], a[128:131], 1.0\n"
  "s_addk_i32 m0, 896\n"
  "ds_write_b32 v233, v237 offset:18784\n"
  "v_mfma_f32_16x16x32_bf16 a[232:235], a[160:163], a[132:135], a[232:235]\n"
  "ds_write_b64 v231, v[64:65] offset:0\n"
  "v_mfma_f32_16x16x32_bf16 a[216:219], v[56:59], a[128:131], 1.0\n"
  "buffer_load_dwordx4 v221, s[40:43], 0 offen offset:128 lds\n"
  "v_fma_f32 v16, v16, s73, v224\n"
  "v_mfma_f32_16x16x32_bf16 a[216:219], v[60:63], a[132:135], a[216:219]\n"
  "s_addk_i32 m0, 896\n"
  "ds_read_b64_tr_b16 a[120:121], v219 offset:16\n"
  "v_fma_f32 v17, v17, s73, v224\n"
  "v_mfma_f32_16x16x32_bf16 a[200:203], v[40:43], a[128:131], 1.0\n"
  "ds_read_b64_tr_b16 a[122:123], v220 offset:16\n"
  "v_fma_f32 v18, v18, s73, v224\n"
  "v_mfma_f32_16x16x32_bf16 a[200:203], v[44:47], a[132:135], a[200:203]\n"
  "ds_read_b64_tr_b16 a[124:125], v219 offset:1040\n"
  "v_fma_f32 v19, v19, s73, v224\n"
  "v_mfma_f32_16x16x32_bf16 a[184:187], v[24:27], a[128:131], 1.0\n"
  "buffer_load_dwordx4 v221, s[40:43], 0 offen offset:256 lds\n"
  "v_fma_f32 v32, v32, s73, v225\n"
  "v_mfma_f32_16x16x32_bf16 a[184:187], v[28:31], a[132:135], a[184:187]\n"
  "s_mov_b32 m0, s60\n"
  "ds_read_b64_tr_b16 a[126:127], v220 offset:1040\n"
  "v_fma_f32 v33, v33, s73, v225\n"
  "v_mfma_f32_16x16x32_bf16 a[168:171], v[8:11], a[128:131], 1.0\n"
  "s_add_u32 s28, s96, 184448\n"
  "v_exp_f32 v0, v0\n"
  "v_mfma_f32_16x16x32_bf16 a[168:171], v[12:15], a[132:135], a[168:171]\n"
  "s_waitcnt lgkmcnt(7)\n"
  "v_exp_f32 v1, v1\n"
  "v_mfma_f32_16x16x32_bf16 a[172:175], v[8:11], a[136:139], 1.0\n"
  "buffer_load_dwordx4 v221, s[32:35], 0 offen offset:0 lds\n"
  "v_add_f32 v252, v0, v1\n"
  "v_mfma_f32_16x16x32_bf16 a[172:175], v[12:15], a[140:143], a[172:175]\n"
  "s_addk_i32 m0, 1024\n"
  "v_exp_f32 v2, v2\n"
  "v_mfma_f32_16x16x32_bf16 a[188:191], v[24:27], a[136:139], 1.0\n"
  "s_add_u32 s40, s40, s31\n"
  "v_exp_f32 v3, v3\n"
  "v_mfma_f32_16x16x32_bf16 a[188:191], v[28:31], a[140:143], a[188:191]\n"
  "s_addc_u32 s41, s41, 0\n"
  "v_exp_f32 v16, v16\n"
  "v_mfma_f32_16x16x32_bf16 a[204:207], v[40:43], a[136:139], 1.0\n"
  "buffer_load_dwordx4 v221, s[44:47], 0 offen offset:0 lds\n"
  "v_add_f32 v252, v252, v2\n"
  "v_mfma_f32_16x16x32_bf16 a[204:207], v[44:47], a[140:143], a[204:207]\n"
  "s_addk_i32 m0, 768\n"
  "v_exp_f32 v17, v17\n"
  "v_mfma_f32_16x16x32_bf16 a[220:223], v[56:59], a[136:139], 1.0\n"
  "s_mov_b64 s[32:33], s[44:45]\n"
  "v_exp_f32 v18, v18\n"
  "v_mfma_f32_16x16x32_bf16 a[220:223], v[60:63], a[140:143], a[220:223]\n"
  "s_waitcnt lgkmcnt(0)\n"
  "v_exp_f32 v19, v19\n"
  "v_mfma_f32_16x16x32_bf16 a[224:227], v[56:59], a[120:123], 1.0\n"
  "buffer_load_dwordx4 v221, s[44:47], 0 offen offset:256 lds\n"
  "v_add_f32 v252, v252, v3\n"
  "v_mfma_f32_16x16x32_bf16 a[224:227], v[60:63], a[124:127], a[224:227]\n"
  "s_add_u32 s44, s44, s31\n"
  "v_exp_f32 v32, v32\n"
  "v_mfma_f32_16x16x32_bf16 a[208:211], v[40:43], a[120:123], 1.0\n"
  "s_addc_u32 s45, s45, 0\n"
  "v_exp_f32 v33, v33\n"
  "v_mfma_f32_16x16x32_bf16 a[208:211], v[44:47], a[124:127], a[208:211]\n"
  "s_cmp_eq_u32 s95, 0\n"
  "s_cselect_b32 s95, 3, s95\n"
  "s_subb_u32 s100, s100, -11\n"
  "v_add_f32 v253, v16, v17\n"
  "v_add_f32 v253, v253, v18\n"
  "v_mfma_f32_16x16x32_bf16 a[192:195], v[24:27], a[120:123], 1.0\n"
  "s_sub_u32 s95, s95, 1\n"
  "v_add_f32 v253, v253, v19\n"
  "v_fma_f32 v34, v34, s73, v225\n"
  "v_mfma_f32_16x16x32_bf16 a[192:195], v[28:31], a[124:127], a[192:195]\n"
  "s_waitcnt vmcnt(6)\n"
  "s_barrier\n"
  "ds_read_b128 a[128:131], v216 offset:12352\n"
  "v_mfma_f32_16x16x32_bf16 a[176:179], v[8:11], a[120:123], 1.0\n"
  "s_lshr_b32 s79, s100, 0\n"
  "ds_read_b128 a[132:135], v216 offset:12416\n"
  "v_fma_f32 v35, v35, s73, v225\n"
  "v_mfma_f32_16x16x32_bf16 a[176:179], v[12:15], a[124:127], a[176:179]\n"
  "s_mul_i32 s79, s79, 7\n"
  "ds_read_b128 a[120:123], v216 offset:13376\n"
  "v_add_f32 v254, v32, v33\n"
  "v_mfma_f32_16x16x32_bf16 v[68:71], v[8:11], a[152:155], 0\n"
  "s_add_u32 s79, s79, s100\n"
  "ds_read_b128 a[124:127], v216 offset:13440\n"
  "v_cvt_pk_bf16_f32 v8, v0, v1\n"
  "v_mfma_f32_16x16x32_bf16 v[68:71], v[12:15], a[152:155], v[68:71]\n"
  "s_add_u32 s79, s79, s101\n"
  "ds_read_b128 a[136:139], v216 offset:14400\n"
  "v_cvt_pk_bf16_f32 v9, v2, v3\n"
  "v_mfma_f32_16x16x32_bf16 v[72:75], v[24:27], a[152:155], 0\n"
  "ds_read_b128 a[140:143], v216 offset:14464\n"
  "v_cvt_pk_bf16_f32 v24, v16, v17\n"
  "v_mfma_f32_16x16x32_bf16 v[72:75], v[28:31], a[152:155], v[72:75]\n"
  "s_mov_b32 m0, s66\n"
  "v_exp_f32 v34, v34\n"
  "v_mfma_f32_16x16x32_bf16 v[76:79], v[40:43], a[152:155], 0\n"
  "s_min_u32 s79, s79, 0x1ff\n"
  "v_exp_f32 v35, v35\n"
  "v_mfma_f32_16x16x32_bf16 v[76:79], v[44:47], a[152:155], v[76:79]\n"
  "s_mul_i32 s96, s79, 196608\n"
  "v_cvt_pk_bf16_f32 v25, v18, v19\n"
  "v_add_f32 v254, v254, v34\n"
  "v_mfma_f32_16x16x32_bf16 v[80:83], v[56:59], a[152:155], 0\n"
  "s_mul_i32 s92, s79, 325632\n"
  "v_add_f32 v254, v254, v35\n"
  "v_cvt_pk_bf16_f32 v40, v32, v33\n"
  "v_mfma_f32_16x16x32_bf16 v[80:83], v[60:63], a[152:155], v[80:83]\n"
  "buffer_load_dwordx4 v221, s[40:43], 0 offen offset:0 lds\n"
  "v_mfma_f32_16x16x32_bf16 v[84:87], a[156:159], a[152:155], 0\n"
  "s_add_u32 s96, s96, s39\n"
  "ds_read_b64 a[156:157], v231 offset:0\n"
  "v_cvt_pk_bf16_f32 v41, v34, v35\n"
  "v_mfma_f32_16x16x32_bf16 v[84:87], a[160:163], a[152:155], v[84:87]\n"
  "L_CUT_%=:\n"
  "s_waitcnt lgkmcnt(5)\n"
  "ds_read_b64 a[158:159], v232 offset:0\n"
  "v_fma_f32 v48, v48, s73, v226\n"
  "v_mfma_f32_16x16x32_bf16 v[64:67], a[128:131], a[96:99], v[248:251]\n"
  "s_addk_i32 m0, 896\n"
  "v_exp_f32 v48, v48\n"
  "v_mfma_f32_16x16x32_bf16 v[64:67], a[132:135], a[100:103], v[64:67]\n"
  "s_waitcnt lgkmcnt(4)\n"
  "buffer_load_dwordx4 v221, s[40:43], 0 offen offset:128 lds\n"
  "v_fma_f32 v49, v49, s73, v226\n"
  "v_mfma_f32_16x16x32_bf16 v[64:67], a[120:123], a[104:107], v[64:67]\n"
  "s_addk_i32 m0, 896\n"
  "v_exp_f32 v49, v49\n"
  "v_mfma_f32_16x16x32_bf16 v[64:67], a[124:127], a[108:111], v[64:67]\n"
  "s_waitcnt lgkmcnt(2)\n"
  "v_fma_f32 v50, v50, s73, v226\n"
  "v_fma_f32 v51, v51, s73, v226\n"
  "v_mfma_f32_16x16x32_bf16 v[64:67], a[136:139], a[112:115], v[64:67]\n"
  "s_mul_i32 s79, s95, 110592\n"
  "v_exp_f32 v50, v50\n"
  "v_mfma_f32_16x16x32_bf16 v[64:67], a[140:143], a[116:119], v[64:67]\n"
  "buffer_load_dwordx4 v221, s[40:43], 0 offen offset:256 lds\n"
  "v_add_f32 v255, v48, v49\n"
  "v_mfma_f32_16x16x32_bf16 v[0:3], a[140:143], a[20:23], v[248:251]\n"
  "s_add_u32 s92, s92, s79\n"
  "v_exp_f32 v51, v51\n"
  "v_mfma_f32_16x16x32_bf16 v[0:3], a[136:139], a[16:19], v[0:3]\n"
  "s_add_u32 s71, s92, s80\n"
  "v_add_f32 v255, v255, v50\n"
  "v_add_f32 v255, v255, v51\n"
  "v_mfma_f32_16x16x32_bf16 v[0:3], a[124:127], a[12:15], v[0:3]\n"
  "v_cvt_pk_bf16_f32 v56, v48, v49\n"
  "v_cvt_pk_bf16_f32 v57, v50, v51\n"
  "v_mfma_f32_16x16x32_bf16 v[0:3], a[120:123], a[8:11], v[0:3]\n"
  "s_add_u32 m0, s58, -128\n"
  "v_fma_f32 v52, v52, s73, v226\n"
  "v_mfma_f32_16x16x32_bf16 v[0:3], a[132:135], a[4:7], v[0:3]\n"
  "s_add_u32 s48, s84, s71\n"
  "v_exp_f32 v52, v52\n"
  "v_mfma_f32_16x16x32_bf16 v[0:3], a[128:131], a[0:3], v[0:3]\n"
  "s_addc_u32 s49, s85, 0\n"
  "v_fma_f32 v53, v53, s73, v226\n"
  "v_fma_f32 v54, v54, s73, v226\n"
  "v_mfma_f32_16x16x32_bf16 v[16:19], a[128:131], a[24:27], v[248:251]\n"
  "s_cmp_eq_u32 s95, 2\n"
  "s_cselect_b32 s94, 17, 18\n"
  "v_exp_f32 v53, v53\n"
  "v_mfma_f32_16x16x32_bf16 v[16:19], a[132:135], a[28:31], v[16:19]\n"
  "buffer_load_dwordx4 v221, s[32:35], 0 offen offset:128 lds\n"
  "v_fma_f32 v55, v55, s73, v226\n"
  "v_mfma_f32_16x16x32_bf16 v[16:19], a[120:123], a[32:35], v[16:19]\n"
  "s_addk_i32 m0, 1024\n"
  "ds_read_b64_tr_b16 a[144:145], v217 offset:12352\n"
  "v_cvt_pk_bf16_f32 v58, v52, v53\n"
  "v_mfma_f32_16x16x32_bf16 v[16:19], a[124:127], a[36:39], v[16:19]\n"
  "s_mul_i32 s94, s94, 6144\n"
  "ds_read_b64_tr_b16 a[146:147], v218 offset:12352\n"
  "v_add_f32 v255, v255, v52\n"
  "v_mfma_f32_16x16x32_bf16 v[16:19], a[136:139], a[40:43], v[16:19]\n"
  "s_cmp_lt_i32 s76, 5\n"
  "s_cselect_b32 s38, s94, 0\n"
  "s_cselect_b32 s28, s96, s28\n"
  "ds_read_b64_tr_b16 a[148:149], v217 offset:13376\n"
  "v_mfma_f32_16x16x32_bf16 v[16:19], a[140:143], a[44:47], v[16:19]\n"
  "buffer_load_dwordx4 v221, s[44:47], 0 offen offset:128 lds\n"
  "v_fma_f32 v36, v36, s73, v225\n"
  "v_mfma_f32_16x16x32_bf16 v[32:35], a[140:143], a[68:71], v[248:251]\n"
  "s_addk_i32 m0, 896\n"
  "ds_read_b64_tr_b16 a[150:151], v218 offset:13376\n"
  "v_fma_f32 v37, v37, s73, v225\n"
  "v_mfma_f32_16x16x32_bf16 v[32:35], a[136:139], a[64:67], v[32:35]\n"
  "s_sub_i32 s71, s38, s80\n"
  "v_exp_f32 v54, v54\n"
  "v_mfma_f32_16x16x32_bf16 v[32:35], a[124:127], a[60:63], v[32:35]\n"
  "s_max_i32 s50, s71, 0\n"
  "v_exp_f32 v55, v55\n"
  "v_mfma_f32_16x16x32_bf16 v[32:35], a[120:123], a[56:59], v[32:35]\n"
  "buffer_load_dwordx4 v221, s[44:47], 0 offen offset:256 lds\n"
  "v_cvt_pk_bf16_f32 v59, v54, v55\n"
  "v_mfma_f32_16x16x32_bf16 v[32:35], a[132:135], a[52:55], v[32:35]\n"
  "s_add_u32 s40, s40, s31\n"
  "v_exp_f32 v36, v36\n"
  "v_mfma_f32_16x16x32_bf16 v[32:35], a[128:131], a[48:51], v[32:35]\n"
  "s_addc_u32 s41, s41, 0\n"
  "v_exp_f32 v37, v37\n"
  "v_mfma_f32_16x16x32_bf16 v[48:51], a[128:131], a[72:75], v[248:251]\n"
  "s_mov_b64 s[32:33], s[44:45]\n"
  "ds_read_b64_tr_b16 a[128:129], v217 offset:12368\n"
  "v_fma_f32 v38, v38, s73, v225\n"
  "v_mfma_f32_16x16x32_bf16 v[48:51], a[132:135], a[76:79], v[48:51]\n"
  "ds_read_b64_tr_b16 a[130:131], v218 offset:12368\n"
  "v_fma_f32 v39, v39, s73, v225\n"
  "v_mfma_f32_16x16x32_bf16 v[48:51], a[120:123], a[80:83], v[48:51]\n"
  "ds_read_b64_tr_b16 a[132:133], v217 offset:13392\n"
  "v_cvt_pk_bf16_f32 v42, v36, v37\n"
  "v_mfma_f32_16x16x32_bf16 v[48:51], a[124:127], a[84:87], v[48:51]\n"
  "s_add_u32 m0, s72, 0\n"
  "ds_read_b64_tr_b16 a[134:135], v218 offset:13392\n"
  "v_fma_f32 v20, v20, s73, v224\n"
  "v_mfma_f32_16x16x32_bf16 v[48:51], a[136:139], a[88:91], v[48:51]\n"
  "s_min_u32 s30, s38, 0xc00\n"
  "v_exp_f32 v38, v38\n"
  "v_mfma_f32_16x16x32_bf16 v[48:51], a[140:143], a[92:95], v[48:51]\n"
  "s_waitcnt lgkmcnt(4)\n"
  "v_exp_f32 v39, v39\n"
  "v_mfma_f32_16x16x32_bf16 a[236:239], a[160:163], a[144:147], a[236:239]\n"
  "v_exp_f32 v20, v20\n"
  "v_mfma_f32_16x16x32_bf16 a[236:239], a[156:159], a[148:151], a[236:239]\n"
  "buffer_load_dwordx4 v221, s[48:51], 0 offen offset:0 nt lds\n"
  "v_cvt_pk_bf16_f32 v43, v38, v39\n"
  "v_mfma_f32_16x16x32_bf16 v[184:187], v[60:63], a[144:147], v[184:187]\n"
  "v_fma_f32 v21, v21, s73, v224\n"
  "v_fma_f32 v22, v22, s73, v224\n"
  "v_mfma_f32_16x16x32_bf16 v[184:187], v[56:59], a[148:151], v[184:187]\n"
  "s_add_u32 s44, s44, s31\n"
  "v_exp_f32 v21, v21\n"
  "v_exp_f32_e64 v22, v22\n"
  "v_fma_f32 v23, v23, s73, v224\n"
  "v_exp_f32 v23, v23\n"
  "v_mfma_f32_16x16x32_bf16 v[152:155], v[44:47], a[144:147], v[152:155]\n"
  "s_addc_u32 s45, s45, 0\n"
  "ds_read_b64_tr_b16 a[120:121], v219 offset:12352\n"
  "v_cvt_pk_bf16_f32 v26, v20, v21\n"
  "v_mfma_f32_16x16x32_bf16 v[152:155], v[40:43], a[148:151], v[152:155]\n"
  "ds_read_b64_tr_b16 a[122:123], v220 offset:12352\n"
  "v_cvt_pk_bf16_f32 v27, v22, v23\n"
  "v_fma_f32 v4, v4, s73, v223\n"
  "v_exp_f32 v4, v4\n"
  "v_fma_f32 v5, v5, s73, v223\n"
  "v_exp_f32 v5, v5\n"
  "v_fma_f32 v6, v6, s73, v223\n"
  "v_exp_f32 v6, v6\n"
  "v_fma_f32 v7, v7, s73, v223\n"
  "v_exp_f32 v7, v7\n"
  "v_mfma_f32_16x16x32_bf16 v[120:123], v[28:31], a[144:147], v[120:123]\n"
  "ds_read_b64_tr_b16 a[124:125], v219 offset:13376\n"
  "v_cvt_pk_bf16_f32 v10, v4, v5\n"
  "v_mfma_f32_16x16x32_bf16 v[120:123], v[24:27], a[148:151], v[120:123]\n"
  "ds_read_b64_tr_b16 a[126:127], v220 offset:13376\n"
  "v_cvt_pk_bf16_f32 v11, v6, v7\n"
  "v_mfma_f32_16x16x32_bf16 v[88:91], v[12:15], a[144:147], v[88:91]\n"
  "s_add_u32 m0, s72, -1984\n"
  "v_add_f32 v252, v252, v4\n"
  "v_add_f32 v252, v252, v5\n"
  "v_mfma_f32_16x16x32_bf16 v[88:91], v[8:11], a[148:151], v[88:91]\n"
  "s_waitcnt lgkmcnt(4)\n"
  "buffer_load_dwordx4 v221, s[48:51], 0 offen offset:3072 nt lds\n"
  "v_add_f32 v252, v252, v6\n"
  "v_mfma_f32_16x16x32_bf16 v[92:95], v[12:15], a[128:131], v[92:95]\n"
  "v_add_f32 v252, v252, v7\n"
  "v_add_f32 v253, v253, v20\n"
  "v_mfma_f32_16x16x32_bf16 v[92:95], v[8:11], a[132:135], v[92:95]\n"
  "v_add_f32 v253, v253, v21\n"
  "v_add_f32 v253, v253, v22\n"
  "v_mfma_f32_16x16x32_bf16 v[124:127], v[28:31], a[128:131], v[124:127]\n"
  "ds_read_b128 a[136:139], v235 offset:12352\n"
  "v_add_f32 v253, v253, v23\n"
  "v_mfma_f32_16x16x32_bf16 v[124:127], v[24:27], a[132:135], v[124:127]\n"
  "s_add_u32 m0, s72, 2112\n"
  "ds_read_b128 a[140:143], v235 offset:12416\n"
  "v_add_f32 v254, v254, v36\n"
  "v_mfma_f32_16x16x32_bf16 v[156:159], v[44:47], a[128:131], v[156:159]\n"
  "ds_read_b128 a[144:147], v235 offset:13376\n"
  "v_add_f32 v254, v254, v37\n"
  "v_mfma_f32_16x16x32_bf16 v[156:159], v[40:43], a[132:135], v[156:159]\n"
  "ds_read_b128 a[148:151], v235 offset:13440\n"
  "v_add_f32 v254, v254, v38\n"
  "v_mfma_f32_16x16x32_bf16 v[188:191], v[60:63], a[128:131], v[188:191]\n"
  "v_add_f32 v254, v254, v39\n"
  "v_add_f32 v255, v255, v53\n"
  "v_mfma_f32_16x16x32_bf16 v[188:191], v[56:59], a[132:135], v[188:191]\n"
  "buffer_load_dwordx4 v221, s[48:51], 0 offen offset:128 nt lds\n"
  "v_add_f32 v255, v255, v54\n"
  "v_mfma_f32_16x16x32_bf16 a[240:243], a[160:163], a[128:131], a[240:243]\n"
  "ds_read_b128 a[128:131], v235 offset:14400\n"
  "v_add_f32 v255, v255, v55\n"
  "v_mfma_f32_16x16x32_bf16 a[240:243], a[156:159], a[132:135], a[240:243]\n"
  "s_waitcnt lgkmcnt(5)\n"
  "ds_read_b128 a[132:135], v235 offset:14464\n"
  "v_fma_f32 v64, v64, s73, v227\n"
  "v_mfma_f32_16x16x32_bf16 v[192:195], v[60:63], a[120:123], v[192:195]\n"
  "v_exp_f32 v64, v64\n"
  "v_mfma_f32_16x16x32_bf16 v[192:195], v[56:59], a[124:127], v[192:195]\n"
  "s_add_u32 m0, s72, 128\n"
  "v_fma_f32 v65, v65, s73, v227\n"
  "v_mfma_f32_16x16x32_bf16 v[160:163], v[44:47], a[120:123], v[160:163]\n"
  "v_exp_f32 v65, v65\n"
  "v_mfma_f32_16x16x32_bf16 v[160:163], v[40:43], a[124:127], v[160:163]\n"
  "v_fma_f32 v66, v66, s73, v227\n"
  "v_fma_f32 v67, v67, s73, v227\n"
  "v_mfma_f32_16x16x32_bf16 v[128:131], v[28:31], a[120:123], v[128:131]\n"
  "v_exp_f32 v66, v66\n"
  "v_mfma_f32_16x16x32_bf16 v[128:131], v[24:27], a[124:127], v[128:131]\n"
  "buffer_load_dwordx4 v221, s[48:51], 0 offen offset:3200 nt lds\n"
  "v_cvt_pk_bf16_f32 v64, v64, v65\n"
  "v_mfma_f32_16x16x32_bf16 v[96:99], v[12:15], a[120:123], v[96:99]\n"
  "v_exp_f32 v67, v67\n"
  "v_mfma_f32_16x16x32_bf16 v[96:99], v[8:11], a[124:127], v[96:99]\n"
  "s_waitcnt lgkmcnt(4)\n"
  "v_cvt_pk_bf16_f32 v65, v66, v67\n"
  "v_fma_f32 v0, v0, s73, v223\n"
  "v_mfma_f32_16x16x32_bf16 v[4:7], a[136:139], a[0:3], v[248:251]\n"
  "v_exp_f32 v0, v0\n"
  "v_mfma_f32_16x16x32_bf16 v[4:7], a[140:143], a[4:7], v[4:7]\n"
  "s_waitcnt lgkmcnt(2)\n"
  "v_fma_f32 v1, v1, s73, v223\n"
  "v_fma_f32 v2, v2, s73, v223\n"
  "v_mfma_f32_16x16x32_bf16 v[4:7], a[144:147], a[8:11], v[4:7]\n"
  "v_exp_f32 v1, v1\n"
  "v_mfma_f32_16x16x32_bf16 v[4:7], a[148:151], a[12:15], v[4:7]\n"
  "s_waitcnt lgkmcnt(0)\n"
  "v_exp_f32 v2, v2\n"
  "v_mfma_f32_16x16x32_bf16 v[4:7], a[128:131], a[16:19], v[4:7]\n"
  "v_fma_f32 v3, v3, s73, v223\n"
  "v_add_f32 v252, v252, v0\n"
  "v_mfma_f32_16x16x32_bf16 v[4:7], a[132:135], a[20:23], v[4:7]\n"
  "ds_write_b64 v231, v[64:65] offset:2048\n"
  "v_mfma_f32_16x16x32_bf16 v[20:23], a[132:135], a[44:47], v[248:251]\n"
  "v_exp_f32 v3, v3\n"
  "v_mfma_f32_16x16x32_bf16 v[20:23], a[128:131], a[40:43], v[20:23]\n"
  "v_add_f32 v252, v252, v1\n"
  "v_add_f32 v252, v252, v2\n"
  "v_mfma_f32_16x16x32_bf16 v[20:23], a[148:151], a[36:39], v[20:23]\n"
  "ds_read_b64_tr_b16 a[120:121], v219 offset:12368\n"
  "v_add_f32 v252, v252, v3\n"
  "v_mfma_f32_16x16x32_bf16 v[20:23], a[144:147], a[32:35], v[20:23]\n"
  "ds_read_b64_tr_b16 a[122:123], v220 offset:12368\n"
  "v_fma_f32 v16, v16, s73, v224\n"
  "v_mfma_f32_16x16x32_bf16 v[20:23], a[140:143], a[28:31], v[20:23]\n"
  "ds_read_b64_tr_b16 a[124:125], v219 offset:13392\n"
  "v_fma_f32 v17, v17, s73, v224\n"
  "v_mfma_f32_16x16x32_bf16 v[20:23], a[136:139], a[24:27], v[20:23]\n"
  "ds_read_b64_tr_b16 a[126:127], v220 offset:13392\n"
  "v_fma_f32 v18, v18, s73, v224\n"
  "v_mfma_f32_16x16x32_bf16 v[36:39], a[136:139], a[48:51], v[248:251]\n"
  "v_exp_f32 v16, v16\n"
  "v_mfma_f32_16x16x32_bf16 v[36:39], a[140:143], a[52:55], v[36:39]\n"
  "v_exp_f32 v17, v17\n"
  "v_mfma_f32_16x16x32_bf16 v[36:39], a[144:147], a[56:59], v[36:39]\n"
  "v_exp_f32 v18, v18\n"
  "v_mfma_f32_16x16x32_bf16 v[36:39], a[148:151], a[60:63], v[36:39]\n"
  "v_fma_f32 v19, v19, s73, v224\n"
  "v_add_f32 v253, v253, v16\n"
  "v_mfma_f32_16x16x32_bf16 v[36:39], a[128:131], a[64:67], v[36:39]\n"
  "v_exp_f32 v19, v19\n"
  "v_mfma_f32_16x16x32_bf16 v[36:39], a[132:135], a[68:71], v[36:39]\n"
  "v_add_f32 v253, v253, v17\n"
  "v_add_f32 v253, v253, v18\n"
  "v_mfma_f32_16x16x32_bf16 v[52:55], a[132:135], a[92:95], v[248:251]\n"
  "ds_read_b64_tr_b16 a[132:133], v217 offset:14400\n"
  "v_add_f32 v253, v253, v19\n"
  "v_mfma_f32_16x16x32_bf16 v[52:55], a[128:131], a[88:91], v[52:55]\n"
  "ds_read_b64_tr_b16 a[134:135], v218 offset:14400\n"
  "v_fma_f32 v32, v32, s73, v225\n"
  "v_mfma_f32_16x16x32_bf16 v[52:55], a[148:151], a[84:87], v[52:55]\n"
  "ds_read_b64_tr_b16 a[128:129], v217 offset:14416\n"
  "v_fma_f32 v33, v33, s73, v225\n"
  "v_mfma_f32_16x16x32_bf16 v[52:55], a[144:147], a[80:83], v[52:55]\n"
  "ds_read_b64_tr_b16 a[130:131], v218 offset:14416\n"
  "v_fma_f32 v34, v34, s73, v225\n"
  "v_mfma_f32_16x16x32_bf16 v[52:55], a[140:143], a[76:79], v[52:55]\n"
  "v_exp_f32 v32, v32\n"
  "v_mfma_f32_16x16x32_bf16 v[52:55], a[136:139], a[72:75], v[52:55]\n"
  "s_waitcnt lgkmcnt(4)\n"
  "v_exp_f32 v33, v33\n"
  "v_mfma_f32_16x16x32_bf16 v[100:103], v[12:15], a[120:123], v[100:103]\n"
  "v_exp_f32 v34, v34\n"
  "v_mfma_f32_16x16x32_bf16 v[100:103], v[8:11], a[124:127], v[100:103]\n"
  "v_cvt_pk_bf16_f32 v12, v0, v1\n"
  "v_cvt_pk_bf16_f32 v13, v2, v3\n"
  "v_mfma_f32_16x16x32_bf16 v[132:135], v[28:31], a[120:123], v[132:135]\n"
  "ds_read_b64_tr_b16 a[140:141], v219 offset:14400\n"
  "v_cvt_pk_bf16_f32 v28, v16, v17\n"
  "v_mfma_f32_16x16x32_bf16 v[132:135], v[24:27], a[124:127], v[132:135]\n"
  "ds_read_b64_tr_b16 a[142:143], v220 offset:14400\n"
  "v_cvt_pk_bf16_f32 v29, v18, v19\n"
  "v_mfma_f32_16x16x32_bf16 v[164:167], v[44:47], a[120:123], v[164:167]\n"
  "ds_read_b64_tr_b16 a[136:137], v219 offset:14416\n"
  "v_fma_f32 v35, v35, s73, v225\n"
  "v_mfma_f32_16x16x32_bf16 v[164:167], v[40:43], a[124:127], v[164:167]\n"
  "ds_read_b64_tr_b16 a[138:139], v220 offset:14416\n"
  "v_add_f32 v254, v254, v32\n"
  "v_mfma_f32_16x16x32_bf16 v[196:199], v[60:63], a[120:123], v[196:199]\n"
  "v_exp_f32 v35, v35\n"
  "v_mfma_f32_16x16x32_bf16 v[196:199], v[56:59], a[124:127], v[196:199]\n"
  "s_waitcnt lgkmcnt(4)\n"
  "v_add_f32 v254, v254, v33\n"
  "v_add_f32 v254, v254, v34\n"
  "v_mfma_f32_16x16x32_bf16 a[244:247], a[156:159], a[132:135], a[244:247]\n"
  "v_add_f32 v254, v254, v35\n"
  "v_cvt_pk_bf16_f32 v44, v32, v33\n"
  "v_mfma_f32_16x16x32_bf16 a[248:251], a[156:159], a[128:131], a[248:251]\n"
  "v_cvt_pk_bf16_f32 v45, v34, v35\n"
  "v_fma_f32 v48, v48, s73, v226\n"
  "v_mfma_f32_16x16x32_bf16 v[200:203], v[56:59], a[132:135], v[200:203]\n"
  "v_exp_f32 v48, v48\n"
  "v_mfma_f32_16x16x32_bf16 v[204:207], v[56:59], a[128:131], v[204:207]\n"
  "v_fma_f32 v49, v49, s73, v226\n"
  "v_fma_f32 v50, v50, s73, v226\n"
  "v_mfma_f32_16x16x32_bf16 v[168:171], v[40:43], a[132:135], v[168:171]\n"
  "v_exp_f32 v49, v49\n"
  "v_mfma_f32_16x16x32_bf16 v[172:175], v[40:43], a[128:131], v[172:175]\n"
  "s_waitcnt vmcnt(10) lgkmcnt(0)\n"
  "s_barrier\n"
  "ds_read_b128 a[120:123], v216 offset:24704\n"
  "v_mfma_f32_16x16x32_bf16 v[136:139], v[24:27], a[132:135], v[136:139]\n"
  "ds_read_b64 a[160:161], v231 offset:2048\n"
  "v_fma_f32 v51, v51, s73, v226\n"
  "v_mfma_f32_16x16x32_bf16 v[140:143], v[24:27], a[128:131], v[140:143]\n"
  "ds_read_b64 a[162:163], v232 offset:2048\n"
  "v_add_f32 v255, v255, v48\n"
  "v_mfma_f32_16x16x32_bf16 v[104:107], v[8:11], a[132:135], v[104:107]\n"
  "ds_read_b128 a[124:127], v216 offset:24768\n"
  "v_add_f32 v255, v255, v49\n"
  "v_mfma_f32_16x16x32_bf16 v[108:111], v[8:11], a[128:131], v[108:111]\n"
  "ds_read_b128 a[132:135], v216 offset:25728\n"
  "v_cvt_pk_bf16_f32 v60, v48, v49\n"
  "v_mfma_f32_16x16x32_bf16 v[112:115], v[8:11], a[140:143], v[112:115]\n"
  "ds_read_b128 a[128:131], v216 offset:25792\n"
  "v_fma_f32 v52, v52, s73, v226\n"
  "v_mfma_f32_16x16x32_bf16 v[116:119], v[8:11], a[136:139], v[116:119]\n"
  "s_mov_b32 m0, s67\n"
  "ds_read_b128 a[148:151], v216 offset:26752\n"
  "v_fma_f32 v53, v53, s73, v226\n"
  "v_mfma_f32_16x16x32_bf16 v[144:147], v[24:27], a[140:143], v[144:147]\n"
  "ds_read_b128 a[144:147], v216 offset:26816\n"
  "v_fma_f32 v54, v54, s73, v226\n"
  "v_mfma_f32_16x16x32_bf16 v[148:151], v[24:27], a[136:139], v[148:151]\n"
  "v_exp_f32 v50, v50\n"
  "v_mfma_f32_16x16x32_bf16 v[176:179], v[40:43], a[140:143], v[176:179]\n"
  "v_exp_f32 v51, v51\n"
  "v_mfma_f32_16x16x32_bf16 v[180:183], v[40:43], a[136:139], v[180:183]\n"
  "buffer_load_dwordx4 v221, s[40:43], 0 offen offset:0 lds\n"
  "v_mfma_f32_16x16x32_bf16 v[208:211], v[56:59], a[140:143], v[208:211]\n"
  "s_addk_i32 m0, 896\n"
  "v_exp_f32 v52, v52\n"
  "v_mfma_f32_16x16x32_bf16 v[212:215], v[56:59], a[136:139], v[212:215]\n"
  "s_waitcnt lgkmcnt(4)\n"
  "v_exp_f32 v53, v53\n"
  "v_mfma_f32_16x16x32_bf16 v[64:67], a[120:123], a[96:99], v[248:251]\n"
  "v_exp_f32 v54, v54\n"
  "v_mfma_f32_16x16x32_bf16 v[64:67], a[124:127], a[100:103], v[64:67]\n"
  "s_waitcnt lgkmcnt(2)\n"
  "buffer_load_dwordx4 v221, s[40:43], 0 offen offset:128 lds\n"
  "v_add_f32 v255, v255, v50\n"
  "v_mfma_f32_16x16x32_bf16 v[64:67], a[132:135], a[104:107], v[64:67]\n"
  "s_addk_i32 m0, 896\n"
  "v_add_f32 v255, v255, v51\n"
  "v_cvt_pk_bf16_f32 v61, v50, v51\n"
  "v_mfma_f32_16x16x32_bf16 v[64:67], a[128:131], a[108:111], v[64:67]\n"
  "s_waitcnt lgkmcnt(0)\n"
  "v_fma_f32 v55, v55, s73, v226\n"
  "v_cvt_pk_bf16_f32 v62, v52, v53\n"
  "v_mfma_f32_16x16x32_bf16 v[64:67], a[148:151], a[112:115], v[64:67]\n"
  "v_exp_f32 v55, v55\n"
  "v_mfma_f32_16x16x32_bf16 v[64:67], a[144:147], a[116:119], v[64:67]\n"
  "buffer_load_dwordx4 v221, s[40:43], 0 offen offset:256 lds\n"
  "v_cvt_pk_bf16_f32 v63, v54, v55\n"
  "v_mfma_f32_16x16x32_bf16 v[0:3], a[144:147], a[20:23], v[248:251]\n"
  "v_fma_f32 v36, v36, s73, v225\n"
  "v_fma_f32 v37, v37, s73, v225\n"
  "v_mfma_f32_16x16x32_bf16 v[0:3], a[148:151], a[16:19], v[0:3]\n"
  "s_add_u32 s40, s40, s31\n"
  "v_exp_f32 v36, v36\n"
  "v_mfma_f32_16x16x32_bf16 v[0:3], a[128:131], a[12:15], v[0:3]\n"
  "s_addc_u32 s41, s41, 0\n"
  "v_exp_f32 v37, v37\n"
  "v_mfma_f32_16x16x32_bf16 v[0:3], a[132:135], a[8:11], v[0:3]\n"
  "s_mov_b32 m0, s59\n"
  "v_fma_f32 v38, v38, s73, v225\n"
  "v_fma_f32 v39, v39, s73, v225\n"
  "v_mfma_f32_16x16x32_bf16 v[0:3], a[124:127], a[4:7], v[0:3]\n"
  "v_exp_f32 v38, v38\n"
  "v_mfma_f32_16x16x32_bf16 v[0:3], a[120:123], a[0:3], v[0:3]\n"
  "v_exp_f32 v39, v39\n"
  "v_mfma_f32_16x16x32_bf16 v[16:19], a[120:123], a[24:27], v[248:251]\n"
  "v_cvt_pk_bf16_f32 v46, v36, v37\n"
  "v_cvt_pk_bf16_f32 v47, v38, v39\n"
  "v_mfma_f32_16x16x32_bf16 v[16:19], a[124:127], a[28:31], v[16:19]\n"
  "buffer_load_dwordx4 v221, s[32:35], 0 offen offset:0 lds\n"
  "v_fma_f32 v20, v20, s73, v224\n"
  "v_mfma_f32_16x16x32_bf16 v[16:19], a[132:135], a[32:35], v[16:19]\n"
  "s_addk_i32 m0, 1024\n"
  "ds_read_b64_tr_b16 a[140:141], v217 offset:24704\n"
  "v_fma_f32 v21, v21, s73, v224\n"
  "v_mfma_f32_16x16x32_bf16 v[16:19], a[128:131], a[36:39], v[16:19]\n"
  "s_mov_b64 s[32:33], s[44:45]\n"
  "ds_read_b64_tr_b16 a[142:143], v218 offset:24704\n"
  "v_fma_f32 v22, v22, s73, v224\n"
  "v_mfma_f32_16x16x32_bf16 v[16:19], a[148:151], a[40:43], v[16:19]\n"
  "ds_read_b64_tr_b16 a[136:137], v217 offset:25728\n"
  "v_fma_f32 v23, v23, s73, v224\n"
  "v_mfma_f32_16x16x32_bf16 v[16:19], a[144:147], a[44:47], v[16:19]\n"
  "buffer_load_dwordx4 v221, s[44:47], 0 offen offset:0 lds\n"
  "v_fma_f32 v4, v4, s73, v223\n"
  "v_mfma_f32_16x16x32_bf16 v[32:35], a[144:147], a[68:71], v[248:251]\n"
  "s_addk_i32 m0, 768\n"
  "ds_read_b64_tr_b16 a[138:139], v218 offset:25728\n"
  "v_fma_f32 v5, v5, s73, v223\n"
  "v_mfma_f32_16x16x32_bf16 v[32:35], a[148:151], a[64:67], v[32:35]\n"
  "v_exp_f32 v20, v20\n"
  "v_mfma_f32_16x16x32_bf16 v[32:35], a[128:131], a[60:63], v[32:35]\n"
  "v_exp_f32 v21, v21\n"
  "v_mfma_f32_16x16x32_bf16 v[32:35], a[132:135], a[56:59], v[32:35]\n"
  "buffer_load_dwordx4 v221, s[44:47], 0 offen offset:256 lds\n"
  "v_cvt_pk_bf16_f32 v30, v20, v21\n"
  "v_mfma_f32_16x16x32_bf16 v[32:35], a[124:127], a[52:55], v[32:35]\n"
  "s_add_u32 s44, s44, s31\n"
  "v_exp_f32 v22, v22\n"
  "v_mfma_f32_16x16x32_bf16 v[32:35], a[120:123], a[48:51], v[32:35]\n"
  "s_addc_u32 s45, s45, 0\n"
  "v_exp_f32 v23, v23\n"
  "v_mfma_f32_16x16x32_bf16 v[48:51], a[120:123], a[72:75], v[248:251]\n"
  "v_exp_f32 v4, v4\n"
  "v_mfma_f32_16x16x32_bf16 v[48:51], a[124:127], a[76:79], v[48:51]\n"
  "v_exp_f32 v5, v5\n"
  "v_mfma_f32_16x16x32_bf16 v[48:51], a[132:135], a[80:83], v[48:51]\n"
  "ds_read_b64_tr_b16 a[120:121], v217 offset:24720\n"
  "v_cvt_pk_bf16_f32 v31, v22, v23\n"
  "v_mfma_f32_16x16x32_bf16 v[48:51], a[128:131], a[84:87], v[48:51]\n"
  "ds_read_b64_tr_b16 a[122:123], v218 offset:24720\n"
  "v_fma_f32 v6, v6, s73, v223\n"
  "v_mfma_f32_16x16x32_bf16 v[48:51], a[148:151], a[88:91], v[48:51]\n"
  "ds_read_b64_tr_b16 a[124:125], v217 offset:25744\n"
  "v_fma_f32 v7, v7, s73, v223\n"
  "v_mfma_f32_16x16x32_bf16 v[48:51], a[144:147], a[92:95], v[48:51]\n"
  "s_waitcnt lgkmcnt(3)\n"
  "ds_read_b64_tr_b16 a[126:127], v218 offset:25744\n"
  "v_cvt_pk_bf16_f32 v14, v4, v5\n"
  "v_mfma_f32_16x16x32_bf16 a[228:231], a[156:159], a[140:143], a[228:231]\n"
  "v_exp_f32 v6, v6\n"
  "v_mfma_f32_16x16x32_bf16 a[228:231], a[160:163], a[136:139], a[228:231]\n"
  "v_exp_f32 v7, v7\n"
  "v_mfma_f32_16x16x32_bf16 a[212:215], v[56:59], a[140:143], a[212:215]\n"
  "v_cvt_pk_bf16_f32 v15, v6, v7\n"
  "v_add_f32 v252, v252, v4\n"
  "v_mfma_f32_16x16x32_bf16 a[212:215], v[60:63], a[136:139], a[212:215]\n"
  "v_add_f32 v252, v252, v5\n"
  "v_add_f32 v252, v252, v6\n"
  "v_mfma_f32_16x16x32_bf16 a[196:199], v[40:43], a[140:143], a[196:199]\n"
  "v_add_f32 v252, v252, v7\n"
  "v_add_f32 v253, v253, v20\n"
  "v_mfma_f32_16x16x32_bf16 a[196:199], v[44:47], a[136:139], a[196:199]\n"
  "v_add_f32 v253, v253, v21\n"
  "v_add_f32 v253, v253, v22\n"
  "v_mfma_f32_16x16x32_bf16 a[180:183], v[24:27], a[140:143], a[180:183]\n"
  "ds_read_b64_tr_b16 a[132:133], v219 offset:24704\n"
  "v_add_f32 v253, v253, v23\n"
  "v_mfma_f32_16x16x32_bf16 a[180:183], v[28:31], a[136:139], a[180:183]\n"
  "ds_read_b64_tr_b16 a[134:135], v220 offset:24704\n"
  "v_add_f32 v254, v254, v36\n"
  "v_mfma_f32_16x16x32_bf16 a[164:167], v[8:11], a[140:143], a[164:167]\n"
  "ds_read_b64_tr_b16 a[128:129], v219 offset:25728\n"
  "v_add_f32 v254, v254, v37\n"
  "v_mfma_f32_16x16x32_bf16 a[164:167], v[12:15], a[136:139], a[164:167]\n"
  "ds_read_b64_tr_b16 a[130:131], v220 offset:25728\n"
  "v_add_f32 v254, v254, v38\n"
  "v_mfma_f32_16x16x32_bf16 v[84:87], a[156:159], a[152:155], v[84:87]\n"
  "v_add_f32 v254, v254, v39\n"
  "v_add_f32 v255, v255, v52\n"
  "v_mfma_f32_16x16x32_bf16 v[84:87], a[160:163], a[152:155], v[84:87]\n"
  "s_waitcnt lgkmcnt(4)\n"
  "v_add_f32 v255, v255, v53\n"
  "v_add_f32 v255, v255, v54\n"
  "v_mfma_f32_16x16x32_bf16 a[168:171], v[8:11], a[120:123], a[168:171]\n"
  "ds_read_b64_tr_b16 v[246:247], v230 offset:5632\n"
  "v_add_f32 v255, v255, v55\n"
  "v_mfma_f32_16x16x32_bf16 a[168:171], v[12:15], a[124:127], a[168:171]\n"
  "v_fma_f32 v64, v64, s73, v227\n"
  "v_fma_f32 v65, v65, s73, v227\n"
  "v_mfma_f32_16x16x32_bf16 a[184:187], v[24:27], a[120:123], a[184:187]\n"
  "ds_read_b128 a[140:143], v235 offset:24704\n"
  "v_fma_f32 v66, v66, s73, v227\n"
  "v_mfma_f32_16x16x32_bf16 a[184:187], v[28:31], a[124:127], a[184:187]\n"
  "ds_read_b128 a[136:139], v235 offset:24768\n"
  "v_fma_f32 v67, v67, s73, v227\n"
  "v_mfma_f32_16x16x32_bf16 a[200:203], v[40:43], a[120:123], a[200:203]\n"
  "ds_read_b128 a[148:151], v235 offset:25728\n"
  "v_fma_f32 v0, v0, s73, v223\n"
  "v_mfma_f32_16x16x32_bf16 a[200:203], v[44:47], a[124:127], a[200:203]\n"
  "ds_read_b128 a[144:147], v235 offset:25792\n"
  "v_fma_f32 v1, v1, s73, v223\n"
  "v_mfma_f32_16x16x32_bf16 a[216:219], v[56:59], a[120:123], a[216:219]\n"
  "v_exp_f32 v64, v64\n"
  "v_mfma_f32_16x16x32_bf16 a[216:219], v[60:63], a[124:127], a[216:219]\n"
  "v_exp_f32 v65, v65\n"
  "v_mfma_f32_16x16x32_bf16 a[232:235], a[156:159], a[120:123], a[232:235]\n"
  "ds_read_b128 a[120:123], v235 offset:26752\n"
  "v_cvt_pk_bf16_f32 v64, v64, v65\n"
  "v_mfma_f32_16x16x32_bf16 a[232:235], a[160:163], a[124:127], a[232:235]\n"
  "s_waitcnt lgkmcnt(6)\n"
  "ds_read_b128 a[124:127], v235 offset:26816\n"
  "v_fma_f32 v2, v2, s73, v223\n"
  "v_mfma_f32_16x16x32_bf16 a[220:223], v[56:59], a[132:135], a[220:223]\n"
  "v_exp_f32 v66, v66\n"
  "v_mfma_f32_16x16x32_bf16 a[220:223], v[60:63], a[128:131], a[220:223]\n"
  "v_exp_f32 v67, v67\n"
  "v_mfma_f32_16x16x32_bf16 a[204:207], v[40:43], a[132:135], a[204:207]\n"
  "v_exp_f32 v0, v0\n"
  "v_mfma_f32_16x16x32_bf16 a[204:207], v[44:47], a[128:131], a[204:207]\n"
  "buffer_store_dwordx4 a[252:255], v228, s[52:55], s82 offen offset:0 nt\n"
  "v_cvt_pk_bf16_f32 v65, v66, v67\n"
  "v_exp_f32 v1, v1\n"
  "v_mfma_f32_16x16x32_bf16 a[188:191], v[24:27], a[132:135], a[188:191]\n"
  "ds_read_b64_tr_b16 a[252:253], v230 offset:6720\n"
  "ds_read_b64_tr_b16 a[254:255], v230 offset:6752\n"
  "v_mfma_f32_16x16x32_bf16 a[188:191], v[28:31], a[128:131], a[188:191]\n"
  "ds_write_b64 v231, v[64:65] offset:0\n"
  "v_mfma_f32_16x16x32_bf16 a[172:175], v[8:11], a[132:135], a[172:175]\n"
  "v_exp_f32 v2, v2\n"
  "v_mfma_f32_16x16x32_bf16 a[172:175], v[12:15], a[128:131], a[172:175]\n"
  "s_waitcnt lgkmcnt(7)\n"
  "v_fma_f32 v3, v3, s73, v223\n"
  "v_add_f32 v252, v252, v0\n"
  "v_mfma_f32_16x16x32_bf16 v[4:7], a[140:143], a[0:3], v[248:251]\n"
  "v_exp_f32 v3, v3\n"
  "v_mfma_f32_16x16x32_bf16 v[4:7], a[136:139], a[4:7], v[4:7]\n"
  "s_waitcnt lgkmcnt(5)\n"
  "v_add_f32 v252, v252, v1\n"
  "v_add_f32 v252, v252, v2\n"
  "v_mfma_f32_16x16x32_bf16 v[4:7], a[148:151], a[8:11], v[4:7]\n"
  "v_add_f32 v252, v252, v3\n"
  "v_fma_f32 v16, v16, s73, v224\n"
  "v_mfma_f32_16x16x32_bf16 v[4:7], a[144:147], a[12:15], v[4:7]\n"
  "s_waitcnt lgkmcnt(3)\n"
  "v_exp_f32 v16, v16\n"
  "v_mfma_f32_16x16x32_bf16 v[4:7], a[120:123], a[16:19], v[4:7]\n"
  "v_fma_f32 v17, v17, s73, v224\n"
  "v_fma_f32 v18, v18, s73, v224\n"
  "v_mfma_f32_16x16x32_bf16 v[4:7], a[124:127], a[20:23], v[4:7]\n"
  "v_exp_f32 v17, v17\n"
  "v_mfma_f32_16x16x32_bf16 v[20:23], a[124:127], a[44:47], v[248:251]\n"
  "v_exp_f32 v18, v18\n"
  "v_mfma_f32_16x16x32_bf16 v[20:23], a[120:123], a[40:43], v[20:23]\n"
  "v_fma_f32 v19, v19, s73, v224\n"
  "v_add_f32 v253, v253, v16\n"
  "v_mfma_f32_16x16x32_bf16 v[20:23], a[144:147], a[36:39], v[20:23]\n"
  "ds_read_b64_tr_b16 a[132:133], v219 offset:24720\n"
  "v_add_f32 v253, v253, v17\n"
  "v_mfma_f32_16x16x32_bf16 v[20:23], a[148:151], a[32:35], v[20:23]\n"
  "ds_read_b64_tr_b16 a[134:135], v220 offset:24720\n"
  "v_add_f32 v253, v253, v18\n"
  "v_mfma_f32_16x16x32_bf16 v[20:23], a[136:139], a[28:31], v[20:23]\n"
  "ds_read_b64_tr_b16 a[128:129], v219 offset:25744\n"
  "v_fma_f32 v32, v32, s73, v225\n"
  "v_mfma_f32_16x16x32_bf16 v[20:23], a[140:143], a[24:27], v[20:23]\n"
  "ds_read_b64_tr_b16 a[130:131], v220 offset:25744\n"
  "v_fma_f32 v33, v33, s73, v225\n"
  "v_mfma_f32_16x16x32_bf16 v[36:39], a[140:143], a[48:51], v[248:251]\n"
  "v_exp_f32 v19, v19\n"
  "v_mfma_f32_16x16x32_bf16 v[36:39], a[136:139], a[52:55], v[36:39]\n"
  "v_exp_f32 v32, v32\n"
  "v_mfma_f32_16x16x32_bf16 v[36:39], a[148:151], a[56:59], v[36:39]\n"
  "v_exp_f32 v33, v33\n"
  "v_mfma_f32_16x16x32_bf16 v[36:39], a[144:147], a[60:63], v[36:39]\n"
  "v_add_f32 v253, v253, v19\n"
  "v_fma_f32 v34, v34, s73, v225\n"
  "v_mfma_f32_16x16x32_bf16 v[36:39], a[120:123], a[64:67], v[36:39]\n"
  "v_exp_f32_e64 v34, v34\n"
  "v_mfma_f32_16x16x32_bf16 v[36:39], a[124:127], a[68:71], v[36:39]\n"
  "buffer_store_dwordx4 v[244:247], v228, s[52:55], s82 offen offset:3072 nt\n"
  "v_fma_f32 v35, v35, s73, v225\n"
  "v_exp_f32_e64 v35, v35\n"
  "v_mfma_f32_16x16x32_bf16 v[52:55], a[124:127], a[92:95], v[248:251]\n"
  "ds_read_b64_tr_b16 a[124:125], v217 offset:26752\n"
  "v_add_f32 v254, v254, v32\n"
  "v_mfma_f32_16x16x32_bf16 v[52:55], a[120:123], a[88:91], v[52:55]\n"
  "ds_read_b64_tr_b16 a[126:127], v218 offset:26752\n"
  "v_add_f32 v254, v254, v33\n"
  "v_mfma_f32_16x16x32_bf16 v[52:55], a[144:147], a[84:87], v[52:55]\n"
  "ds_read_b64_tr_b16 a[120:121], v217 offset:26768\n"
  "v_add_f32 v254, v254, v34\n"
  "v_mfma_f32_16x16x32_bf16 v[52:55], a[148:151], a[80:83], v[52:55]\n"
  "ds_read_b64_tr_b16 a[122:123], v218 offset:26768\n"
  "v_add_f32 v254, v254, v35\n"
  "v_mfma_f32_16x16x32_bf16 v[52:55], a[136:139], a[76:79], v[52:55]\n"
  "v_fma_f32 v48, v48, s73, v226\n"
  "v_fma_f32 v49, v49, s73, v226\n"
  "v_mfma_f32_16x16x32_bf16 v[52:55], a[140:143], a[72:75], v[52:55]\n"
  "s_waitcnt lgkmcnt(4)\n"
  "v_exp_f32 v48, v48\n"
  "v_mfma_f32_16x16x32_bf16 a[176:179], v[8:11], a[132:135], a[176:179]\n"
  "v_exp_f32 v49, v49\n"
  "v_mfma_f32_16x16x32_bf16 a[176:179], v[12:15], a[128:131], a[176:179]\n"
  "v_cvt_pk_bf16_f32 v8, v0, v1\n"
  "v_cvt_pk_bf16_f32 v9, v2, v3\n"
  "v_mfma_f32_16x16x32_bf16 a[192:195], v[24:27], a[132:135], a[192:195]\n"
  "ds_read_b64_tr_b16 a[136:137], v219 offset:26752\n"
  "v_cvt_pk_bf16_f32 v24, v16, v17\n"
  "v_mfma_f32_16x16x32_bf16 a[192:195], v[28:31], a[128:131], a[192:195]\n"
  "ds_read_b64_tr_b16 a[138:139], v220 offset:26752\n"
  "v_cvt_pk_bf16_f32 v25, v18, v19\n"
  "v_mfma_f32_16x16x32_bf16 a[208:211], v[40:43], a[132:135], a[208:211]\n"
  "ds_read_b64_tr_b16 a[140:141], v219 offset:26768\n"
  "v_cvt_pk_bf16_f32 v40, v32, v33\n"
  "v_mfma_f32_16x16x32_bf16 a[208:211], v[44:47], a[128:131], a[208:211]\n"
  "ds_read_b64_tr_b16 a[142:143], v220 offset:26768\n"
  "v_cvt_pk_bf16_f32 v41, v34, v35\n"
  "v_mfma_f32_16x16x32_bf16 a[224:227], v[56:59], a[132:135], a[224:227]\n"
  "v_fma_f32 v50, v50, s73, v226\n"
  "v_fma_f32 v51, v51, s73, v226\n"
  "v_mfma_f32_16x16x32_bf16 a[224:227], v[60:63], a[128:131], a[224:227]\n"
  "s_waitcnt lgkmcnt(4)\n"
  "v_exp_f32 v50, v50\n"
  "v_mfma_f32_16x16x32_bf16 a[244:247], a[160:163], a[124:127], a[244:247]\n"
  "v_exp_f32 v51, v51\n"
  "v_mfma_f32_16x16x32_bf16 a[248:251], a[160:163], a[120:123], a[248:251]\n"
  "v_add_f32 v255, v255, v48\n"
  "v_add_f32 v255, v255, v49\n"
  "v_mfma_f32_16x16x32_bf16 v[200:203], v[60:63], a[124:127], v[200:203]\n"
  "v_add_f32 v255, v255, v50\n"
  "v_add_f32 v255, v255, v51\n"
  "v_mfma_f32_16x16x32_bf16 v[204:207], v[60:63], a[120:123], v[204:207]\n"
  "buffer_store_dwordx4 a[252:255], v228, s[52:55], s83 offen offset:0 nt\n"
  "v_cvt_pk_bf16_f32 v56, v48, v49\n"
  "v_cvt_pk_bf16_f32 v57, v50, v51\n"
  "v_fma_f32 v52, v52, s73, v226\n"
  "v_mfma_f32_16x16x32_bf16 v[168:171], v[44:47], a[124:127], v[168:171]\n"
  "v_exp_f32_e64 v52, v52\n"
  "v_mfma_f32_16x16x32_bf16 v[172:175], v[44:47], a[120:123], v[172:175]\n"
  "s_waitcnt vmcnt(13) lgkmcnt(0)\n"
  "s_barrier\n"
  "ds_read_b128 a[132:135], v216 offset:0\n"
  "v_mfma_f32_16x16x32_bf16 v[136:139], v[28:31], a[124:127], v[136:139]\n"
  "ds_read_b64 a[156:157], v231 offset:0\n"
  "v_fma_f32 v53, v53, s73, v226\n"
  "v_mfma_f32_16x16x32_bf16 v[140:143], v[28:31], a[120:123], v[140:143]\n"
  "ds_read_b64 a[158:159], v232 offset:0\n"
  "v_fma_f32 v54, v54, s73, v226\n"
  "v_mfma_f32_16x16x32_bf16 v[104:107], v[12:15], a[124:127], v[104:107]\n"
  "ds_read_b128 a[128:131], v216 offset:64\n"
  "v_fma_f32 v55, v55, s73, v226\n"
  "v_mfma_f32_16x16x32_bf16 v[108:111], v[12:15], a[120:123], v[108:111]\n"
  "ds_read_b128 a[124:127], v216 offset:1024\n"
  "v_fma_f32 v36, v36, s73, v225\n"
  "v_mfma_f32_16x16x32_bf16 v[112:115], v[12:15], a[136:139], v[112:115]\n"
  "ds_read_b128 a[120:123], v216 offset:1088\n"
  "v_fma_f32 v37, v37, s73, v225\n"
  "v_mfma_f32_16x16x32_bf16 v[116:119], v[12:15], a[140:143], v[116:119]\n"
  "s_mov_b32 m0, s68\n"
  "ds_read_b128 a[144:147], v216 offset:2048\n"
  "v_fma_f32 v38, v38, s73, v225\n"
  "v_mfma_f32_16x16x32_bf16 v[144:147], v[28:31], a[136:139], v[144:147]\n"
  "ds_read_b128 a[148:151], v216 offset:2112\n"
  "v_fma_f32 v39, v39, s73, v225\n"
  "v_mfma_f32_16x16x32_bf16 v[148:151], v[28:31], a[140:143], v[148:151]\n"
  "v_exp_f32 v53, v53\n"
  "v_mfma_f32_16x16x32_bf16 v[176:179], v[44:47], a[136:139], v[176:179]\n"
  "v_exp_f32 v54, v54\n"
  "v_mfma_f32_16x16x32_bf16 v[180:183], v[44:47], a[140:143], v[180:183]\n"
  "buffer_load_dwordx4 v221, s[40:43], 0 offen offset:0 lds\n"
  "v_mfma_f32_16x16x32_bf16 v[208:211], v[60:63], a[136:139], v[208:211]\n"
  "s_addk_i32 m0, 896\n"
  "v_exp_f32 v55, v55\n"
  "v_mfma_f32_16x16x32_bf16 v[212:215], v[60:63], a[140:143], v[212:215]\n"
  "s_waitcnt lgkmcnt(4)\n"
  "v_exp_f32 v36, v36\n"
  "v_mfma_f32_16x16x32_bf16 v[64:67], a[132:135], a[96:99], v[248:251]\n"
  "v_exp_f32 v37, v37\n"
  "v_mfma_f32_16x16x32_bf16 v[64:67], a[128:131], a[100:103], v[64:67]\n"
  "s_waitcnt lgkmcnt(2)\n"
  "buffer_load_dwordx4 v221, s[40:43], 0 offen offset:128 lds\n"
  "v_cvt_pk_bf16_f32 v58, v52, v53\n"
  "v_mfma_f32_16x16x32_bf16 v[64:67], a[124:127], a[104:107], v[64:67]\n"
  "s_addk_i32 m0, 896\n"
  "v_exp_f32 v38, v38\n"
  "v_mfma_f32_16x16x32_bf16 v[64:67], a[120:123], a[108:111], v[64:67]\n"
  "s_waitcnt lgkmcnt(0)\n"
  "v_exp_f32 v39, v39\n"
  "v_mfma_f32_16x16x32_bf16 v[64:67], a[144:147], a[112:115], v[64:67]\n"
  "v_cvt_pk_bf16_f32 v59, v54, v55\n"
  "v_cvt_pk_bf16_f32 v42, v36, v37\n"
  "v_mfma_f32_16x16x32_bf16 v[64:67], a[148:151], a[116:119], v[64:67]\n"
  "buffer_load_dwordx4 v221, s[40:43], 0 offen offset:256 lds\n"
  "v_cvt_pk_bf16_f32 v43, v38, v39\n"
  "v_mfma_f32_16x16x32_bf16 v[0:3], a[148:151], a[20:23], v[248:251]\n"
  "v_fma_f32 v20, v20, s73, v224\n"
  "v_fma_f32 v21, v21, s73, v224\n"
  "v_mfma_f32_16x16x32_bf16 v[0:3], a[144:147], a[16:19], v[0:3]\n"
  "s_add_u32 s40, s40, s31\n"
  "v_exp_f32 v20, v20\n"
  "v_mfma_f32_16x16x32_bf16 v[0:3], a[120:123], a[12:15], v[0:3]\n"
  "s_addc_u32 s41, s41, 0\n"
  "v_exp_f32 v21, v21\n"
  "v_mfma_f32_16x16x32_bf16 v[0:3], a[124:127], a[8:11], v[0:3]\n"
  "s_add_u32 m0, s60, -128\n"
  "v_fma_f32 v22, v22, s73, v224\n"
  "v_add_f32 v253, v253, v20\n"
  "v_mfma_f32_16x16x32_bf16 v[0:3], a[128:131], a[4:7], v[0:3]\n"
  "v_exp_f32 v22, v22\n"
  "v_mfma_f32_16x16x32_bf16 v[0:3], a[132:135], a[0:3], v[0:3]\n"
  "v_fma_f32 v23, v23, s73, v224\n"
  "v_cvt_pk_bf16_f32 v26, v20, v21\n"
  "v_mfma_f32_16x16x32_bf16 v[16:19], a[132:135], a[24:27], v[248:251]\n"
  "v_exp_f32 v23, v23\n"
  "v_mfma_f32_16x16x32_bf16 v[16:19], a[128:131], a[28:31], v[16:19]\n"
  "buffer_load_dwordx4 v221, s[32:35], 0 offen offset:128 lds\n"
  "v_cvt_pk_bf16_f32 v27, v22, v23\n"
  "v_mfma_f32_16x16x32_bf16 v[16:19], a[124:127], a[32:35], v[16:19]\n"
  "s_addk_i32 m0, 1024\n"
  "ds_read_b64_tr_b16 a[136:137], v217 offset:0\n"
  "v_fma_f32 v4, v4, s73, v223\n"
  "v_mfma_f32_16x16x32_bf16 v[16:19], a[120:123], a[36:39], v[16:19]\n"
  "s_mov_b64 s[32:33], s[44:45]\n"
  "ds_read_b64_tr_b16 a[138:139], v218 offset:0\n"
  "v_fma_f32 v5, v5, s73, v223\n"
  "v_mfma_f32_16x16x32_bf16 v[16:19], a[144:147], a[40:43], v[16:19]\n"
  "ds_read_b64_tr_b16 a[140:141], v217 offset:1024\n"
  "v_fma_f32 v6, v6, s73, v223\n"
  "v_mfma_f32_16x16x32_bf16 v[16:19], a[148:151], a[44:47], v[16:19]\n"
  "buffer_load_dwordx4 v221, s[44:47], 0 offen offset:128 lds\n"
  "v_fma_f32 v7, v7, s73, v223\n"
  "v_mfma_f32_16x16x32_bf16 v[32:35], a[148:151], a[68:71], v[248:251]\n"
  "s_addk_i32 m0, 896\n"
  "ds_read_b64_tr_b16 a[142:143], v218 offset:1024\n"
  "v_add_f32 v253, v253, v21\n"
  "v_mfma_f32_16x16x32_bf16 v[32:35], a[144:147], a[64:67], v[32:35]\n"
  "v_exp_f32 v4, v4\n"
  "v_mfma_f32_16x16x32_bf16 v[32:35], a[120:123], a[60:63], v[32:35]\n"
  "v_exp_f32 v5, v5\n"
  "v_mfma_f32_16x16x32_bf16 v[32:35], a[124:127], a[56:59], v[32:35]\n"
  "buffer_load_dwordx4 v221, s[44:47], 0 offen offset:256 lds\n"
  "v_cvt_pk_bf16_f32 v10, v4, v5\n"
  "v_mfma_f32_16x16x32_bf16 v[32:35], a[128:131], a[52:55], v[32:35]\n"
  "s_add_u32 s44, s44, s31\n"
  "v_exp_f32 v6, v6\n"
  "v_mfma_f32_16x16x32_bf16 v[32:35], a[132:135], a[48:51], v[32:35]\n"
  "s_addc_u32 s45, s45, 0\n"
  "v_exp_f32 v7, v7\n"
  "v_mfma_f32_16x16x32_bf16 v[48:51], a[132:135], a[72:75], v[248:251]\n"
  "ds_read_b64_tr_b16 a[132:133], v217 offset:16\n"
  "v_cvt_pk_bf16_f32 v11, v6, v7\n"
  "v_mfma_f32_16x16x32_bf16 v[48:51], a[128:131], a[76:79], v[48:51]\n"
  "ds_read_b64_tr_b16 a[134:135], v218 offset:16\n"
  "v_add_f32 v252, v252, v4\n"
  "v_mfma_f32_16x16x32_bf16 v[48:51], a[124:127], a[80:83], v[48:51]\n"
  "ds_read_b64_tr_b16 a[128:129], v217 offset:1040\n"
  "v_add_f32 v252, v252, v5\n"
  "v_mfma_f32_16x16x32_bf16 v[48:51], a[120:123], a[84:87], v[48:51]\n"
  "s_add_u32 m0, s72, 4224\n"
  "ds_read_b64_tr_b16 a[130:131], v218 offset:1040\n"
  "v_add_f32 v252, v252, v6\n"
  "v_mfma_f32_16x16x32_bf16 v[48:51], a[144:147], a[88:91], v[48:51]\n"
  "v_add_f32 v252, v252, v7\n"
  "v_add_f32 v253, v253, v22\n"
  "v_mfma_f32_16x16x32_bf16 v[48:51], a[148:151], a[92:95], v[48:51]\n"
  "s_waitcnt lgkmcnt(4)\n"
  "v_add_f32 v253, v253, v23\n"
  "v_add_f32 v254, v254, v36\n"
  "v_mfma_f32_16x16x32_bf16 a[236:239], a[160:163], a[136:139], a[236:239]\n"
  "v_add_f32 v254, v254, v37\n"
  "v_add_f32 v254, v254, v38\n"
  "v_mfma_f32_16x16x32_bf16 a[236:239], a[156:159], a[140:143], a[236:239]\n"
  "buffer_load_dwordx4 v221, s[48:51], 0 offen offset:256 nt lds\n"
  "v_add_f32 v254, v254, v39\n"
  "v_mfma_f32_16x16x32_bf16 v[184:187], v[60:63], a[136:139], v[184:187]\n"
  "v_add_f32 v255, v255, v52\n"
  "v_add_f32 v255, v255, v53\n"
  "v_mfma_f32_16x16x32_bf16 v[184:187], v[56:59], a[140:143], v[184:187]\n"
  "v_add_f32 v255, v255, v54\n"
  "v_add_f32 v255, v255, v55\n"
  "v_mfma_f32_16x16x32_bf16 v[152:155], v[44:47], a[136:139], v[152:155]\n"
  "ds_read_b64_tr_b16 a[124:125], v219 offset:0\n"
  "v_fma_f32 v64, v64, s73, v227\n"
  "v_mfma_f32_16x16x32_bf16 v[152:155], v[40:43], a[140:143], v[152:155]\n"
  "s_add_u32 m0, s72, 2240\n"
  "ds_read_b64_tr_b16 a[126:127], v220 offset:0\n"
  "v_mfma_f32_16x16x32_bf16 v[120:123], v[28:31], a[136:139], v[120:123]\n"
  "ds_read_b64_tr_b16 a[120:121], v219 offset:1024\n"
  "v_fma_f32 v65, v65, s73, v227\n"
  "v_mfma_f32_16x16x32_bf16 v[120:123], v[24:27], a[140:143], v[120:123]\n"
  "ds_read_b64_tr_b16 a[122:123], v220 offset:1024\n"
  "v_fma_f32 v66, v66, s73, v227\n"
  "v_mfma_f32_16x16x32_bf16 v[88:91], v[12:15], a[136:139], v[88:91]\n"
  "v_exp_f32 v64, v64\n"
  "v_mfma_f32_16x16x32_bf16 v[88:91], v[8:11], a[140:143], v[88:91]\n"
  "s_waitcnt lgkmcnt(4)\n"
  "buffer_load_dwordx4 v221, s[48:51], 0 offen offset:3328 nt lds\n"
  "v_fma_f32 v67, v67, s73, v227\n"
  "v_mfma_f32_16x16x32_bf16 v[92:95], v[12:15], a[132:135], v[92:95]\n"
  "v_exp_f32 v65, v65\n"
  "v_mfma_f32_16x16x32_bf16 v[92:95], v[8:11], a[128:131], v[92:95]\n"
  "v_exp_f32 v66, v66\n"
  "v_mfma_f32_16x16x32_bf16 v[124:127], v[28:31], a[132:135], v[124:127]\n"
  "ds_read_b128 a[136:139], v235 offset:0\n"
  "v_cvt_pk_bf16_f32 v64, v64, v65\n"
  "v_mfma_f32_16x16x32_bf16 v[124:127], v[24:27], a[128:131], v[124:127]\n"
  "ds_read_b128 a[140:143], v235 offset:64\n"
  "v_fma_f32 v0, v0, s73, v223\n"
  "v_mfma_f32_16x16x32_bf16 v[156:159], v[44:47], a[132:135], v[156:159]\n"
  "ds_read_b128 a[144:147], v235 offset:1024\n"
  "v_fma_f32 v1, v1, s73, v223\n"
  "v_mfma_f32_16x16x32_bf16 v[156:159], v[40:43], a[128:131], v[156:159]\n"
  "ds_read_b128 a[148:151], v235 offset:1088\n"
  "v_fma_f32 v2, v2, s73, v223\n"
  "v_mfma_f32_16x16x32_bf16 v[188:191], v[60:63], a[132:135], v[188:191]\n"
  "v_exp_f32 v67, v67\n"
  "v_mfma_f32_16x16x32_bf16 v[188:191], v[56:59], a[128:131], v[188:191]\n"
  "v_exp_f32 v0, v0\n"
  "v_mfma_f32_16x16x32_bf16 a[240:243], a[160:163], a[132:135], a[240:243]\n"
  "ds_read_b128 a[132:135], v235 offset:2048\n"
  "v_cvt_pk_bf16_f32 v65, v66, v67\n"
  "v_mfma_f32_16x16x32_bf16 a[240:243], a[156:159], a[128:131], a[240:243]\n"
  "s_waitcnt lgkmcnt(5)\n"
  "ds_read_b128 a[128:131], v235 offset:2112\n"
  "v_fma_f32 v3, v3, s73, v223\n"
  "v_mfma_f32_16x16x32_bf16 v[192:195], v[60:63], a[124:127], v[192:195]\n"
  "v_exp_f32_e64 v1, v1\n"
  "v_mfma_f32_16x16x32_bf16 v[192:195], v[56:59], a[120:123], v[192:195]\n"
  "ds_write_b64 v231, v[64:65] offset:2048\n"
  "v_mfma_f32_16x16x32_bf16 v[160:163], v[44:47], a[124:127], v[160:163]\n"
  "v_exp_f32 v2, v2\n"
  "v_mfma_f32_16x16x32_bf16 v[160:163], v[40:43], a[120:123], v[160:163]\n"
  "v_exp_f32 v3, v3\n"
  "v_mfma_f32_16x16x32_bf16 v[128:131], v[28:31], a[124:127], v[128:131]\n"
  "v_add_f32 v252, v252, v0\n"
  "v_add_f32 v252, v252, v1\n"
  "v_mfma_f32_16x16x32_bf16 v[128:131], v[24:27], a[120:123], v[128:131]\n"
  "v_add_f32 v252, v252, v2\n"
  "v_add_f32 v252, v252, v3\n"
  "v_mfma_f32_16x16x32_bf16 v[96:99], v[12:15], a[124:127], v[96:99]\n"
  "v_fma_f32 v16, v16, s73, v224\n"
  "v_fma_f32 v17, v17, s73, v224\n"
  "v_mfma_f32_16x16x32_bf16 v[96:99], v[8:11], a[120:123], v[96:99]\n"
  "s_waitcnt lgkmcnt(5)\n"
  "v_exp_f32 v16, v16\n"
  "v_mfma_f32_16x16x32_bf16 v[4:7], a[136:139], a[0:3], v[248:251]\n"
  "v_exp_f32 v17, v17\n"
  "v_mfma_f32_16x16x32_bf16 v[4:7], a[140:143], a[4:7], v[4:7]\n"
  "s_waitcnt lgkmcnt(3)\n"
  "v_fma_f32 v18, v18, s73, v224\n"
  "v_fma_f32 v19, v19, s73, v224\n"
  "v_mfma_f32_16x16x32_bf16 v[4:7], a[144:147], a[8:11], v[4:7]\n"
  "v_exp_f32 v18, v18\n"
  "v_mfma_f32_16x16x32_bf16 v[4:7], a[148:151], a[12:15], v[4:7]\n"
  "s_waitcnt lgkmcnt(1)\n"
  "v_exp_f32 v19, v19\n"
  "v_mfma_f32_16x16x32_bf16 v[4:7], a[132:135], a[16:19], v[4:7]\n"
  "v_add_f32 v253, v253, v16\n"
  "v_add_f32 v253, v253, v17\n"
  "v_mfma_f32_16x16x32_bf16 v[4:7], a[128:131], a[20:23], v[4:7]\n"
  "v_add_f32 v253, v253, v18\n"
  "v_add_f32 v253, v253, v19\n"
  "v_mfma_f32_16x16x32_bf16 v[20:23], a[128:131], a[44:47], v[248:251]\n"
  "v_fma_f32 v32, v32, s73, v225\n"
  "v_fma_f32 v33, v33, s73, v225\n"
  "v_mfma_f32_16x16x32_bf16 v[20:23], a[132:135], a[40:43], v[20:23]\n"
  "v_exp_f32 v32, v32\n"
  "v_mfma_f32_16x16x32_bf16 v[20:23], a[148:151], a[36:39], v[20:23]\n"
  "ds_read_b64_tr_b16 a[124:125], v219 offset:16\n"
  "v_fma_f32 v34, v34, s73, v225\n"
  "v_mfma_f32_16x16x32_bf16 v[20:23], a[144:147], a[32:35], v[20:23]\n"
  "ds_read_b64_tr_b16 a[126:127], v220 offset:16\n"
  "v_fma_f32 v35, v35, s73, v225\n"
  "v_mfma_f32_16x16x32_bf16 v[20:23], a[140:143], a[28:31], v[20:23]\n"
  "ds_read_b64_tr_b16 a[120:121], v219 offset:1040\n"
  "v_add_f32 v254, v254, v32\n"
  "v_mfma_f32_16x16x32_bf16 v[20:23], a[136:139], a[24:27], v[20:23]\n"
  "ds_read_b64_tr_b16 a[122:123], v220 offset:1040\n"
  "v_fma_f32 v48, v48, s73, v226\n"
  "v_mfma_f32_16x16x32_bf16 v[36:39], a[136:139], a[48:51], v[248:251]\n"
  "v_exp_f32 v33, v33\n"
  "v_mfma_f32_16x16x32_bf16 v[36:39], a[140:143], a[52:55], v[36:39]\n"
  "v_exp_f32 v34, v34\n"
  "v_mfma_f32_16x16x32_bf16 v[36:39], a[144:147], a[56:59], v[36:39]\n"
  "v_exp_f32 v35, v35\n"
  "v_mfma_f32_16x16x32_bf16 v[36:39], a[148:151], a[60:63], v[36:39]\n"
  "v_exp_f32 v48, v48\n"
  "v_mfma_f32_16x16x32_bf16 v[36:39], a[132:135], a[64:67], v[36:39]\n"
  "v_add_f32 v254, v254, v33\n"
  "v_add_f32 v254, v254, v34\n"
  "v_mfma_f32_16x16x32_bf16 v[36:39], a[128:131], a[68:71], v[36:39]\n"
  "v_add_f32 v254, v254, v35\n"
  "v_fma_f32 v49, v49, s73, v226\n"
  "v_mfma_f32_16x16x32_bf16 v[52:55], a[128:131], a[92:95], v[248:251]\n"
  "ds_read_b64_tr_b16 a[128:129], v217 offset:2048\n"
  "v_fma_f32 v50, v50, s73, v226\n"
  "v_mfma_f32_16x16x32_bf16 v[52:55], a[132:135], a[88:91], v[52:55]\n"
  "ds_read_b64_tr_b16 a[130:131], v218 offset:2048\n"
  "v_fma_f32 v51, v51, s73, v226\n"
  "v_mfma_f32_16x16x32_bf16 v[52:55], a[148:151], a[84:87], v[52:55]\n"
  "ds_read_b64_tr_b16 a[132:133], v217 offset:2064\n"
  "v_add_f32 v255, v255, v48\n"
  "v_mfma_f32_16x16x32_bf16 v[52:55], a[144:147], a[80:83], v[52:55]\n"
  "ds_read_b64_tr_b16 a[134:135], v218 offset:2064\n"
  "v_fma_f32 v36, v36, s73, v225\n"
  "v_mfma_f32_16x16x32_bf16 v[52:55], a[140:143], a[76:79], v[52:55]\n"
  "v_exp_f32 v49, v49\n"
  "v_mfma_f32_16x16x32_bf16 v[52:55], a[136:139], a[72:75], v[52:55]\n"
  "s_waitcnt lgkmcnt(4)\n"
  "v_exp_f32 v50, v50\n"
  "v_mfma_f32_16x16x32_bf16 v[100:103], v[12:15], a[124:127], v[100:103]\n"
  "v_exp_f32 v51, v51\n"
  "v_mfma_f32_16x16x32_bf16 v[100:103], v[8:11], a[120:123], v[100:103]\n"
  "v_cvt_pk_bf16_f32 v12, v0, v1\n"
  "v_cvt_pk_bf16_f32 v13, v2, v3\n"
  "v_mfma_f32_16x16x32_bf16 v[132:135], v[28:31], a[124:127], v[132:135]\n"
  "ds_read_b64_tr_b16 a[140:141], v219 offset:2048\n"
  "v_cvt_pk_bf16_f32 v28, v16, v17\n"
  "v_mfma_f32_16x16x32_bf16 v[132:135], v[24:27], a[120:123], v[132:135]\n"
  "ds_read_b64_tr_b16 a[142:143], v220 offset:2048\n"
  "v_cvt_pk_bf16_f32 v29, v18, v19\n"
  "v_mfma_f32_16x16x32_bf16 v[164:167], v[44:47], a[124:127], v[164:167]\n"
  "ds_read_b64_tr_b16 a[136:137], v219 offset:2064\n"
  "v_cvt_pk_bf16_f32 v44, v32, v33\n"
  "v_mfma_f32_16x16x32_bf16 v[164:167], v[40:43], a[120:123], v[164:167]\n"
  "ds_read_b64_tr_b16 a[138:139], v220 offset:2064\n"
  "v_cvt_pk_bf16_f32 v45, v34, v35\n"
  "v_mfma_f32_16x16x32_bf16 v[196:199], v[60:63], a[124:127], v[196:199]\n"
  "v_exp_f32 v36, v36\n"
  "v_mfma_f32_16x16x32_bf16 v[196:199], v[56:59], a[120:123], v[196:199]\n"
  "s_waitcnt lgkmcnt(4)\n"
  "v_add_f32 v255, v255, v49\n"
  "v_add_f32 v255, v255, v50\n"
  "v_mfma_f32_16x16x32_bf16 a[244:247], a[156:159], a[128:131], a[244:247]\n"
  "v_add_f32 v255, v255, v51\n"
  "v_cvt_pk_bf16_f32 v60, v48, v49\n"
  "v_mfma_f32_16x16x32_bf16 a[248:251], a[156:159], a[132:135], a[248:251]\n"
  "v_cvt_pk_bf16_f32 v61, v50, v51\n"
  "v_fma_f32 v52, v52, s73, v226\n"
  "v_mfma_f32_16x16x32_bf16 v[200:203], v[56:59], a[128:131], v[200:203]\n"
  "v_exp_f32 v52, v52\n"
  "v_mfma_f32_16x16x32_bf16 v[204:207], v[56:59], a[132:135], v[204:207]\n"
  "v_fma_f32 v53, v53, s73, v226\n"
  "v_fma_f32 v54, v54, s73, v226\n"
  "v_mfma_f32_16x16x32_bf16 v[168:171], v[40:43], a[128:131], v[168:171]\n"
  "v_exp_f32 v53, v53\n"
  "v_mfma_f32_16x16x32_bf16 v[172:175], v[40:43], a[132:135], v[172:175]\n"
  "s_waitcnt vmcnt(11) lgkmcnt(0)\n"
  "s_barrier\n"
  "ds_read_b128 a[124:127], v216 offset:12352\n"
  "v_mfma_f32_16x16x32_bf16 v[136:139], v[24:27], a[128:131], v[136:139]\n"
  "ds_read_b64 a[160:161], v231 offset:2048\n"
  "v_fma_f32 v55, v55, s73, v226\n"
  "v_mfma_f32_16x16x32_bf16 v[140:143], v[24:27], a[132:135], v[140:143]\n"
  "ds_read_b64 a[162:163], v232 offset:2048\n"
  "v_cvt_pk_bf16_f32 v62, v52, v53\n"
  "v_mfma_f32_16x16x32_bf16 v[104:107], v[8:11], a[128:131], v[104:107]\n"
  "ds_read_b128 a[120:123], v216 offset:12416\n"
  "v_fma_f32 v37, v37, s73, v225\n"
  "v_mfma_f32_16x16x32_bf16 v[108:111], v[8:11], a[132:135], v[108:111]\n"
  "ds_read_b128 a[128:131], v216 offset:13376\n"
  "v_fma_f32 v38, v38, s73, v225\n"
  "v_mfma_f32_16x16x32_bf16 v[112:115], v[8:11], a[140:143], v[112:115]\n"
  "ds_read_b128 a[132:135], v216 offset:13440\n"
  "v_fma_f32 v39, v39, s73, v225\n"
  "v_mfma_f32_16x16x32_bf16 v[116:119], v[8:11], a[136:139], v[116:119]\n"
  "s_mov_b32 m0, s66\n"
  "ds_read_b128 a[148:151], v216 offset:14400\n"
  "v_fma_f32 v20, v20, s73, v224\n"
  "v_mfma_f32_16x16x32_bf16 v[144:147], v[24:27], a[140:143], v[144:147]\n"
  "ds_read_b128 a[144:147], v216 offset:14464\n"
  "v_fma_f32 v21, v21, s73, v224\n"
  "v_mfma_f32_16x16x32_bf16 v[148:151], v[24:27], a[136:139], v[148:151]\n"
  "v_exp_f32 v54, v54\n"
  "v_mfma_f32_16x16x32_bf16 v[176:179], v[40:43], a[140:143], v[176:179]\n"
  "v_exp_f32 v55, v55\n"
  "v_mfma_f32_16x16x32_bf16 v[180:183], v[40:43], a[136:139], v[180:183]\n"
  "buffer_load_dwordx4 v221, s[40:43], 0 offen offset:0 lds\n"
  "v_mfma_f32_16x16x32_bf16 v[208:211], v[56:59], a[140:143], v[208:211]\n"
  "s_addk_i32 m0, 896\n"
  "v_exp_f32 v37, v37\n"
  "v_mfma_f32_16x16x32_bf16 v[212:215], v[56:59], a[136:139], v[212:215]\n"
  "s_waitcnt lgkmcnt(4)\n"
  "v_exp_f32 v38, v38\n"
  "v_mfma_f32_16x16x32_bf16 v[64:67], a[124:127], a[96:99], v[248:251]\n"
  "v_exp_f32 v39, v39\n"
  "v_mfma_f32_16x16x32_bf16 v[64:67], a[120:123], a[100:103], v[64:67]\n"
  "s_waitcnt lgkmcnt(2)\n"
  "buffer_load_dwordx4 v221, s[40:43], 0 offen offset:128 lds\n"
  "v_cvt_pk_bf16_f32 v63, v54, v55\n"
  "v_mfma_f32_16x16x32_bf16 v[64:67], a[128:131], a[104:107], v[64:67]\n"
  "s_addk_i32 m0, 896\n"
  "v_exp_f32 v20, v20\n"
  "v_mfma_f32_16x16x32_bf16 v[64:67], a[132:135], a[108:111], v[64:67]\n"
  "s_waitcnt lgkmcnt(0)\n"
  "v_exp_f32 v21, v21\n"
  "v_mfma_f32_16x16x32_bf16 v[64:67], a[148:151], a[112:115], v[64:67]\n"
  "v_cvt_pk_bf16_f32 v46, v36, v37\n"
  "v_cvt_pk_bf16_f32 v47, v38, v39\n"
  "v_mfma_f32_16x16x32_bf16 v[64:67], a[144:147], a[116:119], v[64:67]\n"
  "buffer_load_dwordx4 v221, s[40:43], 0 offen offset:256 lds\n"
  "v_fma_f32 v22, v22, s73, v224\n"
  "v_mfma_f32_16x16x32_bf16 v[0:3], a[144:147], a[20:23], v[248:251]\n"
  "s_add_u32 s40, s40, s31\n"
  "v_exp_f32 v22, v22\n"
  "v_mfma_f32_16x16x32_bf16 v[0:3], a[148:151], a[16:19], v[0:3]\n"
  "s_addc_u32 s41, s41, 0\n"
  "v_mbcnt_lo_u32_b32 v236, -1, 0\n"
  "v_mbcnt_hi_u32_b32 v236, -1, v236\n"
  "v_mfma_f32_16x16x32_bf16 v[0:3], a[132:135], a[12:15], v[0:3]\n"
  "s_sub_u32 s79, s96, s39\n"
  "v_mul_u32_u24 v236, 0x18c, v236\n"
  "v_fma_f32 v23, v23, s73, v224\n"
  "v_mfma_f32_16x16x32_bf16 v[0:3], a[128:131], a[8:11], v[0:3]\n"
  "s_mov_b32 m0, s58\n"
  "v_exp_f32 v23, v23\n"
  "v_mfma_f32_16x16x32_bf16 v[0:3], a[120:123], a[4:7], v[0:3]\n"
  "v_cvt_pk_bf16_f32 v30, v20, v21\n"
  "v_cvt_pk_bf16_f32 v31, v22, v23\n"
  "v_mfma_f32_16x16x32_bf16 v[0:3], a[124:127], a[0:3], v[0:3]\n"
  "v_fma_f32 v4, v4, s73, v223\n"
  "v_fma_f32 v5, v5, s73, v223\n"
  "v_mfma_f32_16x16x32_bf16 v[16:19], a[124:127], a[24:27], v[248:251]\n"
  "v_exp_f32 v4, v4\n"
  "v_mfma_f32_16x16x32_bf16 v[16:19], a[120:123], a[28:31], v[16:19]\n"
  "buffer_load_dwordx4 v221, s[32:35], 0 offen offset:0 lds\n"
  "v_fma_f32 v6, v6, s73, v223\n"
  "v_mfma_f32_16x16x32_bf16 v[16:19], a[128:131], a[32:35], v[16:19]\n"
  "s_addk_i32 m0, 1024\n"
  "ds_read_b64_tr_b16 a[140:141], v217 offset:12352\n"
  "v_fma_f32 v7, v7, s73, v223\n"
  "v_mfma_f32_16x16x32_bf16 v[16:19], a[132:135], a[36:39], v[16:19]\n"
  "s_mov_b64 s[32:33], s[44:45]\n"
  "ds_read_b64_tr_b16 a[142:143], v218 offset:12352\n"
  "v_add_f32 v252, v252, v4\n"
  "v_mfma_f32_16x16x32_bf16 v[16:19], a[148:151], a[40:43], v[16:19]\n"
  "ds_read_b64_tr_b16 a[136:137], v217 offset:13376\n"
  "v_add_f32 v253, v253, v20\n"
  "v_mfma_f32_16x16x32_bf16 v[16:19], a[144:147], a[44:47], v[16:19]\n"
  "buffer_load_dwordx4 v221, s[44:47], 0 offen offset:0 lds\n"
  "v_add_f32 v253, v253, v21\n"
  "v_mfma_f32_16x16x32_bf16 v[32:35], a[144:147], a[68:71], v[248:251]\n"
  "s_addk_i32 m0, 768\n"
  "ds_read_b64_tr_b16 a[138:139], v218 offset:13376\n"
  "v_add_f32 v253, v253, v22\n"
  "v_mfma_f32_16x16x32_bf16 v[32:35], a[148:151], a[64:67], v[32:35]\n"
  "v_exp_f32 v5, v5\n"
  "v_mfma_f32_16x16x32_bf16 v[32:35], a[132:135], a[60:63], v[32:35]\n"
  "v_exp_f32 v6, v6\n"
  "v_mfma_f32_16x16x32_bf16 v[32:35], a[128:131], a[56:59], v[32:35]\n"
  "buffer_load_dwordx4 v221, s[44:47], 0 offen offset:256 lds\n"
  "v_cvt_pk_bf16_f32 v14, v4, v5\n"
  "v_mfma_f32_16x16x32_bf16 v[32:35], a[120:123], a[52:55], v[32:35]\n"
  "s_add_u32 s44, s44, s31\n"
  "v_exp_f32 v7, v7\n"
  "v_mfma_f32_16x16x32_bf16 v[32:35], a[124:127], a[48:51], v[32:35]\n"
  "s_addc_u32 s45, s45, 0\n"
  "v_cvt_pk_bf16_f32 v15, v6, v7\n"
  "v_add_f32 v252, v252, v5\n"
  "v_mfma_f32_16x16x32_bf16 v[48:51], a[124:127], a[72:75], v[248:251]\n"
  "s_mov_b32 m0, s29\n"
  "v_add_f32 v252, v252, v6\n"
  "v_add_f32 v252, v252, v7\n"
  "v_mfma_f32_16x16x32_bf16 v[48:51], a[120:123], a[76:79], v[48:51]\n"
  "v_add_f32 v253, v253, v23\n"
  "v_add_f32 v254, v254, v36\n"
  "v_mfma_f32_16x16x32_bf16 v[48:51], a[128:131], a[80:83], v[48:51]\n"
  "ds_read_b64_tr_b16 a[124:125], v217 offset:12368\n"
  "v_add_f32 v254, v254, v37\n"
  "v_mfma_f32_16x16x32_bf16 v[48:51], a[132:135], a[84:87], v[48:51]\n"
  "buffer_load_dword v236, s[88:91], s79 offen lds\n"
  "v_add_f32 v254, v254, v38\n"
  "v_mfma_f32_16x16x32_bf16 v[48:51], a[148:151], a[88:91], v[48:51]\n"
  "ds_read_b64_tr_b16 a[126:127], v218 offset:12368\n"
  "v_add_f32 v254, v254, v39\n"
  "v_mfma_f32_16x16x32_bf16 v[48:51], a[144:147], a[92:95], v[48:51]\n"
  "s_waitcnt lgkmcnt(2)\n"
  "ds_read_b64_tr_b16 a[120:121], v217 offset:13392\n"
  "v_add_f32 v255, v255, v52\n"
  "v_mfma_f32_16x16x32_bf16 a[228:231], a[156:159], a[140:143], a[228:231]\n"
  "ds_read_b64_tr_b16 a[122:123], v218 offset:13392\n"
  "v_add_f32 v255, v255, v53\n"
  "v_mfma_f32_16x16x32_bf16 a[228:231], a[160:163], a[136:139], a[228:231]\n"
  "v_add_f32 v255, v255, v54\n"
  "v_add_f32 v255, v255, v55\n"
  "v_mfma_f32_16x16x32_bf16 a[212:215], v[56:59], a[140:143], a[212:215]\n"
  "ds_read_b64_tr_b16 v[244:245], v230 offset:7840\n"
  "ds_read_b64_tr_b16 v[246:247], v230 offset:7872\n"
  "v_mfma_f32_16x16x32_bf16 a[212:215], v[60:63], a[136:139], a[212:215]\n"
  "ds_read_b64_tr_b16 a[252:253], v230 offset:17920\n"
  "ds_read_b64_tr_b16 a[254:255], v230 offset:17952\n"
  "v_mfma_f32_16x16x32_bf16 a[196:199], v[40:43], a[140:143], a[196:199]\n"
  "v_fma_f32 v64, v64, s73, v227\n"
  "v_fma_f32 v65, v65, s73, v227\n"
  "v_mfma_f32_16x16x32_bf16 a[196:199], v[44:47], a[136:139], a[196:199]\n"
  "v_exp_f32 v64, v64\n"
  "v_mfma_f32_16x16x32_bf16 a[180:183], v[24:27], a[140:143], a[180:183]\n"
  "ds_read_b64_tr_b16 a[128:129], v219 offset:12352\n"
  "v_fma_f32 v66, v66, s73, v227\n"
  "v_mfma_f32_16x16x32_bf16 a[180:183], v[28:31], a[136:139], a[180:183]\n"
  "ds_read_b64_tr_b16 a[130:131], v220 offset:12352\n"
  "v_fma_f32 v67, v67, s73, v227\n"
  "v_mfma_f32_16x16x32_bf16 a[164:167], v[8:11], a[140:143], a[164:167]\n"
  "ds_read_b64_tr_b16 a[132:133], v219 offset:13376\n"
  "v_fma_f32 v0, v0, s73, v223\n"
  "v_mfma_f32_16x16x32_bf16 a[164:167], v[12:15], a[136:139], a[164:167]\n"
  "ds_read_b64_tr_b16 a[134:135], v220 offset:13376\n"
  "v_fma_f32 v1, v1, s73, v223\n"
  "v_mfma_f32_16x16x32_bf16 v[84:87], a[156:159], a[152:155], v[84:87]\n"
  "v_exp_f32 v65, v65\n"
  "v_mfma_f32_16x16x32_bf16 v[84:87], a[160:163], a[152:155], v[84:87]\n"
  "s_waitcnt lgkmcnt(8)\n"
  "v_exp_f32 v66, v66\n"
  "v_mfma_f32_16x16x32_bf16 a[168:171], v[8:11], a[124:127], a[168:171]\n"
  "v_exp_f32 v67, v67\n"
  "v_mfma_f32_16x16x32_bf16 a[168:171], v[12:15], a[120:123], a[168:171]\n"
  "v_exp_f32 v0, v0\n"
  "v_mfma_f32_16x16x32_bf16 a[184:187], v[24:27], a[124:127], a[184:187]\n"
  "ds_read_b128 a[140:143], v235 offset:12352\n"
  "v_cvt_pk_bf16_f32 v64, v64, v65\n"
  "v_mfma_f32_16x16x32_bf16 a[184:187], v[28:31], a[120:123], a[184:187]\n"
  "ds_read_b128 a[136:139], v235 offset:12416\n"
  "v_cvt_pk_bf16_f32 v65, v66, v67\n"
  "v_mfma_f32_16x16x32_bf16 a[200:203], v[40:43], a[124:127], a[200:203]\n"
  "ds_read_b128 a[148:151], v235 offset:13376\n"
  "v_fma_f32 v2, v2, s73, v223\n"
  "v_mfma_f32_16x16x32_bf16 a[200:203], v[44:47], a[120:123], a[200:203]\n"
  "ds_read_b128 a[144:147], v235 offset:13440\n"
  "v_fma_f32 v3, v3, s73, v223\n"
  "v_mfma_f32_16x16x32_bf16 a[216:219], v[56:59], a[124:127], a[216:219]\n"
  "v_exp_f32 v1, v1\n"
  "v_mfma_f32_16x16x32_bf16 a[216:219], v[60:63], a[120:123], a[216:219]\n"
  "v_exp_f32 v2, v2\n"
  "v_mfma_f32_16x16x32_bf16 a[232:235], a[156:159], a[124:127], a[232:235]\n"
  "ds_read_b128 a[124:127], v235 offset:14400\n"
  "v_add_f32 v252, v252, v0\n"
  "v_mfma_f32_16x16x32_bf16 a[232:235], a[160:163], a[120:123], a[232:235]\n"
  "s_waitcnt lgkmcnt(5)\n"
  "ds_read_b128 a[120:123], v235 offset:14464\n"
  "v_add_f32 v252, v252, v1\n"
  "v_mfma_f32_16x16x32_bf16 a[220:223], v[56:59], a[128:131], a[220:223]\n"
  "v_exp_f32 v3, v3\n"
  "v_mfma_f32_16x16x32_bf16 a[220:223], v[60:63], a[132:135], a[220:223]\n"
  "ds_write_b64 v231, v[64:65] offset:0\n"
  "v_mfma_f32_16x16x32_bf16 a[204:207], v[40:43], a[128:131], a[204:207]\n"
  "v_add_f32 v252, v252, v2\n"
  "v_add_f32 v252, v252, v3\n"
  "v_mfma_f32_16x16x32_bf16 a[204:207], v[44:47], a[132:135], a[204:207]\n"
  "buffer_store_dwordx4 v[244:247], v228, s[52:55], s83 offen offset:3072 nt\n"
  "v_fma_f32 v16, v16, s73, v224\n"
  "v_exp_f32 v16, v16\n"
  "v_mfma_f32_16x16x32_bf16 a[188:191], v[24:27], a[128:131], a[188:191]\n"
  "ds_read_b64_tr_b16 v[244:245], v230 offset:19040\n"
  "ds_read_b64_tr_b16 v[246:247], v230 offset:19072\n"
  "v_mfma_f32_16x16x32_bf16 a[188:191], v[28:31], a[132:135], a[188:191]\n"
  "v_fma_f32 v17, v17, s73, v224\n"
  "v_fma_f32 v18, v18, s73, v224\n"
  "v_mfma_f32_16x16x32_bf16 a[172:175], v[8:11], a[128:131], a[172:175]\n"
  "v_exp_f32 v17, v17\n"
  "v_mfma_f32_16x16x32_bf16 a[172:175], v[12:15], a[132:135], a[172:175]\n"
  "s_waitcnt lgkmcnt(7)\n"
  "v_exp_f32 v18, v18\n"
  "v_mfma_f32_16x16x32_bf16 v[4:7], a[140:143], a[0:3], v[248:251]\n"
  "v_fma_f32 v19, v19, s73, v224\n"
  "v_add_f32 v253, v253, v16\n"
  "v_mfma_f32_16x16x32_bf16 v[4:7], a[136:139], a[4:7], v[4:7]\n"
  "s_waitcnt lgkmcnt(5)\n"
  "v_exp_f32 v19, v19\n"
  "v_mfma_f32_16x16x32_bf16 v[4:7], a[148:151], a[8:11], v[4:7]\n"
  "v_add_f32 v253, v253, v17\n"
  "v_add_f32 v253, v253, v18\n"
  "v_mfma_f32_16x16x32_bf16 v[4:7], a[144:147], a[12:15], v[4:7]\n"
  "s_waitcnt lgkmcnt(3)\n"
  "v_add_f32 v253, v253, v19\n"
  "v_fma_f32 v32, v32, s73, v225\n"
  "v_mfma_f32_16x16x32_bf16 v[4:7], a[124:127], a[16:19], v[4:7]\n"
  "v_exp_f32 v32, v32\n"
  "v_mfma_f32_16x16x32_bf16 v[4:7], a[120:123], a[20:23], v[4:7]\n"
  "v_fma_f32 v33, v33, s73, v225\n"
  "v_fma_f32 v34, v34, s73, v225\n"
  "v_mfma_f32_16x16x32_bf16 v[20:23], a[120:123], a[44:47], v[248:251]\n"
  "v_exp_f32 v33, v33\n"
  "v_mfma_f32_16x16x32_bf16 v[20:23], a[124:127], a[40:43], v[20:23]\n"
  "v_exp_f32 v34, v34\n"
  "v_mfma_f32_16x16x32_bf16 v[20:23], a[144:147], a[36:39], v[20:23]\n"
  "ds_read_b64_tr_b16 a[128:129], v219 offset:12368\n"
  "v_fma_f32 v35, v35, s73, v225\n"
  "v_mfma_f32_16x16x32_bf16 v[20:23], a[148:151], a[32:35], v[20:23]\n"
  "ds_read_b64_tr_b16 a[130:131], v220 offset:12368\n"
  "v_add_f32 v254, v254, v32\n"
  "v_mfma_f32_16x16x32_bf16 v[20:23], a[136:139], a[28:31], v[20:23]\n"
  "ds_read_b64_tr_b16 a[132:133], v219 offset:13392\n"
  "v_add_f32 v254, v254, v33\n"
  "v_mfma_f32_16x16x32_bf16 v[20:23], a[140:143], a[24:27], v[20:23]\n"
  "ds_read_b64_tr_b16 a[134:135], v220 offset:13392\n"
  "v_add_f32 v254, v254, v34\n"
  "v_mfma_f32_16x16x32_bf16 v[36:39], a[140:143], a[48:51], v[248:251]\n"
  "v_exp_f32 v35, v35\n"
  "v_mfma_f32_16x16x32_bf16 v[36:39], a[136:139], a[52:55], v[36:39]\n"
  "v_add_f32 v254, v254, v35\n"
  "v_fma_f32 v48, v48, s73, v226\n"
  "v_mfma_f32_16x16x32_bf16 v[36:39], a[148:151], a[56:59], v[36:39]\n"
  "v_exp_f32 v48, v48\n"
  "v_mfma_f32_16x16x32_bf16 v[36:39], a[144:147], a[60:63], v[36:39]\n"
  "v_fma_f32 v49, v49, s73, v226\n"
  "v_fma_f32 v50, v50, s73, v226\n"
  "v_mfma_f32_16x16x32_bf16 v[36:39], a[124:127], a[64:67], v[36:39]\n"
  "v_exp_f32_e64 v49, v49\n"
  "v_mfma_f32_16x16x32_bf16 v[36:39], a[120:123], a[68:71], v[36:39]\n"
  "buffer_store_dwordx4 a[252:255], v228, s[52:55], s56 offen offset:0 nt\n"
  "v_exp_f32_e64 v50, v50\n"
  "v_fma_f32 v51, v51, s73, v226\n"
  "v_mfma_f32_16x16x32_bf16 v[52:55], a[120:123], a[92:95], v[248:251]\n"
  "ds_read_b64_tr_b16 a[120:121], v217 offset:14400\n"
  "v_add_f32 v255, v255, v48\n"
  "v_mfma_f32_16x16x32_bf16 v[52:55], a[124:127], a[88:91], v[52:55]\n"
  "ds_read_b64_tr_b16 a[122:123], v218 offset:14400\n"
  "v_add_f32 v255, v255, v49\n"
  "v_mfma_f32_16x16x32_bf16 v[52:55], a[144:147], a[84:87], v[52:55]\n"
  "ds_read_b64_tr_b16 a[124:125], v217 offset:14416\n"
  "v_add_f32 v255, v255, v50\n"
  "v_mfma_f32_16x16x32_bf16 v[52:55], a[148:151], a[80:83], v[52:55]\n"
  "ds_read_b64_tr_b16 a[126:127], v218 offset:14416\n"
  "v_fma_f32 v36, v36, s73, v225\n"
  "v_mfma_f32_16x16x32_bf16 v[52:55], a[136:139], a[76:79], v[52:55]\n"
  "v_exp_f32 v51, v51\n"
  "v_mfma_f32_16x16x32_bf16 v[52:55], a[140:143], a[72:75], v[52:55]\n"
  "s_waitcnt lgkmcnt(4)\n"
  "v_exp_f32 v36, v36\n"
  "v_mfma_f32_16x16x32_bf16 a[176:179], v[8:11], a[128:131], a[176:179]\n"
  "v_cvt_pk_bf16_f32 v8, v0, v1\n"
  "v_cvt_pk_bf16_f32 v9, v2, v3\n"
  "v_mfma_f32_16x16x32_bf16 a[176:179], v[12:15], a[132:135], a[176:179]\n"
  "v_add_f32 v255, v255, v51\n"
  "v_fma_f32 v37, v37, s73, v225\n"
  "v_mfma_f32_16x16x32_bf16 a[192:195], v[24:27], a[128:131], a[192:195]\n"
  "ds_read_b64_tr_b16 a[136:137], v219 offset:14400\n"
  "v_cvt_pk_bf16_f32 v24, v16, v17\n"
  "v_mfma_f32_16x16x32_bf16 a[192:195], v[28:31], a[132:135], a[192:195]\n"
  "ds_read_b64_tr_b16 a[138:139], v220 offset:14400\n"
  "v_cvt_pk_bf16_f32 v25, v18, v19\n"
  "v_mfma_f32_16x16x32_bf16 a[208:211], v[40:43], a[128:131], a[208:211]\n"
  "ds_read_b64_tr_b16 a[140:141], v219 offset:14416\n"
  "v_cvt_pk_bf16_f32 v40, v32, v33\n"
  "v_mfma_f32_16x16x32_bf16 a[208:211], v[44:47], a[132:135], a[208:211]\n"
  "ds_read_b64_tr_b16 a[142:143], v220 offset:14416\n"
  "v_cvt_pk_bf16_f32 v41, v34, v35\n"
  "v_mfma_f32_16x16x32_bf16 a[224:227], v[56:59], a[128:131], a[224:227]\n"
  "v_exp_f32 v37, v37\n"
  "v_mfma_f32_16x16x32_bf16 a[224:227], v[60:63], a[132:135], a[224:227]\n"
  "s_waitcnt lgkmcnt(4)\n"
  "v_cvt_pk_bf16_f32 v56, v48, v49\n"
  "v_cvt_pk_bf16_f32 v57, v50, v51\n"
  "v_mfma_f32_16x16x32_bf16 a[244:247], a[160:163], a[120:123], a[244:247]\n"
  "v_fma_f32 v52, v52, s73, v226\n"
  "v_fma_f32 v53, v53, s73, v226\n"
  "v_mfma_f32_16x16x32_bf16 a[248:251], a[160:163], a[124:127], a[248:251]\n"
  "v_exp_f32 v52, v52\n"
  "v_mfma_f32_16x16x32_bf16 v[200:203], v[60:63], a[120:123], v[200:203]\n"
  "v_exp_f32_e64 v53, v53\n"
  "v_mfma_f32_16x16x32_bf16 v[204:207], v[60:63], a[124:127], v[204:207]\n"
  "buffer_store_dwordx4 v[244:247], v228, s[52:55], s56 offen offset:128 nt\n"
  "v_fma_f32 v54, v54, s73, v226\n"
  "v_exp_f32_e64 v54, v54\n"
  "v_mfma_f32_16x16x32_bf16 v[168:171], v[44:47], a[120:123], v[168:171]\n"
  "v_fma_f32 v55, v55, s73, v226\n"
  "v_cvt_pk_bf16_f32 v58, v52, v53\n"
  "v_mfma_f32_16x16x32_bf16 v[172:175], v[44:47], a[124:127], v[172:175]\n"
  "s_waitcnt vmcnt(12) lgkmcnt(0)\n"
  "s_barrier\n"
  "ds_read_b128 a[128:131], v216 offset:24704\n"
  "v_mfma_f32_16x16x32_bf16 v[136:139], v[28:31], a[120:123], v[136:139]\n"
  "ds_read_b64 a[156:157], v231 offset:0\n"
  "v_fma_f32 v38, v38, s73, v225\n"
  "v_mfma_f32_16x16x32_bf16 v[140:143], v[28:31], a[124:127], v[140:143]\n"
  "ds_read_b64 a[158:159], v232 offset:0\n"
  "v_fma_f32 v39, v39, s73, v225\n"
  "v_mfma_f32_16x16x32_bf16 v[104:107], v[12:15], a[120:123], v[104:107]\n"
  "ds_read_b128 a[132:135], v216 offset:24768\n"
  "v_cvt_pk_bf16_f32 v42, v36, v37\n"
  "v_mfma_f32_16x16x32_bf16 v[108:111], v[12:15], a[124:127], v[108:111]\n"
  "ds_read_b128 a[120:123], v216 offset:25728\n"
  "v_fma_f32 v20, v20, s73, v224\n"
  "v_mfma_f32_16x16x32_bf16 v[112:115], v[12:15], a[136:139], v[112:115]\n"
  "s_add_u32 s71, s92, s81\n"
  "ds_read_b128 a[124:127], v216 offset:25792\n"
  "v_fma_f32 v21, v21, s73, v224\n"
  "v_mfma_f32_16x16x32_bf16 v[116:119], v[12:15], a[140:143], v[116:119]\n"
  "s_mov_b32 m0, s67\n"
  "ds_read_b128 a[144:147], v216 offset:26752\n"
  "v_fma_f32 v22, v22, s73, v224\n"
  "v_mfma_f32_16x16x32_bf16 v[144:147], v[28:31], a[136:139], v[144:147]\n"
  "s_add_u32 s48, s84, s71\n"
  "ds_read_b128 a[148:151], v216 offset:26816\n"
  "v_fma_f32 v23, v23, s73, v224\n"
  "v_mfma_f32_16x16x32_bf16 v[148:151], v[28:31], a[140:143], v[148:151]\n"
  "s_addc_u32 s49, s85, 0\n"
  "v_exp_f32 v55, v55\n"
  "v_mfma_f32_16x16x32_bf16 v[176:179], v[44:47], a[136:139], v[176:179]\n"
  "s_sub_i32 s71, s38, s81\n"
  "v_exp_f32 v38, v38\n"
  "v_mfma_f32_16x16x32_bf16 v[180:183], v[44:47], a[140:143], v[180:183]\n"
  "buffer_load_dwordx4 v221, s[40:43], 0 offen offset:0 lds\n"
  "v_mfma_f32_16x16x32_bf16 v[208:211], v[60:63], a[136:139], v[208:211]\n"
  "s_addk_i32 m0, 896\n"
  "v_exp_f32 v39, v39\n"
  "v_mfma_f32_16x16x32_bf16 v[212:215], v[60:63], a[140:143], v[212:215]\n"
  "s_waitcnt lgkmcnt(4)\n"
  "v_exp_f32 v20, v20\n"
  "v_mfma_f32_16x16x32_bf16 v[64:67], a[128:131], a[96:99], v[248:251]\n"
  "s_max_i32 s50, s71, 0\n"
  "v_exp_f32 v21, v21\n"
  "v_mfma_f32_16x16x32_bf16 v[64:67], a[132:135], a[100:103], v[64:67]\n"
  "s_waitcnt lgkmcnt(2)\n"
  "buffer_load_dwordx4 v221, s[40:43], 0 offen offset:128 lds\n"
  "v_cvt_pk_bf16_f32 v59, v54, v55\n"
  "v_mfma_f32_16x16x32_bf16 v[64:67], a[120:123], a[104:107], v[64:67]\n"
  "s_addk_i32 m0, 896\n"
  "v_exp_f32 v22, v22\n"
  "v_mfma_f32_16x16x32_bf16 v[64:67], a[124:127], a[108:111], v[64:67]\n"
  "s_waitcnt lgkmcnt(0)\n"
  "v_exp_f32 v23, v23\n"
  "v_mfma_f32_16x16x32_bf16 v[64:67], a[144:147], a[112:115], v[64:67]\n"
  "v_cvt_pk_bf16_f32 v43, v38, v39\n"
  "v_cvt_pk_bf16_f32 v26, v20, v21\n"
  "v_mfma_f32_16x16x32_bf16 v[64:67], a[148:151], a[116:119], v[64:67]\n"
  "buffer_load_dwordx4 v221, s[40:43], 0 offen offset:256 lds\n"
  "v_cvt_pk_bf16_f32 v27, v22, v23\n"
  "v_mfma_f32_16x16x32_bf16 v[0:3], a[148:151], a[20:23], v[248:251]\n"
  "v_fma_f32 v4, v4, s73, v223\n"
  "v_fma_f32 v5, v5, s73, v223\n"
  "v_mfma_f32_16x16x32_bf16 v[0:3], a[144:147], a[16:19], v[0:3]\n"
  "s_add_u32 s71, s92, s56\n"
  "v_exp_f32 v4, v4\n"
  "v_mfma_f32_16x16x32_bf16 v[0:3], a[124:127], a[12:15], v[0:3]\n"
  "s_add_u32 s40, s40, s31\n"
  "v_exp_f32 v5, v5\n"
  "v_mfma_f32_16x16x32_bf16 v[0:3], a[120:123], a[8:11], v[0:3]\n"
  "s_addc_u32 s41, s41, 0\n"
  "v_fma_f32 v6, v6, s73, v223\n"
  "v_fma_f32 v7, v7, s73, v223\n"
  "v_mfma_f32_16x16x32_bf16 v[0:3], a[132:135], a[4:7], v[0:3]\n"
  "s_add_u32 m0, s59, -128\n"
  "v_exp_f32 v6, v6\n"
  "v_mfma_f32_16x16x32_bf16 v[0:3], a[128:131], a[0:3], v[0:3]\n"
  "v_exp_f32 v7, v7\n"
  "v_mfma_f32_16x16x32_bf16 v[16:19], a[128:131], a[24:27], v[248:251]\n"
  "v_cvt_pk_bf16_f32 v10, v4, v5\n"
  "v_cvt_pk_bf16_f32 v11, v6, v7\n"
  "v_mfma_f32_16x16x32_bf16 v[16:19], a[132:135], a[28:31], v[16:19]\n"
  "buffer_load_dwordx4 v221, s[32:35], 0 offen offset:128 lds\n"
  "v_add_f32 v252, v252, v4\n"
  "v_mfma_f32_16x16x32_bf16 v[16:19], a[120:123], a[32:35], v[16:19]\n"
  "s_addk_i32 m0, 1024\n"
  "ds_read_b64_tr_b16 a[136:137], v217 offset:24704\n"
  "v_add_f32 v252, v252, v5\n"
  "v_mfma_f32_16x16x32_bf16 v[16:19], a[124:127], a[36:39], v[16:19]\n"
  "s_mov_b64 s[32:33], s[44:45]\n"
  "ds_read_b64_tr_b16 a[138:139], v218 offset:24704\n"
  "v_add_f32 v252, v252, v6\n"
  "v_mfma_f32_16x16x32_bf16 v[16:19], a[144:147], a[40:43], v[16:19]\n"
  "ds_read_b64_tr_b16 a[140:141], v217 offset:25728\n"
  "v_add_f32 v252, v252, v7\n"
  "v_mfma_f32_16x16x32_bf16 v[16:19], a[148:151], a[44:47], v[16:19]\n"
  "buffer_load_dwordx4 v221, s[44:47], 0 offen offset:128 lds\n"
  "v_add_f32 v253, v253, v20\n"
  "v_mfma_f32_16x16x32_bf16 v[32:35], a[148:151], a[68:71], v[248:251]\n"
  "s_addk_i32 m0, 896\n"
  "ds_read_b64_tr_b16 a[142:143], v218 offset:25728\n"
  "v_add_f32 v253, v253, v21\n"
  "v_mfma_f32_16x16x32_bf16 v[32:35], a[144:147], a[64:67], v[32:35]\n"
  "v_add_f32 v253, v253, v22\n"
  "v_add_f32 v253, v253, v23\n"
  "v_mfma_f32_16x16x32_bf16 v[32:35], a[124:127], a[60:63], v[32:35]\n"
  "v_add_f32 v254, v254, v36\n"
  "v_add_f32 v254, v254, v37\n"
  "v_mfma_f32_16x16x32_bf16 v[32:35], a[120:123], a[56:59], v[32:35]\n"
  "buffer_load_dwordx4 v221, s[44:47], 0 offen offset:256 lds\n"
  "v_add_f32 v254, v254, v38\n"
  "v_mfma_f32_16x16x32_bf16 v[32:35], a[132:135], a[52:55], v[32:35]\n"
  "s_add_u32 s44, s44, s31\n"
  "v_add_f32 v254, v254, v39\n"
  "v_add_f32 v255, v255, v52\n"
  "v_mfma_f32_16x16x32_bf16 v[32:35], a[128:131], a[48:51], v[32:35]\n"
  "s_addc_u32 s45, s45, 0\n"
  "v_add_f32 v255, v255, v53\n"
  "v_add_f32 v255, v255, v54\n"
  "v_mfma_f32_16x16x32_bf16 v[48:51], a[128:131], a[72:75], v[248:251]\n"
  "ds_read_b64_tr_b16 a[128:129], v217 offset:24720\n"
  "v_add_f32 v255, v255, v55\n"
  "v_mfma_f32_16x16x32_bf16 v[48:51], a[132:135], a[76:79], v[48:51]\n"
  "ds_read_b64_tr_b16 a[130:131], v218 offset:24720\n"
  "v_fma_f32 v64, v64, s73, v227\n"
  "v_mfma_f32_16x16x32_bf16 v[48:51], a[120:123], a[80:83], v[48:51]\n"
  "ds_read_b64_tr_b16 a[132:133], v217 offset:25744\n"
  "v_fma_f32 v65, v65, s73, v227\n"
  "v_mfma_f32_16x16x32_bf16 v[48:51], a[124:127], a[84:87], v[48:51]\n"
  "s_add_u32 m0, s72, 6464\n"
  "ds_read_b64_tr_b16 a[134:135], v218 offset:25744\n"
  "v_mfma_f32_16x16x32_bf16 v[48:51], a[144:147], a[88:91], v[48:51]\n"
  "v_exp_f32 v64, v64\n"
  "v_mfma_f32_16x16x32_bf16 v[48:51], a[148:151], a[92:95], v[48:51]\n"
  "s_waitcnt lgkmcnt(4)\n"
  "v_exp_f32 v65, v65\n"
  "v_mfma_f32_16x16x32_bf16 a[236:239], a[160:163], a[136:139], a[236:239]\n"
  "v_fma_f32 v66, v66, s73, v227\n"
  "v_fma_f32 v67, v67, s73, v227\n"
  "v_mfma_f32_16x16x32_bf16 a[236:239], a[156:159], a[140:143], a[236:239]\n"
  "buffer_load_dwordx4 v221, s[48:51], 0 offen offset:256 nt lds\n"
  "v_cvt_pk_bf16_f32 v64, v64, v65\n"
  "v_mfma_f32_16x16x32_bf16 v[184:187], v[60:63], a[136:139], v[184:187]\n"
  "v_exp_f32 v66, v66\n"
  "v_mfma_f32_16x16x32_bf16 v[184:187], v[56:59], a[140:143], v[184:187]\n"
  "v_exp_f32 v67, v67\n"
  "v_mfma_f32_16x16x32_bf16 v[152:155], v[44:47], a[136:139], v[152:155]\n"
  "ds_read_b64_tr_b16 a[120:121], v219 offset:24704\n"
  "v_cvt_pk_bf16_f32 v65, v66, v67\n"
  "v_mfma_f32_16x16x32_bf16 v[152:155], v[40:43], a[140:143], v[152:155]\n"
  "s_add_u32 m0, s72, 4480\n"
  "ds_read_b64_tr_b16 a[122:123], v220 offset:24704\n"
  "v_mfma_f32_16x16x32_bf16 v[120:123], v[28:31], a[136:139], v[120:123]\n"
  "ds_read_b64_tr_b16 a[124:125], v219 offset:25728\n"
  "v_fma_f32 v0, v0, s73, v223\n"
  "v_mfma_f32_16x16x32_bf16 v[120:123], v[24:27], a[140:143], v[120:123]\n"
  "ds_read_b64_tr_b16 a[126:127], v220 offset:25728\n"
  "v_fma_f32 v1, v1, s73, v223\n"
  "v_mfma_f32_16x16x32_bf16 v[88:91], v[12:15], a[136:139], v[88:91]\n"
  "v_exp_f32 v0, v0\n"
  "v_mfma_f32_16x16x32_bf16 v[88:91], v[8:11], a[140:143], v[88:91]\n"
  "s_waitcnt lgkmcnt(4)\n"
  "buffer_load_dwordx4 v221, s[48:51], 0 offen offset:3328 nt lds\n"
  "v_fma_f32 v2, v2, s73, v223\n"
  "v_mfma_f32_16x16x32_bf16 v[92:95], v[12:15], a[128:131], v[92:95]\n"
  "s_add_u32 s48, s84, s71\n"
  "v_exp_f32 v1, v1\n"
  "v_mfma_f32_16x16x32_bf16 v[92:95], v[8:11], a[132:135], v[92:95]\n"
  "s_addc_u32 s49, s85, 0\n"
  "v_exp_f32 v2, v2\n"
  "v_mfma_f32_16x16x32_bf16 v[124:127], v[28:31], a[128:131], v[124:127]\n"
  "s_sub_i32 s71, s38, s56\n"
  "ds_read_b128 a[136:139], v235 offset:24704\n"
  "v_fma_f32 v3, v3, s73, v223\n"
  "v_mfma_f32_16x16x32_bf16 v[124:127], v[24:27], a[132:135], v[124:127]\n"
  "s_add_u32 m0, s72, 17920\n"
  "ds_read_b128 a[140:143], v235 offset:24768\n"
  "v_add_f32 v252, v252, v0\n"
  "v_mfma_f32_16x16x32_bf16 v[156:159], v[44:47], a[128:131], v[156:159]\n"
  "s_max_i32 s50, s71, 0\n"
  "ds_read_b128 a[144:147], v235 offset:25728\n"
  "v_add_f32 v252, v252, v1\n"
  "v_mfma_f32_16x16x32_bf16 v[156:159], v[40:43], a[132:135], v[156:159]\n"
  "ds_read_b128 a[148:151], v235 offset:25792\n"
  "v_add_f32 v252, v252, v2\n"
  "v_mfma_f32_16x16x32_bf16 v[188:191], v[60:63], a[128:131], v[188:191]\n"
  "v_exp_f32 v3, v3\n"
  "v_mfma_f32_16x16x32_bf16 v[188:191], v[56:59], a[132:135], v[188:191]\n"
  "buffer_load_dwordx4 v221, s[48:51], 0 offen offset:0 nt lds\n"
  "v_add_f32 v252, v252, v3\n"
  "v_mfma_f32_16x16x32_bf16 a[240:243], a[160:163], a[128:131], a[240:243]\n"
  "ds_read_b128 a[128:131], v235 offset:26752\n"
  "v_fma_f32 v16, v16, s73, v224\n"
  "v_mfma_f32_16x16x32_bf16 a[240:243], a[156:159], a[132:135], a[240:243]\n"
  "s_waitcnt lgkmcnt(5)\n"
  "ds_read_b128 a[132:135], v235 offset:26816\n"
  "v_fma_f32 v17, v17, s73, v224\n"
  "v_mfma_f32_16x16x32_bf16 v[192:195], v[60:63], a[120:123], v[192:195]\n"
  "v_exp_f32 v16, v16\n"
  "v_mfma_f32_16x16x32_bf16 v[192:195], v[56:59], a[124:127], v[192:195]\n"
  "s_add_u32 m0, s72, 18912\n"
  "ds_write_b64 v231, v[64:65] offset:2048\n"
  "v_mfma_f32_16x16x32_bf16 v[160:163], v[44:47], a[120:123], v[160:163]\n"
  "v_exp_f32 v17, v17\n"
  "v_mfma_f32_16x16x32_bf16 v[160:163], v[40:43], a[124:127], v[160:163]\n"
  "v_fma_f32 v18, v18, s73, v224\n"
  "v_fma_f32 v19, v19, s73, v224\n"
  "v_mfma_f32_16x16x32_bf16 v[128:131], v[28:31], a[120:123], v[128:131]\n"
  "v_exp_f32 v18, v18\n"
  "v_mfma_f32_16x16x32_bf16 v[128:131], v[24:27], a[124:127], v[128:131]\n"
  "buffer_load_dwordx4 v221, s[48:51], 0 offen offset:128 nt lds\n"
  "v_add_f32 v253, v253, v16\n"
  "v_mfma_f32_16x16x32_bf16 v[96:99], v[12:15], a[120:123], v[96:99]\n"
  "v_exp_f32 v19, v19\n"
  "v_mfma_f32_16x16x32_bf16 v[96:99], v[8:11], a[124:127], v[96:99]\n"
  "s_waitcnt lgkmcnt(5)\n"
  "v_add_f32 v253, v253, v17\n"
  "v_add_f32 v253, v253, v18\n"
  "v_mfma_f32_16x16x32_bf16 v[4:7], a[136:139], a[0:3], v[248:251]\n"
  "v_add_f32 v253, v253, v19\n"
  "v_fma_f32 v32, v32, s73, v225\n"
  "v_mfma_f32_16x16x32_bf16 v[4:7], a[140:143], a[4:7], v[4:7]\n"
  "s_waitcnt lgkmcnt(3)\n"
  "v_exp_f32 v32, v32\n"
  "v_mfma_f32_16x16x32_bf16 v[4:7], a[144:147], a[8:11], v[4:7]\n"
  "v_fma_f32 v33, v33, s73, v225\n"
  "v_fma_f32 v34, v34, s73, v225\n"
  "v_mfma_f32_16x16x32_bf16 v[4:7], a[148:151], a[12:15], v[4:7]\n"
  "s_waitcnt lgkmcnt(1)\n"
  "v_exp_f32 v33, v33\n"
  "v_mfma_f32_16x16x32_bf16 v[4:7], a[128:131], a[16:19], v[4:7]\n"
  "v_exp_f32 v34, v34\n"
  "v_mfma_f32_16x16x32_bf16 v[4:7], a[132:135], a[20:23], v[4:7]\n"
  "v_fma_f32 v35, v35, s73, v225\n"
  "v_add_f32 v254, v254, v32\n"
  "v_mfma_f32_16x16x32_bf16 v[20:23], a[132:135], a[44:47], v[248:251]\n"
  "v_exp_f32 v35, v35\n"
  "v_mfma_f32_16x16x32_bf16 v[20:23], a[128:131], a[40:43], v[20:23]\n"
  "v_add_f32 v254, v254, v33\n"
  "v_add_f32 v254, v254, v34\n"
  "v_mfma_f32_16x16x32_bf16 v[20:23], a[148:151], a[36:39], v[20:23]\n"
  "ds_read_b64_tr_b16 a[120:121], v219 offset:24720\n"
  "v_add_f32 v254, v254, v35\n"
  "v_mfma_f32_16x16x32_bf16 v[20:23], a[144:147], a[32:35], v[20:23]\n"
  "ds_read_b64_tr_b16 a[122:123], v220 offset:24720\n"
  "v_fma_f32 v48, v48, s73, v226\n"
  "v_mfma_f32_16x16x32_bf16 v[20:23], a[140:143], a[28:31], v[20:23]\n"
  "ds_read_b64_tr_b16 a[124:125], v219 offset:25744\n"
  "v_fma_f32 v49, v49, s73, v226\n"
  "v_mfma_f32_16x16x32_bf16 v[20:23], a[136:139], a[24:27], v[20:23]\n"
  "ds_read_b64_tr_b16 a[126:127], v220 offset:25744\n"
  "v_fma_f32 v50, v50, s73, v226\n"
  "v_mfma_f32_16x16x32_bf16 v[36:39], a[136:139], a[48:51], v[248:251]\n"
  "v_exp_f32 v48, v48\n"
  "v_mfma_f32_16x16x32_bf16 v[36:39], a[140:143], a[52:55], v[36:39]\n"
  "v_exp_f32 v49, v49\n"
  "v_mfma_f32_16x16x32_bf16 v[36:39], a[144:147], a[56:59], v[36:39]\n"
  "v_exp_f32 v50, v50\n"
  "v_mfma_f32_16x16x32_bf16 v[36:39], a[148:151], a[60:63], v[36:39]\n"
  "v_fma_f32 v51, v51, s73, v226\n"
  "v_add_f32 v255, v255, v48\n"
  "v_mfma_f32_16x16x32_bf16 v[36:39], a[128:131], a[64:67], v[36:39]\n"
  "v_exp_f32 v51, v51\n"
  "v_mfma_f32_16x16x32_bf16 v[36:39], a[132:135], a[68:71], v[36:39]\n"
  "v_add_f32 v255, v255, v49\n"
  "v_add_f32 v255, v255, v50\n"
  "v_mfma_f32_16x16x32_bf16 v[52:55], a[132:135], a[92:95], v[248:251]\n"
  "ds_read_b64_tr_b16 a[132:133], v217 offset:26752\n"
  "v_add_f32 v255, v255, v51\n"
  "v_mfma_f32_16x16x32_bf16 v[52:55], a[128:131], a[88:91], v[52:55]\n"
  "ds_read_b64_tr_b16 a[134:135], v218 offset:26752\n"
  "v_fma_f32 v20, v20, s73, v224\n"
  "v_mfma_f32_16x16x32_bf16 v[52:55], a[148:151], a[84:87], v[52:55]\n"
  "ds_read_b64_tr_b16 a[128:129], v217 offset:26768\n"
  "v_fma_f32 v36, v36, s73, v225\n"
  "v_mfma_f32_16x16x32_bf16 v[52:55], a[144:147], a[80:83], v[52:55]\n"
  "ds_read_b64_tr_b16 a[130:131], v218 offset:26768\n"
  "v_fma_f32 v37, v37, s73, v225\n"
  "v_mfma_f32_16x16x32_bf16 v[52:55], a[140:143], a[76:79], v[52:55]\n"
  "v_exp_f32 v36, v36\n"
  "v_mfma_f32_16x16x32_bf16 v[52:55], a[136:139], a[72:75], v[52:55]\n"
  "s_waitcnt lgkmcnt(4)\n"
  "v_exp_f32 v37, v37\n"
  "v_mfma_f32_16x16x32_bf16 v[100:103], v[12:15], a[120:123], v[100:103]\n"
  "v_cvt_pk_bf16_f32 v12, v0, v1\n"
  "v_cvt_pk_bf16_f32 v13, v2, v3\n"
  "v_mfma_f32_16x16x32_bf16 v[100:103], v[8:11], a[124:127], v[100:103]\n"
  "v_exp_f32 v20, v20\n"
  "v_mfma_f32_16x16x32_bf16 v[132:135], v[28:31], a[120:123], v[132:135]\n"
  "ds_read_b64_tr_b16 a[140:141], v219 offset:26752\n"
  "v_cvt_pk_bf16_f32 v28, v16, v17\n"
  "v_mfma_f32_16x16x32_bf16 v[132:135], v[24:27], a[124:127], v[132:135]\n"
  "ds_read_b64_tr_b16 a[142:143], v220 offset:26752\n"
  "v_cvt_pk_bf16_f32 v29, v18, v19\n"
  "v_mfma_f32_16x16x32_bf16 v[164:167], v[44:47], a[120:123], v[164:167]\n"
  "ds_read_b64_tr_b16 a[136:137], v219 offset:26768\n"
  "v_cvt_pk_bf16_f32 v44, v32, v33\n"
  "v_mfma_f32_16x16x32_bf16 v[164:167], v[40:43], a[124:127], v[164:167]\n"
  "ds_read_b64_tr_b16 a[138:139], v220 offset:26768\n"
  "v_cvt_pk_bf16_f32 v45, v34, v35\n"
  "v_mfma_f32_16x16x32_bf16 v[196:199], v[60:63], a[120:123], v[196:199]\n"
  "v_cvt_pk_bf16_f32 v60, v48, v49\n"
  "v_cvt_pk_bf16_f32 v61, v50, v51\n"
  "v_mfma_f32_16x16x32_bf16 v[196:199], v[56:59], a[124:127], v[196:199]\n"
  "s_waitcnt lgkmcnt(4)\n"
  "v_fma_f32 v52, v52, s73, v226\n"
  "v_fma_f32 v53, v53, s73, v226\n"
  "v_mfma_f32_16x16x32_bf16 a[244:247], a[156:159], a[132:135], a[244:247]\n"
  "v_exp_f32 v52, v52\n"
  "v_mfma_f32_16x16x32_bf16 a[248:251], a[156:159], a[128:131], a[248:251]\n"
  "v_exp_f32 v53, v53\n"
  "v_mfma_f32_16x16x32_bf16 v[200:203], v[56:59], a[132:135], v[200:203]\n"
  "v_fma_f32 v54, v54, s73, v226\n"
  "v_fma_f32 v55, v55, s73, v226\n"
  "v_mfma_f32_16x16x32_bf16 v[204:207], v[56:59], a[128:131], v[204:207]\n"
  "v_exp_f32 v54, v54\n"
  "v_mfma_f32_16x16x32_bf16 v[168:171], v[40:43], a[132:135], v[168:171]\n"
  "v_exp_f32_e64 v55, v55\n"
  "v_mfma_f32_16x16x32_bf16 v[172:175], v[40:43], a[128:131], v[172:175]\n"
  "s_waitcnt vmcnt(14) lgkmcnt(0)\n"
  "s_barrier\n"
  "ds_read_b128 a[120:123], v216 offset:0\n"
  "v_mfma_f32_16x16x32_bf16 v[136:139], v[24:27], a[132:135], v[136:139]\n"
  "ds_read_b64 a[160:161], v231 offset:2048\n"
  "v_cvt_pk_bf16_f32 v62, v52, v53\n"
  "v_mfma_f32_16x16x32_bf16 v[140:143], v[24:27], a[128:131], v[140:143]\n"
  "ds_read_b64 a[162:163], v232 offset:2048\n"
  "v_cvt_pk_bf16_f32 v63, v54, v55\n"
  "v_mfma_f32_16x16x32_bf16 v[104:107], v[8:11], a[132:135], v[104:107]\n"
  "ds_read_b128 a[124:127], v216 offset:64\n"
  "v_fma_f32 v38, v38, s73, v225\n"
  "v_mfma_f32_16x16x32_bf16 v[108:111], v[8:11], a[128:131], v[108:111]\n"
  "ds_read_b128 a[132:135], v216 offset:1024\n"
  "v_fma_f32 v39, v39, s73, v225\n"
  "v_mfma_f32_16x16x32_bf16 v[112:115], v[8:11], a[140:143], v[112:115]\n"
  "ds_read_b128 a[128:131], v216 offset:1088\n"
  "v_cvt_pk_bf16_f32 v46, v36, v37\n"
  "v_mfma_f32_16x16x32_bf16 v[116:119], v[8:11], a[136:139], v[116:119]\n"
  "s_mov_b32 m0, s68\n"
  "ds_read_b128 a[148:151], v216 offset:2048\n"
  "v_fma_f32 v21, v21, s73, v224\n"
  "v_mfma_f32_16x16x32_bf16 v[144:147], v[24:27], a[140:143], v[144:147]\n"
  "ds_read_b128 a[144:147], v216 offset:2112\n"
  "v_fma_f32 v22, v22, s73, v224\n"
  "v_mfma_f32_16x16x32_bf16 v[148:151], v[24:27], a[136:139], v[148:151]\n"
  "v_exp_f32 v38, v38\n"
  "v_mfma_f32_16x16x32_bf16 v[176:179], v[40:43], a[140:143], v[176:179]\n"
  "v_exp_f32 v39, v39\n"
  "v_mfma_f32_16x16x32_bf16 v[180:183], v[40:43], a[136:139], v[180:183]\n"
  "buffer_load_dwordx4 v221, s[40:43], 0 offen offset:0 lds\n"
  "v_mfma_f32_16x16x32_bf16 v[208:211], v[56:59], a[140:143], v[208:211]\n"
  "s_addk_i32 m0, 896\n"
  "v_exp_f32 v21, v21\n"
  "v_mfma_f32_16x16x32_bf16 v[212:215], v[56:59], a[136:139], v[212:215]\n"
  "s_waitcnt lgkmcnt(4)\n"
  "v_exp_f32 v22, v22\n"
  "v_mfma_f32_16x16x32_bf16 v[64:67], a[120:123], a[96:99], v[248:251]\n"
  "v_cvt_pk_bf16_f32 v47, v38, v39\n"
  "v_fma_f32 v23, v23, s73, v224\n"
  "v_mfma_f32_16x16x32_bf16 v[64:67], a[124:127], a[100:103], v[64:67]\n"
  "s_waitcnt lgkmcnt(2)\n"
  "buffer_load_dwordx4 v221, s[40:43], 0 offen offset:128 lds\n"
  "v_cvt_pk_bf16_f32 v30, v20, v21\n"
  "v_mfma_f32_16x16x32_bf16 v[64:67], a[132:135], a[104:107], v[64:67]\n"
  "s_addk_i32 m0, 896\n"
  "v_exp_f32 v23, v23\n"
  "v_mfma_f32_16x16x32_bf16 v[64:67], a[128:131], a[108:111], v[64:67]\n"
  "s_waitcnt lgkmcnt(0)\n"
  "v_cvt_pk_bf16_f32 v31, v22, v23\n"
  "v_fma_f32 v4, v4, s73, v223\n"
  "v_mfma_f32_16x16x32_bf16 v[64:67], a[148:151], a[112:115], v[64:67]\n"
  "v_exp_f32 v4, v4\n"
  "v_mfma_f32_16x16x32_bf16 v[64:67], a[144:147], a[116:119], v[64:67]\n"
  "buffer_load_dwordx4 v221, s[40:43], 0 offen offset:256 lds\n"
  "v_fma_f32 v5, v5, s73, v223\n"
  "v_mfma_f32_16x16x32_bf16 v[0:3], a[144:147], a[20:23], v[248:251]\n"
  "s_add_u32 s40, s40, s31\n"
  "v_exp_f32 v5, v5\n"
  "v_mfma_f32_16x16x32_bf16 v[0:3], a[148:151], a[16:19], v[0:3]\n"
  "s_addc_u32 s41, s41, 0\n"
  "v_fma_f32 v6, v6, s73, v223\n"
  "v_fma_f32 v7, v7, s73, v223\n"
  "v_mfma_f32_16x16x32_bf16 v[0:3], a[128:131], a[12:15], v[0:3]\n"
  "v_exp_f32 v6, v6\n"
  "v_mfma_f32_16x16x32_bf16 v[0:3], a[132:135], a[8:11], v[0:3]\n"
  "s_mov_b32 m0, s60\n"
  "v_exp_f32 v7, v7\n"
  "v_mfma_f32_16x16x32_bf16 v[0:3], a[124:127], a[4:7], v[0:3]\n"
  "v_cvt_pk_bf16_f32 v14, v4, v5\n"
  "v_cvt_pk_bf16_f32 v15, v6, v7\n"
  "v_mfma_f32_16x16x32_bf16 v[0:3], a[120:123], a[0:3], v[0:3]\n"
  "v_add_f32 v252, v252, v4\n"
  "v_add_f32 v252, v252, v5\n"
  "v_mfma_f32_16x16x32_bf16 v[16:19], a[120:123], a[24:27], v[248:251]\n"
  "v_add_f32 v252, v252, v6\n"
  "v_add_f32 v252, v252, v7\n"
  "v_mfma_f32_16x16x32_bf16 v[16:19], a[124:127], a[28:31], v[16:19]\n"
  "buffer_load_dwordx4 v221, s[32:35], 0 offen offset:0 lds\n"
  "v_add_f32 v253, v253, v20\n"
  "v_mfma_f32_16x16x32_bf16 v[16:19], a[132:135], a[32:35], v[16:19]\n"
  "s_addk_i32 m0, 1024\n"
  "ds_read_b64_tr_b16 a[140:141], v217 offset:0\n"
  "v_add_f32 v253, v253, v21\n"
  "v_mfma_f32_16x16x32_bf16 v[16:19], a[128:131], a[36:39], v[16:19]\n"
  "s_mov_b64 s[32:33], s[44:45]\n"
  "ds_read_b64_tr_b16 a[142:143], v218 offset:0\n"
  "v_add_f32 v253, v253, v22\n"
  "v_mfma_f32_16x16x32_bf16 v[16:19], a[148:151], a[40:43], v[16:19]\n"
  "ds_read_b64_tr_b16 a[136:137], v217 offset:1024\n"
  "v_add_f32 v253, v253, v23\n"
  "v_mfma_f32_16x16x32_bf16 v[16:19], a[144:147], a[44:47], v[16:19]\n"
  "buffer_load_dwordx4 v221, s[44:47], 0 offen offset:0 lds\n"
  "v_add_f32 v254, v254, v36\n"
  "v_mfma_f32_16x16x32_bf16 v[32:35], a[144:147], a[68:71], v[248:251]\n"
  "s_addk_i32 m0, 768\n"
  "ds_read_b64_tr_b16 a[138:139], v218 offset:1024\n"
  "v_add_f32 v254, v254, v37\n"
  "v_mfma_f32_16x16x32_bf16 v[32:35], a[148:151], a[64:67], v[32:35]\n"
  "v_add_f32 v254, v254, v38\n"
  "v_add_f32 v254, v254, v39\n"
  "v_mfma_f32_16x16x32_bf16 v[32:35], a[128:131], a[60:63], v[32:35]\n"
  "v_add_f32 v255, v255, v52\n"
  "v_add_f32 v255, v255, v53\n"
  "v_mfma_f32_16x16x32_bf16 v[32:35], a[132:135], a[56:59], v[32:35]\n"
  "buffer_load_dwordx4 v221, s[44:47], 0 offen offset:256 lds\n"
  "v_add_f32 v255, v255, v54\n"
  "v_mfma_f32_16x16x32_bf16 v[32:35], a[124:127], a[52:55], v[32:35]\n"
  "s_add_u32 s44, s44, s31\n"
  "ds_read_b64_tr_b16 a[252:253], v230 offset:20160\n"
  "v_add_f32 v255, v255, v55\n"
  "v_mfma_f32_16x16x32_bf16 v[32:35], a[120:123], a[48:51], v[32:35]\n"
  "s_addc_u32 s45, s45, 0\n"
  "ds_read_b64_tr_b16 a[254:255], v230 offset:20192\n"
  "ds_read_b64_tr_b16 v[244:245], v230 offset:8960\n"
  "v_mfma_f32_16x16x32_bf16 v[48:51], a[120:123], a[72:75], v[248:251]\n"
  "ds_read_b64_tr_b16 v[246:247], v230 offset:8992\n"
  "v_fma_f32 v64, v64, s73, v227\n"
  "v_mfma_f32_16x16x32_bf16 v[48:51], a[124:127], a[76:79], v[48:51]\n"
  "v_exp_f32 v64, v64\n"
  "v_mfma_f32_16x16x32_bf16 v[48:51], a[132:135], a[80:83], v[48:51]\n"
  "ds_read_b64_tr_b16 a[120:121], v217 offset:16\n"
  "v_fma_f32 v65, v65, s73, v227\n"
  "v_mfma_f32_16x16x32_bf16 v[48:51], a[128:131], a[84:87], v[48:51]\n"
  "ds_read_b64_tr_b16 a[122:123], v218 offset:16\n"
  "v_fma_f32 v66, v66, s73, v227\n"
  "v_mfma_f32_16x16x32_bf16 v[48:51], a[148:151], a[88:91], v[48:51]\n"
  "ds_read_b64_tr_b16 a[124:125], v217 offset:1040\n"
  "v_fma_f32 v67, v67, s73, v227\n"
  "v_mfma_f32_16x16x32_bf16 v[48:51], a[144:147], a[92:95], v[48:51]\n"
  "s_waitcnt lgkmcnt(7)\n"
  "ds_read_b64_tr_b16 a[126:127], v218 offset:1040\n"
  "v_fma_f32 v0, v0, s73, v223\n"
  "v_mfma_f32_16x16x32_bf16 a[228:231], a[156:159], a[140:143], a[228:231]\n"
  "v_exp_f32 v65, v65\n"
  "v_mfma_f32_16x16x32_bf16 a[228:231], a[160:163], a[136:139], a[228:231]\n"
  "v_exp_f32 v66, v66\n"
  "v_mfma_f32_16x16x32_bf16 a[212:215], v[56:59], a[140:143], a[212:215]\n"
  "v_exp_f32 v67, v67\n"
  "v_mfma_f32_16x16x32_bf16 a[212:215], v[60:63], a[136:139], a[212:215]\n"
  "v_exp_f32 v0, v0\n"
  "v_mfma_f32_16x16x32_bf16 a[196:199], v[40:43], a[140:143], a[196:199]\n"
  "v_cvt_pk_bf16_f32 v64, v64, v65\n"
  "v_cvt_pk_bf16_f32 v65, v66, v67\n"
  "v_mfma_f32_16x16x32_bf16 a[196:199], v[44:47], a[136:139], a[196:199]\n"
  "v_fma_f32 v1, v1, s73, v223\n"
  "v_fma_f32 v2, v2, s73, v223\n"
  "v_mfma_f32_16x16x32_bf16 a[180:183], v[24:27], a[140:143], a[180:183]\n"
  "ds_read_b64_tr_b16 a[132:133], v219 offset:0\n"
  "v_fma_f32 v3, v3, s73, v223\n"
  "v_mfma_f32_16x16x32_bf16 a[180:183], v[28:31], a[136:139], a[180:183]\n"
  "ds_read_b64_tr_b16 a[134:135], v220 offset:0\n"
  "v_add_f32 v252, v252, v0\n"
  "v_mfma_f32_16x16x32_bf16 a[164:167], v[8:11], a[140:143], a[164:167]\n"
  "ds_read_b64_tr_b16 a[128:129], v219 offset:1024\n"
  "v_fma_f32 v16, v16, s73, v224\n"
  "v_mfma_f32_16x16x32_bf16 a[164:167], v[12:15], a[136:139], a[164:167]\n"
  "ds_read_b64_tr_b16 a[130:131], v220 offset:1024\n"
  "v_fma_f32 v17, v17, s73, v224\n"
  "v_mfma_f32_16x16x32_bf16 v[84:87], a[156:159], a[152:155], v[84:87]\n"
  "v_exp_f32 v1, v1\n"
  "v_mfma_f32_16x16x32_bf16 v[84:87], a[160:163], a[152:155], v[84:87]\n"
  "s_waitcnt lgkmcnt(4)\n"
  "v_exp_f32 v2, v2\n"
  "v_mfma_f32_16x16x32_bf16 a[168:171], v[8:11], a[120:123], a[168:171]\n"
  "v_exp_f32 v3, v3\n"
  "v_mfma_f32_16x16x32_bf16 a[168:171], v[12:15], a[124:127], a[168:171]\n"
  "v_exp_f32 v16, v16\n"
  "v_mfma_f32_16x16x32_bf16 a[184:187], v[24:27], a[120:123], a[184:187]\n"
  "ds_read_b128 a[140:143], v235 offset:0\n"
  "v_add_f32 v252, v252, v1\n"
  "v_mfma_f32_16x16x32_bf16 a[184:187], v[28:31], a[124:127], a[184:187]\n"
  "ds_read_b128 a[136:139], v235 offset:64\n"
  "v_add_f32 v252, v252, v2\n"
  "v_mfma_f32_16x16x32_bf16 a[200:203], v[40:43], a[120:123], a[200:203]\n"
  "ds_read_b128 a[148:151], v235 offset:1024\n"
  "v_add_f32 v252, v252, v3\n"
  "v_mfma_f32_16x16x32_bf16 a[200:203], v[44:47], a[124:127], a[200:203]\n"
  "ds_read_b128 a[144:147], v235 offset:1088\n"
  "v_fma_f32 v18, v18, s73, v224\n"
  "v_mfma_f32_16x16x32_bf16 a[216:219], v[56:59], a[120:123], a[216:219]\n"
  "v_exp_f32 v17, v17\n"
  "v_mfma_f32_16x16x32_bf16 a[216:219], v[60:63], a[124:127], a[216:219]\n"
  "v_exp_f32 v18, v18\n"
  "v_mfma_f32_16x16x32_bf16 a[232:235], a[156:159], a[120:123], a[232:235]\n"
  "ds_read_b128 a[120:123], v235 offset:2048\n"
  "v_fma_f32 v19, v19, s73, v224\n"
  "v_mfma_f32_16x16x32_bf16 a[232:235], a[160:163], a[124:127], a[232:235]\n"
  "s_waitcnt lgkmcnt(5)\n"
  "ds_read_b128 a[124:127], v235 offset:2112\n"
  "v_add_f32 v253, v253, v16\n"
  "v_mfma_f32_16x16x32_bf16 a[220:223], v[56:59], a[132:135], a[220:223]\n"
  "v_exp_f32_e64 v19, v19\n"
  "v_mfma_f32_16x16x32_bf16 a[220:223], v[60:63], a[128:131], a[220:223]\n"
  "ds_write_b64 v231, v[64:65] offset:0\n"
  "v_mfma_f32_16x16x32_bf16 a[204:207], v[40:43], a[132:135], a[204:207]\n"
  "v_add_f32 v253, v253, v17\n"
  "v_add_f32 v253, v253, v18\n"
  "v_mfma_f32_16x16x32_bf16 a[204:207], v[44:47], a[128:131], a[204:207]\n"
  "buffer_store_dwordx4 a[252:255], v228, s[52:55], s56 offen offset:256 nt\n"
  "v_add_f32 v253, v253, v19\n"
  "v_fma_f32 v32, v32, s73, v225\n"
  "v_fma_f32 v33, v33, s73, v225\n"
  "v_mfma_f32_16x16x32_bf16 a[188:191], v[24:27], a[132:135], a[188:191]\n"
  "ds_read_b64_tr_b16 a[252:253], v230 offset:10080\n"
  "ds_read_b64_tr_b16 a[254:255], v230 offset:10112\n"
  "v_mfma_f32_16x16x32_bf16 a[188:191], v[28:31], a[128:131], a[188:191]\n"
  "v_exp_f32 v32, v32\n"
  "v_mfma_f32_16x16x32_bf16 a[172:175], v[8:11], a[132:135], a[172:175]\n"
  "v_exp_f32 v33, v33\n"
  "v_mfma_f32_16x16x32_bf16 a[172:175], v[12:15], a[128:131], a[172:175]\n"
  "s_waitcnt lgkmcnt(7)\n"
  "v_fma_f32 v34, v34, s73, v225\n"
  "v_fma_f32 v35, v35, s73, v225\n"
  "v_mfma_f32_16x16x32_bf16 v[4:7], a[140:143], a[0:3], v[248:251]\n"
  "v_exp_f32 v34, v34\n"
  "v_mfma_f32_16x16x32_bf16 v[4:7], a[136:139], a[4:7], v[4:7]\n"
  "s_waitcnt lgkmcnt(5)\n"
  "v_exp_f32 v35, v35\n"
  "v_mfma_f32_16x16x32_bf16 v[4:7], a[148:151], a[8:11], v[4:7]\n"
  "v_add_f32 v254, v254, v32\n"
  "v_add_f32 v254, v254, v33\n"
  "v_mfma_f32_16x16x32_bf16 v[4:7], a[144:147], a[12:15], v[4:7]\n"
  "s_waitcnt lgkmcnt(3)\n"
  "v_add_f32 v254, v254, v34\n"
  "v_add_f32 v254, v254, v35\n"
  "v_mfma_f32_16x16x32_bf16 v[4:7], a[120:123], a[16:19], v[4:7]\n"
  "v_fma_f32 v48, v48, s73, v226\n"
  "v_fma_f32 v49, v49, s73, v226\n"
  "v_mfma_f32_16x16x32_bf16 v[4:7], a[124:127], a[20:23], v[4:7]\n"
  "v_exp_f32 v48, v48\n"
  "v_mfma_f32_16x16x32_bf16 v[20:23], a[124:127], a[44:47], v[248:251]\n"
  "v_exp_f32 v49, v49\n"
  "v_mfma_f32_16x16x32_bf16 v[20:23], a[120:123], a[40:43], v[20:23]\n"
  "v_fma_f32 v50, v50, s73, v226\n"
  "v_fma_f32 v51, v51, s73, v226\n"
  "v_mfma_f32_16x16x32_bf16 v[20:23], a[144:147], a[36:39], v[20:23]\n"
  "ds_read_b64_tr_b16 a[132:133], v219 offset:16\n"
  "v_add_f32 v255, v255, v48\n"
  "v_mfma_f32_16x16x32_bf16 v[20:23], a[148:151], a[32:35], v[20:23]\n"
  "ds_read_b64_tr_b16 a[134:135], v220 offset:16\n"
  "v_add_f32 v255, v255, v49\n"
  "v_mfma_f32_16x16x32_bf16 v[20:23], a[136:139], a[28:31], v[20:23]\n"
  "ds_read_b64_tr_b16 a[128:129], v219 offset:1040\n"
  "v_fma_f32 v4, v4, s73, v223\n"
  "v_mfma_f32_16x16x32_bf16 v[20:23], a[140:143], a[24:27], v[20:23]\n"
  "ds_read_b64_tr_b16 a[130:131], v220 offset:1040\n"
  "v_fma_f32 v5, v5, s73, v223\n"
  "v_mfma_f32_16x16x32_bf16 v[36:39], a[140:143], a[48:51], v[248:251]\n"
  "v_exp_f32 v50, v50\n"
  "v_mfma_f32_16x16x32_bf16 v[36:39], a[136:139], a[52:55], v[36:39]\n"
  "v_exp_f32 v51, v51\n"
  "v_mfma_f32_16x16x32_bf16 v[36:39], a[148:151], a[56:59], v[36:39]\n"
  "v_exp_f32 v4, v4\n"
  "v_mfma_f32_16x16x32_bf16 v[36:39], a[144:147], a[60:63], v[36:39]\n"
  "v_exp_f32 v5, v5\n"
  "v_mfma_f32_16x16x32_bf16 v[36:39], a[120:123], a[64:67], v[36:39]\n"
  "v_add_f32 v255, v255, v50\n"
  "v_add_f32 v255, v255, v51\n"
  "v_mfma_f32_16x16x32_bf16 v[36:39], a[124:127], a[68:71], v[36:39]\n"
  "buffer_store_dwordx4 v[244:247], v228, s[52:55], s80 offen offset:128 nt\n"
  "v_fma_f32 v20, v20, s73, v224\n"
  "v_exp_f32_e64 v20, v20\n"
  "v_mfma_f32_16x16x32_bf16 v[52:55], a[124:127], a[92:95], v[248:251]\n"
  "ds_read_b64_tr_b16 a[124:125], v217 offset:2048\n"
  "v_fma_f32 v21, v21, s73, v224\n"
  "v_mfma_f32_16x16x32_bf16 v[52:55], a[120:123], a[88:91], v[52:55]\n"
  "ds_read_b64_tr_b16 a[126:127], v218 offset:2048\n"
  "v_fma_f32 v22, v22, s73, v224\n"
  "v_mfma_f32_16x16x32_bf16 v[52:55], a[144:147], a[84:87], v[52:55]\n"
  "ds_read_b64_tr_b16 a[120:121], v217 offset:2064\n"
  "v_fma_f32 v36, v36, s73, v225\n"
  "v_mfma_f32_16x16x32_bf16 v[52:55], a[148:151], a[80:83], v[52:55]\n"
  "ds_read_b64_tr_b16 a[122:123], v218 offset:2064\n"
  "v_fma_f32 v37, v37, s73, v225\n"
  "v_mfma_f32_16x16x32_bf16 v[52:55], a[136:139], a[76:79], v[52:55]\n"
  "v_exp_f32 v36, v36\n"
  "v_mfma_f32_16x16x32_bf16 v[52:55], a[140:143], a[72:75], v[52:55]\n"
  "s_waitcnt lgkmcnt(4)\n"
  "v_exp_f32 v37, v37\n"
  "v_mfma_f32_16x16x32_bf16 a[176:179], v[8:11], a[132:135], a[176:179]\n"
  "v_cvt_pk_bf16_f32 v8, v0, v1\n"
  "v_cvt_pk_bf16_f32 v9, v2, v3\n"
  "v_mfma_f32_16x16x32_bf16 a[176:179], v[12:15], a[128:131], a[176:179]\n"
  "v_exp_f32 v21, v21\n"
  "v_mfma_f32_16x16x32_bf16 a[192:195], v[24:27], a[132:135], a[192:195]\n"
  "ds_read_b64_tr_b16 a[136:137], v219 offset:2048\n"
  "v_cvt_pk_bf16_f32 v24, v16, v17\n"
  "v_mfma_f32_16x16x32_bf16 a[192:195], v[28:31], a[128:131], a[192:195]\n"
  "ds_read_b64_tr_b16 a[138:139], v220 offset:2048\n"
  "v_cvt_pk_bf16_f32 v25, v18, v19\n"
  "v_mfma_f32_16x16x32_bf16 a[208:211], v[40:43], a[132:135], a[208:211]\n"
  "ds_read_b64_tr_b16 a[140:141], v219 offset:2064\n"
  "v_cvt_pk_bf16_f32 v40, v32, v33\n"
  "v_mfma_f32_16x16x32_bf16 a[208:211], v[44:47], a[128:131], a[208:211]\n"
  "ds_read_b64_tr_b16 a[142:143], v220 offset:2064\n"
  "v_cvt_pk_bf16_f32 v41, v34, v35\n"
  "v_mfma_f32_16x16x32_bf16 a[224:227], v[56:59], a[132:135], a[224:227]\n"
  "v_exp_f32 v22, v22\n"
  "v_mfma_f32_16x16x32_bf16 a[224:227], v[60:63], a[128:131], a[224:227]\n"
  "s_waitcnt lgkmcnt(4)\n"
  "v_cvt_pk_bf16_f32 v56, v48, v49\n"
  "v_cvt_pk_bf16_f32 v57, v50, v51\n"
  "v_mfma_f32_16x16x32_bf16 a[244:247], a[160:163], a[124:127], a[244:247]\n"
  "v_fma_f32 v52, v52, s73, v226\n"
  "v_fma_f32 v53, v53, s73, v226\n"
  "v_mfma_f32_16x16x32_bf16 a[248:251], a[160:163], a[120:123], a[248:251]\n"
  "v_exp_f32 v52, v52\n"
  "v_mfma_f32_16x16x32_bf16 v[200:203], v[60:63], a[124:127], v[200:203]\n"
  "v_exp_f32 v53, v53\n"
  "v_mfma_f32_16x16x32_bf16 v[204:207], v[60:63], a[120:123], v[204:207]\n"
  "buffer_store_dwordx4 a[252:255], v228, s[52:55], s80 offen offset:3200 nt\n"
  "v_fma_f32 v54, v54, s73, v226\n"
  "v_exp_f32_e64 v54, v54\n"
  "v_mfma_f32_16x16x32_bf16 v[168:171], v[44:47], a[124:127], v[168:171]\n"
  "v_fma_f32 v55, v55, s73, v226\n"
  "v_cvt_pk_bf16_f32 v58, v52, v53\n"
  "v_mfma_f32_16x16x32_bf16 v[172:175], v[44:47], a[120:123], v[172:175]\n"
  "s_waitcnt vmcnt(13) lgkmcnt(0)\n"
  "s_barrier\n"
  "ds_read_b128 a[132:135], v216 offset:12352\n"
  "v_mfma_f32_16x16x32_bf16 v[136:139], v[28:31], a[124:127], v[136:139]\n"
  "ds_read_b64 a[156:157], v231 offset:0\n"
  "v_fma_f32 v38, v38, s73, v225\n"
  "v_mfma_f32_16x16x32_bf16 v[140:143], v[28:31], a[120:123], v[140:143]\n"
  "ds_read_b64 a[158:159], v232 offset:0\n"
  "v_fma_f32 v39, v39, s73, v225\n"
  "v_mfma_f32_16x16x32_bf16 v[104:107], v[12:15], a[124:127], v[104:107]\n"
  "ds_read_b128 a[128:131], v216 offset:12416\n"
  "v_cvt_pk_bf16_f32 v42, v36, v37\n"
  "v_mfma_f32_16x16x32_bf16 v[108:111], v[12:15], a[120:123], v[108:111]\n"
  "ds_read_b128 a[124:127], v216 offset:13376\n"
  "v_fma_f32 v23, v23, s73, v224\n"
  "v_mfma_f32_16x16x32_bf16 v[112:115], v[12:15], a[136:139], v[112:115]\n"
  "ds_read_b128 a[120:123], v216 offset:13440\n"
  "v_cvt_pk_bf16_f32 v26, v20, v21\n"
  "v_mfma_f32_16x16x32_bf16 v[116:119], v[12:15], a[140:143], v[116:119]\n"
  "s_mov_b32 m0, s66\n"
  "ds_read_b128 a[144:147], v216 offset:14400\n"
  "v_fma_f32 v6, v6, s73, v223\n"
  "v_mfma_f32_16x16x32_bf16 v[144:147], v[28:31], a[136:139], v[144:147]\n"
  "ds_read_b128 a[148:151], v216 offset:14464\n"
  "v_fma_f32 v7, v7, s73, v223\n"
  "v_mfma_f32_16x16x32_bf16 v[148:151], v[28:31], a[140:143], v[148:151]\n"
  "v_exp_f32 v55, v55\n"
  "v_mfma_f32_16x16x32_bf16 v[176:179], v[44:47], a[136:139], v[176:179]\n"
  "v_exp_f32 v38, v38\n"
  "v_mfma_f32_16x16x32_bf16 v[180:183], v[44:47], a[140:143], v[180:183]\n"
  "buffer_load_dwordx4 v221, s[40:43], 0 offen offset:0 lds\n"
  "v_mfma_f32_16x16x32_bf16 v[208:211], v[60:63], a[136:139], v[208:211]\n"
  "s_addk_i32 m0, 896\n"
  "v_exp_f32 v39, v39\n"
  "v_mfma_f32_16x16x32_bf16 v[212:215], v[60:63], a[140:143], v[212:215]\n"
  "s_waitcnt lgkmcnt(4)\n"
  "v_exp_f32 v23, v23\n"
  "v_mfma_f32_16x16x32_bf16 v[64:67], a[132:135], a[96:99], v[248:251]\n"
  "s_add_u32 s71, s92, s81\n"
  "v_exp_f32 v6, v6\n"
  "v_mfma_f32_16x16x32_bf16 v[64:67], a[128:131], a[100:103], v[64:67]\n"
  "s_waitcnt lgkmcnt(2)\n"
  "buffer_load_dwordx4 v221, s[40:43], 0 offen offset:128 lds\n"
  "v_cvt_pk_bf16_f32 v59, v54, v55\n"
  "v_mfma_f32_16x16x32_bf16 v[64:67], a[124:127], a[104:107], v[64:67]\n"
  "s_addk_i32 m0, 896\n"
  "v_exp_f32 v7, v7\n"
  "v_mfma_f32_16x16x32_bf16 v[64:67], a[120:123], a[108:111], v[64:67]\n"
  "s_waitcnt lgkmcnt(0)\n"
  "v_cvt_pk_bf16_f32 v43, v38, v39\n"
  "v_cvt_pk_bf16_f32 v27, v22, v23\n"
  "v_mfma_f32_16x16x32_bf16 v[64:67], a[144:147], a[112:115], v[64:67]\n"
  "v_cvt_pk_bf16_f32 v10, v4, v5\n"
  "v_cvt_pk_bf16_f32 v11, v6, v7\n"
  "v_mfma_f32_16x16x32_bf16 v[64:67], a[148:151], a[116:119], v[64:67]\n"
  "buffer_load_dwordx4 v221, s[40:43], 0 offen offset:256 lds\n"
  "v_add_f32 v252, v252, v4\n"
  "v_mfma_f32_16x16x32_bf16 v[0:3], a[148:151], a[20:23], v[248:251]\n"
  "s_add_u32 s40, s40, s31\n"
  "v_add_f32 v252, v252, v5\n"
  "v_add_f32 v252, v252, v6\n"
  "v_mfma_f32_16x16x32_bf16 v[0:3], a[144:147], a[16:19], v[0:3]\n"
  "s_addc_u32 s41, s41, 0\n"
  "v_add_f32 v252, v252, v7\n"
  "v_add_f32 v253, v253, v20\n"
  "v_mfma_f32_16x16x32_bf16 v[0:3], a[120:123], a[12:15], v[0:3]\n"
  "v_add_f32 v253, v253, v21\n"
  "v_add_f32 v253, v253, v22\n"
  "v_mfma_f32_16x16x32_bf16 v[0:3], a[124:127], a[8:11], v[0:3]\n"
  "s_add_u32 m0, s58, -128\n"
  "v_add_f32 v253, v253, v23\n"
  "v_add_f32 v254, v254, v36\n"
  "v_mfma_f32_16x16x32_bf16 v[0:3], a[128:131], a[4:7], v[0:3]\n"
  "v_add_f32 v254, v254, v37\n"
  "v_add_f32 v254, v254, v38\n"
  "v_mfma_f32_16x16x32_bf16 v[0:3], a[132:135], a[0:3], v[0:3]\n"
  "v_add_f32 v254, v254, v39\n"
  "v_bfe_u32 v237, v231, 1, 8\n"
  "v_mfma_f32_16x16x32_bf16 v[16:19], a[132:135], a[24:27], v[248:251]\n"
  "v_add_u32 v237, s29, v237\n"
  "v_add_f32 v255, v255, v52\n"
  "v_mfma_f32_16x16x32_bf16 v[16:19], a[128:131], a[28:31], v[16:19]\n"
  "buffer_load_dwordx4 v221, s[32:35], 0 offen offset:128 lds\n"
  "ds_read_b32 v236, v237\n"
  "v_mfma_f32_16x16x32_bf16 v[16:19], a[124:127], a[32:35], v[16:19]\n"
  "s_addk_i32 m0, 1024\n"
  "ds_read_b64_tr_b16 a[136:137], v217 offset:12352\n"
  "v_add_f32 v255, v255, v53\n"
  "v_mfma_f32_16x16x32_bf16 v[16:19], a[120:123], a[36:39], v[16:19]\n"
  "s_mov_b64 s[32:33], s[44:45]\n"
  "ds_read_b64_tr_b16 a[138:139], v218 offset:12352\n"
  "v_add_f32 v255, v255, v54\n"
  "v_mfma_f32_16x16x32_bf16 v[16:19], a[144:147], a[40:43], v[16:19]\n"
  "ds_read_b64_tr_b16 a[140:141], v217 offset:13376\n"
  "v_add_f32 v255, v255, v55\n"
  "v_mfma_f32_16x16x32_bf16 v[16:19], a[148:151], a[44:47], v[16:19]\n"
  "buffer_load_dwordx4 v221, s[44:47], 0 offen offset:128 lds\n"
  "v_fma_f32 v64, v64, s73, v227\n"
  "v_mfma_f32_16x16x32_bf16 v[32:35], a[148:151], a[68:71], v[248:251]\n"
  "s_addk_i32 m0, 896\n"
  "ds_read_b64_tr_b16 a[142:143], v218 offset:13376\n"
  "v_fma_f32 v65, v65, s73, v227\n"
  "v_mfma_f32_16x16x32_bf16 v[32:35], a[144:147], a[64:67], v[32:35]\n"
  "s_waitcnt lgkmcnt(4)\n"
  "v_exp_f32 v64, v64\n"
  "v_mfma_f32_16x16x32_bf16 v[32:35], a[120:123], a[60:63], v[32:35]\n"
  "v_exp_f32 v65, v65\n"
  "v_mfma_f32_16x16x32_bf16 v[32:35], a[124:127], a[56:59], v[32:35]\n"
  "buffer_load_dwordx4 v221, s[44:47], 0 offen offset:256 lds\n"
  "v_fma_f32 v66, v66, s73, v227\n"
  "v_mfma_f32_16x16x32_bf16 v[32:35], a[128:131], a[52:55], v[32:35]\n"
  "s_add_u32 s44, s44, s31\n"
  "v_exp_f32 v66, v66\n"
  "v_mfma_f32_16x16x32_bf16 v[32:35], a[132:135], a[48:51], v[32:35]\n"
  "s_addc_u32 s45, s45, 0\n"
  "v_fma_f32 v67, v67, s73, v227\n"
  "v_cvt_pk_bf16_f32 v64, v64, v65\n"
  "v_mfma_f32_16x16x32_bf16 v[48:51], a[132:135], a[72:75], v[248:251]\n"
  "ds_read_b64_tr_b16 a[132:133], v217 offset:12368\n"
  "v_fma_f32 v0, v0, s73, v223\n"
  "v_mfma_f32_16x16x32_bf16 v[48:51], a[128:131], a[76:79], v[48:51]\n"
  "ds_read_b64_tr_b16 a[134:135], v218 offset:12368\n"
  "v_fma_f32 v1, v1, s73, v223\n"
  "v_mfma_f32_16x16x32_bf16 v[48:51], a[124:127], a[80:83], v[48:51]\n"
  "ds_read_b64_tr_b16 a[128:129], v217 offset:13392\n"
  "v_fma_f32 v2, v2, s73, v223\n"
  "v_mfma_f32_16x16x32_bf16 v[48:51], a[120:123], a[84:87], v[48:51]\n"
  "s_add_u32 m0, s72, 19904\n"
  "ds_read_b64_tr_b16 a[130:131], v218 offset:13392\n"
  "v_mfma_f32_16x16x32_bf16 v[48:51], a[144:147], a[88:91], v[48:51]\n"
  "v_exp_f32 v67, v67\n"
  "v_mfma_f32_16x16x32_bf16 v[48:51], a[148:151], a[92:95], v[48:51]\n"
  "s_waitcnt lgkmcnt(4)\n"
  "v_exp_f32 v0, v0\n"
  "v_mfma_f32_16x16x32_bf16 a[236:239], a[160:163], a[136:139], a[236:239]\n"
  "v_exp_f32 v1, v1\n"
  "v_mfma_f32_16x16x32_bf16 a[236:239], a[156:159], a[140:143], a[236:239]\n"
  "buffer_load_dwordx4 v221, s[48:51], 0 offen offset:256 nt lds\n"
  "v_cvt_pk_bf16_f32 v65, v66, v67\n"
  "v_mfma_f32_16x16x32_bf16 v[184:187], v[60:63], a[136:139], v[184:187]\n"
  "s_add_u32 s48, s84, s71\n"
  "v_exp_f32 v2, v2\n"
  "v_mfma_f32_16x16x32_bf16 v[184:187], v[56:59], a[140:143], v[184:187]\n"
  "s_addc_u32 s49, s85, 0\n"
  "v_fma_f32 v3, v3, s73, v223\n"
  "v_add_f32 v252, v252, v0\n"
  "v_mfma_f32_16x16x32_bf16 v[152:155], v[44:47], a[136:139], v[152:155]\n"
  "s_sub_i32 s71, s38, s81\n"
  "ds_read_b64_tr_b16 a[124:125], v219 offset:12352\n"
  "v_add_f32 v252, v252, v1\n"
  "v_mfma_f32_16x16x32_bf16 v[152:155], v[40:43], a[140:143], v[152:155]\n"
  "s_add_u32 m0, s72, 8832\n"
  "ds_read_b64_tr_b16 a[126:127], v220 offset:12352\n"
  "v_add_f32 v252, v252, v2\n"
  "v_mfma_f32_16x16x32_bf16 v[120:123], v[28:31], a[136:139], v[120:123]\n"
  "s_max_i32 s50, s71, 0\n"
  "ds_read_b64_tr_b16 a[120:121], v219 offset:13376\n"
  "v_fma_f32 v16, v16, s73, v224\n"
  "v_mfma_f32_16x16x32_bf16 v[120:123], v[24:27], a[140:143], v[120:123]\n"
  "ds_read_b64_tr_b16 a[122:123], v220 offset:13376\n"
  "v_fma_f32 v17, v17, s73, v224\n"
  "v_mfma_f32_16x16x32_bf16 v[88:91], v[12:15], a[136:139], v[88:91]\n"
  "v_exp_f32 v3, v3\n"
  "v_mfma_f32_16x16x32_bf16 v[88:91], v[8:11], a[140:143], v[88:91]\n"
  "s_waitcnt lgkmcnt(4)\n"
  "buffer_load_dwordx4 v221, s[48:51], 0 offen offset:128 nt lds\n"
  "v_add_f32 v252, v252, v3\n"
  "v_mfma_f32_16x16x32_bf16 v[92:95], v[12:15], a[132:135], v[92:95]\n"
  "v_exp_f32 v16, v16\n"
  "v_mfma_f32_16x16x32_bf16 v[92:95], v[8:11], a[128:131], v[92:95]\n"
  "v_exp_f32 v17, v17\n"
  "v_mfma_f32_16x16x32_bf16 v[124:127], v[28:31], a[132:135], v[124:127]\n"
  "ds_read_b128 a[136:139], v235 offset:12352\n"
  "v_fma_f32 v18, v18, s73, v224\n"
  "v_mfma_f32_16x16x32_bf16 v[124:127], v[24:27], a[128:131], v[124:127]\n"
  "s_add_u32 m0, s72, 6848\n"
  "ds_read_b128 a[140:143], v235 offset:12416\n"
  "v_add_f32 v253, v253, v16\n"
  "v_mfma_f32_16x16x32_bf16 v[156:159], v[44:47], a[132:135], v[156:159]\n"
  "ds_read_b128 a[144:147], v235 offset:13376\n"
  "v_fma_f32 v19, v19, s73, v224\n"
  "v_mfma_f32_16x16x32_bf16 v[156:159], v[40:43], a[128:131], v[156:159]\n"
  "ds_read_b128 a[148:151], v235 offset:13440\n"
  "v_add_f32 v253, v253, v17\n"
  "v_mfma_f32_16x16x32_bf16 v[188:191], v[60:63], a[132:135], v[188:191]\n"
  "v_exp_f32 v18, v18\n"
  "v_mfma_f32_16x16x32_bf16 v[188:191], v[56:59], a[128:131], v[188:191]\n"
  "buffer_load_dwordx4 v221, s[48:51], 0 offen offset:3200 nt lds\n"
  "v_add_f32 v253, v253, v18\n"
  "v_mfma_f32_16x16x32_bf16 a[240:243], a[160:163], a[132:135], a[240:243]\n"
  "ds_read_b128 a[132:135], v235 offset:14400\n"
  "v_fma_f32 v32, v32, s73, v225\n"
  "v_mfma_f32_16x16x32_bf16 a[240:243], a[156:159], a[128:131], a[240:243]\n"
  "s_waitcnt lgkmcnt(5)\n"
  "ds_read_b128 a[128:131], v235 offset:14464\n"
  "v_fma_f32 v33, v33, s73, v225\n"
  "v_mfma_f32_16x16x32_bf16 v[192:195], v[60:63], a[124:127], v[192:195]\n"
  "v_exp_f32 v19, v19\n"
  "v_mfma_f32_16x16x32_bf16 v[192:195], v[56:59], a[120:123], v[192:195]\n"
  "ds_write_b64 v231, v[64:65] offset:2048\n"
  "v_mfma_f32_16x16x32_bf16 v[160:163], v[44:47], a[124:127], v[160:163]\n"
  "v_exp_f32 v32, v32\n"
  "v_mfma_f32_16x16x32_bf16 v[160:163], v[40:43], a[120:123], v[160:163]\n"
  "v_exp_f32 v33, v33\n"
  "v_mfma_f32_16x16x32_bf16 v[128:131], v[28:31], a[124:127], v[128:131]\n"
  "v_add_f32 v253, v253, v19\n"
  "v_fma_f32 v34, v34, s73, v225\n"
  "v_mfma_f32_16x16x32_bf16 v[128:131], v[24:27], a[120:123], v[128:131]\n"
  "v_exp_f32 v34, v34\n"
  "v_mfma_f32_16x16x32_bf16 v[96:99], v[12:15], a[124:127], v[96:99]\n"
  "v_fma_f32 v35, v35, s73, v225\n"
  "v_add_f32 v254, v254, v32\n"
  "v_mfma_f32_16x16x32_bf16 v[96:99], v[8:11], a[120:123], v[96:99]\n"
  "s_waitcnt lgkmcnt(5)\n"
  "v_exp_f32 v35, v35\n"
  "v_mfma_f32_16x16x32_bf16 v[4:7], a[136:139], a[0:3], v[248:251]\n"
  "v_add_f32 v254, v254, v33\n"
  "v_add_f32 v254, v254, v34\n"
  "v_mfma_f32_16x16x32_bf16 v[4:7], a[140:143], a[4:7], v[4:7]\n"
  "s_waitcnt lgkmcnt(3)\n"
  "v_add_f32 v254, v254, v35\n"
  "v_fma_f32 v48, v48, s73, v226\n"
  "v_mfma_f32_16x16x32_bf16 v[4:7], a[144:147], a[8:11], v[4:7]\n"
  "v_exp_f32 v48, v48\n"
  "v_mfma_f32_16x16x32_bf16 v[4:7], a[148:151], a[12:15], v[4:7]\n"
  "s_waitcnt lgkmcnt(1)\n"
  "v_fma_f32 v49, v49, s73, v226\n"
  "v_fma_f32 v50, v50, s73, v226\n"
  "v_mfma_f32_16x16x32_bf16 v[4:7], a[132:135], a[16:19], v[4:7]\n"
  "v_exp_f32 v49, v49\n"
  "v_mfma_f32_16x16x32_bf16 v[4:7], a[128:131], a[20:23], v[4:7]\n"
  "v_exp_f32 v50, v50\n"
  "v_mfma_f32_16x16x32_bf16 v[20:23], a[128:131], a[44:47], v[248:251]\n"
  "v_fma_f32 v51, v51, s73, v226\n"
  "v_add_f32 v255, v255, v48\n"
  "v_mfma_f32_16x16x32_bf16 v[20:23], a[132:135], a[40:43], v[20:23]\n"
  "v_exp_f32 v51, v51\n"
  "v_mfma_f32_16x16x32_bf16 v[20:23], a[148:151], a[36:39], v[20:23]\n"
  "ds_read_b64_tr_b16 a[124:125], v219 offset:12368\n"
  "v_add_f32 v255, v255, v49\n"
  "v_mfma_f32_16x16x32_bf16 v[20:23], a[144:147], a[32:35], v[20:23]\n"
  "ds_read_b64_tr_b16 a[126:127], v220 offset:12368\n"
  "v_add_f32 v255, v255, v50\n"
  "v_mfma_f32_16x16x32_bf16 v[20:23], a[140:143], a[28:31], v[20:23]\n"
  "ds_read_b64_tr_b16 a[120:121], v219 offset:13392\n"
  "v_add_f32 v255, v255, v51\n"
  "v_mfma_f32_16x16x32_bf16 v[20:23], a[136:139], a[24:27], v[20:23]\n"
  "ds_read_b64_tr_b16 a[122:123], v220 offset:13392\n"
  "v_fma_f32 v4, v4, s73, v223\n"
  "v_mfma_f32_16x16x32_bf16 v[36:39], a[136:139], a[48:51], v[248:251]\n"
  "v_exp_f32 v4, v4\n"
  "v_mfma_f32_16x16x32_bf16 v[36:39], a[140:143], a[52:55], v[36:39]\n"
  "v_fma_f32 v5, v5, s73, v223\n"
  "v_fma_f32 v6, v6, s73, v223\n"
  "v_mfma_f32_16x16x32_bf16 v[36:39], a[144:147], a[56:59], v[36:39]\n"
  "v_exp_f32 v5, v5\n"
  "v_mfma_f32_16x16x32_bf16 v[36:39], a[148:151], a[60:63], v[36:39]\n"
  "v_exp_f32 v6, v6\n"
  "v_mfma_f32_16x16x32_bf16 v[36:39], a[132:135], a[64:67], v[36:39]\n"
  "v_fma_f32 v20, v20, s73, v224\n"
  "v_fma_f32 v21, v21, s73, v224\n"
  "v_mfma_f32_16x16x32_bf16 v[36:39], a[128:131], a[68:71], v[36:39]\n"
  "v_exp_f32 v20, v20\n"
  "v_mfma_f32_16x16x32_bf16 v[52:55], a[128:131], a[92:95], v[248:251]\n"
  "ds_read_b64_tr_b16 a[128:129], v217 offset:14400\n"
  "v_fma_f32 v22, v22, s73, v224\n"
  "v_mfma_f32_16x16x32_bf16 v[52:55], a[132:135], a[88:91], v[52:55]\n"
  "ds_read_b64_tr_b16 a[130:131], v218 offset:14400\n"
  "v_fma_f32 v23, v23, s73, v224\n"
  "v_mfma_f32_16x16x32_bf16 v[52:55], a[148:151], a[84:87], v[52:55]\n"
  "ds_read_b64_tr_b16 a[132:133], v217 offset:14416\n"
  "v_fma_f32 v36, v36, s73, v225\n"
  "v_mfma_f32_16x16x32_bf16 v[52:55], a[144:147], a[80:83], v[52:55]\n"
  "ds_read_b64_tr_b16 a[134:135], v218 offset:14416\n"
  "v_fma_f32 v37, v37, s73, v225\n"
  "v_mfma_f32_16x16x32_bf16 v[52:55], a[140:143], a[76:79], v[52:55]\n"
  "v_exp_f32 v36, v36\n"
  "v_mfma_f32_16x16x32_bf16 v[52:55], a[136:139], a[72:75], v[52:55]\n"
  "s_waitcnt lgkmcnt(4)\n"
  "v_exp_f32 v37, v37\n"
  "v_mfma_f32_16x16x32_bf16 v[100:103], v[12:15], a[124:127], v[100:103]\n"
  "v_cvt_pk_bf16_f32 v12, v0, v1\n"
  "v_cvt_pk_bf16_f32 v13, v2, v3\n"
  "v_mfma_f32_16x16x32_bf16 v[100:103], v[8:11], a[120:123], v[100:103]\n"
  "v_exp_f32 v21, v21\n"
  "v_mfma_f32_16x16x32_bf16 v[132:135], v[28:31], a[124:127], v[132:135]\n"
  "ds_read_b64_tr_b16 a[140:141], v219 offset:14400\n"
  "v_cvt_pk_bf16_f32 v28, v16, v17\n"
  "v_mfma_f32_16x16x32_bf16 v[132:135], v[24:27], a[120:123], v[132:135]\n"
  "ds_read_b64_tr_b16 a[142:143], v220 offset:14400\n"
  "v_cvt_pk_bf16_f32 v29, v18, v19\n"
  "v_mfma_f32_16x16x32_bf16 v[164:167], v[44:47], a[124:127], v[164:167]\n"
  "ds_read_b64_tr_b16 a[136:137], v219 offset:14416\n"
  "v_cvt_pk_bf16_f32 v44, v32, v33\n"
  "v_mfma_f32_16x16x32_bf16 v[164:167], v[40:43], a[120:123], v[164:167]\n"
  "ds_read_b64_tr_b16 a[138:139], v220 offset:14416\n"
  "v_cvt_pk_bf16_f32 v45, v34, v35\n"
  "v_mfma_f32_16x16x32_bf16 v[196:199], v[60:63], a[124:127], v[196:199]\n"
  "v_exp_f32 v22, v22\n"
  "v_mfma_f32_16x16x32_bf16 v[196:199], v[56:59], a[120:123], v[196:199]\n"
  "s_waitcnt lgkmcnt(4)\n"
  "v_exp_f32 v23, v23\n"
  "v_mfma_f32_16x16x32_bf16 a[244:247], a[156:159], a[128:131], a[244:247]\n"
  "v_cvt_pk_bf16_f32 v60, v48, v49\n"
  "v_cvt_pk_bf16_f32 v61, v50, v51\n"
  "v_mfma_f32_16x16x32_bf16 a[248:251], a[156:159], a[132:135], a[248:251]\n"
  "v_fma_f32 v52, v52, s73, v226\n"
  "v_fma_f32 v53, v53, s73, v226\n"
  "v_mfma_f32_16x16x32_bf16 v[200:203], v[56:59], a[128:131], v[200:203]\n"
  "v_exp_f32 v52, v52\n"
  "v_mfma_f32_16x16x32_bf16 v[204:207], v[56:59], a[132:135], v[204:207]\n"
  "v_exp_f32_e64 v53, v53\n"
  "v_mfma_f32_16x16x32_bf16 v[168:171], v[40:43], a[128:131], v[168:171]\n"
  "v_fma_f32 v54, v54, s73, v226\n"
  "v_fma_f32 v55, v55, s73, v226\n"
  "v_mfma_f32_16x16x32_bf16 v[172:175], v[40:43], a[132:135], v[172:175]\n"
  "s_waitcnt vmcnt(12) lgkmcnt(0)\n"
  "s_barrier\n"
  "ds_read_b128 a[124:127], v216 offset:24704\n"
  "v_mfma_f32_16x16x32_bf16 v[136:139], v[24:27], a[128:131], v[136:139]\n"
  "ds_read_b64 a[160:161], v231 offset:2048\n"
  "v_cvt_pk_bf16_f32 v62, v52, v53\n"
  "v_mfma_f32_16x16x32_bf16 v[140:143], v[24:27], a[132:135], v[140:143]\n"
  "ds_read_b64 a[162:163], v232 offset:2048\n"
  "v_fma_f32 v38, v38, s73, v225\n"
  "v_mfma_f32_16x16x32_bf16 v[104:107], v[8:11], a[128:131], v[104:107]\n"
  "ds_read_b128 a[120:123], v216 offset:24768\n"
  "v_fma_f32 v39, v39, s73, v225\n"
  "v_mfma_f32_16x16x32_bf16 v[108:111], v[8:11], a[132:135], v[108:111]\n"
  "ds_read_b128 a[128:131], v216 offset:25728\n"
  "v_cvt_pk_bf16_f32 v46, v36, v37\n"
  "v_mfma_f32_16x16x32_bf16 v[112:115], v[8:11], a[140:143], v[112:115]\n"
  "ds_read_b128 a[132:135], v216 offset:25792\n"
  "v_cvt_pk_bf16_f32 v30, v20, v21\n"
  "v_mfma_f32_16x16x32_bf16 v[116:119], v[8:11], a[136:139], v[116:119]\n"
  "s_mov_b32 m0, s67\n"
  "ds_read_b128 a[148:151], v216 offset:26752\n"
  "v_cvt_pk_bf16_f32 v31, v22, v23\n"
  "v_mfma_f32_16x16x32_bf16 v[144:147], v[24:27], a[140:143], v[144:147]\n"
  "ds_read_b128 a[144:147], v216 offset:26816\n"
  "v_fma_f32 v7, v7, s73, v223\n"
  "v_mfma_f32_16x16x32_bf16 v[148:151], v[24:27], a[136:139], v[148:151]\n"
  "v_exp_f32 v54, v54\n"
  "v_mfma_f32_16x16x32_bf16 v[176:179], v[40:43], a[140:143], v[176:179]\n"
  "v_exp_f32 v55, v55\n"
  "v_mfma_f32_16x16x32_bf16 v[180:183], v[40:43], a[136:139], v[180:183]\n"
  "buffer_load_dwordx4 v221, s[40:43], 0 offen offset:0 lds\n"
  "v_mfma_f32_16x16x32_bf16 v[208:211], v[56:59], a[140:143], v[208:211]\n"
  "s_addk_i32 m0, 896\n"
  "v_exp_f32 v38, v38\n"
  "v_mfma_f32_16x16x32_bf16 v[212:215], v[56:59], a[136:139], v[212:215]\n"
  "s_waitcnt lgkmcnt(4)\n"
  "v_exp_f32 v39, v39\n"
  "v_mfma_f32_16x16x32_bf16 v[64:67], a[124:127], a[96:99], v[248:251]\n"
  "v_exp_f32 v7, v7\n"
  "v_mfma_f32_16x16x32_bf16 v[64:67], a[120:123], a[100:103], v[64:67]\n"
  "s_waitcnt lgkmcnt(2)\n"
  "buffer_load_dwordx4 v221, s[40:43], 0 offen offset:128 lds\n"
  "v_cvt_pk_bf16_f32 v63, v54, v55\n"
  "v_mfma_f32_16x16x32_bf16 v[64:67], a[128:131], a[104:107], v[64:67]\n"
  "s_addk_i32 m0, 896\n"
  "v_cvt_pk_bf16_f32 v47, v38, v39\n"
  "v_cvt_pk_bf16_f32 v14, v4, v5\n"
  "v_mfma_f32_16x16x32_bf16 v[64:67], a[132:135], a[108:111], v[64:67]\n"
  "s_waitcnt lgkmcnt(0)\n"
  "v_cvt_pk_bf16_f32 v15, v6, v7\n"
  "v_add_f32 v252, v252, v4\n"
  "v_mfma_f32_16x16x32_bf16 v[64:67], a[148:151], a[112:115], v[64:67]\n"
  "v_add_f32 v252, v252, v5\n"
  "v_add_f32 v252, v252, v6\n"
  "v_mfma_f32_16x16x32_bf16 v[64:67], a[144:147], a[116:119], v[64:67]\n"
  "buffer_load_dwordx4 v221, s[40:43], 0 offen offset:256 lds\n"
  "v_add_f32 v252, v252, v7\n"
  "v_mfma_f32_16x16x32_bf16 v[0:3], a[144:147], a[20:23], v[248:251]\n"
  "s_add_u32 s40, s40, s31\n"
  "v_add_f32 v253, v253, v20\n"
  "v_add_f32 v253, v253, v21\n"
  "v_mfma_f32_16x16x32_bf16 v[0:3], a[148:151], a[16:19], v[0:3]\n"
  "s_addc_u32 s41, s41, 0\n"
  "v_add_f32 v253, v253, v22\n"
  "v_add_f32 v253, v253, v23\n"
  "v_mfma_f32_16x16x32_bf16 v[0:3], a[132:135], a[12:15], v[0:3]\n"
  "v_add_f32 v254, v254, v36\n"
  "v_add_f32 v254, v254, v37\n"
  "v_mfma_f32_16x16x32_bf16 v[0:3], a[128:131], a[8:11], v[0:3]\n"
  "s_mov_b32 m0, s59\n"
  "v_add_f32 v254, v254, v38\n"
  "v_add_f32 v254, v254, v39\n"
  "v_mfma_f32_16x16x32_bf16 v[0:3], a[120:123], a[4:7], v[0:3]\n"
  "v_add_f32 v255, v255, v52\n"
  "v_add_f32 v255, v255, v53\n"
  "v_mfma_f32_16x16x32_bf16 v[0:3], a[124:127], a[0:3], v[0:3]\n"
  "v_add_f32 v255, v255, v54\n"
  "v_add_f32 v255, v255, v55\n"
  "v_mfma_f32_16x16x32_bf16 v[16:19], a[124:127], a[24:27], v[248:251]\n"
  "ds_read_b64_tr_b16 v[244:245], v230 offset:11200\n"
  "ds_read_b64_tr_b16 v[246:247], v230 offset:11232\n"
  "v_mfma_f32_16x16x32_bf16 v[16:19], a[120:123], a[28:31], v[16:19]\n"
  "buffer_load_dwordx4 v221, s[32:35], 0 offen offset:0 lds\n"
  "ds_read_b64_tr_b16 a[252:253], v230 offset:12320\n"
  "v_mfma_f32_16x16x32_bf16 v[16:19], a[128:131], a[32:35], v[16:19]\n"
  "s_addk_i32 m0, 1024\n"
  "ds_read_b64_tr_b16 a[140:141], v217 offset:24704\n"
  "ds_read_b64_tr_b16 a[254:255], v230 offset:12352\n"
  "v_mfma_f32_16x16x32_bf16 v[16:19], a[132:135], a[36:39], v[16:19]\n"
  "s_mov_b64 s[32:33], s[44:45]\n"
  "ds_read_b64_tr_b16 a[142:143], v218 offset:24704\n"
  "v_fma_f32 v64, v64, s73, v227\n"
  "v_mfma_f32_16x16x32_bf16 v[16:19], a[148:151], a[40:43], v[16:19]\n"
  "ds_read_b64_tr_b16 a[136:137], v217 offset:25728\n"
  "v_fma_f32 v65, v65, s73, v227\n"
  "v_mfma_f32_16x16x32_bf16 v[16:19], a[144:147], a[44:47], v[16:19]\n"
  "buffer_load_dwordx4 v221, s[44:47], 0 offen offset:0 lds\n"
  "v_fma_f32 v66, v66, s73, v227\n"
  "v_mfma_f32_16x16x32_bf16 v[32:35], a[144:147], a[68:71], v[248:251]\n"
  "s_addk_i32 m0, 768\n"
  "ds_read_b64_tr_b16 a[138:139], v218 offset:25728\n"
  "v_fma_f32 v67, v67, s73, v227\n"
  "v_mfma_f32_16x16x32_bf16 v[32:35], a[148:151], a[64:67], v[32:35]\n"
  "v_exp_f32 v64, v64\n"
  "v_mfma_f32_16x16x32_bf16 v[32:35], a[132:135], a[60:63], v[32:35]\n"
  "v_exp_f32 v65, v65\n"
  "v_mfma_f32_16x16x32_bf16 v[32:35], a[128:131], a[56:59], v[32:35]\n"
  "buffer_load_dwordx4 v221, s[44:47], 0 offen offset:256 lds\n"
  "v_cvt_pk_bf16_f32 v64, v64, v65\n"
  "v_mfma_f32_16x16x32_bf16 v[32:35], a[120:123], a[52:55], v[32:35]\n"
  "s_add_u32 s44, s44, s31\n"
  "v_exp_f32 v66, v66\n"
  "v_mfma_f32_16x16x32_bf16 v[32:35], a[124:127], a[48:51], v[32:35]\n"
  "s_addc_u32 s45, s45, 0\n"
  "v_exp_f32 v67, v67\n"
  "v_mfma_f32_16x16x32_bf16 v[48:51], a[124:127], a[72:75], v[248:251]\n"
  "v_cvt_pk_bf16_f32 v65, v66, v67\n"
  "v_fma_f32 v0, v0, s73, v223\n"
  "v_mfma_f32_16x16x32_bf16 v[48:51], a[120:123], a[76:79], v[48:51]\n"
  "v_exp_f32 v0, v0\n"
  "v_mfma_f32_16x16x32_bf16 v[48:51], a[128:131], a[80:83], v[48:51]\n"
  "ds_read_b64_tr_b16 a[124:125], v217 offset:24720\n"
  "v_fma_f32 v1, v1, s73, v223\n"
  "v_mfma_f32_16x16x32_bf16 v[48:51], a[132:135], a[84:87], v[48:51]\n"
  "ds_read_b64_tr_b16 a[126:127], v218 offset:24720\n"
  "v_fma_f32 v2, v2, s73, v223\n"
  "v_mfma_f32_16x16x32_bf16 v[48:51], a[148:151], a[88:91], v[48:51]\n"
  "ds_read_b64_tr_b16 a[120:121], v217 offset:25744\n"
  "v_fma_f32 v3, v3, s73, v223\n"
  "v_mfma_f32_16x16x32_bf16 v[48:51], a[144:147], a[92:95], v[48:51]\n"
  "s_waitcnt lgkmcnt(3)\n"
  "ds_read_b64_tr_b16 a[122:123], v218 offset:25744\n"
  "v_add_f32 v252, v252, v0\n"
  "v_mfma_f32_16x16x32_bf16 a[228:231], a[156:159], a[140:143], a[228:231]\n"
  "v_exp_f32 v1, v1\n"
  "v_mfma_f32_16x16x32_bf16 a[228:231], a[160:163], a[136:139], a[228:231]\n"
  "v_exp_f32 v2, v2\n"
  "v_mfma_f32_16x16x32_bf16 a[212:215], v[56:59], a[140:143], a[212:215]\n"
  "v_exp_f32 v3, v3\n"
  "v_mfma_f32_16x16x32_bf16 a[212:215], v[60:63], a[136:139], a[212:215]\n"
  "v_add_f32 v252, v252, v1\n"
  "v_add_f32 v252, v252, v2\n"
  "v_mfma_f32_16x16x32_bf16 a[196:199], v[40:43], a[140:143], a[196:199]\n"
  "v_add_f32 v252, v252, v3\n"
  "v_fma_f32 v16, v16, s73, v224\n"
  "v_mfma_f32_16x16x32_bf16 a[196:199], v[44:47], a[136:139], a[196:199]\n"
  "v_exp_f32 v16, v16\n"
  "v_mfma_f32_16x16x32_bf16 a[180:183], v[24:27], a[140:143], a[180:183]\n"
  "ds_read_b64_tr_b16 a[128:129], v219 offset:24704\n"
  "v_fma_f32 v17, v17, s73, v224\n"
  "v_mfma_f32_16x16x32_bf16 a[180:183], v[28:31], a[136:139], a[180:183]\n"
  "ds_read_b64_tr_b16 a[130:131], v220 offset:24704\n"
  "v_fma_f32 v18, v18, s73, v224\n"
  "v_mfma_f32_16x16x32_bf16 a[164:167], v[8:11], a[140:143], a[164:167]\n"
  "ds_read_b64_tr_b16 a[132:133], v219 offset:25728\n"
  "v_fma_f32 v19, v19, s73, v224\n"
  "v_mfma_f32_16x16x32_bf16 a[164:167], v[12:15], a[136:139], a[164:167]\n"
  "ds_read_b64_tr_b16 a[134:135], v220 offset:25728\n"
  "v_add_f32 v253, v253, v16\n"
  "v_mfma_f32_16x16x32_bf16 v[84:87], a[156:159], a[152:155], v[84:87]\n"
  "v_exp_f32 v17, v17\n"
  "v_mfma_f32_16x16x32_bf16 v[84:87], a[160:163], a[152:155], v[84:87]\n"
  "s_waitcnt lgkmcnt(4)\n"
  "v_exp_f32 v18, v18\n"
  "v_mfma_f32_16x16x32_bf16 a[168:171], v[8:11], a[124:127], a[168:171]\n"
  "v_exp_f32 v19, v19\n"
  "v_mfma_f32_16x16x32_bf16 a[168:171], v[12:15], a[120:123], a[168:171]\n"
  "v_add_f32 v253, v253, v17\n"
  "v_add_f32 v253, v253, v18\n"
  "v_mfma_f32_16x16x32_bf16 a[184:187], v[24:27], a[124:127], a[184:187]\n"
  "ds_read_b128 a[140:143], v235 offset:24704\n"
  "v_add_f32 v253, v253, v19\n"
  "v_mfma_f32_16x16x32_bf16 a[184:187], v[28:31], a[120:123], a[184:187]\n"
  "ds_read_b128 a[136:139], v235 offset:24768\n"
  "v_fma_f32 v32, v32, s73, v225\n"
  "v_mfma_f32_16x16x32_bf16 a[200:203], v[40:43], a[124:127], a[200:203]\n"
  "ds_read_b128 a[148:151], v235 offset:25728\n"
  "v_fma_f32 v33, v33, s73, v225\n"
  "v_mfma_f32_16x16x32_bf16 a[200:203], v[44:47], a[120:123], a[200:203]\n"
  "ds_read_b128 a[144:147], v235 offset:25792\n"
  "v_fma_f32 v34, v34, s73, v225\n"
  "v_mfma_f32_16x16x32_bf16 a[216:219], v[56:59], a[124:127], a[216:219]\n"
  "v_exp_f32 v32, v32\n"
  "v_mfma_f32_16x16x32_bf16 a[216:219], v[60:63], a[120:123], a[216:219]\n"
  "v_exp_f32 v33, v33\n"
  "v_mfma_f32_16x16x32_bf16 a[232:235], a[156:159], a[124:127], a[232:235]\n"
  "ds_read_b128 a[124:127], v235 offset:26752\n"
  "v_fma_f32 v35, v35, s73, v225\n"
  "v_mfma_f32_16x16x32_bf16 a[232:235], a[160:163], a[120:123], a[232:235]\n"
  "s_waitcnt lgkmcnt(5)\n"
  "ds_read_b128 a[120:123], v235 offset:26816\n"
  "v_add_f32 v254, v254, v32\n"
  "v_mfma_f32_16x16x32_bf16 a[220:223], v[56:59], a[128:131], a[220:223]\n"
  "v_exp_f32 v34, v34\n"
  "v_mfma_f32_16x16x32_bf16 a[220:223], v[60:63], a[132:135], a[220:223]\n"
  "ds_write_b64 v231, v[64:65] offset:0\n"
  "v_mfma_f32_16x16x32_bf16 a[204:207], v[40:43], a[128:131], a[204:207]\n"
  "v_exp_f32_e64 v35, v35\n"
  "v_mfma_f32_16x16x32_bf16 a[204:207], v[44:47], a[132:135], a[204:207]\n"
  "buffer_store_dwordx4 v[244:247], v228, s[52:55], s81 offen offset:128 nt\n"
  "v_add_f32 v254, v254, v33\n"
  "v_add_f32 v254, v254, v34\n"
  "v_add_f32_e64 v254, v254, v35\n"
  "v_mfma_f32_16x16x32_bf16 a[188:191], v[24:27], a[128:131], a[188:191]\n"
  "ds_read_b64_tr_b16 v[244:245], v230 offset:13440\n"
  "ds_read_b64_tr_b16 v[246:247], v230 offset:13472\n"
  "v_mfma_f32_16x16x32_bf16 a[188:191], v[28:31], a[132:135], a[188:191]\n"
  "v_fma_f32 v48, v48, s73, v226\n"
  "v_fma_f32 v49, v49, s73, v226\n"
  "v_mfma_f32_16x16x32_bf16 a[172:175], v[8:11], a[128:131], a[172:175]\n"
  "v_exp_f32 v48, v48\n"
  "v_mfma_f32_16x16x32_bf16 a[172:175], v[12:15], a[132:135], a[172:175]\n"
  "s_waitcnt lgkmcnt(7)\n"
  "v_exp_f32 v49, v49\n"
  "v_mfma_f32_16x16x32_bf16 v[4:7], a[140:143], a[0:3], v[248:251]\n"
  "v_fma_f32 v50, v50, s73, v226\n"
  "v_fma_f32 v51, v51, s73, v226\n"
  "v_mfma_f32_16x16x32_bf16 v[4:7], a[136:139], a[4:7], v[4:7]\n"
  "s_waitcnt lgkmcnt(5)\n"
  "v_exp_f32 v50, v50\n"
  "v_mfma_f32_16x16x32_bf16 v[4:7], a[148:151], a[8:11], v[4:7]\n"
  "v_exp_f32 v51, v51\n"
  "v_mfma_f32_16x16x32_bf16 v[4:7], a[144:147], a[12:15], v[4:7]\n"
  "s_waitcnt lgkmcnt(3)\n"
  "v_add_f32 v255, v255, v48\n"
  "v_add_f32 v255, v255, v49\n"
  "v_mfma_f32_16x16x32_bf16 v[4:7], a[124:127], a[16:19], v[4:7]\n"
  "v_add_f32 v255, v255, v50\n"
  "v_add_f32 v255, v255, v51\n"
  "v_mfma_f32_16x16x32_bf16 v[4:7], a[120:123], a[20:23], v[4:7]\n"
  "v_and_b32 v236, 0x7fff7fff, v236\n"
  "v_lshlrev_b32 v237, 16, v236\n"
  "v_mfma_f32_16x16x32_bf16 v[20:23], a[120:123], a[44:47], v[248:251]\n"
  "v_max_u32 v236, v236, v237\n"
  "v_mfma_f32_16x16x32_bf16 v[20:23], a[124:127], a[40:43], v[20:23]\n"
  "s_nop 0\n"
  "v_max_u32_dpp v236, v236, v236 quad_perm:[1,0,3,2] row_mask:0xf bank_mask:0xf\n"
  "v_mfma_f32_16x16x32_bf16 v[20:23], a[144:147], a[36:39], v[20:23]\n"
  "ds_read_b64_tr_b16 a[128:129], v219 offset:24720\n"
  "v_fma_f32 v4, v4, s73, v223\n"
  "v_mfma_f32_16x16x32_bf16 v[20:23], a[148:151], a[32:35], v[20:23]\n"
  "ds_read_b64_tr_b16 a[130:131], v220 offset:24720\n"
  "v_fma_f32 v5, v5, s73, v223\n"
  "v_mfma_f32_16x16x32_bf16 v[20:23], a[136:139], a[28:31], v[20:23]\n"
  "ds_read_b64_tr_b16 a[132:133], v219 offset:25744\n"
  "v_fma_f32 v6, v6, s73, v223\n"
  "v_mfma_f32_16x16x32_bf16 v[20:23], a[140:143], a[24:27], v[20:23]\n"
  "ds_read_b64_tr_b16 a[134:135], v220 offset:25744\n"
  "v_fma_f32 v7, v7, s73, v223\n"
  "v_mfma_f32_16x16x32_bf16 v[36:39], a[140:143], a[48:51], v[248:251]\n"
  "v_exp_f32 v4, v4\n"
  "v_mfma_f32_16x16x32_bf16 v[36:39], a[136:139], a[52:55], v[36:39]\n"
  "v_exp_f32 v5, v5\n"
  "v_mfma_f32_16x16x32_bf16 v[36:39], a[148:151], a[56:59], v[36:39]\n"
  "v_exp_f32 v6, v6\n"
  "v_mfma_f32_16x16x32_bf16 v[36:39], a[144:147], a[60:63], v[36:39]\n"
  "v_exp_f32 v7, v7\n"
  "v_mfma_f32_16x16x32_bf16 v[36:39], a[124:127], a[64:67], v[36:39]\n"
  "v_fma_f32 v20, v20, s73, v224\n"
  "v_fma_f32 v21, v21, s73, v224\n"
  "v_mfma_f32_16x16x32_bf16 v[36:39], a[120:123], a[68:71], v[36:39]\n"
  "buffer_store_dwordx4 a[252:255], v228, s[52:55], s81 offen offset:3200 nt\n"
  "v_exp_f32_e64 v20, v20\n"
  "v_fma_f32 v22, v22, s73, v224\n"
  "v_mfma_f32_16x16x32_bf16 v[52:55], a[120:123], a[92:95], v[248:251]\n"
  "ds_read_b64_tr_b16 a[120:121], v217 offset:26752\n"
  "v_fma_f32 v23, v23, s73, v224\n"
  "v_mfma_f32_16x16x32_bf16 v[52:55], a[124:127], a[88:91], v[52:55]\n"
  "ds_read_b64_tr_b16 a[122:123], v218 offset:26752\n"
  "v_add_f32 v252, v252, v4\n"
  "v_mfma_f32_16x16x32_bf16 v[52:55], a[144:147], a[84:87], v[52:55]\n"
  "ds_read_b64_tr_b16 a[124:125], v217 offset:26768\n"
  "v_fma_f32 v36, v36, s73, v225\n"
  "v_mfma_f32_16x16x32_bf16 v[52:55], a[148:151], a[80:83], v[52:55]\n"
  "ds_read_b64_tr_b16 a[126:127], v218 offset:26768\n"
  "v_fma_f32 v37, v37, s73, v225\n"
  "v_mfma_f32_16x16x32_bf16 v[52:55], a[136:139], a[76:79], v[52:55]\n"
  "v_exp_f32 v36, v36\n"
  "v_mfma_f32_16x16x32_bf16 v[52:55], a[140:143], a[72:75], v[52:55]\n"
  "s_waitcnt lgkmcnt(4)\n"
  "v_exp_f32 v37, v37\n"
  "v_mfma_f32_16x16x32_bf16 a[176:179], v[8:11], a[128:131], a[176:179]\n"
  "v_cvt_pk_bf16_f32 v8, v0, v1\n"
  "v_cvt_pk_bf16_f32 v9, v2, v3\n"
  "v_mfma_f32_16x16x32_bf16 a[176:179], v[12:15], a[132:135], a[176:179]\n"
  "v_exp_f32 v21, v21\n"
  "v_mfma_f32_16x16x32_bf16 a[192:195], v[24:27], a[128:131], a[192:195]\n"
  "ds_read_b64_tr_b16 a[136:137], v219 offset:26752\n"
  "v_cvt_pk_bf16_f32 v24, v16, v17\n"
  "v_mfma_f32_16x16x32_bf16 a[192:195], v[28:31], a[132:135], a[192:195]\n"
  "ds_read_b64_tr_b16 a[138:139], v220 offset:26752\n"
  "v_cvt_pk_bf16_f32 v25, v18, v19\n"
  "v_mfma_f32_16x16x32_bf16 a[208:211], v[40:43], a[128:131], a[208:211]\n"
  "ds_read_b64_tr_b16 a[140:141], v219 offset:26768\n"
  "v_cvt_pk_bf16_f32 v40, v32, v33\n"
  "v_mfma_f32_16x16x32_bf16 a[208:211], v[44:47], a[132:135], a[208:211]\n"
  "ds_read_b64_tr_b16 a[142:143], v220 offset:26768\n"
  "v_cvt_pk_bf16_f32 v41, v34, v35\n"
  "v_mfma_f32_16x16x32_bf16 a[224:227], v[56:59], a[128:131], a[224:227]\n"
  "v_exp_f32 v22, v22\n"
  "v_mfma_f32_16x16x32_bf16 a[224:227], v[60:63], a[132:135], a[224:227]\n"
  "s_waitcnt lgkmcnt(4)\n"
  "v_exp_f32 v23, v23\n"
  "v_mfma_f32_16x16x32_bf16 a[244:247], a[160:163], a[120:123], a[244:247]\n"
  "v_cvt_pk_bf16_f32 v56, v48, v49\n"
  "v_cvt_pk_bf16_f32 v57, v50, v51\n"
  "v_mfma_f32_16x16x32_bf16 a[248:251], a[160:163], a[124:127], a[248:251]\n"
  "v_fma_f32 v52, v52, s73, v226\n"
  "v_fma_f32 v53, v53, s73, v226\n"
  "v_mfma_f32_16x16x32_bf16 v[200:203], v[60:63], a[120:123], v[200:203]\n"
  "v_exp_f32_e64 v52, v52\n"
  "v_mfma_f32_16x16x32_bf16 v[204:207], v[60:63], a[124:127], v[204:207]\n"
  "buffer_store_dwordx4 v[244:247], v228, s[52:55], s82 offen offset:128 nt\n"
  "v_exp_f32 v53, v53\n"
  "v_fma_f32 v54, v54, s73, v226\n"
  "v_mfma_f32_16x16x32_bf16 v[168:171], v[44:47], a[120:123], v[168:171]\n"
  "v_exp_f32 v54, v54\n"
  "v_mfma_f32_16x16x32_bf16 v[172:175], v[44:47], a[124:127], v[172:175]\n"
  "s_waitcnt vmcnt(12) lgkmcnt(0)\n"
  "s_barrier\n"
  "ds_read_b128 a[128:131], v216 offset:0\n"
  "v_mfma_f32_16x16x32_bf16 v[136:139], v[28:31], a[120:123], v[136:139]\n"
  "ds_read_b64 a[156:157], v231 offset:0\n"
  "v_fma_f32 v55, v55, s73, v226\n"
  "v_mfma_f32_16x16x32_bf16 v[140:143], v[28:31], a[124:127], v[140:143]\n"
  "ds_read_b64 a[158:159], v232 offset:0\n"
  "v_cvt_pk_bf16_f32 v58, v52, v53\n"
  "v_mfma_f32_16x16x32_bf16 v[104:107], v[12:15], a[120:123], v[104:107]\n"
  "ds_read_b128 a[132:135], v216 offset:64\n"
  "v_fma_f32 v38, v38, s73, v225\n"
  "v_mfma_f32_16x16x32_bf16 v[108:111], v[12:15], a[124:127], v[108:111]\n"
  "ds_read_b128 a[120:123], v216 offset:1024\n"
  "v_fma_f32 v39, v39, s73, v225\n"
  "v_mfma_f32_16x16x32_bf16 v[112:115], v[12:15], a[136:139], v[112:115]\n"
  "ds_read_b128 a[124:127], v216 offset:1088\n"
  "v_cvt_pk_bf16_f32 v42, v36, v37\n"
  "v_mfma_f32_16x16x32_bf16 v[116:119], v[12:15], a[140:143], v[116:119]\n"
  "s_mov_b32 m0, s68\n"
  "ds_read_b128 a[144:147], v216 offset:2048\n"
  "v_cvt_pk_bf16_f32 v26, v20, v21\n"
  "v_mfma_f32_16x16x32_bf16 v[144:147], v[28:31], a[136:139], v[144:147]\n"
  "ds_read_b128 a[148:151], v216 offset:2112\n"
  "v_cvt_pk_bf16_f32 v27, v22, v23\n"
  "v_mfma_f32_16x16x32_bf16 v[148:151], v[28:31], a[140:143], v[148:151]\n"
  "v_exp_f32 v55, v55\n"
  "v_mfma_f32_16x16x32_bf16 v[176:179], v[44:47], a[136:139], v[176:179]\n"
  "v_exp_f32 v38, v38\n"
  "v_mfma_f32_16x16x32_bf16 v[180:183], v[44:47], a[140:143], v[180:183]\n"
  "buffer_load_dwordx4 v221, s[40:43], 0 offen offset:0 lds\n"
  "v_mfma_f32_16x16x32_bf16 v[208:211], v[60:63], a[136:139], v[208:211]\n"
  "s_addk_i32 m0, 896\n"
  "v_exp_f32 v39, v39\n"
  "v_mfma_f32_16x16x32_bf16 v[212:215], v[60:63], a[140:143], v[212:215]\n"
  "s_waitcnt lgkmcnt(4)\n"
  "v_cvt_pk_bf16_f32 v59, v54, v55\n"
  "v_cvt_pk_bf16_f32 v43, v38, v39\n"
  "v_mfma_f32_16x16x32_bf16 v[64:67], a[128:131], a[96:99], v[248:251]\n"
  "v_cvt_pk_bf16_f32 v10, v4, v5\n"
  "v_cvt_pk_bf16_f32 v11, v6, v7\n"
  "v_mfma_f32_16x16x32_bf16 v[64:67], a[132:135], a[100:103], v[64:67]\n"
  "s_waitcnt lgkmcnt(2)\n"
  "buffer_load_dwordx4 v221, s[40:43], 0 offen offset:128 lds\n"
  "v_add_f32 v252, v252, v5\n"
  "v_mfma_f32_16x16x32_bf16 v[64:67], a[120:123], a[104:107], v[64:67]\n"
  "s_addk_i32 m0, 896\n"
  "v_add_f32 v252, v252, v6\n"
  "v_add_f32 v252, v252, v7\n"
  "v_mfma_f32_16x16x32_bf16 v[64:67], a[124:127], a[108:111], v[64:67]\n"
  "s_waitcnt lgkmcnt(0)\n"
  "v_add_f32 v253, v253, v20\n"
  "v_add_f32 v253, v253, v21\n"
  "v_mfma_f32_16x16x32_bf16 v[64:67], a[144:147], a[112:115], v[64:67]\n"
  "v_add_f32 v253, v253, v22\n"
  "v_add_f32 v253, v253, v23\n"
  "v_mfma_f32_16x16x32_bf16 v[64:67], a[148:151], a[116:119], v[64:67]\n"
  "buffer_load_dwordx4 v221, s[40:43], 0 offen offset:256 lds\n"
  "v_add_f32 v254, v254, v36\n"
  "v_mfma_f32_16x16x32_bf16 v[0:3], a[148:151], a[20:23], v[248:251]\n"
  "s_add_u32 s40, s40, s31\n"
  "v_add_f32 v254, v254, v37\n"
  "v_add_f32 v254, v254, v38\n"
  "v_mfma_f32_16x16x32_bf16 v[0:3], a[144:147], a[16:19], v[0:3]\n"
  "s_addc_u32 s41, s41, 0\n"
  "v_add_f32 v254, v254, v39\n"
  "v_add_f32 v255, v255, v52\n"
  "v_mfma_f32_16x16x32_bf16 v[0:3], a[124:127], a[12:15], v[0:3]\n"
  "v_add_f32 v255, v255, v53\n"
  "v_add_f32 v255, v255, v54\n"
  "v_mfma_f32_16x16x32_bf16 v[0:3], a[120:123], a[8:11], v[0:3]\n"
  "s_add_u32 m0, s60, -128\n"
  "ds_read_b64_tr_b16 a[252:253], v230 offset:14560\n"
  "v_add_f32 v255, v255, v55\n"
  "v_mfma_f32_16x16x32_bf16 v[0:3], a[132:135], a[4:7], v[0:3]\n"
  "ds_read_b64_tr_b16 a[254:255], v230 offset:14592\n"
  "ds_read_b64_tr_b16 v[244:245], v230 offset:15680\n"
  "v_mfma_f32_16x16x32_bf16 v[0:3], a[128:131], a[0:3], v[0:3]\n"
  "ds_read_b64_tr_b16 v[246:247], v230 offset:15712\n"
  "v_fma_f32 v64, v64, s73, v227\n"
  "v_mfma_f32_16x16x32_bf16 v[16:19], a[128:131], a[24:27], v[248:251]\n"
  "v_exp_f32 v64, v64\n"
  "v_mfma_f32_16x16x32_bf16 v[16:19], a[132:135], a[28:31], v[16:19]\n"
  "buffer_load_dwordx4 v221, s[32:35], 0 offen offset:128 lds\n"
  "v_fma_f32 v65, v65, s73, v227\n"
  "v_mfma_f32_16x16x32_bf16 v[16:19], a[120:123], a[32:35], v[16:19]\n"
  "s_addk_i32 m0, 1024\n"
  "ds_read_b64_tr_b16 a[136:137], v217 offset:0\n"
  "v_fma_f32 v66, v66, s73, v227\n"
  "v_mfma_f32_16x16x32_bf16 v[16:19], a[124:127], a[36:39], v[16:19]\n"
  "s_mov_b64 s[32:33], s[44:45]\n"
  "ds_read_b64_tr_b16 a[138:139], v218 offset:0\n"
  "v_fma_f32 v67, v67, s73, v227\n"
  "v_mfma_f32_16x16x32_bf16 v[16:19], a[144:147], a[40:43], v[16:19]\n"
  "ds_read_b64_tr_b16 a[140:141], v217 offset:1024\n"
  "v_fma_f32 v0, v0, s73, v223\n"
  "v_mfma_f32_16x16x32_bf16 v[16:19], a[148:151], a[44:47], v[16:19]\n"
  "buffer_load_dwordx4 v221, s[44:47], 0 offen offset:128 lds\n"
  "v_fma_f32 v1, v1, s73, v223\n"
  "v_mfma_f32_16x16x32_bf16 v[32:35], a[148:151], a[68:71], v[248:251]\n"
  "s_addk_i32 m0, 896\n"
  "ds_read_b64_tr_b16 a[142:143], v218 offset:1024\n"
  "v_fma_f32 v2, v2, s73, v223\n"
  "v_mfma_f32_16x16x32_bf16 v[32:35], a[144:147], a[64:67], v[32:35]\n"
  "v_exp_f32 v65, v65\n"
  "v_mfma_f32_16x16x32_bf16 v[32:35], a[124:127], a[60:63], v[32:35]\n"
  "v_exp_f32 v66, v66\n"
  "v_mfma_f32_16x16x32_bf16 v[32:35], a[120:123], a[56:59], v[32:35]\n"
  "buffer_load_dwordx4 v221, s[44:47], 0 offen offset:256 lds\n"
  "v_cvt_pk_bf16_f32 v64, v64, v65\n"
  "v_mfma_f32_16x16x32_bf16 v[32:35], a[132:135], a[52:55], v[32:35]\n"
  "s_add_u32 s44, s44, s31\n"
  "v_exp_f32 v67, v67\n"
  "v_mfma_f32_16x16x32_bf16 v[32:35], a[128:131], a[48:51], v[32:35]\n"
  "s_addc_u32 s45, s45, 0\n"
  "v_exp_f32 v0, v0\n"
  "v_mfma_f32_16x16x32_bf16 v[48:51], a[128:131], a[72:75], v[248:251]\n"
  "ds_read_b64_tr_b16 a[128:129], v217 offset:16\n"
  "v_cvt_pk_bf16_f32 v65, v66, v67\n"
  "v_mfma_f32_16x16x32_bf16 v[48:51], a[132:135], a[76:79], v[48:51]\n"
  "ds_read_b64_tr_b16 a[130:131], v218 offset:16\n"
  "v_fma_f32 v3, v3, s73, v223\n"
  "v_mfma_f32_16x16x32_bf16 v[48:51], a[120:123], a[80:83], v[48:51]\n"
  "ds_read_b64_tr_b16 a[132:133], v217 offset:1040\n"
  "v_add_f32 v252, v252, v0\n"
  "v_mfma_f32_16x16x32_bf16 v[48:51], a[124:127], a[84:87], v[48:51]\n"
  "s_add_u32 m0, s72, 11200\n"
  "ds_read_b64_tr_b16 a[134:135], v218 offset:1040\n"
  "v_mfma_f32_16x16x32_bf16 v[48:51], a[144:147], a[88:91], v[48:51]\n"
  "v_exp_f32 v1, v1\n"
  "v_mfma_f32_16x16x32_bf16 v[48:51], a[148:151], a[92:95], v[48:51]\n"
  "s_waitcnt lgkmcnt(4)\n"
  "v_exp_f32 v2, v2\n"
  "v_mfma_f32_16x16x32_bf16 a[236:239], a[160:163], a[136:139], a[236:239]\n"
  "v_exp_f32 v3, v3\n"
  "v_mfma_f32_16x16x32_bf16 a[236:239], a[156:159], a[140:143], a[236:239]\n"
  "buffer_load_dwordx4 v221, s[48:51], 0 offen offset:0 nt lds\n"
  "v_add_f32 v252, v252, v1\n"
  "v_mfma_f32_16x16x32_bf16 v[184:187], v[60:63], a[136:139], v[184:187]\n"
  "v_add_f32 v252, v252, v2\n"
  "v_add_f32 v252, v252, v3\n"
  "v_mfma_f32_16x16x32_bf16 v[184:187], v[56:59], a[140:143], v[184:187]\n"
  "v_fma_f32 v16, v16, s73, v224\n"
  "v_fma_f32 v17, v17, s73, v224\n"
  "v_mfma_f32_16x16x32_bf16 v[152:155], v[44:47], a[136:139], v[152:155]\n"
  "ds_read_b64_tr_b16 a[120:121], v219 offset:0\n"
  "v_fma_f32 v18, v18, s73, v224\n"
  "v_mfma_f32_16x16x32_bf16 v[152:155], v[40:43], a[140:143], v[152:155]\n"
  "s_add_u32 m0, s72, 9216\n"
  "ds_read_b64_tr_b16 a[122:123], v220 offset:0\n"
  "v_mfma_f32_16x16x32_bf16 v[120:123], v[28:31], a[136:139], v[120:123]\n"
  "ds_read_b64_tr_b16 a[124:125], v219 offset:1024\n"
  "v_fma_f32 v19, v19, s73, v224\n"
  "v_mfma_f32_16x16x32_bf16 v[120:123], v[24:27], a[140:143], v[120:123]\n"
  "ds_read_b64_tr_b16 a[126:127], v220 offset:1024\n"
  "v_fma_f32 v32, v32, s73, v225\n"
  "v_mfma_f32_16x16x32_bf16 v[88:91], v[12:15], a[136:139], v[88:91]\n"
  "v_exp_f32 v16, v16\n"
  "v_mfma_f32_16x16x32_bf16 v[88:91], v[8:11], a[140:143], v[88:91]\n"
  "s_waitcnt lgkmcnt(4)\n"
  "buffer_load_dwordx4 v221, s[48:51], 0 offen offset:3072 nt lds\n"
  "v_add_f32 v253, v253, v16\n"
  "v_mfma_f32_16x16x32_bf16 v[92:95], v[12:15], a[128:131], v[92:95]\n"
  "v_exp_f32 v17, v17\n"
  "v_mfma_f32_16x16x32_bf16 v[92:95], v[8:11], a[132:135], v[92:95]\n"
  "v_exp_f32 v18, v18\n"
  "v_mfma_f32_16x16x32_bf16 v[124:127], v[28:31], a[128:131], v[124:127]\n"
  "ds_read_b128 a[136:139], v235 offset:0\n"
  "v_add_f32 v253, v253, v17\n"
  "v_mfma_f32_16x16x32_bf16 v[124:127], v[24:27], a[132:135], v[124:127]\n"
  "ds_read_b128 a[140:143], v235 offset:64\n"
  "v_add_f32 v253, v253, v18\n"
  "v_mfma_f32_16x16x32_bf16 v[156:159], v[44:47], a[128:131], v[156:159]\n"
  "ds_read_b128 a[144:147], v235 offset:1024\n"
  "v_fma_f32 v33, v33, s73, v225\n"
  "v_mfma_f32_16x16x32_bf16 v[156:159], v[40:43], a[132:135], v[156:159]\n"
  "ds_read_b128 a[148:151], v235 offset:1088\n"
  "v_fma_f32 v34, v34, s73, v225\n"
  "v_mfma_f32_16x16x32_bf16 v[188:191], v[60:63], a[128:131], v[188:191]\n"
  "v_exp_f32 v19, v19\n"
  "v_mfma_f32_16x16x32_bf16 v[188:191], v[56:59], a[132:135], v[188:191]\n"
  "v_exp_f32 v32, v32\n"
  "v_mfma_f32_16x16x32_bf16 a[240:243], a[160:163], a[128:131], a[240:243]\n"
  "ds_read_b128 a[128:131], v235 offset:2048\n"
  "v_add_f32 v253, v253, v19\n"
  "v_mfma_f32_16x16x32_bf16 a[240:243], a[156:159], a[132:135], a[240:243]\n"
  "s_waitcnt lgkmcnt(5)\n"
  "ds_read_b128 a[132:135], v235 offset:2112\n"
  "v_fma_f32 v35, v35, s73, v225\n"
  "v_mfma_f32_16x16x32_bf16 v[192:195], v[60:63], a[120:123], v[192:195]\n"
  "v_exp_f32 v33, v33\n"
  "v_mfma_f32_16x16x32_bf16 v[192:195], v[56:59], a[124:127], v[192:195]\n"
  "ds_write_b64 v231, v[64:65] offset:2048\n"
  "v_mfma_f32_16x16x32_bf16 v[160:163], v[44:47], a[120:123], v[160:163]\n"
  "v_exp_f32 v34, v34\n"
  "v_mfma_f32_16x16x32_bf16 v[160:163], v[40:43], a[124:127], v[160:163]\n"
  "v_exp_f32 v35, v35\n"
  "v_mfma_f32_16x16x32_bf16 v[128:131], v[28:31], a[120:123], v[128:131]\n"
  "v_add_f32 v254, v254, v32\n"
  "v_add_f32 v254, v254, v33\n"
  "v_mfma_f32_16x16x32_bf16 v[128:131], v[24:27], a[124:127], v[128:131]\n"
  "buffer_store_dwordx4 a[252:255], v228, s[52:55], s82 offen offset:3200 nt\n"
  "v_add_f32 v254, v254, v34\n"
  "v_add_f32 v254, v254, v35\n"
  "v_max_u32_dpp v236, v236, v236 quad_perm:[2,3,0,1] row_mask:0xf bank_mask:0xf\n"
  "v_mfma_f32_16x16x32_bf16 v[96:99], v[12:15], a[120:123], v[96:99]\n"
  "ds_read_b64_tr_b16 a[252:253], v230 offset:16800\n"
  "ds_read_b64_tr_b16 a[254:255], v230 offset:16832\n"
  "v_mfma_f32_16x16x32_bf16 v[96:99], v[8:11], a[124:127], v[96:99]\n"
  "s_waitcnt lgkmcnt(7)\n"
  "v_add_u32 v236, -1, v236\n"
  "v_fma_f32 v48, v48, s73, v226\n"
  "v_mfma_f32_16x16x32_bf16 v[4:7], a[136:139], a[0:3], v[248:251]\n"
  "v_exp_f32 v48, v48\n"
  "v_mfma_f32_16x16x32_bf16 v[4:7], a[140:143], a[4:7], v[4:7]\n"
  "s_waitcnt lgkmcnt(5)\n"
  "v_min_u32_dpp v236, v236, v236 row_shr:4 row_mask:0xf bank_mask:0xf\n"
  "v_fma_f32 v49, v49, s73, v226\n"
  "v_mfma_f32_16x16x32_bf16 v[4:7], a[144:147], a[8:11], v[4:7]\n"
  "v_exp_f32 v49, v49\n"
  "v_mfma_f32_16x16x32_bf16 v[4:7], a[148:151], a[12:15], v[4:7]\n"
  "s_waitcnt lgkmcnt(3)\n"
  "v_min_u32_dpp v236, v236, v236 row_shr:8 row_mask:0xf bank_mask:0xf\n"
  "v_fma_f32 v50, v50, s73, v226\n"
  "v_mfma_f32_16x16x32_bf16 v[4:7], a[128:131], a[16:19], v[4:7]\n"
  "v_exp_f32 v50, v50\n"
  "v_mfma_f32_16x16x32_bf16 v[4:7], a[132:135], a[20:23], v[4:7]\n"
  "v_min_u32_dpp v236, v236, v236 row_bcast:15 row_mask:0xa bank_mask:0xf\n"
  "v_fma_f32 v51, v51, s73, v226\n"
  "v_mfma_f32_16x16x32_bf16 v[20:23], a[132:135], a[44:47], v[248:251]\n"
  "v_exp_f32 v51, v51\n"
  "v_mfma_f32_16x16x32_bf16 v[20:23], a[128:131], a[40:43], v[20:23]\n"
  "v_min_u32_dpp v236, v236, v236 row_bcast:31 row_mask:0xc bank_mask:0xf\n"
  "v_add_f32 v255, v255, v48\n"
  "v_mfma_f32_16x16x32_bf16 v[20:23], a[148:151], a[36:39], v[20:23]\n"
  "ds_read_b64_tr_b16 a[120:121], v219 offset:16\n"
  "v_add_f32 v255, v255, v49\n"
  "v_mfma_f32_16x16x32_bf16 v[20:23], a[144:147], a[32:35], v[20:23]\n"
  "ds_read_b64_tr_b16 a[122:123], v220 offset:16\n"
  "v_add_f32 v255, v255, v50\n"
  "v_mfma_f32_16x16x32_bf16 v[20:23], a[140:143], a[28:31], v[20:23]\n"
  "ds_read_b64_tr_b16 a[124:125], v219 offset:1040\n"
  "v_add_f32 v255, v255, v51\n"
  "v_mfma_f32_16x16x32_bf16 v[20:23], a[136:139], a[24:27], v[20:23]\n"
  "ds_read_b64_tr_b16 a[126:127], v220 offset:1040\n"
  "v_fma_f32 v4, v4, s73, v223\n"
  "v_mfma_f32_16x16x32_bf16 v[36:39], a[136:139], a[48:51], v[248:251]\n"
  "v_readlane_b32 s65, v236, 63\n"
  "v_mfma_f32_16x16x32_bf16 v[36:39], a[140:143], a[52:55], v[36:39]\n"
  "v_exp_f32 v4, v4\n"
  "v_mfma_f32_16x16x32_bf16 v[36:39], a[144:147], a[56:59], v[36:39]\n"
  "s_add_u32 s65, s65, 1\n"
  "v_fma_f32 v20, v20, s73, v224\n"
  "v_fma_f32 v21, v21, s73, v224\n"
  "v_mfma_f32_16x16x32_bf16 v[36:39], a[148:151], a[60:63], v[36:39]\n"
  "s_lshr_b32 s65, s65, 23\n"
  "v_exp_f32 v20, v20\n"
  "v_mfma_f32_16x16x32_bf16 v[36:39], a[128:131], a[64:67], v[36:39]\n"
  "s_sub_i32 s65, 0x77, s65\n"
  "v_exp_f32 v21, v21\n"
  "v_mfma_f32_16x16x32_bf16 v[36:39], a[132:135], a[68:71], v[36:39]\n"
  "s_and_b32 s65, s65, -4\n"
  "buffer_store_dwordx4 v[244:247], v228, s[52:55], s83 offen offset:128 nt\n"
  "v_fma_f32 v22, v22, s73, v224\n"
  "v_exp_f32 v22, v22\n"
  "v_mfma_f32_16x16x32_bf16 v[52:55], a[132:135], a[92:95], v[248:251]\n"
  "s_max_i32 s65, s65, 0xffffffc4\n"
  "ds_read_b64_tr_b16 a[132:133], v217 offset:2048\n"
  "v_add_f32 v252, v252, v4\n"
  "v_mfma_f32_16x16x32_bf16 v[52:55], a[128:131], a[88:91], v[52:55]\n"
  "ds_read_b64_tr_b16 a[134:135], v218 offset:2048\n"
  "v_cvt_f32_i32 v236, s65\n"
  "v_mfma_f32_16x16x32_bf16 v[52:55], a[148:151], a[84:87], v[52:55]\n"
  "ds_read_b64_tr_b16 a[128:129], v217 offset:2064\n"
  "v_fma_f32 v36, v36, s73, v225\n"
  "v_mfma_f32_16x16x32_bf16 v[52:55], a[144:147], a[80:83], v[52:55]\n"
  "ds_read_b64_tr_b16 a[130:131], v218 offset:2064\n"
  "v_fma_f32 v37, v37, s73, v225\n"
  "v_mfma_f32_16x16x32_bf16 v[52:55], a[140:143], a[76:79], v[52:55]\n"
  "s_add_u32 s65, s65, 0xab\n"
  "v_readfirstlane_b32 s64, v236\n"
  "v_mfma_f32_16x16x32_bf16 v[52:55], a[136:139], a[72:75], v[52:55]\n"
  "s_waitcnt lgkmcnt(4)\n"
  "v_exp_f32 v36, v36\n"
  "v_mfma_f32_16x16x32_bf16 v[100:103], v[12:15], a[120:123], v[100:103]\n"
  "v_cvt_pk_bf16_f32 v12, v0, v1\n"
  "v_cvt_pk_bf16_f32 v13, v2, v3\n"
  "v_mfma_f32_16x16x32_bf16 v[100:103], v[8:11], a[124:127], v[100:103]\n"
  "s_cmpk_gt_u32 s65, 0xd3\n"
  "s_cselect_b32 s65, 0, s65\n"
  "v_exp_f32 v37, v37\n"
  "v_mfma_f32_16x16x32_bf16 v[132:135], v[28:31], a[120:123], v[132:135]\n"
  "s_lshl_b32 s65, s65, 23\n"
  "ds_read_b64_tr_b16 a[140:141], v219 offset:2048\n"
  "v_cvt_pk_bf16_f32 v28, v16, v17\n"
  "v_mfma_f32_16x16x32_bf16 v[132:135], v[24:27], a[124:127], v[132:135]\n"
  "ds_read_b64_tr_b16 a[142:143], v220 offset:2048\n"
  "v_cvt_pk_bf16_f32 v29, v18, v19\n"
  "v_mfma_f32_16x16x32_bf16 v[164:167], v[44:47], a[120:123], v[164:167]\n"
  "ds_read_b64_tr_b16 a[136:137], v219 offset:2064\n"
  "v_cvt_pk_bf16_f32 v44, v32, v33\n"
  "v_mfma_f32_16x16x32_bf16 v[164:167], v[40:43], a[124:127], v[164:167]\n"
  "ds_read_b64_tr_b16 a[138:139], v220 offset:2064\n"
  "v_cvt_pk_bf16_f32 v45, v34, v35\n"
  "v_mfma_f32_16x16x32_bf16 v[196:199], v[60:63], a[120:123], v[196:199]\n"
  "v_cvt_pk_bf16_f32 v60, v48, v49\n"
  "v_cvt_pk_bf16_f32 v61, v50, v51\n"
  "v_mfma_f32_16x16x32_bf16 v[196:199], v[56:59], a[124:127], v[196:199]\n"
  "s_waitcnt lgkmcnt(4)\n"
  "v_fma_f32 v52, v52, s73, v226\n"
  "v_fma_f32 v53, v53, s73, v226\n"
  "v_mfma_f32_16x16x32_bf16 a[244:247], a[156:159], a[132:135], a[244:247]\n"
  "v_exp_f32 v52, v52\n"
  "v_mfma_f32_16x16x32_bf16 a[248:251], a[156:159], a[128:131], a[248:251]\n"
  "v_exp_f32_e64 v53, v53\n"
  "v_mfma_f32_16x16x32_bf16 v[200:203], v[56:59], a[132:135], v[200:203]\n"
  "v_fma_f32 v54, v54, s73, v226\n"
  "v_fma_f32 v55, v55, s73, v226\n"
  "v_mfma_f32_16x16x32_bf16 v[204:207], v[56:59], a[128:131], v[204:207]\n"
  "buffer_store_dwordx4 a[252:255], v228, s[52:55], s83 offen offset:3200 nt\n"
  "v_exp_f32_e64 v54, v54\n"
  "v_cvt_pk_bf16_f32 v62, v52, v53\n"
  "v_mfma_f32_16x16x32_bf16 v[168:171], v[40:43], a[132:135], v[168:171]\n"
  "v_exp_f32_e64 v55, v55\n"
  "v_mfma_f32_16x16x32_bf16 v[172:175], v[40:43], a[128:131], v[172:175]\n"
  "s_waitcnt vmcnt(14) lgkmcnt(0)\n"
  "s_barrier\n"
  "ds_read_b128 a[120:123], v216 offset:12352\n"
  "v_mfma_f32_16x16x32_bf16 v[136:139], v[24:27], a[132:135], v[136:139]\n"
  "ds_read_b64 a[160:161], v231 offset:2048\n"
  "v_cvt_pk_bf16_f32 v63, v54, v55\n"
  "v_mfma_f32_16x16x32_bf16 v[140:143], v[24:27], a[128:131], v[140:143]\n"
  "ds_read_b64 a[162:163], v232 offset:2048\n"
  "v_fma_f32 v38, v38, s73, v225\n"
  "v_mfma_f32_16x16x32_bf16 v[104:107], v[8:11], a[132:135], v[104:107]\n"
  "ds_read_b128 a[124:127], v216 offset:12416\n"
  "v_fma_f32 v39, v39, s73, v225\n"
  "v_mfma_f32_16x16x32_bf16 v[108:111], v[8:11], a[128:131], v[108:111]\n"
  "ds_read_b128 a[132:135], v216 offset:13376\n"
  "v_cvt_pk_bf16_f32 v46, v36, v37\n"
  "v_mfma_f32_16x16x32_bf16 v[112:115], v[8:11], a[140:143], v[112:115]\n"
  "s_add_u32 s71, s92, s82\n"
  "ds_read_b128 a[128:131], v216 offset:13440\n"
  "v_fma_f32 v23, v23, s73, v224\n"
  "v_mfma_f32_16x16x32_bf16 v[116:119], v[8:11], a[136:139], v[116:119]\n"
  "s_mov_b32 m0, s66\n"
  "ds_read_b128 a[148:151], v216 offset:14400\n"
  "v_cvt_pk_bf16_f32 v30, v20, v21\n"
  "v_mfma_f32_16x16x32_bf16 v[144:147], v[24:27], a[140:143], v[144:147]\n"
  "s_add_u32 s48, s84, s71\n"
  "ds_read_b128 a[144:147], v216 offset:14464\n"
  "v_fma_f32 v5, v5, s73, v223\n"
  "v_mfma_f32_16x16x32_bf16 v[148:151], v[24:27], a[136:139], v[148:151]\n"
  "s_addc_u32 s49, s85, 0\n"
  "v_exp_f32 v38, v38\n"
  "v_mfma_f32_16x16x32_bf16 v[176:179], v[40:43], a[140:143], v[176:179]\n"
  "s_sub_i32 s71, s38, s82\n"
  "v_exp_f32 v39, v39\n"
  "v_mfma_f32_16x16x32_bf16 v[180:183], v[40:43], a[136:139], v[180:183]\n"
  "buffer_load_dwordx4 v221, s[40:43], 0 offen offset:0 lds\n"
  "v_mfma_f32_16x16x32_bf16 v[208:211], v[56:59], a[140:143], v[208:211]\n"
  "s_addk_i32 m0, 896\n"
  "v_exp_f32 v23, v23\n"
  "v_mfma_f32_16x16x32_bf16 v[212:215], v[56:59], a[136:139], v[212:215]\n"
  "s_waitcnt lgkmcnt(4)\n"
  "v_exp_f32 v5, v5\n"
  "v_mfma_f32_16x16x32_bf16 v[64:67], a[120:123], a[96:99], v[248:251]\n"
  "s_max_i32 s50, s71, 0\n"
  "v_cvt_pk_bf16_f32 v47, v38, v39\n"
  "v_cvt_pk_bf16_f32 v31, v22, v23\n"
  "v_mfma_f32_16x16x32_bf16 v[64:67], a[124:127], a[100:103], v[64:67]\n"
  "s_waitcnt lgkmcnt(2)\n"
  "buffer_load_dwordx4 v221, s[40:43], 0 offen offset:128 lds\n"
  "v_fma_f32 v6, v6, s73, v223\n"
  "v_mfma_f32_16x16x32_bf16 v[64:67], a[132:135], a[104:107], v[64:67]\n"
  "s_addk_i32 m0, 896\n"
  "v_exp_f32 v6, v6\n"
  "v_mfma_f32_16x16x32_bf16 v[64:67], a[128:131], a[108:111], v[64:67]\n"
  "s_waitcnt lgkmcnt(0)\n"
  "v_fma_f32 v7, v7, s73, v223\n"
  "v_cvt_pk_bf16_f32 v14, v4, v5\n"
  "v_mfma_f32_16x16x32_bf16 v[64:67], a[148:151], a[112:115], v[64:67]\n"
  "v_exp_f32 v7, v7\n"
  "v_mfma_f32_16x16x32_bf16 v[64:67], a[144:147], a[116:119], v[64:67]\n"
  "buffer_load_dwordx4 v221, s[40:43], 0 offen offset:256 lds\n"
  "v_cvt_pk_bf16_f32 v15, v6, v7\n"
  "v_mfma_f32_16x16x32_bf16 v[0:3], a[144:147], a[20:23], v[248:251]\n"
  "s_add_u32 s40, s40, s31\n"
  "v_add_f32 v252, v252, v5\n"
  "v_add_f32 v252, v252, v6\n"
  "v_mfma_f32_16x16x32_bf16 v[0:3], a[148:151], a[16:19], v[0:3]\n"
  "s_addc_u32 s41, s41, 0\n"
  "v_bfe_u32 v236, v231, 3, 6\n"
  "v_lshlrev_b32 v236, 7, v236\n"
  "v_mfma_f32_16x16x32_bf16 v[0:3], a[128:131], a[12:15], v[0:3]\n"
  "s_cmp_eq_u32 s76, s24\n"
  "s_cselect_b32 s22, s25, 0\n"
  "v_add_f32 v252, v252, v7\n"
  "v_add_f32 v253, v253, v20\n"
  "v_mfma_f32_16x16x32_bf16 v[0:3], a[132:135], a[8:11], v[0:3]\n"
  "s_mov_b32 m0, s58\n"
  "v_add_f32 v253, v253, v21\n"
  "v_add_f32 v253, v253, v22\n"
  "v_mfma_f32_16x16x32_bf16 v[0:3], a[124:127], a[4:7], v[0:3]\n"
  "v_add_f32 v253, v253, v23\n"
  "v_add_f32 v254, v254, v36\n"
  "v_mfma_f32_16x16x32_bf16 v[0:3], a[120:123], a[0:3], v[0:3]\n"
  "v_add_f32 v254, v254, v37\n"
  "v_add_f32 v254, v254, v38\n"
  "v_mfma_f32_16x16x32_bf16 v[16:19], a[120:123], a[24:27], v[248:251]\n"
  "v_add_f32 v254, v254, v39\n"
  "v_add_f32 v255, v255, v52\n"
  "v_mfma_f32_16x16x32_bf16 v[16:19], a[124:127], a[28:31], v[16:19]\n"
  "buffer_load_dwordx4 v221, s[32:35], 0 offen offset:0 lds\n"
  "v_add_f32 v255, v255, v53\n"
  "v_mfma_f32_16x16x32_bf16 v[16:19], a[132:135], a[32:35], v[16:19]\n"
  "s_addk_i32 m0, 1024\n"
  "ds_read_b64_tr_b16 a[140:141], v217 offset:12352\n"
  "v_add_f32 v255, v255, v54\n"
  "v_mfma_f32_16x16x32_bf16 v[16:19], a[128:131], a[36:39], v[16:19]\n"
  "s_mov_b64 s[32:33], s[44:45]\n"
  "ds_read_b64_tr_b16 a[142:143], v218 offset:12352\n"
  "v_add_f32 v255, v255, v55\n"
  "v_mfma_f32_16x16x32_bf16 v[16:19], a[148:151], a[40:43], v[16:19]\n"
  "ds_read_b64_tr_b16 a[136:137], v217 offset:13376\n"
  "v_fma_f32 v64, v64, s73, v227\n"
  "v_mfma_f32_16x16x32_bf16 v[16:19], a[144:147], a[44:47], v[16:19]\n"
  "buffer_load_dwordx4 v221, s[44:47], 0 offen offset:0 lds\n"
  "v_fma_f32 v65, v65, s73, v227\n"
  "v_mfma_f32_16x16x32_bf16 v[32:35], a[144:147], a[68:71], v[248:251]\n"
  "s_addk_i32 m0, 768\n"
  "ds_read_b64_tr_b16 a[138:139], v218 offset:13376\n"
  "v_fma_f32 v66, v66, s73, v227\n"
  "v_mfma_f32_16x16x32_bf16 v[32:35], a[148:151], a[64:67], v[32:35]\n"
  "v_exp_f32 v64, v64\n"
  "v_mfma_f32_16x16x32_bf16 v[32:35], a[128:131], a[60:63], v[32:35]\n"
  "v_exp_f32 v65, v65\n"
  "v_mfma_f32_16x16x32_bf16 v[32:35], a[132:135], a[56:59], v[32:35]\n"
  "buffer_load_dwordx4 v221, s[44:47], 0 offen offset:256 lds\n"
  "v_fma_f32 v67, v67, s73, v227\n"
  "v_mfma_f32_16x16x32_bf16 v[32:35], a[124:127], a[52:55], v[32:35]\n"
  "s_add_u32 s44, s44, s31\n"
  "v_exp_f32 v66, v66\n"
  "v_mfma_f32_16x16x32_bf16 v[32:35], a[120:123], a[48:51], v[32:35]\n"
  "s_addc_u32 s45, s45, 0\n"
  "v_exp_f32 v67, v67\n"
  "v_mfma_f32_16x16x32_bf16 v[48:51], a[120:123], a[72:75], v[248:251]\n"
  "s_mov_b32 m0, s29\n"
  "v_cvt_pk_bf16_f32 v64, v64, v65\n"
  "v_cvt_pk_bf16_f32 v65, v66, v67\n"
  "v_mfma_f32_16x16x32_bf16 v[48:51], a[124:127], a[76:79], v[48:51]\n"
  "v_fma_f32 v0, v0, s73, v223\n"
  "v_fma_f32 v1, v1, s73, v223\n"
  "v_mfma_f32_16x16x32_bf16 v[48:51], a[132:135], a[80:83], v[48:51]\n"
  "ds_read_b64_tr_b16 a[120:121], v217 offset:12368\n"
  "v_fma_f32 v2, v2, s73, v223\n"
  "v_mfma_f32_16x16x32_bf16 v[48:51], a[128:131], a[84:87], v[48:51]\n"
  "buffer_load_dword v236, s[20:23], 0 offen lds\n"
  "v_fma_f32 v3, v3, s73, v223\n"
  "v_mfma_f32_16x16x32_bf16 v[48:51], a[148:151], a[88:91], v[48:51]\n"
  "s_add_u32 m0, s72, 13440\n"
  "ds_read_b64_tr_b16 a[122:123], v218 offset:12368\n"
  "v_mfma_f32_16x16x32_bf16 v[48:51], a[144:147], a[92:95], v[48:51]\n"
  "s_waitcnt lgkmcnt(2)\n"
  "ds_read_b64_tr_b16 a[124:125], v217 offset:13392\n"
  "v_fma_f32 v16, v16, s73, v224\n"
  "v_mfma_f32_16x16x32_bf16 a[228:231], a[156:159], a[140:143], a[228:231]\n"
  "ds_read_b64_tr_b16 a[126:127], v218 offset:13392\n"
  "v_fma_f32 v17, v17, s73, v224\n"
  "v_mfma_f32_16x16x32_bf16 a[228:231], a[160:163], a[136:139], a[228:231]\n"
  "buffer_load_dwordx4 v221, s[48:51], 0 offen offset:0 nt lds\n"
  "v_fma_f32 v18, v18, s73, v224\n"
  "v_mfma_f32_16x16x32_bf16 a[212:215], v[56:59], a[140:143], a[212:215]\n"
  "v_exp_f32 v0, v0\n"
  "v_mfma_f32_16x16x32_bf16 a[212:215], v[60:63], a[136:139], a[212:215]\n"
  "v_exp_f32 v1, v1\n"
  "v_mfma_f32_16x16x32_bf16 a[196:199], v[40:43], a[140:143], a[196:199]\n"
  "v_exp_f32 v2, v2\n"
  "v_mfma_f32_16x16x32_bf16 a[196:199], v[44:47], a[136:139], a[196:199]\n"
  "s_add_u32 m0, s72, 11456\n"
  "v_exp_f32 v3, v3\n"
  "v_mfma_f32_16x16x32_bf16 a[180:183], v[24:27], a[140:143], a[180:183]\n"
  "ds_read_b64_tr_b16 a[132:133], v219 offset:12352\n"
  "v_add_f32 v252, v252, v0\n"
  "v_mfma_f32_16x16x32_bf16 a[180:183], v[28:31], a[136:139], a[180:183]\n"
  "ds_read_b64_tr_b16 a[134:135], v220 offset:12352\n"
  "v_add_f32 v252, v252, v1\n"
  "v_mfma_f32_16x16x32_bf16 a[164:167], v[8:11], a[140:143], a[164:167]\n"
  "ds_read_b64_tr_b16 a[128:129], v219 offset:13376\n"
  "v_add_f32 v252, v252, v2\n"
  "v_mfma_f32_16x16x32_bf16 a[164:167], v[12:15], a[136:139], a[164:167]\n"
  "buffer_load_dwordx4 v221, s[48:51], 0 offen offset:3072 nt lds\n"
  "v_add_f32 v252, v252, v3\n"
  "v_mfma_f32_16x16x32_bf16 v[84:87], a[156:159], a[152:155], v[84:87]\n"
  "ds_read_b64_tr_b16 a[130:131], v220 offset:13376\n"
  "v_fma_f32 v19, v19, s73, v224\n"
  "v_mfma_f32_16x16x32_bf16 v[84:87], a[160:163], a[152:155], v[84:87]\n"
  "s_waitcnt lgkmcnt(4)\n"
  "v_exp_f32 v16, v16\n"
  "v_mfma_f32_16x16x32_bf16 a[168:171], v[8:11], a[120:123], a[168:171]\n"
  "v_exp_f32 v17, v17\n"
  "v_mfma_f32_16x16x32_bf16 a[168:171], v[12:15], a[124:127], a[168:171]\n"
  "s_add_u32 m0, s72, 15552\n"
  "v_exp_f32 v18, v18\n"
  "v_mfma_f32_16x16x32_bf16 a[184:187], v[24:27], a[120:123], a[184:187]\n"
  "ds_read_b128 a[140:143], v235 offset:12352\n"
  "v_add_f32 v253, v253, v16\n"
  "v_mfma_f32_16x16x32_bf16 a[184:187], v[28:31], a[124:127], a[184:187]\n"
  "ds_read_b128 a[136:139], v235 offset:12416\n"
  "v_add_f32 v253, v253, v17\n"
  "v_mfma_f32_16x16x32_bf16 a[200:203], v[40:43], a[120:123], a[200:203]\n"
  "ds_read_b128 a[148:151], v235 offset:13376\n"
  "v_add_f32 v253, v253, v18\n"
  "v_mfma_f32_16x16x32_bf16 a[200:203], v[44:47], a[124:127], a[200:203]\n"
  "buffer_load_dwordx4 v221, s[48:51], 0 offen offset:128 nt lds\n"
  "v_fma_f32 v32, v32, s73, v225\n"
  "v_mfma_f32_16x16x32_bf16 a[216:219], v[56:59], a[120:123], a[216:219]\n"
  "ds_read_b128 a[144:147], v235 offset:13440\n"
  "v_fma_f32 v33, v33, s73, v225\n"
  "v_mfma_f32_16x16x32_bf16 a[216:219], v[60:63], a[124:127], a[216:219]\n"
  "v_exp_f32 v19, v19\n"
  "v_mfma_f32_16x16x32_bf16 a[232:235], a[156:159], a[120:123], a[232:235]\n"
  "ds_read_b128 a[120:123], v235 offset:14400\n"
  "v_add_f32 v253, v253, v19\n"
  "v_mfma_f32_16x16x32_bf16 a[232:235], a[160:163], a[124:127], a[232:235]\n"
  "s_waitcnt lgkmcnt(5)\n"
  "ds_read_b128 a[124:127], v235 offset:14464\n"
  "v_fma_f32 v34, v34, s73, v225\n"
  "v_mfma_f32_16x16x32_bf16 a[220:223], v[56:59], a[132:135], a[220:223]\n"
  "s_add_u32 m0, s72, 13568\n"
  "v_exp_f32 v32, v32\n"
  "v_mfma_f32_16x16x32_bf16 a[220:223], v[60:63], a[128:131], a[220:223]\n"
  "ds_write_b64 v231, v[64:65] offset:0\n"
  "v_mfma_f32_16x16x32_bf16 a[204:207], v[40:43], a[132:135], a[204:207]\n"
  "v_exp_f32 v33, v33\n"
  "v_mfma_f32_16x16x32_bf16 a[204:207], v[44:47], a[128:131], a[204:207]\n"
  "buffer_load_dwordx4 v221, s[48:51], 0 offen offset:3200 nt lds\n"
  "v_fma_f32 v35, v35, s73, v225\n"
  "v_mfma_f32_16x16x32_bf16 a[188:191], v[24:27], a[132:135], a[188:191]\n"
  "v_exp_f32 v34, v34\n"
  "v_mfma_f32_16x16x32_bf16 a[188:191], v[28:31], a[128:131], a[188:191]\n"
  "v_exp_f32 v35, v35\n"
  "v_mfma_f32_16x16x32_bf16 a[172:175], v[8:11], a[132:135], a[172:175]\n"
  "v_add_f32 v254, v254, v32\n"
  "v_add_f32 v254, v254, v33\n"
  "v_mfma_f32_16x16x32_bf16 a[172:175], v[12:15], a[128:131], a[172:175]\n"
  "s_waitcnt lgkmcnt(5)\n"
  "v_add_f32 v254, v254, v34\n"
  "v_add_f32 v254, v254, v35\n"
  "v_mfma_f32_16x16x32_bf16 v[4:7], a[140:143], a[0:3], v[248:251]\n"
  "v_fma_f32 v48, v48, s73, v226\n"
  "v_fma_f32 v49, v49, s73, v226\n"
  "v_mfma_f32_16x16x32_bf16 v[4:7], a[136:139], a[4:7], v[4:7]\n"
  "s_waitcnt lgkmcnt(3)\n"
  "v_exp_f32 v48, v48\n"
  "v_mfma_f32_16x16x32_bf16 v[4:7], a[148:151], a[8:11], v[4:7]\n"
  "v_exp_f32 v49, v49\n"
  "v_mfma_f32_16x16x32_bf16 v[4:7], a[144:147], a[12:15], v[4:7]\n"
  "s_waitcnt lgkmcnt(1)\n"
  "v_fma_f32 v50, v50, s73, v226\n"
  "v_fma_f32 v51, v51, s73, v226\n"
  "v_mfma_f32_16x16x32_bf16 v[4:7], a[120:123], a[16:19], v[4:7]\n"
  "v_exp_f32 v50, v50\n"
  "v_mfma_f32_16x16x32_bf16 v[4:7], a[124:127], a[20:23], v[4:7]\n"
  "v_exp_f32 v51, v51\n"
  "v_mfma_f32_16x16x32_bf16 v[20:23], a[124:127], a[44:47], v[248:251]\n"
  "v_add_f32 v255, v255, v48\n"
  "v_add_f32 v255, v255, v49\n"
  "v_mfma_f32_16x16x32_bf16 v[20:23], a[120:123], a[40:43], v[20:23]\n"
  "v_add_f32 v255, v255, v50\n"
  "v_add_f32 v255, v255, v51\n"
  "v_mfma_f32_16x16x32_bf16 v[20:23], a[144:147], a[36:39], v[20:23]\n"
  "ds_read_b64_tr_b16 a[132:133], v219 offset:12368\n"
  "v_fma_f32 v4, v4, s73, v223\n"
  "v_mfma_f32_16x16x32_bf16 v[20:23], a[148:151], a[32:35], v[20:23]\n"
  "ds_read_b64_tr_b16 a[134:135], v220 offset:12368\n"
  "v_fma_f32 v5, v5, s73, v223\n"
  "v_mfma_f32_16x16x32_bf16 v[20:23], a[136:139], a[28:31], v[20:23]\n"
  "ds_read_b64_tr_b16 a[128:129], v219 offset:13392\n"
  "v_fma_f32 v6, v6, s73, v223\n"
  "v_mfma_f32_16x16x32_bf16 v[20:23], a[140:143], a[24:27], v[20:23]\n"
  "ds_read_b64_tr_b16 a[130:131], v220 offset:13392\n"
  "v_fma_f32 v7, v7, s73, v223\n"
  "v_mfma_f32_16x16x32_bf16 v[36:39], a[140:143], a[48:51], v[248:251]\n"
  "v_exp_f32 v4, v4\n"
  "v_mfma_f32_16x16x32_bf16 v[36:39], a[136:139], a[52:55], v[36:39]\n"
  "v_exp_f32 v5, v5\n"
  "v_mfma_f32_16x16x32_bf16 v[36:39], a[148:151], a[56:59], v[36:39]\n"
  "v_exp_f32 v6, v6\n"
  "v_mfma_f32_16x16x32_bf16 v[36:39], a[144:147], a[60:63], v[36:39]\n"
  "v_exp_f32 v7, v7\n"
  "v_mfma_f32_16x16x32_bf16 v[36:39], a[120:123], a[64:67], v[36:39]\n"
  "v_fma_f32 v20, v20, s73, v224\n"
  "v_fma_f32 v21, v21, s73, v224\n"
  "v_mfma_f32_16x16x32_bf16 v[36:39], a[124:127], a[68:71], v[36:39]\n"
  "v_exp_f32 v20, v20\n"
  "v_mfma_f32_16x16x32_bf16 v[52:55], a[124:127], a[92:95], v[248:251]\n"
  "ds_read_b64_tr_b16 a[124:125], v217 offset:14400\n"
  "v_fma_f32 v22, v22, s73, v224\n"
  "v_mfma_f32_16x16x32_bf16 v[52:55], a[120:123], a[88:91], v[52:55]\n"
  "ds_read_b64_tr_b16 a[126:127], v218 offset:14400\n"
  "v_fma_f32 v23, v23, s73, v224\n"
  "v_mfma_f32_16x16x32_bf16 v[52:55], a[144:147], a[84:87], v[52:55]\n"
  "ds_read_b64_tr_b16 a[120:121], v217 offset:14416\n"
  "v_fma_f32 v36, v36, s73, v225\n"
  "v_mfma_f32_16x16x32_bf16 v[52:55], a[148:151], a[80:83], v[52:55]\n"
  "ds_read_b64_tr_b16 a[122:123], v218 offset:14416\n"
  "v_fma_f32 v37, v37, s73, v225\n"
  "v_mfma_f32_16x16x32_bf16 v[52:55], a[136:139], a[76:79], v[52:55]\n"
  "v_exp_f32 v36, v36\n"
  "v_mfma_f32_16x16x32_bf16 v[52:55], a[140:143], a[72:75], v[52:55]\n"
  "s_waitcnt lgkmcnt(4)\n"
  "v_exp_f32 v37, v37\n"
  "v_mfma_f32_16x16x32_bf16 a[176:179], v[8:11], a[132:135], a[176:179]\n"
  "v_cvt_pk_bf16_f32 v8, v0, v1\n"
  "v_cvt_pk_bf16_f32 v9, v2, v3\n"
  "v_mfma_f32_16x16x32_bf16 a[176:179], v[12:15], a[128:131], a[176:179]\n"
  "v_exp_f32 v21, v21\n"
  "v_mfma_f32_16x16x32_bf16 a[192:195], v[24:27], a[132:135], a[192:195]\n"
  "ds_read_b64_tr_b16 a[136:137], v219 offset:14400\n"
  "v_cvt_pk_bf16_f32 v24, v16, v17\n"
  "v_mfma_f32_16x16x32_bf16 a[192:195], v[28:31], a[128:131], a[192:195]\n"
  "ds_read_b64_tr_b16 a[138:139], v220 offset:14400\n"
  "v_cvt_pk_bf16_f32 v25, v18, v19\n"
  "v_mfma_f32_16x16x32_bf16 a[208:211], v[40:43], a[132:135], a[208:211]\n"
  "ds_read_b64_tr_b16 a[140:141], v219 offset:14416\n"
  "v_cvt_pk_bf16_f32 v40, v32, v33\n"
  "v_mfma_f32_16x16x32_bf16 a[208:211], v[44:47], a[128:131], a[208:211]\n"
  "ds_read_b64_tr_b16 a[142:143], v220 offset:14416\n"
  "v_cvt_pk_bf16_f32 v41, v34, v35\n"
  "v_mfma_f32_16x16x32_bf16 a[224:227], v[56:59], a[132:135], a[224:227]\n"
  "v_exp_f32 v22, v22\n"
  "v_mfma_f32_16x16x32_bf16 a[224:227], v[60:63], a[128:131], a[224:227]\n"
  "s_waitcnt lgkmcnt(4)\n"
  "v_exp_f32 v23, v23\n"
  "v_mfma_f32_16x16x32_bf16 a[244:247], a[160:163], a[124:127], a[244:247]\n"
  "v_cvt_pk_bf16_f32 v56, v48, v49\n"
  "v_cvt_pk_bf16_f32 v57, v50, v51\n"
  "v_mfma_f32_16x16x32_bf16 a[248:251], a[160:163], a[120:123], a[248:251]\n"
  "v_fma_f32 v52, v52, s73, v226\n"
  "v_fma_f32 v53, v53, s73, v226\n"
  "v_mfma_f32_16x16x32_bf16 v[200:203], v[60:63], a[124:127], v[200:203]\n"
  "v_exp_f32 v52, v52\n"
  "v_mfma_f32_16x16x32_bf16 v[204:207], v[60:63], a[120:123], v[204:207]\n"
  "v_exp_f32 v53, v53\n"
  "v_mfma_f32_16x16x32_bf16 v[168:171], v[44:47], a[124:127], v[168:171]\n"
  "v_fma_f32 v54, v54, s73, v226\n"
  "v_fma_f32 v55, v55, s73, v226\n"
  "v_mfma_f32_16x16x32_bf16 v[172:175], v[44:47], a[120:123], v[172:175]\n"
  "s_waitcnt vmcnt(16) lgkmcnt(0)\n"
  "s_barrier\n"
  "ds_read_b128 a[132:135], v216 offset:24704\n"
  "v_mfma_f32_16x16x32_bf16 v[136:139], v[28:31], a[124:127], v[136:139]\n"
  "ds_read_b64 a[156:157], v231 offset:0\n"
  "v_cvt_pk_bf16_f32 v58, v52, v53\n"
  "v_mfma_f32_16x16x32_bf16 v[140:143], v[28:31], a[120:123], v[140:143]\n"
  "ds_read_b64 a[158:159], v232 offset:0\n"
  "v_fma_f32 v38, v38, s73, v225\n"
  "v_mfma_f32_16x16x32_bf16 v[104:107], v[12:15], a[124:127], v[104:107]\n"
  "ds_read_b128 a[128:131], v216 offset:24768\n"
  "v_fma_f32 v39, v39, s73, v225\n"
  "v_mfma_f32_16x16x32_bf16 v[108:111], v[12:15], a[120:123], v[108:111]\n"
  "ds_read_b128 a[124:127], v216 offset:25728\n"
  "v_cvt_pk_bf16_f32 v42, v36, v37\n"
  "v_mfma_f32_16x16x32_bf16 v[112:115], v[12:15], a[136:139], v[112:115]\n"
  "ds_read_b128 a[120:123], v216 offset:25792\n"
  "v_cvt_pk_bf16_f32 v26, v20, v21\n"
  "v_mfma_f32_16x16x32_bf16 v[116:119], v[12:15], a[140:143], v[116:119]\n"
  "s_mov_b32 m0, s67\n"
  "ds_read_b128 a[144:147], v216 offset:26752\n"
  "v_cvt_pk_bf16_f32 v27, v22, v23\n"
  "v_mfma_f32_16x16x32_bf16 v[144:147], v[28:31], a[136:139], v[144:147]\n"
  "ds_read_b128 a[148:151], v216 offset:26816\n"
  "v_cvt_pk_bf16_f32 v10, v4, v5\n"
  "v_mfma_f32_16x16x32_bf16 v[148:151], v[28:31], a[140:143], v[148:151]\n"
  "v_exp_f32 v54, v54\n"
  "v_mfma_f32_16x16x32_bf16 v[176:179], v[44:47], a[136:139], v[176:179]\n"
  "v_exp_f32 v55, v55\n"
  "v_mfma_f32_16x16x32_bf16 v[180:183], v[44:47], a[140:143], v[180:183]\n"
  "buffer_load_dwordx4 v221, s[40:43], 0 offen offset:0 lds\n"
  "v_mfma_f32_16x16x32_bf16 v[208:211], v[60:63], a[136:139], v[208:211]\n"
  "s_addk_i32 m0, 896\n"
  "v_exp_f32 v38, v38\n"
  "v_mfma_f32_16x16x32_bf16 v[212:215], v[60:63], a[140:143], v[212:215]\n"
  "s_waitcnt lgkmcnt(4)\n"
  "v_exp_f32 v39, v39\n"
  "v_mfma_f32_16x16x32_bf16 v[64:67], a[132:135], a[96:99], v[248:251]\n"
  "v_cvt_pk_bf16_f32 v59, v54, v55\n"
  "v_cvt_pk_bf16_f32 v43, v38, v39\n"
  "v_mfma_f32_16x16x32_bf16 v[64:67], a[128:131], a[100:103], v[64:67]\n"
  "s_waitcnt lgkmcnt(2)\n"
  "buffer_load_dwordx4 v221, s[40:43], 0 offen offset:128 lds\n"
  "v_cvt_pk_bf16_f32 v11, v6, v7\n"
  "v_mfma_f32_16x16x32_bf16 v[64:67], a[124:127], a[104:107], v[64:67]\n"
  "s_addk_i32 m0, 896\n"
  "v_add_f32 v252, v252, v4\n"
  "v_add_f32 v252, v252, v5\n"
  "v_mfma_f32_16x16x32_bf16 v[64:67], a[120:123], a[108:111], v[64:67]\n"
  "s_waitcnt lgkmcnt(0)\n"
  "v_add_f32 v252, v252, v6\n"
  "v_add_f32 v252, v252, v7\n"
  "v_mfma_f32_16x16x32_bf16 v[64:67], a[144:147], a[112:115], v[64:67]\n"
  "v_add_f32 v253, v253, v20\n"
  "v_add_f32 v253, v253, v21\n"
  "v_mfma_f32_16x16x32_bf16 v[64:67], a[148:151], a[116:119], v[64:67]\n"
  "buffer_load_dwordx4 v221, s[40:43], 0 offen offset:256 lds\n"
  "v_add_f32 v253, v253, v22\n"
  "v_mfma_f32_16x16x32_bf16 v[0:3], a[148:151], a[20:23], v[248:251]\n"
  "s_add_u32 s40, s86, s96\n"
  "v_add_f32 v253, v253, v23\n"
  "v_add_f32 v254, v254, v36\n"
  "v_mfma_f32_16x16x32_bf16 v[0:3], a[144:147], a[16:19], v[0:3]\n"
  "s_addc_u32 s41, s87, 0\n"
  "v_add_f32 v254, v254, v37\n"
  "v_add_f32 v254, v254, v38\n"
  "v_mfma_f32_16x16x32_bf16 v[0:3], a[120:123], a[12:15], v[0:3]\n"
  "s_mov_b32 s42, s30\n"
  "v_add_f32 v254, v254, v39\n"
  "v_add_f32 v255, v255, v52\n"
  "v_mfma_f32_16x16x32_bf16 v[0:3], a[124:127], a[8:11], v[0:3]\n"
  "s_add_u32 m0, s59, -128\n"
  "v_add_f32 v255, v255, v53\n"
  "v_add_f32 v255, v255, v54\n"
  "v_mfma_f32_16x16x32_bf16 v[0:3], a[128:131], a[4:7], v[0:3]\n"
  "v_add_f32 v255, v255, v55\n"
  "v_fma_f32 v64, v64, s73, v227\n"
  "v_mfma_f32_16x16x32_bf16 v[0:3], a[132:135], a[0:3], v[0:3]\n"
  "v_exp_f32 v64, v64\n"
  "v_mfma_f32_16x16x32_bf16 v[16:19], a[132:135], a[24:27], v[248:251]\n"
  "v_fma_f32 v65, v65, s73, v227\n"
  "v_fma_f32 v66, v66, s73, v227\n"
  "v_mfma_f32_16x16x32_bf16 v[16:19], a[128:131], a[28:31], v[16:19]\n"
  "buffer_load_dwordx4 v221, s[32:35], 0 offen offset:128 lds\n"
  "v_fma_f32 v67, v67, s73, v227\n"
  "v_mfma_f32_16x16x32_bf16 v[16:19], a[124:127], a[32:35], v[16:19]\n"
  "s_addk_i32 m0, 1024\n"
  "ds_read_b64_tr_b16 a[136:137], v217 offset:24704\n"
  "v_fma_f32 v0, v0, s73, v223\n"
  "v_mfma_f32_16x16x32_bf16 v[16:19], a[120:123], a[36:39], v[16:19]\n"
  "s_mov_b64 s[32:33], s[44:45]\n"
  "ds_read_b64_tr_b16 a[138:139], v218 offset:24704\n"
  "v_fma_f32 v1, v1, s73, v223\n"
  "v_mfma_f32_16x16x32_bf16 v[16:19], a[144:147], a[40:43], v[16:19]\n"
  "ds_read_b64_tr_b16 a[140:141], v217 offset:25728\n"
  "v_fma_f32 v2, v2, s73, v223\n"
  "v_mfma_f32_16x16x32_bf16 v[16:19], a[148:151], a[44:47], v[16:19]\n"
  "buffer_load_dwordx4 v221, s[44:47], 0 offen offset:128 lds\n"
  "v_fma_f32 v3, v3, s73, v223\n"
  "v_mfma_f32_16x16x32_bf16 v[32:35], a[148:151], a[68:71], v[248:251]\n"
  "s_addk_i32 m0, 896\n"
  "ds_read_b64_tr_b16 a[142:143], v218 offset:25728\n"
  "v_mfma_f32_16x16x32_bf16 v[32:35], a[144:147], a[64:67], v[32:35]\n"
  "v_exp_f32 v65, v65\n"
  "v_mfma_f32_16x16x32_bf16 v[32:35], a[120:123], a[60:63], v[32:35]\n"
  "v_exp_f32 v66, v66\n"
  "v_mfma_f32_16x16x32_bf16 v[32:35], a[124:127], a[56:59], v[32:35]\n"
  "buffer_load_dwordx4 v221, s[44:47], 0 offen offset:256 lds\n"
  "v_cvt_pk_bf16_f32 v64, v64, v65\n"
  "v_mfma_f32_16x16x32_bf16 v[32:35], a[128:131], a[52:55], v[32:35]\n"
  "s_add_u32 s44, s44, s31\n"
  "v_exp_f32 v67, v67\n"
  "v_mfma_f32_16x16x32_bf16 v[32:35], a[132:135], a[48:51], v[32:35]\n"
  "s_addc_u32 s45, s45, 0\n"
  "v_exp_f32 v0, v0\n"
  "v_mfma_f32_16x16x32_bf16 v[48:51], a[132:135], a[72:75], v[248:251]\n"
  "ds_read_b64_tr_b16 a[132:133], v217 offset:24720\n"
  "v_cvt_pk_bf16_f32 v65, v66, v67\n"
  "v_mfma_f32_16x16x32_bf16 v[48:51], a[128:131], a[76:79], v[48:51]\n"
  "ds_read_b64_tr_b16 a[134:135], v218 offset:24720\n"
  "v_add_f32 v252, v252, v0\n"
  "v_mfma_f32_16x16x32_bf16 v[48:51], a[124:127], a[80:83], v[48:51]\n"
  "ds_read_b64_tr_b16 a[128:129], v217 offset:25744\n"
  "v_fma_f32 v16, v16, s73, v224\n"
  "v_mfma_f32_16x16x32_bf16 v[48:51], a[120:123], a[84:87], v[48:51]\n"
  "ds_read_b64_tr_b16 a[130:131], v218 offset:25744\n"
  "v_fma_f32 v17, v17, s73, v224\n"
  "v_mfma_f32_16x16x32_bf16 v[48:51], a[144:147], a[88:91], v[48:51]\n"
  "v_exp_f32 v1, v1\n"
  "v_mfma_f32_16x16x32_bf16 v[48:51], a[148:151], a[92:95], v[48:51]\n"
  "s_waitcnt lgkmcnt(4)\n"
  "v_exp_f32 v2, v2\n"
  "v_mfma_f32_16x16x32_bf16 a[236:239], a[160:163], a[136:139], a[236:239]\n"
  "v_exp_f32 v3, v3\n"
  "v_mfma_f32_16x16x32_bf16 a[236:239], a[156:159], a[140:143], a[236:239]\n"
  "v_exp_f32 v16, v16\n"
  "v_mfma_f32_16x16x32_bf16 v[184:187], v[60:63], a[136:139], v[184:187]\n"
  "v_exp_f32 v17, v17\n"
  "v_mfma_f32_16x16x32_bf16 v[184:187], v[56:59], a[140:143], v[184:187]\n"
  "v_add_f32 v252, v252, v1\n"
  "v_add_f32 v252, v252, v2\n"
  "v_mfma_f32_16x16x32_bf16 v[152:155], v[44:47], a[136:139], v[152:155]\n"
  "ds_read_b64_tr_b16 a[124:125], v219 offset:24704\n"
  "v_add_f32 v252, v252, v3\n"
  "v_mfma_f32_16x16x32_bf16 v[152:155], v[40:43], a[140:143], v[152:155]\n"
  "ds_read_b64_tr_b16 a[126:127], v220 offset:24704\n"
  "v_fma_f32 v18, v18, s73, v224\n"
  "v_mfma_f32_16x16x32_bf16 v[120:123], v[28:31], a[136:139], v[120:123]\n"
  "ds_read_b64_tr_b16 a[120:121], v219 offset:25728\n"
  "v_fma_f32 v19, v19, s73, v224\n"
  "v_mfma_f32_16x16x32_bf16 v[120:123], v[24:27], a[140:143], v[120:123]\n"
  "ds_read_b64_tr_b16 a[122:123], v220 offset:25728\n"
  "v_fma_f32 v32, v32, s73, v225\n"
  "v_mfma_f32_16x16x32_bf16 v[88:91], v[12:15], a[136:139], v[88:91]\n"
  "v_exp_f32 v18, v18\n"
  "v_mfma_f32_16x16x32_bf16 v[88:91], v[8:11], a[140:143], v[88:91]\n"
  "s_waitcnt lgkmcnt(4)\n"
  "v_exp_f32 v19, v19\n"
  "v_mfma_f32_16x16x32_bf16 v[92:95], v[12:15], a[132:135], v[92:95]\n"
  "v_exp_f32 v32, v32\n"
  "v_mfma_f32_16x16x32_bf16 v[92:95], v[8:11], a[128:131], v[92:95]\n"
  "v_fma_f32 v33, v33, s73, v225\n"
  "v_fma_f32 v34, v34, s73, v225\n"
  "v_mfma_f32_16x16x32_bf16 v[124:127], v[28:31], a[132:135], v[124:127]\n"
  "ds_read_b128 a[136:139], v235 offset:24704\n"
  "v_fma_f32 v35, v35, s73, v225\n"
  "v_mfma_f32_16x16x32_bf16 v[124:127], v[24:27], a[128:131], v[124:127]\n"
  "ds_read_b128 a[140:143], v235 offset:24768\n"
  "v_fma_f32 v48, v48, s73, v226\n"
  "v_mfma_f32_16x16x32_bf16 v[156:159], v[44:47], a[132:135], v[156:159]\n"
  "ds_read_b128 a[144:147], v235 offset:25728\n"
  "v_fma_f32 v49, v49, s73, v226\n"
  "v_mfma_f32_16x16x32_bf16 v[156:159], v[40:43], a[128:131], v[156:159]\n"
  "ds_read_b128 a[148:151], v235 offset:25792\n"
  "v_fma_f32 v50, v50, s73, v226\n"
  "v_mfma_f32_16x16x32_bf16 v[188:191], v[60:63], a[132:135], v[188:191]\n"
  "v_exp_f32 v33, v33\n"
  "v_mfma_f32_16x16x32_bf16 v[188:191], v[56:59], a[128:131], v[188:191]\n"
  "v_exp_f32 v34, v34\n"
  "v_mfma_f32_16x16x32_bf16 a[240:243], a[160:163], a[132:135], a[240:243]\n"
  "ds_read_b128 a[132:135], v235 offset:26752\n"
  "v_fma_f32 v51, v51, s73, v226\n"
  "v_mfma_f32_16x16x32_bf16 a[240:243], a[156:159], a[128:131], a[240:243]\n"
  "s_waitcnt lgkmcnt(5)\n"
  "ds_read_b128 a[128:131], v235 offset:26816\n"
  "v_add_f32 v253, v253, v16\n"
  "v_mfma_f32_16x16x32_bf16 v[192:195], v[60:63], a[124:127], v[192:195]\n"
  "v_exp_f32 v35, v35\n"
  "v_mfma_f32_16x16x32_bf16 v[192:195], v[56:59], a[120:123], v[192:195]\n"
  "ds_write_b64 v231, v[64:65] offset:2048\n"
  "v_mfma_f32_16x16x32_bf16 v[160:163], v[44:47], a[124:127], v[160:163]\n"
  "v_exp_f32 v48, v48\n"
  "v_mfma_f32_16x16x32_bf16 v[160:163], v[40:43], a[120:123], v[160:163]\n"
  "v_exp_f32 v49, v49\n"
  "v_mfma_f32_16x16x32_bf16 v[128:131], v[28:31], a[124:127], v[128:131]\n"
  "v_exp_f32 v50, v50\n"
  "v_mfma_f32_16x16x32_bf16 v[128:131], v[24:27], a[120:123], v[128:131]\n"
  "v_exp_f32 v51, v51\n"
  "v_mfma_f32_16x16x32_bf16 v[96:99], v[12:15], a[124:127], v[96:99]\n"
  "v_add_f32 v253, v253, v17\n"
  "v_add_f32 v253, v253, v18\n"
  "v_mfma_f32_16x16x32_bf16 v[96:99], v[8:11], a[120:123], v[96:99]\n"
  "s_waitcnt lgkmcnt(5)\n"
  "v_add_f32 v253, v253, v19\n"
  "v_add_f32 v254, v254, v32\n"
  "v_mfma_f32_16x16x32_bf16 v[4:7], a[136:139], a[0:3], v[248:251]\n"
  "v_add_f32 v254, v254, v33\n"
  "v_add_f32 v254, v254, v34\n"
  "v_mfma_f32_16x16x32_bf16 v[4:7], a[140:143], a[4:7], v[4:7]\n"
  "s_waitcnt lgkmcnt(3)\n"
  "v_add_f32 v254, v254, v35\n"
  "v_add_f32 v255, v255, v48\n"
  "v_mfma_f32_16x16x32_bf16 v[4:7], a[144:147], a[8:11], v[4:7]\n"
  "v_add_f32 v255, v255, v49\n"
  "v_add_f32 v255, v255, v50\n"
  "v_mfma_f32_16x16x32_bf16 v[4:7], a[148:151], a[12:15], v[4:7]\n"
  "s_waitcnt lgkmcnt(1)\n"
  "v_add_f32 v255, v255, v51\n"
  "v_mfma_f32_16x16x32_bf16 v[4:7], a[132:135], a[16:19], v[4:7]\n"
  "v_mfma_f32_16x16x32_bf16 v[4:7], a[128:131], a[20:23], v[4:7]\n"
  "v_mfma_f32_16x16x32_bf16 v[20:23], a[128:131], a[44:47], v[248:251]\n"
  "v_mfma_f32_16x16x32_bf16 v[20:23], a[132:135], a[40:43], v[20:23]\n"
  "v_mfma_f32_16x16x32_bf16 v[20:23], a[148:151], a[36:39], v[20:23]\n"
  "ds_read_b64_tr_b16 a[124:125], v219 offset:24720\n"
  "v_fma_f32 v4, v4, s73, v223\n"
  "v_mfma_f32_16x16x32_bf16 v[20:23], a[144:147], a[32:35], v[20:23]\n"
  "ds_read_b64_tr_b16 a[126:127], v220 offset:24720\n"
  "v_fma_f32 v5, v5, s73, v223\n"
  "v_mfma_f32_16x16x32_bf16 v[20:23], a[140:143], a[28:31], v[20:23]\n"
  "ds_read_b64_tr_b16 a[120:121], v219 offset:25744\n"
  "v_fma_f32 v6, v6, s73, v223\n"
  "v_mfma_f32_16x16x32_bf16 v[20:23], a[136:139], a[24:27], v[20:23]\n"
  "ds_read_b64_tr_b16 a[122:123], v220 offset:25744\n"
  "v_fma_f32 v7, v7, s73, v223\n"
  "v_mfma_f32_16x16x32_bf16 v[36:39], a[136:139], a[48:51], v[248:251]\n"
  "v_exp_f32 v4, v4\n"
  "v_mfma_f32_16x16x32_bf16 v[36:39], a[140:143], a[52:55], v[36:39]\n"
  "v_exp_f32 v5, v5\n"
  "v_mfma_f32_16x16x32_bf16 v[36:39], a[144:147], a[56:59], v[36:39]\n"
  "v_exp_f32 v6, v6\n"
  "v_mfma_f32_16x16x32_bf16 v[36:39], a[148:151], a[60:63], v[36:39]\n"
  "v_exp_f32 v7, v7\n"
  "v_mfma_f32_16x16x32_bf16 v[36:39], a[132:135], a[64:67], v[36:39]\n"
  "v_fma_f32 v20, v20, s73, v224\n"
  "v_fma_f32 v21, v21, s73, v224\n"
  "v_mfma_f32_16x16x32_bf16 v[36:39], a[128:131], a[68:71], v[36:39]\n"
  "v_exp_f32 v20, v20\n"
  "v_mfma_f32_16x16x32_bf16 v[52:55], a[128:131], a[92:95], v[248:251]\n"
  "ds_read_b64_tr_b16 a[128:129], v217 offset:26752\n"
  "v_fma_f32 v22, v22, s73, v224\n"
  "v_mfma_f32_16x16x32_bf16 v[52:55], a[132:135], a[88:91], v[52:55]\n"
  "ds_read_b64_tr_b16 a[130:131], v218 offset:26752\n"
  "v_fma_f32 v23, v23, s73, v224\n"
  "v_mfma_f32_16x16x32_bf16 v[52:55], a[148:151], a[84:87], v[52:55]\n"
  "ds_read_b64_tr_b16 a[132:133], v217 offset:26768\n"
  "v_fma_f32 v36, v36, s73, v225\n"
  "v_mfma_f32_16x16x32_bf16 v[52:55], a[144:147], a[80:83], v[52:55]\n"
  "ds_read_b64_tr_b16 a[134:135], v218 offset:26768\n"
  "v_fma_f32 v37, v37, s73, v225\n"
  "v_mfma_f32_16x16x32_bf16 v[52:55], a[140:143], a[76:79], v[52:55]\n"
  "v_exp_f32 v21, v21\n"
  "v_mfma_f32_16x16x32_bf16 v[52:55], a[136:139], a[72:75], v[52:55]\n"
  "s_waitcnt lgkmcnt(4)\n"
  "v_exp_f32 v22, v22\n"
  "v_mfma_f32_16x16x32_bf16 v[100:103], v[12:15], a[124:127], v[100:103]\n"
  "v_exp_f32 v23, v23\n"
  "v_mfma_f32_16x16x32_bf16 v[100:103], v[8:11], a[120:123], v[100:103]\n"
  "v_exp_f32 v36, v36\n"
  "v_mfma_f32_16x16x32_bf16 v[132:135], v[28:31], a[124:127], v[132:135]\n"
  "ds_read_b64_tr_b16 a[148:149], v219 offset:26752\n"
  "v_cvt_pk_bf16_f32 v12, v0, v1\n"
  "v_mfma_f32_16x16x32_bf16 v[132:135], v[24:27], a[120:123], v[132:135]\n"
  "ds_read_b64_tr_b16 a[150:151], v220 offset:26752\n"
  "v_cvt_pk_bf16_f32 v13, v2, v3\n"
  "v_mfma_f32_16x16x32_bf16 v[164:167], v[44:47], a[124:127], v[164:167]\n"
  "ds_read_b64_tr_b16 a[144:145], v219 offset:26768\n"
  "v_fma_f32 v38, v38, s73, v225\n"
  "v_mfma_f32_16x16x32_bf16 v[164:167], v[40:43], a[120:123], v[164:167]\n"
  "ds_read_b64_tr_b16 a[146:147], v220 offset:26768\n"
  "v_fma_f32 v39, v39, s73, v225\n"
  "v_mfma_f32_16x16x32_bf16 v[196:199], v[60:63], a[124:127], v[196:199]\n"
  "v_exp_f32 v37, v37\n"
  "v_mfma_f32_16x16x32_bf16 v[196:199], v[56:59], a[120:123], v[196:199]\n"
  "s_waitcnt lgkmcnt(4)\n"
  "v_exp_f32 v38, v38\n"
  "v_mfma_f32_16x16x32_bf16 a[244:247], a[156:159], a[128:131], a[244:247]\n"
  "v_exp_f32 v39, v39\n"
  "v_mfma_f32_16x16x32_bf16 a[248:251], a[156:159], a[132:135], a[248:251]\n"
  "v_fma_f32 v52, v52, s73, v226\n"
  "v_fma_f32 v53, v53, s73, v226\n"
  "v_mfma_f32_16x16x32_bf16 v[200:203], v[56:59], a[128:131], v[200:203]\n"
  "v_exp_f32 v52, v52\n"
  "v_mfma_f32_16x16x32_bf16 v[204:207], v[56:59], a[132:135], v[204:207]\n"
  "v_exp_f32 v53, v53\n"
  "v_mfma_f32_16x16x32_bf16 v[168:171], v[40:43], a[128:131], v[168:171]\n"
  "v_fma_f32 v54, v54, s73, v226\n"
  "v_fma_f32 v55, v55, s73, v226\n"
  "v_mfma_f32_16x16x32_bf16 v[172:175], v[40:43], a[132:135], v[172:175]\n"
  "s_waitcnt vmcnt(11) lgkmcnt(0)\n"
  "s_barrier\n"
  "ds_read_b128 a[120:123], v216 offset:0\n"
  "v_mfma_f32_16x16x32_bf16 v[136:139], v[24:27], a[128:131], v[136:139]\n"
  "ds_read_b64 a[160:161], v231 offset:2048\n"
  "v_add_f32 v252, v252, v4\n"
  "v_mfma_f32_16x16x32_bf16 v[140:143], v[24:27], a[132:135], v[140:143]\n"
  "ds_read_b64 a[162:163], v232 offset:2048\n"
  "v_add_f32 v252, v252, v5\n"
  "v_mfma_f32_16x16x32_bf16 v[104:107], v[8:11], a[128:131], v[104:107]\n"
  "ds_read_b128 a[124:127], v216 offset:64\n"
  "v_add_f32 v252, v252, v6\n"
  "v_mfma_f32_16x16x32_bf16 v[108:111], v[8:11], a[132:135], v[108:111]\n"
  "ds_read_b128 a[128:131], v216 offset:1024\n"
  "v_add_f32 v252, v252, v7\n"
  "v_mfma_f32_16x16x32_bf16 v[112:115], v[8:11], a[148:151], v[112:115]\n"
  "ds_read_b128 a[132:135], v216 offset:1088\n"
  "v_add_f32 v253, v253, v20\n"
  "v_mfma_f32_16x16x32_bf16 v[116:119], v[8:11], a[144:147], v[116:119]\n"
  "s_mov_b32 m0, s68\n"
  "ds_read_b128 a[136:139], v216 offset:2048\n"
  "v_add_f32 v253, v253, v21\n"
  "v_mfma_f32_16x16x32_bf16 v[144:147], v[24:27], a[148:151], v[144:147]\n"
  "ds_read_b128 a[140:143], v216 offset:2112\n"
  "v_add_f32 v253, v253, v22\n"
  "v_mfma_f32_16x16x32_bf16 v[148:151], v[24:27], a[144:147], v[148:151]\n"
  "v_exp_f32 v54, v54\n"
  "v_mfma_f32_16x16x32_bf16 v[176:179], v[40:43], a[148:151], v[176:179]\n"
  "v_exp_f32 v55, v55\n"
  "v_mfma_f32_16x16x32_bf16 v[180:183], v[40:43], a[144:147], v[180:183]\n"
  "buffer_load_dwordx4 v221, s[40:43], 0 offen offset:0 lds\n"
  "v_mfma_f32_16x16x32_bf16 v[208:211], v[56:59], a[148:151], v[208:211]\n"
  "s_addk_i32 m0, 896\n"
  "v_add_f32 v253, v253, v23\n"
  "v_add_f32 v254, v254, v36\n"
  "v_mfma_f32_16x16x32_bf16 v[212:215], v[56:59], a[144:147], v[212:215]\n"
  "s_waitcnt lgkmcnt(4)\n"
  "v_add_f32 v254, v254, v37\n"
  "v_add_f32 v254, v254, v38\n"
  "v_mfma_f32_16x16x32_bf16 v[64:67], a[120:123], a[96:99], v[248:251]\n"
  "v_add_f32 v254, v254, v39\n"
  "v_add_f32 v255, v255, v52\n"
  "v_mfma_f32_16x16x32_bf16 v[64:67], a[124:127], a[100:103], v[64:67]\n"
  "s_waitcnt lgkmcnt(2)\n"
  "buffer_load_dwordx4 v221, s[40:43], 0 offen offset:128 lds\n"
  "v_add_f32 v255, v255, v53\n"
  "v_mfma_f32_16x16x32_bf16 v[64:67], a[128:131], a[104:107], v[64:67]\n"
  "s_addk_i32 m0, 896\n"
  "v_add_f32 v255, v255, v54\n"
  "v_add_f32 v255, v255, v55\n"
  "v_mfma_f32_16x16x32_bf16 v[64:67], a[132:135], a[108:111], v[64:67]\n"
  "s_waitcnt lgkmcnt(0)\n"
  "v_cvt_pk_bf16_f32 v14, v4, v5\n"
  "v_cvt_pk_bf16_f32 v15, v6, v7\n"
  "v_mfma_f32_16x16x32_bf16 v[64:67], a[136:139], a[112:115], v[64:67]\n"
  "v_cvt_pk_bf16_f32 v28, v16, v17\n"
  "v_cvt_pk_bf16_f32 v29, v18, v19\n"
  "v_mfma_f32_16x16x32_bf16 v[64:67], a[140:143], a[116:119], v[64:67]\n"
  "buffer_load_dwordx4 v221, s[40:43], 0 offen offset:256 lds\n"
  "v_cvt_pk_bf16_f32 v30, v20, v21\n"
  "v_mfma_f32_16x16x32_bf16 v[0:3], a[140:143], a[20:23], v[248:251]\n"
  "v_cvt_pk_bf16_f32 v31, v22, v23\n"
  "v_cvt_pk_bf16_f32 v44, v32, v33\n"
  "v_mfma_f32_16x16x32_bf16 v[0:3], a[136:139], a[16:19], v[0:3]\n"
  "v_cvt_pk_bf16_f32 v45, v34, v35\n"
  "v_cvt_pk_bf16_f32 v46, v36, v37\n"
  "v_mfma_f32_16x16x32_bf16 v[0:3], a[132:135], a[12:15], v[0:3]\n"
  "v_cvt_pk_bf16_f32 v47, v38, v39\n"
  "v_cvt_pk_bf16_f32 v60, v48, v49\n"
  "v_mfma_f32_16x16x32_bf16 v[0:3], a[128:131], a[8:11], v[0:3]\n"
  "v_cvt_pk_bf16_f32 v61, v50, v51\n"
  "v_cvt_pk_bf16_f32 v62, v52, v53\n"
  "v_mfma_f32_16x16x32_bf16 v[0:3], a[124:127], a[4:7], v[0:3]\n"
  "v_cvt_pk_bf16_f32 v63, v54, v55\n"
  "v_mfma_f32_16x16x32_bf16 v[0:3], a[120:123], a[0:3], v[0:3]\n"
  "s_add_u32 s40, s40, 24576\n"
  "s_addc_u32 s41, s41, 0\n"
  "s_branch L_BIG_%=\n"
  ".p2align 5\n"
  "L_TAIL_%=:\n"
  "ds_read_b64_tr_b16 a[120:121], v217 offset:26752\n"
  "ds_read_b64_tr_b16 a[122:123], v218 offset:26752\n"
  "v_exp_f32_e64 v55, v55\n"
  "v_cvt_pk_bf16_f32 v45, v34, v35\n"
  "ds_read_b64_tr_b16 a[124:125], v217 offset:26768\n"
  "ds_read_b64_tr_b16 a[126:127], v218 offset:26768\n"
  "v_cvt_pk_bf16_f32 v46, v36, v37\n"
  "v_cvt_pk_bf16_f32 v47, v38, v39\n"
  "v_cvt_pk_bf16_f32 v60, v48, v49\n"
  "s_waitcnt lgkmcnt(8)\n"
  "ds_read_b64_tr_b16 a[128:129], v219 offset:26752\n"
  "ds_read_b64_tr_b16 a[130:131], v220 offset:26752\n"
  "v_cvt_pk_bf16_f32 v61, v50, v51\n"
  "v_cvt_pk_bf16_f32 v62, v52, v53\n"
  "v_cvt_pk_bf16_f32 v63, v54, v55\n"
  "v_mfma_f32_16x16x32_bf16 v[84:87], a[156:159], a[152:155], v[84:87]\n"
  "s_nop 0\n"
  "ds_read_b64_tr_b16 a[132:133], v219 offset:26768\n"
  "ds_read_b64_tr_b16 a[134:135], v220 offset:26768\n"
  "v_add_u32 v233, s61, v229\n"
  "s_mov_b32 exec_lo, 0\n"
  "v_add_u32 v233, s69, v233\n"
  "s_mov_b32 exec_lo, -1\n"
  "v_mfma_f32_16x16x32_bf16 v[84:87], a[160:163], a[152:155], v[84:87]\n"
  "ds_read_b64_tr_b16 a[136:137], v217 offset:24704\n"
  "ds_read_b64_tr_b16 a[138:139], v218 offset:24704\n"
  "v_mfma_f32_16x16x32_bf16 v[68:71], v[12:15], a[152:155], v[68:71]\n"
  "ds_read_b64_tr_b16 a[140:141], v217 offset:25728\n"
  "ds_read_b64_tr_b16 a[142:143], v218 offset:25728\n"
  "v_mfma_f32_16x16x32_bf16 v[72:75], v[28:31], a[152:155], v[72:75]\n"
  "ds_read_b64_tr_b16 a[144:145], v217 offset:24720\n"
  "ds_read_b64_tr_b16 a[146:147], v218 offset:24720\n"
  "v_mfma_f32_16x16x32_bf16 v[76:79], v[44:47], a[152:155], v[76:79]\n"
  "ds_read_b64_tr_b16 a[148:149], v217 offset:25744\n"
  "ds_read_b64_tr_b16 a[150:151], v218 offset:25744\n"
  "v_mfma_f32_16x16x32_bf16 v[80:83], v[60:63], a[152:155], v[80:83]\n"
  "s_waitcnt lgkmcnt(12)\n"
  "v_maximum3_f32 v236, v68, v69, v70\n"
  "v_rcp_f32 v68, v68\n"
  "v_rcp_f32_e64 v69, v69\n"
  "v_mfma_f32_16x16x32_bf16 a[244:247], a[160:163], a[120:123], a[244:247]\n"
  "v_maximum3_f32 v236, v236, v71, v72\n"
  "v_rcp_f32 v70, v70\n"
  "v_rcp_f32 v71, v71\n"
  "v_mfma_f32_16x16x32_bf16 a[248:251], a[160:163], a[124:127], a[248:251]\n"
  "v_maximum3_f32 v236, v236, v73, v74\n"
  "v_rcp_f32 v72, v72\n"
  "v_rcp_f32 v73, v73\n"
  "v_mfma_f32_16x16x32_bf16 v[104:107], v[12:15], a[120:123], v[104:107]\n"
  "v_maximum3_f32 v236, v236, v75, v76\n"
  "v_rcp_f32 v74, v74\n"
  "v_rcp_f32 v75, v75\n"
  "v_mfma_f32_16x16x32_bf16 v[108:111], v[12:15], a[124:127], v[108:111]\n"
  "s_waitcnt lgkmcnt(8)\n"
  "v_maximum3_f32 v236, v236, v77, v78\n"
  "v_rcp_f32 v76, v76\n"
  "v_rcp_f32 v77, v77\n"
  "v_mfma_f32_16x16x32_bf16 v[112:115], v[12:15], a[128:131], v[112:115]\n"
  "v_maximum3_f32 v236, v236, v79, v80\n"
  "v_rcp_f32 v78, v78\n"
  "v_rcp_f32_e64 v79, v79\n"
  "v_mfma_f32_16x16x32_bf16 v[116:119], v[12:15], a[132:135], v[116:119]\n"
  "v_maximum3_f32 v236, v236, v81, v82\n"
  "v_rcp_f32 v80, v80\n"
  "v_rcp_f32 v81, v81\n"
  "v_pk_fma_f32 v[104:105], v[104:105], v[68:69], v[68:69] neg_lo:[0,0,1] neg_hi:[0,0,1]\n"
  "v_pk_fma_f32 v[106:107], v[106:107], v[70:71], v[70:71] neg_lo:[0,0,1] neg_hi:[0,0,1]\n"
  "v_mfma_f32_16x16x32_bf16 v[136:139], v[28:31], a[120:123], v[136:139]\n"
  "v_maximum3_f32 v236, v236, v83, v84\n"
  "v_rcp_f32 v82, v82\n"
  "v_rcp_f32 v83, v83\n"
  "v_pk_fma_f32 v[108:109], v[108:109], v[68:69], v[68:69] neg_lo:[0,0,1] neg_hi:[0,0,1]\n"
  "v_pk_fma_f32 v[110:111], v[110:111], v[70:71], v[70:71] neg_lo:[0,0,1] neg_hi:[0,0,1]\n"
  "v_mfma_f32_16x16x32_bf16 v[140:143], v[28:31], a[124:127], v[140:143]\n"
  "v_cvt_pk_bf16_f32 v104, v104, v108\n"
  "v_cvt_pk_bf16_f32 v108, v105, v109\n"
  "v_cvt_pk_bf16_f32 v106, v106, v110\n"
  "v_cvt_pk_bf16_f32 v110, v107, v111\n"
  "v_maximum3_f32 v236, v236, v85, v86\n"
  "v_pk_fma_f32 v[112:113], v[112:113], v[68:69], v[68:69] neg_lo:[0,0,1] neg_hi:[0,0,1]\n"
  "v_pk_fma_f32 v[114:115], v[114:115], v[70:71], v[70:71] neg_lo:[0,0,1] neg_hi:[0,0,1]\n"
  "v_mfma_f32_16x16x32_bf16 v[144:147], v[28:31], a[128:131], v[144:147]\n"
  "v_maximum3_f32 v236, v236, v87, v87\n"
  "v_rcp_f32_e64 v84, v84\n"
  "v_rcp_f32_e64 v85, v85\n"
  "v_pk_fma_f32 v[116:117], v[116:117], v[68:69], v[68:69] neg_lo:[0,0,1] neg_hi:[0,0,1]\n"
  "v_pk_fma_f32 v[118:119], v[118:119], v[70:71], v[70:71] neg_lo:[0,0,1] neg_hi:[0,0,1]\n"
  "v_mfma_f32_16x16x32_bf16 v[148:151], v[28:31], a[132:135], v[148:151]\n"
  "v_cvt_pk_bf16_f32 v105, v112, v116\n"
  "v_cvt_pk_bf16_f32 v109, v113, v117\n"
  "v_cvt_pk_bf16_f32 v107, v114, v118\n"
  "v_cvt_pk_bf16_f32 v111, v115, v119\n"
  "v_cmp_ngt_f32_e64 vcc, s75, v236\n"
  "v_pk_fma_f32 v[136:137], v[136:137], v[72:73], v[72:73] neg_lo:[0,0,1] neg_hi:[0,0,1]\n"
  "v_pk_fma_f32 v[138:139], v[138:139], v[74:75], v[74:75] neg_lo:[0,0,1] neg_hi:[0,0,1]\n"
  "v_mfma_f32_16x16x32_bf16 v[168:171], v[44:47], a[120:123], v[168:171]\n"
  "ds_write_b64 v229, v[104:105] offset:8960\n"
  "v_rcp_f32_e64 v86, v86\n"
  "v_pk_fma_f32 v[140:141], v[140:141], v[72:73], v[72:73] neg_lo:[0,0,1] neg_hi:[0,0,1]\n"
  "v_pk_fma_f32 v[142:143], v[142:143], v[74:75], v[74:75] neg_lo:[0,0,1] neg_hi:[0,0,1]\n"
  "v_mfma_f32_16x16x32_bf16 v[172:175], v[44:47], a[124:127], v[172:175]\n"
  "s_cmp_lg_u64 vcc, 0\n"
  "s_cselect_b32 s79, s77, 0\n"
  "s_or_b32 s78, s78, s79\n"
  "s_lshl_b32 s77, s77, 1\n"
  "ds_write_b64 v229, v[108:109] offset:9248\n"
  "v_cvt_pk_bf16_f32 v136, v136, v140\n"
  "v_cvt_pk_bf16_f32 v140, v137, v141\n"
  "v_pk_fma_f32 v[144:145], v[144:145], v[72:73], v[72:73] neg_lo:[0,0,1] neg_hi:[0,0,1]\n"
  "v_pk_fma_f32 v[146:147], v[146:147], v[74:75], v[74:75] neg_lo:[0,0,1] neg_hi:[0,0,1]\n"
  "v_mfma_f32_16x16x32_bf16 v[176:179], v[44:47], a[128:131], v[176:179]\n"
  "ds_write_b64 v229, v[106:107] offset:9536\n"
  "v_cvt_pk_bf16_f32 v138, v138, v142\n"
  "v_cvt_pk_bf16_f32 v142, v139, v143\n"
  "v_pk_fma_f32 v[148:149], v[148:149], v[72:73], v[72:73] neg_lo:[0,0,1] neg_hi:[0,0,1]\n"
  "v_pk_fma_f32 v[150:151], v[150:151], v[74:75], v[74:75] neg_lo:[0,0,1] neg_hi:[0,0,1]\n"
  "v_mfma_f32_16x16x32_bf16 v[180:183], v[44:47], a[132:135], v[180:183]\n"
  "ds_write_b64 v229, v[110:111] offset:9824\n"
  "v_cvt_pk_bf16_f32 v137, v144, v148\n"
  "v_cvt_pk_bf16_f32 v141, v145, v149\n"
  "v_pk_fma_f32 v[168:169], v[168:169], v[76:77], v[76:77] neg_lo:[0,0,1] neg_hi:[0,0,1]\n"
  "v_pk_fma_f32 v[170:171], v[170:171], v[78:79], v[78:79] neg_lo:[0,0,1] neg_hi:[0,0,1]\n"
  "v_mfma_f32_16x16x32_bf16 v[200:203], v[60:63], a[120:123], v[200:203]\n"
  "ds_read_b64_tr_b16 a[120:121], v219 offset:24704\n"
  "ds_read_b64_tr_b16 a[122:123], v220 offset:24704\n"
  "ds_write_b64 v229, v[136:137] offset:11200\n"
  "v_pk_fma_f32 v[172:173], v[172:173], v[76:77], v[76:77] neg_lo:[0,0,1] neg_hi:[0,0,1]\n"
  "v_pk_fma_f32 v[174:175], v[174:175], v[78:79], v[78:79] neg_lo:[0,0,1] neg_hi:[0,0,1]\n"
  "v_mfma_f32_16x16x32_bf16 v[204:207], v[60:63], a[124:127], v[204:207]\n"
  "ds_read_b64_tr_b16 a[124:125], v219 offset:25728\n"
  "ds_read_b64_tr_b16 a[126:127], v220 offset:25728\n"
  "ds_write_b64 v229, v[140:141] offset:11488\n"
  "v_pk_fma_f32 v[176:177], v[176:177], v[76:77], v[76:77] neg_lo:[0,0,1] neg_hi:[0,0,1]\n"
  "v_pk_fma_f32 v[178:179], v[178:179], v[78:79], v[78:79] neg_lo:[0,0,1] neg_hi:[0,0,1]\n"
  "v_mfma_f32_16x16x32_bf16 v[208:211], v[60:63], a[128:131], v[208:211]\n"
  "ds_read_b64_tr_b16 a[128:129], v219 offset:24720\n"
  "ds_read_b64_tr_b16 a[130:131], v220 offset:24720\n"
  "ds_read_b64_tr_b16 v[244:245], v230 offset:8960\n"
  "ds_read_b64_tr_b16 v[246:247], v230 offset:8992\n"
  "v_cvt_pk_bf16_f32 v139, v146, v150\n"
  "v_pk_fma_f32 v[180:181], v[180:181], v[76:77], v[76:77] neg_lo:[0,0,1] neg_hi:[0,0,1]\n"
  "v_pk_fma_f32 v[182:183], v[182:183], v[78:79], v[78:79] neg_lo:[0,0,1] neg_hi:[0,0,1]\n"
  "v_mfma_f32_16x16x32_bf16 v[212:215], v[60:63], a[132:135], v[212:215]\n"
  "s_nop 0\n"
  "s_waitcnt lgkmcnt(15)\n"
  "ds_read_b64_tr_b16 a[132:133], v219 offset:25744\n"
  "ds_read_b64_tr_b16 a[134:135], v220 offset:25744\n"
  "ds_write_b64 v229, v[138:139] offset:11776\n"
  "v_pk_fma_f32 v[200:201], v[200:201], v[80:81], v[80:81] neg_lo:[0,0,1] neg_hi:[0,0,1]\n"
  "v_pk_fma_f32 v[202:203], v[202:203], v[82:83], v[82:83] neg_lo:[0,0,1] neg_hi:[0,0,1]\n"
  "v_mfma_f32_16x16x32_bf16 a[228:231], a[156:159], a[136:139], a[228:231]\n"
  "ds_read_b64_tr_b16 a[252:253], v230 offset:10080\n"
  "ds_read_b64_tr_b16 a[254:255], v230 offset:10112\n"
  "v_cvt_pk_bf16_f32 v143, v147, v151\n"
  "v_rcp_f32 v87, v87\n"
  "v_pk_fma_f32 v[204:205], v[204:205], v[80:81], v[80:81] neg_lo:[0,0,1] neg_hi:[0,0,1]\n"
  "v_pk_fma_f32 v[206:207], v[206:207], v[82:83], v[82:83] neg_lo:[0,0,1] neg_hi:[0,0,1]\n"
  "v_mfma_f32_16x16x32_bf16 a[228:231], a[160:163], a[140:143], a[228:231]\n"
  "s_waitcnt lgkmcnt(15)\n"
  "ds_write_b64 v229, v[142:143] offset:12064\n"
  "v_cvt_pk_bf16_f32 v168, v168, v172\n"
  "v_cvt_pk_bf16_f32 v172, v169, v173\n"
  "v_pk_fma_f32 v[208:209], v[208:209], v[80:81], v[80:81] neg_lo:[0,0,1] neg_hi:[0,0,1]\n"
  "v_pk_fma_f32 v[210:211], v[210:211], v[82:83], v[82:83] neg_lo:[0,0,1] neg_hi:[0,0,1]\n"
  "v_mfma_f32_16x16x32_bf16 a[232:235], a[156:159], a[144:147], a[232:235]\n"
  "v_cvt_pk_bf16_f32 v169, v176, v180\n"
  "v_cvt_pk_bf16_f32 v173, v177, v181\n"
  "v_cvt_pk_bf16_f32 v170, v170, v174\n"
  "v_cvt_pk_bf16_f32 v174, v171, v175\n"
  "v_cvt_pk_bf16_f32 v171, v178, v182\n"
  "v_pk_fma_f32 v[212:213], v[212:213], v[80:81], v[80:81] neg_lo:[0,0,1] neg_hi:[0,0,1]\n"
  "v_pk_fma_f32 v[214:215], v[214:215], v[82:83], v[82:83] neg_lo:[0,0,1] neg_hi:[0,0,1]\n"
  "v_mfma_f32_16x16x32_bf16 a[232:235], a[160:163], a[148:151], a[232:235]\n"
  "ds_read_b64_tr_b16 v[0:1], v230 offset:11200\n"
  "ds_read_b64_tr_b16 v[2:3], v230 offset:11232\n"
  "ds_write_b64 v229, v[168:169] offset:13440\n"
  "v_mfma_f32_16x16x32_bf16 a[164:167], v[8:11], a[136:139], a[164:167]\n"
  "s_waitcnt lgkmcnt(9)\n"
  "buffer_store_dwordx4 v[244:247], v228, s[52:55], s80 offen offset:256 nt\n"
  "ds_write_b64 v229, v[172:173] offset:13728\n"
  "v_cvt_pk_bf16_f32 v175, v179, v183\n"
  "v_mfma_f32_16x16x32_bf16 a[164:167], v[12:15], a[140:143], a[164:167]\n"
  "ds_read_b64_tr_b16 v[4:5], v230 offset:12320\n"
  "ds_read_b64_tr_b16 v[6:7], v230 offset:12352\n"
  "ds_write_b64 v229, v[170:171] offset:14016\n"
  "v_mfma_f32_16x16x32_bf16 a[168:171], v[8:11], a[144:147], a[168:171]\n"
  "s_waitcnt lgkmcnt(8)\n"
  "buffer_store_dwordx4 a[252:255], v228, s[52:55], s80 offen offset:3328 nt\n"
  "ds_write_b64 v229, v[174:175] offset:14304\n"
  "v_cvt_pk_bf16_f32 v200, v200, v204\n"
  "v_mfma_f32_16x16x32_bf16 a[168:171], v[12:15], a[148:151], a[168:171]\n"
  "v_cvt_pk_bf16_f32 v204, v201, v205\n"
  "v_cvt_pk_bf16_f32 v201, v208, v212\n"
  "v_cvt_pk_bf16_f32 v205, v209, v213\n"
  "v_cvt_pk_bf16_f32 v202, v202, v206\n"
  "v_cvt_pk_bf16_f32 v206, v203, v207\n"
  "v_mfma_f32_16x16x32_bf16 a[180:183], v[24:27], a[136:139], a[180:183]\n"
  "ds_read_b64_tr_b16 v[16:17], v230 offset:13440\n"
  "ds_read_b64_tr_b16 v[18:19], v230 offset:13472\n"
  "ds_write_b64 v229, v[200:201] offset:15680\n"
  "v_mfma_f32_16x16x32_bf16 a[180:183], v[28:31], a[140:143], a[180:183]\n"
  "s_waitcnt lgkmcnt(9)\n"
  "buffer_store_dwordx4 v[0:3], v228, s[52:55], s81 offen offset:256 nt\n"
  "ds_write_b64 v229, v[204:205] offset:15968\n"
  "v_cvt_pk_bf16_f32 v203, v210, v214\n"
  "v_mfma_f32_16x16x32_bf16 a[184:187], v[24:27], a[144:147], a[184:187]\n"
  "ds_read_b64_tr_b16 v[20:21], v230 offset:14560\n"
  "ds_write_b64 v229, v[202:203] offset:16256\n"
  "v_cvt_pk_bf16_f32 v207, v211, v215\n"
  "v_mfma_f32_16x16x32_bf16 a[184:187], v[28:31], a[148:151], a[184:187]\n"
  "s_waitcnt lgkmcnt(8)\n"
  "buffer_store_dwordx4 v[4:7], v228, s[52:55], s81 offen offset:3328 nt\n"
  "ds_read_b64_tr_b16 v[22:23], v230 offset:14592\n"
  "ds_write_b64 v229, v[206:207] offset:16544\n"
  "v_mfma_f32_16x16x32_bf16 a[196:199], v[40:43], a[136:139], a[196:199]\n"
  "v_accvgpr_read_b32 v104, a164\n"
  "v_accvgpr_read_b32 v105, a165\n"
  "v_accvgpr_read_b32 v108, a168\n"
  "v_accvgpr_read_b32 v109, a169\n"
  "v_accvgpr_read_b32 v106, a166\n"
  "v_pk_fma_f32 v[104:105], v[104:105], v[68:69], v[68:69] neg_lo:[0,0,1] neg_hi:[0,0,1]\n"
  "v_pk_fma_f32 v[108:109], v[108:109], v[68:69], v[68:69] neg_lo:[0,0,1] neg_hi:[0,0,1]\n"
  "v_mfma_f32_16x16x32_bf16 a[196:199], v[44:47], a[140:143], a[196:199]\n"
  "ds_read_b64_tr_b16 v[244:245], v230 offset:15680\n"
  "ds_read_b64_tr_b16 v[246:247], v230 offset:15712\n"
  "v_accvgpr_read_b32 v107, a167\n"
  "v_accvgpr_read_b32 v110, a170\n"
  "v_accvgpr_read_b32 v111, a171\n"
  "v_pk_fma_f32 v[106:107], v[106:107], v[70:71], v[70:71] neg_lo:[0,0,1] neg_hi:[0,0,1]\n"
  "v_pk_fma_f32 v[110:111], v[110:111], v[70:71], v[70:71] neg_lo:[0,0,1] neg_hi:[0,0,1]\n"
  "v_mfma_f32_16x16x32_bf16 a[200:203], v[40:43], a[144:147], a[200:203]\n"
  "s_nop 0\n"
  "s_waitcnt lgkmcnt(8)\n"
  "buffer_store_dwordx4 v[16:19], v228, s[52:55], s82 offen offset:256 nt\n"
  "v_cvt_pk_bf16_f32 v104, v104, v108\n"
  "v_cvt_pk_bf16_f32 v108, v105, v109\n"
  "v_cvt_pk_bf16_f32 v106, v106, v110\n"
  "v_cvt_pk_bf16_f32 v110, v107, v111\n"
  "v_mfma_f32_16x16x32_bf16 a[200:203], v[44:47], a[148:151], a[200:203]\n"
  "ds_read_b64_tr_b16 a[252:253], v230 offset:16800\n"
  "ds_read_b64_tr_b16 a[254:255], v230 offset:16832\n"
  "v_accvgpr_read_b32 v136, a180\n"
  "v_accvgpr_read_b32 v137, a181\n"
  "v_accvgpr_read_b32 v140, a184\n"
  "v_pk_fma_f32 v[136:137], v[136:137], v[72:73], v[72:73] neg_lo:[0,0,1] neg_hi:[0,0,1]\n"
  "v_mfma_f32_16x16x32_bf16 a[212:215], v[56:59], a[136:139], a[212:215]\n"
  "v_accvgpr_read_b32 v141, a185\n"
  "v_accvgpr_read_b32 v138, a182\n"
  "v_accvgpr_read_b32 v139, a183\n"
  "v_accvgpr_read_b32 v142, a186\n"
  "v_accvgpr_read_b32 v143, a187\n"
  "v_pk_fma_f32 v[140:141], v[140:141], v[72:73], v[72:73] neg_lo:[0,0,1] neg_hi:[0,0,1]\n"
  "v_pk_fma_f32 v[138:139], v[138:139], v[74:75], v[74:75] neg_lo:[0,0,1] neg_hi:[0,0,1]\n"
  "v_pk_fma_f32 v[142:143], v[142:143], v[74:75], v[74:75] neg_lo:[0,0,1] neg_hi:[0,0,1]\n"
  "v_mfma_f32_16x16x32_bf16 a[212:215], v[60:63], a[140:143], a[212:215]\n"
  "s_nop 0\n"
  "s_waitcnt lgkmcnt(5)\n"
  "buffer_store_dwordx4 v[20:23], v228, s[52:55], s82 offen offset:3328 nt\n"
  "v_cvt_pk_bf16_f32 v136, v136, v140\n"
  "v_cvt_pk_bf16_f32 v140, v137, v141\n"
  "v_cvt_pk_bf16_f32 v138, v138, v142\n"
  "v_cvt_pk_bf16_f32 v142, v139, v143\n"
  "v_mfma_f32_16x16x32_bf16 a[216:219], v[56:59], a[144:147], a[216:219]\n"
  "v_accvgpr_read_b32 v168, a196\n"
  "v_accvgpr_read_b32 v169, a197\n"
  "v_accvgpr_read_b32 v172, a200\n"
  "v_accvgpr_read_b32 v173, a201\n"
  "v_accvgpr_read_b32 v170, a198\n"
  "v_pk_fma_f32 v[168:169], v[168:169], v[76:77], v[76:77] neg_lo:[0,0,1] neg_hi:[0,0,1]\n"
  "v_pk_fma_f32 v[172:173], v[172:173], v[76:77], v[76:77] neg_lo:[0,0,1] neg_hi:[0,0,1]\n"
  "v_mfma_f32_16x16x32_bf16 a[216:219], v[60:63], a[148:151], a[216:219]\n"
  "s_nop 0\n"
  "s_waitcnt lgkmcnt(2)\n"
  "buffer_store_dwordx4 v[244:247], v228, s[52:55], s83 offen offset:256 nt\n"
  "v_accvgpr_read_b32 v171, a199\n"
  "v_accvgpr_read_b32 v174, a202\n"
  "v_accvgpr_read_b32 v175, a203\n"
  "v_cvt_pk_bf16_f32 v168, v168, v172\n"
  "v_pk_fma_f32 v[170:171], v[170:171], v[78:79], v[78:79] neg_lo:[0,0,1] neg_hi:[0,0,1]\n"
  "v_pk_fma_f32 v[174:175], v[174:175], v[78:79], v[78:79] neg_lo:[0,0,1] neg_hi:[0,0,1]\n"
  "v_mfma_f32_16x16x32_bf16 a[172:175], v[8:11], a[120:123], a[172:175]\n"
  "s_waitcnt vmcnt(10)\n"
  "s_barrier\n"
  "ds_read_b64_tr_b16 a[136:137], v217 offset:0\n"
  "ds_read_b64_tr_b16 a[138:139], v218 offset:0\n"
  "v_cvt_pk_bf16_f32 v172, v169, v173\n"
  "v_mfma_f32_16x16x32_bf16 a[172:175], v[12:15], a[124:127], a[172:175]\n"
  "s_nop 0\n"
  "s_waitcnt lgkmcnt(2)\n"
  "buffer_store_dwordx4 a[252:255], v228, s[52:55], s83 offen offset:3328 nt\n"
  "ds_read_b64_tr_b16 a[140:141], v217 offset:16\n"
  "ds_read_b64_tr_b16 a[142:143], v218 offset:16\n"
  "v_cvt_pk_bf16_f32 v170, v170, v174\n"
  "v_cvt_pk_bf16_f32 v174, v171, v175\n"
  "v_mfma_f32_16x16x32_bf16 a[176:179], v[8:11], a[128:131], a[176:179]\n"
  "ds_read_b64_tr_b16 a[144:145], v219 offset:0\n"
  "ds_read_b64_tr_b16 a[146:147], v220 offset:0\n"
  "v_accvgpr_read_b32 v240, a244\n"
  "v_accvgpr_read_b32 v241, a245\n"
  "v_accvgpr_read_b32 v242, a248\n"
  "v_pk_fma_f32 v[240:241], v[240:241], v[84:85], v[84:85] neg_lo:[0,0,1] neg_hi:[0,0,1]\n"
  "v_mfma_f32_16x16x32_bf16 a[176:179], v[12:15], a[132:135], a[176:179]\n"
  "ds_read_b64_tr_b16 a[148:149], v219 offset:16\n"
  "ds_read_b64_tr_b16 a[150:151], v220 offset:16\n"
  "v_accvgpr_read_b32 v243, a249\n"
  "v_accvgpr_read_b32 v236, a246\n"
  "v_accvgpr_read_b32 v237, a247\n"
  "v_pk_fma_f32 v[242:243], v[242:243], v[84:85], v[84:85] neg_lo:[0,0,1] neg_hi:[0,0,1]\n"
  "v_pk_fma_f32 v[236:237], v[236:237], v[86:87], v[86:87] neg_lo:[0,0,1] neg_hi:[0,0,1]\n"
  "v_mfma_f32_16x16x32_bf16 a[188:191], v[24:27], a[120:123], a[188:191]\n"
  "v_accvgpr_read_b32 v112, a172\n"
  "v_accvgpr_read_b32 v113, a173\n"
  "v_accvgpr_read_b32 v114, a174\n"
  "v_accvgpr_read_b32 v115, a175\n"
  "v_accvgpr_read_b32 v238, a250\n"
  "v_pk_fma_f32 v[112:113], v[112:113], v[68:69], v[68:69] neg_lo:[0,0,1] neg_hi:[0,0,1]\n"
  "v_pk_fma_f32 v[114:115], v[114:115], v[70:71], v[70:71] neg_lo:[0,0,1] neg_hi:[0,0,1]\n"
  "v_mfma_f32_16x16x32_bf16 a[188:191], v[28:31], a[124:127], a[188:191]\n"
  "v_accvgpr_read_b32 v239, a251\n"
  "v_accvgpr_read_b32 v200, a212\n"
  "v_accvgpr_read_b32 v201, a213\n"
  "v_accvgpr_read_b32 v204, a216\n"
  "v_accvgpr_read_b32 v205, a217\n"
  "v_pk_fma_f32 v[238:239], v[238:239], v[86:87], v[86:87] neg_lo:[0,0,1] neg_hi:[0,0,1]\n"
  "v_pk_fma_f32 v[200:201], v[200:201], v[80:81], v[80:81] neg_lo:[0,0,1] neg_hi:[0,0,1]\n"
  "v_pk_fma_f32 v[204:205], v[204:205], v[80:81], v[80:81] neg_lo:[0,0,1] neg_hi:[0,0,1]\n"
  "v_mfma_f32_16x16x32_bf16 a[192:195], v[24:27], a[128:131], a[192:195]\n"
  "v_accvgpr_read_b32 v116, a176\n"
  "v_accvgpr_read_b32 v117, a177\n"
  "v_accvgpr_read_b32 v118, a178\n"
  "v_accvgpr_read_b32 v119, a179\n"
  "v_accvgpr_read_b32 v202, a214\n"
  "v_pk_fma_f32 v[116:117], v[116:117], v[68:69], v[68:69] neg_lo:[0,0,1] neg_hi:[0,0,1]\n"
  "v_pk_fma_f32 v[118:119], v[118:119], v[70:71], v[70:71] neg_lo:[0,0,1] neg_hi:[0,0,1]\n"
  "v_mfma_f32_16x16x32_bf16 a[192:195], v[28:31], a[132:135], a[192:195]\n"
  "v_cvt_pk_bf16_f32 v105, v112, v116\n"
  "v_cvt_pk_bf16_f32 v109, v113, v117\n"
  "v_cvt_pk_bf16_f32 v107, v114, v118\n"
  "v_cvt_pk_bf16_f32 v111, v115, v119\n"
  "v_accvgpr_read_b32 v203, a215\n"
  "v_pk_fma_f32 v[202:203], v[202:203], v[82:83], v[82:83] neg_lo:[0,0,1] neg_hi:[0,0,1]\n"
  "v_mfma_f32_16x16x32_bf16 a[204:207], v[40:43], a[120:123], a[204:207]\n"
  "ds_write_b64 v229, v[104:105] offset:0\n"
  "v_accvgpr_read_b32 v144, a188\n"
  "v_accvgpr_read_b32 v145, a189\n"
  "v_pk_fma_f32 v[144:145], v[144:145], v[72:73], v[72:73] neg_lo:[0,0,1] neg_hi:[0,0,1]\n"
  "v_mfma_f32_16x16x32_bf16 a[204:207], v[44:47], a[124:127], a[204:207]\n"
  "ds_write_b64 v229, v[108:109] offset:288\n"
  "v_accvgpr_read_b32 v146, a190\n"
  "v_accvgpr_read_b32 v147, a191\n"
  "v_pk_fma_f32 v[146:147], v[146:147], v[74:75], v[74:75] neg_lo:[0,0,1] neg_hi:[0,0,1]\n"
  "v_mfma_f32_16x16x32_bf16 a[208:211], v[40:43], a[128:131], a[208:211]\n"
  "ds_write_b64 v229, v[106:107] offset:576\n"
  "ds_write_b64 v229, v[110:111] offset:864\n"
  "v_mfma_f32_16x16x32_bf16 a[208:211], v[44:47], a[132:135], a[208:211]\n"
  "v_accvgpr_read_b32 v148, a192\n"
  "v_accvgpr_read_b32 v149, a193\n"
  "v_accvgpr_read_b32 v150, a194\n"
  "v_accvgpr_read_b32 v151, a195\n"
  "v_accvgpr_read_b32 v206, a218\n"
  "v_pk_fma_f32 v[148:149], v[148:149], v[72:73], v[72:73] neg_lo:[0,0,1] neg_hi:[0,0,1]\n"
  "v_pk_fma_f32 v[150:151], v[150:151], v[74:75], v[74:75] neg_lo:[0,0,1] neg_hi:[0,0,1]\n"
  "v_mfma_f32_16x16x32_bf16 a[220:223], v[56:59], a[120:123], a[220:223]\n"
  "ds_read_b64_tr_b16 v[0:1], v230 offset:0\n"
  "ds_read_b64_tr_b16 v[2:3], v230 offset:32\n"
  "v_cvt_pk_bf16_f32 v137, v144, v148\n"
  "v_cvt_pk_bf16_f32 v141, v145, v149\n"
  "v_cvt_pk_bf16_f32 v139, v146, v150\n"
  "v_mfma_f32_16x16x32_bf16 a[220:223], v[60:63], a[124:127], a[220:223]\n"
  "ds_read_b64_tr_b16 v[4:5], v230 offset:1120\n"
  "ds_write_b64 v229, v[136:137] offset:2240\n"
  "v_cvt_pk_bf16_f32 v143, v147, v151\n"
  "v_mfma_f32_16x16x32_bf16 a[224:227], v[56:59], a[128:131], a[224:227]\n"
  "ds_read_b64_tr_b16 v[6:7], v230 offset:1152\n"
  "ds_write_b64 v229, v[140:141] offset:2528\n"
  "v_accvgpr_read_b32 v176, a204\n"
  "v_mfma_f32_16x16x32_bf16 a[224:227], v[60:63], a[132:135], a[224:227]\n"
  "s_nop 0\n"
  "s_waitcnt lgkmcnt(14)\n"
  "ds_write_b64 v229, v[138:139] offset:2816\n"
  "ds_write_b64 v229, v[142:143] offset:3104\n"
  "v_mfma_f32_16x16x32_bf16 a[236:239], a[160:163], a[136:139], a[236:239]\n"
  "v_accvgpr_read_b32 v177, a205\n"
  "v_accvgpr_read_b32 v180, a208\n"
  "v_accvgpr_read_b32 v181, a209\n"
  "v_accvgpr_read_b32 v178, a206\n"
  "v_accvgpr_read_b32 v179, a207\n"
  "v_pk_fma_f32 v[176:177], v[176:177], v[76:77], v[76:77] neg_lo:[0,0,1] neg_hi:[0,0,1]\n"
  "v_pk_fma_f32 v[180:181], v[180:181], v[76:77], v[76:77] neg_lo:[0,0,1] neg_hi:[0,0,1]\n"
  "v_pk_fma_f32 v[178:179], v[178:179], v[78:79], v[78:79] neg_lo:[0,0,1] neg_hi:[0,0,1]\n"
  "v_mfma_f32_16x16x32_bf16 a[240:243], a[160:163], a[140:143], a[240:243]\n"
  "ds_read_b64_tr_b16 v[16:17], v230 offset:2240\n"
  "ds_read_b64_tr_b16 v[18:19], v230 offset:2272\n"
  "v_accvgpr_read_b32 v182, a210\n"
  "v_accvgpr_read_b32 v183, a211\n"
  "v_cvt_pk_bf16_f32 v169, v176, v180\n"
  "v_pk_fma_f32 v[182:183], v[182:183], v[78:79], v[78:79] neg_lo:[0,0,1] neg_hi:[0,0,1]\n"
  "v_mfma_f32_16x16x32_bf16 v[88:91], v[12:15], a[136:139], v[88:91]\n"
  "s_waitcnt lgkmcnt(8)\n"
  "buffer_store_dwordx4 v[0:3], v228, s[52:55], s80 offen offset:0 nt\n"
  "ds_write_b64 v229, v[168:169] offset:4480\n"
  "v_cvt_pk_bf16_f32 v173, v177, v181\n"
  "v_mfma_f32_16x16x32_bf16 v[92:95], v[12:15], a[140:143], v[92:95]\n"
  "ds_write_b64 v229, v[172:173] offset:4768\n"
  "v_cvt_pk_bf16_f32 v171, v178, v182\n"
  "v_cvt_pk_bf16_f32 v175, v179, v183\n"
  "v_mfma_f32_16x16x32_bf16 v[96:99], v[12:15], a[144:147], v[96:99]\n"
  "s_waitcnt lgkmcnt(7)\n"
  "buffer_store_dwordx4 v[4:7], v228, s[52:55], s80 offen offset:3072 nt\n"
  "ds_read_b64_tr_b16 v[20:21], v230 offset:3360\n"
  "ds_read_b64_tr_b16 v[22:23], v230 offset:3392\n"
  "ds_write_b64 v229, v[170:171] offset:5056\n"
  "ds_write_b64 v229, v[174:175] offset:5344\n"
  "v_mfma_f32_16x16x32_bf16 v[100:103], v[12:15], a[148:151], v[100:103]\n"
  "v_accvgpr_read_b32 v208, a220\n"
  "v_accvgpr_read_b32 v209, a221\n"
  "v_accvgpr_read_b32 v212, a224\n"
  "v_accvgpr_read_b32 v213, a225\n"
  "v_accvgpr_read_b32 v207, a219\n"
  "v_pk_fma_f32 v[208:209], v[208:209], v[80:81], v[80:81] neg_lo:[0,0,1] neg_hi:[0,0,1]\n"
  "v_pk_fma_f32 v[212:213], v[212:213], v[80:81], v[80:81] neg_lo:[0,0,1] neg_hi:[0,0,1]\n"
  "v_pk_fma_f32 v[206:207], v[206:207], v[82:83], v[82:83] neg_lo:[0,0,1] neg_hi:[0,0,1]\n"
  "v_pk_fma_f32 v[88:89], v[88:89], v[68:69], v[68:69] neg_lo:[0,0,1] neg_hi:[0,0,1]\n"
  "v_mfma_f32_16x16x32_bf16 v[120:123], v[28:31], a[136:139], v[120:123]\n"
  "ds_read_b64_tr_b16 v[244:245], v230 offset:4480\n"
  "ds_read_b64_tr_b16 v[246:247], v230 offset:4512\n"
  "v_accvgpr_read_b32 v210, a222\n"
  "v_accvgpr_read_b32 v211, a223\n"
  "v_accvgpr_read_b32 v214, a226\n"
  "v_pk_fma_f32 v[210:211], v[210:211], v[82:83], v[82:83] neg_lo:[0,0,1] neg_hi:[0,0,1]\n"
  "v_pk_fma_f32 v[92:93], v[92:93], v[68:69], v[68:69] neg_lo:[0,0,1] neg_hi:[0,0,1]\n"
  "v_pk_fma_f32 v[90:91], v[90:91], v[70:71], v[70:71] neg_lo:[0,0,1] neg_hi:[0,0,1]\n"
  "v_pk_fma_f32 v[94:95], v[94:95], v[70:71], v[70:71] neg_lo:[0,0,1] neg_hi:[0,0,1]\n"
  "v_mfma_f32_16x16x32_bf16 v[124:127], v[28:31], a[140:143], v[124:127]\n"
  "s_nop 0\n"
  "s_waitcnt lgkmcnt(8)\n"
  "buffer_store_dwordx4 v[16:19], v228, s[52:55], s81 offen offset:0 nt\n"
  "v_accvgpr_read_b32 v215, a227\n"
  "v_cvt_pk_bf16_f32 v240, v240, v242\n"
  "v_cvt_pk_bf16_f32 v241, v241, v243\n"
  "v_pk_fma_f32 v[214:215], v[214:215], v[82:83], v[82:83] neg_lo:[0,0,1] neg_hi:[0,0,1]\n"
  "v_pk_fma_f32 v[96:97], v[96:97], v[68:69], v[68:69] neg_lo:[0,0,1] neg_hi:[0,0,1]\n"
  "v_pk_fma_f32 v[98:99], v[98:99], v[70:71], v[70:71] neg_lo:[0,0,1] neg_hi:[0,0,1]\n"
  "v_mfma_f32_16x16x32_bf16 v[128:131], v[28:31], a[144:147], v[128:131]\n"
  "v_cvt_pk_bf16_f32 v236, v236, v238\n"
  "v_cvt_pk_bf16_f32 v237, v237, v239\n"
  "v_cvt_pk_bf16_f32 v200, v200, v204\n"
  "v_cvt_pk_bf16_f32 v204, v201, v205\n"
  "v_cvt_pk_bf16_f32 v201, v208, v212\n"
  "v_cvt_pk_bf16_f32 v205, v209, v213\n"
  "v_cvt_pk_bf16_f32 v202, v202, v206\n"
  "v_cvt_pk_bf16_f32 v206, v203, v207\n"
  "v_cvt_pk_bf16_f32 v203, v210, v214\n"
  "v_cvt_pk_bf16_f32 v207, v211, v215\n"
  "v_pk_fma_f32 v[100:101], v[100:101], v[68:69], v[68:69] neg_lo:[0,0,1] neg_hi:[0,0,1]\n"
  "v_pk_fma_f32 v[102:103], v[102:103], v[70:71], v[70:71] neg_lo:[0,0,1] neg_hi:[0,0,1]\n"
  "v_mfma_f32_16x16x32_bf16 v[132:135], v[28:31], a[148:151], v[132:135]\n"
  "ds_read_b64_tr_b16 a[252:253], v230 offset:5600\n"
  "ds_read_b64_tr_b16 a[254:255], v230 offset:5632\n"
  "ds_write_b32 v233, v240 offset:20160\n"
  "ds_write_b32 v233, v241 offset:20448\n"
  "ds_write_b32 v233, v236 offset:20736\n"
  "ds_write_b32 v233, v237 offset:21024\n"
  "ds_write_b64 v229, v[200:201] offset:6720\n"
  "ds_write_b64 v229, v[204:205] offset:7008\n"
  "ds_write_b64 v229, v[202:203] offset:7296\n"
  "ds_write_b64 v229, v[206:207] offset:7584\n"
  "v_pk_fma_f32 v[120:121], v[120:121], v[72:73], v[72:73] neg_lo:[0,0,1] neg_hi:[0,0,1]\n"
  "v_pk_fma_f32 v[122:123], v[122:123], v[74:75], v[74:75] neg_lo:[0,0,1] neg_hi:[0,0,1]\n"
  "v_mfma_f32_16x16x32_bf16 v[152:155], v[44:47], a[136:139], v[152:155]\n"
  "s_nop 0\n"
  "s_waitcnt lgkmcnt(14)\n"
  "buffer_store_dwordx4 v[20:23], v228, s[52:55], s81 offen offset:3072 nt\n"
  "v_accvgpr_read_b32 v240, a228\n"
  "v_accvgpr_read_b32 v241, a229\n"
  "v_accvgpr_read_b32 v242, a232\n"
  "v_accvgpr_read_b32 v243, a233\n"
  "v_accvgpr_read_b32 v236, a230\n"
  "v_accvgpr_read_b32 v237, a231\n"
  "v_accvgpr_read_b32 v238, a234\n"
  "v_accvgpr_read_b32 v239, a235\n"
  "v_pk_fma_f32 v[240:241], v[240:241], v[84:85], v[84:85] neg_lo:[0,0,1] neg_hi:[0,0,1]\n"
  "v_pk_fma_f32 v[242:243], v[242:243], v[84:85], v[84:85] neg_lo:[0,0,1] neg_hi:[0,0,1]\n"
  "v_pk_fma_f32 v[236:237], v[236:237], v[86:87], v[86:87] neg_lo:[0,0,1] neg_hi:[0,0,1]\n"
  "v_pk_fma_f32 v[238:239], v[238:239], v[86:87], v[86:87] neg_lo:[0,0,1] neg_hi:[0,0,1]\n"
  "v_mfma_f32_16x16x32_bf16 v[156:159], v[44:47], a[140:143], v[156:159]\n"
  "ds_read_b64_tr_b16 v[0:1], v230 offset:6720\n"
  "ds_read_b64_tr_b16 v[2:3], v230 offset:6752\n"
  "v_cvt_pk_bf16_f32 v240, v240, v242\n"
  "v_cvt_pk_bf16_f32 v241, v241, v243\n"
  "v_cvt_pk_bf16_f32 v236, v236, v238\n"
  "v_cvt_pk_bf16_f32 v237, v237, v239\n"
  "v_pk_fma_f32 v[124:125], v[124:125], v[72:73], v[72:73] neg_lo:[0,0,1] neg_hi:[0,0,1]\n"
  "v_pk_fma_f32 v[128:129], v[128:129], v[72:73], v[72:73] neg_lo:[0,0,1] neg_hi:[0,0,1]\n"
  "v_pk_fma_f32 v[126:127], v[126:127], v[74:75], v[74:75] neg_lo:[0,0,1] neg_hi:[0,0,1]\n"
  "v_pk_fma_f32 v[130:131], v[130:131], v[74:75], v[74:75] neg_lo:[0,0,1] neg_hi:[0,0,1]\n"
  "v_mfma_f32_16x16x32_bf16 v[160:163], v[44:47], a[144:147], v[160:163]\n"
  "s_nop 0\n"
  "s_waitcnt lgkmcnt(12)\n"
  "buffer_store_dwordx4 v[244:247], v228, s[52:55], s82 offen offset:0 nt\n"
  "ds_write_b32 v233, v240 offset:17920\n"
  "ds_write_b32 v233, v241 offset:18208\n"
  "ds_write_b32 v233, v236 offset:18496\n"
  "ds_write_b32 v233, v237 offset:18784\n"
  "v_pk_fma_f32 v[132:133], v[132:133], v[72:73], v[72:73] neg_lo:[0,0,1] neg_hi:[0,0,1]\n"
  "v_pk_fma_f32 v[134:135], v[134:135], v[74:75], v[74:75] neg_lo:[0,0,1] neg_hi:[0,0,1]\n"
  "v_mfma_f32_16x16x32_bf16 v[164:167], v[44:47], a[148:151], v[164:167]\n"
  "v_accvgpr_read_b32 v240, a236\n"
  "v_accvgpr_read_b32 v241, a237\n"
  "v_accvgpr_read_b32 v242, a240\n"
  "v_accvgpr_read_b32 v243, a241\n"
  "v_accvgpr_read_b32 v236, a238\n"
  "v_accvgpr_read_b32 v237, a239\n"
  "v_accvgpr_read_b32 v238, a242\n"
  "v_accvgpr_read_b32 v239, a243\n"
  "v_cvt_pk_bf16_f32 v88, v88, v92\n"
  "v_cvt_pk_bf16_f32 v92, v89, v93\n"
  "v_cvt_pk_bf16_f32 v89, v96, v100\n"
  "v_cvt_pk_bf16_f32 v93, v97, v101\n"
  "v_cvt_pk_bf16_f32 v90, v90, v94\n"
  "v_cvt_pk_bf16_f32 v94, v91, v95\n"
  "v_cvt_pk_bf16_f32 v91, v98, v102\n"
  "v_cvt_pk_bf16_f32 v95, v99, v103\n"
  "v_pk_fma_f32 v[240:241], v[240:241], v[84:85], v[84:85] neg_lo:[0,0,1] neg_hi:[0,0,1]\n"
  "v_pk_fma_f32 v[242:243], v[242:243], v[84:85], v[84:85] neg_lo:[0,0,1] neg_hi:[0,0,1]\n"
  "v_pk_fma_f32 v[236:237], v[236:237], v[86:87], v[86:87] neg_lo:[0,0,1] neg_hi:[0,0,1]\n"
  "v_pk_fma_f32 v[238:239], v[238:239], v[86:87], v[86:87] neg_lo:[0,0,1] neg_hi:[0,0,1]\n"
  "v_mfma_f32_16x16x32_bf16 v[184:187], v[60:63], a[136:139], v[184:187]\n"
  "ds_read_b64_tr_b16 v[4:5], v230 offset:7840\n"
  "ds_read_b64_tr_b16 v[6:7], v230 offset:7872\n"
  "ds_write_b64 v229, v[88:89] offset:8960\n"
  "ds_write_b64 v229, v[92:93] offset:9248\n"
  "ds_write_b64 v229, v[90:91] offset:9536\n"
  "ds_write_b64 v229, v[94:95] offset:9824\n"
  "v_cvt_pk_bf16_f32 v240, v240, v242\n"
  "v_cvt_pk_bf16_f32 v241, v241, v243\n"
  "v_cvt_pk_bf16_f32 v236, v236, v238\n"
  "v_cvt_pk_bf16_f32 v237, v237, v239\n"
  "v_pk_fma_f32 v[152:153], v[152:153], v[76:77], v[76:77] neg_lo:[0,0,1] neg_hi:[0,0,1]\n"
  "v_pk_fma_f32 v[156:157], v[156:157], v[76:77], v[76:77] neg_lo:[0,0,1] neg_hi:[0,0,1]\n"
  "v_pk_fma_f32 v[154:155], v[154:155], v[78:79], v[78:79] neg_lo:[0,0,1] neg_hi:[0,0,1]\n"
  "v_pk_fma_f32 v[158:159], v[158:159], v[78:79], v[78:79] neg_lo:[0,0,1] neg_hi:[0,0,1]\n"
  "v_mfma_f32_16x16x32_bf16 v[188:191], v[60:63], a[140:143], v[188:191]\n"
  "s_nop 0\n"
  "s_waitcnt lgkmcnt(15)\n"
  "buffer_store_dwordx4 a[252:255], v228, s[52:55], s82 offen offset:3072 nt\n"
  "ds_write_b32 v233, v240 offset:19040\n"
  "ds_write_b32 v233, v241 offset:19328\n"
  "ds_write_b32 v233, v236 offset:19616\n"
  "ds_write_b32 v233, v237 offset:19904\n"
  "v_pk_fma_f32 v[160:161], v[160:161], v[76:77], v[76:77] neg_lo:[0,0,1] neg_hi:[0,0,1]\n"
  "v_pk_fma_f32 v[162:163], v[162:163], v[78:79], v[78:79] neg_lo:[0,0,1] neg_hi:[0,0,1]\n"
  "v_mfma_f32_16x16x32_bf16 v[192:195], v[60:63], a[144:147], v[192:195]\n"
  "ds_read_b64_tr_b16 v[16:17], v230 offset:8960\n"
  "ds_read_b64_tr_b16 v[18:19], v230 offset:8992\n"
  "ds_read_b64_tr_b16 v[20:21], v230 offset:10080\n"
  "ds_read_b64_tr_b16 v[22:23], v230 offset:10112\n"
  "v_cvt_pk_bf16_f32 v120, v120, v124\n"
  "v_pk_fma_f32 v[164:165], v[164:165], v[76:77], v[76:77] neg_lo:[0,0,1] neg_hi:[0,0,1]\n"
  "v_pk_fma_f32 v[166:167], v[166:167], v[78:79], v[78:79] neg_lo:[0,0,1] neg_hi:[0,0,1]\n"
  "v_mfma_f32_16x16x32_bf16 v[196:199], v[60:63], a[148:151], v[196:199]\n"
  "s_waitcnt lgkmcnt(4)\n"
  "s_barrier\n"
  "buffer_store_dwordx4 v[0:3], v228, s[52:55], s83 offen offset:0 nt\n"
  "v_cvt_pk_bf16_f32 v124, v121, v125\n"
  "v_cvt_pk_bf16_f32 v121, v128, v132\n"
  "v_pk_fma_f32 v[184:185], v[184:185], v[80:81], v[80:81] neg_lo:[0,0,1] neg_hi:[0,0,1]\n"
  "v_pk_fma_f32 v[186:187], v[186:187], v[82:83], v[82:83] neg_lo:[0,0,1] neg_hi:[0,0,1]\n"
  "ds_read_b64_tr_b16 v[244:245], v230 offset:17920\n"
  "ds_read_b64_tr_b16 v[246:247], v230 offset:17952\n"
  "ds_write_b64 v229, v[120:121] offset:11200\n"
  "v_pk_fma_f32 v[188:189], v[188:189], v[80:81], v[80:81] neg_lo:[0,0,1] neg_hi:[0,0,1]\n"
  "v_pk_fma_f32 v[190:191], v[190:191], v[82:83], v[82:83] neg_lo:[0,0,1] neg_hi:[0,0,1]\n"
  "ds_read_b64_tr_b16 a[252:253], v230 offset:19040\n"
  "ds_read_b64_tr_b16 a[254:255], v230 offset:19072\n"
  "ds_read_b64_tr_b16 v[0:1], v230 offset:20160\n"
  "ds_read_b64_tr_b16 v[2:3], v230 offset:20192\n"
  "v_cvt_pk_bf16_f32 v125, v129, v133\n"
  "v_pk_fma_f32 v[192:193], v[192:193], v[80:81], v[80:81] neg_lo:[0,0,1] neg_hi:[0,0,1]\n"
  "v_pk_fma_f32 v[194:195], v[194:195], v[82:83], v[82:83] neg_lo:[0,0,1] neg_hi:[0,0,1]\n"
  "buffer_store_dwordx4 v[4:7], v228, s[52:55], s83 offen offset:3072 nt\n"
  "ds_write_b64 v229, v[124:125] offset:11488\n"
  "v_cvt_pk_bf16_f32 v122, v122, v126\n"
  "v_pk_fma_f32 v[196:197], v[196:197], v[80:81], v[80:81] neg_lo:[0,0,1] neg_hi:[0,0,1]\n"
  "v_pk_fma_f32 v[198:199], v[198:199], v[82:83], v[82:83] neg_lo:[0,0,1] neg_hi:[0,0,1]\n"
  "v_cvt_pk_bf16_f32 v126, v123, v127\n"
  "v_cvt_pk_bf16_f32 v123, v130, v134\n"
  "v_cvt_pk_bf16_f32 v127, v131, v135\n"
  "v_cvt_pk_bf16_f32 v152, v152, v156\n"
  "v_cvt_pk_bf16_f32 v156, v153, v157\n"
  "s_waitcnt lgkmcnt(10)\n"
  "buffer_store_dwordx4 v[16:19], v228, s[52:55], s80 offen offset:128 nt\n"
  "ds_write_b64 v229, v[122:123] offset:11776\n"
  "v_cvt_pk_bf16_f32 v153, v160, v164\n"
  "ds_write_b64 v229, v[126:127] offset:12064\n"
  "v_cvt_pk_bf16_f32 v157, v161, v165\n"
  "v_cvt_pk_bf16_f32 v154, v154, v158\n"
  "s_waitcnt lgkmcnt(10)\n"
  "buffer_store_dwordx4 v[20:23], v228, s[52:55], s80 offen offset:3200 nt\n"
  "ds_write_b64 v229, v[152:153] offset:13440\n"
  "v_cvt_pk_bf16_f32 v158, v155, v159\n"
  "ds_read_b64_tr_b16 v[4:5], v230 offset:11200\n"
  "ds_read_b64_tr_b16 v[6:7], v230 offset:11232\n"
  "ds_write_b64 v229, v[156:157] offset:13728\n"
  "s_waitcnt lgkmcnt(12)\n"
  "buffer_store_dwordx4 v[244:247], v228, s[52:55], s56 offen offset:0 nt\n"
  "ds_read_b64_tr_b16 v[16:17], v230 offset:12320\n"
  "ds_read_b64_tr_b16 v[18:19], v230 offset:12352\n"
  "v_cvt_pk_bf16_f32 v155, v162, v166\n"
  "v_cvt_pk_bf16_f32 v159, v163, v167\n"
  "ds_write_b64 v229, v[154:155] offset:14016\n"
  "v_cvt_pk_bf16_f32 v184, v184, v188\n"
  "v_cvt_pk_bf16_f32 v188, v185, v189\n"
  "s_waitcnt lgkmcnt(12)\n"
  "buffer_store_dwordx4 a[252:255], v228, s[52:55], s56 offen offset:128 nt\n"
  "ds_write_b64 v229, v[158:159] offset:14304\n"
  "v_cvt_pk_bf16_f32 v185, v192, v196\n"
  "ds_write_b64 v229, v[184:185] offset:15680\n"
  "v_cvt_pk_bf16_f32 v189, v193, v197\n"
  "v_cvt_pk_bf16_f32 v186, v186, v190\n"
  "s_waitcnt lgkmcnt(12)\n"
  "buffer_store_dwordx4 v[0:3], v228, s[52:55], s56 offen offset:256 nt\n"
  "ds_read_b64_tr_b16 v[20:21], v230 offset:13440\n"
  "ds_write_b64 v229, v[188:189] offset:15968\n"
  "ds_read_b64_tr_b16 v[22:23], v230 offset:13472\n"
  "ds_read_b64_tr_b16 v[244:245], v230 offset:14560\n"
  "ds_read_b64_tr_b16 v[246:247], v230 offset:14592\n"
  "v_cvt_pk_bf16_f32 v190, v187, v191\n"
  "v_cvt_pk_bf16_f32 v187, v194, v198\n"
  "s_waitcnt lgkmcnt(11)\n"
  "buffer_store_dwordx4 v[4:7], v228, s[52:55], s81 offen offset:128 nt\n"
  "ds_write_b64 v229, v[186:187] offset:16256\n"
  "v_cvt_pk_bf16_f32 v191, v195, v199\n"
  "ds_write_b64 v229, v[190:191] offset:16544\n"
  "s_waitcnt lgkmcnt(10)\n"
  "buffer_store_dwordx4 v[16:19], v228, s[52:55], s81 offen offset:3200 nt\n"
  "ds_read_b64_tr_b16 a[252:253], v230 offset:15680\n"
  "ds_read_b64_tr_b16 a[254:255], v230 offset:15712\n"
  "ds_read_b64_tr_b16 v[0:1], v230 offset:16800\n"
  "ds_read_b64_tr_b16 v[2:3], v230 offset:16832\n"
  "s_waitcnt lgkmcnt(8)\n"
  "buffer_store_dwordx4 v[20:23], v228, s[52:55], s82 offen offset:128 nt\n"
  "s_waitcnt lgkmcnt(6)\n"
  "buffer_store_dwordx4 v[244:247], v228, s[52:55], s82 offen offset:3200 nt\n"
  "s_waitcnt lgkmcnt(2)\n"
  "buffer_store_dwordx4 a[252:255], v228, s[52:55], s83 offen offset:128 nt\n"
  "s_waitcnt lgkmcnt(0)\n"
  "buffer_store_dwordx4 v[0:3], v228, s[52:55], s83 offen offset:3200 nt\n"
  "L_END_%=:\n"
  "s_mov_b32 %0, s78\n"
  : "=&s"(bad)
  : "s"((unsigned long long)__builtin_amdgcn_kernarg_segment_ptr()), "s"(wg), "s"(tid0), "s"((unsigned)(size_t)lds)
  : "v0", "v1", "v2", "v3", "v4", "v5", "v6", "v7", "v8", "v9", "v10", "v11", "v12", "v13", "v14", "v15", "v16", "v17", "v18", "v19", "v20", "v21", "v22", "v23", "v24", "v25", "v26", "v27", "v28", "v29", "v30", "v31", "v32", "v33", "v34", "v35", "v36", "v37", "v38", "v39", "v40", "v41", "v42", "v43", "v44", "v45", "v46", "v47", "v48", "v49", "v50", "v51", "v52", "v53", "v54", "v55", "v56", "v57", "v58", "v59", "v60", "v61", "v62", "v63", "v64", "v65", "v66", "v67", "v68", "v69", "v70", "v71", "v72", "v73", "v74", "v75", "v76", "v77", "v78", "v79", "v80", "v81", "v82", "v83", "v84", "v85", "v86", "v87", "v88", "v89", "v90", "v91", "v92", "v93", "v94", "v95", "v96", "v97", "v98", "v99", "v100", "v101", "v102", "v103", "v104", "v105", "v106", "v107", "v108", "v109", "v110", "v111", "v112", "v113", "v114", "v115", "v116", "v117", "v118", "v119", "v120", "v121", "v122", "v123", "v124", "v125", "v126", "v127", "v128", "v129", "v130", "v131", "v132", "v133", "v134", "v135", "v136", "v137", "v138", "v139", "v140", "v141", "v142", "v143", "v144", "v145", "v146", "v147", "v148", "v149", "v150", "v151", "v152", "v153", "v154", "v155", "v156", "v157", "v158", "v159", "v160", "v161", "v162", "v163", "v164", "v165", "v166", "v167", "v168", "v169", "v170", "v171", "v172", "v173", "v174", "v175", "v176", "v177", "v178", "v179", "v180", "v181", "v182", "v183", "v184", "v185", "v186", "v187", "v188", "v189", "v190", "v191", "v192", "v193", "v194", "v195", "v196", "v197", "v198", "v199", "v200", "v201", "v202", "v203", "v204", "v205", "v206", "v207", "v208", "v209", "v210", "v211", "v212", "v213", "v214", "v215", "v216", "v217", "v218", "v219", "v220", "v221", "v222", "v223", "v224", "v225", "v226", "v227", "v228", "v229", "v230", "v231", "v232", "v233", "v234", "v235", "v236", "v237", "v238", "v239", "v240", "v241", "v242", "v243", "v244", "v245", "v246", "v247", "v248", "v249", "v250", "v251", "v252", "v253", "v254", "v255", "a0", "a1", "a2", "a3", "a4", "a5", "a6", "a7", "a8", "a9", "a10", "a11", "a12", "a13", "a14", "a15", "a16", "a17", "a18", "a19", "a20", "a21", "a22", "a23", "a24", "a25", "a26", "a27", "a28", "a29", "a30", "a31", "a32", "a33", "a34", "a35", "a36", "a37", "a38", "a39", "a40", "a41", "a42", "a43", "a44", "a45", "a46", "a47", "a48", "a49", "a50", "a51", "a52", "a53", "a54", "a55", "a56", "a57", "a58", "a59", "a60", "a61", "a62", "a63", "a64", "a65", "a66", "a67", "a68", "a69", "a70", "a71", "a72", "a73", "a74", "a75", "a76", "a77", "a78", "a79", "a80", "a81", "a82", "a83", "a84", "a85", "a86", "a87", "a88", "a89", "a90", "a91", "a92", "a93", "a94", "a95", "a96", "a97", "a98", "a99", "a100", "a101", "a102", "a103", "a104", "a105", "a106", "a107", "a108", "a109", "a110", "a111", "a112", "a113", "a114", "a115", "a116", "a117", "a118", "a119", "a120", "a121", "a122", "a123", "a124", "a125", "a126", "a127", "a128", "a129", "a130", "a131", "a132", "a133", "a134", "a135", "a136", "a137", "a138", "a139", "a140", "a141", "a142", "a143", "a144", "a145", "a146", "a147", "a148", "a149", "a150", "a151", "a152", "a153", "a154", "a155", "a156", "a157", "a158", "a159", "a160", "a161", "a162", "a163", "a164", "a165", "a166", "a167", "a168", "a169", "a170", "a171", "a172", "a173", "a174", "a175", "a176", "a177", "a178", "a179", "a180", "a181", "a182", "a183", "a184", "a185", "a186", "a187", "a188", "a189", "a190", "a191", "a192", "a193", "a194", "a195", "a196", "a197", "a198", "a199", "a200", "a201", "a202", "a203", "a204", "a205", "a206", "a207", "a208", "a209", "a210", "a211", "a212", "a213", "a214", "a215", "a216", "a217", "a218", "a219", "a220", "a221", "a222", "a223", "a224", "a225", "a226", "a227", "a228", "a229", "a230", "a231", "a232", "a233", "a234", "a235", "a236", "a237", "a238", "a239", "a240", "a241", "a242", "a243", "a244", "a245", "a246", "a247", "a248", "a249", "a250", "a251", "a252", "a253", "a254", "a255", "s20", "s21", "s22", "s23", "s24", "s25", "s26", "s27", "s28", "s29", "s30", "s31", "s32", "s33", "s34", "s35", "s36", "s37", "s38", "s39", "s40", "s41", "s42", "s43", "s44", "s45", "s46", "s47", "s48", "s49", "s50", "s51", "s52", "s53", "s54", "s55", "s56", "s57", "s58", "s59", "s60", "s61", "s62", "s63", "s64", "s65", "s66", "s67", "s68", "s69", "s70", "s71", "s72", "s73", "s74", "s75", "s76", "s77", "s78", "s79", "s80", "s81", "s82", "s83", "s84", "s85", "s86", "s87", "s88", "s89", "s90", "s91", "s92", "s93", "s94", "s95", "s96", "s97", "s98", "s99", "s100", "s101", "vcc", "scc", "memory");
  if (bad) {
    unsigned long long ka = (unsigned long long)__builtin_amdgcn_kernarg_segment_ptr();
    asm volatile("s_waitcnt vmcnt(0)" : "+s"(ka) :: "memory");
    const unsigned short* const* arg = (const unsigned short* const*)ka;
    const int xcd = wg & 7, slot = wg >> 3;
    constexpr int HB = 0;
    auto head_of = [&](int lh) { return lh + (lh >> HB) * (7 << HB) + (xcd << HB); };
#pragma unroll 1
    for (int it = 0; it < 6; ++it) {
      if (!((bad >> it) & 1u)) continue;
      const int p = slot + 32 * it, head = head_of(p / 3), r0 = 288 * (p % 3);
#pragma unroll 1
      for (int Y = 0; Y < 5; ++Y) {
        const int row = r0 + (Y < 4 ? 64 * wid + 16 * Y : 256 + 16 * (wid >> 1)), lo = Y < 4 ? 0 : 8 * (wid & 1);
        if (row < SQ) exact_tile(arg[0], arg[1], arg[2], (unsigned short*)arg[3], head, row, lo, Y < 4 ? 16 : lo + 8);
      }
    }
  }
}

extern "C" void kernel_launch(void* const* d_in, const int* in_sizes, int n_in,
                              void* d_out, int out_size, void* d_ws, size_t ws_size,
                              hipStream_t stream) {
  (void)hipFuncSetAttribute((const void*)attn_fwd_g, hipFuncAttributeMaxDynamicSharedMemorySize, LDS_BYTES);
  hipLaunchKernelGGL(attn_fwd_g, dim3(256), dim3(256), LDS_BYTES, stream, (const unsigned short*)d_in[0], (const unsigned short*)d_in[1],
                     (const unsigned short*)d_in[2], (unsigned short*)d_out);
}
```
